```python
import torch

def _softmax(x, axis=-1):
    return torch.softmax(x, dim=axis)

def _gelu(x):
    return torch.nn.functional.gelu(x, approximate='tanh')

def _silu(x):
    return torch.nn.functional.silu(x)

def _sigmoid(x):
    return torch.sigmoid(x)

def _layernorm(x, axis=-1, eps=1e-05):
    m = x.mean(dim=axis, keepdim=True)
    v = x.var(dim=axis, keepdim=True, unbiased=False)
    return (x - m) / torch.sqrt(v + eps)

def _scatter_add(tgt, idx, src, axis=0):
    return tgt.clone().index_add_(axis, idx, src)

def _segment_sum(src, seg, n):
    out = torch.zeros((n,) + src.shape[1:], dtype=src.dtype)
    return out.index_add_(0, seg, src)

def _pad(x, pw):
    flat = tuple((v for pair in reversed(pw) for v in pair))
    return torch.nn.functional.pad(x, flat)
kernel_name = 'standalone__dilated_atrous__vision'

def setup_inputs():

    _w = lambda a: (lambda t: t.to(torch.bfloat16) if t.is_floating_point() else t)(torch.as_tensor(a))
    H = W = 512
    C = 96
    k = 3
    return {'x': _w((lambda *s: torch.randn(*s, dtype=torch.bfloat16) * 0.05)(1, H, W, C)), 'w': _w((lambda *s: torch.randn(*s, dtype=torch.bfloat16) * 0.05)(k, k, C, C) * (1.0 / (k * C ** 0.5)))}

def reference(x, w):
    B, H, W, C = x.shape
    k = 3
    rate = 2
    pad = (k - 1) * rate // 2
    xp = _pad(x, ((0, 0), (pad, pad), (pad, pad), (0, 0)))
    out = torch.zeros_like(x)
    for i in range(k):
        for j in range(k):
            tap = xp[:, i * rate:i * rate + H, j * rate:j * rate + W, :].clone()
            out = out + torch.einsum('bhwc,co->bhwo', tap, w[i, j])
    return out
```

```cpp
#include <hip/hip_runtime.h>
#include <hip/hip_bf16.h>
#include <stdint.h>

__global__ void __launch_bounds__(256, 1)
conv_kernel(const char* __restrict__ x, const char* __restrict__ w, char* __restrict__ out, char* __restrict__ dbg) {
  __shared__ __attribute__((aligned(1024))) char smem[163840];
  unsigned tid = threadIdx.x;
  asm volatile(
      "s_load_dwordx2 s[12:13], s[0:1], 8\n"
      "s_load_dwordx2 s[4:5], s[0:1], 0\n"
      "s_load_dwordx2 s[8:9], s[0:1], 16\n"
      "v_readfirstlane_b32 s16, v0\n"
      "s_getpc_b64 s[26:27]\n"
      ".Lwg_pc:\n"
      "v_and_b32 v37, 3, v0\n"
      "s_lshr_b32 s23, s2, 3\n"
      "s_lshl_b32 s28, s16, 3\n"
      "v_lshlrev_b32 v40, 7, v37\n"
      "s_lshl_b32 s29, s23, 11\n"
      "s_mov_b32 m0, 161280\n"
      "s_add_u32 s28, s28, s29\n"
      "v_and_b32 v36, 63, v0\n"
      "v_add_u32 v40, s28, v40\n"
      "s_lshr_b32 s16, s16, 6\n"
      "v_min_u32 v40, .Lwg_end-.Lwg_pc-4, v40\n"
      "s_and_b32 s18, s16, 1\n"
      "global_load_lds_dword v40, s[26:27]\n"
      "v_lshlrev_b32 v31, 4, v36\n"
      "s_lshr_b32 s17, s16, 1\n"
      "s_and_b32 s28, s2, 7\n"
      "s_mul_i32 s19, s28, 6291456\n"
      "s_and_b32 s28, s23, 1\n"
      "s_mul_i32 s20, s28, 98304\n"
      "s_lshr_b32 s28, s23, 1\n"
      "s_mul_i32 s29, s28, 6144\n"
      "s_add_u32 s21, s20, s29\n"
      "s_add_u32 s6, s19, 6492160\n"
      "s_min_u32 s6, s6, 50335744\n"
      "s_mov_b32 s7, 0x20000\n"
      "s_mov_b32 s10, 50331648\n"
      "s_mov_b32 s11, 0x20000\n"
      "s_mov_b32 s14, 165888\n"
      "s_mov_b32 s15, 0x20000\n"
      "s_mul_i32 s63, s18, 3072\n"
      "s_lshl_b32 s49, s18, 12\n"
      "s_mul_i32 s56, s18, 7176\n"
      "s_mul_i32 s57, s18, 7416\n"
      "s_add_u32 s54, s19, s20\n"
      "s_sub_u32 s54, s54, 196608\n"
      "s_mul_i32 s64, s17, 24832\n"
      "s_lshl_b32 s30, s18, 8\n"
      "s_add_u32 s64, s64, s30\n"
      "v_and_b32 v43, 31, v36\n"
      "v_lshlrev_b32 v44, 5, v43\n"
      "v_and_b32 v45, 16, v31\n"
      "v_and_b32 v44, 0x3c0, v44\n"
      "v_and_b32 v46, 32, v36\n"
      "s_add_u32 s29, s29, 3712\n"
      "v_or3_b32 v44, v44, v45, v46\n"
      "s_add_u32 s30, s29, s49\n"
      "s_mul_i32 s31, s18, 196608\n"
      "v_add_u32 v45, s30, v44\n"
      "s_add_u32 s31, s31, s29\n"
      "s_cmp_eq_u32 s18, 1\n"
      "s_cselect_b64 vcc, -1, 0\n"
      "v_xor_b32 v46, 16, v45\n"
      "v_bitop3_b32 v47, v36, v45, 16 bitop3:0x6c\n"
      "v_add_u32 v18, s31, v44\n"
      "s_cselect_b32 s31, 24, 32\n"
      "v_cndmask_b32 v15, v45, v47, vcc\n"
      "v_cndmask_b32 v16, v47, v46, vcc\n"
      "v_cndmask_b32 v17, v46, v45, vcc\n"
      "s_cmp_eq_u32 s30, 3712\n"
      "s_cselect_b32 s36, 12, 0\n"
      "s_cmp_eq_u32 s30, 99968\n"
      "s_cselect_b32 s31, 12, s31\n"
      "v_cmp_le_u32 vcc, s36, v43\n"
      "v_cmp_gt_u32 s[36:37], s31, v43\n"
      "s_sub_u32 s53, s54, 196608\n"
      "s_cmp_eq_u32 s19, 0\n"
      "v_cndmask_b32 v15, 2.0, v15, vcc\n"
      "s_cselect_b32 s53, 0x80000000, s53\n"
      "v_cndmask_b32 v17, 2.0, v17, s[36:37]\n"
      "v_bfe_u32 v38, v36, 2, 2\n"
      "v_lshrrev_b32 v39, 4, v36\n"
      "v_lshrrev_b32 v41, 5, v36\n"
      "s_cmp_eq_u32 s17, 1\n"
      "s_cbranch_scc1 LBD_%=\n"
      "s_add_u32 s34, s19, s21\n"
      "s_add_u32 m0, s63, 18432\n"
      "s_add_u32 s52, s63, 0\n"
      "s_waitcnt lgkmcnt(0)\n"
      "s_load_dword s44, s[4:5], s34\n"
      "s_and_b32 s13, s13, 0xffff\n"
      "buffer_load_dwordx4 v31, s[12:15], s52 offen offset:0 lds\n"
      "buffer_load_dwordx4 v31, s[12:15], s52 offen offset:1024 lds\n"
      "buffer_load_dwordx4 v31, s[12:15], s52 offen offset:2048 lds\n"
      "s_add_u32 s4, s4, -4096\n"
      "s_addc_u32 s5, s5, -1\n"
      "s_and_b32 s5, s5, 0xffff\n"
      "s_add_u32 m0, s57, 121992\n"
      "s_nop 0\n"
      "buffer_load_dwordx4 v18, s[4:7], s53 offen offset:3072 nt lds\n"
      "s_add_u32 s53, s53, 196608\n"
      "s_add_u32 m0, s49, 129408\n"
      "s_nop 0\n"
      "buffer_load_dwordx4 v15, s[4:7], s53 offen offset:0 nt lds\n"
      "buffer_load_dwordx4 v16, s[4:7], s53 offen offset:1024 nt lds\n"
      "buffer_load_dwordx4 v17, s[4:7], s53 offen offset:2048 nt lds\n"
      "s_add_u32 s54, s54, 196608\n"
      "s_add_u32 m0, s49, 78336\n"
      "s_nop 0\n"
      "buffer_load_dwordx4 v15, s[4:7], s54 offen offset:0 nt lds\n"
      "buffer_load_dwordx4 v16, s[4:7], s54 offen offset:1024 nt lds\n"
      "buffer_load_dwordx4 v17, s[4:7], s54 offen offset:2048 nt lds\n"
      "s_add_u32 m0, s56, 78336\n"
      "s_nop 0\n"
      "buffer_load_dwordx4 v18, s[4:7], s54 offen offset:3072 nt lds\n"
      "s_branch LS_%=\n"
      "LBD_%=:\n"
      "v_lshlrev_b32 v40, 3, v31\n"
      "s_mul_i32 s53, s18, 2688\n"
      "s_mul_i32 s34, s23, 5184\n"
      "v_min_u32 v40, 2560, v40\n"
      "s_add_u32 s53, s53, s34\n"
      "s_waitcnt lgkmcnt(0)\n"
      "s_and_b32 s13, s13, 0xffff\n"
      "buffer_load_dword v40, s[12:15], s53 offen lds\n"
      "s_add_u32 m0, s63, 24576\n"
      "s_add_u32 s52, s63, 55296\n"
      "buffer_load_dwordx4 v31, s[12:15], s52 offen offset:0 lds\n"
      "buffer_load_dwordx4 v31, s[12:15], s52 offen offset:1024 lds\n"
      "buffer_load_dwordx4 v31, s[12:15], s52 offen offset:2048 lds\n"
      "s_add_u32 m0, s63, 30720\n"
      "s_add_u32 s52, s63, 110592\n"
      "buffer_load_dwordx4 v31, s[12:15], s52 offen offset:0 lds\n"
      "buffer_load_dwordx4 v31, s[12:15], s52 offen offset:1024 lds\n"
      "buffer_load_dwordx4 v31, s[12:15], s52 offen offset:2048 lds\n"
      "s_add_u32 s4, s4, -4096\n"
      "s_addc_u32 s5, s5, -1\n"
      "s_and_b32 s5, s5, 0xffff\n"
      "s_and_b32 s9, s9, 0xffff\n"
      "v_lshlrev_b32 v43, 3, v37\n"
      "v_mul_u32_u24 v44, 192, v38\n"
      "v_mul_u32_u24 v45, 1536, v39\n"
      "s_mul_i32 s26, s17, 12288\n"
      "s_lshl_b32 s27, s18, 5\n"
      "v_add_u32 v43, v43, v44\n"
      "s_sub_u32 s26, s27, s26\n"
      "v_mul_u32_u24 v44, 1536, v41\n"
      "s_add_u32 s26, s26, 30720\n"
      "v_add3_u32 v33, v43, v45, s26\n"
      "v_and_b32 v45, 16, v36\n"
      "v_add_u32 v43, v43, v44\n"
      "s_lshl_b32 s27, s18, 6\n"
      "v_lshl_add_u32 v43, v45, 1, v43\n"
      "s_add_u32 s27, s27, 18432\n"
      "v_add_u32 v34, 18560, v43\n"
      "v_add_u32 v32, s27, v43\n"
      "v_lshlrev_b32 v43, 4, v37\n"
      "s_movk_i32 s26, 0x3f00\n"
      "s_mov_b32 s27, 0\n"
      "s_lshl_b32 s36, s17, 7\n"
      "s_mov_b32 s29, 0\n"
      "s_sub_u32 s28, s26, s36\n"
      "s_mov_b32 s31, 0\n"
      "s_sub_u32 s30, s28, s36\n"
      "s_lshl_b32 s36, s18, 15\n"
      "v_lshlrev_b64 v[44:45], v43, s[26:27]\n"
      "s_or_b32 s28, s28, s36\n"
      "v_lshlrev_b64 v[48:49], v43, s[30:31]\n"
      "v_lshlrev_b64 v[46:47], v43, s[28:29]\n"
      "v_lshlrev_b32 v50, 27, v36\n"
      "v_accvgpr_write_b32 a168, 0\n"
      "v_accvgpr_write_b32 a169, 0\n"
      "v_accvgpr_write_b32 a170, 0\n"
      "v_accvgpr_write_b32 a171, 0\n"
      "v_accvgpr_write_b32 a172, 0\n"
      "v_accvgpr_write_b32 a173, 0\n"
      "v_accvgpr_write_b32 a174, 0\n"
      "v_accvgpr_write_b32 a175, 0\n"
      "v_accvgpr_write_b32 a176, 0\n"
      "v_accvgpr_write_b32 a177, 0\n"
      "v_accvgpr_write_b32 a178, 0\n"
      "v_accvgpr_write_b32 a179, 0\n"
      "v_accvgpr_write_b32 a238, v44\n"
      "v_accvgpr_write_b32 a239, v45\n"
      "v_accvgpr_write_b32 a240, v46\n"
      "v_accvgpr_write_b32 a241, v47\n"
      "v_accvgpr_write_b32 a254, v48\n"
      "v_accvgpr_write_b32 a255, v49\n"
      "v_ashrrev_i32 v50, 31, v50\n"
      "v_and_b32 v50, 0x80008000, v50\n"
      "v_xor_b32 v46, v50, v46\n"
      "v_xor_b32 v47, v50, v47\n"
      "s_mov_b32 exec_lo, 0x000f000f\n"
      "s_mov_b32 exec_hi, 0x00f000f0\n"
      "v_accvgpr_write_b32 a168, v44\n"
      "v_accvgpr_write_b32 a169, v45\n"
      "v_accvgpr_write_b32 a172, v46\n"
      "v_accvgpr_write_b32 a173, v47\n"
      "v_accvgpr_write_b32 a176, v48\n"
      "v_accvgpr_write_b32 a177, v49\n"
      "s_lshl_b64 exec, exec, 8\n"
      "v_accvgpr_write_b32 a170, v44\n"
      "v_accvgpr_write_b32 a171, v45\n"
      "v_accvgpr_write_b32 a174, v46\n"
      "v_accvgpr_write_b32 a175, v47\n"
      "v_accvgpr_write_b32 a178, v48\n"
      "v_accvgpr_write_b32 a179, v49\n"
      "s_mov_b64 exec, -1\n"
      "v_lshrrev_b32 v51, 1, v36\n"
      "v_and_b32 v42, 15, v36\n"
      "v_bitop3_b32 v42, v51, v42, 4 bitop3:0x6c\n"
      "s_mul_i32 s26, s17, 21888\n"
      "v_lshlrev_b32 v43, 1, v42\n"
      "s_add_u32 s26, s26, 78336\n"
      "v_mul_u32_u24 v44, 96, v42\n"
      "v_lshl_add_u32 v45, v41, 9, s26\n"
      "v_bitop3_b32 v46, v43, v36, 16 bitop3:0x28\n"
      "v_add_u32 v46, v46, v45\n"
      "v_and_b32 v47, 0x1e00, v44\n"
      "v_add3_u32 v3, v46, v44, v47\n"
      "v_add_u32 v44, 32, v44\n"
      "v_and_b32 v47, 0x1e00, v44\n"
      "v_add3_u32 v4, v46, v44, v47\n"
      "v_add_u32 v44, 32, v44\n"
      "v_and_b32 v47, 0x1e00, v44\n"
      "v_add3_u32 v5, v46, v44, v47\n"
      "v_add_u32 v43, 4, v43\n"
      "v_bitop3_b32 v46, v43, v36, 16 bitop3:0x28\n"
      "v_add_u32 v46, v46, v45\n"
      "v_add_u32 v44, 128, v44\n"
      "v_and_b32 v47, 0x1e00, v44\n"
      "v_add3_u32 v6, v46, v44, v47\n"
      "v_add_u32 v44, 32, v44\n"
      "v_and_b32 v47, 0x1e00, v44\n"
      "v_add3_u32 v7, v46, v44, v47\n"
      "v_add_u32 v44, 32, v44\n"
      "v_and_b32 v47, 0x1e00, v44\n"
      "v_add3_u32 v8, v46, v44, v47\n"
      "v_add_u32 v43, 4, v43\n"
      "v_bitop3_b32 v46, v43, v36, 16 bitop3:0x28\n"
      "v_add_u32 v46, v46, v45\n"
      "v_add_u32 v44, 128, v44\n"
      "v_and_b32 v47, 0x1e00, v44\n"
      "v_add3_u32 v9, v46, v44, v47\n"
      "v_add_u32 v44, 32, v44\n"
      "v_and_b32 v47, 0x1e00, v44\n"
      "v_add3_u32 v10, v46, v44, v47\n"
      "v_add_u32 v44, 32, v44\n"
      "v_and_b32 v47, 0x1e00, v44\n"
      "v_add3_u32 v11, v46, v44, v47\n"
      "v_lshlrev_b32 v43, 31, v36\n"
      "v_and_b32 v44, 2, v36\n"
      "v_or_b32 v43, 0x3f803f80, v43\n"
      "v_mul_u32_u24 v45, 0xc00080, v44\n"
      "v_lshl_add_u32 v44, v44, 22, v43\n"
      "v_add_u32 v45, v45, v43\n"
      "v_accvgpr_write_b32 a232, v44\n"
      "v_accvgpr_write_b32 a233, v45\n"
      "v_lshlrev_b32 v43, 3, v38\n"
      "s_mov_b32 s26, 806420736\n"
      "s_lshl_b32 s27, s18, 8\n"
      "v_bfe_u32 v43, s26, v43, 8\n"
      "v_mul_u32_u24 v44, 7168, v38\n"
      "s_add_u32 s28, s27, 78336\n"
      "v_lshl_add_u32 v43, v43, 3, v44\n"
      "v_lshl_add_u32 v44, v39, 2, v37\n"
      "v_lshl_add_u32 v43, v44, 4, v43\n"
      "s_lshl_b32 s27, s27, 1\n"
      "s_sub_u32 s27, s64, s27\n"
      "s_add_u32 s27, s27, 256\n"
      "v_add_u32 v14, s28, v43\n"
      "v_lshl_add_u32 v35, v36, 2, s27\n"
      "s_mul_i32 s26, s16, 3584\n"
      "v_and_b32 v43, 31, v36\n"
      "s_add_u32 s26, s26, 24832\n"
      "v_mul_u32_u24 v44, 96, v43\n"
      "v_lshlrev_b32 v43, 1, v43\n"
      "v_add_u32 v44, s26, v44\n"
      "v_bitop3_b32 v46, v43, v51, 16 bitop3:0x28\n"
      "v_add_u32 v0, v46, v44\n"
      "v_add_u32 v43, 4, v43\n"
      "v_add_u32 v44, 192, v44\n"
      "v_bitop3_b32 v46, v43, v51, 16 bitop3:0x28\n"
      "v_add_u32 v1, v46, v44\n"
      "v_add_u32 v43, 4, v43\n"
      "v_add_u32 v44, 192, v44\n"
      "v_bitop3_b32 v46, v43, v51, 16 bitop3:0x28\n"
      "v_add_u32 v2, v46, v44\n"
      "v_and_b32 v43, 4, v51\n"
      "v_or_b32 v43, v43, v37\n"
      "v_mul_u32_u24 v43, 3104, v43\n"
      "v_and_b32 v44, 4, v42\n"
      "v_lshl_add_u32 v43, v44, 5, v43\n"
      "s_lshl_b32 s26, s18, 6\n"
      "v_lshl_add_u32 v43, v39, 4, v43\n"
      "s_mul_i32 s27, s17, 6144\n"
      "v_add_u32 v12, s26, v43\n"
      "s_sub_u32 s27, 142848, s27\n"
      "v_bfe_u32 v43, v42, 1, 2\n"
      "s_lshl_b32 s26, s18, 1\n"
      "v_mul_u32_u24 v44, 192, v42\n"
      "v_bitop3_b32 v43, s26, v43, v41 bitop3:0x96\n"
      "v_and_b32 v45, 8, v51\n"
      "v_lshl_add_u32 v44, v43, 4, v44\n"
      "s_mul_i32 s26, s18, 12288\n"
      "v_add3_u32 v13, v44, v45, s27\n"
      "v_mul_u32_u24 v43, 171, v36\n"
      "s_add_u32 s26, s26, 136704\n"
      "v_lshrrev_b32 v44, 8, v43\n"
      "v_bitop3_b32 v44, v44, v31, 48 bitop3:0x6c\n"
      "v_add_u32 v28, s26, v44\n"
      "v_add_u32 v43, 10944, v43\n"
      "s_add_u32 s26, s26, 1024\n"
      "v_lshrrev_b32 v44, 8, v43\n"
      "v_bitop3_b32 v44, v44, v31, 48 bitop3:0x6c\n"
      "v_add_u32 v29, s26, v44\n"
      "v_add_u32 v43, 10944, v43\n"
      "s_add_u32 s26, s26, 1024\n"
      "v_lshrrev_b32 v44, 8, v43\n"
      "v_bitop3_b32 v44, v44, v31, 48 bitop3:0x6c\n"
      "v_add_u32 v30, s26, v44\n"
      "s_mul_i32 s62, s18, 393216\n"
      "s_mov_b32 s55, 0\n"
      "s_add_u32 s62, s62, s19\n"
      "s_mov_b32 s60, 0x80000000\n"
      "s_add_u32 s62, s62, s21\n"
      "s_mov_b32 s61, 0x80000000\n"
      "s_sub_u32 s62, s62, 1572864\n"
      "s_add_u32 s58, s62, 786432\n"
      "s_mov_b32 s45, 0xc0a00000\n"
      "s_mov_b32 s46, 0x3f2aaaab\n"
      "s_mov_b32 s47, -0.5\n"
      "s_mov_b32 s48, 4.0\n"
      "s_add_u32 s54, s54, 786432\n"
      "v_accvgpr_write_b32 a234, v28\n"
      "v_accvgpr_write_b32 a235, v29\n"
      "v_accvgpr_write_b32 a236, v30\n"
      "v_accvgpr_write_b32 a237, v31\n"
      "LDpro_%=:\n"
      "s_add_u32 m0, s63, 36864\n"
      "s_add_u32 s52, s63, 6144\n"
      "buffer_load_dwordx4 v31, s[12:15], s52 offen offset:0 lds\n"
      "buffer_load_dwordx4 v31, s[12:15], s52 offen offset:1024 lds\n"
      "buffer_load_dwordx4 v31, s[12:15], s52 offen offset:2048 lds\n"
      "s_add_u32 m0, s63, 12288\n"
      "s_add_u32 s52, s63, 122880\n"
      "s_waitcnt vmcnt(3)\n"
      "s_barrier\n"
      "ds_read_b64_tr_b16 a[132:133], v32 offset:0\n"
      "ds_read_b64_tr_b16 a[134:135], v32 offset:768\n"
      "ds_read_b64_tr_b16 a[136:137], v32 offset:6144\n"
      "ds_read_b64_tr_b16 a[138:139], v32 offset:6912\n"
      "ds_read_b64_tr_b16 a[140:141], v32 offset:12288\n"
      "ds_read_b64_tr_b16 a[142:143], v32 offset:13056\n"
      "ds_read_b64_tr_b16 a[144:145], v34 offset:0\n"
      "ds_read_b64_tr_b16 a[146:147], v34 offset:768\n"
      "ds_read_b64_tr_b16 a[148:149], v34 offset:6144\n"
      "ds_read_b64_tr_b16 a[150:151], v34 offset:6912\n"
      "ds_read_b64_tr_b16 a[152:153], v34 offset:12288\n"
      "ds_read_b64_tr_b16 a[154:155], v34 offset:13056\n"
      "s_waitcnt lgkmcnt(10)\n"
      "v_mfma_f32_32x32x16_bf16 v[52:67], a[168:171], a[132:135], 0\n"
      "buffer_load_dwordx4 v31, s[12:15], s52 offen offset:0 lds\n"
      "ds_read_b64_tr_b16 a[156:157], v32 offset:3072\n"
      "ds_read_b64_tr_b16 a[158:159], v32 offset:3840\n"
      "ds_read_b64_tr_b16 a[0:1], v33 offset:0\n"
      "s_waitcnt lgkmcnt(11)\n"
      "v_mfma_f32_32x32x16_bf16 v[52:67], a[172:175], a[136:139], v[52:67]\n"
      "buffer_load_dwordx4 v31, s[12:15], s52 offen offset:1024 lds\n"
      "ds_read_b64_tr_b16 a[160:161], v32 offset:9216\n"
      "ds_read_b64_tr_b16 a[162:163], v32 offset:9984\n"
      "ds_read_b64_tr_b16 a[2:3], v33 offset:768\n"
      "s_waitcnt lgkmcnt(10)\n"
      "v_mfma_f32_32x32x16_bf16 v[52:67], a[176:179], a[140:143], v[52:67]\n"
      "buffer_load_dwordx4 v31, s[12:15], s52 offen offset:2048 lds\n"
      "ds_read_b64_tr_b16 a[164:165], v32 offset:15360\n"
      "ds_read_b64_tr_b16 a[166:167], v32 offset:16128\n"
      "ds_read_b64_tr_b16 a[4:5], v33 offset:64\n"
      "v_add_u32 v32, 18432, v32\n"
      "v_mfma_f32_4x4x4_16b_bf16 v[84:87], a[238:239], a[144:145], 0\n"
      "ds_read_b64_tr_b16 a[132:133], v34 offset:3072\n"
      "ds_read_b64_tr_b16 a[6:7], v33 offset:832\n"
      "v_mfma_f32_4x4x4_16b_bf16 v[88:91], a[238:239], a[146:147], 0\n"
      "ds_read_b64_tr_b16 a[134:135], v34 offset:3840\n"
      "s_waitcnt lgkmcnt(9)\n"
      "v_mfma_f32_4x4x4_16b_bf16 v[84:87], a[240:241], a[148:149], v[84:87]\n"
      "ds_read_b64_tr_b16 a[136:137], v34 offset:9216\n"
      "ds_read_b64_tr_b16 a[8:9], v33 offset:128\n"
      "v_mfma_f32_4x4x4_16b_bf16 v[88:91], a[240:241], a[150:151], v[88:91]\n"
      "ds_read_b64_tr_b16 a[138:139], v34 offset:9984\n"
      "v_mfma_f32_4x4x4_16b_bf16 v[84:87], a[254:255], a[152:153], v[84:87]\n"
      "ds_read_b64_tr_b16 a[140:141], v34 offset:15360\n"
      "ds_read_b64_tr_b16 a[10:11], v33 offset:896\n"
      "v_mfma_f32_4x4x4_16b_bf16 v[88:91], a[254:255], a[154:155], v[88:91]\n"
      "ds_read_b64_tr_b16 a[142:143], v34 offset:16128\n"
      "v_add_u32 v34, 18432, v34\n"
      "s_add_u32 m0, s63, 66048\n"
      "s_add_u32 s52, s63, 73728\n"
      "v_add_u32 v33, 18432, v33\n"
      "s_waitcnt vmcnt(3)\n"
      "s_barrier\n"
      "v_mfma_f32_32x32x16_bf16 v[68:83], a[168:171], a[156:159], 0\n"
      "buffer_load_dwordx4 v31, s[12:15], s52 offen offset:0 lds\n"
      "ds_read_b64_tr_b16 a[144:145], v32 offset:0\n"
      "ds_read_b64_tr_b16 a[146:147], v32 offset:768\n"
      "ds_read_b64_tr_b16 a[12:13], v33 offset:0\n"
      "v_cvt_pk_bf16_f32 v148, v52, v56\n"
      "v_cvt_pk_bf16_f32 v149, v53, v57\n"
      "s_waitcnt lgkmcnt(4)\n"
      "v_mfma_f32_32x32x16_bf16 v[68:83], a[172:175], a[160:163], v[68:83]\n"
      "buffer_load_dwordx4 v31, s[12:15], s52 offen offset:1024 lds\n"
      "ds_read_b64_tr_b16 a[148:149], v32 offset:6144\n"
      "ds_read_b64_tr_b16 a[150:151], v32 offset:6912\n"
      "ds_read_b64_tr_b16 a[14:15], v33 offset:768\n"
      "ds_read_b64_tr_b16 a[16:17], v33 offset:64\n"
      "v_cvt_pk_bf16_f32 v150, v54, v58\n"
      "v_cvt_pk_bf16_f32 v151, v55, v59\n"
      "v_mfma_f32_32x32x16_bf16 v[68:83], a[176:179], a[164:167], v[68:83]\n"
      "buffer_load_dwordx4 v31, s[12:15], s52 offen offset:2048 lds\n"
      "ds_read_b64_tr_b16 a[152:153], v32 offset:12288\n"
      "ds_read_b64_tr_b16 a[154:155], v32 offset:13056\n"
      "ds_read_b64_tr_b16 a[18:19], v33 offset:832\n"
      "v_cvt_pk_bf16_f32 v44, v60, v64\n"
      "v_cvt_pk_bf16_f32 v220, v84, v88\n"
      "v_cvt_pk_bf16_f32 v45, v61, v65\n"
      "v_mfma_f32_4x4x4_16b_bf16 v[92:95], a[238:239], a[132:133], 0\n"
      "ds_read_b64_tr_b16 a[156:157], v34 offset:0\n"
      "v_mfma_f32_4x4x4_16b_bf16 v[96:99], a[238:239], a[134:135], 0\n"
      "ds_read_b64_tr_b16 a[158:159], v34 offset:768\n"
      "v_mfma_f32_4x4x4_16b_bf16 v[92:95], a[240:241], a[136:137], v[92:95]\n"
      "ds_read_b64_tr_b16 a[160:161], v34 offset:6144\n"
      "v_mfma_f32_4x4x4_16b_bf16 v[96:99], a[240:241], a[138:139], v[96:99]\n"
      "ds_read_b64_tr_b16 a[162:163], v34 offset:6912\n"
      "ds_read_b64_tr_b16 a[20:21], v33 offset:128\n"
      "v_mfma_f32_4x4x4_16b_bf16 v[92:95], a[254:255], a[140:141], v[92:95]\n"
      "ds_read_b64_tr_b16 a[164:165], v34 offset:12288\n"
      "s_nop 0\n"
      "s_waitcnt lgkmcnt(8)\n"
      "v_mfma_f32_4x4x4_16b_bf16 v[96:99], a[254:255], a[142:143], v[96:99]\n"
      "ds_read_b64_tr_b16 a[166:167], v34 offset:13056\n"
      "s_add_u32 m0, s63, 72192\n"
      "s_add_u32 s52, s63, 129024\n"
      "v_cvt_pk_bf16_f32 v46, v62, v66\n"
      "v_cvt_pk_bf16_f32 v47, v63, v67\n"
      "v_mfma_f32_32x32x16_bf16 v[52:67], a[168:171], a[144:147], 0\n"
      "buffer_load_dwordx4 v31, s[12:15], s52 offen offset:0 lds\n"
      "ds_read_b64_tr_b16 a[132:133], v32 offset:3072\n"
      "ds_read_b64_tr_b16 a[134:135], v32 offset:3840\n"
      "ds_read_b128 a[224:227], v3 offset:29184\n"
      "v_cvt_pk_bf16_f32 v184, v68, v72\n"
      "v_cvt_pk_bf16_f32 v221, v85, v89\n"
      "v_cvt_pk_bf16_f32 v185, v69, v73\n"
      "v_mfma_f32_32x32x16_bf16 v[52:67], a[172:175], a[148:151], v[52:67]\n"
      "buffer_load_dwordx4 v31, s[12:15], s52 offen offset:1024 lds\n"
      "ds_read_b64_tr_b16 a[136:137], v32 offset:9216\n"
      "ds_read_b64_tr_b16 a[138:139], v32 offset:9984\n"
      "ds_read_b64_tr_b16 a[22:23], v33 offset:896\n"
      "v_cvt_pk_bf16_f32 v186, v70, v74\n"
      "v_cvt_pk_bf16_f32 v187, v71, v75\n"
      "s_nop 0\n"
      "s_waitcnt lgkmcnt(6)\n"
      "v_mfma_f32_32x32x16_bf16 v[52:67], a[176:179], a[152:155], v[52:67]\n"
      "buffer_load_dwordx4 v31, s[12:15], s52 offen offset:2048 lds\n"
      "ds_read_b64_tr_b16 a[140:141], v32 offset:15360\n"
      "ds_read_b64_tr_b16 a[142:143], v32 offset:16128\n"
      "ds_read_b128 a[228:231], v3 offset:32256\n"
      "v_subrev_u32 v32, 36864, v32\n"
      "v_cvt_pk_bf16_f32 v222, v86, v90\n"
      "v_cvt_pk_bf16_f32 v223, v87, v91\n"
      "v_mfma_f32_4x4x4_16b_bf16 v[84:87], a[238:239], a[156:157], 0\n"
      "ds_read_b64_tr_b16 a[144:145], v34 offset:3072\n"
      "v_mfma_f32_4x4x4_16b_bf16 v[88:91], a[238:239], a[158:159], 0\n"
      "ds_read_b64_tr_b16 a[146:147], v34 offset:3840\n"
      "v_mfma_f32_4x4x4_16b_bf16 v[84:87], a[240:241], a[160:161], v[84:87]\n"
      "ds_read_b64_tr_b16 a[148:149], v34 offset:9216\n"
      "v_mfma_f32_4x4x4_16b_bf16 v[88:91], a[240:241], a[162:163], v[88:91]\n"
      "ds_read_b64_tr_b16 a[150:151], v34 offset:9984\n"
      "v_mfma_f32_4x4x4_16b_bf16 v[84:87], a[254:255], a[164:165], v[84:87]\n"
      "ds_read_b64_tr_b16 a[152:153], v34 offset:15360\n"
      "v_mfma_f32_4x4x4_16b_bf16 v[88:91], a[254:255], a[166:167], v[88:91]\n"
      "ds_read_b64_tr_b16 a[154:155], v34 offset:16128\n"
      "v_subrev_u32 v34, 36864, v34\n"
      "s_add_u32 m0, s63, 148992\n"
      "s_add_u32 s52, s63, 135168\n"
      "v_cvt_pk_bf16_f32 v100, v76, v80\n"
      "v_cvt_pk_bf16_f32 v101, v77, v81\n"
      "v_cvt_pk_bf16_f32 v102, v78, v82\n"
      "v_cvt_pk_bf16_f32 v103, v79, v83\n"
      "v_subrev_u32 v33, 36864, v33\n"
      "s_waitcnt vmcnt(6)\n"
      "s_barrier\n"
      "s_nop 0\n"
      "s_waitcnt lgkmcnt(2)\n"
      "v_mfma_f32_32x32x16_bf16 v[68:83], a[168:171], a[132:135], 0\n"
      "buffer_load_dwordx4 v31, s[12:15], s52 offen offset:0 lds\n"
      "ds_read_b64_tr_b16 a[156:157], v32 offset:0\n"
      "ds_read_b64_tr_b16 a[158:159], v32 offset:768\n"
      "ds_read_b64_tr_b16 a[24:25], v33 offset:0\n"
      "v_cvt_pk_bf16_f32 v92, v92, v96\n"
      "v_mfma_f32_32x32x16_bf16 v[68:83], a[172:175], a[136:139], v[68:83]\n"
      "buffer_load_dwordx4 v31, s[12:15], s52 offen offset:1024 lds\n"
      "ds_read_b64_tr_b16 a[160:161], v32 offset:6144\n"
      "ds_read_b64_tr_b16 a[162:163], v32 offset:6912\n"
      "v_cvt_pk_bf16_f32 v152, v52, v56\n"
      "v_cvt_pk_bf16_f32 v93, v93, v97\n"
      "v_cvt_pk_bf16_f32 v153, v53, v57\n"
      "v_mfma_f32_32x32x16_bf16 v[68:83], a[176:179], a[140:143], v[68:83]\n"
      "buffer_load_dwordx4 v31, s[12:15], s52 offen offset:2048 lds\n"
      "ds_read_b64_tr_b16 a[164:165], v32 offset:12288\n"
      "ds_read_b64_tr_b16 a[166:167], v32 offset:13056\n"
      "s_add_u32 m0, s63, 113664\n"
      "s_add_u32 s52, s63, 86016\n"
      "v_mfma_f32_16x16x32_bf16 v[124:127], a[0:3], a[224:227], 0\n"
      "buffer_load_dwordx4 v31, s[12:15], s52 offen offset:0 lds\n"
      "ds_read_b64_tr_b16 a[26:27], v33 offset:768\n"
      "v_mfma_f32_16x16x32_bf16 v[128:131], a[4:7], a[224:227], 0\n"
      "v_cvt_pk_bf16_f32 v94, v94, v98\n"
      "v_cvt_pk_bf16_f32 v154, v54, v58\n"
      "v_cvt_pk_bf16_f32 v95, v95, v99\n"
      "v_cvt_pk_bf16_f32 v224, v84, v88\n"
      "v_mfma_f32_16x16x32_bf16 v[132:135], a[8:11], a[224:227], 0\n"
      "buffer_load_dwordx4 v31, s[12:15], s52 offen offset:1024 lds\n"
      "v_mfma_f32_16x16x32_bf16 v[136:139], a[0:3], a[228:231], 0\n"
      "ds_read_b64_tr_b16 a[28:29], v33 offset:64\n"
      "v_cvt_pk_bf16_f32 v155, v55, v59\n"
      "v_accvgpr_write_b32 a180, v92\n"
      "v_cvt_pk_bf16_f32 v48, v60, v64\n"
      "v_mfma_f32_16x16x32_bf16 v[140:143], a[4:7], a[228:231], 0\n"
      "buffer_load_dwordx4 v31, s[12:15], s52 offen offset:2048 lds\n"
      "v_mfma_f32_16x16x32_bf16 v[144:147], a[8:11], a[228:231], 0\n"
      "ds_read_b64_tr_b16 a[30:31], v33 offset:832\n"
      "v_accvgpr_write_b32 a181, v93\n"
      "v_accvgpr_write_b32 a182, v94\n"
      "v_accvgpr_write_b32 a183, v95\n"
      "v_mfma_f32_4x4x4_16b_bf16 v[92:95], a[238:239], a[144:145], 0\n"
      "ds_read_b64_tr_b16 a[132:133], v34 offset:0\n"
      "v_mfma_f32_4x4x4_16b_bf16 v[96:99], a[238:239], a[146:147], 0\n"
      "ds_read_b64_tr_b16 a[134:135], v34 offset:768\n"
      "v_mfma_f32_4x4x4_16b_bf16 v[92:95], a[240:241], a[148:149], v[92:95]\n"
      "ds_read_b64_tr_b16 a[136:137], v34 offset:6144\n"
      "v_mfma_f32_4x4x4_16b_bf16 v[96:99], a[240:241], a[150:151], v[96:99]\n"
      "ds_read_b64_tr_b16 a[138:139], v34 offset:6912\n"
      "s_nop 0\n"
      "s_waitcnt lgkmcnt(5)\n"
      "v_mfma_f32_4x4x4_16b_bf16 v[92:95], a[254:255], a[152:153], v[92:95]\n"
      "ds_read_b64_tr_b16 a[140:141], v34 offset:12288\n"
      "v_mfma_f32_4x4x4_16b_bf16 v[96:99], a[254:255], a[154:155], v[96:99]\n"
      "ds_read_b64_tr_b16 a[142:143], v34 offset:13056\n"
      "s_add_u32 m0, s63, 119808\n"
      "s_add_u32 s52, s63, 141312\n"
      "v_cvt_pk_bf16_f32 v49, v61, v65\n"
      "v_cvt_pk_bf16_f32 v50, v62, v66\n"
      "v_cvt_pk_bf16_f32 v51, v63, v67\n"
      "v_mfma_f32_32x32x16_bf16 v[52:67], a[168:171], a[156:159], 0\n"
      "buffer_load_dwordx4 v31, s[12:15], s52 offen offset:0 lds\n"
      "ds_read_b64_tr_b16 a[144:145], v32 offset:3072\n"
      "ds_read_b64_tr_b16 a[146:147], v32 offset:3840\n"
      "ds_read_b64_tr_b16 a[32:33], v33 offset:128\n"
      "ds_read_b128 a[216:219], v4 offset:29184\n"
      "v_cvt_pk_bf16_f32 v188, v68, v72\n"
      "v_cvt_pk_bf16_f32 v225, v85, v89\n"
      "v_mfma_f32_32x32x16_bf16 v[52:67], a[172:175], a[160:163], v[52:67]\n"
      "buffer_load_dwordx4 v31, s[12:15], s52 offen offset:1024 lds\n"
      "ds_read_b64_tr_b16 a[148:149], v32 offset:9216\n"
      "ds_read_b64_tr_b16 a[150:151], v32 offset:9984\n"
      "ds_read_b64_tr_b16 a[34:35], v33 offset:896\n"
      "v_cvt_pk_bf16_f32 v189, v69, v73\n"
      "v_cvt_pk_bf16_f32 v190, v70, v74\n"
      "v_cvt_pk_bf16_f32 v191, v71, v75\n"
      "v_mfma_f32_32x32x16_bf16 v[52:67], a[176:179], a[164:167], v[52:67]\n"
      "buffer_load_dwordx4 v31, s[12:15], s52 offen offset:2048 lds\n"
      "ds_read_b64_tr_b16 a[152:153], v32 offset:15360\n"
      "ds_read_b64_tr_b16 a[154:155], v32 offset:16128\n"
      "ds_read_b128 a[220:223], v4 offset:32256\n"
      "v_add_u32 v32, 59904, v32\n"
      "v_cvt_pk_bf16_f32 v226, v86, v90\n"
      "v_cvt_pk_bf16_f32 v227, v87, v91\n"
      "s_nop 0\n"
      "s_waitcnt lgkmcnt(6)\n"
      "v_mfma_f32_4x4x4_16b_bf16 v[84:87], a[238:239], a[132:133], 0\n"
      "ds_read_b64_tr_b16 a[156:157], v34 offset:3072\n"
      "v_mfma_f32_4x4x4_16b_bf16 v[88:91], a[238:239], a[134:135], 0\n"
      "ds_read_b64_tr_b16 a[158:159], v34 offset:3840\n"
      "v_mfma_f32_4x4x4_16b_bf16 v[84:87], a[240:241], a[136:137], v[84:87]\n"
      "ds_read_b64_tr_b16 a[160:161], v34 offset:9216\n"
      "v_mfma_f32_4x4x4_16b_bf16 v[88:91], a[240:241], a[138:139], v[88:91]\n"
      "ds_read_b64_tr_b16 a[162:163], v34 offset:9984\n"
      "v_mfma_f32_4x4x4_16b_bf16 v[84:87], a[254:255], a[140:141], v[84:87]\n"
      "ds_read_b64_tr_b16 a[164:165], v34 offset:15360\n"
      "v_mfma_f32_4x4x4_16b_bf16 v[88:91], a[254:255], a[142:143], v[88:91]\n"
      "ds_read_b64_tr_b16 a[166:167], v34 offset:16128\n"
      "v_add_u32 v34, 59904, v34\n"
      "v_cvt_pk_bf16_f32 v104, v76, v80\n"
      "v_cvt_pk_bf16_f32 v105, v77, v81\n"
      "v_cvt_pk_bf16_f32 v106, v78, v82\n"
      "v_cvt_pk_bf16_f32 v107, v79, v83\n"
      "v_add_u32 v33, 59904, v33\n"
      "s_waitcnt vmcnt(9)\n"
      "s_barrier\n"
      "v_mfma_f32_32x32x16_bf16 v[68:83], a[168:171], a[144:147], 0\n"
      "ds_read_b64_tr_b16 a[132:133], v32 offset:0\n"
      "ds_read_b64_tr_b16 a[134:135], v32 offset:768\n"
      "ds_read_b64_tr_b16 a[36:37], v33 offset:0\n"
      "v_cvt_pk_bf16_f32 v92, v92, v96\n"
      "v_cvt_pk_bf16_f32 v156, v52, v56\n"
      "s_nop 0\n"
      "s_waitcnt lgkmcnt(3)\n"
      "v_mfma_f32_32x32x16_bf16 v[68:83], a[172:175], a[148:151], v[68:83]\n"
      "ds_read_b64_tr_b16 a[136:137], v32 offset:6144\n"
      "ds_read_b64_tr_b16 a[138:139], v32 offset:6912\n"
      "ds_read_b64_tr_b16 a[38:39], v33 offset:768\n"
      "v_cvt_pk_bf16_f32 v93, v93, v97\n"
      "v_cvt_pk_bf16_f32 v157, v53, v57\n"
      "v_cvt_pk_bf16_f32 v228, v84, v88\n"
      "v_mfma_f32_32x32x16_bf16 v[68:83], a[176:179], a[152:155], v[68:83]\n"
      "ds_read_b64_tr_b16 a[140:141], v32 offset:12288\n"
      "ds_read_b64_tr_b16 a[142:143], v32 offset:13056\n"
      "ds_read_b64_tr_b16 a[40:41], v33 offset:64\n"
      "v_cvt_pk_bf16_f32 v94, v94, v98\n"
      "v_cvt_pk_bf16_f32 v158, v54, v58\n"
      "v_cvt_pk_bf16_f32 v95, v95, v99\n"
      "v_mfma_f32_16x16x32_bf16 v[124:127], a[12:15], a[216:219], v[124:127]\n"
      "ds_read_b64_tr_b16 a[42:43], v33 offset:832\n"
      "v_cvt_pk_bf16_f32 v159, v55, v59\n"
      "v_mfma_f32_16x16x32_bf16 v[128:131], a[16:19], a[216:219], v[128:131]\n"
      "ds_read_b64_tr_b16 a[44:45], v33 offset:128\n"
      "v_cvt_pk_bf16_f32 v229, v85, v89\n"
      "v_mfma_f32_16x16x32_bf16 v[132:135], a[20:23], a[216:219], v[132:135]\n"
      "v_accvgpr_write_b32 a184, v92\n"
      "v_cvt_pk_bf16_f32 v36, v60, v64\n"
      "v_mfma_f32_16x16x32_bf16 v[136:139], a[12:15], a[220:223], v[136:139]\n"
      "ds_read_b64_tr_b16 a[46:47], v33 offset:896\n"
      "v_accvgpr_write_b32 a185, v93\n"
      "v_mfma_f32_16x16x32_bf16 v[140:143], a[16:19], a[220:223], v[140:143]\n"
      "v_cvt_pk_bf16_f32 v37, v61, v65\n"
      "v_cvt_pk_bf16_f32 v230, v86, v90\n"
      "v_mfma_f32_16x16x32_bf16 v[144:147], a[20:23], a[220:223], v[144:147]\n"
      "v_accvgpr_write_b32 a186, v94\n"
      "v_accvgpr_write_b32 a187, v95\n"
      "v_mfma_f32_4x4x4_16b_bf16 v[92:95], a[238:239], a[156:157], 0\n"
      "ds_read_b64_tr_b16 a[144:145], v34 offset:0\n"
      "v_mfma_f32_4x4x4_16b_bf16 v[96:99], a[238:239], a[158:159], 0\n"
      "ds_read_b64_tr_b16 a[146:147], v34 offset:768\n"
      "v_mfma_f32_4x4x4_16b_bf16 v[92:95], a[240:241], a[160:161], v[92:95]\n"
      "ds_read_b64_tr_b16 a[148:149], v34 offset:6144\n"
      "v_mfma_f32_4x4x4_16b_bf16 v[96:99], a[240:241], a[162:163], v[96:99]\n"
      "ds_read_b64_tr_b16 a[150:151], v34 offset:6912\n"
      "v_mfma_f32_4x4x4_16b_bf16 v[92:95], a[254:255], a[164:165], v[92:95]\n"
      "ds_read_b64_tr_b16 a[152:153], v34 offset:12288\n"
      "v_mfma_f32_4x4x4_16b_bf16 v[96:99], a[254:255], a[166:167], v[96:99]\n"
      "ds_read_b64_tr_b16 a[154:155], v34 offset:13056\n"
      "v_cvt_pk_bf16_f32 v38, v62, v66\n"
      "v_cvt_pk_bf16_f32 v39, v63, v67\n"
      "v_cvt_pk_bf16_f32 v192, v68, v72\n"
      "v_cvt_pk_bf16_f32 v231, v87, v91\n"
      "v_add_u32 v33, 76800, v33\n"
      "s_nop 0\n"
      "s_waitcnt lgkmcnt(6)\n"
      "v_mfma_f32_32x32x16_bf16 v[52:67], a[168:171], a[132:135], 0\n"
      "ds_read_b64_tr_b16 a[156:157], v32 offset:3072\n"
      "ds_read_b64_tr_b16 a[158:159], v32 offset:3840\n"
      "ds_read_b128 a[224:227], v5 offset:29184\n"
      "v_cvt_pk_bf16_f32 v193, v69, v73\n"
      "v_cvt_pk_bf16_f32 v92, v92, v96\n"
      "v_cvt_pk_bf16_f32 v194, v70, v74\n"
      "v_cvt_pk_bf16_f32 v93, v93, v97\n"
      "v_cvt_pk_bf16_f32 v195, v71, v75\n"
      "v_cvt_pk_bf16_f32 v94, v94, v98\n"
      "v_cvt_pk_bf16_f32 v108, v76, v80\n"
      "v_mfma_f32_32x32x16_bf16 v[52:67], a[172:175], a[136:139], v[52:67]\n"
      "ds_read_b64_tr_b16 a[160:161], v32 offset:9216\n"
      "ds_read_b64_tr_b16 a[162:163], v32 offset:9984\n"
      "s_mov_b32 m0, s64\n"
      "s_nop 0\n"
      "ds_write_addtid_b32 v44 offset:20480\n"
      "ds_write_addtid_b32 v45 offset:20992\n"
      "ds_write_addtid_b32 v46 offset:21504\n"
      "ds_write_addtid_b32 v47 offset:22016\n"
      "ds_write_addtid_b32 v48 offset:22528\n"
      "ds_write_addtid_b32 v49 offset:23040\n"
      "ds_write_addtid_b32 v50 offset:23552\n"
      "ds_write_addtid_b32 v51 offset:24064\n"
      "v_mfma_f32_32x32x16_bf16 v[52:67], a[176:179], a[140:143], v[52:67]\n"
      "ds_read_b64_tr_b16 a[164:165], v32 offset:15360\n"
      "ds_read_b64_tr_b16 a[166:167], v32 offset:16128\n"
      "ds_write_addtid_b32 v36 offset:24576\n"
      "ds_write_addtid_b32 v37 offset:25088\n"
      "ds_write_addtid_b32 v38 offset:25600\n"
      "ds_write_addtid_b32 v39 offset:26112\n"
      "ds_read_b128 a[228:231], v5 offset:32256\n"
      "v_add_u32 v32, 76800, v32\n"
      "s_nop 0\n"
      "s_waitcnt lgkmcnt(15)\n"
      "v_mfma_f32_4x4x4_16b_bf16 v[84:87], a[238:239], a[144:145], 0\n"
      "ds_read_b64_tr_b16 a[132:133], v34 offset:3072\n"
      "v_mfma_f32_4x4x4_16b_bf16 v[88:91], a[238:239], a[146:147], 0\n"
      "ds_read_b64_tr_b16 a[134:135], v34 offset:3840\n"
      "v_mfma_f32_4x4x4_16b_bf16 v[84:87], a[240:241], a[148:149], v[84:87]\n"
      "ds_read_b64_tr_b16 a[136:137], v34 offset:9216\n"
      "v_mfma_f32_4x4x4_16b_bf16 v[88:91], a[240:241], a[150:151], v[88:91]\n"
      "ds_read_b64_tr_b16 a[138:139], v34 offset:9984\n"
      "v_mfma_f32_4x4x4_16b_bf16 v[84:87], a[254:255], a[152:153], v[84:87]\n"
      "ds_read_b64_tr_b16 a[140:141], v34 offset:15360\n"
      "v_mfma_f32_4x4x4_16b_bf16 v[88:91], a[254:255], a[154:155], v[88:91]\n"
      "ds_read_b64_tr_b16 a[142:143], v34 offset:16128\n"
      "v_add_u32 v34, 76800, v34\n"
      "s_add_u32 m0, s63, 0\n"
      "s_add_u32 s52, s63, 36864\n"
      "v_cvt_pk_bf16_f32 v109, v77, v81\n"
      "v_cvt_pk_bf16_f32 v110, v78, v82\n"
      "v_cvt_pk_bf16_f32 v111, v79, v83\n"
      "s_waitcnt lgkmcnt(5)\n"
      "s_waitcnt vmcnt(6)\n"
      "s_barrier\n"
      "v_mfma_f32_32x32x16_bf16 v[68:83], a[168:171], a[156:159], 0\n"
      "buffer_load_dwordx4 v31, s[12:15], s52 offen offset:0 lds\n"
      "ds_read_b64_tr_b16 a[144:145], v32 offset:0\n"
      "ds_read_b64_tr_b16 a[146:147], v32 offset:768\n"
      "v_cvt_pk_bf16_f32 v95, v95, v99\n"
      "v_cvt_pk_bf16_f32 v160, v52, v56\n"
      "v_accvgpr_write_b32 a188, v92\n"
      "v_cvt_pk_bf16_f32 v161, v53, v57\n"
      "v_mfma_f32_32x32x16_bf16 v[68:83], a[172:175], a[160:163], v[68:83]\n"
      "buffer_load_dwordx4 v31, s[12:15], s52 offen offset:1024 lds\n"
      "ds_read_b64_tr_b16 a[148:149], v32 offset:6144\n"
      "ds_read_b64_tr_b16 a[150:151], v32 offset:6912\n"
      "ds_read_b64_tr_b16 a[48:49], v33 offset:0\n"
      "ds_read_b64_tr_b16 a[50:51], v33 offset:768\n"
      "v_cvt_pk_bf16_f32 v162, v54, v58\n"
      "v_cvt_pk_bf16_f32 v232, v84, v88\n"
      "v_mfma_f32_32x32x16_bf16 v[68:83], a[176:179], a[164:167], v[68:83]\n"
      "buffer_load_dwordx4 v31, s[12:15], s52 offen offset:2048 lds\n"
      "ds_read_b64_tr_b16 a[152:153], v32 offset:12288\n"
      "ds_read_b64_tr_b16 a[154:155], v32 offset:13056\n"
      "s_add_u32 m0, s63, 6144\n"
      "s_add_u32 s52, s63, 92160\n"
      "v_accvgpr_write_b32 a189, v93\n"
      "v_mfma_f32_16x16x32_bf16 v[124:127], a[24:27], a[224:227], v[124:127]\n"
      "buffer_load_dwordx4 v31, s[12:15], s52 offen offset:0 lds\n"
      "v_mfma_f32_16x16x32_bf16 v[128:131], a[28:31], a[224:227], v[128:131]\n"
      "ds_read_b64_tr_b16 a[52:53], v33 offset:64\n"
      "ds_read2st64_b32 a[108:109], v35 offset0:80 offset1:82\n"
      "ds_read2st64_b32 a[112:113], v35 offset0:88 offset1:90\n"
      "ds_read2st64_b32 a[116:117], v35 offset0:96 offset1:98\n"
      "v_cvt_pk_bf16_f32 v163, v55, v59\n"
      "v_mfma_f32_16x16x32_bf16 v[132:135], a[32:35], a[224:227], v[132:135]\n"
      "buffer_load_dwordx4 v31, s[12:15], s52 offen offset:1024 lds\n"
      "v_mfma_f32_16x16x32_bf16 v[136:139], a[24:27], a[228:231], v[136:139]\n"
      "ds_read_b64_tr_b16 a[54:55], v33 offset:832\n"
      "v_cvt_pk_bf16_f32 v40, v60, v64\n"
      "v_cvt_pk_bf16_f32 v233, v85, v89\n"
      "v_cvt_pk_bf16_f32 v41, v61, v65\n"
      "v_mfma_f32_16x16x32_bf16 v[140:143], a[28:31], a[228:231], v[140:143]\n"
      "buffer_load_dwordx4 v31, s[12:15], s52 offen offset:2048 lds\n"
      "ds_read_b64_tr_b16 a[56:57], v33 offset:128\n"
      "v_mfma_f32_16x16x32_bf16 v[144:147], a[32:35], a[228:231], v[144:147]\n"
      "ds_read2st64_b32 a[110:111], v35 offset0:84 offset1:86\n"
      "ds_read2st64_b32 a[114:115], v35 offset0:92 offset1:94\n"
      "ds_read2st64_b32 a[118:119], v35 offset0:100 offset1:102\n"
      "ds_read_b64_tr_b16 a[58:59], v33 offset:896\n"
      "v_accvgpr_write_b32 a190, v94\n"
      "v_accvgpr_write_b32 a191, v95\n"
      "v_mfma_f32_4x4x4_16b_bf16 v[92:95], a[238:239], a[132:133], 0\n"
      "ds_read_b64_tr_b16 a[156:157], v34 offset:0\n"
      "s_waitcnt lgkmcnt(6)\n"
      "v_mfma_f32_4x4x4_16b_bf16 v[96:99], a[238:239], a[134:135], 0\n"
      "ds_read_b64_tr_b16 a[158:159], v34 offset:768\n"
      "v_mfma_f32_4x4x4_16b_bf16 v[92:95], a[240:241], a[136:137], v[92:95]\n"
      "ds_read_b64_tr_b16 a[160:161], v34 offset:6144\n"
      "v_mfma_f32_4x4x4_16b_bf16 v[96:99], a[240:241], a[138:139], v[96:99]\n"
      "ds_read_b64_tr_b16 a[162:163], v34 offset:6912\n"
      "v_mfma_f32_4x4x4_16b_bf16 v[92:95], a[254:255], a[140:141], v[92:95]\n"
      "ds_read_b64_tr_b16 a[164:165], v34 offset:12288\n"
      "v_mfma_f32_4x4x4_16b_bf16 v[96:99], a[254:255], a[142:143], v[96:99]\n"
      "ds_read_b64_tr_b16 a[166:167], v34 offset:13056\n"
      "s_add_u32 m0, s63, 12288\n"
      "s_add_u32 s52, s63, 147456\n"
      "v_cvt_pk_bf16_f32 v42, v62, v66\n"
      "v_cvt_pk_bf16_f32 v43, v63, v67\n"
      "s_mov_b32 m0, s64\n"
      "s_nop 0\n"
      "ds_write_addtid_b32 v40 offset:26624\n"
      "ds_write_addtid_b32 v41 offset:27136\n"
      "ds_write_addtid_b32 v42 offset:27648\n"
      "ds_write_addtid_b32 v43 offset:28160\n"
      "v_mfma_f32_32x32x16_bf16 v[52:67], a[168:171], a[144:147], 0\n"
      "s_add_u32 m0, s63, 12288\n"
      "s_nop 0\n"
      "buffer_load_dwordx4 v31, s[12:15], s52 offen offset:0 lds\n"
      "ds_read_b64_tr_b16 a[132:133], v32 offset:3072\n"
      "ds_read_b64_tr_b16 a[134:135], v32 offset:3840\n"
      "v_mfma_f32_32x32x16_bf16 v[52:67], a[172:175], a[148:151], v[52:67]\n"
      "buffer_load_dwordx4 v31, s[12:15], s52 offen offset:1024 lds\n"
      "ds_read_b64_tr_b16 a[136:137], v32 offset:9216\n"
      "ds_read_b64_tr_b16 a[138:139], v32 offset:9984\n"
      "ds_read_b128 a[216:219], v6 offset:29184\n"
      "v_cvt_pk_bf16_f32 v196, v68, v72\n"
      "v_cvt_pk_bf16_f32 v197, v69, v73\n"
      "v_cvt_pk_bf16_f32 v198, v70, v74\n"
      "v_cvt_pk_bf16_f32 v199, v71, v75\n"
      "v_mfma_f32_32x32x16_bf16 v[52:67], a[176:179], a[152:155], v[52:67]\n"
      "buffer_load_dwordx4 v31, s[12:15], s52 offen offset:2048 lds\n"
      "ds_read_b64_tr_b16 a[140:141], v32 offset:15360\n"
      "ds_read_b64_tr_b16 a[142:143], v32 offset:16128\n"
      "ds_read_b128 a[220:223], v6 offset:32256\n"
      "v_subrev_u32 v32, 29184, v32\n"
      "v_cvt_pk_bf16_f32 v234, v86, v90\n"
      "v_cvt_pk_bf16_f32 v235, v87, v91\n"
      "s_waitcnt lgkmcnt(6)\n"
      "v_mfma_f32_4x4x4_16b_bf16 v[84:87], a[238:239], a[156:157], 0\n"
      "ds_read_b64_tr_b16 a[144:145], v34 offset:3072\n"
      "v_mfma_f32_4x4x4_16b_bf16 v[88:91], a[238:239], a[158:159], 0\n"
      "ds_read_b64_tr_b16 a[146:147], v34 offset:3840\n"
      "v_mfma_f32_4x4x4_16b_bf16 v[84:87], a[240:241], a[160:161], v[84:87]\n"
      "ds_read_b64_tr_b16 a[148:149], v34 offset:9216\n"
      "v_mfma_f32_4x4x4_16b_bf16 v[88:91], a[240:241], a[162:163], v[88:91]\n"
      "ds_read_b64_tr_b16 a[150:151], v34 offset:9984\n"
      "v_mfma_f32_4x4x4_16b_bf16 v[84:87], a[254:255], a[164:165], v[84:87]\n"
      "ds_read_b64_tr_b16 a[152:153], v34 offset:15360\n"
      "v_mfma_f32_4x4x4_16b_bf16 v[88:91], a[254:255], a[166:167], v[88:91]\n"
      "ds_read_b64_tr_b16 a[154:155], v34 offset:16128\n"
      "v_subrev_u32 v34, 29184, v34\n"
      "s_add_u32 m0, s63, 59904\n"
      "s_add_u32 s52, s63, 43008\n"
      "v_cvt_pk_bf16_f32 v112, v76, v80\n"
      "v_cvt_pk_bf16_f32 v113, v77, v81\n"
      "v_cvt_pk_bf16_f32 v114, v78, v82\n"
      "v_cvt_pk_bf16_f32 v115, v79, v83\n"
      "v_subrev_u32 v33, 29184, v33\n"
      "s_waitcnt vmcnt(9)\n"
      "s_barrier\n"
      "v_mfma_f32_32x32x16_bf16 v[68:83], a[168:171], a[132:135], 0\n"
      "buffer_load_dwordx4 v31, s[12:15], s52 offen offset:0 lds\n"
      "ds_read_b64_tr_b16 a[156:157], v32 offset:0\n"
      "ds_read_b64_tr_b16 a[158:159], v32 offset:768\n"
      "ds_read_b64_tr_b16 a[60:61], v33 offset:0\n"
      "s_waitcnt lgkmcnt(3)\n"
      "v_mfma_f32_32x32x16_bf16 v[68:83], a[172:175], a[136:139], v[68:83]\n"
      "buffer_load_dwordx4 v31, s[12:15], s52 offen offset:1024 lds\n"
      "ds_read_b64_tr_b16 a[160:161], v32 offset:6144\n"
      "ds_read_b64_tr_b16 a[162:163], v32 offset:6912\n"
      "v_cvt_pk_bf16_f32 v92, v92, v96\n"
      "v_cvt_pk_bf16_f32 v164, v52, v56\n"
      "v_cvt_pk_bf16_f32 v93, v93, v97\n"
      "v_cvt_pk_bf16_f32 v165, v53, v57\n"
      "v_mfma_f32_32x32x16_bf16 v[68:83], a[176:179], a[140:143], v[68:83]\n"
      "buffer_load_dwordx4 v31, s[12:15], s52 offen offset:2048 lds\n"
      "ds_read_b64_tr_b16 a[164:165], v32 offset:12288\n"
      "ds_read_b64_tr_b16 a[166:167], v32 offset:13056\n"
      "ds_read_b64_tr_b16 a[62:63], v33 offset:768\n"
      "s_add_u32 m0, s63, 66048\n"
      "s_add_u32 s52, s63, 98304\n"
      "v_mfma_f32_16x16x32_bf16 v[124:127], a[36:39], a[216:219], v[124:127]\n"
      "buffer_load_dwordx4 v31, s[12:15], s52 offen offset:0 lds\n"
      "v_mfma_f32_16x16x32_bf16 v[128:131], a[40:43], a[216:219], v[128:131]\n"
      "ds_read2st64_b32 a[120:121], v35 offset0:104 offset1:106\n"
      "v_cvt_pk_bf16_f32 v94, v94, v98\n"
      "v_cvt_pk_bf16_f32 v166, v54, v58\n"
      "v_cvt_pk_bf16_f32 v95, v95, v99\n"
      "v_mfma_f32_16x16x32_bf16 v[132:135], a[44:47], a[216:219], v[132:135]\n"
      "buffer_load_dwordx4 v31, s[12:15], s52 offen offset:1024 lds\n"
      "v_cvt_pk_bf16_f32 v236, v84, v88\n"
      "v_mfma_f32_16x16x32_bf16 v[136:139], a[36:39], a[220:223], v[136:139]\n"
      "ds_read_b64_tr_b16 a[64:65], v33 offset:64\n"
      "v_cvt_pk_bf16_f32 v167, v55, v59\n"
      "v_accvgpr_write_b32 a192, v92\n"
      "v_cvt_pk_bf16_f32 v44, v60, v64\n"
      "v_mfma_f32_16x16x32_bf16 v[140:143], a[40:43], a[220:223], v[140:143]\n"
      "buffer_load_dwordx4 v31, s[12:15], s52 offen offset:2048 lds\n"
      "v_mfma_f32_16x16x32_bf16 v[144:147], a[44:47], a[220:223], v[144:147]\n"
      "ds_read_b64_tr_b16 a[66:67], v33 offset:832\n"
      "v_accvgpr_write_b32 a193, v93\n"
      "v_accvgpr_write_b32 a194, v94\n"
      "v_accvgpr_write_b32 a195, v95\n"
      "v_mfma_f32_4x4x4_16b_bf16 v[92:95], a[238:239], a[144:145], 0\n"
      "ds_read_b64_tr_b16 a[132:133], v34 offset:0\n"
      "v_mfma_f32_4x4x4_16b_bf16 v[96:99], a[238:239], a[146:147], 0\n"
      "ds_read_b64_tr_b16 a[134:135], v34 offset:768\n"
      "v_mfma_f32_4x4x4_16b_bf16 v[92:95], a[240:241], a[148:149], v[92:95]\n"
      "ds_read_b64_tr_b16 a[136:137], v34 offset:6144\n"
      "v_mfma_f32_4x4x4_16b_bf16 v[96:99], a[240:241], a[150:151], v[96:99]\n"
      "ds_read_b64_tr_b16 a[138:139], v34 offset:6912\n"
      "v_mfma_f32_4x4x4_16b_bf16 v[92:95], a[254:255], a[152:153], v[92:95]\n"
      "ds_read_b64_tr_b16 a[140:141], v34 offset:12288\n"
      "v_mfma_f32_4x4x4_16b_bf16 v[96:99], a[254:255], a[154:155], v[96:99]\n"
      "ds_read_b64_tr_b16 a[142:143], v34 offset:13056\n"
      "s_add_u32 m0, s63, 72192\n"
      "s_add_u32 s52, s63, 153600\n"
      "v_cvt_pk_bf16_f32 v45, v61, v65\n"
      "v_cvt_pk_bf16_f32 v46, v62, v66\n"
      "v_cvt_pk_bf16_f32 v47, v63, v67\n"
      "s_nop 0\n"
      "s_waitcnt lgkmcnt(6)\n"
      "v_mfma_f32_32x32x16_bf16 v[52:67], a[168:171], a[156:159], 0\n"
      "buffer_load_dwordx4 v31, s[12:15], s52 offen offset:0 lds\n"
      "ds_read_b64_tr_b16 a[144:145], v32 offset:3072\n"
      "ds_read_b64_tr_b16 a[146:147], v32 offset:3840\n"
      "ds_read_b64_tr_b16 a[68:69], v33 offset:128\n"
      "ds_read_b128 a[224:227], v7 offset:29184\n"
      "v_cvt_pk_bf16_f32 v200, v68, v72\n"
      "v_cvt_pk_bf16_f32 v237, v85, v89\n"
      "v_cvt_pk_bf16_f32 v201, v69, v73\n"
      "v_mfma_f32_32x32x16_bf16 v[52:67], a[172:175], a[160:163], v[52:67]\n"
      "buffer_load_dwordx4 v31, s[12:15], s52 offen offset:1024 lds\n"
      "ds_read_b64_tr_b16 a[148:149], v32 offset:9216\n"
      "ds_read_b64_tr_b16 a[150:151], v32 offset:9984\n"
      "ds_read_b64_tr_b16 a[70:71], v33 offset:896\n"
      "ds_read2st64_b32 a[122:123], v35 offset0:108 offset1:110\n"
      "v_cvt_pk_bf16_f32 v202, v70, v74\n"
      "v_cvt_pk_bf16_f32 v203, v71, v75\n"
      "v_mfma_f32_32x32x16_bf16 v[52:67], a[176:179], a[164:167], v[52:67]\n"
      "buffer_load_dwordx4 v31, s[12:15], s52 offen offset:2048 lds\n"
      "ds_read_b64_tr_b16 a[152:153], v32 offset:15360\n"
      "ds_read_b64_tr_b16 a[154:155], v32 offset:16128\n"
      "ds_read_b128 a[228:231], v7 offset:32256\n"
      "v_subrev_u32 v32, 107520, v32\n"
      "v_cvt_pk_bf16_f32 v238, v86, v90\n"
      "v_cvt_pk_bf16_f32 v239, v87, v91\n"
      "s_nop 0\n"
      "s_waitcnt lgkmcnt(7)\n"
      "v_mfma_f32_4x4x4_16b_bf16 v[84:87], a[238:239], a[132:133], 0\n"
      "ds_read_b64_tr_b16 a[156:157], v34 offset:3072\n"
      "v_mfma_f32_4x4x4_16b_bf16 v[88:91], a[238:239], a[134:135], 0\n"
      "ds_read_b64_tr_b16 a[158:159], v34 offset:3840\n"
      "v_mfma_f32_4x4x4_16b_bf16 v[84:87], a[240:241], a[136:137], v[84:87]\n"
      "ds_read_b64_tr_b16 a[160:161], v34 offset:9216\n"
      "v_mfma_f32_4x4x4_16b_bf16 v[88:91], a[240:241], a[138:139], v[88:91]\n"
      "ds_read_b64_tr_b16 a[162:163], v34 offset:9984\n"
      "v_mfma_f32_4x4x4_16b_bf16 v[84:87], a[254:255], a[140:141], v[84:87]\n"
      "ds_read_b64_tr_b16 a[164:165], v34 offset:15360\n"
      "v_mfma_f32_4x4x4_16b_bf16 v[88:91], a[254:255], a[142:143], v[88:91]\n"
      "ds_read_b64_tr_b16 a[166:167], v34 offset:16128\n"
      "v_subrev_u32 v34, 107520, v34\n"
      "s_add_u32 m0, s63, 136704\n"
      "s_add_u32 s52, s63, 49152\n"
      "v_cvt_pk_bf16_f32 v116, v76, v80\n"
      "v_cvt_pk_bf16_f32 v117, v77, v81\n"
      "v_cvt_pk_bf16_f32 v118, v78, v82\n"
      "v_cvt_pk_bf16_f32 v119, v79, v83\n"
      "v_subrev_u32 v33, 107520, v33\n"
      "s_waitcnt vmcnt(9)\n"
      "s_barrier\n"
      "v_mfma_f32_32x32x16_bf16 v[68:83], a[168:171], a[144:147], 0\n"
      "buffer_load_dwordx4 v31, s[12:15], s52 offen offset:0 lds\n"
      "ds_read_b64_tr_b16 a[132:133], v32 offset:0\n"
      "ds_read_b64_tr_b16 a[134:135], v32 offset:768\n"
      "ds_read_b64_tr_b16 a[72:73], v33 offset:0\n"
      "s_nop 0\n"
      "s_waitcnt lgkmcnt(3)\n"
      "v_mfma_f32_32x32x16_bf16 v[68:83], a[172:175], a[148:151], v[68:83]\n"
      "buffer_load_dwordx4 v31, s[12:15], s52 offen offset:1024 lds\n"
      "ds_read_b64_tr_b16 a[136:137], v32 offset:6144\n"
      "ds_read_b64_tr_b16 a[138:139], v32 offset:6912\n"
      "v_cvt_pk_bf16_f32 v92, v92, v96\n"
      "v_cvt_pk_bf16_f32 v168, v52, v56\n"
      "v_cvt_pk_bf16_f32 v93, v93, v97\n"
      "v_cvt_pk_bf16_f32 v169, v53, v57\n"
      "v_mfma_f32_32x32x16_bf16 v[68:83], a[176:179], a[152:155], v[68:83]\n"
      "buffer_load_dwordx4 v31, s[12:15], s52 offen offset:2048 lds\n"
      "ds_read_b64_tr_b16 a[140:141], v32 offset:12288\n"
      "ds_read_b64_tr_b16 a[142:143], v32 offset:13056\n"
      "s_add_u32 m0, s63, 142848\n"
      "s_add_u32 s52, s63, 104448\n"
      "v_mfma_f32_16x16x32_bf16 v[124:127], a[48:51], a[224:227], v[124:127]\n"
      "buffer_load_dwordx4 v31, s[12:15], s52 offen offset:0 lds\n"
      "ds_read_b64_tr_b16 a[74:75], v33 offset:768\n"
      "v_mfma_f32_16x16x32_bf16 v[128:131], a[52:55], a[224:227], v[128:131]\n"
      "v_cvt_pk_bf16_f32 v94, v94, v98\n"
      "v_cvt_pk_bf16_f32 v170, v54, v58\n"
      "v_cvt_pk_bf16_f32 v95, v95, v99\n"
      "v_mfma_f32_16x16x32_bf16 v[132:135], a[56:59], a[224:227], v[132:135]\n"
      "buffer_load_dwordx4 v31, s[12:15], s52 offen offset:1024 lds\n"
      "v_cvt_pk_bf16_f32 v240, v84, v88\n"
      "v_mfma_f32_16x16x32_bf16 v[136:139], a[48:51], a[228:231], v[136:139]\n"
      "ds_read_b64_tr_b16 a[76:77], v33 offset:64\n"
      "v_cvt_pk_bf16_f32 v171, v55, v59\n"
      "v_accvgpr_write_b32 a196, v92\n"
      "v_cvt_pk_bf16_f32 v48, v60, v64\n"
      "v_mfma_f32_16x16x32_bf16 v[140:143], a[52:55], a[228:231], v[140:143]\n"
      "buffer_load_dwordx4 v31, s[12:15], s52 offen offset:2048 lds\n"
      "v_mfma_f32_16x16x32_bf16 v[144:147], a[56:59], a[228:231], v[144:147]\n"
      "ds_read_b64_tr_b16 a[78:79], v33 offset:832\n"
      "v_accvgpr_write_b32 a197, v93\n"
      "v_accvgpr_write_b32 a198, v94\n"
      "v_accvgpr_write_b32 a199, v95\n"
      "v_mfma_f32_4x4x4_16b_bf16 v[92:95], a[238:239], a[156:157], 0\n"
      "ds_read_b64_tr_b16 a[144:145], v34 offset:0\n"
      "v_mfma_f32_4x4x4_16b_bf16 v[96:99], a[238:239], a[158:159], 0\n"
      "ds_read_b64_tr_b16 a[146:147], v34 offset:768\n"
      "v_mfma_f32_4x4x4_16b_bf16 v[92:95], a[240:241], a[160:161], v[92:95]\n"
      "ds_read_b64_tr_b16 a[148:149], v34 offset:6144\n"
      "v_mfma_f32_4x4x4_16b_bf16 v[96:99], a[240:241], a[162:163], v[96:99]\n"
      "ds_read_b64_tr_b16 a[150:151], v34 offset:6912\n"
      "v_mfma_f32_4x4x4_16b_bf16 v[92:95], a[254:255], a[164:165], v[92:95]\n"
      "ds_read_b64_tr_b16 a[152:153], v34 offset:12288\n"
      "v_mfma_f32_4x4x4_16b_bf16 v[96:99], a[254:255], a[166:167], v[96:99]\n"
      "ds_read_b64_tr_b16 a[154:155], v34 offset:13056\n"
      "s_add_u32 m0, s63, 148992\n"
      "s_add_u32 s52, s63, 159744\n"
      "v_cvt_pk_bf16_f32 v49, v61, v65\n"
      "v_cvt_pk_bf16_f32 v50, v62, v66\n"
      "v_cvt_pk_bf16_f32 v51, v63, v67\n"
      "s_nop 0\n"
      "s_waitcnt lgkmcnt(6)\n"
      "v_mfma_f32_32x32x16_bf16 v[52:67], a[168:171], a[132:135], 0\n"
      "buffer_load_dwordx4 v31, s[12:15], s52 offen offset:0 lds\n"
      "ds_read_b64_tr_b16 a[156:157], v32 offset:3072\n"
      "ds_read_b64_tr_b16 a[158:159], v32 offset:3840\n"
      "ds_read_b64_tr_b16 a[80:81], v33 offset:128\n"
      "ds_read_b128 a[216:219], v8 offset:29184\n"
      "v_cvt_pk_bf16_f32 v204, v68, v72\n"
      "v_cvt_pk_bf16_f32 v241, v85, v89\n"
      "v_mfma_f32_32x32x16_bf16 v[52:67], a[172:175], a[136:139], v[52:67]\n"
      "buffer_load_dwordx4 v31, s[12:15], s52 offen offset:1024 lds\n"
      "ds_read_b64_tr_b16 a[160:161], v32 offset:9216\n"
      "ds_read_b64_tr_b16 a[162:163], v32 offset:9984\n"
      "ds_read_b64_tr_b16 a[82:83], v33 offset:896\n"
      "v_cvt_pk_bf16_f32 v205, v69, v73\n"
      "v_cvt_pk_bf16_f32 v206, v70, v74\n"
      "v_cvt_pk_bf16_f32 v207, v71, v75\n"
      "v_mfma_f32_32x32x16_bf16 v[52:67], a[176:179], a[140:143], v[52:67]\n"
      "buffer_load_dwordx4 v31, s[12:15], s52 offen offset:2048 lds\n"
      "ds_read_b64_tr_b16 a[164:165], v32 offset:15360\n"
      "ds_read_b64_tr_b16 a[166:167], v32 offset:16128\n"
      "ds_read_b128 a[220:223], v8 offset:32256\n"
      "v_add_u32 v32, 59904, v32\n"
      "v_cvt_pk_bf16_f32 v242, v86, v90\n"
      "v_cvt_pk_bf16_f32 v243, v87, v91\n"
      "s_nop 0\n"
      "s_waitcnt lgkmcnt(6)\n"
      "v_mfma_f32_4x4x4_16b_bf16 v[84:87], a[238:239], a[144:145], 0\n"
      "ds_read_b64_tr_b16 a[132:133], v34 offset:3072\n"
      "v_mfma_f32_4x4x4_16b_bf16 v[88:91], a[238:239], a[146:147], 0\n"
      "ds_read_b64_tr_b16 a[134:135], v34 offset:3840\n"
      "v_mfma_f32_4x4x4_16b_bf16 v[84:87], a[240:241], a[148:149], v[84:87]\n"
      "ds_read_b64_tr_b16 a[136:137], v34 offset:9216\n"
      "v_mfma_f32_4x4x4_16b_bf16 v[88:91], a[240:241], a[150:151], v[88:91]\n"
      "ds_read_b64_tr_b16 a[138:139], v34 offset:9984\n"
      "v_mfma_f32_4x4x4_16b_bf16 v[84:87], a[254:255], a[152:153], v[84:87]\n"
      "ds_read_b64_tr_b16 a[140:141], v34 offset:15360\n"
      "v_mfma_f32_4x4x4_16b_bf16 v[88:91], a[254:255], a[154:155], v[88:91]\n"
      "ds_read_b64_tr_b16 a[142:143], v34 offset:16128\n"
      "v_add_u32 v34, 59904, v34\n"
      "v_cvt_pk_bf16_f32 v120, v76, v80\n"
      "v_cvt_pk_bf16_f32 v121, v77, v81\n"
      "v_cvt_pk_bf16_f32 v122, v78, v82\n"
      "v_cvt_pk_bf16_f32 v123, v79, v83\n"
      "v_add_u32 v33, 59904, v33\n"
      "s_waitcnt vmcnt(9)\n"
      "s_barrier\n"
      "v_mfma_f32_32x32x16_bf16 v[68:83], a[168:171], a[156:159], 0\n"
      "ds_read_b64_tr_b16 a[144:145], v32 offset:0\n"
      "ds_read_b64_tr_b16 a[146:147], v32 offset:768\n"
      "ds_read_b64_tr_b16 a[84:85], v33 offset:0\n"
      "v_cvt_pk_bf16_f32 v92, v92, v96\n"
      "v_cvt_pk_bf16_f32 v172, v52, v56\n"
      "s_nop 0\n"
      "s_waitcnt lgkmcnt(3)\n"
      "v_mfma_f32_32x32x16_bf16 v[68:83], a[172:175], a[160:163], v[68:83]\n"
      "ds_read_b64_tr_b16 a[148:149], v32 offset:6144\n"
      "ds_read_b64_tr_b16 a[150:151], v32 offset:6912\n"
      "ds_read_b64_tr_b16 a[86:87], v33 offset:768\n"
      "v_cvt_pk_bf16_f32 v93, v93, v97\n"
      "v_cvt_pk_bf16_f32 v173, v53, v57\n"
      "v_cvt_pk_bf16_f32 v244, v84, v88\n"
      "v_mfma_f32_32x32x16_bf16 v[68:83], a[176:179], a[164:167], v[68:83]\n"
      "ds_read_b64_tr_b16 a[152:153], v32 offset:12288\n"
      "ds_read_b64_tr_b16 a[154:155], v32 offset:13056\n"
      "ds_read_b64_tr_b16 a[88:89], v33 offset:64\n"
      "v_cvt_pk_bf16_f32 v94, v94, v98\n"
      "v_cvt_pk_bf16_f32 v174, v54, v58\n"
      "v_cvt_pk_bf16_f32 v95, v95, v99\n"
      "v_mfma_f32_16x16x32_bf16 v[124:127], a[60:63], a[216:219], v[124:127]\n"
      "ds_read_b64_tr_b16 a[90:91], v33 offset:832\n"
      "v_cvt_pk_bf16_f32 v175, v55, v59\n"
      "v_mfma_f32_16x16x32_bf16 v[128:131], a[64:67], a[216:219], v[128:131]\n"
      "ds_read_b64_tr_b16 a[92:93], v33 offset:128\n"
      "v_cvt_pk_bf16_f32 v245, v85, v89\n"
      "v_mfma_f32_16x16x32_bf16 v[132:135], a[68:71], a[216:219], v[132:135]\n"
      "v_accvgpr_write_b32 a200, v92\n"
      "v_cvt_pk_bf16_f32 v36, v60, v64\n"
      "v_mfma_f32_16x16x32_bf16 v[136:139], a[60:63], a[220:223], v[136:139]\n"
      "ds_read_b64_tr_b16 a[94:95], v33 offset:896\n"
      "v_accvgpr_write_b32 a201, v93\n"
      "v_mfma_f32_16x16x32_bf16 v[140:143], a[64:67], a[220:223], v[140:143]\n"
      "v_cvt_pk_bf16_f32 v37, v61, v65\n"
      "v_cvt_pk_bf16_f32 v246, v86, v90\n"
      "v_mfma_f32_16x16x32_bf16 v[144:147], a[68:71], a[220:223], v[144:147]\n"
      "v_accvgpr_write_b32 a202, v94\n"
      "v_accvgpr_write_b32 a203, v95\n"
      "v_mfma_f32_4x4x4_16b_bf16 v[92:95], a[238:239], a[132:133], 0\n"
      "ds_read_b64_tr_b16 a[156:157], v34 offset:0\n"
      "v_mfma_f32_4x4x4_16b_bf16 v[96:99], a[238:239], a[134:135], 0\n"
      "ds_read_b64_tr_b16 a[158:159], v34 offset:768\n"
      "v_mfma_f32_4x4x4_16b_bf16 v[92:95], a[240:241], a[136:137], v[92:95]\n"
      "ds_read_b64_tr_b16 a[160:161], v34 offset:6144\n"
      "v_mfma_f32_4x4x4_16b_bf16 v[96:99], a[240:241], a[138:139], v[96:99]\n"
      "ds_read_b64_tr_b16 a[162:163], v34 offset:6912\n"
      "v_mfma_f32_4x4x4_16b_bf16 v[92:95], a[254:255], a[140:141], v[92:95]\n"
      "ds_read_b64_tr_b16 a[164:165], v34 offset:12288\n"
      "v_mfma_f32_4x4x4_16b_bf16 v[96:99], a[254:255], a[142:143], v[96:99]\n"
      "ds_read_b64_tr_b16 a[166:167], v34 offset:13056\n"
      "ds_read_b128 a[224:227], v9 offset:29184\n"
      "v_cvt_pk_bf16_f32 v38, v62, v66\n"
      "v_cvt_pk_bf16_f32 v39, v63, v67\n"
      "v_cvt_pk_bf16_f32 v208, v68, v72\n"
      "s_waitcnt lgkmcnt(7)\n"
      "v_mfma_f32_32x32x16_bf16 v[52:67], a[168:171], a[144:147], 0\n"
      "ds_read_b64_tr_b16 a[132:133], v32 offset:3072\n"
      "ds_read_b64_tr_b16 a[134:135], v32 offset:3840\n"
      "s_mov_b32 m0, s64\n"
      "s_nop 0\n"
      "s_nop 0\n"
      "ds_write_addtid_b32 v44 offset:28672\n"
      "ds_write_addtid_b32 v45 offset:29184\n"
      "ds_write_addtid_b32 v46 offset:29696\n"
      "ds_write_addtid_b32 v47 offset:30208\n"
      "v_cvt_pk_bf16_f32 v209, v69, v73\n"
      "v_cvt_pk_bf16_f32 v210, v70, v74\n"
      "v_cvt_pk_bf16_f32 v92, v92, v96\n"
      "v_mfma_f32_32x32x16_bf16 v[52:67], a[172:175], a[148:151], v[52:67]\n"
      "ds_read_b64_tr_b16 a[136:137], v32 offset:9216\n"
      "ds_read_b64_tr_b16 a[138:139], v32 offset:9984\n"
      "ds_write_addtid_b32 v48 offset:30720\n"
      "ds_write_addtid_b32 v49 offset:31232\n"
      "ds_write_addtid_b32 v50 offset:31744\n"
      "ds_write_addtid_b32 v51 offset:32256\n"
      "v_cvt_pk_bf16_f32 v211, v71, v75\n"
      "v_cvt_pk_bf16_f32 v93, v93, v97\n"
      "v_mfma_f32_32x32x16_bf16 v[52:67], a[176:179], a[152:155], v[52:67]\n"
      "ds_read_b64_tr_b16 a[140:141], v32 offset:15360\n"
      "ds_read_b64_tr_b16 a[142:143], v32 offset:16128\n"
      "ds_read_b128 a[228:231], v9 offset:32256\n"
      "v_add_u32 v32, 76800, v32\n"
      "v_cvt_pk_bf16_f32 v247, v87, v91\n"
      "v_cvt_pk_bf16_f32 v40, v76, v80\n"
      "v_cvt_pk_bf16_f32 v94, v94, v98\n"
      "v_cvt_pk_bf16_f32 v41, v77, v81\n"
      "s_nop 0\n"
      "s_waitcnt lgkmcnt(7)\n"
      "v_mfma_f32_4x4x4_16b_bf16 v[84:87], a[238:239], a[156:157], 0\n"
      "ds_read_b64_tr_b16 a[144:145], v34 offset:3072\n"
      "v_mfma_f32_4x4x4_16b_bf16 v[88:91], a[238:239], a[158:159], 0\n"
      "ds_read_b64_tr_b16 a[146:147], v34 offset:3840\n"
      "v_mfma_f32_4x4x4_16b_bf16 v[84:87], a[240:241], a[160:161], v[84:87]\n"
      "ds_read_b64_tr_b16 a[148:149], v34 offset:9216\n"
      "v_mfma_f32_4x4x4_16b_bf16 v[88:91], a[240:241], a[162:163], v[88:91]\n"
      "ds_read_b64_tr_b16 a[150:151], v34 offset:9984\n"
      "v_mfma_f32_4x4x4_16b_bf16 v[84:87], a[254:255], a[164:165], v[84:87]\n"
      "ds_read_b64_tr_b16 a[152:153], v34 offset:15360\n"
      "v_mfma_f32_4x4x4_16b_bf16 v[88:91], a[254:255], a[166:167], v[88:91]\n"
      "ds_read_b64_tr_b16 a[154:155], v34 offset:16128\n"
      "v_add_u32 v34, 76800, v34\n"
      "s_add_u32 s54, s54, 196608\n"
      "s_add_u32 m0, s49, 107520\n"
      "v_cvt_pk_bf16_f32 v42, v78, v82\n"
      "v_add_u32 v33, 76800, v33\n"
      "v_cvt_pk_bf16_f32 v43, v79, v83\n"
      "s_waitcnt lgkmcnt(6)\n"
      "s_waitcnt vmcnt(0)\n"
      "s_nop 0\n"
      "s_barrier\n"
      "v_mfma_f32_32x32x16_bf16 v[68:83], a[168:171], a[132:135], 0\n"
      "buffer_load_dwordx4 v15, s[4:7], s54 offen offset:0 nt lds\n"
      "ds_read_b64_tr_b16 a[156:157], v32 offset:0\n"
      "ds_read_b64_tr_b16 a[158:159], v32 offset:768\n"
      "ds_read_b64_tr_b16 a[96:97], v33 offset:0\n"
      "v_cvt_pk_bf16_f32 v95, v95, v99\n"
      "v_cvt_pk_bf16_f32 v176, v52, v56\n"
      "v_mfma_f32_32x32x16_bf16 v[68:83], a[172:175], a[136:139], v[68:83]\n"
      "buffer_load_dwordx4 v16, s[4:7], s54 offen offset:1024 nt lds\n"
      "ds_read_b64_tr_b16 a[160:161], v32 offset:6144\n"
      "ds_read_b64_tr_b16 a[162:163], v32 offset:6912\n"
      "ds_read_b64_tr_b16 a[242:243], v14 offset:5120\n"
      "v_accvgpr_write_b32 a204, v92\n"
      "v_cvt_pk_bf16_f32 v177, v53, v57\n"
      "v_accvgpr_write_b32 a205, v93\n"
      "v_mfma_f32_32x32x16_bf16 v[68:83], a[176:179], a[140:143], v[68:83]\n"
      "buffer_load_dwordx4 v17, s[4:7], s54 offen offset:2048 nt lds\n"
      "ds_read_b64_tr_b16 a[164:165], v32 offset:12288\n"
      "ds_read_b64_tr_b16 a[166:167], v32 offset:13056\n"
      "ds_read_b64_tr_b16 a[98:99], v33 offset:768\n"
      "s_add_u32 m0, s56, 107520\n"
      "v_mfma_f32_16x16x32_bf16 v[124:127], a[72:75], a[224:227], v[124:127]\n"
      "buffer_load_dwordx4 v18, s[4:7], s54 offen offset:3072 nt lds\n"
      "ds_read2st64_b32 a[124:125], v35 offset0:112 offset1:114\n"
      "v_mfma_f32_16x16x32_bf16 v[128:131], a[76:79], a[224:227], v[128:131]\n"
      "s_add_u32 s54, s54, 196608\n"
      "s_add_u32 m0, s49, 114696\n"
      "v_mfma_f32_16x16x32_bf16 v[132:135], a[80:83], a[224:227], v[132:135]\n"
      "buffer_load_dwordx4 v15, s[4:7], s54 offen offset:0 nt lds\n"
      "ds_read2st64_b32 a[128:129], v35 offset0:120 offset1:122\n"
      "v_mfma_f32_16x16x32_bf16 v[136:139], a[72:75], a[228:231], v[136:139]\n"
      "ds_read_b64_tr_b16 a[244:245], v14 offset:5128\n"
      "v_cvt_pk_bf16_f32 v178, v54, v58\n"
      "v_cvt_pk_bf16_f32 v248, v84, v88\n"
      "v_mfma_f32_16x16x32_bf16 v[140:143], a[76:79], a[228:231], v[140:143]\n"
      "buffer_load_dwordx4 v16, s[4:7], s54 offen offset:1024 nt lds\n"
      "v_cvt_pk_bf16_f32 v179, v55, v59\n"
      "v_mfma_f32_16x16x32_bf16 v[144:147], a[80:83], a[228:231], v[144:147]\n"
      "ds_read_b64_tr_b16 a[100:101], v33 offset:64\n"
      "v_accvgpr_write_b32 a206, v94\n"
      "v_accvgpr_write_b32 a207, v95\n"
      "s_nop 0\n"
      "s_waitcnt lgkmcnt(3)\n"
      "v_mfma_f32_4x4x4_16b_bf16 v[92:95], a[238:239], a[144:145], 0\n"
      "ds_read_b64_tr_b16 a[132:133], v34 offset:0\n"
      "v_mfma_f32_4x4x4_16b_bf16 v[96:99], a[238:239], a[146:147], 0\n"
      "ds_read_b64_tr_b16 a[134:135], v34 offset:768\n"
      "v_mfma_f32_4x4x4_16b_bf16 v[92:95], a[240:241], a[148:149], v[92:95]\n"
      "ds_read_b64_tr_b16 a[136:137], v34 offset:6144\n"
      "v_mfma_f32_4x4x4_16b_bf16 v[96:99], a[240:241], a[150:151], v[96:99]\n"
      "ds_read_b64_tr_b16 a[138:139], v34 offset:6912\n"
      "v_mfma_f32_4x4x4_16b_bf16 v[92:95], a[254:255], a[152:153], v[92:95]\n"
      "ds_read_b64_tr_b16 a[140:141], v34 offset:12288\n"
      "v_mfma_f32_4x4x4_16b_bf16 v[96:99], a[254:255], a[154:155], v[96:99]\n"
      "ds_read_b64_tr_b16 a[142:143], v34 offset:13056\n"
      "ds_read_b64_tr_b16 a[102:103], v33 offset:832\n"
      "v_cvt_pk_bf16_f32 v44, v60, v64\n"
      "v_cvt_pk_bf16_f32 v249, v85, v89\n"
      "v_cvt_pk_bf16_f32 v45, v61, v65\n"
      "s_nop 0\n"
      "s_waitcnt lgkmcnt(7)\n"
      "v_mfma_f32_4x4x4_16b_bf16 v[20:23], a[232:233], a[242:243], 0\n"
      "v_cvt_pk_bf16_f32 v212, v68, v72\n"
      "v_cvt_pk_bf16_f32 v250, v86, v90\n"
      "v_mfma_f32_4x4x4_16b_bf16 v[24:27], a[232:233], a[244:245], 0\n"
      "v_cvt_pk_bf16_f32 v46, v62, v66\n"
      "v_cvt_pk_bf16_f32 v47, v63, v67\n"
      "v_cvt_pk_bf16_f32 v213, v69, v73\n"
      "v_cvt_pk_bf16_f32 v214, v70, v74\n"
      "v_mfma_f32_32x32x16_bf16 v[52:67], a[168:171], a[156:159], 0\n"
      "buffer_load_dwordx4 v17, s[4:7], s54 offen offset:2048 nt lds\n"
      "ds_read_b64_tr_b16 a[144:145], v32 offset:3072\n"
      "ds_read_b64_tr_b16 a[146:147], v32 offset:3840\n"
      "s_add_u32 s54, s54, 196608\n"
      "s_add_u32 m0, s49, 121992\n"
      "v_mfma_f32_32x32x16_bf16 v[52:67], a[172:175], a[160:163], v[52:67]\n"
      "buffer_load_dwordx4 v15, s[4:7], s54 offen offset:0 nt lds\n"
      "ds_read_b64_tr_b16 a[148:149], v32 offset:9216\n"
      "ds_read_b64_tr_b16 a[150:151], v32 offset:9984\n"
      "ds_read_b64_tr_b16 a[242:243], v14 offset:6144\n"
      "v_cvt_pk_bf16_f32 v20, v20, v24\n"
      "v_cvt_pk_bf16_f32 v21, v21, v25\n"
      "v_cvt_pk_bf16_f32 v251, v87, v91\n"
      "v_mfma_f32_32x32x16_bf16 v[52:67], a[176:179], a[164:167], v[52:67]\n"
      "buffer_load_dwordx4 v16, s[4:7], s54 offen offset:1024 nt lds\n"
      "ds_read_b64_tr_b16 a[152:153], v32 offset:15360\n"
      "ds_read_b64_tr_b16 a[154:155], v32 offset:16128\n"
      "ds_read_b64_tr_b16 a[244:245], v14 offset:6152\n"
      "v_cvt_pk_bf16_f32 v22, v22, v26\n"
      "v_cvt_pk_bf16_f32 v23, v23, v27\n"
      "v_cvt_pk_bf16_f32 v215, v71, v75\n"
      "s_nop 0\n"
      "s_waitcnt lgkmcnt(6)\n"
      "v_mfma_f32_4x4x4_16b_bf16 v[84:87], a[238:239], a[132:133], 0\n"
      "ds_read_b64_tr_b16 a[156:157], v34 offset:3072\n"
      "v_mfma_f32_4x4x4_16b_bf16 v[88:91], a[238:239], a[134:135], 0\n"
      "ds_read_b64_tr_b16 a[158:159], v34 offset:3840\n"
      "v_mfma_f32_4x4x4_16b_bf16 v[84:87], a[240:241], a[136:137], v[84:87]\n"
      "ds_read_b64_tr_b16 a[160:161], v34 offset:9216\n"
      "s_mov_b32 m0, s64\n"
      "s_nop 0\n"
      "ds_write_addtid_b32 v20 offset:2560\n"
      "ds_write_addtid_b32 v21 offset:6144\n"
      "ds_write_addtid_b32 v22 offset:9728\n"
      "ds_write_addtid_b32 v23 offset:13312\n"
      "v_mfma_f32_4x4x4_16b_bf16 v[88:91], a[240:241], a[138:139], v[88:91]\n"
      "ds_read_b64_tr_b16 a[162:163], v34 offset:9984\n"
      "v_mfma_f32_4x4x4_16b_bf16 v[84:87], a[254:255], a[140:141], v[84:87]\n"
      "ds_read_b64_tr_b16 a[164:165], v34 offset:15360\n"
      "v_mfma_f32_4x4x4_16b_bf16 v[88:91], a[254:255], a[142:143], v[88:91]\n"
      "ds_read_b64_tr_b16 a[166:167], v34 offset:16128\n"
      "ds_read_b64_tr_b16 a[104:105], v33 offset:128\n"
      "ds_read2st64_b32 a[126:127], v35 offset0:116 offset1:118\n"
      "s_waitcnt lgkmcnt(11)\n"
      "v_mfma_f32_4x4x4_16b_bf16 v[20:23], a[232:233], a[242:243], 0\n"
      "ds_read2st64_b32 a[130:131], v35 offset0:124 offset1:126\n"
      "v_mfma_f32_4x4x4_16b_bf16 v[24:27], a[232:233], a[244:245], 0\n"
      "ds_read_b128 a[216:219], v10 offset:29184\n"
      "ds_read_b64_tr_b16 a[106:107], v33 offset:896\n"
      "v_cvt_pk_bf16_f32 v48, v76, v80\n"
      "v_cvt_pk_bf16_f32 v49, v77, v81\n"
      "v_cvt_pk_bf16_f32 v50, v78, v82\n"
      "v_cvt_pk_bf16_f32 v51, v79, v83\n"
      "v_mfma_f32_32x32x16_bf16 v[68:83], a[168:171], a[144:147], 0\n"
      "s_add_u32 m0, s49, 121992\n"
      "s_nop 0\n"
      "buffer_load_dwordx4 v17, s[4:7], s54 offen offset:2048 nt lds\n"
      "ds_read_b128 a[220:223], v10 offset:32256\n"
      "v_cvt_pk_bf16_f32 v92, v92, v96\n"
      "v_mfma_f32_32x32x16_bf16 v[68:83], a[172:175], a[148:151], v[68:83]\n"
      "ds_read_b64_tr_b16 a[242:243], v14 offset:4608\n"
      "v_cvt_pk_bf16_f32 v93, v93, v97\n"
      "v_cvt_pk_bf16_f32 v20, v20, v24\n"
      "v_cvt_pk_bf16_f32 v94, v94, v98\n"
      "v_cvt_pk_bf16_f32 v180, v52, v56\n"
      "v_cvt_pk_bf16_f32 v21, v21, v25\n"
      "v_mfma_f32_32x32x16_bf16 v[68:83], a[176:179], a[152:155], v[68:83]\n"
      "s_mov_b32 m0, s64\n"
      "s_nop 0\n"
      "ds_write_addtid_b32 v20 offset:3072\n"
      "ds_read_b128 a[224:227], v11 offset:29184\n"
      "ds_write_addtid_b32 v21 offset:6656\n"
      "v_cvt_pk_bf16_f32 v95, v95, v99\n"
      "v_accvgpr_write_b32 a208, v92\n"
      "v_cvt_pk_bf16_f32 v181, v53, v57\n"
      "v_cvt_pk_bf16_f32 v252, v84, v88\n"
      "s_nop 0\n"
      "s_waitcnt lgkmcnt(3)\n"
      "v_mfma_f32_16x16x32_bf16 v[124:127], a[84:87], a[216:219], v[124:127]\n"
      "ds_read_b64_tr_b16 a[244:245], v14 offset:4616\n"
      "v_accvgpr_write_b32 a209, v93\n"
      "v_mfma_f32_16x16x32_bf16 v[128:131], a[88:91], a[216:219], v[128:131]\n"
      "ds_read_b128 a[228:231], v11 offset:32256\n"
      "v_cvt_pk_bf16_f32 v22, v22, v26\n"
      "v_mfma_f32_16x16x32_bf16 v[132:135], a[92:95], a[216:219], v[132:135]\n"
      "ds_write_addtid_b32 v22 offset:10240\n"
      "v_accvgpr_write_b32 a210, v94\n"
      "v_accvgpr_write_b32 a211, v95\n"
      "v_cvt_pk_bf16_f32 v23, v23, v27\n"
      "v_mfma_f32_4x4x4_16b_bf16 v[92:95], a[238:239], a[156:157], 0\n"
      "v_cvt_pk_bf16_f32 v182, v54, v58\n"
      "v_mfma_f32_4x4x4_16b_bf16 v[96:99], a[238:239], a[158:159], 0\n"
      "v_cvt_pk_bf16_f32 v183, v55, v59\n"
      "v_mfma_f32_4x4x4_16b_bf16 v[92:95], a[240:241], a[160:161], v[92:95]\n"
      "ds_write_addtid_b32 v23 offset:13824\n"
      "v_mfma_f32_4x4x4_16b_bf16 v[96:99], a[240:241], a[162:163], v[96:99]\n"
      "v_cvt_pk_bf16_f32 v253, v85, v89\n"
      "v_mfma_f32_4x4x4_16b_bf16 v[92:95], a[254:255], a[164:165], v[92:95]\n"
      "v_cvt_pk_bf16_f32 v28, v60, v64\n"
      "v_mfma_f32_4x4x4_16b_bf16 v[96:99], a[254:255], a[166:167], v[96:99]\n"
      "v_cvt_pk_bf16_f32 v29, v61, v65\n"
      "v_cvt_pk_bf16_f32 v30, v62, v66\n"
      "v_cvt_pk_bf16_f32 v254, v86, v90\n"
      "v_cvt_pk_bf16_f32 v31, v63, v67\n"
      "v_cvt_pk_bf16_f32 v255, v87, v91\n"
      "s_waitcnt lgkmcnt(0)\n"
      "s_barrier\n"
      "v_mfma_f32_4x4x4_16b_bf16 v[20:23], a[232:233], a[242:243], 0\n"
      "v_mfma_f32_4x4x4_16b_bf16 v[24:27], a[232:233], a[244:245], 0\n"
      "v_mfma_f32_16x16x32_bf16 v[136:139], a[84:87], a[220:223], v[136:139]\n"
      "ds_read_b128 a[216:219], v0 offset:0\n"
      "s_add_u32 m0, s57, 121992\n"
      "v_mfma_f32_16x16x32_bf16 v[140:143], a[88:91], a[220:223], v[140:143]\n"
      "buffer_load_dwordx4 v18, s[4:7], s54 offen offset:3072 nt lds\n"
      "v_mfma_f32_16x16x32_bf16 v[144:147], a[92:95], a[220:223], v[144:147]\n"
      "ds_read_b128 a[220:223], v0 offset:32\n"
      "v_cvt_pk_bf16_f32 v216, v68, v72\n"
      "v_mfma_f32_16x16x32_bf16 v[124:127], a[96:99], a[224:227], v[124:127]\n"
      "v_cvt_pk_bf16_f32 v217, v69, v73\n"
      "v_cvt_pk_bf16_f32 v218, v70, v74\n"
      "v_mfma_f32_16x16x32_bf16 v[128:131], a[100:103], a[224:227], v[128:131]\n"
      "s_add_u32 s54, s54, 196608\n"
      "s_add_u32 m0, s49, 129408\n"
      "v_mfma_f32_16x16x32_bf16 v[132:135], a[104:107], a[224:227], v[132:135]\n"
      "buffer_load_dwordx4 v15, s[4:7], s54 offen offset:0 nt lds\n"
      "v_mfma_f32_16x16x32_bf16 v[136:139], a[96:99], a[228:231], v[136:139]\n"
      "ds_read_b128 a[224:227], v0 offset:64\n"
      "v_cvt_pk_bf16_f32 v219, v71, v75\n"
      "v_mfma_f32_16x16x32_bf16 v[140:143], a[100:103], a[228:231], v[140:143]\n"
      "ds_read_b64_tr_b16 a[242:243], v14 offset:5632\n"
      "ds_read_b64_tr_b16 a[244:245], v14 offset:5640\n"
      "v_mfma_f32_16x16x32_bf16 v[144:147], a[104:107], a[228:231], v[144:147]\n"
      "v_cvt_pk_bf16_f32 v68, v76, v80\n"
      "v_cvt_pk_bf16_f32 v69, v77, v81\n"
      "v_cvt_pk_bf16_f32 v70, v78, v82\n"
      "v_cvt_pk_bf16_f32 v71, v79, v83\n"
      "s_nop 0\n"
      "s_waitcnt lgkmcnt(3)\n"
      "v_mfma_f32_32x32x16_bf16 v[52:67], a[216:219], v[148:151], 0\n"
      "ds_read_b128 a[228:231], v1 offset:0\n"
      "v_cvt_pk_bf16_f32 v92, v92, v96\n"
      "v_cvt_pk_bf16_f32 v93, v93, v97\n"
      "v_cvt_pk_bf16_f32 v20, v20, v24\n"
      "v_cvt_pk_bf16_f32 v94, v94, v98\n"
      "v_cvt_pk_bf16_f32 v21, v21, v25\n"
      "v_mfma_f32_32x32x16_bf16 v[52:67], a[220:223], v[152:155], v[52:67]\n"
      "buffer_load_dwordx4 v16, s[4:7], s54 offen offset:1024 nt lds\n"
      "s_mov_b32 m0, s64\n"
      "s_nop 0\n"
      "ds_write_addtid_b32 v68 offset:51200\n"
      "ds_write_addtid_b32 v69 offset:51712\n"
      "ds_write_addtid_b32 v70 offset:52224\n"
      "ds_write_addtid_b32 v71 offset:52736\n"
      "v_mfma_f32_32x32x16_bf16 v[68:83], a[216:219], a[108:111], 0\n"
      "ds_write_addtid_b32 v20 offset:16384\n"
      "ds_write_addtid_b32 v21 offset:19968\n"
      "v_cvt_pk_bf16_f32 v95, v95, v99\n"
      "v_accvgpr_write_b32 a212, v92\n"
      "v_accvgpr_write_b32 a213, v93\n"
      "v_accvgpr_write_b32 a214, v94\n"
      "v_accvgpr_write_b32 a215, v95\n"
      "v_mfma_f32_32x32x16_bf16 v[68:83], a[220:223], a[112:115], v[68:83]\n"
      "v_cvt_pk_bf16_f32 v22, v22, v26\n"
      "v_cvt_pk_bf16_f32 v23, v23, v27\n"
      "v_mfma_f32_32x32x16_bf16 v[84:99], a[216:219], v[220:223], 0\n"
      "s_add_u32 m0, s49, 129408\n"
      "s_nop 0\n"
      "buffer_load_dwordx4 v17, s[4:7], s54 offen offset:2048 nt lds\n"
      "ds_read_b128 a[216:219], v1 offset:32\n"
      "s_mov_b32 m0, s64\n"
      "s_nop 0\n"
      "ds_write_addtid_b32 v22 offset:23552\n"
      "v_mfma_f32_32x32x16_bf16 v[84:99], a[220:223], v[224:227], v[84:99]\n"
      "ds_write_addtid_b32 v23 offset:27136\n"
      "s_waitcnt lgkmcnt(3)\n"
      "v_mfma_f32_4x4x4_16b_bf16 v[20:23], a[232:233], a[242:243], 0\n"
      "ds_write_addtid_b32 v36 offset:28672\n"
      "v_mfma_f32_4x4x4_16b_bf16 v[24:27], a[232:233], a[244:245], 0\n"
      "ds_write_addtid_b32 v37 offset:29184\n"
      "v_mfma_f32_32x32x16_bf16 v[52:67], a[224:227], v[156:159], v[52:67]\n"
      "ds_read_b128 a[220:223], v1 offset:64\n"
      "ds_read_b64_tr_b16 a[242:243], v14 offset:6656\n"
      "ds_read_b64_tr_b16 a[244:245], v14 offset:6664\n"
      "v_cvt_pk_bf16_f32 v20, v20, v24\n"
      "v_cvt_pk_bf16_f32 v21, v21, v25\n"
      "v_cvt_pk_bf16_f32 v22, v22, v26\n"
      "v_mfma_f32_32x32x16_bf16 v[68:83], a[224:227], a[116:119], v[68:83]\n"
      "ds_write_addtid_b32 v20 offset:16896\n"
      "ds_write_addtid_b32 v21 offset:20480\n"
      "ds_write_addtid_b32 v22 offset:24064\n"
      "v_cvt_pk_bf16_f32 v23, v23, v27\n"
      "v_mfma_f32_32x32x16_bf16 v[84:99], a[224:227], v[228:231], v[84:99]\n"
      "ds_write_addtid_b32 v23 offset:27648\n"
      "ds_write_addtid_b32 v38 offset:29696\n"
      "ds_write_addtid_b32 v39 offset:30208\n"
      "v_mfma_f32_32x32x16_bf16 v[52:67], a[228:231], v[160:163], v[52:67]\n"
      "ds_read_b128 a[224:227], v2 offset:0\n"
      "ds_write_addtid_b32 v44 offset:30720\n"
      "ds_write_addtid_b32 v45 offset:31232\n"
      "v_mfma_f32_32x32x16_bf16 v[68:83], a[228:231], a[120:123], v[68:83]\n"
      "ds_write_addtid_b32 v46 offset:31744\n"
      "ds_write_addtid_b32 v47 offset:32256\n"
      "ds_write_addtid_b32 v28 offset:32768\n"
      "ds_write_addtid_b32 v29 offset:33280\n"
      "ds_write_addtid_b32 v30 offset:33792\n"
      "ds_write_addtid_b32 v31 offset:34304\n"
      "v_mfma_f32_32x32x16_bf16 v[84:99], a[228:231], v[232:235], v[84:99]\n"
      "ds_write_addtid_b32 v100 offset:34816\n"
      "s_nop 0\n"
      "s_waitcnt lgkmcnt(10)\n"
      "v_mfma_f32_4x4x4_16b_bf16 v[20:23], a[232:233], a[242:243], 0\n"
      "ds_write_addtid_b32 v101 offset:35328\n"
      "v_mfma_f32_4x4x4_16b_bf16 v[24:27], a[232:233], a[244:245], 0\n"
      "ds_write_addtid_b32 v102 offset:35840\n"
      "v_mfma_f32_32x32x16_bf16 v[52:67], a[216:219], v[164:167], v[52:67]\n"
      "ds_read_b128 a[228:231], v2 offset:32\n"
      "ds_write_addtid_b32 v103 offset:36352\n"
      "ds_write_addtid_b32 v104 offset:36864\n"
      "v_cvt_pk_bf16_f32 v20, v20, v24\n"
      "v_cvt_pk_bf16_f32 v21, v21, v25\n"
      "v_cvt_pk_bf16_f32 v22, v22, v26\n"
      "s_waitcnt lgkmcnt(5)\n"
      "s_barrier\n"
      "v_mfma_f32_32x32x16_bf16 v[68:83], a[216:219], a[124:127], v[68:83]\n"
      "ds_write_addtid_b32 v20 offset:17408\n"
      "ds_write_addtid_b32 v21 offset:20992\n"
      "ds_write_addtid_b32 v22 offset:24576\n"
      "v_cvt_pk_bf16_f32 v23, v23, v27\n"
      "v_mfma_f32_32x32x16_bf16 v[84:99], a[216:219], v[236:239], v[84:99]\n"
      "ds_write_addtid_b32 v23 offset:28160\n"
      "ds_write_addtid_b32 v105 offset:37376\n"
      "ds_write_addtid_b32 v106 offset:37888\n"
      "v_mfma_f32_32x32x16_bf16 v[52:67], a[220:223], v[168:171], v[52:67]\n"
      "ds_read_b128 a[216:219], v2 offset:64\n"
      "ds_read2st64_b32 a[132:133], v35 offset0:112 offset1:114\n"
      "ds_read2st64_b32 a[134:135], v35 offset0:116 offset1:118\n"
      "v_mfma_f32_32x32x16_bf16 v[68:83], a[220:223], a[128:131], v[68:83]\n"
      "ds_write_addtid_b32 v107 offset:38400\n"
      "ds_write_addtid_b32 v108 offset:38912\n"
      "ds_write_addtid_b32 v109 offset:39424\n"
      "v_mfma_f32_32x32x16_bf16 v[84:99], a[220:223], v[240:243], v[84:99]\n"
      "ds_write_addtid_b32 v110 offset:39936\n"
      "ds_write_addtid_b32 v111 offset:40448\n"
      "ds_read2st64_b32 a[136:137], v35 offset0:120 offset1:122\n"
      "v_mfma_f32_32x32x16_bf16 v[52:67], a[224:227], v[172:175], v[52:67]\n"
      "ds_write_addtid_b32 v112 offset:40960\n"
      "ds_write_addtid_b32 v113 offset:41472\n"
      "ds_write_addtid_b32 v114 offset:41984\n"
      "ds_read2st64_b32 a[138:139], v35 offset0:124 offset1:126\n"
      "ds_write_addtid_b32 v115 offset:42496\n"
      "s_waitcnt lgkmcnt(11)\n"
      "v_mfma_f32_32x32x16_bf16 v[68:83], a[224:227], a[132:135], v[68:83]\n"
      "ds_read2st64_b32 a[140:141], v35 offset0:128 offset1:130\n"
      "ds_read2st64_b32 a[142:143], v35 offset0:132 offset1:134\n"
      "v_mfma_f32_32x32x16_bf16 v[84:99], a[224:227], v[244:247], v[84:99]\n"
      "ds_write_addtid_b32 v116 offset:43008\n"
      "ds_write_addtid_b32 v117 offset:43520\n"
      "s_waitcnt lgkmcnt(4)\n"
      "s_nop 0\n"
      "s_barrier\n"
      "v_mfma_f32_32x32x16_bf16 v[52:67], a[228:231], v[176:179], v[52:67]\n"
      "ds_read_b128 a[220:223], v0 offset:14336\n"
      "ds_write_addtid_b32 v118 offset:44032\n"
      "ds_read2st64_b32 a[144:145], v35 offset0:136 offset1:138\n"
      "v_mfma_f32_32x32x16_bf16 v[68:83], a[228:231], a[136:139], v[68:83]\n"
      "ds_read_b128 a[224:227], v0 offset:14368\n"
      "ds_write_addtid_b32 v119 offset:44544\n"
      "v_mfma_f32_32x32x16_bf16 v[84:99], a[228:231], v[248:251], v[84:99]\n"
      "ds_write_addtid_b32 v120 offset:45056\n"
      "ds_read2st64_b32 a[148:149], v35 offset0:144 offset1:146\n"
      "ds_read2st64_b32 a[146:147], v35 offset0:140 offset1:142\n"
      "s_add_u32 s54, s54, 196608\n"
      "s_add_u32 m0, s49, 78336\n"
      "v_mfma_f32_32x32x16_bf16 v[52:67], a[216:219], v[180:183], v[52:67]\n"
      "buffer_load_dwordx4 v15, s[4:7], s54 offen offset:0 nt lds\n"
      "ds_read_b128 a[228:231], v0 offset:14400\n"
      "ds_read2st64_b32 a[152:153], v35 offset0:152 offset1:154\n"
      "s_waitcnt lgkmcnt(6)\n"
      "v_mfma_f32_32x32x16_bf16 v[68:83], a[216:219], a[140:143], v[68:83]\n"
      "v_mfma_f32_32x32x16_bf16 v[84:99], a[216:219], v[252:255], v[84:99]\n"
      "ds_read2st64_b32 a[150:151], v35 offset0:148 offset1:150\n"
      "v_mfma_f32_32x32x16_bf16 v[52:67], a[220:223], v[184:187], v[52:67]\n"
      "ds_read_b128 a[216:219], v1 offset:14336\n"
      "s_waitcnt lgkmcnt(3)\n"
      "v_mfma_f32_32x32x16_bf16 v[68:83], a[220:223], a[144:147], v[68:83]\n"
      "buffer_load_dwordx4 v16, s[4:7], s54 offen offset:1024 nt lds\n"
      "v_mfma_f32_32x32x16_bf16 v[84:99], a[220:223], a[180:183], v[84:99]\n"
      "ds_read2st64_b32 a[154:155], v35 offset0:156 offset1:158\n"
      "v_mfma_f32_32x32x16_bf16 v[52:67], a[224:227], v[188:191], v[52:67]\n"
      "ds_read_b128 a[220:223], v1 offset:14368\n"
      "s_mov_b32 m0, s64\n"
      "s_nop 0\n"
      "ds_write_addtid_b32 v121 offset:45568\n"
      "ds_write_addtid_b32 v122 offset:46080\n"
      "ds_write_addtid_b32 v123 offset:46592\n"
      "ds_write_addtid_b32 v40 offset:47104\n"
      "ds_write_addtid_b32 v41 offset:47616\n"
      "ds_write_addtid_b32 v42 offset:48128\n"
      "ds_write_addtid_b32 v43 offset:48640\n"
      "ds_write_addtid_b32 v48 offset:49152\n"
      "ds_write_addtid_b32 v49 offset:49664\n"
      "ds_write_addtid_b32 v50 offset:50176\n"
      "ds_write_addtid_b32 v51 offset:50688\n"
      "s_waitcnt lgkmcnt(12)\n"
      "v_mfma_f32_32x32x16_bf16 v[68:83], a[224:227], a[148:151], v[68:83]\n"
      "ds_read2st64_b32 a[156:157], v35 offset0:160 offset1:162\n"
      "v_mfma_f32_32x32x16_bf16 v[84:99], a[224:227], a[184:187], v[84:99]\n"
      "s_add_u32 m0, s49, 78336\n"
      "s_nop 0\n"
      "buffer_load_dwordx4 v17, s[4:7], s54 offen offset:2048 nt lds\n"
      "ds_read2st64_b32 a[158:159], v35 offset0:164 offset1:166\n"
      "s_waitcnt lgkmcnt(2)\n"
      "s_barrier\n"
      "v_mfma_f32_32x32x16_bf16 v[52:67], a[228:231], v[192:195], v[52:67]\n"
      "ds_read_b128 a[224:227], v1 offset:14400\n"
      "v_mfma_f32_32x32x16_bf16 v[68:83], a[228:231], a[152:155], v[68:83]\n"
      "ds_read2st64_b32 a[160:161], v35 offset0:168 offset1:170\n"
      "v_mfma_f32_32x32x16_bf16 v[84:99], a[228:231], a[188:191], v[84:99]\n"
      "ds_read2st64_b32 a[162:163], v35 offset0:172 offset1:174\n"
      "s_add_u32 m0, s56, 78336\n"
      "v_mfma_f32_32x32x16_bf16 v[52:67], a[216:219], v[196:199], v[52:67]\n"
      "buffer_load_dwordx4 v18, s[4:7], s54 offen offset:3072 nt lds\n"
      "ds_read_b128 a[228:231], v2 offset:14336\n"
      "s_waitcnt lgkmcnt(3)\n"
      "v_mfma_f32_32x32x16_bf16 v[68:83], a[216:219], a[156:159], v[68:83]\n"
      "ds_read2st64_b32 a[164:165], v35 offset0:176 offset1:178\n"
      "v_mfma_f32_32x32x16_bf16 v[84:99], a[216:219], a[192:195], v[84:99]\n"
      "ds_read2st64_b32 a[176:177], v35 offset0:200 offset1:202\n"
      "ds_read2st64_b32 a[168:169], v35 offset0:184 offset1:186\n"
      "ds_read2st64_b32 a[166:167], v35 offset0:180 offset1:182\n"
      "v_mfma_f32_32x32x16_bf16 v[52:67], a[220:223], v[200:203], v[52:67]\n"
      "ds_read_b128 a[216:219], v2 offset:14368\n"
      "ds_read2st64_b32 a[172:173], v35 offset0:192 offset1:194\n"
      "s_waitcnt lgkmcnt(6)\n"
      "v_mfma_f32_32x32x16_bf16 v[68:83], a[220:223], a[160:163], v[68:83]\n"
      "v_mfma_f32_32x32x16_bf16 v[84:99], a[220:223], a[196:199], v[84:99]\n"
      "ds_read_b128 a[220:223], v2 offset:14400\n"
      "ds_read2st64_b32 a[170:171], v35 offset0:188 offset1:190\n"
      "v_mfma_f32_32x32x16_bf16 v[52:67], a[224:227], v[204:207], v[52:67]\n"
      "ds_read2st64_b32 a[178:179], v35 offset0:204 offset1:206\n"
      "s_waitcnt lgkmcnt(4)\n"
      "v_mfma_f32_32x32x16_bf16 v[68:83], a[224:227], a[164:167], v[68:83]\n"
      "ds_read2st64_b32 a[174:175], v35 offset0:196 offset1:198\n"
      "v_mfma_f32_32x32x16_bf16 v[84:99], a[224:227], a[200:203], v[84:99]\n"
      "v_mfma_f32_32x32x16_bf16 v[52:67], a[228:231], v[208:211], v[52:67]\n"
      "s_waitcnt lgkmcnt(0)\n"
      "s_waitcnt vmcnt(4)\n"
      "s_barrier\n"
      "v_mfma_f32_32x32x16_bf16 v[68:83], a[228:231], a[168:171], v[68:83]\n"
      "ds_read_b64_tr_b16 a[242:243], v14 offset:32256\n"
      "ds_read_b64_tr_b16 a[244:245], v14 offset:32264\n"
      "v_mfma_f32_32x32x16_bf16 v[84:99], a[228:231], a[204:207], v[84:99]\n"
      "v_mfma_f32_32x32x16_bf16 v[52:67], a[216:219], v[212:215], v[52:67]\n"
      "ds_read_b128 a[224:227], v3 offset:0\n"
      "v_mfma_f32_32x32x16_bf16 v[52:67], a[220:223], v[216:219], v[52:67]\n"
      "v_mfma_f32_32x32x16_bf16 v[68:83], a[216:219], a[172:175], v[68:83]\n"
      "ds_read_b128 a[250:253], v4 offset:0\n"
      "v_mfma_f32_32x32x16_bf16 v[68:83], a[220:223], a[176:179], v[68:83]\n"
      "s_add_u32 s54, s54, 196608\n"
      "s_add_u32 m0, s49, 85512\n"
      "s_nop 0\n"
      "s_waitcnt lgkmcnt(1)\n"
      "v_mfma_f32_4x4x4_16b_bf16 v[20:23], a[232:233], a[242:243], 0\n"
      "v_mfma_f32_4x4x4_16b_bf16 v[24:27], a[232:233], a[244:245], 0\n"
      "v_mfma_f32_32x32x16_bf16 v[84:99], a[216:219], a[208:211], v[84:99]\n"
      "buffer_load_dwordx4 v15, s[4:7], s54 offen offset:0 nt lds\n"
      "ds_read_b128 a[216:219], v5 offset:0\n"
      "v_mfma_f32_32x32x16_bf16 v[84:99], a[220:223], a[212:215], v[84:99]\n"
      "ds_read_b128 a[220:223], v6 offset:0\n"
      "v_cvt_pk_bf16_f32 v20, v20, v24\n"
      "v_cvt_pk_bf16_f32 v21, v21, v25\n"
      "v_cvt_pk_bf16_f32 v22, v22, v26\n"
      "v_cvt_pk_bf16_f32 v23, v23, v27\n"
      "v_mfma_f32_16x16x32_bf16 v[100:103], a[0:3], a[224:227], 0\n"
      "buffer_load_dwordx4 v16, s[4:7], s54 offen offset:1024 nt lds\n"
      "v_mfma_f32_16x16x32_bf16 v[104:107], a[4:7], a[224:227], 0\n"
      "v_mfma_f32_16x16x32_bf16 v[108:111], a[8:11], a[224:227], 0\n"
      "buffer_load_dwordx4 v17, s[4:7], s54 offen offset:2048 nt lds\n"
      "ds_read_b128 a[224:227], v7 offset:0\n"
      "s_nop 0\n"
      "s_waitcnt lgkmcnt(1)\n"
      "v_mfma_f32_16x16x32_bf16 v[100:103], a[12:15], a[250:253], v[100:103]\n"
      "s_mov_b32 m0, s64\n"
      "s_nop 0\n"
      "ds_write_addtid_b32 v20 offset:1536\n"
      "ds_write_addtid_b32 v21 offset:5120\n"
      "v_mfma_f32_16x16x32_bf16 v[104:107], a[16:19], a[250:253], v[104:107]\n"
      "ds_read_b64_tr_b16 a[242:243], v14 offset:33280\n"
      "ds_read_b64_tr_b16 a[244:245], v14 offset:33288\n"
      "v_mfma_f32_16x16x32_bf16 v[108:111], a[20:23], a[250:253], v[108:111]\n"
      "ds_read_b128 a[228:231], v8 offset:0\n"
      "ds_write_addtid_b32 v52 offset:28672\n"
      "v_mfma_f32_16x16x32_bf16 v[100:103], a[24:27], a[216:219], v[100:103]\n"
      "ds_write_addtid_b32 v22 offset:8704\n"
      "ds_write_addtid_b32 v53 offset:31776\n"
      "v_mfma_f32_16x16x32_bf16 v[104:107], a[28:31], a[216:219], v[104:107]\n"
      "ds_write_addtid_b32 v54 offset:34880\n"
      "ds_write_addtid_b32 v23 offset:12288\n"
      "v_mfma_f32_16x16x32_bf16 v[108:111], a[32:35], a[216:219], v[108:111]\n"
      "ds_read_b128 a[216:219], v9 offset:0\n"
      "ds_write_addtid_b32 v55 offset:37984\n"
      "v_mfma_f32_16x16x32_bf16 v[100:103], a[36:39], a[220:223], v[100:103]\n"
      "ds_write_addtid_b32 v56 offset:41088\n"
      "ds_write_addtid_b32 v57 offset:44192\n"
      "s_nop 0\n"
      "s_waitcnt lgkmcnt(10)\n"
      "v_mfma_f32_4x4x4_16b_bf16 v[20:23], a[232:233], a[242:243], 0\n"
      "ds_write_addtid_b32 v58 offset:47296\n"
      "ds_write_addtid_b32 v59 offset:50400\n"
      "v_mfma_f32_4x4x4_16b_bf16 v[24:27], a[232:233], a[244:245], 0\n"
      "ds_write_addtid_b32 v60 offset:29184\n"
      "ds_write_addtid_b32 v61 offset:32288\n"
      "v_mfma_f32_16x16x32_bf16 v[104:107], a[40:43], a[220:223], v[104:107]\n"
      "ds_write_addtid_b32 v62 offset:35392\n"
      "ds_write_addtid_b32 v63 offset:38496\n"
      "v_cvt_pk_bf16_f32 v20, v20, v24\n"
      "v_cvt_pk_bf16_f32 v21, v21, v25\n"
      "v_mfma_f32_16x16x32_bf16 v[108:111], a[44:47], a[220:223], v[108:111]\n"
      "ds_read_b128 a[220:223], v10 offset:0\n"
      "ds_write_addtid_b32 v20 offset:2048\n"
      "v_mfma_f32_16x16x32_bf16 v[100:103], a[48:51], a[224:227], v[100:103]\n"
      "ds_write_addtid_b32 v21 offset:5632\n"
      "ds_write_addtid_b32 v64 offset:41600\n"
      "v_mfma_f32_16x16x32_bf16 v[104:107], a[52:55], a[224:227], v[104:107]\n"
      "ds_write_addtid_b32 v65 offset:44704\n"
      "ds_write_addtid_b32 v66 offset:47808\n"
      "v_mfma_f32_16x16x32_bf16 v[108:111], a[56:59], a[224:227], v[108:111]\n"
      "ds_read_b128 a[224:227], v11 offset:0\n"
      "ds_write_addtid_b32 v67 offset:50912\n"
      "s_nop 0\n"
      "s_waitcnt lgkmcnt(9)\n"
      "v_mfma_f32_16x16x32_bf16 v[100:103], a[60:63], a[228:231], v[100:103]\n"
      "ds_write_addtid_b32 v68 offset:29696\n"
      "ds_write_addtid_b32 v69 offset:32800\n"
      "v_mfma_f32_16x16x32_bf16 v[104:107], a[64:67], a[228:231], v[104:107]\n"
      "ds_write_addtid_b32 v70 offset:35904\n"
      "ds_write_addtid_b32 v71 offset:39008\n"
      "v_mfma_f32_16x16x32_bf16 v[108:111], a[68:71], a[228:231], v[108:111]\n"
      "ds_read_b128 a[228:231], v3 offset:3072\n"
      "ds_read_b64_tr_b16 a[242:243], v14 offset:34304\n"
      "v_mfma_f32_16x16x32_bf16 v[100:103], a[72:75], a[216:219], v[100:103]\n"
      "ds_read_b64_tr_b16 a[244:245], v14 offset:34312\n"
      "ds_write_addtid_b32 v72 offset:42112\n"
      "v_mfma_f32_16x16x32_bf16 v[104:107], a[76:79], a[216:219], v[104:107]\n"
      "ds_write_addtid_b32 v73 offset:45216\n"
      "ds_write_addtid_b32 v74 offset:48320\n"
      "v_mfma_f32_16x16x32_bf16 v[108:111], a[80:83], a[216:219], v[108:111]\n"
      "ds_read_b128 a[216:219], v4 offset:3072\n"
      "s_waitcnt lgkmcnt(9)\n"
      "v_mfma_f32_16x16x32_bf16 v[100:103], a[84:87], a[220:223], v[100:103]\n"
      "ds_write_addtid_b32 v75 offset:51424\n"
      "v_cvt_pk_bf16_f32 v22, v22, v26\n"
      "v_mfma_f32_16x16x32_bf16 v[104:107], a[88:91], a[220:223], v[104:107]\n"
      "ds_write_addtid_b32 v22 offset:9216\n"
      "ds_write_addtid_b32 v76 offset:30208\n"
      "v_mfma_f32_16x16x32_bf16 v[108:111], a[92:95], a[220:223], v[108:111]\n"
      "ds_read_b128 a[220:223], v5 offset:3072\n"
      "ds_write_addtid_b32 v77 offset:33312\n"
      "v_cvt_pk_bf16_f32 v23, v23, v27\n"
      "v_mfma_f32_16x16x32_bf16 v[100:103], a[96:99], a[224:227], v[100:103]\n"
      "ds_write_addtid_b32 v23 offset:12800\n"
      "ds_write_addtid_b32 v78 offset:36416\n"
      "s_waitcnt lgkmcnt(7)\n"
      "v_mfma_f32_4x4x4_16b_bf16 v[20:23], a[232:233], a[242:243], 0\n"
      "ds_write_addtid_b32 v79 offset:39520\n"
      "v_mfma_f32_4x4x4_16b_bf16 v[24:27], a[232:233], a[244:245], 0\n"
      "ds_write_addtid_b32 v80 offset:42624\n"
      "v_mfma_f32_16x16x32_bf16 v[104:107], a[100:103], a[224:227], v[104:107]\n"
      "v_mfma_f32_16x16x32_bf16 v[108:111], a[104:107], a[224:227], v[108:111]\n"
      "ds_read_b128 a[224:227], v6 offset:3072\n"
      "v_mfma_f32_16x16x32_bf16 v[112:115], a[0:3], a[228:231], 0\n"
      "ds_read_b64_tr_b16 a[242:243], v14 offset:35328\n"
      "ds_read_b64_tr_b16 a[244:245], v14 offset:35336\n"
      "v_mfma_f32_16x16x32_bf16 v[116:119], a[4:7], a[228:231], 0\n"
      "v_cvt_pk_bf16_f32 v20, v20, v24\n"
      "v_cvt_pk_bf16_f32 v21, v21, v25\n"
      "v_mfma_f32_16x16x32_bf16 v[120:123], a[8:11], a[228:231], 0\n"
      "ds_read_b128 a[228:231], v7 offset:3072\n"
      "ds_write_addtid_b32 v20 offset:2560\n"
      "v_mfma_f32_16x16x32_bf16 v[112:115], a[12:15], a[216:219], v[112:115]\n"
      "s_add_u32 s54, s54, 196608\n"
      "s_add_u32 m0, s49, 92808\n"
      "v_mfma_f32_16x16x32_bf16 v[116:119], a[16:19], a[216:219], v[116:119]\n"
      "buffer_load_dwordx4 v15, s[4:7], s54 offen offset:0 nt lds\n"
      "v_mfma_f32_16x16x32_bf16 v[120:123], a[20:23], a[216:219], v[120:123]\n"
      "ds_read_b128 a[216:219], v8 offset:3072\n"
      "s_nop 0\n"
      "s_waitcnt lgkmcnt(3)\n"
      "v_mfma_f32_16x16x32_bf16 v[112:115], a[24:27], a[220:223], v[112:115]\n"
      "v_cvt_pk_bf16_f32 v22, v22, v26\n"
      "v_cvt_pk_bf16_f32 v23, v23, v27\n"
      "v_mfma_f32_16x16x32_bf16 v[116:119], a[28:31], a[220:223], v[116:119]\n"
      "buffer_load_dwordx4 v16, s[4:7], s54 offen offset:1024 nt lds\n"
      "s_mov_b32 m0, s64\n"
      "s_nop 0\n"
      "ds_write_addtid_b32 v21 offset:6144\n"
      "ds_write_addtid_b32 v22 offset:9728\n"
      "ds_write_addtid_b32 v23 offset:13312\n"
      "v_mfma_f32_4x4x4_16b_bf16 v[20:23], a[232:233], a[242:243], 0\n"
      "ds_write_addtid_b32 v81 offset:45728\n"
      "v_mfma_f32_4x4x4_16b_bf16 v[24:27], a[232:233], a[244:245], 0\n"
      "ds_write_addtid_b32 v82 offset:48832\n"
      "ds_write_addtid_b32 v83 offset:51936\n"
      "v_mfma_f32_16x16x32_bf16 v[120:123], a[32:35], a[220:223], v[120:123]\n"
      "ds_read_b128 a[220:223], v9 offset:3072\n"
      "ds_read_b64_tr_b16 a[242:243], v14 offset:33792\n"
      "v_mfma_f32_16x16x32_bf16 v[112:115], a[36:39], a[224:227], v[112:115]\n"
      "ds_read_b64_tr_b16 a[244:245], v14 offset:33800\n"
      "v_cvt_pk_bf16_f32 v20, v20, v24\n"
      "v_mfma_f32_16x16x32_bf16 v[116:119], a[40:43], a[224:227], v[116:119]\n"
      "s_add_u32 m0, s49, 92808\n"
      "s_nop 0\n"
      "buffer_load_dwordx4 v17, s[4:7], s54 offen offset:2048 nt lds\n"
      "v_mfma_f32_16x16x32_bf16 v[120:123], a[44:47], a[224:227], v[120:123]\n"
      "ds_read_b128 a[224:227], v10 offset:3072\n"
      "s_waitcnt lgkmcnt(4)\n"
      "v_mfma_f32_16x16x32_bf16 v[112:115], a[48:51], a[228:231], v[112:115]\n"
      "ds_read_b128 a[246:249], v11 offset:3072\n"
      "s_mov_b32 m0, s64\n"
      "s_nop 0\n"
      "ds_write_addtid_b32 v20 offset:3072\n"
      "v_mfma_f32_16x16x32_bf16 v[116:119], a[52:55], a[228:231], v[116:119]\n"
      "v_cvt_pk_bf16_f32 v21, v21, v25\n"
      "v_cvt_pk_bf16_f32 v22, v22, v26\n"
      "v_mfma_f32_16x16x32_bf16 v[120:123], a[56:59], a[228:231], v[120:123]\n"
      "ds_write_addtid_b32 v21 offset:6656\n"
      "ds_write_addtid_b32 v22 offset:10240\n"
      "v_cvt_pk_bf16_f32 v23, v23, v27\n"
      "v_mfma_f32_16x16x32_bf16 v[112:115], a[60:63], a[216:219], v[112:115]\n"
      "ds_write_addtid_b32 v23 offset:13824\n"
      "v_mfma_f32_16x16x32_bf16 v[116:119], a[64:67], a[216:219], v[116:119]\n"
      "v_mfma_f32_16x16x32_bf16 v[120:123], a[68:71], a[216:219], v[120:123]\n"
      "s_waitcnt lgkmcnt(0)\n"
      "s_barrier\n"
      "LDp0end_%=:\n"
      ".p2align 6\n"
      "LDloop_%=:\n"
      "v_mfma_f32_4x4x4_16b_bf16 v[20:23], a[232:233], a[242:243], 0\n"
      "v_mfma_f32_4x4x4_16b_bf16 v[24:27], a[232:233], a[244:245], 0\n"
      "v_mfma_f32_16x16x32_bf16 v[112:115], a[72:75], a[220:223], v[112:115]\n"
      "ds_read_b128 a[216:219], v0 offset:0\n"
      "s_add_u32 m0, s57, 92808\n"
      "v_mfma_f32_16x16x32_bf16 v[116:119], a[76:79], a[220:223], v[116:119]\n"
      "buffer_load_dwordx4 v18, s[4:7], s54 offen offset:3072 nt lds\n"
      "v_mfma_f32_16x16x32_bf16 v[120:123], a[80:83], a[220:223], v[120:123]\n"
      "ds_read_b128 a[220:223], v0 offset:32\n"
      "ds_read_b128 v[28:31], v12 offset:0\n"
      "v_mfma_f32_16x16x32_bf16 v[112:115], a[84:87], a[224:227], v[112:115]\n"
      "ds_read_b128 v[32:35], v12 offset:53504\n"
      "ds_read_b128 v[44:47], v12 offset:1280\n"
      "v_mfma_f32_16x16x32_bf16 v[116:119], a[88:91], a[224:227], v[116:119]\n"
      "s_add_u32 s54, s54, 196608\n"
      "s_add_u32 m0, s49, 100224\n"
      "v_mfma_f32_16x16x32_bf16 v[120:123], a[92:95], a[224:227], v[120:123]\n"
      "buffer_load_dwordx4 v15, s[4:7], s54 offen offset:0 nt lds\n"
      "v_mfma_f32_16x16x32_bf16 v[112:115], a[96:99], a[246:249], v[112:115]\n"
      "ds_read_b128 a[224:227], v0 offset:64\n"
      "ds_read_b128 v[48:51], v12 offset:54784\n"
      "v_mfma_f32_16x16x32_bf16 v[116:119], a[100:103], a[246:249], v[116:119]\n"
      "ds_read_b64_tr_b16 a[242:243], v14 offset:34816\n"
      "ds_read_b64_tr_b16 a[244:245], v14 offset:34824\n"
      "v_mfma_f32_16x16x32_bf16 v[120:123], a[104:107], a[246:249], v[120:123]\n"
      "ds_read_b128 v[36:39], v12 offset:1024\n"
      "s_waitcnt lgkmcnt(8)\n"
      "v_mfma_f32_32x32x16_bf16 v[52:67], a[216:219], v[148:151], 0\n"
      "ds_read_b128 a[228:231], v1 offset:0\n"
      "ds_read_b128 v[40:43], v12 offset:54528\n"
      "v_cvt_pk_bf16_f32 v20, v20, v24\n"
      "s_waitcnt lgkmcnt(7)\n"
      "v_sub_f32 v28, v28, v44\n"
      "v_sub_f32 v29, v29, v45\n"
      "v_mfma_f32_32x32x16_bf16 v[52:67], a[220:223], v[152:155], v[52:67]\n"
      "buffer_load_dwordx4 v16, s[4:7], s54 offen offset:1024 nt lds\n"
      "v_sub_f32 v30, v30, v46\n"
      "v_sub_f32 v31, v31, v47\n"
      "v_cvt_pk_bf16_f32 v21, v21, v25\n"
      "v_cvt_pk_bf16_f32 v22, v22, v26\n"
      "v_cvt_pk_bf16_f32 v23, v23, v27\n"
      "v_mfma_f32_32x32x16_bf16 v[68:83], a[216:219], a[108:111], 0\n"
      "s_waitcnt lgkmcnt(5)\n"
      "v_sub_f32 v32, v32, v48\n"
      "v_sub_f32 v33, v33, v49\n"
      "v_sub_f32 v34, v34, v50\n"
      "v_sub_f32 v35, v35, v51\n"
      "v_fmac_f32 v32, -0.5, v28\n"
      "v_mfma_f32_32x32x16_bf16 v[68:83], a[220:223], a[112:115], v[68:83]\n"
      "s_mov_b32 m0, s64\n"
      "s_nop 0\n"
      "ds_write_addtid_b32 v84 offset:30720\n"
      "ds_write_addtid_b32 v85 offset:33824\n"
      "ds_write_addtid_b32 v86 offset:36928\n"
      "ds_write_addtid_b32 v87 offset:40032\n"
      "ds_write_addtid_b32 v88 offset:43136\n"
      "ds_write_addtid_b32 v89 offset:46240\n"
      "ds_write_addtid_b32 v90 offset:49344\n"
      "ds_write_addtid_b32 v91 offset:52448\n"
      "ds_write_addtid_b32 v92 offset:31232\n"
      "ds_write_addtid_b32 v93 offset:34336\n"
      "ds_write_addtid_b32 v94 offset:37440\n"
      "ds_write_addtid_b32 v95 offset:40544\n"
      "ds_write_addtid_b32 v96 offset:43648\n"
      "ds_write_addtid_b32 v97 offset:46752\n"
      "ds_write_addtid_b32 v98 offset:49856\n"
      "ds_write_addtid_b32 v99 offset:52960\n"
      "v_mfma_f32_32x32x16_bf16 v[84:99], a[216:219], v[220:223], 0\n"
      "s_add_u32 m0, s49, 100224\n"
      "s_nop 0\n"
      "s_nop 0\n"
      "buffer_load_dwordx4 v17, s[4:7], s54 offen offset:2048 nt lds\n"
      "ds_read_b128 a[216:219], v1 offset:32\n"
      "s_mov_b32 m0, s64\n"
      "s_nop 0\n"
      "ds_write_addtid_b32 v20 offset:16384\n"
      "v_mfma_f32_32x32x16_bf16 v[84:99], a[220:223], v[224:227], v[84:99]\n"
      "ds_write_addtid_b32 v21 offset:19968\n"
      "ds_write_addtid_b32 v22 offset:23552\n"
      "ds_write_addtid_b32 v23 offset:27136\n"
      "v_fmac_f32 v33, -0.5, v29\n"
      "v_fmac_f32 v34, -0.5, v30\n"
      "v_fmac_f32 v35, -0.5, v31\n"
      "s_waitcnt lgkmcnt(10)\n"
      "v_mfma_f32_4x4x4_16b_bf16 v[20:23], a[232:233], a[242:243], 0\n"
      "ds_read_b128 v[44:47], v12 offset:256\n"
      "v_mfma_f32_4x4x4_16b_bf16 v[24:27], a[232:233], a[244:245], 0\n"
      "ds_read_b128 v[48:51], v12 offset:53760\n"
      "v_add_f32 v28, v28, v124\n"
      "v_add_f32 v29, v29, v125\n"
      "v_mfma_f32_32x32x16_bf16 v[52:67], a[224:227], v[156:159], v[52:67]\n"
      "ds_read_b128 a[220:223], v1 offset:64\n"
      "v_add_f32 v30, v30, v126\n"
      "v_add_f32 v31, v31, v127\n"
      "v_mul_f32 v32, s46, v32\n"
      "v_mul_f32 v33, s46, v33\n"
      "v_mul_f32 v34, s46, v34\n"
      "v_mfma_f32_32x32x16_bf16 v[68:83], a[224:227], a[116:119], v[68:83]\n"
      "ds_read_b64_tr_b16 a[242:243], v14 offset:35840\n"
      "ds_read_b64_tr_b16 a[244:245], v14 offset:35848\n"
      "v_mul_f32 v35, s46, v35\n"
      "v_sub_f32 v28, v28, v32\n"
      "v_sub_f32 v29, v29, v33\n"
      "v_cvt_pk_bf16_f32 v20, v20, v24\n"
      "v_mfma_f32_32x32x16_bf16 v[84:99], a[224:227], v[228:231], v[84:99]\n"
      "ds_write_addtid_b32 v20 offset:16896\n"
      "v_sub_f32 v30, v30, v34\n"
      "v_sub_f32 v31, v31, v35\n"
      "v_fmac_f32 v32, s45, v100\n"
      "v_fmac_f32 v33, s45, v101\n"
      "v_cvt_pk_bf16_f32 v21, v21, v25\n"
      "v_mfma_f32_32x32x16_bf16 v[52:67], a[228:231], v[160:163], v[52:67]\n"
      "ds_read_b128 a[224:227], v2 offset:0\n"
      "ds_write_addtid_b32 v21 offset:20480\n"
      "v_fmac_f32 v34, s45, v102\n"
      "v_fmac_f32 v35, s45, v103\n"
      "v_fmac_f32 v28, 4.0, v100\n"
      "v_cvt_pk_bf16_f32 v22, v22, v26\n"
      "v_mfma_f32_32x32x16_bf16 v[68:83], a[228:231], a[120:123], v[68:83]\n"
      "ds_write_addtid_b32 v22 offset:24064\n"
      "v_cvt_pk_bf16_f32 v23, v23, v27\n"
      "v_fmac_f32 v29, 4.0, v101\n"
      "v_fmac_f32 v30, 4.0, v102\n"
      "v_fmac_f32 v31, 4.0, v103\n"
      "v_cvt_pk_bf16_f32 v32, v32, v33\n"
      "v_mfma_f32_32x32x16_bf16 v[84:99], a[228:231], v[232:235], v[84:99]\n"
      "ds_write_addtid_b32 v23 offset:27648\n"
      "v_cvt_pk_bf16_f32 v33, v34, v35\n"
      "v_cvt_pk_bf16_f32 v28, v28, v29\n"
      "v_cvt_pk_bf16_f32 v29, v30, v31\n"
      "s_waitcnt lgkmcnt(4)\n"
      "v_sub_f32 v36, v36, v44\n"
      "v_mfma_f32_4x4x4_16b_bf16 v[20:23], a[232:233], a[242:243], 0\n"
      "v_sub_f32_e64 v37, v37, v45\n"
      "v_mfma_f32_4x4x4_16b_bf16 v[24:27], a[232:233], a[244:245], 0\n"
      "v_sub_f32 v38, v38, v46\n"
      "v_sub_f32 v39, v39, v47\n"
      "v_mfma_f32_32x32x16_bf16 v[52:67], a[216:219], v[164:167], v[52:67]\n"
      "ds_read_b128 a[228:231], v2 offset:32\n"
      "ds_write_b64 v13, v[32:33] offset:12288\n"
      "ds_write_b64 v13, v[28:29] offset:0\n"
      "v_mfma_f32_32x32x16_bf16 v[68:83], a[216:219], a[124:127], v[68:83]\n"
      "v_sub_f32 v40, v40, v48\n"
      "v_sub_f32 v41, v41, v49\n"
      "v_sub_f32 v42, v42, v50\n"
      "v_sub_f32 v43, v43, v51\n"
      "v_fmac_f32 v40, -0.5, v36\n"
      "v_fmac_f32 v41, -0.5, v37\n"
      "v_mfma_f32_32x32x16_bf16 v[84:99], a[216:219], v[236:239], v[84:99]\n"
      "ds_read_b128 v[28:31], v12 offset:512\n"
      "ds_read_b128 v[32:35], v12 offset:54016\n"
      "ds_read_b128 v[44:47], v12 offset:1792\n"
      "v_fmac_f32 v42, -0.5, v38\n"
      "v_fmac_f32 v43, -0.5, v39\n"
      "v_add_f32_e64 v36, v36, v128\n"
      "v_mfma_f32_32x32x16_bf16 v[52:67], a[220:223], v[168:171], v[52:67]\n"
      "ds_read_b128 a[216:219], v2 offset:64\n"
      "ds_read_b128 v[48:51], v12 offset:55296\n"
      "v_add_f32 v37, v37, v129\n"
      "v_add_f32 v38, v38, v130\n"
      "v_add_f32 v39, v39, v131\n"
      "v_mul_f32 v40, s46, v40\n"
      "v_mfma_f32_32x32x16_bf16 v[68:83], a[220:223], a[128:131], v[68:83]\n"
      "v_mul_f32 v41, s46, v41\n"
      "v_mul_f32 v42, s46, v42\n"
      "v_mul_f32 v43, s46, v43\n"
      "v_sub_f32 v36, v36, v40\n"
      "v_cvt_pk_bf16_f32 v20, v20, v24\n"
      "v_sub_f32 v37, v37, v41\n"
      "v_cvt_pk_bf16_f32 v21, v21, v25\n"
      "v_mfma_f32_32x32x16_bf16 v[84:99], a[220:223], v[240:243], v[84:99]\n"
      "ds_write_addtid_b32 v20 offset:17408\n"
      "ds_write_addtid_b32 v21 offset:20992\n"
      "v_cvt_pk_bf16_f32 v22, v22, v26\n"
      "v_cvt_pk_bf16_f32 v23, v23, v27\n"
      "v_sub_f32 v38, v38, v42\n"
      "v_sub_f32 v39, v39, v43\n"
      "s_waitcnt lgkmcnt(2)\n"
      "v_mfma_f32_32x32x16_bf16 v[52:67], a[224:227], v[172:175], v[52:67]\n"
      "ds_write_addtid_b32 v22 offset:24576\n"
      "ds_write_addtid_b32 v23 offset:28160\n"
      "v_fmac_f32 v40, s45, v104\n"
      "v_fmac_f32 v41, s45, v105\n"
      "v_fmac_f32 v42, s45, v106\n"
      "v_fmac_f32 v43, s45, v107\n"
      "v_fmac_f32 v36, 4.0, v104\n"
      "v_mfma_f32_32x32x16_bf16 v[68:83], a[224:227], a[132:135], v[68:83]\n"
      "v_fmac_f32 v37, 4.0, v105\n"
      "v_fmac_f32 v38, 4.0, v106\n"
      "v_fmac_f32 v39, 4.0, v107\n"
      "v_cvt_pk_bf16_f32 v40, v40, v41\n"
      "v_cvt_pk_bf16_f32 v41, v42, v43\n"
      "v_cvt_pk_bf16_f32 v36, v36, v37\n"
      "v_cvt_pk_bf16_f32 v37, v38, v39\n"
      "v_mfma_f32_32x32x16_bf16 v[84:99], a[224:227], v[244:247], v[84:99]\n"
      "ds_write_b64 v13, v[40:41] offset:12352\n"
      "ds_write_b64 v13, v[36:37] offset:64\n"
      "v_sub_f32 v28, v28, v44\n"
      "s_waitcnt lgkmcnt(2)\n"
      "s_barrier\n"
      "v_mfma_f32_32x32x16_bf16 v[52:67], a[228:231], v[176:179], v[52:67]\n"
      "ds_read_b128 a[220:223], v0 offset:14336\n"
      "ds_read_b128 v[36:39], v12 offset:2048\n"
      "ds_read_b128 v[40:43], v12 offset:55552\n"
      "v_sub_f32 v29, v29, v45\n"
      "v_sub_f32 v30, v30, v46\n"
      "v_sub_f32 v31, v31, v47\n"
      "v_mfma_f32_32x32x16_bf16 v[68:83], a[228:231], a[136:139], v[68:83]\n"
      "ds_read_b128 a[224:227], v0 offset:14368\n"
      "v_sub_f32 v32, v32, v48\n"
      "v_sub_f32 v33, v33, v49\n"
      "v_sub_f32 v34, v34, v50\n"
      "v_sub_f32 v35, v35, v51\n"
      "v_fmac_f32_e64 v32, -0.5, v28\n"
      "v_mfma_f32_32x32x16_bf16 v[84:99], a[228:231], v[248:251], v[84:99]\n"
      "ds_read_b128 v[44:47], v12 offset:2304\n"
      "ds_read_b128 v[48:51], v12 offset:55808\n"
      "s_add_u32 s54, s54, 196608\n"
      "s_add_u32 m0, s49, 107520\n"
      "v_fmac_f32 v33, -0.5, v29\n"
      "v_fmac_f32 v34, -0.5, v30\n"
      "v_mfma_f32_32x32x16_bf16 v[52:67], a[216:219], v[180:183], v[52:67]\n"
      "buffer_load_dwordx4 v15, s[4:7], s54 offen offset:0 nt lds\n"
      "ds_read_b128 a[228:231], v0 offset:14400\n"
      "v_fmac_f32 v35, -0.5, v31\n"
      "v_add_f32 v28, v28, v136\n"
      "v_add_f32 v29, v29, v137\n"
      "v_mfma_f32_32x32x16_bf16 v[68:83], a[216:219], a[140:143], v[68:83]\n"
      "v_add_f32 v30, v30, v138\n"
      "v_add_f32 v31, v31, v139\n"
      "v_mul_f32 v32, s46, v32\n"
      "v_mul_f32 v33, s46, v33\n"
      "v_mul_f32 v34, s46, v34\n"
      "v_mul_f32 v35, s46, v35\n"
      "v_sub_f32 v28, v28, v32\n"
      "v_mfma_f32_32x32x16_bf16 v[84:99], a[216:219], v[252:255], v[84:99]\n"
      "v_sub_f32 v29, v29, v33\n"
      "v_sub_f32 v30, v30, v34\n"
      "v_sub_f32 v31, v31, v35\n"
      "v_fmac_f32 v32, s45, v112\n"
      "v_fmac_f32 v33, s45, v113\n"
      "v_fmac_f32 v34, s45, v114\n"
      "v_fmac_f32 v35, s45, v115\n"
      "s_waitcnt lgkmcnt(0)\n"
      "v_mfma_f32_32x32x16_bf16 v[52:67], a[220:223], v[184:187], v[52:67]\n"
      "ds_read_b128 a[216:219], v1 offset:14336\n"
      "v_fmac_f32 v28, 4.0, v112\n"
      "v_fmac_f32 v29, 4.0, v113\n"
      "v_fmac_f32 v30, 4.0, v114\n"
      "v_fmac_f32 v31, 4.0, v115\n"
      "v_cvt_pk_bf16_f32 v32, v32, v33\n"
      "v_mfma_f32_32x32x16_bf16 v[68:83], a[220:223], a[144:147], v[68:83]\n"
      "buffer_load_dwordx4 v16, s[4:7], s54 offen offset:1024 nt lds\n"
      "v_cvt_pk_bf16_f32 v33, v34, v35\n"
      "v_cvt_pk_bf16_f32 v28, v28, v29\n"
      "v_cvt_pk_bf16_f32 v29, v30, v31\n"
      "v_sub_f32 v36, v36, v44\n"
      "v_mfma_f32_32x32x16_bf16 v[84:99], a[220:223], a[180:183], v[84:99]\n"
      "ds_write_b64 v13, v[32:33] offset:15360\n"
      "ds_write_b64 v13, v[28:29] offset:3072\n"
      "v_sub_f32 v37, v37, v45\n"
      "v_mfma_f32_32x32x16_bf16 v[52:67], a[224:227], v[188:191], v[52:67]\n"
      "ds_read_b128 a[220:223], v1 offset:14368\n"
      "ds_read_b128 v[28:31], v12 offset:1536\n"
      "ds_read_b128 v[32:35], v12 offset:55040\n"
      "v_sub_f32 v38, v38, v46\n"
      "v_sub_f32 v39, v39, v47\n"
      "v_sub_f32 v40, v40, v48\n"
      "v_mfma_f32_32x32x16_bf16 v[68:83], a[224:227], a[148:151], v[68:83]\n"
      "v_sub_f32 v41, v41, v49\n"
      "v_sub_f32 v42, v42, v50\n"
      "v_sub_f32 v43, v43, v51\n"
      "v_fmac_f32 v40, -0.5, v36\n"
      "v_fmac_f32 v41, -0.5, v37\n"
      "v_fmac_f32_e64 v42, -0.5, v38\n"
      "v_mfma_f32_32x32x16_bf16 v[84:99], a[224:227], a[184:187], v[84:99]\n"
      "buffer_load_dwordx4 v17, s[4:7], s54 offen offset:2048 nt lds\n"
      "ds_read_b128 v[44:47], v12 offset:768\n"
      "ds_read_b128 v[48:51], v12 offset:54272\n"
      "v_fmac_f32 v43, -0.5, v39\n"
      "v_add_f32 v36, v36, v132\n"
      "v_add_f32 v37, v37, v133\n"
      "v_mfma_f32_32x32x16_bf16 v[52:67], a[228:231], v[192:195], v[52:67]\n"
      "ds_read_b128 a[224:227], v1 offset:14400\n"
      "v_add_f32 v38, v38, v134\n"
      "v_add_f32 v39, v39, v135\n"
      "v_mul_f32 v40, s46, v40\n"
      "v_mul_f32 v41, s46, v41\n"
      "v_mul_f32 v42, s46, v42\n"
      "v_mfma_f32_32x32x16_bf16 v[68:83], a[228:231], a[152:155], v[68:83]\n"
      "v_mul_f32 v43, s46, v43\n"
      "v_sub_f32 v36, v36, v40\n"
      "v_sub_f32 v37, v37, v41\n"
      "v_sub_f32 v38, v38, v42\n"
      "v_sub_f32 v39, v39, v43\n"
      "v_fmac_f32 v40, s45, v108\n"
      "v_fmac_f32_e64 v41, s45, v109\n"
      "v_mfma_f32_32x32x16_bf16 v[84:99], a[228:231], a[188:191], v[84:99]\n"
      "s_add_u32 m0, s56, 107520\n"
      "v_fmac_f32 v42, s45, v110\n"
      "v_fmac_f32 v43, s45, v111\n"
      "v_fmac_f32 v36, 4.0, v108\n"
      "v_fmac_f32 v37, 4.0, v109\n"
      "v_fmac_f32 v38, 4.0, v110\n"
      "v_fmac_f32_e64 v39, 4.0, v111\n"
      "s_waitcnt lgkmcnt(0)\n"
      "v_mfma_f32_32x32x16_bf16 v[52:67], a[216:219], v[196:199], v[52:67]\n"
      "buffer_load_dwordx4 v18, s[4:7], s54 offen offset:3072 nt lds\n"
      "ds_read_b128 a[228:231], v2 offset:14336\n"
      "v_cvt_pk_bf16_f32 v40, v40, v41\n"
      "v_cvt_pk_bf16_f32 v41, v42, v43\n"
      "v_cvt_pk_bf16_f32 v36, v36, v37\n"
      "v_mfma_f32_32x32x16_bf16 v[68:83], a[216:219], a[156:159], v[68:83]\n"
      "ds_write_b64 v13, v[40:41] offset:12416\n"
      "v_cvt_pk_bf16_f32 v37, v38, v39\n"
      "v_sub_f32 v28, v28, v44\n"
      "v_sub_f32 v29, v29, v45\n"
      "v_mfma_f32_32x32x16_bf16 v[84:99], a[216:219], a[192:195], v[84:99]\n"
      "ds_write_b64 v13, v[36:37] offset:128\n"
      "v_sub_f32 v30, v30, v46\n"
      "v_sub_f32 v31, v31, v47\n"
      "v_sub_f32 v32, v32, v48\n"
      "v_mfma_f32_32x32x16_bf16 v[52:67], a[220:223], v[200:203], v[52:67]\n"
      "ds_read_b128 a[216:219], v2 offset:14368\n"
      "ds_read_b128 v[36:39], v12 offset:2560\n"
      "ds_read_b128 v[40:43], v12 offset:56064\n"
      "v_sub_f32 v33, v33, v49\n"
      "v_sub_f32 v34, v34, v50\n"
      "v_sub_f32 v35, v35, v51\n"
      "v_fmac_f32 v32, -0.5, v28\n"
      "v_fmac_f32 v33, -0.5, v29\n"
      "v_fmac_f32 v34, -0.5, v30\n"
      "v_fmac_f32 v35, -0.5, v31\n"
      "v_add_f32 v28, v28, v140\n"
      "v_add_f32 v29, v29, v141\n"
      "v_add_f32 v30, v30, v142\n"
      "v_add_f32 v31, v31, v143\n"
      "v_mfma_f32_32x32x16_bf16 v[68:83], a[220:223], a[160:163], v[68:83]\n"
      "ds_read_b128 v[44:47], v12 offset:2816\n"
      "ds_read_b128 v[48:51], v12 offset:56320\n"
      "v_mul_f32 v32, s46, v32\n"
      "v_mul_f32 v33, s46, v33\n"
      "v_mul_f32 v34, s46, v34\n"
      "v_mul_f32 v35, s46, v35\n"
      "v_mfma_f32_32x32x16_bf16 v[84:99], a[220:223], a[196:199], v[84:99]\n"
      "ds_read_b128 a[220:223], v2 offset:14400\n"
      "v_sub_f32 v28, v28, v32\n"
      "v_sub_f32 v29, v29, v33\n"
      "v_sub_f32 v30, v30, v34\n"
      "v_sub_f32 v31, v31, v35\n"
      "v_fmac_f32 v32, s45, v116\n"
      "v_mfma_f32_32x32x16_bf16 v[52:67], a[224:227], v[204:207], v[52:67]\n"
      "v_fmac_f32 v33, s45, v117\n"
      "v_fmac_f32 v34, s45, v118\n"
      "v_fmac_f32 v35, s45, v119\n"
      "v_fmac_f32 v28, 4.0, v116\n"
      "v_fmac_f32 v29, 4.0, v117\n"
      "v_fmac_f32 v30, 4.0, v118\n"
      "s_waitcnt lgkmcnt(1)\n"
      "s_waitcnt vmcnt(4)\n"
      "s_barrier\n"
      "v_mfma_f32_32x32x16_bf16 v[68:83], a[224:227], a[164:167], v[68:83]\n"
      "v_fmac_f32_e64 v31, 4.0, v119\n"
      "v_cvt_pk_bf16_f32 v32, v32, v33\n"
      "v_cvt_pk_bf16_f32 v33, v34, v35\n"
      "v_cvt_pk_bf16_f32 v28, v28, v29\n"
      "v_cvt_pk_bf16_f32 v29, v30, v31\n"
      "v_mfma_f32_32x32x16_bf16 v[84:99], a[224:227], a[200:203], v[84:99]\n"
      "ds_read_b64_tr_b16 a[242:243], v14 offset:3072\n"
      "ds_read_b64_tr_b16 a[244:245], v14 offset:3080\n"
      "ds_write_b64 v13, v[32:33] offset:15424\n"
      "v_mfma_f32_32x32x16_bf16 v[52:67], a[228:231], v[208:211], v[52:67]\n"
      "ds_write_b64 v13, v[28:29] offset:3136\n"
      "v_sub_f32 v36, v36, v44\n"
      "v_sub_f32 v37, v37, v45\n"
      "v_sub_f32 v38, v38, v46\n"
      "v_mfma_f32_32x32x16_bf16 v[68:83], a[228:231], a[168:171], v[68:83]\n"
      "v_sub_f32 v39, v39, v47\n"
      "v_sub_f32 v40, v40, v48\n"
      "v_sub_f32 v41, v41, v49\n"
      "v_sub_f32 v42, v42, v50\n"
      "v_sub_f32 v43, v43, v51\n"
      "v_fmac_f32 v40, -0.5, v36\n"
      "v_mfma_f32_32x32x16_bf16 v[84:99], a[228:231], a[204:207], v[84:99]\n"
      "v_fmac_f32 v41, -0.5, v37\n"
      "v_fmac_f32 v42, -0.5, v38\n"
      "v_fmac_f32 v43, -0.5, v39\n"
      "v_add_f32 v36, v36, v144\n"
      "v_add_f32 v37, v37, v145\n"
      "v_add_f32 v38, v38, v146\n"
      "v_mfma_f32_32x32x16_bf16 v[52:67], a[216:219], v[212:215], v[52:67]\n"
      "ds_read_b128 a[224:227], v3 offset:29184\n"
      "v_add_f32 v39, v39, v147\n"
      "v_mul_f32 v40, s46, v40\n"
      "v_mul_f32 v41, s46, v41\n"
      "v_mul_f32 v42, s46, v42\n"
      "s_waitcnt lgkmcnt(1)\n"
      "v_mfma_f32_32x32x16_bf16 v[52:67], a[220:223], v[216:219], v[52:67]\n"
      "v_mul_f32 v43, s46, v43\n"
      "v_sub_f32 v36, v36, v40\n"
      "v_sub_f32 v37, v37, v41\n"
      "v_sub_f32 v38, v38, v42\n"
      "v_sub_f32 v39, v39, v43\n"
      "v_fmac_f32 v40, s45, v120\n"
      "v_mfma_f32_32x32x16_bf16 v[68:83], a[216:219], a[172:175], v[68:83]\n"
      "ds_read_b128 a[250:253], v4 offset:29184\n"
      "v_fmac_f32 v41, s45, v121\n"
      "v_fmac_f32 v42, s45, v122\n"
      "v_fmac_f32_e64 v43, s45, v123\n"
      "v_fmac_f32_e64 v36, 4.0, v120\n"
      "v_fmac_f32_e64 v37, 4.0, v121\n"
      "v_mfma_f32_32x32x16_bf16 v[68:83], a[220:223], a[176:179], v[68:83]\n"
      "s_mov_b32 m0, s64\n"
      "s_nop 0\n"
      "ds_write_addtid_b32 v52 offset:28672\n"
      "ds_write_addtid_b32 v53 offset:31776\n"
      "s_add_u32 s54, s54, 196608\n"
      "s_add_u32 m0, s49, 114696\n"
      "v_mfma_f32_4x4x4_16b_bf16 v[20:23], a[232:233], a[242:243], 0\n"
      "v_fmac_f32_e64 v38, 4.0, v122\n"
      "v_mfma_f32_4x4x4_16b_bf16 v[24:27], a[232:233], a[244:245], 0\n"
      "v_fmac_f32_e64 v39, 4.0, v123\n"
      "v_mfma_f32_32x32x16_bf16 v[84:99], a[216:219], a[208:211], v[84:99]\n"
      "buffer_load_dwordx4 v15, s[4:7], s54 offen offset:0 nt lds\n"
      "ds_read_b128 a[216:219], v5 offset:29184\n"
      "v_cvt_pk_bf16_f32 v40, v40, v41\n"
      "v_cvt_pk_bf16_f32 v20, v20, v24\n"
      "v_cvt_pk_bf16_f32 v21, v21, v25\n"
      "v_mfma_f32_32x32x16_bf16 v[84:99], a[220:223], a[212:215], v[84:99]\n"
      "ds_read_b128 a[220:223], v6 offset:29184\n"
      "v_cvt_pk_bf16_f32 v22, v22, v26\n"
      "v_cvt_pk_bf16_f32 v23, v23, v27\n"
      "s_nop 0\n"
      "s_waitcnt lgkmcnt(2)\n"
      "v_mfma_f32_16x16x32_bf16 v[124:127], a[0:3], a[224:227], 0\n"
      "buffer_load_dwordx4 v16, s[4:7], s54 offen offset:1024 nt lds\n"
      "v_mfma_f32_16x16x32_bf16 v[128:131], a[4:7], a[224:227], 0\n"
      "v_cvt_pk_bf16_f32 v41, v42, v43\n"
      "v_mfma_f32_16x16x32_bf16 v[132:135], a[8:11], a[224:227], 0\n"
      "buffer_load_dwordx4 v17, s[4:7], s54 offen offset:2048 nt lds\n"
      "ds_read_b128 a[224:227], v7 offset:29184\n"
      "v_mfma_f32_16x16x32_bf16 v[124:127], a[12:15], a[250:253], v[124:127]\n"
      "ds_write_b64 v13, v[40:41] offset:15488\n"
      "v_mfma_f32_16x16x32_bf16 v[128:131], a[16:19], a[250:253], v[128:131]\n"
      "ds_read_b64_tr_b16 a[242:243], v14 offset:4096\n"
      "ds_read_b64_tr_b16 a[244:245], v14 offset:4104\n"
      "v_mfma_f32_16x16x32_bf16 v[132:135], a[20:23], a[250:253], v[132:135]\n"
      "ds_read_b128 a[228:231], v8 offset:29184\n"
      "s_mov_b32 m0, s64\n"
      "s_nop 0\n"
      "ds_write_addtid_b32 v20 offset:1536\n"
      "s_nop 0\n"
      "s_waitcnt lgkmcnt(6)\n"
      "v_mfma_f32_16x16x32_bf16 v[124:127], a[24:27], a[216:219], v[124:127]\n"
      "ds_write_addtid_b32 v21 offset:5120\n"
      "ds_write_addtid_b32 v22 offset:8704\n"
      "v_mfma_f32_16x16x32_bf16 v[128:131], a[28:31], a[216:219], v[128:131]\n"
      "ds_write_addtid_b32 v23 offset:12288\n"
      "ds_write_addtid_b32 v54 offset:34880\n"
      "v_mfma_f32_16x16x32_bf16 v[132:135], a[32:35], a[216:219], v[132:135]\n"
      "ds_read_b128 a[216:219], v9 offset:29184\n"
      "ds_write_addtid_b32 v55 offset:37984\n"
      "v_mfma_f32_16x16x32_bf16 v[124:127], a[36:39], a[220:223], v[124:127]\n"
      "ds_write_addtid_b32 v56 offset:41088\n"
      "ds_write_addtid_b32 v57 offset:44192\n"
      "s_nop 0\n"
      "s_waitcnt lgkmcnt(9)\n"
      "v_mfma_f32_4x4x4_16b_bf16 v[20:23], a[232:233], a[242:243], 0\n"
      "ds_write_addtid_b32 v58 offset:47296\n"
      "ds_write_addtid_b32 v59 offset:50400\n"
      "v_mfma_f32_4x4x4_16b_bf16 v[24:27], a[232:233], a[244:245], 0\n"
      "ds_write_addtid_b32 v60 offset:29184\n"
      "ds_write_addtid_b32 v61 offset:32288\n"
      "v_cvt_pk_bf16_f32 v36, v36, v37\n"
      "v_mfma_f32_16x16x32_bf16 v[128:131], a[40:43], a[220:223], v[128:131]\n"
      "ds_write_addtid_b32 v62 offset:35392\n"
      "ds_write_addtid_b32 v63 offset:38496\n"
      "v_mfma_f32_16x16x32_bf16 v[132:135], a[44:47], a[220:223], v[132:135]\n"
      "ds_read_b128 a[220:223], v10 offset:29184\n"
      "ds_write_addtid_b32 v64 offset:41600\n"
      "v_mfma_f32_16x16x32_bf16 v[124:127], a[48:51], a[224:227], v[124:127]\n"
      "ds_write_addtid_b32 v65 offset:44704\n"
      "ds_write_addtid_b32 v66 offset:47808\n"
      "v_mfma_f32_16x16x32_bf16 v[128:131], a[52:55], a[224:227], v[128:131]\n"
      "ds_write_addtid_b32 v67 offset:50912\n"
      "ds_write_addtid_b32 v68 offset:29696\n"
      "v_mfma_f32_16x16x32_bf16 v[132:135], a[56:59], a[224:227], v[132:135]\n"
      "ds_read_b128 a[224:227], v11 offset:29184\n"
      "ds_write_addtid_b32 v69 offset:32800\n"
      "v_mfma_f32_16x16x32_bf16 v[124:127], a[60:63], a[228:231], v[124:127]\n"
      "ds_write_addtid_b32 v70 offset:35904\n"
      "v_cvt_pk_bf16_f32 v20, v20, v24\n"
      "v_mfma_f32_16x16x32_bf16 v[128:131], a[64:67], a[228:231], v[128:131]\n"
      "ds_write_addtid_b32 v20 offset:2048\n"
      "ds_write_addtid_b32 v71 offset:39008\n"
      "v_mfma_f32_16x16x32_bf16 v[132:135], a[68:71], a[228:231], v[132:135]\n"
      "ds_read_b128 a[228:231], v3 offset:32256\n"
      "s_nop 0\n"
      "s_waitcnt lgkmcnt(7)\n"
      "v_mfma_f32_16x16x32_bf16 v[124:127], a[72:75], a[216:219], v[124:127]\n"
      "ds_read_b64_tr_b16 a[242:243], v14 offset:5120\n"
      "ds_read_b64_tr_b16 a[244:245], v14 offset:5128\n"
      "v_mfma_f32_16x16x32_bf16 v[128:131], a[76:79], a[216:219], v[128:131]\n"
      "ds_write_addtid_b32 v72 offset:42112\n"
      "v_cvt_pk_bf16_f32 v21, v21, v25\n"
      "v_mfma_f32_16x16x32_bf16 v[132:135], a[80:83], a[216:219], v[132:135]\n"
      "ds_read_b128 a[216:219], v4 offset:32256\n"
      "ds_write_addtid_b32 v21 offset:5632\n"
      "v_mfma_f32_16x16x32_bf16 v[124:127], a[84:87], a[220:223], v[124:127]\n"
      "ds_write_addtid_b32 v73 offset:45216\n"
      "v_cvt_pk_bf16_f32 v22, v22, v26\n"
      "v_mfma_f32_16x16x32_bf16 v[128:131], a[88:91], a[220:223], v[128:131]\n"
      "ds_write_addtid_b32 v22 offset:9216\n"
      "ds_write_addtid_b32 v74 offset:48320\n"
      "v_mfma_f32_16x16x32_bf16 v[132:135], a[92:95], a[220:223], v[132:135]\n"
      "ds_read_b128 a[220:223], v5 offset:32256\n"
      "v_cvt_pk_bf16_f32 v23, v23, v27\n"
      "s_nop 0\n"
      "s_waitcnt lgkmcnt(6)\n"
      "v_mfma_f32_16x16x32_bf16 v[124:127], a[96:99], a[224:227], v[124:127]\n"
      "ds_write_addtid_b32 v23 offset:12800\n"
      "ds_write_addtid_b32 v75 offset:51424\n"
      "v_mfma_f32_4x4x4_16b_bf16 v[20:23], a[232:233], a[242:243], 0\n"
      "ds_write_addtid_b32 v76 offset:30208\n"
      "v_mfma_f32_4x4x4_16b_bf16 v[24:27], a[232:233], a[244:245], 0\n"
      "ds_write_addtid_b32 v77 offset:33312\n"
      "v_cvt_pk_bf16_f32 v37, v38, v39\n"
      "v_mfma_f32_16x16x32_bf16 v[128:131], a[100:103], a[224:227], v[128:131]\n"
      "ds_write_b64 v13, v[36:37] offset:3200\n"
      "v_mfma_f32_16x16x32_bf16 v[132:135], a[104:107], a[224:227], v[132:135]\n"
      "ds_read_b128 a[224:227], v6 offset:32256\n"
      "ds_write_addtid_b32 v78 offset:36416\n"
      "v_mfma_f32_16x16x32_bf16 v[136:139], a[0:3], a[228:231], 0\n"
      "ds_read_b64_tr_b16 a[242:243], v14 offset:6144\n"
      "ds_read_b64_tr_b16 a[244:245], v14 offset:6152\n"
      "v_mfma_f32_16x16x32_bf16 v[140:143], a[4:7], a[228:231], 0\n"
      "v_cvt_pk_bf16_f32 v20, v20, v24\n"
      "v_cvt_pk_bf16_f32 v21, v21, v25\n"
      "v_mfma_f32_16x16x32_bf16 v[144:147], a[8:11], a[228:231], 0\n"
      "ds_read_b128 a[228:231], v7 offset:32256\n"
      "s_nop 0\n"
      "s_waitcnt lgkmcnt(6)\n"
      "v_mfma_f32_16x16x32_bf16 v[136:139], a[12:15], a[216:219], v[136:139]\n"
      "s_add_u32 s54, s54, 196608\n"
      "s_add_u32 m0, s49, 121992\n"
      "v_mfma_f32_16x16x32_bf16 v[140:143], a[16:19], a[216:219], v[140:143]\n"
      "buffer_load_dwordx4 v15, s[4:7], s54 offen offset:0 nt lds\n"
      "v_mfma_f32_16x16x32_bf16 v[144:147], a[20:23], a[216:219], v[144:147]\n"
      "ds_read_b128 a[216:219], v8 offset:32256\n"
      "v_cvt_pk_bf16_f32 v22, v22, v26\n"
      "v_mfma_f32_16x16x32_bf16 v[136:139], a[24:27], a[220:223], v[136:139]\n"
      "v_cvt_pk_bf16_f32 v23, v23, v27\n"
      "v_mfma_f32_16x16x32_bf16 v[140:143], a[28:31], a[220:223], v[140:143]\n"
      "buffer_load_dwordx4 v16, s[4:7], s54 offen offset:1024 nt lds\n"
      "s_mov_b32 m0, s64\n"
      "s_nop 0\n"
      "ds_write_addtid_b32 v20 offset:2560\n"
      "ds_write_addtid_b32 v21 offset:6144\n"
      "ds_write_addtid_b32 v22 offset:9728\n"
      "ds_write_addtid_b32 v23 offset:13312\n"
      "s_nop 0\n"
      "s_waitcnt lgkmcnt(5)\n"
      "v_mfma_f32_4x4x4_16b_bf16 v[20:23], a[232:233], a[242:243], 0\n"
      "ds_write_addtid_b32 v79 offset:39520\n"
      "v_mfma_f32_4x4x4_16b_bf16 v[24:27], a[232:233], a[244:245], 0\n"
      "ds_write_addtid_b32 v80 offset:42624\n"
      "ds_write_addtid_b32 v81 offset:45728\n"
      "v_mfma_f32_16x16x32_bf16 v[144:147], a[32:35], a[220:223], v[144:147]\n"
      "ds_read_b128 a[220:223], v9 offset:32256\n"
      "ds_read_b64_tr_b16 a[242:243], v14 offset:4608\n"
      "v_mfma_f32_16x16x32_bf16 v[136:139], a[36:39], a[224:227], v[136:139]\n"
      "ds_read_b64_tr_b16 a[244:245], v14 offset:4616\n"
      "ds_write_addtid_b32 v82 offset:48832\n"
      "v_mfma_f32_16x16x32_bf16 v[140:143], a[40:43], a[224:227], v[140:143]\n"
      "s_add_u32 m0, s49, 121992\n"
      "s_nop 0\n"
      "buffer_load_dwordx4 v17, s[4:7], s54 offen offset:2048 nt lds\n"
      "v_mfma_f32_16x16x32_bf16 v[144:147], a[44:47], a[224:227], v[144:147]\n"
      "ds_read_b128 a[224:227], v10 offset:32256\n"
      "v_cvt_pk_bf16_f32 v20, v20, v24\n"
      "v_mfma_f32_16x16x32_bf16 v[136:139], a[48:51], a[228:231], v[136:139]\n"
      "ds_read_b128 a[246:249], v11 offset:32256\n"
      "s_mov_b32 m0, s64\n"
      "s_nop 0\n"
      "ds_write_addtid_b32 v20 offset:3072\n"
      "v_mfma_f32_16x16x32_bf16 v[140:143], a[52:55], a[228:231], v[140:143]\n"
      "v_cvt_pk_bf16_f32 v21, v21, v25\n"
      "v_cvt_pk_bf16_f32 v22, v22, v26\n"
      "v_mfma_f32_16x16x32_bf16 v[144:147], a[56:59], a[228:231], v[144:147]\n"
      "ds_write_addtid_b32 v21 offset:6656\n"
      "v_cvt_pk_bf16_f32 v23, v23, v27\n"
      "s_waitcnt lgkmcnt(4)\n"
      "v_mfma_f32_16x16x32_bf16 v[136:139], a[60:63], a[216:219], v[136:139]\n"
      "ds_write_addtid_b32 v22 offset:10240\n"
      "ds_write_addtid_b32 v23 offset:13824\n"
      "ds_write_addtid_b32 v83 offset:51936\n"
      "v_mfma_f32_16x16x32_bf16 v[140:143], a[64:67], a[216:219], v[140:143]\n"
      "v_mfma_f32_16x16x32_bf16 v[144:147], a[68:71], a[216:219], v[144:147]\n"
      "s_waitcnt lgkmcnt(0)\n"
      "s_barrier\n"
      "LDmid_%=:\n"
      "v_mfma_f32_4x4x4_16b_bf16 v[20:23], a[232:233], a[242:243], 0\n"
      "v_mfma_f32_4x4x4_16b_bf16 v[24:27], a[232:233], a[244:245], 0\n"
      "v_mfma_f32_16x16x32_bf16 v[136:139], a[72:75], a[220:223], v[136:139]\n"
      "ds_read_b128 a[216:219], v0 offset:0\n"
      "s_add_u32 m0, s57, 121992\n"
      "v_mfma_f32_16x16x32_bf16 v[140:143], a[76:79], a[220:223], v[140:143]\n"
      "buffer_load_dwordx4 v18, s[4:7], s54 offen offset:3072 nt lds\n"
      "v_mfma_f32_16x16x32_bf16 v[144:147], a[80:83], a[220:223], v[144:147]\n"
      "ds_read_b128 a[220:223], v0 offset:32\n"
      "ds_read_b128 v[28:31], v12 offset:0\n"
      "v_mfma_f32_16x16x32_bf16 v[136:139], a[84:87], a[224:227], v[136:139]\n"
      "ds_read_b128 v[32:35], v12 offset:53504\n"
      "ds_read_b128 v[44:47], v12 offset:1280\n"
      "v_mfma_f32_16x16x32_bf16 v[140:143], a[88:91], a[224:227], v[140:143]\n"
      "s_add_u32 s54, s54, 196608\n"
      "s_add_u32 m0, s49, 129408\n"
      "v_mfma_f32_16x16x32_bf16 v[144:147], a[92:95], a[224:227], v[144:147]\n"
      "buffer_load_dwordx4 v15, s[4:7], s54 offen offset:0 nt lds\n"
      "v_mfma_f32_16x16x32_bf16 v[136:139], a[96:99], a[246:249], v[136:139]\n"
      "ds_read_b128 a[224:227], v0 offset:64\n"
      "ds_read_b128 v[48:51], v12 offset:54784\n"
      "v_mfma_f32_16x16x32_bf16 v[140:143], a[100:103], a[246:249], v[140:143]\n"
      "ds_read_b64_tr_b16 a[242:243], v14 offset:5632\n"
      "ds_read_b64_tr_b16 a[244:245], v14 offset:5640\n"
      "v_mfma_f32_16x16x32_bf16 v[144:147], a[104:107], a[246:249], v[144:147]\n"
      "ds_read_b128 v[36:39], v12 offset:1024\n"
      "s_waitcnt lgkmcnt(8)\n"
      "v_mfma_f32_32x32x16_bf16 v[52:67], a[216:219], v[148:151], 0\n"
      "ds_read_b128 a[228:231], v1 offset:0\n"
      "ds_read_b128 v[40:43], v12 offset:54528\n"
      "v_cvt_pk_bf16_f32 v20, v20, v24\n"
      "s_waitcnt lgkmcnt(7)\n"
      "v_sub_f32 v28, v28, v44\n"
      "v_sub_f32 v29, v29, v45\n"
      "v_mfma_f32_32x32x16_bf16 v[52:67], a[220:223], v[152:155], v[52:67]\n"
      "buffer_load_dwordx4 v16, s[4:7], s54 offen offset:1024 nt lds\n"
      "v_sub_f32 v30, v30, v46\n"
      "v_sub_f32 v31, v31, v47\n"
      "v_cvt_pk_bf16_f32 v21, v21, v25\n"
      "v_cvt_pk_bf16_f32 v22, v22, v26\n"
      "v_cvt_pk_bf16_f32 v23, v23, v27\n"
      "v_mfma_f32_32x32x16_bf16 v[68:83], a[216:219], a[108:111], 0\n"
      "s_waitcnt lgkmcnt(5)\n"
      "v_sub_f32 v32, v32, v48\n"
      "v_sub_f32 v33, v33, v49\n"
      "v_sub_f32 v34, v34, v50\n"
      "v_sub_f32 v35, v35, v51\n"
      "v_fmac_f32 v32, -0.5, v28\n"
      "v_mfma_f32_32x32x16_bf16 v[68:83], a[220:223], a[112:115], v[68:83]\n"
      "s_mov_b32 m0, s64\n"
      "s_nop 0\n"
      "ds_write_addtid_b32 v84 offset:30720\n"
      "ds_write_addtid_b32 v85 offset:33824\n"
      "ds_write_addtid_b32 v86 offset:36928\n"
      "ds_write_addtid_b32 v87 offset:40032\n"
      "ds_write_addtid_b32 v88 offset:43136\n"
      "ds_write_addtid_b32 v89 offset:46240\n"
      "ds_write_addtid_b32 v90 offset:49344\n"
      "ds_write_addtid_b32 v91 offset:52448\n"
      "ds_write_addtid_b32 v92 offset:31232\n"
      "ds_write_addtid_b32 v93 offset:34336\n"
      "ds_write_addtid_b32 v94 offset:37440\n"
      "ds_write_addtid_b32 v95 offset:40544\n"
      "ds_write_addtid_b32 v96 offset:43648\n"
      "ds_write_addtid_b32 v97 offset:46752\n"
      "ds_write_addtid_b32 v98 offset:49856\n"
      "ds_write_addtid_b32 v99 offset:52960\n"
      "v_mfma_f32_32x32x16_bf16 v[84:99], a[216:219], v[220:223], 0\n"
      "s_add_u32 m0, s49, 129408\n"
      "s_nop 0\n"
      "buffer_load_dwordx4 v17, s[4:7], s54 offen offset:2048 nt lds\n"
      "ds_read_b128 a[216:219], v1 offset:32\n"
      "s_mov_b32 m0, s64\n"
      "s_nop 0\n"
      "ds_write_addtid_b32 v20 offset:16384\n"
      "v_mfma_f32_32x32x16_bf16 v[84:99], a[220:223], v[224:227], v[84:99]\n"
      "ds_write_addtid_b32 v21 offset:19968\n"
      "ds_write_addtid_b32 v22 offset:23552\n"
      "ds_write_addtid_b32 v23 offset:27136\n"
      "v_fmac_f32 v33, -0.5, v29\n"
      "v_fmac_f32 v34, -0.5, v30\n"
      "v_fmac_f32_e64 v35, -0.5, v31\n"
      "s_waitcnt lgkmcnt(10)\n"
      "v_mfma_f32_4x4x4_16b_bf16 v[20:23], a[232:233], a[242:243], 0\n"
      "ds_read_b128 v[44:47], v12 offset:256\n"
      "v_mfma_f32_4x4x4_16b_bf16 v[24:27], a[232:233], a[244:245], 0\n"
      "ds_read_b128 v[48:51], v12 offset:53760\n"
      "v_add_f32 v28, v28, v100\n"
      "v_add_f32 v29, v29, v101\n"
      "v_mfma_f32_32x32x16_bf16 v[52:67], a[224:227], v[156:159], v[52:67]\n"
      "ds_read_b128 a[220:223], v1 offset:64\n"
      "v_add_f32 v30, v30, v102\n"
      "v_add_f32 v31, v31, v103\n"
      "v_mul_f32 v32, s46, v32\n"
      "v_mul_f32 v33, s46, v33\n"
      "v_mul_f32 v34, s46, v34\n"
      "v_mfma_f32_32x32x16_bf16 v[68:83], a[224:227], a[116:119], v[68:83]\n"
      "ds_read_b64_tr_b16 a[242:243], v14 offset:6656\n"
      "ds_read_b64_tr_b16 a[244:245], v14 offset:6664\n"
      "v_mul_f32 v35, s46, v35\n"
      "v_sub_f32 v28, v28, v32\n"
      "v_sub_f32 v29, v29, v33\n"
      "v_cvt_pk_bf16_f32 v20, v20, v24\n"
      "v_mfma_f32_32x32x16_bf16 v[84:99], a[224:227], v[228:231], v[84:99]\n"
      "ds_write_addtid_b32 v20 offset:16896\n"
      "v_sub_f32 v30, v30, v34\n"
      "v_sub_f32 v31, v31, v35\n"
      "v_fmac_f32 v32, s45, v124\n"
      "v_fmac_f32 v33, s45, v125\n"
      "v_cvt_pk_bf16_f32 v21, v21, v25\n"
      "v_mfma_f32_32x32x16_bf16 v[52:67], a[228:231], v[160:163], v[52:67]\n"
      "ds_read_b128 a[224:227], v2 offset:0\n"
      "ds_write_addtid_b32 v21 offset:20480\n"
      "v_fmac_f32 v34, s45, v126\n"
      "v_fmac_f32 v35, s45, v127\n"
      "v_fmac_f32 v28, 4.0, v124\n"
      "v_cvt_pk_bf16_f32 v22, v22, v26\n"
      "v_mfma_f32_32x32x16_bf16 v[68:83], a[228:231], a[120:123], v[68:83]\n"
      "ds_write_addtid_b32 v22 offset:24064\n"
      "v_cvt_pk_bf16_f32 v23, v23, v27\n"
      "v_fmac_f32 v29, 4.0, v125\n"
      "v_fmac_f32 v30, 4.0, v126\n"
      "v_fmac_f32 v31, 4.0, v127\n"
      "v_cvt_pk_bf16_f32 v32, v32, v33\n"
      "v_mfma_f32_32x32x16_bf16 v[84:99], a[228:231], v[232:235], v[84:99]\n"
      "ds_write_addtid_b32 v23 offset:27648\n"
      "v_cvt_pk_bf16_f32 v33, v34, v35\n"
      "v_cvt_pk_bf16_f32 v28, v28, v29\n"
      "v_cvt_pk_bf16_f32 v29, v30, v31\n"
      "s_waitcnt lgkmcnt(4)\n"
      "v_sub_f32 v36, v36, v44\n"
      "v_mfma_f32_4x4x4_16b_bf16 v[20:23], a[232:233], a[242:243], 0\n"
      "v_sub_f32 v37, v37, v45\n"
      "v_mfma_f32_4x4x4_16b_bf16 v[24:27], a[232:233], a[244:245], 0\n"
      "v_sub_f32 v38, v38, v46\n"
      "v_sub_f32 v39, v39, v47\n"
      "v_mfma_f32_32x32x16_bf16 v[52:67], a[216:219], v[164:167], v[52:67]\n"
      "ds_read_b128 a[228:231], v2 offset:32\n"
      "ds_write_b64 v13, v[32:33] offset:12288\n"
      "ds_write_b64 v13, v[28:29] offset:0\n"
      "v_mfma_f32_32x32x16_bf16 v[68:83], a[216:219], a[124:127], v[68:83]\n"
      "v_sub_f32 v40, v40, v48\n"
      "v_sub_f32 v41, v41, v49\n"
      "v_sub_f32 v42, v42, v50\n"
      "v_sub_f32 v43, v43, v51\n"
      "v_fmac_f32 v40, -0.5, v36\n"
      "v_fmac_f32 v41, -0.5, v37\n"
      "v_mfma_f32_32x32x16_bf16 v[84:99], a[216:219], v[236:239], v[84:99]\n"
      "ds_read_b128 v[28:31], v12 offset:512\n"
      "ds_read_b128 v[32:35], v12 offset:54016\n"
      "ds_read_b128 v[44:47], v12 offset:1792\n"
      "v_fmac_f32 v42, -0.5, v38\n"
      "v_fmac_f32 v43, -0.5, v39\n"
      "v_add_f32 v36, v36, v104\n"
      "v_mfma_f32_32x32x16_bf16 v[52:67], a[220:223], v[168:171], v[52:67]\n"
      "ds_read_b128 a[216:219], v2 offset:64\n"
      "ds_read_b128 v[48:51], v12 offset:55296\n"
      "v_add_f32 v37, v37, v105\n"
      "v_add_f32 v38, v38, v106\n"
      "v_add_f32 v39, v39, v107\n"
      "v_mul_f32 v40, s46, v40\n"
      "v_mfma_f32_32x32x16_bf16 v[68:83], a[220:223], a[128:131], v[68:83]\n"
      "v_mul_f32 v41, s46, v41\n"
      "v_mul_f32 v42, s46, v42\n"
      "v_mul_f32 v43, s46, v43\n"
      "v_sub_f32 v36, v36, v40\n"
      "v_cvt_pk_bf16_f32 v20, v20, v24\n"
      "v_sub_f32_e64 v37, v37, v41\n"
      "v_cvt_pk_bf16_f32 v21, v21, v25\n"
      "v_mfma_f32_32x32x16_bf16 v[84:99], a[220:223], v[240:243], v[84:99]\n"
      "ds_write_addtid_b32 v20 offset:17408\n"
      "ds_write_addtid_b32 v21 offset:20992\n"
      "v_cvt_pk_bf16_f32 v22, v22, v26\n"
      "v_cvt_pk_bf16_f32 v23, v23, v27\n"
      "v_sub_f32 v38, v38, v42\n"
      "v_sub_f32_e64 v39, v39, v43\n"
      "s_waitcnt lgkmcnt(2)\n"
      "v_mfma_f32_32x32x16_bf16 v[52:67], a[224:227], v[172:175], v[52:67]\n"
      "ds_write_addtid_b32 v22 offset:24576\n"
      "ds_write_addtid_b32 v23 offset:28160\n"
      "v_fmac_f32 v40, s45, v128\n"
      "v_fmac_f32 v41, s45, v129\n"
      "v_fmac_f32 v42, s45, v130\n"
      "v_fmac_f32 v43, s45, v131\n"
      "v_fmac_f32 v36, 4.0, v128\n"
      "v_mfma_f32_32x32x16_bf16 v[68:83], a[224:227], a[132:135], v[68:83]\n"
      "v_fmac_f32 v37, 4.0, v129\n"
      "v_fmac_f32 v38, 4.0, v130\n"
      "v_fmac_f32 v39, 4.0, v131\n"
      "v_cvt_pk_bf16_f32 v40, v40, v41\n"
      "v_cvt_pk_bf16_f32 v41, v42, v43\n"
      "v_cvt_pk_bf16_f32 v36, v36, v37\n"
      "v_cvt_pk_bf16_f32 v37, v38, v39\n"
      "v_mfma_f32_32x32x16_bf16 v[84:99], a[224:227], v[244:247], v[84:99]\n"
      "ds_write_b64 v13, v[40:41] offset:12352\n"
      "ds_write_b64 v13, v[36:37] offset:64\n"
      "v_sub_f32 v28, v28, v44\n"
      "s_waitcnt lgkmcnt(2)\n"
      "s_barrier\n"
      "v_mfma_f32_32x32x16_bf16 v[52:67], a[228:231], v[176:179], v[52:67]\n"
      "ds_read_b128 a[220:223], v0 offset:14336\n"
      "ds_read_b128 v[36:39], v12 offset:2048\n"
      "ds_read_b128 v[40:43], v12 offset:55552\n"
      "v_sub_f32 v29, v29, v45\n"
      "v_sub_f32 v30, v30, v46\n"
      "v_sub_f32 v31, v31, v47\n"
      "v_mfma_f32_32x32x16_bf16 v[68:83], a[228:231], a[136:139], v[68:83]\n"
      "ds_read_b128 a[224:227], v0 offset:14368\n"
      "v_sub_f32 v32, v32, v48\n"
      "v_sub_f32 v33, v33, v49\n"
      "v_sub_f32 v34, v34, v50\n"
      "v_sub_f32 v35, v35, v51\n"
      "v_fmac_f32 v32, -0.5, v28\n"
      "v_mfma_f32_32x32x16_bf16 v[84:99], a[228:231], v[248:251], v[84:99]\n"
      "ds_read_b128 v[44:47], v12 offset:2304\n"
      "ds_read_b128 v[48:51], v12 offset:55808\n"
      "s_add_u32 s54, s54, 196608\n"
      "s_add_u32 m0, s49, 78336\n"
      "v_fmac_f32 v33, -0.5, v29\n"
      "v_fmac_f32 v34, -0.5, v30\n"
      "v_mfma_f32_32x32x16_bf16 v[52:67], a[216:219], v[180:183], v[52:67]\n"
      "buffer_load_dwordx4 v15, s[4:7], s54 offen offset:0 nt lds\n"
      "ds_read_b128 a[228:231], v0 offset:14400\n"
      "v_fmac_f32 v35, -0.5, v31\n"
      "v_add_f32 v28, v28, v112\n"
      "v_add_f32 v29, v29, v113\n"
      "v_mfma_f32_32x32x16_bf16 v[68:83], a[216:219], a[140:143], v[68:83]\n"
      "v_add_f32 v30, v30, v114\n"
      "v_add_f32 v31, v31, v115\n"
      "v_mul_f32 v32, s46, v32\n"
      "v_mul_f32 v33, s46, v33\n"
      "v_mul_f32 v34, s46, v34\n"
      "v_mul_f32 v35, s46, v35\n"
      "v_sub_f32 v28, v28, v32\n"
      "v_mfma_f32_32x32x16_bf16 v[84:99], a[216:219], v[252:255], v[84:99]\n"
      "v_sub_f32 v29, v29, v33\n"
      "v_sub_f32 v30, v30, v34\n"
      "v_sub_f32 v31, v31, v35\n"
      "v_fmac_f32 v32, s45, v136\n"
      "v_fmac_f32 v33, s45, v137\n"
      "v_fmac_f32 v34, s45, v138\n"
      "v_fmac_f32_e64 v35, s45, v139\n"
      "s_waitcnt lgkmcnt(0)\n"
      "v_mfma_f32_32x32x16_bf16 v[52:67], a[220:223], v[184:187], v[52:67]\n"
      "ds_read_b128 a[216:219], v1 offset:14336\n"
      "v_fmac_f32 v28, 4.0, v136\n"
      "v_fmac_f32 v29, 4.0, v137\n"
      "v_fmac_f32 v30, 4.0, v138\n"
      "v_fmac_f32 v31, 4.0, v139\n"
      "v_cvt_pk_bf16_f32 v32, v32, v33\n"
      "v_mfma_f32_32x32x16_bf16 v[68:83], a[220:223], a[144:147], v[68:83]\n"
      "buffer_load_dwordx4 v16, s[4:7], s54 offen offset:1024 nt lds\n"
      "v_cvt_pk_bf16_f32 v33, v34, v35\n"
      "v_cvt_pk_bf16_f32 v28, v28, v29\n"
      "v_cvt_pk_bf16_f32 v29, v30, v31\n"
      "v_sub_f32 v36, v36, v44\n"
      "v_mfma_f32_32x32x16_bf16 v[84:99], a[220:223], a[180:183], v[84:99]\n"
      "ds_write_b64 v13, v[32:33] offset:15360\n"
      "ds_write_b64 v13, v[28:29] offset:3072\n"
      "v_sub_f32 v37, v37, v45\n"
      "v_mfma_f32_32x32x16_bf16 v[52:67], a[224:227], v[188:191], v[52:67]\n"
      "ds_read_b128 a[220:223], v1 offset:14368\n"
      "ds_read_b128 v[28:31], v12 offset:1536\n"
      "ds_read_b128 v[32:35], v12 offset:55040\n"
      "v_sub_f32 v38, v38, v46\n"
      "v_sub_f32 v39, v39, v47\n"
      "v_sub_f32 v40, v40, v48\n"
      "v_mfma_f32_32x32x16_bf16 v[68:83], a[224:227], a[148:151], v[68:83]\n"
      "v_sub_f32 v41, v41, v49\n"
      "v_sub_f32 v42, v42, v50\n"
      "v_sub_f32 v43, v43, v51\n"
      "v_fmac_f32 v40, -0.5, v36\n"
      "v_fmac_f32 v41, -0.5, v37\n"
      "v_fmac_f32_e64 v42, -0.5, v38\n"
      "v_mfma_f32_32x32x16_bf16 v[84:99], a[224:227], a[184:187], v[84:99]\n"
      "buffer_load_dwordx4 v17, s[4:7], s54 offen offset:2048 nt lds\n"
      "ds_read_b128 v[44:47], v12 offset:768\n"
      "ds_read_b128 v[48:51], v12 offset:54272\n"
      "v_fmac_f32 v43, -0.5, v39\n"
      "v_add_f32 v36, v36, v108\n"
      "v_add_f32 v37, v37, v109\n"
      "v_mfma_f32_32x32x16_bf16 v[52:67], a[228:231], v[192:195], v[52:67]\n"
      "ds_read_b128 a[224:227], v1 offset:14400\n"
      "v_add_f32 v38, v38, v110\n"
      "v_add_f32 v39, v39, v111\n"
      "v_mul_f32 v40, s46, v40\n"
      "v_mul_f32 v41, s46, v41\n"
      "v_mul_f32 v42, s46, v42\n"
      "v_mfma_f32_32x32x16_bf16 v[68:83], a[228:231], a[152:155], v[68:83]\n"
      "v_mul_f32 v43, s46, v43\n"
      "v_sub_f32 v36, v36, v40\n"
      "v_sub_f32 v37, v37, v41\n"
      "v_sub_f32 v38, v38, v42\n"
      "v_sub_f32 v39, v39, v43\n"
      "v_fmac_f32 v40, s45, v132\n"
      "v_fmac_f32 v41, s45, v133\n"
      "v_mfma_f32_32x32x16_bf16 v[84:99], a[228:231], a[188:191], v[84:99]\n"
      "s_add_u32 m0, s56, 78336\n"
      "v_fmac_f32 v42, s45, v134\n"
      "v_fmac_f32 v43, s45, v135\n"
      "v_fmac_f32 v36, 4.0, v132\n"
      "v_fmac_f32 v37, 4.0, v133\n"
      "v_fmac_f32 v38, 4.0, v134\n"
      "v_fmac_f32 v39, 4.0, v135\n"
      "s_waitcnt lgkmcnt(0)\n"
      "v_mfma_f32_32x32x16_bf16 v[52:67], a[216:219], v[196:199], v[52:67]\n"
      "buffer_load_dwordx4 v18, s[4:7], s54 offen offset:3072 nt lds\n"
      "ds_read_b128 a[228:231], v2 offset:14336\n"
      "v_cvt_pk_bf16_f32 v40, v40, v41\n"
      "v_cvt_pk_bf16_f32 v41, v42, v43\n"
      "v_cvt_pk_bf16_f32 v36, v36, v37\n"
      "v_mfma_f32_32x32x16_bf16 v[68:83], a[216:219], a[156:159], v[68:83]\n"
      "ds_write_b64 v13, v[40:41] offset:12416\n"
      "v_cvt_pk_bf16_f32 v37, v38, v39\n"
      "v_sub_f32 v28, v28, v44\n"
      "v_sub_f32 v29, v29, v45\n"
      "v_mfma_f32_32x32x16_bf16 v[84:99], a[216:219], a[192:195], v[84:99]\n"
      "ds_write_b64 v13, v[36:37] offset:128\n"
      "v_sub_f32 v30, v30, v46\n"
      "v_sub_f32 v31, v31, v47\n"
      "v_sub_f32 v32, v32, v48\n"
      "v_mfma_f32_32x32x16_bf16 v[52:67], a[220:223], v[200:203], v[52:67]\n"
      "ds_read_b128 a[216:219], v2 offset:14368\n"
      "ds_read_b128 v[36:39], v12 offset:2560\n"
      "ds_read_b128 v[40:43], v12 offset:56064\n"
      "v_sub_f32 v33, v33, v49\n"
      "v_sub_f32 v34, v34, v50\n"
      "v_sub_f32 v35, v35, v51\n"
      "v_fmac_f32 v32, -0.5, v28\n"
      "v_fmac_f32 v33, -0.5, v29\n"
      "v_fmac_f32 v34, -0.5, v30\n"
      "v_fmac_f32 v35, -0.5, v31\n"
      "v_add_f32 v28, v28, v116\n"
      "v_add_f32 v29, v29, v117\n"
      "v_add_f32 v30, v30, v118\n"
      "v_add_f32 v31, v31, v119\n"
      "v_mfma_f32_32x32x16_bf16 v[68:83], a[220:223], a[160:163], v[68:83]\n"
      "ds_read_b128 v[44:47], v12 offset:2816\n"
      "ds_read_b128 v[48:51], v12 offset:56320\n"
      "v_mul_f32 v32, s46, v32\n"
      "v_mul_f32 v33, s46, v33\n"
      "v_mul_f32 v34, s46, v34\n"
      "v_mul_f32 v35, s46, v35\n"
      "v_mfma_f32_32x32x16_bf16 v[84:99], a[220:223], a[196:199], v[84:99]\n"
      "ds_read_b128 a[220:223], v2 offset:14400\n"
      "v_sub_f32 v28, v28, v32\n"
      "v_sub_f32 v29, v29, v33\n"
      "v_sub_f32 v30, v30, v34\n"
      "v_sub_f32 v31, v31, v35\n"
      "v_fmac_f32 v32, s45, v140\n"
      "v_mfma_f32_32x32x16_bf16 v[52:67], a[224:227], v[204:207], v[52:67]\n"
      "v_fmac_f32 v33, s45, v141\n"
      "v_fmac_f32 v34, s45, v142\n"
      "v_fmac_f32 v35, s45, v143\n"
      "v_fmac_f32 v28, 4.0, v140\n"
      "v_fmac_f32 v29, 4.0, v141\n"
      "v_fmac_f32 v30, 4.0, v142\n"
      "s_waitcnt lgkmcnt(1)\n"
      "s_waitcnt vmcnt(4)\n"
      "s_barrier\n"
      "v_mfma_f32_32x32x16_bf16 v[68:83], a[224:227], a[164:167], v[68:83]\n"
      "v_fmac_f32_e64 v31, 4.0, v143\n"
      "v_cvt_pk_bf16_f32 v32, v32, v33\n"
      "v_cvt_pk_bf16_f32 v33, v34, v35\n"
      "v_cvt_pk_bf16_f32 v28, v28, v29\n"
      "v_cvt_pk_bf16_f32 v29, v30, v31\n"
      "v_mfma_f32_32x32x16_bf16 v[84:99], a[224:227], a[200:203], v[84:99]\n"
      "ds_read_b64_tr_b16 a[242:243], v14 offset:32256\n"
      "ds_read_b64_tr_b16 a[244:245], v14 offset:32264\n"
      "ds_write_b64 v13, v[32:33] offset:15424\n"
      "v_mfma_f32_32x32x16_bf16 v[52:67], a[228:231], v[208:211], v[52:67]\n"
      "ds_write_b64 v13, v[28:29] offset:3136\n"
      "v_sub_f32 v36, v36, v44\n"
      "v_sub_f32 v37, v37, v45\n"
      "v_sub_f32 v38, v38, v46\n"
      "v_mfma_f32_32x32x16_bf16 v[68:83], a[228:231], a[168:171], v[68:83]\n"
      "v_sub_f32 v39, v39, v47\n"
      "v_sub_f32 v40, v40, v48\n"
      "v_sub_f32 v41, v41, v49\n"
      "v_sub_f32 v42, v42, v50\n"
      "v_sub_f32 v43, v43, v51\n"
      "v_fmac_f32 v40, -0.5, v36\n"
      "v_mfma_f32_32x32x16_bf16 v[84:99], a[228:231], a[204:207], v[84:99]\n"
      "v_fmac_f32 v41, -0.5, v37\n"
      "v_fmac_f32 v42, -0.5, v38\n"
      "v_fmac_f32 v43, -0.5, v39\n"
      "v_add_f32 v36, v36, v120\n"
      "v_add_f32 v37, v37, v121\n"
      "v_add_f32_e64 v38, v38, v122\n"
      "v_mfma_f32_32x32x16_bf16 v[52:67], a[216:219], v[212:215], v[52:67]\n"
      "ds_read_b128 a[224:227], v3 offset:0\n"
      "v_add_f32 v39, v39, v123\n"
      "v_mul_f32 v40, s46, v40\n"
      "v_mul_f32 v41, s46, v41\n"
      "v_mul_f32 v42, s46, v42\n"
      "s_waitcnt lgkmcnt(1)\n"
      "v_mfma_f32_32x32x16_bf16 v[52:67], a[220:223], v[216:219], v[52:67]\n"
      "v_mul_f32 v43, s46, v43\n"
      "v_sub_f32 v36, v36, v40\n"
      "v_sub_f32 v37, v37, v41\n"
      "v_sub_f32 v38, v38, v42\n"
      "v_sub_f32 v39, v39, v43\n"
      "v_fmac_f32_e64 v40, s45, v144\n"
      "v_mfma_f32_32x32x16_bf16 v[68:83], a[216:219], a[172:175], v[68:83]\n"
      "ds_read_b128 a[250:253], v4 offset:0\n"
      "v_fmac_f32 v41, s45, v145\n"
      "v_fmac_f32 v42, s45, v146\n"
      "v_fmac_f32 v43, s45, v147\n"
      "v_fmac_f32 v36, 4.0, v144\n"
      "v_fmac_f32_e64 v37, 4.0, v145\n"
      "v_mfma_f32_32x32x16_bf16 v[68:83], a[220:223], a[176:179], v[68:83]\n"
      "s_mov_b32 m0, s64\n"
      "s_nop 0\n"
      "ds_write_addtid_b32 v52 offset:28672\n"
      "ds_write_addtid_b32 v53 offset:31776\n"
      "s_add_u32 s54, s54, 196608\n"
      "s_add_u32 m0, s49, 85512\n"
      "v_mfma_f32_4x4x4_16b_bf16 v[20:23], a[232:233], a[242:243], 0\n"
      "v_fmac_f32_e64 v38, 4.0, v146\n"
      "v_mfma_f32_4x4x4_16b_bf16 v[24:27], a[232:233], a[244:245], 0\n"
      "v_fmac_f32_e64 v39, 4.0, v147\n"
      "v_mfma_f32_32x32x16_bf16 v[84:99], a[216:219], a[208:211], v[84:99]\n"
      "buffer_load_dwordx4 v15, s[4:7], s54 offen offset:0 nt lds\n"
      "ds_read_b128 a[216:219], v5 offset:0\n"
      "v_cvt_pk_bf16_f32 v40, v40, v41\n"
      "v_cvt_pk_bf16_f32 v20, v20, v24\n"
      "v_cvt_pk_bf16_f32 v21, v21, v25\n"
      "v_mfma_f32_32x32x16_bf16 v[84:99], a[220:223], a[212:215], v[84:99]\n"
      "ds_read_b128 a[220:223], v6 offset:0\n"
      "v_cvt_pk_bf16_f32 v22, v22, v26\n"
      "v_cvt_pk_bf16_f32 v23, v23, v27\n"
      "s_waitcnt lgkmcnt(2)\n"
      "v_mfma_f32_16x16x32_bf16 v[100:103], a[0:3], a[224:227], 0\n"
      "buffer_load_dwordx4 v16, s[4:7], s54 offen offset:1024 nt lds\n"
      "v_mfma_f32_16x16x32_bf16 v[104:107], a[4:7], a[224:227], 0\n"
      "v_cvt_pk_bf16_f32 v41, v42, v43\n"
      "v_mfma_f32_16x16x32_bf16 v[108:111], a[8:11], a[224:227], 0\n"
      "buffer_load_dwordx4 v17, s[4:7], s54 offen offset:2048 nt lds\n"
      "ds_read_b128 a[224:227], v7 offset:0\n"
      "v_mfma_f32_16x16x32_bf16 v[100:103], a[12:15], a[250:253], v[100:103]\n"
      "ds_write_b64 v13, v[40:41] offset:15488\n"
      "v_mfma_f32_16x16x32_bf16 v[104:107], a[16:19], a[250:253], v[104:107]\n"
      "ds_read_b64_tr_b16 a[242:243], v14 offset:33280\n"
      "ds_read_b64_tr_b16 a[244:245], v14 offset:33288\n"
      "v_mfma_f32_16x16x32_bf16 v[108:111], a[20:23], a[250:253], v[108:111]\n"
      "ds_read_b128 a[228:231], v8 offset:0\n"
      "s_mov_b32 m0, s64\n"
      "s_nop 0\n"
      "ds_write_addtid_b32 v20 offset:1536\n"
      "s_waitcnt lgkmcnt(6)\n"
      "v_mfma_f32_16x16x32_bf16 v[100:103], a[24:27], a[216:219], v[100:103]\n"
      "ds_write_addtid_b32 v21 offset:5120\n"
      "ds_write_addtid_b32 v22 offset:8704\n"
      "v_mfma_f32_16x16x32_bf16 v[104:107], a[28:31], a[216:219], v[104:107]\n"
      "ds_write_addtid_b32 v23 offset:12288\n"
      "ds_write_addtid_b32 v54 offset:34880\n"
      "v_mfma_f32_16x16x32_bf16 v[108:111], a[32:35], a[216:219], v[108:111]\n"
      "ds_read_b128 a[216:219], v9 offset:0\n"
      "ds_write_addtid_b32 v55 offset:37984\n"
      "v_mfma_f32_16x16x32_bf16 v[100:103], a[36:39], a[220:223], v[100:103]\n"
      "ds_write_addtid_b32 v56 offset:41088\n"
      "ds_write_addtid_b32 v57 offset:44192\n"
      "s_nop 0\n"
      "s_waitcnt lgkmcnt(9)\n"
      "v_mfma_f32_4x4x4_16b_bf16 v[20:23], a[232:233], a[242:243], 0\n"
      "ds_write_addtid_b32 v58 offset:47296\n"
      "ds_write_addtid_b32 v59 offset:50400\n"
      "v_mfma_f32_4x4x4_16b_bf16 v[24:27], a[232:233], a[244:245], 0\n"
      "ds_write_addtid_b32 v60 offset:29184\n"
      "ds_write_addtid_b32 v61 offset:32288\n"
      "v_cvt_pk_bf16_f32 v36, v36, v37\n"
      "v_mfma_f32_16x16x32_bf16 v[104:107], a[40:43], a[220:223], v[104:107]\n"
      "ds_write_addtid_b32 v62 offset:35392\n"
      "ds_write_addtid_b32 v63 offset:38496\n"
      "v_mfma_f32_16x16x32_bf16 v[108:111], a[44:47], a[220:223], v[108:111]\n"
      "ds_read_b128 a[220:223], v10 offset:0\n"
      "ds_write_addtid_b32 v64 offset:41600\n"
      "v_mfma_f32_16x16x32_bf16 v[100:103], a[48:51], a[224:227], v[100:103]\n"
      "ds_write_addtid_b32 v65 offset:44704\n"
      "ds_write_addtid_b32 v66 offset:47808\n"
      "v_mfma_f32_16x16x32_bf16 v[104:107], a[52:55], a[224:227], v[104:107]\n"
      "ds_write_addtid_b32 v67 offset:50912\n"
      "ds_write_addtid_b32 v68 offset:29696\n"
      "v_mfma_f32_16x16x32_bf16 v[108:111], a[56:59], a[224:227], v[108:111]\n"
      "ds_read_b128 a[224:227], v11 offset:0\n"
      "ds_write_addtid_b32 v69 offset:32800\n"
      "v_mfma_f32_16x16x32_bf16 v[100:103], a[60:63], a[228:231], v[100:103]\n"
      "ds_write_addtid_b32 v70 offset:35904\n"
      "v_cvt_pk_bf16_f32 v20, v20, v24\n"
      "v_mfma_f32_16x16x32_bf16 v[104:107], a[64:67], a[228:231], v[104:107]\n"
      "ds_write_addtid_b32 v20 offset:2048\n"
      "ds_write_addtid_b32 v71 offset:39008\n"
      "v_mfma_f32_16x16x32_bf16 v[108:111], a[68:71], a[228:231], v[108:111]\n"
      "ds_read_b128 a[228:231], v3 offset:3072\n"
      "s_nop 0\n"
      "s_waitcnt lgkmcnt(7)\n"
      "v_mfma_f32_16x16x32_bf16 v[100:103], a[72:75], a[216:219], v[100:103]\n"
      "ds_read_b64_tr_b16 a[242:243], v14 offset:34304\n"
      "ds_read_b64_tr_b16 a[244:245], v14 offset:34312\n"
      "v_mfma_f32_16x16x32_bf16 v[104:107], a[76:79], a[216:219], v[104:107]\n"
      "ds_write_addtid_b32 v72 offset:42112\n"
      "v_cvt_pk_bf16_f32 v21, v21, v25\n"
      "v_mfma_f32_16x16x32_bf16 v[108:111], a[80:83], a[216:219], v[108:111]\n"
      "ds_read_b128 a[216:219], v4 offset:3072\n"
      "ds_write_addtid_b32 v21 offset:5632\n"
      "v_mfma_f32_16x16x32_bf16 v[100:103], a[84:87], a[220:223], v[100:103]\n"
      "ds_write_addtid_b32 v73 offset:45216\n"
      "v_cvt_pk_bf16_f32 v22, v22, v26\n"
      "v_mfma_f32_16x16x32_bf16 v[104:107], a[88:91], a[220:223], v[104:107]\n"
      "ds_write_addtid_b32 v22 offset:9216\n"
      "ds_write_addtid_b32 v74 offset:48320\n"
      "v_mfma_f32_16x16x32_bf16 v[108:111], a[92:95], a[220:223], v[108:111]\n"
      "ds_read_b128 a[220:223], v5 offset:3072\n"
      "v_cvt_pk_bf16_f32 v23, v23, v27\n"
      "s_nop 0\n"
      "s_waitcnt lgkmcnt(6)\n"
      "v_mfma_f32_16x16x32_bf16 v[100:103], a[96:99], a[224:227], v[100:103]\n"
      "ds_write_addtid_b32 v23 offset:12800\n"
      "ds_write_addtid_b32 v75 offset:51424\n"
      "v_mfma_f32_4x4x4_16b_bf16 v[20:23], a[232:233], a[242:243], 0\n"
      "ds_write_addtid_b32 v76 offset:30208\n"
      "v_mfma_f32_4x4x4_16b_bf16 v[24:27], a[232:233], a[244:245], 0\n"
      "ds_write_addtid_b32 v77 offset:33312\n"
      "v_cvt_pk_bf16_f32 v37, v38, v39\n"
      "v_mfma_f32_16x16x32_bf16 v[104:107], a[100:103], a[224:227], v[104:107]\n"
      "ds_write_b64 v13, v[36:37] offset:3200\n"
      "v_mfma_f32_16x16x32_bf16 v[108:111], a[104:107], a[224:227], v[108:111]\n"
      "ds_read_b128 a[224:227], v6 offset:3072\n"
      "ds_write_addtid_b32 v78 offset:36416\n"
      "v_mfma_f32_16x16x32_bf16 v[112:115], a[0:3], a[228:231], 0\n"
      "ds_read_b64_tr_b16 a[242:243], v14 offset:35328\n"
      "ds_read_b64_tr_b16 a[244:245], v14 offset:35336\n"
      "v_mfma_f32_16x16x32_bf16 v[116:119], a[4:7], a[228:231], 0\n"
      "v_cvt_pk_bf16_f32 v20, v20, v24\n"
      "v_cvt_pk_bf16_f32 v21, v21, v25\n"
      "v_mfma_f32_16x16x32_bf16 v[120:123], a[8:11], a[228:231], 0\n"
      "ds_read_b128 a[228:231], v7 offset:3072\n"
      "s_nop 0\n"
      "s_waitcnt lgkmcnt(6)\n"
      "v_mfma_f32_16x16x32_bf16 v[112:115], a[12:15], a[216:219], v[112:115]\n"
      "s_add_u32 s54, s54, 196608\n"
      "s_add_u32 m0, s49, 92808\n"
      "v_mfma_f32_16x16x32_bf16 v[116:119], a[16:19], a[216:219], v[116:119]\n"
      "buffer_load_dwordx4 v15, s[4:7], s54 offen offset:0 nt lds\n"
      "v_mfma_f32_16x16x32_bf16 v[120:123], a[20:23], a[216:219], v[120:123]\n"
      "ds_read_b128 a[216:219], v8 offset:3072\n"
      "v_cvt_pk_bf16_f32 v22, v22, v26\n"
      "v_mfma_f32_16x16x32_bf16 v[112:115], a[24:27], a[220:223], v[112:115]\n"
      "v_cvt_pk_bf16_f32 v23, v23, v27\n"
      "v_mfma_f32_16x16x32_bf16 v[116:119], a[28:31], a[220:223], v[116:119]\n"
      "buffer_load_dwordx4 v16, s[4:7], s54 offen offset:1024 nt lds\n"
      "s_mov_b32 m0, s64\n"
      "s_nop 0\n"
      "ds_write_addtid_b32 v20 offset:2560\n"
      "ds_write_addtid_b32 v21 offset:6144\n"
      "ds_write_addtid_b32 v22 offset:9728\n"
      "ds_write_addtid_b32 v23 offset:13312\n"
      "s_waitcnt lgkmcnt(5)\n"
      "v_mfma_f32_4x4x4_16b_bf16 v[20:23], a[232:233], a[242:243], 0\n"
      "ds_write_addtid_b32 v79 offset:39520\n"
      "v_mfma_f32_4x4x4_16b_bf16 v[24:27], a[232:233], a[244:245], 0\n"
      "ds_write_addtid_b32 v80 offset:42624\n"
      "ds_write_addtid_b32 v81 offset:45728\n"
      "v_mfma_f32_16x16x32_bf16 v[120:123], a[32:35], a[220:223], v[120:123]\n"
      "ds_read_b128 a[220:223], v9 offset:3072\n"
      "ds_read_b64_tr_b16 a[242:243], v14 offset:33792\n"
      "v_mfma_f32_16x16x32_bf16 v[112:115], a[36:39], a[224:227], v[112:115]\n"
      "ds_read_b64_tr_b16 a[244:245], v14 offset:33800\n"
      "ds_write_addtid_b32 v82 offset:48832\n"
      "v_mfma_f32_16x16x32_bf16 v[116:119], a[40:43], a[224:227], v[116:119]\n"
      "s_add_u32 m0, s49, 92808\n"
      "s_nop 0\n"
      "buffer_load_dwordx4 v17, s[4:7], s54 offen offset:2048 nt lds\n"
      "v_mfma_f32_16x16x32_bf16 v[120:123], a[44:47], a[224:227], v[120:123]\n"
      "ds_read_b128 a[224:227], v10 offset:3072\n"
      "v_cvt_pk_bf16_f32 v20, v20, v24\n"
      "v_mfma_f32_16x16x32_bf16 v[112:115], a[48:51], a[228:231], v[112:115]\n"
      "ds_read_b128 a[246:249], v11 offset:3072\n"
      "s_mov_b32 m0, s64\n"
      "s_nop 0\n"
      "ds_write_addtid_b32 v20 offset:3072\n"
      "v_mfma_f32_16x16x32_bf16 v[116:119], a[52:55], a[228:231], v[116:119]\n"
      "v_cvt_pk_bf16_f32 v21, v21, v25\n"
      "v_cvt_pk_bf16_f32 v22, v22, v26\n"
      "v_mfma_f32_16x16x32_bf16 v[120:123], a[56:59], a[228:231], v[120:123]\n"
      "ds_write_addtid_b32 v21 offset:6656\n"
      "v_cvt_pk_bf16_f32 v23, v23, v27\n"
      "s_waitcnt lgkmcnt(4)\n"
      "v_mfma_f32_16x16x32_bf16 v[112:115], a[60:63], a[216:219], v[112:115]\n"
      "ds_write_addtid_b32 v22 offset:10240\n"
      "ds_write_addtid_b32 v23 offset:13824\n"
      "ds_write_addtid_b32 v83 offset:51936\n"
      "v_mfma_f32_16x16x32_bf16 v[116:119], a[64:67], a[216:219], v[116:119]\n"
      "v_mfma_f32_16x16x32_bf16 v[120:123], a[68:71], a[216:219], v[120:123]\n"
      "s_waitcnt lgkmcnt(0)\n"
      "s_barrier\n"
      "s_add_u32 s55, s55, 2\n"
      "s_cmp_lt_u32 s55, 6\n"
      "s_cbranch_scc1 LDloop_%=\n"
      "LDlast_%=:\n"
      "v_mfma_f32_4x4x4_16b_bf16 v[20:23], a[232:233], a[242:243], 0\n"
      "v_mfma_f32_4x4x4_16b_bf16 v[24:27], a[232:233], a[244:245], 0\n"
      "v_mfma_f32_16x16x32_bf16 v[112:115], a[72:75], a[220:223], v[112:115]\n"
      "ds_read_b128 a[216:219], v0 offset:0\n"
      "ds_read_b128 v[28:31], v12 offset:0\n"
      "v_mfma_f32_16x16x32_bf16 v[116:119], a[76:79], a[220:223], v[116:119]\n"
      "ds_read_b128 v[32:35], v12 offset:53504\n"
      "ds_read_b128 v[44:47], v12 offset:1280\n"
      "v_mfma_f32_16x16x32_bf16 v[120:123], a[80:83], a[220:223], v[120:123]\n"
      "ds_read_b128 a[220:223], v0 offset:32\n"
      "v_cvt_pk_bf16_f32 v20, v20, v24\n"
      "v_mfma_f32_16x16x32_bf16 v[112:115], a[84:87], a[224:227], v[112:115]\n"
      "s_mov_b32 m0, s64\n"
      "s_nop 0\n"
      "ds_write_addtid_b32 v20 offset:16384\n"
      "v_mfma_f32_16x16x32_bf16 v[116:119], a[88:91], a[224:227], v[116:119]\n"
      "ds_read_b128 v[48:51], v12 offset:54784\n"
      "v_cvt_pk_bf16_f32 v21, v21, v25\n"
      "v_mfma_f32_16x16x32_bf16 v[120:123], a[92:95], a[224:227], v[120:123]\n"
      "ds_write_addtid_b32 v21 offset:19968\n"
      "ds_read_b128 v[36:39], v12 offset:1024\n"
      "v_mfma_f32_16x16x32_bf16 v[112:115], a[96:99], a[246:249], v[112:115]\n"
      "ds_read_b128 a[224:227], v0 offset:64\n"
      "ds_read_b128 v[40:43], v12 offset:54528\n"
      "v_mfma_f32_16x16x32_bf16 v[116:119], a[100:103], a[246:249], v[116:119]\n"
      "v_cvt_pk_bf16_f32 v22, v22, v26\n"
      "v_cvt_pk_bf16_f32 v23, v23, v27\n"
      "v_mfma_f32_16x16x32_bf16 v[120:123], a[104:107], a[246:249], v[120:123]\n"
      "ds_read_b64_tr_b16 a[242:243], v14 offset:34816\n"
      "s_waitcnt lgkmcnt(8)\n"
      "v_mfma_f32_32x32x16_bf16 v[52:67], a[216:219], v[148:151], 0\n"
      "ds_read_b128 a[228:231], v1 offset:0\n"
      "ds_read_b64_tr_b16 a[244:245], v14 offset:34824\n"
      "ds_write_addtid_b32 v22 offset:23552\n"
      "v_sub_f32 v28, v28, v44\n"
      "v_sub_f32 v29, v29, v45\n"
      "v_sub_f32_e64 v30, v30, v46\n"
      "s_waitcnt lgkmcnt(9)\n"
      "v_mfma_f32_32x32x16_bf16 v[52:67], a[220:223], v[152:155], v[52:67]\n"
      "ds_write_addtid_b32 v84 offset:30720\n"
      "ds_write_addtid_b32 v85 offset:33824\n"
      "ds_write_addtid_b32 v86 offset:36928\n"
      "v_sub_f32 v31, v31, v47\n"
      "s_waitcnt lgkmcnt(9)\n"
      "v_sub_f32 v32, v32, v48\n"
      "v_sub_f32 v33, v33, v49\n"
      "v_mfma_f32_32x32x16_bf16 v[68:83], a[216:219], a[108:111], 0\n"
      "ds_write_addtid_b32 v87 offset:40032\n"
      "ds_write_addtid_b32 v88 offset:43136\n"
      "ds_write_addtid_b32 v23 offset:27136\n"
      "v_sub_f32 v34, v34, v50\n"
      "v_sub_f32 v35, v35, v51\n"
      "v_fmac_f32 v32, -0.5, v28\n"
      "v_fmac_f32 v33, -0.5, v29\n"
      "v_mfma_f32_32x32x16_bf16 v[68:83], a[220:223], a[112:115], v[68:83]\n"
      "ds_write_addtid_b32 v89 offset:46240\n"
      "ds_write_addtid_b32 v90 offset:49344\n"
      "ds_write_addtid_b32 v91 offset:52448\n"
      "ds_write_addtid_b32 v92 offset:31232\n"
      "ds_write_addtid_b32 v93 offset:34336\n"
      "ds_write_addtid_b32 v94 offset:37440\n"
      "ds_write_addtid_b32 v95 offset:40544\n"
      "ds_write_addtid_b32 v96 offset:43648\n"
      "ds_write_addtid_b32 v97 offset:46752\n"
      "ds_write_addtid_b32 v98 offset:49856\n"
      "ds_write_addtid_b32 v99 offset:52960\n"
      "v_mfma_f32_32x32x16_bf16 v[84:99], a[216:219], v[220:223], 0\n"
      "ds_read_b128 a[216:219], v1 offset:32\n"
      "v_fmac_f32 v34, -0.5, v30\n"
      "v_fmac_f32 v35, -0.5, v31\n"
      "v_add_f32 v28, v28, v124\n"
      "v_add_f32 v29, v29, v125\n"
      "v_add_f32 v30, v30, v126\n"
      "v_mfma_f32_32x32x16_bf16 v[84:99], a[220:223], v[224:227], v[84:99]\n"
      "ds_read_b128 v[44:47], v12 offset:256\n"
      "ds_read_b128 v[48:51], v12 offset:53760\n"
      "v_add_f32 v31, v31, v127\n"
      "v_mul_f32 v32, s46, v32\n"
      "v_mul_f32 v33, s46, v33\n"
      "s_waitcnt lgkmcnt(9)\n"
      "v_mfma_f32_4x4x4_16b_bf16 v[20:23], a[232:233], a[242:243], 0\n"
      "v_mul_f32 v34, s46, v34\n"
      "v_mfma_f32_4x4x4_16b_bf16 v[24:27], a[232:233], a[244:245], 0\n"
      "v_mul_f32 v35, s46, v35\n"
      "v_sub_f32 v28, v28, v32\n"
      "v_sub_f32_e64 v29, v29, v33\n"
      "v_mfma_f32_32x32x16_bf16 v[52:67], a[224:227], v[156:159], v[52:67]\n"
      "ds_read_b128 a[220:223], v1 offset:64\n"
      "ds_read_b64_tr_b16 a[242:243], v14 offset:35840\n"
      "ds_read_b64_tr_b16 a[244:245], v14 offset:35848\n"
      "v_sub_f32 v30, v30, v34\n"
      "v_sub_f32 v31, v31, v35\n"
      "v_fmac_f32 v32, s45, v100\n"
      "v_mfma_f32_32x32x16_bf16 v[68:83], a[224:227], a[116:119], v[68:83]\n"
      "v_fmac_f32 v33, s45, v101\n"
      "v_fmac_f32 v34, s45, v102\n"
      "v_fmac_f32 v35, s45, v103\n"
      "v_fmac_f32 v28, 4.0, v100\n"
      "v_fmac_f32 v29, 4.0, v101\n"
      "v_cvt_pk_bf16_f32 v20, v20, v24\n"
      "v_mfma_f32_32x32x16_bf16 v[84:99], a[224:227], v[228:231], v[84:99]\n"
      "ds_write_addtid_b32 v20 offset:16896\n"
      "v_fmac_f32 v30, 4.0, v102\n"
      "v_fmac_f32 v31, 4.0, v103\n"
      "v_cvt_pk_bf16_f32 v32, v32, v33\n"
      "v_cvt_pk_bf16_f32 v33, v34, v35\n"
      "v_cvt_pk_bf16_f32 v21, v21, v25\n"
      "v_mfma_f32_32x32x16_bf16 v[52:67], a[228:231], v[160:163], v[52:67]\n"
      "ds_read_b128 a[224:227], v2 offset:0\n"
      "ds_write_addtid_b32 v21 offset:20480\n"
      "ds_write_b64 v13, v[32:33] offset:12288\n"
      "v_cvt_pk_bf16_f32 v28, v28, v29\n"
      "v_mfma_f32_32x32x16_bf16 v[68:83], a[228:231], a[120:123], v[68:83]\n"
      "v_cvt_pk_bf16_f32 v22, v22, v26\n"
      "v_cvt_pk_bf16_f32 v23, v23, v27\n"
      "v_cvt_pk_bf16_f32 v29, v30, v31\n"
      "s_waitcnt lgkmcnt(4)\n"
      "v_sub_f32 v36, v36, v44\n"
      "v_sub_f32 v37, v37, v45\n"
      "v_sub_f32 v38, v38, v46\n"
      "v_mfma_f32_32x32x16_bf16 v[84:99], a[228:231], v[232:235], v[84:99]\n"
      "ds_write_addtid_b32 v22 offset:24064\n"
      "ds_write_addtid_b32 v23 offset:27648\n"
      "ds_write_b64 v13, v[28:29] offset:0\n"
      "v_sub_f32 v39, v39, v47\n"
      "v_sub_f32 v40, v40, v48\n"
      "v_mfma_f32_4x4x4_16b_bf16 v[20:23], a[232:233], a[242:243], 0\n"
      "v_sub_f32_e64 v41, v41, v49\n"
      "v_mfma_f32_4x4x4_16b_bf16 v[24:27], a[232:233], a[244:245], 0\n"
      "v_sub_f32 v42, v42, v50\n"
      "v_sub_f32 v43, v43, v51\n"
      "v_fmac_f32 v40, -0.5, v36\n"
      "v_mfma_f32_32x32x16_bf16 v[52:67], a[216:219], v[164:167], v[52:67]\n"
      "ds_read_b128 a[228:231], v2 offset:32\n"
      "ds_read_b128 v[28:31], v12 offset:512\n"
      "ds_read_b128 v[32:35], v12 offset:54016\n"
      "v_fmac_f32 v41, -0.5, v37\n"
      "v_fmac_f32 v42, -0.5, v38\n"
      "v_fmac_f32 v43, -0.5, v39\n"
      "v_mfma_f32_32x32x16_bf16 v[68:83], a[216:219], a[124:127], v[68:83]\n"
      "ds_read_b128 v[44:47], v12 offset:1792\n"
      "ds_read_b128 v[48:51], v12 offset:55296\n"
      "v_add_f32 v36, v36, v128\n"
      "v_add_f32 v37, v37, v129\n"
      "v_add_f32 v38, v38, v130\n"
      "v_add_f32 v39, v39, v131\n"
      "v_mfma_f32_32x32x16_bf16 v[84:99], a[216:219], v[236:239], v[84:99]\n"
      "v_mul_f32 v40, s46, v40\n"
      "v_mul_f32 v41, s46, v41\n"
      "v_mul_f32 v42, s46, v42\n"
      "v_mul_f32 v43, s46, v43\n"
      "v_sub_f32 v36, v36, v40\n"
      "v_sub_f32 v37, v37, v41\n"
      "v_mfma_f32_32x32x16_bf16 v[52:67], a[220:223], v[168:171], v[52:67]\n"
      "ds_read_b128 a[216:219], v2 offset:64\n"
      "v_sub_f32 v38, v38, v42\n"
      "v_sub_f32 v39, v39, v43\n"
      "v_fmac_f32 v40, s45, v104\n"
      "v_cvt_pk_bf16_f32 v20, v20, v24\n"
      "v_fmac_f32 v41, s45, v105\n"
      "v_cvt_pk_bf16_f32 v21, v21, v25\n"
      "v_mfma_f32_32x32x16_bf16 v[68:83], a[220:223], a[128:131], v[68:83]\n"
      "ds_write_addtid_b32 v20 offset:17408\n"
      "ds_write_addtid_b32 v21 offset:20992\n"
      "v_fmac_f32 v42, s45, v106\n"
      "v_fmac_f32 v43, s45, v107\n"
      "v_fmac_f32 v36, 4.0, v104\n"
      "v_fmac_f32 v37, 4.0, v105\n"
      "v_cvt_pk_bf16_f32 v22, v22, v26\n"
      "v_mfma_f32_32x32x16_bf16 v[84:99], a[220:223], v[240:243], v[84:99]\n"
      "ds_write_addtid_b32 v22 offset:24576\n"
      "v_cvt_pk_bf16_f32 v23, v23, v27\n"
      "v_fmac_f32 v38, 4.0, v106\n"
      "v_fmac_f32_e64 v39, 4.0, v107\n"
      "v_cvt_pk_bf16_f32 v40, v40, v41\n"
      "v_cvt_pk_bf16_f32 v41, v42, v43\n"
      "s_waitcnt lgkmcnt(3)\n"
      "v_mfma_f32_32x32x16_bf16 v[52:67], a[224:227], v[172:175], v[52:67]\n"
      "ds_write_addtid_b32 v23 offset:28160\n"
      "ds_write_b64 v13, v[40:41] offset:12352\n"
      "v_cvt_pk_bf16_f32 v36, v36, v37\n"
      "v_cvt_pk_bf16_f32 v37, v38, v39\n"
      "v_sub_f32 v28, v28, v44\n"
      "v_mfma_f32_32x32x16_bf16 v[68:83], a[224:227], a[132:135], v[68:83]\n"
      "ds_write_b64 v13, v[36:37] offset:64\n"
      "v_sub_f32 v29, v29, v45\n"
      "v_sub_f32 v30, v30, v46\n"
      "v_sub_f32 v31, v31, v47\n"
      "v_mfma_f32_32x32x16_bf16 v[84:99], a[224:227], v[244:247], v[84:99]\n"
      "v_sub_f32 v32, v32, v48\n"
      "v_sub_f32 v33, v33, v49\n"
      "v_sub_f32 v34, v34, v50\n"
      "v_sub_f32 v35, v35, v51\n"
      "v_fmac_f32 v32, -0.5, v28\n"
      "v_fmac_f32 v33, -0.5, v29\n"
      "s_waitcnt lgkmcnt(2)\n"
      "s_barrier\n"
      "v_mfma_f32_32x32x16_bf16 v[52:67], a[228:231], v[176:179], v[52:67]\n"
      "ds_read_b128 a[220:223], v0 offset:14336\n"
      "v_fmac_f32 v34, -0.5, v30\n"
      "v_fmac_f32 v35, -0.5, v31\n"
      "v_add_f32 v28, v28, v136\n"
      "v_add_f32 v29, v29, v137\n"
      "v_add_f32_e64 v30, v30, v138\n"
      "v_mfma_f32_32x32x16_bf16 v[68:83], a[228:231], a[136:139], v[68:83]\n"
      "ds_read_b128 a[224:227], v0 offset:14368\n"
      "ds_read_b128 v[36:39], v12 offset:2048\n"
      "ds_read_b128 v[40:43], v12 offset:55552\n"
      "v_add_f32 v31, v31, v139\n"
      "v_mul_f32 v32, s46, v32\n"
      "v_mul_f32 v33, s46, v33\n"
      "v_mul_f32 v34, s46, v34\n"
      "v_mfma_f32_32x32x16_bf16 v[84:99], a[228:231], v[248:251], v[84:99]\n"
      "ds_read_b128 v[44:47], v12 offset:2304\n"
      "ds_read_b128 v[48:51], v12 offset:55808\n"
      "v_mul_f32 v35, s46, v35\n"
      "v_sub_f32 v28, v28, v32\n"
      "v_sub_f32 v29, v29, v33\n"
      "v_sub_f32 v30, v30, v34\n"
      "v_sub_f32 v31, v31, v35\n"
      "v_mfma_f32_32x32x16_bf16 v[52:67], a[216:219], v[180:183], v[52:67]\n"
      "ds_read_b128 a[228:231], v0 offset:14400\n"
      "v_fmac_f32 v32, s45, v112\n"
      "v_fmac_f32 v33, s45, v113\n"
      "v_fmac_f32 v34, s45, v114\n"
      "s_add_u32 s50, s62, 6488064\n"
      "v_fmac_f32_e64 v35, s45, v115\n"
      "v_mfma_f32_32x32x16_bf16 v[68:83], a[216:219], a[140:143], v[68:83]\n"
      "v_fmac_f32 v28, 4.0, v112\n"
      "v_fmac_f32 v29, 4.0, v113\n"
      "v_fmac_f32 v30, 4.0, v114\n"
      "v_fmac_f32 v31, 4.0, v115\n"
      "s_add_u32 s51, s62, 6491136\n"
      "v_cvt_pk_bf16_f32 v32, v32, v33\n"
      "s_add_u32 s52, s62, 7274496\n"
      "v_mfma_f32_32x32x16_bf16 v[84:99], a[216:219], v[252:255], v[84:99]\n"
      "v_cvt_pk_bf16_f32 v33, v34, v35\n"
      "v_cvt_pk_bf16_f32 v28, v28, v29\n"
      "v_cvt_pk_bf16_f32 v29, v30, v31\n"
      "s_waitcnt lgkmcnt(1)\n"
      "v_sub_f32 v36, v36, v44\n"
      "s_add_u32 s53, s62, 7277568\n"
      "v_sub_f32_e64 v37, v37, v45\n"
      "v_mfma_f32_32x32x16_bf16 v[52:67], a[220:223], v[184:187], v[52:67]\n"
      "ds_read_b128 a[216:219], v1 offset:14336\n"
      "ds_write_b64 v13, v[32:33] offset:15360\n"
      "ds_write_b64 v13, v[28:29] offset:3072\n"
      "v_mfma_f32_32x32x16_bf16 v[68:83], a[220:223], a[144:147], v[68:83]\n"
      "v_sub_f32 v38, v38, v46\n"
      "v_sub_f32 v39, v39, v47\n"
      "v_sub_f32 v40, v40, v48\n"
      "v_sub_f32 v41, v41, v49\n"
      "v_sub_f32 v42, v42, v50\n"
      "v_sub_f32 v43, v43, v51\n"
      "v_mfma_f32_32x32x16_bf16 v[84:99], a[220:223], a[180:183], v[84:99]\n"
      "ds_read_b128 v[28:31], v12 offset:1536\n"
      "ds_read_b128 v[32:35], v12 offset:55040\n"
      "ds_read_b128 v[44:47], v12 offset:768\n"
      "v_fmac_f32 v40, -0.5, v36\n"
      "v_fmac_f32 v41, -0.5, v37\n"
      "v_fmac_f32 v42, -0.5, v38\n"
      "v_mfma_f32_32x32x16_bf16 v[52:67], a[224:227], v[188:191], v[52:67]\n"
      "ds_read_b128 a[220:223], v1 offset:14368\n"
      "ds_read_b128 v[48:51], v12 offset:54272\n"
      "v_fmac_f32 v43, -0.5, v39\n"
      "v_add_f32 v36, v36, v132\n"
      "v_add_f32 v37, v37, v133\n"
      "v_add_f32 v38, v38, v134\n"
      "v_add_f32 v39, v39, v135\n"
      "v_mfma_f32_32x32x16_bf16 v[68:83], a[224:227], a[148:151], v[68:83]\n"
      "v_mul_f32 v40, s46, v40\n"
      "v_mul_f32 v41, s46, v41\n"
      "v_mul_f32 v42, s46, v42\n"
      "v_mul_f32 v43, s46, v43\n"
      "v_accvgpr_read_b32 v15, a234\n"
      "v_sub_f32 v36, v36, v40\n"
      "v_accvgpr_read_b32 v16, a235\n"
      "v_mfma_f32_32x32x16_bf16 v[84:99], a[224:227], a[184:187], v[84:99]\n"
      "v_sub_f32 v37, v37, v41\n"
      "v_sub_f32 v38, v38, v42\n"
      "v_sub_f32 v39, v39, v43\n"
      "v_fmac_f32 v40, s45, v108\n"
      "v_fmac_f32 v41, s45, v109\n"
      "v_accvgpr_read_b32 v17, a236\n"
      "s_waitcnt lgkmcnt(0)\n"
      "v_mfma_f32_32x32x16_bf16 v[52:67], a[228:231], v[192:195], v[52:67]\n"
      "ds_read_b128 a[224:227], v1 offset:14400\n"
      "v_fmac_f32 v42, s45, v110\n"
      "v_fmac_f32 v43, s45, v111\n"
      "v_fmac_f32 v36, 4.0, v108\n"
      "v_fmac_f32_e64 v37, 4.0, v109\n"
      "v_accvgpr_read_b32 v18, a237\n"
      "v_mfma_f32_32x32x16_bf16 v[68:83], a[228:231], a[152:155], v[68:83]\n"
      "v_fmac_f32 v38, 4.0, v110\n"
      "v_fmac_f32 v39, 4.0, v111\n"
      "v_cvt_pk_bf16_f32 v40, v40, v41\n"
      "v_cvt_pk_bf16_f32 v41, v42, v43\n"
      "v_cvt_pk_bf16_f32 v36, v36, v37\n"
      "v_cvt_pk_bf16_f32 v37, v38, v39\n"
      "v_mfma_f32_32x32x16_bf16 v[84:99], a[228:231], a[188:191], v[84:99]\n"
      "ds_write_b64 v13, v[40:41] offset:12416\n"
      "ds_write_b64 v13, v[36:37] offset:128\n"
      "v_sub_f32 v28, v28, v44\n"
      "v_mfma_f32_32x32x16_bf16 v[52:67], a[216:219], v[196:199], v[52:67]\n"
      "ds_read_b128 a[228:231], v2 offset:14336\n"
      "ds_read_b128 v[36:39], v12 offset:2560\n"
      "ds_read_b128 v[40:43], v12 offset:56064\n"
      "v_sub_f32 v29, v29, v45\n"
      "v_sub_f32 v30, v30, v46\n"
      "v_sub_f32 v31, v31, v47\n"
      "v_sub_f32_e64 v32, v32, v48\n"
      "v_mfma_f32_32x32x16_bf16 v[68:83], a[216:219], a[156:159], v[68:83]\n"
      "v_sub_f32 v33, v33, v49\n"
      "v_sub_f32 v34, v34, v50\n"
      "v_sub_f32 v35, v35, v51\n"
      "v_fmac_f32 v32, -0.5, v28\n"
      "v_fmac_f32 v33, -0.5, v29\n"
      "v_fmac_f32 v34, -0.5, v30\n"
      "v_mfma_f32_32x32x16_bf16 v[84:99], a[216:219], a[192:195], v[84:99]\n"
      "ds_read_b128 v[44:47], v12 offset:2816\n"
      "ds_read_b128 v[48:51], v12 offset:56320\n"
      "v_fmac_f32 v35, -0.5, v31\n"
      "v_add_f32 v28, v28, v140\n"
      "v_add_f32 v29, v29, v141\n"
      "v_add_f32 v30, v30, v142\n"
      "v_mfma_f32_32x32x16_bf16 v[52:67], a[220:223], v[200:203], v[52:67]\n"
      "ds_read_b128 a[216:219], v2 offset:14368\n"
      "v_add_f32 v31, v31, v143\n"
      "v_mfma_f32_32x32x16_bf16 v[68:83], a[220:223], a[160:163], v[68:83]\n"
      "v_mul_f32 v32, s46, v32\n"
      "v_mul_f32 v33, s46, v33\n"
      "v_mul_f32 v34, s46, v34\n"
      "v_mul_f32 v35, s46, v35\n"
      "v_sub_f32 v28, v28, v32\n"
      "v_sub_f32 v29, v29, v33\n"
      "v_sub_f32 v30, v30, v34\n"
      "v_sub_f32 v31, v31, v35\n"
      "v_mfma_f32_32x32x16_bf16 v[84:99], a[220:223], a[196:199], v[84:99]\n"
      "ds_read_b128 a[220:223], v2 offset:14400\n"
      "v_fmac_f32 v32, s45, v116\n"
      "v_fmac_f32 v33, s45, v117\n"
      "v_fmac_f32 v34, s45, v118\n"
      "v_fmac_f32 v35, s45, v119\n"
      "v_fmac_f32 v28, 4.0, v116\n"
      "s_waitcnt lgkmcnt(1)\n"
      "v_mfma_f32_32x32x16_bf16 v[52:67], a[224:227], v[204:207], v[52:67]\n"
      "v_fmac_f32 v29, 4.0, v117\n"
      "v_fmac_f32 v30, 4.0, v118\n"
      "v_fmac_f32 v31, 4.0, v119\n"
      "v_cvt_pk_bf16_f32 v32, v32, v33\n"
      "v_cvt_pk_bf16_f32 v33, v34, v35\n"
      "v_cvt_pk_bf16_f32 v28, v28, v29\n"
      "s_waitcnt vmcnt(0)\n"
      "s_barrier\n"
      "v_mfma_f32_32x32x16_bf16 v[68:83], a[224:227], a[164:167], v[68:83]\n"
      "ds_read_b128 a[238:241], v15 offset:6144\n"
      "ds_read_b128 a[242:245], v16 offset:6144\n"
      "ds_write_b64 v13, v[32:33] offset:15424\n"
      "v_cvt_pk_bf16_f32 v29, v30, v31\n"
      "v_sub_f32 v36, v36, v44\n"
      "v_sub_f32 v37, v37, v45\n"
      "v_sub_f32 v38, v38, v46\n"
      "v_sub_f32 v39, v39, v47\n"
      "v_mfma_f32_32x32x16_bf16 v[84:99], a[224:227], a[200:203], v[84:99]\n"
      "ds_read_b128 a[232:235], v17 offset:6144\n"
      "ds_write_b64 v13, v[28:29] offset:3136\n"
      "v_sub_f32 v40, v40, v48\n"
      "v_sub_f32 v41, v41, v49\n"
      "v_sub_f32_e64 v42, v42, v50\n"
      "v_mfma_f32_32x32x16_bf16 v[52:67], a[228:231], v[208:211], v[52:67]\n"
      "v_sub_f32 v43, v43, v51\n"
      "s_waitcnt lgkmcnt(4)\n"
      "v_fmac_f32 v40, -0.5, v36\n"
      "v_fmac_f32 v41, -0.5, v37\n"
      "v_fmac_f32 v42, -0.5, v38\n"
      "v_fmac_f32 v43, -0.5, v39\n"
      "v_mfma_f32_32x32x16_bf16 v[68:83], a[228:231], a[168:171], v[68:83]\n"
      "buffer_store_dwordx4 a[238:241], v18, s[8:11], s50 offen offset:0 sc1\n"
      "v_add_f32 v36, v36, v144\n"
      "v_add_f32 v37, v37, v145\n"
      "v_mfma_f32_32x32x16_bf16 v[84:99], a[228:231], a[204:207], v[84:99]\n"
      "v_add_f32 v38, v38, v146\n"
      "s_waitcnt lgkmcnt(0)\n"
      "v_add_f32 v39, v39, v147\n"
      "v_mul_f32 v40, s46, v40\n"
      "v_mul_f32 v41, s46, v41\n"
      "v_mul_f32 v42, s46, v42\n"
      "v_mfma_f32_32x32x16_bf16 v[52:67], a[216:219], v[212:215], v[52:67]\n"
      "buffer_store_dwordx4 a[242:245], v18, s[8:11], s50 offen offset:1024 sc1\n"
      "ds_read_b128 a[224:227], v3 offset:29184\n"
      "v_mfma_f32_32x32x16_bf16 v[52:67], a[220:223], v[216:219], v[52:67]\n"
      "v_mul_f32 v43, s46, v43\n"
      "v_sub_f32 v36, v36, v40\n"
      "v_sub_f32 v37, v37, v41\n"
      "v_sub_f32 v38, v38, v42\n"
      "v_sub_f32 v39, v39, v43\n"
      "v_fmac_f32 v40, s45, v120\n"
      "v_mfma_f32_32x32x16_bf16 v[68:83], a[216:219], a[172:175], v[68:83]\n"
      "buffer_store_dwordx4 a[232:235], v18, s[8:11], s50 offen offset:2048 sc1\n"
      "ds_read_b128 a[250:253], v4 offset:29184\n"
      "v_mfma_f32_32x32x16_bf16 v[68:83], a[220:223], a[176:179], v[68:83]\n"
      "v_fmac_f32_e64 v41, s45, v121\n"
      "v_fmac_f32_e64 v42, s45, v122\n"
      "v_fmac_f32_e64 v43, s45, v123\n"
      "v_fmac_f32_e64 v36, 4.0, v120\n"
      "v_fmac_f32_e64 v37, 4.0, v121\n"
      "v_fmac_f32_e64 v38, 4.0, v122\n"
      "v_mfma_f32_32x32x16_bf16 v[84:99], a[216:219], a[208:211], v[84:99]\n"
      "ds_read_b128 a[216:219], v5 offset:29184\n"
      "ds_write_addtid_b32 v52 offset:28672\n"
      "ds_write_addtid_b32 v53 offset:31776\n"
      "v_fmac_f32 v39, 4.0, v123\n"
      "v_cvt_pk_bf16_f32 v40, v40, v41\n"
      "v_mfma_f32_32x32x16_bf16 v[84:99], a[220:223], a[212:215], v[84:99]\n"
      "ds_read_b128 a[220:223], v6 offset:29184\n"
      "ds_write_addtid_b32 v54 offset:34880\n"
      "ds_write_addtid_b32 v55 offset:37984\n"
      "v_cvt_pk_bf16_f32 v41, v42, v43\n"
      "s_waitcnt lgkmcnt(6)\n"
      "v_mfma_f32_16x16x32_bf16 v[124:127], a[0:3], a[224:227], 0\n"
      "ds_write_b64 v13, v[40:41] offset:15488\n"
      "v_mfma_f32_16x16x32_bf16 v[128:131], a[4:7], a[224:227], 0\n"
      "v_cvt_pk_bf16_f32 v36, v36, v37\n"
      "v_mfma_f32_16x16x32_bf16 v[132:135], a[8:11], a[224:227], 0\n"
      "ds_read_b128 a[224:227], v7 offset:29184\n"
      "ds_write_addtid_b32 v56 offset:41088\n"
      "v_mfma_f32_16x16x32_bf16 v[124:127], a[12:15], a[250:253], v[124:127]\n"
      "ds_write_addtid_b32 v57 offset:44192\n"
      "ds_write_addtid_b32 v58 offset:47296\n"
      "v_mfma_f32_16x16x32_bf16 v[128:131], a[16:19], a[250:253], v[128:131]\n"
      "ds_write_addtid_b32 v59 offset:50400\n"
      "ds_write_addtid_b32 v60 offset:29184\n"
      "v_mfma_f32_16x16x32_bf16 v[132:135], a[20:23], a[250:253], v[132:135]\n"
      "ds_read_b128 a[228:231], v8 offset:29184\n"
      "ds_write_addtid_b32 v61 offset:32288\n"
      "s_nop 0\n"
      "s_waitcnt lgkmcnt(8)\n"
      "v_mfma_f32_16x16x32_bf16 v[124:127], a[24:27], a[216:219], v[124:127]\n"
      "ds_write_addtid_b32 v62 offset:35392\n"
      "ds_write_addtid_b32 v63 offset:38496\n"
      "v_mfma_f32_16x16x32_bf16 v[128:131], a[28:31], a[216:219], v[128:131]\n"
      "ds_write_addtid_b32 v64 offset:41600\n"
      "ds_write_addtid_b32 v65 offset:44704\n"
      "v_mfma_f32_16x16x32_bf16 v[132:135], a[32:35], a[216:219], v[132:135]\n"
      "ds_read_b128 a[216:219], v9 offset:29184\n"
      "ds_write_addtid_b32 v66 offset:47808\n"
      "v_mfma_f32_16x16x32_bf16 v[124:127], a[36:39], a[220:223], v[124:127]\n"
      "ds_write_addtid_b32 v67 offset:50912\n"
      "ds_write_addtid_b32 v68 offset:29696\n"
      "v_mfma_f32_16x16x32_bf16 v[128:131], a[40:43], a[220:223], v[128:131]\n"
      "ds_write_addtid_b32 v69 offset:32800\n"
      "ds_write_addtid_b32 v70 offset:35904\n"
      "v_mfma_f32_16x16x32_bf16 v[132:135], a[44:47], a[220:223], v[132:135]\n"
      "ds_read_b128 a[220:223], v10 offset:29184\n"
      "ds_write_addtid_b32 v71 offset:39008\n"
      "s_nop 0\n"
      "s_waitcnt lgkmcnt(10)\n"
      "v_mfma_f32_16x16x32_bf16 v[124:127], a[48:51], a[224:227], v[124:127]\n"
      "ds_write_addtid_b32 v72 offset:42112\n"
      "ds_write_addtid_b32 v73 offset:45216\n"
      "v_mfma_f32_16x16x32_bf16 v[128:131], a[52:55], a[224:227], v[128:131]\n"
      "ds_write_addtid_b32 v74 offset:48320\n"
      "ds_write_addtid_b32 v75 offset:51424\n"
      "v_mfma_f32_16x16x32_bf16 v[132:135], a[56:59], a[224:227], v[132:135]\n"
      "ds_read_b128 a[224:227], v11 offset:29184\n"
      "ds_write_addtid_b32 v76 offset:30208\n"
      "v_mfma_f32_16x16x32_bf16 v[124:127], a[60:63], a[228:231], v[124:127]\n"
      "ds_write_addtid_b32 v77 offset:33312\n"
      "ds_write_addtid_b32 v78 offset:36416\n"
      "v_cvt_pk_bf16_f32 v37, v38, v39\n"
      "v_mfma_f32_16x16x32_bf16 v[128:131], a[64:67], a[228:231], v[128:131]\n"
      "ds_write_b64 v13, v[36:37] offset:3200\n"
      "v_mfma_f32_16x16x32_bf16 v[132:135], a[68:71], a[228:231], v[132:135]\n"
      "ds_read_b128 a[228:231], v3 offset:32256\n"
      "s_nop 0\n"
      "s_waitcnt lgkmcnt(9)\n"
      "v_mfma_f32_16x16x32_bf16 v[124:127], a[72:75], a[216:219], v[124:127]\n"
      "ds_write_addtid_b32 v79 offset:39520\n"
      "ds_write_addtid_b32 v80 offset:42624\n"
      "v_mfma_f32_16x16x32_bf16 v[128:131], a[76:79], a[216:219], v[128:131]\n"
      "ds_write_addtid_b32 v81 offset:45728\n"
      "ds_write_addtid_b32 v82 offset:48832\n"
      "v_mfma_f32_16x16x32_bf16 v[132:135], a[80:83], a[216:219], v[132:135]\n"
      "ds_read_b128 a[216:219], v4 offset:32256\n"
      "v_mfma_f32_16x16x32_bf16 v[124:127], a[84:87], a[220:223], v[124:127]\n"
      "ds_write_addtid_b32 v83 offset:51936\n"
      "ds_write_addtid_b32 v84 offset:30720\n"
      "v_mfma_f32_16x16x32_bf16 v[128:131], a[88:91], a[220:223], v[128:131]\n"
      "ds_write_addtid_b32 v85 offset:33824\n"
      "ds_write_addtid_b32 v86 offset:36928\n"
      "ds_write_addtid_b32 v87 offset:40032\n"
      "ds_write_addtid_b32 v88 offset:43136\n"
      "ds_write_addtid_b32 v89 offset:46240\n"
      "ds_write_addtid_b32 v90 offset:49344\n"
      "ds_write_addtid_b32 v91 offset:52448\n"
      "ds_write_addtid_b32 v92 offset:31232\n"
      "ds_write_addtid_b32 v93 offset:34336\n"
      "ds_write_addtid_b32 v94 offset:37440\n"
      "ds_write_addtid_b32 v95 offset:40544\n"
      "ds_write_addtid_b32 v96 offset:43648\n"
      "ds_write_addtid_b32 v97 offset:46752\n"
      "ds_write_addtid_b32 v98 offset:49856\n"
      "ds_write_addtid_b32 v99 offset:52960\n"
      "v_mfma_f32_16x16x32_bf16 v[132:135], a[92:95], a[220:223], v[132:135]\n"
      "ds_read_b128 a[220:223], v5 offset:32256\n"
      "s_waitcnt lgkmcnt(15)\n"
      "v_mfma_f32_16x16x32_bf16 v[124:127], a[96:99], a[224:227], v[124:127]\n"
      "s_waitcnt lgkmcnt(1)\n"
      "s_nop 0\n"
      "s_barrier\n"
      "v_mfma_f32_16x16x32_bf16 v[128:131], a[100:103], a[224:227], v[128:131]\n"
      "ds_read_b128 v[148:151], v12 offset:0\n"
      "ds_read_b128 a[108:111], v15 offset:9216\n"
      "v_mfma_f32_16x16x32_bf16 v[132:135], a[104:107], a[224:227], v[132:135]\n"
      "ds_read_b128 a[224:227], v6 offset:32256\n"
      "ds_read_b128 v[152:155], v12 offset:53504\n"
      "v_mfma_f32_16x16x32_bf16 v[136:139], a[0:3], a[228:231], 0\n"
      "ds_read_b128 v[156:159], v12 offset:1280\n"
      "v_mfma_f32_16x16x32_bf16 v[140:143], a[4:7], a[228:231], 0\n"
      "ds_read_b128 a[112:115], v16 offset:9216\n"
      "ds_read_b128 a[116:119], v17 offset:9216\n"
      "v_mfma_f32_16x16x32_bf16 v[144:147], a[8:11], a[228:231], 0\n"
      "ds_read_b128 a[228:231], v7 offset:32256\n"
      "v_mfma_f32_16x16x32_bf16 v[136:139], a[12:15], a[216:219], v[136:139]\n"
      "v_mfma_f32_16x16x32_bf16 v[140:143], a[16:19], a[216:219], v[140:143]\n"
      "ds_read_b128 v[160:163], v12 offset:54784\n"
      "s_nop 0\n"
      "s_waitcnt lgkmcnt(7)\n"
      "ds_read_b128 v[164:167], v12 offset:1024\n"
      "v_mfma_f32_16x16x32_bf16 v[144:147], a[20:23], a[216:219], v[144:147]\n"
      "ds_read_b128 a[216:219], v8 offset:32256\n"
      "v_mfma_f32_16x16x32_bf16 v[136:139], a[24:27], a[220:223], v[136:139]\n"
      "v_mfma_f32_16x16x32_bf16 v[140:143], a[28:31], a[220:223], v[140:143]\n"
      "ds_read_b128 v[168:171], v12 offset:54528\n"
      "ds_read_b128 v[172:175], v12 offset:256\n"
      "s_waitcnt lgkmcnt(8)\n"
      "v_sub_f32 v148, v148, v156\n"
      "v_sub_f32 v149, v149, v157\n"
      "v_sub_f32 v150, v150, v158\n"
      "v_mfma_f32_16x16x32_bf16 v[144:147], a[32:35], a[220:223], v[144:147]\n"
      "ds_read_b128 a[220:223], v9 offset:32256\n"
      "v_sub_f32 v151, v151, v159\n"
      "v_mfma_f32_16x16x32_bf16 v[136:139], a[36:39], a[224:227], v[136:139]\n"
      "buffer_store_dwordx4 a[108:111], v18, s[8:11], s51 offen offset:0 sc1\n"
      "v_mfma_f32_16x16x32_bf16 v[140:143], a[40:43], a[224:227], v[140:143]\n"
      "ds_read_b128 v[176:179], v12 offset:53760\n"
      "ds_read_b128 v[180:183], v12 offset:2048\n"
      "ds_read_b128 v[184:187], v12 offset:55552\n"
      "s_waitcnt lgkmcnt(6)\n"
      "v_sub_f32 v152, v152, v160\n"
      "v_sub_f32 v153, v153, v161\n"
      "v_mfma_f32_16x16x32_bf16 v[144:147], a[44:47], a[224:227], v[144:147]\n"
      "ds_read_b128 a[224:227], v10 offset:32256\n"
      "v_sub_f32 v154, v154, v162\n"
      "v_sub_f32 v155, v155, v163\n"
      "v_fmac_f32 v152, -0.5, v148\n"
      "v_fmac_f32 v153, -0.5, v149\n"
      "v_fmac_f32_e64 v154, -0.5, v150\n"
      "v_mfma_f32_16x16x32_bf16 v[136:139], a[48:51], a[228:231], v[136:139]\n"
      "ds_read_b128 a[246:249], v11 offset:32256\n"
      "ds_read_b128 v[188:191], v12 offset:2304\n"
      "ds_read_b128 v[192:195], v12 offset:55808\n"
      "v_fmac_f32 v155, -0.5, v151\n"
      "v_mfma_f32_16x16x32_bf16 v[140:143], a[52:55], a[228:231], v[140:143]\n"
      "v_add_f32 v148, v148, v100\n"
      "v_add_f32 v149, v149, v101\n"
      "v_add_f32 v150, v150, v102\n"
      "v_mfma_f32_16x16x32_bf16 v[144:147], a[56:59], a[228:231], v[144:147]\n"
      "ds_read_b128 v[196:199], v12 offset:512\n"
      "ds_read_b128 v[200:203], v12 offset:54016\n"
      "ds_read_b128 v[204:207], v12 offset:1792\n"
      "v_mfma_f32_16x16x32_bf16 v[136:139], a[60:63], a[216:219], v[136:139]\n"
      "v_add_f32 v151, v151, v103\n"
      "v_mul_f32 v152, s46, v152\n"
      "v_mul_f32 v153, s46, v153\n"
      "v_mfma_f32_16x16x32_bf16 v[140:143], a[64:67], a[216:219], v[140:143]\n"
      "v_mul_f32 v154, s46, v154\n"
      "v_mul_f32 v155, s46, v155\n"
      "v_sub_f32 v148, v148, v152\n"
      "v_sub_f32_e64 v149, v149, v153\n"
      "v_mfma_f32_16x16x32_bf16 v[144:147], a[68:71], a[216:219], v[144:147]\n"
      "ds_read_b128 v[208:211], v12 offset:55296\n"
      "ds_read_b128 v[212:215], v12 offset:1536\n"
      "v_sub_f32 v150, v150, v154\n"
      "s_waitcnt lgkmcnt(5)\n"
      "v_mfma_f32_16x16x32_bf16 v[136:139], a[72:75], a[220:223], v[136:139]\n"
      "ds_read_b128 v[216:219], v12 offset:55040\n"
      "v_sub_f32 v151, v151, v155\n"
      "v_mfma_f32_16x16x32_bf16 v[140:143], a[76:79], a[220:223], v[140:143]\n"
      "ds_read_b128 v[220:223], v12 offset:768\n"
      "v_fmac_f32 v152, s45, v124\n"
      "v_fmac_f32 v153, s45, v125\n"
      "v_mfma_f32_16x16x32_bf16 v[144:147], a[80:83], a[220:223], v[144:147]\n"
      "ds_read_b128 v[224:227], v12 offset:54272\n"
      "ds_read_b128 v[228:231], v12 offset:2560\n"
      "v_fmac_f32 v154, s45, v126\n"
      "v_fmac_f32 v155, s45, v127\n"
      "v_mfma_f32_16x16x32_bf16 v[136:139], a[84:87], a[224:227], v[136:139]\n"
      "v_fmac_f32 v148, 4.0, v124\n"
      "v_fmac_f32 v149, 4.0, v125\n"
      "v_fmac_f32 v150, 4.0, v126\n"
      "v_mfma_f32_16x16x32_bf16 v[140:143], a[88:91], a[224:227], v[140:143]\n"
      "buffer_store_dwordx4 a[112:115], v18, s[8:11], s51 offen offset:1024 sc1\n"
      "v_mfma_f32_16x16x32_bf16 v[144:147], a[92:95], a[224:227], v[144:147]\n"
      "ds_read_b128 v[232:235], v12 offset:56064\n"
      "v_fmac_f32_e64 v151, 4.0, v127\n"
      "v_mfma_f32_16x16x32_bf16 v[136:139], a[96:99], a[246:249], v[136:139]\n"
      "ds_read_b128 v[236:239], v12 offset:2816\n"
      "v_cvt_pk_bf16_f32 v152, v152, v153\n"
      "v_mfma_f32_16x16x32_bf16 v[140:143], a[100:103], a[246:249], v[140:143]\n"
      "ds_read_b128 v[240:243], v12 offset:56320\n"
      "v_cvt_pk_bf16_f32 v153, v154, v155\n"
      "v_cvt_pk_bf16_f32 v148, v148, v149\n"
      "v_mfma_f32_16x16x32_bf16 v[144:147], a[104:107], a[246:249], v[144:147]\n"
      "ds_write_b64 v13, v[152:153] offset:12288\n"
      "buffer_store_dwordx4 a[116:119], v18, s[8:11], s51 offen offset:2048 sc1\n"
      "v_cvt_pk_bf16_f32 v149, v150, v151\n"
      "v_sub_f32 v164, v164, v172\n"
      "v_sub_f32 v165, v165, v173\n"
      "v_sub_f32_e64 v166, v166, v174\n"
      "v_sub_f32_e64 v167, v167, v175\n"
      "v_sub_f32_e64 v168, v168, v176\n"
      "v_sub_f32_e64 v169, v169, v177\n"
      "v_sub_f32_e64 v170, v170, v178\n"
      "v_sub_f32_e64 v171, v171, v179\n"
      "v_fmac_f32_e64 v168, -0.5, v164\n"
      "v_fmac_f32_e64 v169, -0.5, v165\n"
      "v_pk_fma_f32 v[170:171], v[166:167], s[46:47], v[170:171] op_sel:[0,1,0] op_sel_hi:[1,1,1]\n"
      "v_pk_add_f32 v[164:165], v[164:165], v[104:105]\n"
      "v_pk_add_f32 v[166:167], v[166:167], v[106:107]\n"
      "ds_write_b64 v13, v[148:149] offset:0\n"
      "v_pk_mul_f32 v[168:169], v[168:169], s[46:47] op_sel:[0,0] op_sel_hi:[1,0]\n"
      "v_pk_mul_f32 v[170:171], v[170:171], s[46:47] op_sel:[0,0] op_sel_hi:[1,0]\n"
      "v_pk_add_f32 v[164:165], v[164:165], v[168:169] neg_lo:[0,1] neg_hi:[0,1]\n"
      "v_pk_add_f32 v[166:167], v[166:167], v[170:171] neg_lo:[0,1] neg_hi:[0,1]\n"
      "v_pk_fma_f32 v[168:169], v[128:129], s[44:45], v[168:169] op_sel:[0,1,0] op_sel_hi:[1,1,1]\n"
      "v_pk_fma_f32 v[170:171], v[130:131], s[44:45], v[170:171] op_sel:[0,1,0] op_sel_hi:[1,1,1]\n"
      "v_pk_fma_f32 v[164:165], v[128:129], s[48:49], v[164:165] op_sel:[0,0,0] op_sel_hi:[1,0,1]\n"
      "v_pk_fma_f32 v[166:167], v[130:131], s[48:49], v[166:167] op_sel:[0,0,0] op_sel_hi:[1,0,1]\n"
      "v_cvt_pk_bf16_f32 v168, v168, v169\n"
      "v_cvt_pk_bf16_f32 v169, v170, v171\n"
      "v_cvt_pk_bf16_f32 v164, v164, v165\n"
      "v_cvt_pk_bf16_f32 v165, v166, v167\n"
      "v_pk_add_f32 v[180:181], v[180:181], v[188:189] neg_lo:[0,1] neg_hi:[0,1]\n"
      "ds_write_b64 v13, v[168:169] offset:12352\n"
      "ds_write_b64 v13, v[164:165] offset:64\n"
      "v_pk_add_f32 v[182:183], v[182:183], v[190:191] neg_lo:[0,1] neg_hi:[0,1]\n"
      "v_pk_add_f32 v[184:185], v[184:185], v[192:193] neg_lo:[0,1] neg_hi:[0,1]\n"
      "v_pk_add_f32 v[186:187], v[186:187], v[194:195] neg_lo:[0,1] neg_hi:[0,1]\n"
      "v_pk_fma_f32 v[184:185], v[180:181], s[46:47], v[184:185] op_sel:[0,1,0] op_sel_hi:[1,1,1]\n"
      "v_pk_fma_f32 v[186:187], v[182:183], s[46:47], v[186:187] op_sel:[0,1,0] op_sel_hi:[1,1,1]\n"
      "v_pk_add_f32 v[180:181], v[180:181], v[108:109]\n"
      "v_pk_add_f32 v[182:183], v[182:183], v[110:111]\n"
      "v_pk_mul_f32 v[184:185], v[184:185], s[46:47] op_sel:[0,0] op_sel_hi:[1,0]\n"
      "v_pk_mul_f32 v[186:187], v[186:187], s[46:47] op_sel:[0,0] op_sel_hi:[1,0]\n"
      "v_pk_add_f32 v[180:181], v[180:181], v[184:185] neg_lo:[0,1] neg_hi:[0,1]\n"
      "v_pk_add_f32 v[182:183], v[182:183], v[186:187] neg_lo:[0,1] neg_hi:[0,1]\n"
      "v_pk_fma_f32 v[184:185], v[132:133], s[44:45], v[184:185] op_sel:[0,1,0] op_sel_hi:[1,1,1]\n"
      "v_pk_fma_f32 v[186:187], v[134:135], s[44:45], v[186:187] op_sel:[0,1,0] op_sel_hi:[1,1,1]\n"
      "v_pk_fma_f32 v[180:181], v[132:133], s[48:49], v[180:181] op_sel:[0,0,0] op_sel_hi:[1,0,1]\n"
      "v_pk_fma_f32 v[182:183], v[134:135], s[48:49], v[182:183] op_sel:[0,0,0] op_sel_hi:[1,0,1]\n"
      "v_cvt_pk_bf16_f32 v184, v184, v185\n"
      "v_cvt_pk_bf16_f32 v185, v186, v187\n"
      "v_cvt_pk_bf16_f32 v180, v180, v181\n"
      "v_cvt_pk_bf16_f32 v181, v182, v183\n"
      "ds_write_b64 v13, v[184:185] offset:12416\n"
      "ds_write_b64 v13, v[180:181] offset:128\n"
      "s_waitcnt lgkmcnt(2)\n"
      "v_pk_add_f32 v[196:197], v[196:197], v[204:205] neg_lo:[0,1] neg_hi:[0,1]\n"
      "v_pk_add_f32 v[198:199], v[198:199], v[206:207] neg_lo:[0,1] neg_hi:[0,1]\n"
      "v_pk_add_f32 v[200:201], v[200:201], v[208:209] neg_lo:[0,1] neg_hi:[0,1]\n"
      "v_pk_add_f32 v[202:203], v[202:203], v[210:211] neg_lo:[0,1] neg_hi:[0,1]\n"
      "v_pk_fma_f32 v[200:201], v[196:197], s[46:47], v[200:201] op_sel:[0,1,0] op_sel_hi:[1,1,1]\n"
      "v_pk_fma_f32 v[202:203], v[198:199], s[46:47], v[202:203] op_sel:[0,1,0] op_sel_hi:[1,1,1]\n"
      "v_pk_add_f32 v[196:197], v[196:197], v[112:113]\n"
      "v_add_f32 v198, v198, v114\n"
      "s_waitcnt lgkmcnt(0)\n"
      "s_barrier\n"
      "ds_read_b128 a[120:123], v15 offset:6144\n"
      "ds_read_b128 a[124:127], v16 offset:6144\n"
      "ds_read_b128 a[128:131], v17 offset:6144\n"
      "v_add_f32_e64 v199, v199, v115\n"
      "v_pk_mul_f32 v[200:201], v[200:201], s[46:47] op_sel:[0,0] op_sel_hi:[1,0]\n"
      "v_pk_mul_f32 v[202:203], v[202:203], s[46:47] op_sel:[0,0] op_sel_hi:[1,0]\n"
      "v_pk_add_f32 v[196:197], v[196:197], v[200:201] neg_lo:[0,1] neg_hi:[0,1]\n"
      "v_pk_add_f32 v[198:199], v[198:199], v[202:203] neg_lo:[0,1] neg_hi:[0,1]\n"
      "v_pk_add_f32 v[212:213], v[212:213], v[220:221] neg_lo:[0,1] neg_hi:[0,1]\n"
      "v_pk_add_f32 v[214:215], v[214:215], v[222:223] neg_lo:[0,1] neg_hi:[0,1]\n"
      "v_pk_add_f32 v[216:217], v[216:217], v[224:225] neg_lo:[0,1] neg_hi:[0,1]\n"
      "v_sub_f32 v218, v218, v226\n"
      "s_waitcnt lgkmcnt(2)\n"
      "v_sub_f32_e64 v219, v219, v227\n"
      "v_pk_fma_f32 v[216:217], v[212:213], s[46:47], v[216:217] op_sel:[0,1,0] op_sel_hi:[1,1,1]\n"
      "v_pk_fma_f32 v[218:219], v[214:215], s[46:47], v[218:219] op_sel:[0,1,0] op_sel_hi:[1,1,1]\n"
      "v_pk_add_f32 v[212:213], v[212:213], v[116:117]\n"
      "v_pk_add_f32 v[214:215], v[214:215], v[118:119]\n"
      "v_pk_mul_f32 v[216:217], v[216:217], s[46:47] op_sel:[0,0] op_sel_hi:[1,0]\n"
      "v_pk_mul_f32 v[218:219], v[218:219], s[46:47] op_sel:[0,0] op_sel_hi:[1,0]\n"
      "v_pk_add_f32 v[212:213], v[212:213], v[216:217] neg_lo:[0,1] neg_hi:[0,1]\n"
      "v_pk_add_f32 v[214:215], v[214:215], v[218:219] neg_lo:[0,1] neg_hi:[0,1]\n"
      "v_pk_add_f32 v[228:229], v[228:229], v[236:237] neg_lo:[0,1] neg_hi:[0,1]\n"
      "v_pk_add_f32 v[230:231], v[230:231], v[238:239] neg_lo:[0,1] neg_hi:[0,1]\n"
      "v_sub_f32 v232, v232, v240\n"
      "buffer_store_dwordx4 a[120:123], v18, s[8:11], s52 offen offset:0 sc1\n"
      "v_sub_f32_e64 v233, v233, v241\n"
      "v_pk_add_f32 v[234:235], v[234:235], v[242:243] neg_lo:[0,1] neg_hi:[0,1]\n"
      "v_pk_fma_f32 v[232:233], v[228:229], s[46:47], v[232:233] op_sel:[0,1,0] op_sel_hi:[1,1,1]\n"
      "s_waitcnt lgkmcnt(0)\n"
      "v_pk_fma_f32 v[234:235], v[230:231], s[46:47], v[234:235] op_sel:[0,1,0] op_sel_hi:[1,1,1]\n"
      "v_pk_add_f32 v[228:229], v[228:229], v[120:121]\n"
      "v_pk_add_f32 v[230:231], v[230:231], v[122:123]\n"
      "v_pk_mul_f32 v[232:233], v[232:233], s[46:47] op_sel:[0,0] op_sel_hi:[1,0]\n"
      "v_pk_mul_f32 v[234:235], v[234:235], s[46:47] op_sel:[0,0] op_sel_hi:[1,0]\n"
      "v_pk_add_f32 v[228:229], v[228:229], v[232:233] neg_lo:[0,1] neg_hi:[0,1]\n"
      "v_pk_add_f32 v[230:231], v[230:231], v[234:235] neg_lo:[0,1] neg_hi:[0,1]\n"
      "v_pk_fma_f32 v[200:201], v[136:137], s[44:45], v[200:201] op_sel:[0,1,0] op_sel_hi:[1,1,1]\n"
      "v_pk_fma_f32 v[202:203], v[138:139], s[44:45], v[202:203] op_sel:[0,1,0] op_sel_hi:[1,1,1]\n"
      "v_pk_fma_f32 v[196:197], v[136:137], s[48:49], v[196:197] op_sel:[0,0,0] op_sel_hi:[1,0,1]\n"
      "v_pk_fma_f32 v[198:199], v[138:139], s[48:49], v[198:199] op_sel:[0,0,0] op_sel_hi:[1,0,1]\n"
      "v_cvt_pk_bf16_f32 v200, v200, v201\n"
      "v_cvt_pk_bf16_f32 v201, v202, v203\n"
      "v_cvt_pk_bf16_f32 v196, v196, v197\n"
      "v_cvt_pk_bf16_f32 v197, v198, v199\n"
      "v_fmac_f32 v216, s45, v140\n"
      "buffer_store_dwordx4 a[124:127], v18, s[8:11], s52 offen offset:1024 sc1\n"
      "ds_write_b64 v13, v[200:201] offset:15360\n"
      "ds_write_b64 v13, v[196:197] offset:3072\n"
      "v_fmac_f32 v217, s45, v141\n"
      "v_pk_fma_f32 v[218:219], v[142:143], s[44:45], v[218:219] op_sel:[0,1,0] op_sel_hi:[1,1,1]\n"
      "v_pk_fma_f32 v[212:213], v[140:141], s[48:49], v[212:213] op_sel:[0,0,0] op_sel_hi:[1,0,1]\n"
      "v_pk_fma_f32 v[214:215], v[142:143], s[48:49], v[214:215] op_sel:[0,0,0] op_sel_hi:[1,0,1]\n"
      "v_cvt_pk_bf16_f32 v216, v216, v217\n"
      "v_cvt_pk_bf16_f32 v217, v218, v219\n"
      "v_cvt_pk_bf16_f32 v212, v212, v213\n"
      "v_cvt_pk_bf16_f32 v213, v214, v215\n"
      "v_pk_fma_f32 v[232:233], v[144:145], s[44:45], v[232:233] op_sel:[0,1,0] op_sel_hi:[1,1,1]\n"
      "v_pk_fma_f32 v[234:235], v[146:147], s[44:45], v[234:235] op_sel:[0,1,0] op_sel_hi:[1,1,1]\n"
      "buffer_store_dwordx4 a[128:131], v18, s[8:11], s52 offen offset:2048 sc1\n"
      "ds_write_b64 v13, v[216:217] offset:15424\n"
      "v_pk_fma_f32 v[228:229], v[144:145], s[48:49], v[228:229] op_sel:[0,0,0] op_sel_hi:[1,0,1]\n"
      "v_pk_fma_f32 v[230:231], v[146:147], s[48:49], v[230:231] op_sel:[0,0,0] op_sel_hi:[1,0,1]\n"
      "v_cvt_pk_bf16_f32 v232, v232, v233\n"
      "ds_write_b64 v13, v[212:213] offset:3136\n"
      "v_cvt_pk_bf16_f32 v233, v234, v235\n"
      "v_cvt_pk_bf16_f32 v228, v228, v229\n"
      "v_cvt_pk_bf16_f32 v229, v230, v231\n"
      "ds_write_b64 v13, v[232:233] offset:15488\n"
      "ds_write_b64 v13, v[228:229] offset:3200\n"
      "s_waitcnt lgkmcnt(0)\n"
      "s_barrier\n"
      "ds_read_b128 a[132:135], v15 offset:9216\n"
      "ds_read_b128 a[136:139], v16 offset:9216\n"
      "ds_read_b128 a[140:143], v17 offset:9216\n"
      "s_waitcnt lgkmcnt(2)\n"
      "buffer_store_dwordx4 a[132:135], v18, s[8:11], s53 offen offset:0 sc1\n"
      "s_waitcnt lgkmcnt(1)\n"
      "buffer_store_dwordx4 a[136:139], v18, s[8:11], s53 offen offset:1024 sc1\n"
      "s_waitcnt lgkmcnt(0)\n"
      "buffer_store_dwordx4 a[140:143], v18, s[8:11], s53 offen offset:2048 sc1\n"
      "LDend_%=:\n"
      "s_branch Lfin_%=\n"
      ".p2align 6\n"
      "LS_%=:\n"
      "s_and_b32 s9, s9, 0xffff\n"
      "v_lshlrev_b32 v43, 3, v37\n"
      "v_mul_u32_u24 v44, 192, v38\n"
      "v_mul_u32_u24 v45, 1536, v39\n"
      "s_mul_i32 s26, s17, 12288\n"
      "s_lshl_b32 s27, s18, 5\n"
      "v_add_u32 v43, v43, v44\n"
      "s_sub_u32 s26, s27, s26\n"
      "v_mul_u32_u24 v44, 1536, v41\n"
      "s_add_u32 s26, s26, 30720\n"
      "v_add3_u32 v33, v43, v45, s26\n"
      "v_and_b32 v45, 16, v36\n"
      "v_add_u32 v43, v43, v44\n"
      "s_lshl_b32 s27, s18, 6\n"
      "v_lshl_add_u32 v43, v45, 1, v43\n"
      "s_add_u32 s27, s27, 18432\n"
      "v_add_u32 v34, 18560, v43\n"
      "v_add_u32 v32, s27, v43\n"
      "v_lshlrev_b32 v43, 4, v37\n"
      "s_movk_i32 s26, 0x3f00\n"
      "s_mov_b32 s27, 0\n"
      "s_lshl_b32 s36, s17, 7\n"
      "s_mov_b32 s29, 0\n"
      "s_sub_u32 s28, s26, s36\n"
      "s_mov_b32 s31, 0\n"
      "s_sub_u32 s30, s28, s36\n"
      "s_lshl_b32 s36, s18, 15\n"
      "v_lshlrev_b64 v[44:45], v43, s[26:27]\n"
      "s_or_b32 s28, s28, s36\n"
      "v_lshlrev_b64 v[48:49], v43, s[30:31]\n"
      "v_lshlrev_b64 v[46:47], v43, s[28:29]\n"
      "v_lshlrev_b32 v50, 27, v36\n"
      "v_accvgpr_write_b32 a168, 0\n"
      "v_accvgpr_write_b32 a169, 0\n"
      "v_accvgpr_write_b32 a170, 0\n"
      "v_accvgpr_write_b32 a171, 0\n"
      "v_accvgpr_write_b32 a172, 0\n"
      "v_accvgpr_write_b32 a173, 0\n"
      "v_accvgpr_write_b32 a174, 0\n"
      "v_accvgpr_write_b32 a175, 0\n"
      "v_accvgpr_write_b32 a176, 0\n"
      "v_accvgpr_write_b32 a177, 0\n"
      "v_accvgpr_write_b32 a178, 0\n"
      "v_accvgpr_write_b32 a179, 0\n"
      "v_accvgpr_write_b32 a238, v44\n"
      "v_accvgpr_write_b32 a239, v45\n"
      "v_accvgpr_write_b32 a240, v46\n"
      "v_accvgpr_write_b32 a241, v47\n"
      "v_accvgpr_write_b32 a254, v48\n"
      "v_accvgpr_write_b32 a255, v49\n"
      "v_ashrrev_i32 v50, 31, v50\n"
      "v_and_b32 v50, 0x80008000, v50\n"
      "v_xor_b32 v46, v50, v46\n"
      "v_xor_b32 v47, v50, v47\n"
      "s_mov_b32 exec_lo, 0x000f000f\n"
      "s_mov_b32 exec_hi, 0x00f000f0\n"
      "v_accvgpr_write_b32 a168, v44\n"
      "v_accvgpr_write_b32 a169, v45\n"
      "v_accvgpr_write_b32 a172, v46\n"
      "v_accvgpr_write_b32 a173, v47\n"
      "v_accvgpr_write_b32 a176, v48\n"
      "v_accvgpr_write_b32 a177, v49\n"
      "s_lshl_b64 exec, exec, 8\n"
      "v_accvgpr_write_b32 a170, v44\n"
      "v_accvgpr_write_b32 a171, v45\n"
      "v_accvgpr_write_b32 a174, v46\n"
      "v_accvgpr_write_b32 a175, v47\n"
      "v_accvgpr_write_b32 a178, v48\n"
      "v_accvgpr_write_b32 a179, v49\n"
      "s_mov_b64 exec, -1\n"
      "v_lshrrev_b32 v51, 1, v36\n"
      "v_and_b32 v42, 15, v36\n"
      "v_bitop3_b32 v42, v51, v42, 4 bitop3:0x6c\n"
      "s_mul_i32 s26, s17, 21888\n"
      "v_lshlrev_b32 v43, 1, v42\n"
      "s_add_u32 s26, s26, 78336\n"
      "v_mul_u32_u24 v44, 96, v42\n"
      "v_lshl_add_u32 v45, v41, 9, s26\n"
      "v_bitop3_b32 v46, v43, v36, 16 bitop3:0x28\n"
      "v_add_u32 v46, v46, v45\n"
      "v_and_b32 v47, 0x1e00, v44\n"
      "v_add3_u32 v3, v46, v44, v47\n"
      "v_add_u32 v44, 32, v44\n"
      "v_and_b32 v47, 0x1e00, v44\n"
      "v_add3_u32 v4, v46, v44, v47\n"
      "v_add_u32 v44, 32, v44\n"
      "v_and_b32 v47, 0x1e00, v44\n"
      "v_add3_u32 v5, v46, v44, v47\n"
      "v_add_u32 v43, 4, v43\n"
      "v_bitop3_b32 v46, v43, v36, 16 bitop3:0x28\n"
      "v_add_u32 v46, v46, v45\n"
      "v_add_u32 v44, 128, v44\n"
      "v_and_b32 v47, 0x1e00, v44\n"
      "v_add3_u32 v6, v46, v44, v47\n"
      "v_add_u32 v44, 32, v44\n"
      "v_and_b32 v47, 0x1e00, v44\n"
      "v_add3_u32 v7, v46, v44, v47\n"
      "v_add_u32 v44, 32, v44\n"
      "v_and_b32 v47, 0x1e00, v44\n"
      "v_add3_u32 v8, v46, v44, v47\n"
      "v_add_u32 v43, 4, v43\n"
      "v_bitop3_b32 v46, v43, v36, 16 bitop3:0x28\n"
      "v_add_u32 v46, v46, v45\n"
      "v_add_u32 v44, 128, v44\n"
      "v_and_b32 v47, 0x1e00, v44\n"
      "v_add3_u32 v9, v46, v44, v47\n"
      "v_add_u32 v44, 32, v44\n"
      "v_and_b32 v47, 0x1e00, v44\n"
      "v_add3_u32 v10, v46, v44, v47\n"
      "v_add_u32 v44, 32, v44\n"
      "v_and_b32 v47, 0x1e00, v44\n"
      "v_add3_u32 v11, v46, v44, v47\n"
      "v_lshlrev_b32 v43, 31, v36\n"
      "v_and_b32 v44, 2, v36\n"
      "v_or_b32 v43, 0x3f803f80, v43\n"
      "v_mul_u32_u24 v45, 0xc00080, v44\n"
      "v_lshl_add_u32 v44, v44, 22, v43\n"
      "v_add_u32 v45, v45, v43\n"
      "v_accvgpr_write_b32 a232, v44\n"
      "v_accvgpr_write_b32 a233, v45\n"
      "v_lshlrev_b32 v43, 3, v38\n"
      "s_mov_b32 s26, 806420736\n"
      "s_lshl_b32 s27, s18, 8\n"
      "v_bfe_u32 v43, s26, v43, 8\n"
      "v_mul_u32_u24 v44, 7168, v38\n"
      "s_add_u32 s28, s27, 78336\n"
      "v_lshl_add_u32 v43, v43, 3, v44\n"
      "v_lshl_add_u32 v44, v39, 2, v37\n"
      "v_lshl_add_u32 v43, v44, 4, v43\n"
      "s_lshl_b32 s27, s27, 1\n"
      "s_sub_u32 s27, s64, s27\n"
      "s_add_u32 s27, s27, 256\n"
      "v_add_u32 v14, s28, v43\n"
      "v_lshl_add_u32 v35, v36, 2, s27\n"
      "s_mul_i32 s26, s16, 3584\n"
      "v_and_b32 v43, 31, v36\n"
      "s_add_u32 s26, s26, 24832\n"
      "v_mul_u32_u24 v44, 96, v43\n"
      "v_lshlrev_b32 v43, 1, v43\n"
      "v_add_u32 v44, s26, v44\n"
      "v_bitop3_b32 v46, v43, v51, 16 bitop3:0x28\n"
      "v_add_u32 v0, v46, v44\n"
      "v_add_u32 v43, 4, v43\n"
      "v_add_u32 v44, 192, v44\n"
      "v_bitop3_b32 v46, v43, v51, 16 bitop3:0x28\n"
      "v_add_u32 v1, v46, v44\n"
      "v_add_u32 v43, 4, v43\n"
      "v_add_u32 v44, 192, v44\n"
      "v_bitop3_b32 v46, v43, v51, 16 bitop3:0x28\n"
      "v_add_u32 v2, v46, v44\n"
      "v_and_b32 v43, 4, v51\n"
      "v_or_b32 v43, v43, v37\n"
      "v_mul_u32_u24 v43, 3104, v43\n"
      "v_and_b32 v44, 4, v42\n"
      "v_lshl_add_u32 v43, v44, 5, v43\n"
      "s_lshl_b32 s26, s18, 6\n"
      "v_lshl_add_u32 v43, v39, 4, v43\n"
      "s_mul_i32 s27, s17, 6144\n"
      "v_add_u32 v12, s26, v43\n"
      "s_sub_u32 s27, 142848, s27\n"
      "v_bfe_u32 v43, v42, 1, 2\n"
      "s_lshl_b32 s26, s18, 1\n"
      "v_mul_u32_u24 v44, 192, v42\n"
      "v_bitop3_b32 v43, s26, v43, v41 bitop3:0x96\n"
      "v_and_b32 v45, 8, v51\n"
      "v_lshl_add_u32 v44, v43, 4, v44\n"
      "s_mul_i32 s26, s18, 12288\n"
      "v_add3_u32 v13, v44, v45, s27\n"
      "v_mul_u32_u24 v43, 171, v36\n"
      "s_add_u32 s26, s26, 136704\n"
      "v_lshrrev_b32 v44, 8, v43\n"
      "v_bitop3_b32 v44, v44, v31, 48 bitop3:0x6c\n"
      "v_add_u32 v28, s26, v44\n"
      "v_add_u32 v43, 10944, v43\n"
      "s_add_u32 s26, s26, 1024\n"
      "v_lshrrev_b32 v44, 8, v43\n"
      "v_bitop3_b32 v44, v44, v31, 48 bitop3:0x6c\n"
      "v_add_u32 v29, s26, v44\n"
      "v_add_u32 v43, 10944, v43\n"
      "s_add_u32 s26, s26, 1024\n"
      "v_lshrrev_b32 v44, 8, v43\n"
      "v_bitop3_b32 v44, v44, v31, 48 bitop3:0x6c\n"
      "v_add_u32 v30, s26, v44\n"
      "s_mul_i32 s62, s18, 393216\n"
      "s_mov_b32 s55, 0\n"
      "s_add_u32 s62, s62, s19\n"
      "s_mov_b32 s60, 0x80000000\n"
      "s_add_u32 s62, s62, s21\n"
      "s_mov_b32 s61, 0x80000000\n"
      "s_sub_u32 s62, s62, 1572864\n"
      "s_add_u32 s58, s62, 786432\n"
      "s_mov_b32 s45, 0xbeaaaaab\n"
      "s_mov_b32 s46, 0xbfa00000\n"
      "s_mov_b32 s47, 0x3eaaaaab\n"
      "s_mov_b32 s48, 0x3f400000\n"
      "LSpro_%=:\n"
      "s_add_u32 m0, s63, 43008\n"
      "s_add_u32 s52, s63, 61440\n"
      "buffer_load_dwordx4 v31, s[12:15], s52 offen offset:0 lds\n"
      "buffer_load_dwordx4 v31, s[12:15], s52 offen offset:1024 lds\n"
      "buffer_load_dwordx4 v31, s[12:15], s52 offen offset:2048 lds\n"
      "s_add_u32 m0, s63, 49152\n"
      "s_add_u32 s52, s63, 116736\n"
      "buffer_load_dwordx4 v31, s[12:15], s52 offen offset:0 lds\n"
      "buffer_load_dwordx4 v31, s[12:15], s52 offen offset:1024 lds\n"
      "buffer_load_dwordx4 v31, s[12:15], s52 offen offset:2048 lds\n"
      "s_add_u32 m0, s63, 0\n"
      "s_add_u32 s52, s63, 12288\n"
      "s_waitcnt vmcnt(14)\n"
      "s_nop 0\n"
      "s_barrier\n"
      "ds_read_b64_tr_b16 a[132:133], v32 offset:0\n"
      "ds_read_b64_tr_b16 a[134:135], v32 offset:768\n"
      "ds_read_b64_tr_b16 a[136:137], v32 offset:6144\n"
      "ds_read_b64_tr_b16 a[138:139], v32 offset:6912\n"
      "ds_read_b64_tr_b16 a[140:141], v32 offset:12288\n"
      "ds_read_b64_tr_b16 a[142:143], v32 offset:13056\n"
      "ds_read_b64_tr_b16 a[144:145], v34 offset:0\n"
      "ds_read_b64_tr_b16 a[146:147], v34 offset:768\n"
      "ds_read_b64_tr_b16 a[148:149], v34 offset:6144\n"
      "ds_read_b64_tr_b16 a[150:151], v34 offset:6912\n"
      "ds_read_b64_tr_b16 a[152:153], v34 offset:12288\n"
      "ds_read_b64_tr_b16 a[154:155], v34 offset:13056\n"
      "s_waitcnt lgkmcnt(10)\n"
      "v_mfma_f32_32x32x16_bf16 v[52:67], a[168:171], a[132:135], 0\n"
      "buffer_load_dwordx4 v31, s[12:15], s52 offen offset:0 lds\n"
      "ds_read_b64_tr_b16 a[156:157], v32 offset:3072\n"
      "ds_read_b64_tr_b16 a[158:159], v32 offset:3840\n"
      "ds_read_b64_tr_b16 a[0:1], v33 offset:0\n"
      "s_waitcnt lgkmcnt(11)\n"
      "v_mfma_f32_32x32x16_bf16 v[52:67], a[172:175], a[136:139], v[52:67]\n"
      "buffer_load_dwordx4 v31, s[12:15], s52 offen offset:1024 lds\n"
      "ds_read_b64_tr_b16 a[160:161], v32 offset:9216\n"
      "ds_read_b64_tr_b16 a[162:163], v32 offset:9984\n"
      "ds_read_b64_tr_b16 a[2:3], v33 offset:768\n"
      "s_waitcnt lgkmcnt(10)\n"
      "v_mfma_f32_32x32x16_bf16 v[52:67], a[176:179], a[140:143], v[52:67]\n"
      "buffer_load_dwordx4 v31, s[12:15], s52 offen offset:2048 lds\n"
      "ds_read_b64_tr_b16 a[164:165], v32 offset:15360\n"
      "ds_read_b64_tr_b16 a[166:167], v32 offset:16128\n"
      "ds_read_b64_tr_b16 a[4:5], v33 offset:64\n"
      "v_add_u32 v32, 18432, v32\n"
      "v_mfma_f32_4x4x4_16b_bf16 v[84:87], a[238:239], a[144:145], 0\n"
      "ds_read_b64_tr_b16 a[132:133], v34 offset:3072\n"
      "ds_read_b64_tr_b16 a[6:7], v33 offset:832\n"
      "v_mfma_f32_4x4x4_16b_bf16 v[88:91], a[238:239], a[146:147], 0\n"
      "ds_read_b64_tr_b16 a[134:135], v34 offset:3840\n"
      "s_waitcnt lgkmcnt(9)\n"
      "v_mfma_f32_4x4x4_16b_bf16 v[84:87], a[240:241], a[148:149], v[84:87]\n"
      "ds_read_b64_tr_b16 a[136:137], v34 offset:9216\n"
      "ds_read_b64_tr_b16 a[8:9], v33 offset:128\n"
      "v_mfma_f32_4x4x4_16b_bf16 v[88:91], a[240:241], a[150:151], v[88:91]\n"
      "ds_read_b64_tr_b16 a[138:139], v34 offset:9984\n"
      "v_mfma_f32_4x4x4_16b_bf16 v[84:87], a[254:255], a[152:153], v[84:87]\n"
      "ds_read_b64_tr_b16 a[140:141], v34 offset:15360\n"
      "ds_read_b64_tr_b16 a[10:11], v33 offset:896\n"
      "v_mfma_f32_4x4x4_16b_bf16 v[88:91], a[254:255], a[154:155], v[88:91]\n"
      "ds_read_b64_tr_b16 a[142:143], v34 offset:16128\n"
      "v_add_u32 v34, 18432, v34\n"
      "s_add_u32 m0, s63, 6144\n"
      "s_add_u32 s52, s63, 67584\n"
      "v_add_u32 v33, 18432, v33\n"
      "s_waitcnt vmcnt(3)\n"
      "s_barrier\n"
      "v_mfma_f32_32x32x16_bf16 v[68:83], a[168:171], a[156:159], 0\n"
      "buffer_load_dwordx4 v31, s[12:15], s52 offen offset:0 lds\n"
      "ds_read_b64_tr_b16 a[144:145], v32 offset:0\n"
      "ds_read_b64_tr_b16 a[146:147], v32 offset:768\n"
      "ds_read_b64_tr_b16 a[12:13], v33 offset:0\n"
      "ds_read_b64_tr_b16 a[14:15], v33 offset:768\n"
      "v_cvt_pk_bf16_f32 v148, v52, v56\n"
      "v_cvt_pk_bf16_f32 v149, v53, v57\n"
      "s_nop 0\n"
      "s_waitcnt lgkmcnt(4)\n"
      "v_mfma_f32_32x32x16_bf16 v[68:83], a[172:175], a[160:163], v[68:83]\n"
      "buffer_load_dwordx4 v31, s[12:15], s52 offen offset:1024 lds\n"
      "ds_read_b64_tr_b16 a[148:149], v32 offset:6144\n"
      "ds_read_b64_tr_b16 a[150:151], v32 offset:6912\n"
      "ds_read_b64_tr_b16 a[16:17], v33 offset:64\n"
      "v_cvt_pk_bf16_f32 v150, v54, v58\n"
      "v_cvt_pk_bf16_f32 v151, v55, v59\n"
      "v_cvt_pk_bf16_f32 v44, v60, v64\n"
      "v_mfma_f32_32x32x16_bf16 v[68:83], a[176:179], a[164:167], v[68:83]\n"
      "buffer_load_dwordx4 v31, s[12:15], s52 offen offset:2048 lds\n"
      "ds_read_b64_tr_b16 a[152:153], v32 offset:12288\n"
      "ds_read_b64_tr_b16 a[154:155], v32 offset:13056\n"
      "ds_read_b64_tr_b16 a[18:19], v33 offset:832\n"
      "ds_read_b64_tr_b16 a[20:21], v33 offset:128\n"
      "v_cvt_pk_bf16_f32 v220, v84, v88\n"
      "v_cvt_pk_bf16_f32 v45, v61, v65\n"
      "v_mfma_f32_4x4x4_16b_bf16 v[92:95], a[238:239], a[132:133], 0\n"
      "ds_read_b64_tr_b16 a[156:157], v34 offset:0\n"
      "v_mfma_f32_4x4x4_16b_bf16 v[96:99], a[238:239], a[134:135], 0\n"
      "ds_read_b64_tr_b16 a[158:159], v34 offset:768\n"
      "v_mfma_f32_4x4x4_16b_bf16 v[92:95], a[240:241], a[136:137], v[92:95]\n"
      "ds_read_b64_tr_b16 a[160:161], v34 offset:6144\n"
      "v_mfma_f32_4x4x4_16b_bf16 v[96:99], a[240:241], a[138:139], v[96:99]\n"
      "ds_read_b64_tr_b16 a[162:163], v34 offset:6912\n"
      "v_mfma_f32_4x4x4_16b_bf16 v[92:95], a[254:255], a[140:141], v[92:95]\n"
      "ds_read_b64_tr_b16 a[164:165], v34 offset:12288\n"
      "v_mfma_f32_4x4x4_16b_bf16 v[96:99], a[254:255], a[142:143], v[96:99]\n"
      "ds_read_b64_tr_b16 a[166:167], v34 offset:13056\n"
      "s_add_u32 m0, s63, 59904\n"
      "s_add_u32 s52, s63, 18432\n"
      "v_cvt_pk_bf16_f32 v46, v62, v66\n"
      "v_cvt_pk_bf16_f32 v47, v63, v67\n"
      "s_nop 0\n"
      "s_waitcnt lgkmcnt(6)\n"
      "v_mfma_f32_32x32x16_bf16 v[52:67], a[168:171], a[144:147], 0\n"
      "buffer_load_dwordx4 v31, s[12:15], s52 offen offset:0 lds\n"
      "ds_read_b64_tr_b16 a[132:133], v32 offset:3072\n"
      "ds_read_b64_tr_b16 a[134:135], v32 offset:3840\n"
      "ds_read_b128 a[224:227], v3 offset:0\n"
      "ds_read_b64_tr_b16 a[22:23], v33 offset:896\n"
      "v_cvt_pk_bf16_f32 v184, v68, v72\n"
      "v_cvt_pk_bf16_f32 v185, v69, v73\n"
      "v_cvt_pk_bf16_f32 v221, v85, v89\n"
      "v_mfma_f32_32x32x16_bf16 v[52:67], a[172:175], a[148:151], v[52:67]\n"
      "buffer_load_dwordx4 v31, s[12:15], s52 offen offset:1024 lds\n"
      "ds_read_b64_tr_b16 a[136:137], v32 offset:9216\n"
      "ds_read_b64_tr_b16 a[138:139], v32 offset:9984\n"
      "v_cvt_pk_bf16_f32 v186, v70, v74\n"
      "v_cvt_pk_bf16_f32 v187, v71, v75\n"
      "v_cvt_pk_bf16_f32 v222, v86, v90\n"
      "v_cvt_pk_bf16_f32 v100, v76, v80\n"
      "v_mfma_f32_32x32x16_bf16 v[52:67], a[176:179], a[152:155], v[52:67]\n"
      "buffer_load_dwordx4 v31, s[12:15], s52 offen offset:2048 lds\n"
      "ds_read_b64_tr_b16 a[140:141], v32 offset:15360\n"
      "ds_read_b64_tr_b16 a[142:143], v32 offset:16128\n"
      "ds_read_b128 a[228:231], v3 offset:3072\n"
      "v_subrev_u32 v32, 36864, v32\n"
      "v_cvt_pk_bf16_f32 v223, v87, v91\n"
      "s_nop 0\n"
      "s_waitcnt lgkmcnt(5)\n"
      "v_mfma_f32_4x4x4_16b_bf16 v[84:87], a[238:239], a[156:157], 0\n"
      "ds_read_b64_tr_b16 a[144:145], v34 offset:3072\n"
      "v_mfma_f32_4x4x4_16b_bf16 v[88:91], a[238:239], a[158:159], 0\n"
      "ds_read_b64_tr_b16 a[146:147], v34 offset:3840\n"
      "v_mfma_f32_4x4x4_16b_bf16 v[84:87], a[240:241], a[160:161], v[84:87]\n"
      "ds_read_b64_tr_b16 a[148:149], v34 offset:9216\n"
      "v_mfma_f32_4x4x4_16b_bf16 v[88:91], a[240:241], a[162:163], v[88:91]\n"
      "ds_read_b64_tr_b16 a[150:151], v34 offset:9984\n"
      "v_mfma_f32_4x4x4_16b_bf16 v[84:87], a[254:255], a[164:165], v[84:87]\n"
      "ds_read_b64_tr_b16 a[152:153], v34 offset:15360\n"
      "v_mfma_f32_4x4x4_16b_bf16 v[88:91], a[254:255], a[166:167], v[88:91]\n"
      "ds_read_b64_tr_b16 a[154:155], v34 offset:16128\n"
      "v_subrev_u32 v34, 36864, v34\n"
      "s_add_u32 m0, s63, 136704\n"
      "s_add_u32 s52, s63, 24576\n"
      "v_cvt_pk_bf16_f32 v101, v77, v81\n"
      "v_cvt_pk_bf16_f32 v102, v78, v82\n"
      "v_subrev_u32 v33, 36864, v33\n"
      "v_cvt_pk_bf16_f32 v103, v79, v83\n"
      "s_waitcnt vmcnt(3)\n"
      "s_barrier\n"
      "v_mfma_f32_32x32x16_bf16 v[68:83], a[168:171], a[132:135], 0\n"
      "buffer_load_dwordx4 v31, s[12:15], s52 offen offset:0 lds\n"
      "ds_read_b64_tr_b16 a[156:157], v32 offset:0\n"
      "ds_read_b64_tr_b16 a[158:159], v32 offset:768\n"
      "ds_read_b64_tr_b16 a[24:25], v33 offset:0\n"
      "v_cvt_pk_bf16_f32 v92, v92, v96\n"
      "v_cvt_pk_bf16_f32 v93, v93, v97\n"
      "s_nop 0\n"
      "s_waitcnt lgkmcnt(3)\n"
      "v_mfma_f32_32x32x16_bf16 v[68:83], a[172:175], a[136:139], v[68:83]\n"
      "buffer_load_dwordx4 v31, s[12:15], s52 offen offset:1024 lds\n"
      "ds_read_b64_tr_b16 a[160:161], v32 offset:6144\n"
      "ds_read_b64_tr_b16 a[162:163], v32 offset:6912\n"
      "ds_read_b64_tr_b16 a[26:27], v33 offset:768\n"
      "v_cvt_pk_bf16_f32 v152, v52, v56\n"
      "v_cvt_pk_bf16_f32 v94, v94, v98\n"
      "v_cvt_pk_bf16_f32 v153, v53, v57\n"
      "v_mfma_f32_32x32x16_bf16 v[68:83], a[176:179], a[140:143], v[68:83]\n"
      "buffer_load_dwordx4 v31, s[12:15], s52 offen offset:2048 lds\n"
      "ds_read_b64_tr_b16 a[164:165], v32 offset:12288\n"
      "ds_read_b64_tr_b16 a[166:167], v32 offset:13056\n"
      "s_add_u32 m0, s63, 142848\n"
      "s_add_u32 s52, s63, 79872\n"
      "v_cvt_pk_bf16_f32 v95, v95, v99\n"
      "v_mfma_f32_16x16x32_bf16 v[124:127], a[0:3], a[224:227], 0\n"
      "buffer_load_dwordx4 v31, s[12:15], s52 offen offset:0 lds\n"
      "v_mfma_f32_16x16x32_bf16 v[128:131], a[4:7], a[224:227], 0\n"
      "ds_read_b64_tr_b16 a[28:29], v33 offset:64\n"
      "v_cvt_pk_bf16_f32 v154, v54, v58\n"
      "v_cvt_pk_bf16_f32 v224, v84, v88\n"
      "v_cvt_pk_bf16_f32 v155, v55, v59\n"
      "v_mfma_f32_16x16x32_bf16 v[132:135], a[8:11], a[224:227], 0\n"
      "buffer_load_dwordx4 v31, s[12:15], s52 offen offset:1024 lds\n"
      "ds_read_b64_tr_b16 a[30:31], v33 offset:832\n"
      "v_mfma_f32_16x16x32_bf16 v[136:139], a[0:3], a[228:231], 0\n"
      "ds_read_b64_tr_b16 a[32:33], v33 offset:128\n"
      "v_accvgpr_write_b32 a180, v92\n"
      "v_cvt_pk_bf16_f32 v48, v60, v64\n"
      "v_accvgpr_write_b32 a181, v93\n"
      "v_mfma_f32_16x16x32_bf16 v[140:143], a[4:7], a[228:231], 0\n"
      "buffer_load_dwordx4 v31, s[12:15], s52 offen offset:2048 lds\n"
      "v_mfma_f32_16x16x32_bf16 v[144:147], a[8:11], a[228:231], 0\n"
      "ds_read_b64_tr_b16 a[34:35], v33 offset:896\n"
      "v_accvgpr_write_b32 a182, v94\n"
      "v_accvgpr_write_b32 a183, v95\n"
      "v_cvt_pk_bf16_f32 v49, v61, v65\n"
      "v_cvt_pk_bf16_f32 v225, v85, v89\n"
      "v_mfma_f32_4x4x4_16b_bf16 v[92:95], a[238:239], a[144:145], 0\n"
      "ds_read_b64_tr_b16 a[132:133], v34 offset:0\n"
      "v_mfma_f32_4x4x4_16b_bf16 v[96:99], a[238:239], a[146:147], 0\n"
      "ds_read_b64_tr_b16 a[134:135], v34 offset:768\n"
      "v_mfma_f32_4x4x4_16b_bf16 v[92:95], a[240:241], a[148:149], v[92:95]\n"
      "ds_read_b64_tr_b16 a[136:137], v34 offset:6144\n"
      "v_mfma_f32_4x4x4_16b_bf16 v[96:99], a[240:241], a[150:151], v[96:99]\n"
      "ds_read_b64_tr_b16 a[138:139], v34 offset:6912\n"
      "v_mfma_f32_4x4x4_16b_bf16 v[92:95], a[254:255], a[152:153], v[92:95]\n"
      "ds_read_b64_tr_b16 a[140:141], v34 offset:12288\n"
      "v_mfma_f32_4x4x4_16b_bf16 v[96:99], a[254:255], a[154:155], v[96:99]\n"
      "ds_read_b64_tr_b16 a[142:143], v34 offset:13056\n"
      "s_add_u32 m0, s63, 107520\n"
      "s_add_u32 s52, s63, 30720\n"
      "v_cvt_pk_bf16_f32 v50, v62, v66\n"
      "v_cvt_pk_bf16_f32 v51, v63, v67\n"
      "s_nop 0\n"
      "s_waitcnt lgkmcnt(6)\n"
      "v_mfma_f32_32x32x16_bf16 v[52:67], a[168:171], a[156:159], 0\n"
      "buffer_load_dwordx4 v31, s[12:15], s52 offen offset:0 lds\n"
      "ds_read_b64_tr_b16 a[144:145], v32 offset:3072\n"
      "ds_read_b64_tr_b16 a[146:147], v32 offset:3840\n"
      "ds_read_b128 a[216:219], v4 offset:0\n"
      "v_cvt_pk_bf16_f32 v188, v68, v72\n"
      "v_cvt_pk_bf16_f32 v189, v69, v73\n"
      "v_cvt_pk_bf16_f32 v190, v70, v74\n"
      "v_cvt_pk_bf16_f32 v226, v86, v90\n"
      "v_mfma_f32_32x32x16_bf16 v[52:67], a[172:175], a[160:163], v[52:67]\n"
      "buffer_load_dwordx4 v31, s[12:15], s52 offen offset:1024 lds\n"
      "ds_read_b64_tr_b16 a[148:149], v32 offset:9216\n"
      "ds_read_b64_tr_b16 a[150:151], v32 offset:9984\n"
      "v_cvt_pk_bf16_f32 v191, v71, v75\n"
      "v_cvt_pk_bf16_f32 v92, v92, v96\n"
      "v_cvt_pk_bf16_f32 v104, v76, v80\n"
      "v_cvt_pk_bf16_f32 v93, v93, v97\n"
      "v_mfma_f32_32x32x16_bf16 v[52:67], a[176:179], a[164:167], v[52:67]\n"
      "buffer_load_dwordx4 v31, s[12:15], s52 offen offset:2048 lds\n"
      "ds_read_b64_tr_b16 a[152:153], v32 offset:15360\n"
      "ds_read_b64_tr_b16 a[154:155], v32 offset:16128\n"
      "ds_read_b128 a[220:223], v4 offset:3072\n"
      "v_add_u32 v32, 59904, v32\n"
      "v_cvt_pk_bf16_f32 v227, v87, v91\n"
      "s_nop 0\n"
      "s_waitcnt lgkmcnt(5)\n"
      "v_mfma_f32_4x4x4_16b_bf16 v[84:87], a[238:239], a[132:133], 0\n"
      "ds_read_b64_tr_b16 a[156:157], v34 offset:3072\n"
      "v_mfma_f32_4x4x4_16b_bf16 v[88:91], a[238:239], a[134:135], 0\n"
      "ds_read_b64_tr_b16 a[158:159], v34 offset:3840\n"
      "v_mfma_f32_4x4x4_16b_bf16 v[84:87], a[240:241], a[136:137], v[84:87]\n"
      "ds_read_b64_tr_b16 a[160:161], v34 offset:9216\n"
      "v_mfma_f32_4x4x4_16b_bf16 v[88:91], a[240:241], a[138:139], v[88:91]\n"
      "ds_read_b64_tr_b16 a[162:163], v34 offset:9984\n"
      "v_mfma_f32_4x4x4_16b_bf16 v[84:87], a[254:255], a[140:141], v[84:87]\n"
      "ds_read_b64_tr_b16 a[164:165], v34 offset:15360\n"
      "v_mfma_f32_4x4x4_16b_bf16 v[88:91], a[254:255], a[142:143], v[88:91]\n"
      "ds_read_b64_tr_b16 a[166:167], v34 offset:16128\n"
      "v_add_u32 v34, 59904, v34\n"
      "s_add_u32 s54, s54, 196608\n"
      "s_add_u32 m0, s49, 85512\n"
      "v_cvt_pk_bf16_f32 v105, v77, v81\n"
      "v_cvt_pk_bf16_f32 v106, v78, v82\n"
      "v_cvt_pk_bf16_f32 v107, v79, v83\n"
      "v_add_u32 v33, 59904, v33\n"
      "s_waitcnt vmcnt(9)\n"
      "s_barrier\n"
      "v_mfma_f32_32x32x16_bf16 v[68:83], a[168:171], a[144:147], 0\n"
      "buffer_load_dwordx4 v15, s[4:7], s54 offen offset:0 nt lds\n"
      "ds_read_b64_tr_b16 a[132:133], v32 offset:0\n"
      "ds_read_b64_tr_b16 a[134:135], v32 offset:768\n"
      "ds_read_b64_tr_b16 a[36:37], v33 offset:0\n"
      "v_cvt_pk_bf16_f32 v94, v94, v98\n"
      "v_cvt_pk_bf16_f32 v156, v52, v56\n"
      "s_nop 0\n"
      "s_waitcnt lgkmcnt(3)\n"
      "v_mfma_f32_32x32x16_bf16 v[68:83], a[172:175], a[148:151], v[68:83]\n"
      "buffer_load_dwordx4 v16, s[4:7], s54 offen offset:1024 nt lds\n"
      "ds_read_b64_tr_b16 a[136:137], v32 offset:6144\n"
      "ds_read_b64_tr_b16 a[138:139], v32 offset:6912\n"
      "ds_read_b64_tr_b16 a[38:39], v33 offset:768\n"
      "ds_read_b64_tr_b16 a[40:41], v33 offset:64\n"
      "v_cvt_pk_bf16_f32 v157, v53, v57\n"
      "v_cvt_pk_bf16_f32 v95, v95, v99\n"
      "v_mfma_f32_32x32x16_bf16 v[68:83], a[176:179], a[152:155], v[68:83]\n"
      "buffer_load_dwordx4 v17, s[4:7], s54 offen offset:2048 nt lds\n"
      "ds_read_b64_tr_b16 a[140:141], v32 offset:12288\n"
      "ds_read_b64_tr_b16 a[142:143], v32 offset:13056\n"
      "s_add_u32 s54, s54, 196608\n"
      "s_add_u32 m0, s49, 92808\n"
      "v_cvt_pk_bf16_f32 v158, v54, v58\n"
      "v_mfma_f32_16x16x32_bf16 v[124:127], a[12:15], a[216:219], v[124:127]\n"
      "buffer_load_dwordx4 v15, s[4:7], s54 offen offset:0 nt lds\n"
      "v_mfma_f32_16x16x32_bf16 v[128:131], a[16:19], a[216:219], v[128:131]\n"
      "ds_read_b64_tr_b16 a[42:43], v33 offset:832\n"
      "v_cvt_pk_bf16_f32 v228, v84, v88\n"
      "v_accvgpr_write_b32 a184, v92\n"
      "v_cvt_pk_bf16_f32 v159, v55, v59\n"
      "v_mfma_f32_16x16x32_bf16 v[132:135], a[20:23], a[216:219], v[132:135]\n"
      "buffer_load_dwordx4 v16, s[4:7], s54 offen offset:1024 nt lds\n"
      "v_cvt_pk_bf16_f32 v229, v85, v89\n"
      "v_mfma_f32_16x16x32_bf16 v[136:139], a[12:15], a[220:223], v[136:139]\n"
      "ds_read_b64_tr_b16 a[44:45], v33 offset:128\n"
      "ds_read_b64_tr_b16 a[46:47], v33 offset:896\n"
      "v_cvt_pk_bf16_f32 v36, v60, v64\n"
      "v_accvgpr_write_b32 a185, v93\n"
      "v_mfma_f32_16x16x32_bf16 v[140:143], a[16:19], a[220:223], v[140:143]\n"
      "buffer_load_dwordx4 v17, s[4:7], s54 offen offset:2048 nt lds\n"
      "v_mfma_f32_16x16x32_bf16 v[144:147], a[20:23], a[220:223], v[144:147]\n"
      "v_accvgpr_write_b32 a186, v94\n"
      "v_accvgpr_write_b32 a187, v95\n"
      "v_cvt_pk_bf16_f32 v37, v61, v65\n"
      "v_cvt_pk_bf16_f32 v38, v62, v66\n"
      "v_mfma_f32_4x4x4_16b_bf16 v[92:95], a[238:239], a[156:157], 0\n"
      "ds_read_b64_tr_b16 a[144:145], v34 offset:0\n"
      "v_mfma_f32_4x4x4_16b_bf16 v[96:99], a[238:239], a[158:159], 0\n"
      "ds_read_b64_tr_b16 a[146:147], v34 offset:768\n"
      "v_mfma_f32_4x4x4_16b_bf16 v[92:95], a[240:241], a[160:161], v[92:95]\n"
      "ds_read_b64_tr_b16 a[148:149], v34 offset:6144\n"
      "v_mfma_f32_4x4x4_16b_bf16 v[96:99], a[240:241], a[162:163], v[96:99]\n"
      "ds_read_b64_tr_b16 a[150:151], v34 offset:6912\n"
      "v_mfma_f32_4x4x4_16b_bf16 v[92:95], a[254:255], a[164:165], v[92:95]\n"
      "ds_read_b64_tr_b16 a[152:153], v34 offset:12288\n"
      "v_mfma_f32_4x4x4_16b_bf16 v[96:99], a[254:255], a[166:167], v[96:99]\n"
      "ds_read_b64_tr_b16 a[154:155], v34 offset:13056\n"
      "s_add_u32 m0, s57, 92808\n"
      "v_cvt_pk_bf16_f32 v39, v63, v67\n"
      "v_cvt_pk_bf16_f32 v230, v86, v90\n"
      "v_cvt_pk_bf16_f32 v192, v68, v72\n"
      "v_cvt_pk_bf16_f32 v231, v87, v91\n"
      "s_nop 0\n"
      "s_waitcnt lgkmcnt(6)\n"
      "v_mfma_f32_32x32x16_bf16 v[52:67], a[168:171], a[132:135], 0\n"
      "buffer_load_dwordx4 v18, s[4:7], s54 offen offset:3072 nt lds\n"
      "ds_read_b64_tr_b16 a[156:157], v32 offset:3072\n"
      "ds_read_b64_tr_b16 a[158:159], v32 offset:3840\n"
      "ds_read_b128 a[224:227], v5 offset:0\n"
      "s_add_u32 s54, s54, 196608\n"
      "s_add_u32 m0, s49, 100224\n"
      "v_cvt_pk_bf16_f32 v193, v69, v73\n"
      "v_add_u32 v33, 76800, v33\n"
      "v_mfma_f32_32x32x16_bf16 v[52:67], a[172:175], a[136:139], v[52:67]\n"
      "buffer_load_dwordx4 v15, s[4:7], s54 offen offset:0 nt lds\n"
      "ds_read_b64_tr_b16 a[160:161], v32 offset:9216\n"
      "ds_read_b64_tr_b16 a[162:163], v32 offset:9984\n"
      "v_cvt_pk_bf16_f32 v194, v70, v74\n"
      "v_cvt_pk_bf16_f32 v195, v71, v75\n"
      "v_cvt_pk_bf16_f32 v108, v76, v80\n"
      "v_cvt_pk_bf16_f32 v92, v92, v96\n"
      "v_cvt_pk_bf16_f32 v109, v77, v81\n"
      "v_cvt_pk_bf16_f32 v93, v93, v97\n"
      "v_mfma_f32_32x32x16_bf16 v[52:67], a[176:179], a[140:143], v[52:67]\n"
      "buffer_load_dwordx4 v16, s[4:7], s54 offen offset:1024 nt lds\n"
      "ds_read_b64_tr_b16 a[164:165], v32 offset:15360\n"
      "ds_read_b64_tr_b16 a[166:167], v32 offset:16128\n"
      "ds_read_b128 a[228:231], v5 offset:3072\n"
      "v_add_u32 v32, 76800, v32\n"
      "v_cvt_pk_bf16_f32 v110, v78, v82\n"
      "v_cvt_pk_bf16_f32 v111, v79, v83\n"
      "s_nop 0\n"
      "s_waitcnt lgkmcnt(4)\n"
      "v_mfma_f32_4x4x4_16b_bf16 v[84:87], a[238:239], a[144:145], 0\n"
      "ds_read_b64_tr_b16 a[132:133], v34 offset:3072\n"
      "v_mfma_f32_4x4x4_16b_bf16 v[88:91], a[238:239], a[146:147], 0\n"
      "ds_read_b64_tr_b16 a[134:135], v34 offset:3840\n"
      "v_mfma_f32_4x4x4_16b_bf16 v[84:87], a[240:241], a[148:149], v[84:87]\n"
      "ds_read_b64_tr_b16 a[136:137], v34 offset:9216\n"
      "v_mfma_f32_4x4x4_16b_bf16 v[88:91], a[240:241], a[150:151], v[88:91]\n"
      "ds_read_b64_tr_b16 a[138:139], v34 offset:9984\n"
      "v_mfma_f32_4x4x4_16b_bf16 v[84:87], a[254:255], a[152:153], v[84:87]\n"
      "ds_read_b64_tr_b16 a[140:141], v34 offset:15360\n"
      "v_mfma_f32_4x4x4_16b_bf16 v[88:91], a[254:255], a[154:155], v[88:91]\n"
      "ds_read_b64_tr_b16 a[142:143], v34 offset:16128\n"
      "s_mov_b32 m0, s64\n"
      "s_nop 0\n"
      "ds_write_addtid_b32 v44 offset:39168\n"
      "ds_write_addtid_b32 v45 offset:39680\n"
      "ds_write_addtid_b32 v46 offset:40192\n"
      "ds_write_addtid_b32 v47 offset:40704\n"
      "ds_write_addtid_b32 v48 offset:41216\n"
      "ds_write_addtid_b32 v49 offset:41728\n"
      "ds_write_addtid_b32 v50 offset:42240\n"
      "ds_write_addtid_b32 v51 offset:42752\n"
      "ds_write_addtid_b32 v36 offset:43264\n"
      "ds_write_addtid_b32 v37 offset:43776\n"
      "ds_write_addtid_b32 v38 offset:44288\n"
      "ds_write_addtid_b32 v39 offset:44800\n"
      "v_add_u32 v34, 76800, v34\n"
      "s_waitcnt lgkmcnt(0)\n"
      "s_waitcnt vmcnt(12)\n"
      "s_barrier\n"
      "v_mfma_f32_32x32x16_bf16 v[68:83], a[168:171], a[156:159], 0\n"
      "s_add_u32 m0, s49, 100224\n"
      "s_nop 0\n"
      "buffer_load_dwordx4 v17, s[4:7], s54 offen offset:2048 nt lds\n"
      "ds_read_b64_tr_b16 a[144:145], v32 offset:0\n"
      "ds_read_b64_tr_b16 a[146:147], v32 offset:768\n"
      "v_mfma_f32_32x32x16_bf16 v[68:83], a[172:175], a[160:163], v[68:83]\n"
      "ds_read_b64_tr_b16 a[148:149], v32 offset:6144\n"
      "ds_read_b64_tr_b16 a[150:151], v32 offset:6912\n"
      "ds_read_b64_tr_b16 a[48:49], v33 offset:0\n"
      "v_cvt_pk_bf16_f32 v160, v52, v56\n"
      "v_cvt_pk_bf16_f32 v161, v53, v57\n"
      "v_cvt_pk_bf16_f32 v232, v84, v88\n"
      "v_mfma_f32_32x32x16_bf16 v[68:83], a[176:179], a[164:167], v[68:83]\n"
      "ds_read_b64_tr_b16 a[152:153], v32 offset:12288\n"
      "ds_read_b64_tr_b16 a[154:155], v32 offset:13056\n"
      "ds_read_b64_tr_b16 a[50:51], v33 offset:768\n"
      "ds_read2st64_b32 a[108:109], v35 offset0:153 offset1:155\n"
      "v_cvt_pk_bf16_f32 v94, v94, v98\n"
      "v_cvt_pk_bf16_f32 v162, v54, v58\n"
      "v_mfma_f32_16x16x32_bf16 v[124:127], a[24:27], a[224:227], v[124:127]\n"
      "ds_read2st64_b32 a[112:113], v35 offset0:161 offset1:163\n"
      "ds_read2st64_b32 a[116:117], v35 offset0:169 offset1:171\n"
      "v_mfma_f32_16x16x32_bf16 v[128:131], a[28:31], a[224:227], v[128:131]\n"
      "ds_read_b64_tr_b16 a[52:53], v33 offset:64\n"
      "v_cvt_pk_bf16_f32 v95, v95, v99\n"
      "v_mfma_f32_16x16x32_bf16 v[132:135], a[32:35], a[224:227], v[132:135]\n"
      "v_cvt_pk_bf16_f32 v163, v55, v59\n"
      "v_accvgpr_write_b32 a188, v92\n"
      "v_cvt_pk_bf16_f32 v40, v60, v64\n"
      "v_mfma_f32_16x16x32_bf16 v[136:139], a[24:27], a[228:231], v[136:139]\n"
      "ds_read_b64_tr_b16 a[54:55], v33 offset:832\n"
      "v_accvgpr_write_b32 a189, v93\n"
      "v_mfma_f32_16x16x32_bf16 v[140:143], a[28:31], a[228:231], v[140:143]\n"
      "v_cvt_pk_bf16_f32 v41, v61, v65\n"
      "v_cvt_pk_bf16_f32 v233, v85, v89\n"
      "v_accvgpr_write_b32 a190, v94\n"
      "v_mfma_f32_16x16x32_bf16 v[144:147], a[32:35], a[228:231], v[144:147]\n"
      "ds_read_b64_tr_b16 a[56:57], v33 offset:128\n"
      "v_accvgpr_write_b32 a191, v95\n"
      "v_mfma_f32_4x4x4_16b_bf16 v[92:95], a[238:239], a[132:133], 0\n"
      "ds_read_b64_tr_b16 a[156:157], v34 offset:0\n"
      "v_mfma_f32_4x4x4_16b_bf16 v[96:99], a[238:239], a[134:135], 0\n"
      "ds_read_b64_tr_b16 a[158:159], v34 offset:768\n"
      "v_mfma_f32_4x4x4_16b_bf16 v[92:95], a[240:241], a[136:137], v[92:95]\n"
      "ds_read_b64_tr_b16 a[160:161], v34 offset:6144\n"
      "v_mfma_f32_4x4x4_16b_bf16 v[96:99], a[240:241], a[138:139], v[96:99]\n"
      "ds_read_b64_tr_b16 a[162:163], v34 offset:6912\n"
      "v_mfma_f32_4x4x4_16b_bf16 v[92:95], a[254:255], a[140:141], v[92:95]\n"
      "ds_read_b64_tr_b16 a[164:165], v34 offset:12288\n"
      "v_mfma_f32_4x4x4_16b_bf16 v[96:99], a[254:255], a[142:143], v[96:99]\n"
      "ds_read_b64_tr_b16 a[166:167], v34 offset:13056\n"
      "v_cvt_pk_bf16_f32 v42, v62, v66\n"
      "v_cvt_pk_bf16_f32 v43, v63, v67\n"
      "s_mov_b32 m0, s64\n"
      "s_nop 0\n"
      "ds_write_addtid_b32 v40 offset:18432\n"
      "ds_write_addtid_b32 v41 offset:18944\n"
      "ds_write_addtid_b32 v42 offset:19456\n"
      "ds_write_addtid_b32 v43 offset:19968\n"
      "s_waitcnt lgkmcnt(9)\n"
      "v_mfma_f32_32x32x16_bf16 v[52:67], a[168:171], a[144:147], 0\n"
      "ds_read_b64_tr_b16 a[132:133], v32 offset:3072\n"
      "ds_read_b64_tr_b16 a[134:135], v32 offset:3840\n"
      "ds_read_b128 a[216:219], v6 offset:0\n"
      "ds_read2st64_b32 a[110:111], v35 offset0:157 offset1:159\n"
      "v_cvt_pk_bf16_f32 v196, v68, v72\n"
      "v_cvt_pk_bf16_f32 v197, v69, v73\n"
      "v_mfma_f32_32x32x16_bf16 v[52:67], a[172:175], a[148:151], v[52:67]\n"
      "ds_read_b64_tr_b16 a[136:137], v32 offset:9216\n"
      "ds_read_b64_tr_b16 a[138:139], v32 offset:9984\n"
      "ds_read_b64_tr_b16 a[58:59], v33 offset:896\n"
      "ds_read2st64_b32 a[114:115], v35 offset0:165 offset1:167\n"
      "v_cvt_pk_bf16_f32 v198, v70, v74\n"
      "v_cvt_pk_bf16_f32 v199, v71, v75\n"
      "v_mfma_f32_32x32x16_bf16 v[52:67], a[176:179], a[152:155], v[52:67]\n"
      "ds_read_b64_tr_b16 a[140:141], v32 offset:15360\n"
      "ds_read_b64_tr_b16 a[142:143], v32 offset:16128\n"
      "ds_read2st64_b32 a[118:119], v35 offset0:173 offset1:175\n"
      "ds_read_b128 a[220:223], v6 offset:3072\n"
      "v_subrev_u32 v32, 29184, v32\n"
      "v_cvt_pk_bf16_f32 v234, v86, v90\n"
      "v_cvt_pk_bf16_f32 v235, v87, v91\n"
      "v_mfma_f32_4x4x4_16b_bf16 v[84:87], a[238:239], a[156:157], 0\n"
      "ds_read_b64_tr_b16 a[144:145], v34 offset:3072\n"
      "s_waitcnt lgkmcnt(8)\n"
      "v_mfma_f32_4x4x4_16b_bf16 v[88:91], a[238:239], a[158:159], 0\n"
      "ds_read_b64_tr_b16 a[146:147], v34 offset:3840\n"
      "v_mfma_f32_4x4x4_16b_bf16 v[84:87], a[240:241], a[160:161], v[84:87]\n"
      "ds_read_b64_tr_b16 a[148:149], v34 offset:9216\n"
      "v_mfma_f32_4x4x4_16b_bf16 v[88:91], a[240:241], a[162:163], v[88:91]\n"
      "ds_read_b64_tr_b16 a[150:151], v34 offset:9984\n"
      "v_mfma_f32_4x4x4_16b_bf16 v[84:87], a[254:255], a[164:165], v[84:87]\n"
      "ds_read_b64_tr_b16 a[152:153], v34 offset:15360\n"
      "v_mfma_f32_4x4x4_16b_bf16 v[88:91], a[254:255], a[166:167], v[88:91]\n"
      "ds_read_b64_tr_b16 a[154:155], v34 offset:16128\n"
      "v_subrev_u32 v34, 29184, v34\n"
      "v_cvt_pk_bf16_f32 v112, v76, v80\n"
      "v_cvt_pk_bf16_f32 v113, v77, v81\n"
      "v_cvt_pk_bf16_f32 v114, v78, v82\n"
      "v_subrev_u32 v33, 29184, v33\n"
      "v_cvt_pk_bf16_f32 v115, v79, v83\n"
      "s_waitcnt vmcnt(10)\n"
      "s_barrier\n"
      "v_mfma_f32_32x32x16_bf16 v[68:83], a[168:171], a[132:135], 0\n"
      "ds_read_b64_tr_b16 a[156:157], v32 offset:0\n"
      "ds_read_b64_tr_b16 a[158:159], v32 offset:768\n"
      "ds_read_b64_tr_b16 a[60:61], v33 offset:0\n"
      "v_cvt_pk_bf16_f32 v92, v92, v96\n"
      "v_cvt_pk_bf16_f32 v164, v52, v56\n"
      "v_cvt_pk_bf16_f32 v236, v84, v88\n"
      "s_nop 0\n"
      "s_waitcnt lgkmcnt(3)\n"
      "v_mfma_f32_32x32x16_bf16 v[68:83], a[172:175], a[136:139], v[68:83]\n"
      "ds_read_b64_tr_b16 a[160:161], v32 offset:6144\n"
      "ds_read_b64_tr_b16 a[162:163], v32 offset:6912\n"
      "ds_read_b64_tr_b16 a[62:63], v33 offset:768\n"
      "v_cvt_pk_bf16_f32 v93, v93, v97\n"
      "v_cvt_pk_bf16_f32 v165, v53, v57\n"
      "v_cvt_pk_bf16_f32 v94, v94, v98\n"
      "v_mfma_f32_32x32x16_bf16 v[68:83], a[176:179], a[140:143], v[68:83]\n"
      "ds_read_b64_tr_b16 a[164:165], v32 offset:12288\n"
      "ds_read_b64_tr_b16 a[166:167], v32 offset:13056\n"
      "ds_read2st64_b32 a[120:121], v35 offset0:72 offset1:74\n"
      "v_cvt_pk_bf16_f32 v166, v54, v58\n"
      "v_cvt_pk_bf16_f32 v95, v95, v99\n"
      "v_cvt_pk_bf16_f32 v167, v55, v59\n"
      "v_mfma_f32_16x16x32_bf16 v[124:127], a[36:39], a[216:219], v[124:127]\n"
      "ds_read_b64_tr_b16 a[64:65], v33 offset:64\n"
      "v_accvgpr_write_b32 a192, v92\n"
      "v_mfma_f32_16x16x32_bf16 v[128:131], a[40:43], a[216:219], v[128:131]\n"
      "v_cvt_pk_bf16_f32 v44, v60, v64\n"
      "v_cvt_pk_bf16_f32 v237, v85, v89\n"
      "v_mfma_f32_16x16x32_bf16 v[132:135], a[44:47], a[216:219], v[132:135]\n"
      "ds_read_b64_tr_b16 a[66:67], v33 offset:832\n"
      "v_accvgpr_write_b32 a193, v93\n"
      "v_mfma_f32_16x16x32_bf16 v[136:139], a[36:39], a[220:223], v[136:139]\n"
      "v_cvt_pk_bf16_f32 v45, v61, v65\n"
      "v_accvgpr_write_b32 a194, v94\n"
      "v_mfma_f32_16x16x32_bf16 v[140:143], a[40:43], a[220:223], v[140:143]\n"
      "ds_read_b64_tr_b16 a[68:69], v33 offset:128\n"
      "v_cvt_pk_bf16_f32 v46, v62, v66\n"
      "v_mfma_f32_16x16x32_bf16 v[144:147], a[44:47], a[220:223], v[144:147]\n"
      "v_accvgpr_write_b32 a195, v95\n"
      "v_cvt_pk_bf16_f32 v238, v86, v90\n"
      "v_mfma_f32_4x4x4_16b_bf16 v[92:95], a[238:239], a[144:145], 0\n"
      "ds_read_b64_tr_b16 a[132:133], v34 offset:0\n"
      "v_mfma_f32_4x4x4_16b_bf16 v[96:99], a[238:239], a[146:147], 0\n"
      "ds_read_b64_tr_b16 a[134:135], v34 offset:768\n"
      "v_mfma_f32_4x4x4_16b_bf16 v[92:95], a[240:241], a[148:149], v[92:95]\n"
      "ds_read_b64_tr_b16 a[136:137], v34 offset:6144\n"
      "v_mfma_f32_4x4x4_16b_bf16 v[96:99], a[240:241], a[150:151], v[96:99]\n"
      "ds_read_b64_tr_b16 a[138:139], v34 offset:6912\n"
      "v_mfma_f32_4x4x4_16b_bf16 v[92:95], a[254:255], a[152:153], v[92:95]\n"
      "ds_read_b64_tr_b16 a[140:141], v34 offset:12288\n"
      "v_mfma_f32_4x4x4_16b_bf16 v[96:99], a[254:255], a[154:155], v[96:99]\n"
      "ds_read_b64_tr_b16 a[142:143], v34 offset:13056\n"
      "ds_read2st64_b32 a[122:123], v35 offset0:76 offset1:78\n"
      "v_cvt_pk_bf16_f32 v47, v63, v67\n"
      "s_nop 0\n"
      "s_waitcnt lgkmcnt(7)\n"
      "v_mfma_f32_32x32x16_bf16 v[52:67], a[168:171], a[156:159], 0\n"
      "ds_read_b64_tr_b16 a[144:145], v32 offset:3072\n"
      "ds_read_b64_tr_b16 a[146:147], v32 offset:3840\n"
      "ds_read_b128 a[224:227], v7 offset:0\n"
      "v_cvt_pk_bf16_f32 v200, v68, v72\n"
      "v_cvt_pk_bf16_f32 v201, v69, v73\n"
      "v_cvt_pk_bf16_f32 v202, v70, v74\n"
      "v_mfma_f32_32x32x16_bf16 v[52:67], a[172:175], a[160:163], v[52:67]\n"
      "ds_read_b64_tr_b16 a[148:149], v32 offset:9216\n"
      "ds_read_b64_tr_b16 a[150:151], v32 offset:9984\n"
      "ds_read_b64_tr_b16 a[70:71], v33 offset:896\n"
      "v_cvt_pk_bf16_f32 v203, v71, v75\n"
      "v_cvt_pk_bf16_f32 v116, v76, v80\n"
      "v_mov_b32 v15, v28\n"
      "v_mfma_f32_32x32x16_bf16 v[52:67], a[176:179], a[164:167], v[52:67]\n"
      "ds_read_b64_tr_b16 a[152:153], v32 offset:15360\n"
      "ds_read_b64_tr_b16 a[154:155], v32 offset:16128\n"
      "ds_read_b128 a[228:231], v7 offset:3072\n"
      "v_subrev_u32 v32, 107520, v32\n"
      "v_cvt_pk_bf16_f32 v239, v87, v91\n"
      "s_waitcnt lgkmcnt(6)\n"
      "v_mfma_f32_4x4x4_16b_bf16 v[84:87], a[238:239], a[132:133], 0\n"
      "ds_read_b64_tr_b16 a[156:157], v34 offset:3072\n"
      "v_mfma_f32_4x4x4_16b_bf16 v[88:91], a[238:239], a[134:135], 0\n"
      "ds_read_b64_tr_b16 a[158:159], v34 offset:3840\n"
      "v_mfma_f32_4x4x4_16b_bf16 v[84:87], a[240:241], a[136:137], v[84:87]\n"
      "ds_read_b64_tr_b16 a[160:161], v34 offset:9216\n"
      "v_mfma_f32_4x4x4_16b_bf16 v[88:91], a[240:241], a[138:139], v[88:91]\n"
      "ds_read_b64_tr_b16 a[162:163], v34 offset:9984\n"
      "v_mfma_f32_4x4x4_16b_bf16 v[84:87], a[254:255], a[140:141], v[84:87]\n"
      "ds_read_b64_tr_b16 a[164:165], v34 offset:15360\n"
      "v_mfma_f32_4x4x4_16b_bf16 v[88:91], a[254:255], a[142:143], v[88:91]\n"
      "ds_read_b64_tr_b16 a[166:167], v34 offset:16128\n"
      "v_subrev_u32 v34, 107520, v34\n"
      "v_cvt_pk_bf16_f32 v117, v77, v81\n"
      "v_cvt_pk_bf16_f32 v118, v78, v82\n"
      "v_cvt_pk_bf16_f32 v119, v79, v83\n"
      "v_subrev_u32 v33, 107520, v33\n"
      "s_waitcnt vmcnt(0)\n"
      "s_barrier\n"
      "v_mfma_f32_32x32x16_bf16 v[68:83], a[168:171], a[144:147], 0\n"
      "ds_read_b64_tr_b16 a[132:133], v32 offset:0\n"
      "ds_read_b64_tr_b16 a[134:135], v32 offset:768\n"
      "ds_read_b64_tr_b16 a[72:73], v33 offset:0\n"
      "v_cvt_pk_bf16_f32 v92, v92, v96\n"
      "v_cvt_pk_bf16_f32 v168, v52, v56\n"
      "s_nop 0\n"
      "s_waitcnt lgkmcnt(3)\n"
      "v_mfma_f32_32x32x16_bf16 v[68:83], a[172:175], a[148:151], v[68:83]\n"
      "ds_read_b64_tr_b16 a[136:137], v32 offset:6144\n"
      "ds_read_b64_tr_b16 a[138:139], v32 offset:6912\n"
      "ds_read_b64_tr_b16 a[74:75], v33 offset:768\n"
      "v_cvt_pk_bf16_f32 v93, v93, v97\n"
      "v_cvt_pk_bf16_f32 v169, v53, v57\n"
      "v_cvt_pk_bf16_f32 v94, v94, v98\n"
      "v_mfma_f32_32x32x16_bf16 v[68:83], a[176:179], a[152:155], v[68:83]\n"
      "ds_read_b64_tr_b16 a[140:141], v32 offset:12288\n"
      "ds_read_b64_tr_b16 a[142:143], v32 offset:13056\n"
      "ds_read_b64_tr_b16 a[76:77], v33 offset:64\n"
      "v_cvt_pk_bf16_f32 v170, v54, v58\n"
      "v_cvt_pk_bf16_f32 v240, v84, v88\n"
      "v_cvt_pk_bf16_f32 v95, v95, v99\n"
      "v_mfma_f32_16x16x32_bf16 v[124:127], a[48:51], a[224:227], v[124:127]\n"
      "v_cvt_pk_bf16_f32 v171, v55, v59\n"
      "v_mov_b32_e64 v16, v29\n"
      "v_mfma_f32_16x16x32_bf16 v[128:131], a[52:55], a[224:227], v[128:131]\n"
      "ds_read_b64_tr_b16 a[78:79], v33 offset:832\n"
      "v_accvgpr_write_b32 a196, v92\n"
      "v_mfma_f32_16x16x32_bf16 v[132:135], a[56:59], a[224:227], v[132:135]\n"
      "v_cvt_pk_bf16_f32 v48, v60, v64\n"
      "v_cvt_pk_bf16_f32 v241, v85, v89\n"
      "v_mfma_f32_16x16x32_bf16 v[136:139], a[48:51], a[228:231], v[136:139]\n"
      "ds_read_b64_tr_b16 a[80:81], v33 offset:128\n"
      "v_accvgpr_write_b32 a197, v93\n"
      "v_mfma_f32_16x16x32_bf16 v[140:143], a[52:55], a[228:231], v[140:143]\n"
      "v_cvt_pk_bf16_f32 v49, v61, v65\n"
      "v_accvgpr_write_b32 a198, v94\n"
      "v_mfma_f32_16x16x32_bf16 v[144:147], a[56:59], a[228:231], v[144:147]\n"
      "ds_read_b64_tr_b16 a[82:83], v33 offset:896\n"
      "v_accvgpr_write_b32 a199, v95\n"
      "v_mfma_f32_4x4x4_16b_bf16 v[92:95], a[238:239], a[156:157], 0\n"
      "ds_read_b64_tr_b16 a[144:145], v34 offset:0\n"
      "v_mfma_f32_4x4x4_16b_bf16 v[96:99], a[238:239], a[158:159], 0\n"
      "ds_read_b64_tr_b16 a[146:147], v34 offset:768\n"
      "v_mfma_f32_4x4x4_16b_bf16 v[92:95], a[240:241], a[160:161], v[92:95]\n"
      "ds_read_b64_tr_b16 a[148:149], v34 offset:6144\n"
      "ds_read_b64_tr_b16 a[242:243], v14 offset:0\n"
      "v_mfma_f32_4x4x4_16b_bf16 v[96:99], a[240:241], a[162:163], v[96:99]\n"
      "ds_read_b64_tr_b16 a[150:151], v34 offset:6912\n"
      "v_mfma_f32_4x4x4_16b_bf16 v[92:95], a[254:255], a[164:165], v[92:95]\n"
      "ds_read_b64_tr_b16 a[152:153], v34 offset:12288\n"
      "v_mfma_f32_4x4x4_16b_bf16 v[96:99], a[254:255], a[166:167], v[96:99]\n"
      "ds_read_b64_tr_b16 a[154:155], v34 offset:13056\n"
      "ds_read_b64_tr_b16 a[244:245], v14 offset:8\n"
      "v_cvt_pk_bf16_f32 v50, v62, v66\n"
      "v_cvt_pk_bf16_f32 v51, v63, v67\n"
      "s_waitcnt lgkmcnt(8)\n"
      "v_mfma_f32_32x32x16_bf16 v[52:67], a[168:171], a[132:135], 0\n"
      "ds_read_b64_tr_b16 a[156:157], v32 offset:3072\n"
      "ds_read_b64_tr_b16 a[158:159], v32 offset:3840\n"
      "ds_read_b128 a[216:219], v8 offset:0\n"
      "v_cvt_pk_bf16_f32 v204, v68, v72\n"
      "v_cvt_pk_bf16_f32 v242, v86, v90\n"
      "v_cvt_pk_bf16_f32 v205, v69, v73\n"
      "v_mfma_f32_32x32x16_bf16 v[52:67], a[172:175], a[136:139], v[52:67]\n"
      "ds_read_b64_tr_b16 a[160:161], v32 offset:9216\n"
      "ds_read_b64_tr_b16 a[162:163], v32 offset:9984\n"
      "v_cvt_pk_bf16_f32 v206, v70, v74\n"
      "v_cvt_pk_bf16_f32 v207, v71, v75\n"
      "v_cvt_pk_bf16_f32 v92, v92, v96\n"
      "v_mov_b32 v17, v30\n"
      "v_mfma_f32_32x32x16_bf16 v[52:67], a[176:179], a[140:143], v[52:67]\n"
      "ds_read_b64_tr_b16 a[164:165], v32 offset:15360\n"
      "ds_read_b64_tr_b16 a[166:167], v32 offset:16128\n"
      "ds_read_b128 a[220:223], v8 offset:3072\n"
      "v_add_u32 v32, 59904, v32\n"
      "v_cvt_pk_bf16_f32 v120, v76, v80\n"
      "s_nop 0\n"
      "s_waitcnt lgkmcnt(5)\n"
      "v_mfma_f32_4x4x4_16b_bf16 v[20:23], a[232:233], a[242:243], 0\n"
      "v_cvt_pk_bf16_f32 v121, v77, v81\n"
      "v_cvt_pk_bf16_f32 v93, v93, v97\n"
      "v_mfma_f32_4x4x4_16b_bf16 v[24:27], a[232:233], a[244:245], 0\n"
      "v_cvt_pk_bf16_f32 v243, v87, v91\n"
      "v_cvt_pk_bf16_f32 v122, v78, v82\n"
      "v_add_u32 v33, 59904, v33\n"
      "v_mfma_f32_4x4x4_16b_bf16 v[84:87], a[238:239], a[144:145], 0\n"
      "ds_read_b64_tr_b16 a[132:133], v34 offset:3072\n"
      "v_mfma_f32_4x4x4_16b_bf16 v[88:91], a[238:239], a[146:147], 0\n"
      "ds_read_b64_tr_b16 a[134:135], v34 offset:3840\n"
      "ds_read_b64_tr_b16 a[242:243], v14 offset:1024\n"
      "v_mfma_f32_4x4x4_16b_bf16 v[84:87], a[240:241], a[148:149], v[84:87]\n"
      "ds_read_b64_tr_b16 a[136:137], v34 offset:9216\n"
      "v_mfma_f32_4x4x4_16b_bf16 v[88:91], a[240:241], a[150:151], v[88:91]\n"
      "ds_read_b64_tr_b16 a[138:139], v34 offset:9984\n"
      "v_mfma_f32_4x4x4_16b_bf16 v[84:87], a[254:255], a[152:153], v[84:87]\n"
      "ds_read_b64_tr_b16 a[140:141], v34 offset:15360\n"
      "ds_read_b64_tr_b16 a[244:245], v14 offset:1032\n"
      "v_mfma_f32_4x4x4_16b_bf16 v[88:91], a[254:255], a[154:155], v[88:91]\n"
      "ds_read_b64_tr_b16 a[142:143], v34 offset:16128\n"
      "v_add_u32 v34, 59904, v34\n"
      "v_cvt_pk_bf16_f32 v20, v20, v24\n"
      "v_cvt_pk_bf16_f32 v21, v21, v25\n"
      "v_cvt_pk_bf16_f32 v22, v22, v26\n"
      "v_cvt_pk_bf16_f32 v123, v79, v83\n"
      "v_cvt_pk_bf16_f32 v23, v23, v27\n"
      "s_barrier\n"
      "v_mfma_f32_32x32x16_bf16 v[68:83], a[168:171], a[156:159], 0\n"
      "ds_read_b64_tr_b16 a[144:145], v32 offset:0\n"
      "ds_read_b64_tr_b16 a[146:147], v32 offset:768\n"
      "ds_write_addtid_b32 v20 offset:24832\n"
      "v_cvt_pk_bf16_f32 v94, v94, v98\n"
      "v_cvt_pk_bf16_f32 v172, v52, v56\n"
      "s_waitcnt lgkmcnt(3)\n"
      "v_mfma_f32_32x32x16_bf16 v[68:83], a[172:175], a[160:163], v[68:83]\n"
      "ds_read_b64_tr_b16 a[148:149], v32 offset:6144\n"
      "ds_read_b64_tr_b16 a[150:151], v32 offset:6912\n"
      "ds_write_addtid_b32 v21 offset:28416\n"
      "v_cvt_pk_bf16_f32 v95, v95, v99\n"
      "v_cvt_pk_bf16_f32 v244, v84, v88\n"
      "v_cvt_pk_bf16_f32 v173, v53, v57\n"
      "v_mfma_f32_32x32x16_bf16 v[68:83], a[176:179], a[164:167], v[68:83]\n"
      "ds_read_b64_tr_b16 a[152:153], v32 offset:12288\n"
      "ds_read_b64_tr_b16 a[154:155], v32 offset:13056\n"
      "ds_write_addtid_b32 v22 offset:32000\n"
      "ds_write_addtid_b32 v23 offset:35584\n"
      "v_cvt_pk_bf16_f32 v174, v54, v58\n"
      "v_accvgpr_write_b32 a200, v92\n"
      "v_mfma_f32_16x16x32_bf16 v[124:127], a[60:63], a[216:219], v[124:127]\n"
      "ds_read_b64_tr_b16 a[84:85], v33 offset:0\n"
      "ds_read_b64_tr_b16 a[86:87], v33 offset:768\n"
      "v_mfma_f32_16x16x32_bf16 v[128:131], a[64:67], a[216:219], v[128:131]\n"
      "ds_read_b64_tr_b16 a[88:89], v33 offset:64\n"
      "ds_read_b64_tr_b16 a[90:91], v33 offset:832\n"
      "v_mfma_f32_16x16x32_bf16 v[132:135], a[68:71], a[216:219], v[132:135]\n"
      "v_cvt_pk_bf16_f32 v175, v55, v59\n"
      "v_cvt_pk_bf16_f32 v245, v85, v89\n"
      "v_mfma_f32_16x16x32_bf16 v[136:139], a[60:63], a[220:223], v[136:139]\n"
      "ds_read_b64_tr_b16 a[92:93], v33 offset:128\n"
      "v_cvt_pk_bf16_f32 v36, v60, v64\n"
      "v_mfma_f32_16x16x32_bf16 v[140:143], a[64:67], a[220:223], v[140:143]\n"
      "v_accvgpr_write_b32 a201, v93\n"
      "v_cvt_pk_bf16_f32 v37, v61, v65\n"
      "v_mfma_f32_16x16x32_bf16 v[144:147], a[68:71], a[220:223], v[144:147]\n"
      "ds_read_b64_tr_b16 a[94:95], v33 offset:896\n"
      "v_cvt_pk_bf16_f32 v246, v86, v90\n"
      "v_mfma_f32_4x4x4_16b_bf16 v[20:23], a[232:233], a[242:243], 0\n"
      "v_accvgpr_write_b32 a202, v94\n"
      "v_mfma_f32_4x4x4_16b_bf16 v[24:27], a[232:233], a[244:245], 0\n"
      "v_accvgpr_write_b32 a203, v95\n"
      "v_cvt_pk_bf16_f32 v38, v62, v66\n"
      "v_cvt_pk_bf16_f32 v39, v63, v67\n"
      "v_mfma_f32_4x4x4_16b_bf16 v[92:95], a[238:239], a[132:133], 0\n"
      "ds_read_b64_tr_b16 a[156:157], v34 offset:0\n"
      "v_mfma_f32_4x4x4_16b_bf16 v[96:99], a[238:239], a[134:135], 0\n"
      "ds_read_b64_tr_b16 a[158:159], v34 offset:768\n"
      "v_mfma_f32_4x4x4_16b_bf16 v[92:95], a[240:241], a[136:137], v[92:95]\n"
      "ds_read_b64_tr_b16 a[160:161], v34 offset:6144\n"
      "ds_read_b64_tr_b16 a[242:243], v14 offset:2048\n"
      "v_mfma_f32_4x4x4_16b_bf16 v[96:99], a[240:241], a[138:139], v[96:99]\n"
      "ds_read_b64_tr_b16 a[162:163], v34 offset:6912\n"
      "v_mfma_f32_4x4x4_16b_bf16 v[92:95], a[254:255], a[140:141], v[92:95]\n"
      "ds_read_b64_tr_b16 a[164:165], v34 offset:12288\n"
      "v_mfma_f32_4x4x4_16b_bf16 v[96:99], a[254:255], a[142:143], v[96:99]\n"
      "ds_read_b64_tr_b16 a[166:167], v34 offset:13056\n"
      "ds_read_b64_tr_b16 a[244:245], v14 offset:2056\n"
      "v_cvt_pk_bf16_f32 v20, v20, v24\n"
      "v_cvt_pk_bf16_f32 v21, v21, v25\n"
      "v_cvt_pk_bf16_f32 v22, v22, v26\n"
      "v_cvt_pk_bf16_f32 v23, v23, v27\n"
      "s_nop 0\n"
      "s_waitcnt lgkmcnt(8)\n"
      "v_mfma_f32_32x32x16_bf16 v[52:67], a[168:171], a[144:147], 0\n"
      "ds_read_b64_tr_b16 a[132:133], v32 offset:3072\n"
      "ds_read_b64_tr_b16 a[134:135], v32 offset:3840\n"
      "ds_write_addtid_b32 v20 offset:25344\n"
      "ds_read_b128 a[224:227], v9 offset:0\n"
      "v_cvt_pk_bf16_f32 v208, v68, v72\n"
      "v_cvt_pk_bf16_f32 v209, v69, v73\n"
      "v_cvt_pk_bf16_f32 v247, v87, v91\n"
      "v_add_u32 v33, 76800, v33\n"
      "v_mfma_f32_32x32x16_bf16 v[52:67], a[172:175], a[148:151], v[52:67]\n"
      "ds_read_b64_tr_b16 a[136:137], v32 offset:9216\n"
      "ds_read_b64_tr_b16 a[138:139], v32 offset:9984\n"
      "ds_write_addtid_b32 v21 offset:28928\n"
      "ds_write_addtid_b32 v22 offset:32512\n"
      "ds_write_addtid_b32 v44 offset:20480\n"
      "ds_write_addtid_b32 v45 offset:20992\n"
      "ds_write_addtid_b32 v46 offset:21504\n"
      "ds_write_addtid_b32 v47 offset:22016\n"
      "v_mfma_f32_32x32x16_bf16 v[52:67], a[176:179], a[152:155], v[52:67]\n"
      "ds_read_b64_tr_b16 a[140:141], v32 offset:15360\n"
      "ds_read_b64_tr_b16 a[142:143], v32 offset:16128\n"
      "ds_write_addtid_b32 v23 offset:36096\n"
      "ds_write_addtid_b32 v48 offset:22528\n"
      "ds_write_addtid_b32 v49 offset:23040\n"
      "ds_write_addtid_b32 v50 offset:23552\n"
      "ds_write_addtid_b32 v51 offset:24064\n"
      "v_add_u32 v32, 76800, v32\n"
      "s_nop 0\n"
      "s_waitcnt lgkmcnt(14)\n"
      "v_mfma_f32_4x4x4_16b_bf16 v[20:23], a[232:233], a[242:243], 0\n"
      "ds_read_b128 a[228:231], v9 offset:3072\n"
      "v_mfma_f32_4x4x4_16b_bf16 v[24:27], a[232:233], a[244:245], 0\n"
      "v_cvt_pk_bf16_f32 v210, v70, v74\n"
      "v_cvt_pk_bf16_f32 v211, v71, v75\n"
      "v_cvt_pk_bf16_f32 v40, v76, v80\n"
      "v_cvt_pk_bf16_f32 v41, v77, v81\n"
      "v_mfma_f32_4x4x4_16b_bf16 v[84:87], a[238:239], a[156:157], 0\n"
      "ds_read_b64_tr_b16 a[144:145], v34 offset:3072\n"
      "v_mfma_f32_4x4x4_16b_bf16 v[88:91], a[238:239], a[158:159], 0\n"
      "ds_read_b64_tr_b16 a[146:147], v34 offset:3840\n"
      "ds_read_b64_tr_b16 a[242:243], v14 offset:3072\n"
      "v_mfma_f32_4x4x4_16b_bf16 v[84:87], a[240:241], a[160:161], v[84:87]\n"
      "ds_read_b64_tr_b16 a[148:149], v34 offset:9216\n"
      "v_mfma_f32_4x4x4_16b_bf16 v[88:91], a[240:241], a[162:163], v[88:91]\n"
      "ds_read_b64_tr_b16 a[150:151], v34 offset:9984\n"
      "v_mfma_f32_4x4x4_16b_bf16 v[84:87], a[254:255], a[164:165], v[84:87]\n"
      "ds_read_b64_tr_b16 a[152:153], v34 offset:15360\n"
      "ds_read_b64_tr_b16 a[244:245], v14 offset:3080\n"
      "v_mfma_f32_4x4x4_16b_bf16 v[88:91], a[254:255], a[166:167], v[88:91]\n"
      "ds_read_b64_tr_b16 a[154:155], v34 offset:16128\n"
      "v_add_u32 v34, 76800, v34\n"
      "v_cvt_pk_bf16_f32 v20, v20, v24\n"
      "v_cvt_pk_bf16_f32 v21, v21, v25\n"
      "v_cvt_pk_bf16_f32 v22, v22, v26\n"
      "v_cvt_pk_bf16_f32 v42, v78, v82\n"
      "v_cvt_pk_bf16_f32 v23, v23, v27\n"
      "v_cvt_pk_bf16_f32 v43, v79, v83\n"
      "s_waitcnt lgkmcnt(5)\n"
      "s_barrier\n"
      "v_mfma_f32_32x32x16_bf16 v[68:83], a[168:171], a[132:135], 0\n"
      "ds_read_b64_tr_b16 a[156:157], v32 offset:0\n"
      "ds_read_b64_tr_b16 a[158:159], v32 offset:768\n"
      "ds_write_addtid_b32 v20 offset:25856\n"
      "v_cvt_pk_bf16_f32 v92, v92, v96\n"
      "v_cvt_pk_bf16_f32 v176, v52, v56\n"
      "v_mov_b32_e64 v18, v31\n"
      "v_mfma_f32_32x32x16_bf16 v[68:83], a[172:175], a[136:139], v[68:83]\n"
      "ds_read_b64_tr_b16 a[160:161], v32 offset:6144\n"
      "ds_read_b64_tr_b16 a[162:163], v32 offset:6912\n"
      "ds_write_addtid_b32 v21 offset:29440\n"
      "v_cvt_pk_bf16_f32 v93, v93, v97\n"
      "v_cvt_pk_bf16_f32 v177, v53, v57\n"
      "v_cvt_pk_bf16_f32 v248, v84, v88\n"
      "v_mfma_f32_32x32x16_bf16 v[68:83], a[176:179], a[140:143], v[68:83]\n"
      "ds_read_b64_tr_b16 a[164:165], v32 offset:12288\n"
      "ds_read_b64_tr_b16 a[166:167], v32 offset:13056\n"
      "ds_write_addtid_b32 v22 offset:33024\n"
      "ds_write_addtid_b32 v23 offset:36608\n"
      "v_cvt_pk_bf16_f32 v94, v94, v98\n"
      "v_cvt_pk_bf16_f32 v178, v54, v58\n"
      "v_mfma_f32_16x16x32_bf16 v[124:127], a[72:75], a[224:227], v[124:127]\n"
      "ds_read_b64_tr_b16 a[96:97], v33 offset:0\n"
      "ds_read_b64_tr_b16 a[98:99], v33 offset:768\n"
      "v_mfma_f32_16x16x32_bf16 v[128:131], a[76:79], a[224:227], v[128:131]\n"
      "ds_read2st64_b32 a[124:125], v35 offset0:80 offset1:82\n"
      "ds_read2st64_b32 a[128:129], v35 offset0:88 offset1:90\n"
      "v_mfma_f32_16x16x32_bf16 v[132:135], a[80:83], a[224:227], v[132:135]\n"
      "ds_read_b64_tr_b16 a[100:101], v33 offset:64\n"
      "v_cvt_pk_bf16_f32 v95, v95, v99\n"
      "v_mfma_f32_16x16x32_bf16 v[136:139], a[72:75], a[228:231], v[136:139]\n"
      "v_cvt_pk_bf16_f32 v179, v55, v59\n"
      "v_accvgpr_write_b32 a204, v92\n"
      "v_mfma_f32_16x16x32_bf16 v[140:143], a[76:79], a[228:231], v[140:143]\n"
      "v_cvt_pk_bf16_f32 v44, v60, v64\n"
      "v_accvgpr_write_b32 a205, v93\n"
      "v_mfma_f32_16x16x32_bf16 v[144:147], a[80:83], a[228:231], v[144:147]\n"
      "ds_read_b64_tr_b16 a[102:103], v33 offset:832\n"
      "v_cvt_pk_bf16_f32 v45, v61, v65\n"
      "s_nop 0\n"
      "s_waitcnt lgkmcnt(3)\n"
      "v_mfma_f32_4x4x4_16b_bf16 v[20:23], a[232:233], a[242:243], 0\n"
      "v_cvt_pk_bf16_f32 v249, v85, v89\n"
      "v_accvgpr_write_b32 a206, v94\n"
      "v_mfma_f32_4x4x4_16b_bf16 v[24:27], a[232:233], a[244:245], 0\n"
      "v_accvgpr_write_b32 a207, v95\n"
      "v_cvt_pk_bf16_f32 v46, v62, v66\n"
      "v_cvt_pk_bf16_f32 v47, v63, v67\n"
      "v_mfma_f32_4x4x4_16b_bf16 v[92:95], a[238:239], a[144:145], 0\n"
      "ds_read_b64_tr_b16 a[132:133], v34 offset:0\n"
      "v_mfma_f32_4x4x4_16b_bf16 v[96:99], a[238:239], a[146:147], 0\n"
      "ds_read_b64_tr_b16 a[134:135], v34 offset:768\n"
      "v_mfma_f32_4x4x4_16b_bf16 v[92:95], a[240:241], a[148:149], v[92:95]\n"
      "ds_read_b64_tr_b16 a[136:137], v34 offset:6144\n"
      "ds_read_b64_tr_b16 a[242:243], v14 offset:4096\n"
      "v_mfma_f32_4x4x4_16b_bf16 v[96:99], a[240:241], a[150:151], v[96:99]\n"
      "ds_read_b64_tr_b16 a[138:139], v34 offset:6912\n"
      "v_mfma_f32_4x4x4_16b_bf16 v[92:95], a[254:255], a[152:153], v[92:95]\n"
      "ds_read_b64_tr_b16 a[140:141], v34 offset:12288\n"
      "v_mfma_f32_4x4x4_16b_bf16 v[96:99], a[254:255], a[154:155], v[96:99]\n"
      "ds_read_b64_tr_b16 a[142:143], v34 offset:13056\n"
      "ds_read_b64_tr_b16 a[244:245], v14 offset:4104\n"
      "v_cvt_pk_bf16_f32 v20, v20, v24\n"
      "v_cvt_pk_bf16_f32 v21, v21, v25\n"
      "v_cvt_pk_bf16_f32 v22, v22, v26\n"
      "v_cvt_pk_bf16_f32 v23, v23, v27\n"
      "v_mfma_f32_32x32x16_bf16 v[52:67], a[168:171], a[156:159], 0\n"
      "ds_read_b64_tr_b16 a[144:145], v32 offset:3072\n"
      "ds_read_b64_tr_b16 a[146:147], v32 offset:3840\n"
      "ds_write_addtid_b32 v20 offset:26368\n"
      "v_cvt_pk_bf16_f32 v212, v68, v72\n"
      "v_cvt_pk_bf16_f32 v250, v86, v90\n"
      "v_cvt_pk_bf16_f32 v213, v69, v73\n"
      "v_mfma_f32_32x32x16_bf16 v[52:67], a[172:175], a[160:163], v[52:67]\n"
      "ds_read_b64_tr_b16 a[148:149], v32 offset:9216\n"
      "ds_read_b64_tr_b16 a[150:151], v32 offset:9984\n"
      "ds_write_addtid_b32 v21 offset:29952\n"
      "v_cvt_pk_bf16_f32 v214, v70, v74\n"
      "v_cvt_pk_bf16_f32 v215, v71, v75\n"
      "v_cvt_pk_bf16_f32 v251, v87, v91\n"
      "v_mfma_f32_32x32x16_bf16 v[52:67], a[176:179], a[164:167], v[52:67]\n"
      "ds_read_b64_tr_b16 a[152:153], v32 offset:15360\n"
      "ds_read_b64_tr_b16 a[154:155], v32 offset:16128\n"
      "ds_write_addtid_b32 v22 offset:33536\n"
      "ds_write_addtid_b32 v23 offset:37120\n"
      "v_cvt_pk_bf16_f32 v48, v76, v80\n"
      "v_cvt_pk_bf16_f32 v49, v77, v81\n"
      "s_nop 0\n"
      "s_waitcnt lgkmcnt(7)\n"
      "v_mfma_f32_4x4x4_16b_bf16 v[20:23], a[232:233], a[242:243], 0\n"
      "ds_read_b64_tr_b16 a[104:105], v33 offset:128\n"
      "v_mfma_f32_4x4x4_16b_bf16 v[24:27], a[232:233], a[244:245], 0\n"
      "ds_read2st64_b32 a[126:127], v35 offset0:84 offset1:86\n"
      "ds_read2st64_b32 a[130:131], v35 offset0:92 offset1:94\n"
      "v_cvt_pk_bf16_f32 v50, v78, v82\n"
      "v_mfma_f32_4x4x4_16b_bf16 v[84:87], a[238:239], a[132:133], 0\n"
      "ds_read_b64_tr_b16 a[156:157], v34 offset:3072\n"
      "v_mfma_f32_4x4x4_16b_bf16 v[88:91], a[238:239], a[134:135], 0\n"
      "ds_read_b64_tr_b16 a[158:159], v34 offset:3840\n"
      "ds_read_b128 a[216:219], v10 offset:0\n"
      "v_mfma_f32_4x4x4_16b_bf16 v[84:87], a[240:241], a[136:137], v[84:87]\n"
      "ds_read_b64_tr_b16 a[160:161], v34 offset:9216\n"
      "v_mfma_f32_4x4x4_16b_bf16 v[88:91], a[240:241], a[138:139], v[88:91]\n"
      "ds_read_b64_tr_b16 a[162:163], v34 offset:9984\n"
      "v_mfma_f32_4x4x4_16b_bf16 v[84:87], a[254:255], a[140:141], v[84:87]\n"
      "ds_read_b64_tr_b16 a[164:165], v34 offset:15360\n"
      "ds_read_b64_tr_b16 a[106:107], v33 offset:896\n"
      "v_mfma_f32_4x4x4_16b_bf16 v[88:91], a[254:255], a[142:143], v[88:91]\n"
      "ds_read_b64_tr_b16 a[166:167], v34 offset:16128\n"
      "v_cvt_pk_bf16_f32 v20, v20, v24\n"
      "v_cvt_pk_bf16_f32 v21, v21, v25\n"
      "v_cvt_pk_bf16_f32 v22, v22, v26\n"
      "v_cvt_pk_bf16_f32 v23, v23, v27\n"
      "v_cvt_pk_bf16_f32 v51, v79, v83\n"
      "v_mfma_f32_32x32x16_bf16 v[68:83], a[168:171], a[144:147], 0\n"
      "ds_write_addtid_b32 v20 offset:26880\n"
      "ds_read_b128 a[220:223], v10 offset:3072\n"
      "v_cvt_pk_bf16_f32 v92, v92, v96\n"
      "v_cvt_pk_bf16_f32 v93, v93, v97\n"
      "v_cvt_pk_bf16_f32 v180, v52, v56\n"
      "s_nop 0\n"
      "s_waitcnt lgkmcnt(6)\n"
      "v_mfma_f32_32x32x16_bf16 v[68:83], a[172:175], a[148:151], v[68:83]\n"
      "ds_write_addtid_b32 v21 offset:30464\n"
      "ds_read_b64_tr_b16 a[242:243], v14 offset:512\n"
      "ds_write_addtid_b32 v22 offset:34048\n"
      "v_cvt_pk_bf16_f32 v94, v94, v98\n"
      "v_cvt_pk_bf16_f32 v252, v84, v88\n"
      "v_cvt_pk_bf16_f32 v95, v95, v99\n"
      "v_mfma_f32_32x32x16_bf16 v[68:83], a[176:179], a[152:155], v[68:83]\n"
      "ds_write_addtid_b32 v23 offset:37632\n"
      "ds_read_b128 a[224:227], v11 offset:0\n"
      "v_accvgpr_write_b32 a208, v92\n"
      "v_cvt_pk_bf16_f32 v181, v53, v57\n"
      "v_accvgpr_write_b32 a209, v93\n"
      "v_cvt_pk_bf16_f32 v182, v54, v58\n"
      "v_mfma_f32_16x16x32_bf16 v[124:127], a[84:87], a[216:219], v[124:127]\n"
      "ds_read_b64_tr_b16 a[244:245], v14 offset:520\n"
      "v_cvt_pk_bf16_f32 v253, v85, v89\n"
      "v_mfma_f32_16x16x32_bf16 v[128:131], a[88:91], a[216:219], v[128:131]\n"
      "ds_read_b128 a[228:231], v11 offset:3072\n"
      "v_accvgpr_write_b32 a210, v94\n"
      "v_mfma_f32_16x16x32_bf16 v[132:135], a[92:95], a[216:219], v[132:135]\n"
      "v_accvgpr_write_b32 a211, v95\n"
      "v_cvt_pk_bf16_f32 v183, v55, v59\n"
      "v_mfma_f32_4x4x4_16b_bf16 v[92:95], a[238:239], a[156:157], 0\n"
      "v_cvt_pk_bf16_f32 v28, v60, v64\n"
      "v_mfma_f32_4x4x4_16b_bf16 v[96:99], a[238:239], a[158:159], 0\n"
      "v_cvt_pk_bf16_f32 v254, v86, v90\n"
      "v_mfma_f32_4x4x4_16b_bf16 v[92:95], a[240:241], a[160:161], v[92:95]\n"
      "s_nop 0\n"
      "s_waitcnt lgkmcnt(2)\n"
      "v_mfma_f32_4x4x4_16b_bf16 v[96:99], a[240:241], a[162:163], v[96:99]\n"
      "v_cvt_pk_bf16_f32 v29, v61, v65\n"
      "v_mfma_f32_4x4x4_16b_bf16 v[92:95], a[254:255], a[164:165], v[92:95]\n"
      "v_cvt_pk_bf16_f32 v30, v62, v66\n"
      "v_mfma_f32_4x4x4_16b_bf16 v[96:99], a[254:255], a[166:167], v[96:99]\n"
      "v_cvt_pk_bf16_f32 v255, v87, v91\n"
      "v_cvt_pk_bf16_f32 v31, v63, v67\n"
      "s_waitcnt lgkmcnt(0)\n"
      "s_barrier\n"
      "v_mfma_f32_16x16x32_bf16 v[136:139], a[84:87], a[220:223], v[136:139]\n"
      "ds_read_b128 a[216:219], v0 offset:0\n"
      "v_cvt_pk_bf16_f32 v216, v68, v72\n"
      "v_mfma_f32_16x16x32_bf16 v[140:143], a[88:91], a[220:223], v[140:143]\n"
      "v_cvt_pk_bf16_f32 v217, v69, v73\n"
      "v_cvt_pk_bf16_f32 v218, v70, v74\n"
      "v_mfma_f32_16x16x32_bf16 v[144:147], a[92:95], a[220:223], v[144:147]\n"
      "ds_read_b128 a[220:223], v0 offset:32\n"
      "v_cvt_pk_bf16_f32 v219, v71, v75\n"
      "v_mfma_f32_4x4x4_16b_bf16 v[20:23], a[232:233], a[242:243], 0\n"
      "v_cvt_pk_bf16_f32 v68, v76, v80\n"
      "v_mfma_f32_4x4x4_16b_bf16 v[24:27], a[232:233], a[244:245], 0\n"
      "v_cvt_pk_bf16_f32 v69, v77, v81\n"
      "v_cvt_pk_bf16_f32 v70, v78, v82\n"
      "v_cvt_pk_bf16_f32 v71, v79, v83\n"
      "v_mfma_f32_16x16x32_bf16 v[124:127], a[96:99], a[224:227], v[124:127]\n"
      "ds_read_b64_tr_b16 a[242:243], v14 offset:1536\n"
      "ds_write_addtid_b32 v68 offset:22528\n"
      "v_mfma_f32_16x16x32_bf16 v[128:131], a[100:103], a[224:227], v[128:131]\n"
      "ds_read_b64_tr_b16 a[244:245], v14 offset:1544\n"
      "ds_write_addtid_b32 v69 offset:23040\n"
      "v_mfma_f32_16x16x32_bf16 v[132:135], a[104:107], a[224:227], v[132:135]\n"
      "ds_write_addtid_b32 v70 offset:23552\n"
      "ds_write_addtid_b32 v71 offset:24064\n"
      "v_mfma_f32_16x16x32_bf16 v[136:139], a[96:99], a[228:231], v[136:139]\n"
      "ds_read_b128 a[224:227], v0 offset:64\n"
      "v_cvt_pk_bf16_f32 v20, v20, v24\n"
      "v_mfma_f32_16x16x32_bf16 v[140:143], a[100:103], a[228:231], v[140:143]\n"
      "ds_write_addtid_b32 v20 offset:39168\n"
      "v_cvt_pk_bf16_f32 v21, v21, v25\n"
      "v_cvt_pk_bf16_f32 v22, v22, v26\n"
      "v_cvt_pk_bf16_f32 v23, v23, v27\n"
      "v_mfma_f32_16x16x32_bf16 v[144:147], a[104:107], a[228:231], v[144:147]\n"
      "ds_write_addtid_b32 v21 offset:42752\n"
      "ds_write_addtid_b32 v22 offset:46336\n"
      "ds_write_addtid_b32 v23 offset:49920\n"
      "s_nop 0\n"
      "s_waitcnt lgkmcnt(6)\n"
      "v_mfma_f32_4x4x4_16b_bf16 v[20:23], a[232:233], a[242:243], 0\n"
      "v_cvt_pk_bf16_f32 v92, v92, v96\n"
      "v_mfma_f32_4x4x4_16b_bf16 v[24:27], a[232:233], a[244:245], 0\n"
      "v_cvt_pk_bf16_f32 v93, v93, v97\n"
      "v_cvt_pk_bf16_f32 v94, v94, v98\n"
      "v_cvt_pk_bf16_f32 v95, v95, v99\n"
      "v_mfma_f32_32x32x16_bf16 v[52:67], a[216:219], v[148:151], 0\n"
      "ds_read_b128 a[228:231], v1 offset:0\n"
      "v_cvt_pk_bf16_f32 v20, v20, v24\n"
      "v_cvt_pk_bf16_f32 v21, v21, v25\n"
      "v_accvgpr_write_b32 a212, v92\n"
      "v_cvt_pk_bf16_f32 v22, v22, v26\n"
      "v_accvgpr_write_b32 a213, v93\n"
      "v_mfma_f32_32x32x16_bf16 v[52:67], a[220:223], v[152:155], v[52:67]\n"
      "ds_write_addtid_b32 v20 offset:39680\n"
      "ds_read_b64_tr_b16 a[242:243], v14 offset:2560\n"
      "ds_write_addtid_b32 v21 offset:43264\n"
      "v_accvgpr_write_b32 a214, v94\n"
      "v_accvgpr_write_b32 a215, v95\n"
      "v_cvt_pk_bf16_f32 v23, v23, v27\n"
      "v_mfma_f32_32x32x16_bf16 v[68:83], a[216:219], a[108:111], 0\n"
      "ds_read_b64_tr_b16 a[244:245], v14 offset:2568\n"
      "ds_write_addtid_b32 v22 offset:46848\n"
      "ds_write_addtid_b32 v23 offset:50432\n"
      "v_mfma_f32_32x32x16_bf16 v[68:83], a[220:223], a[112:115], v[68:83]\n"
      "ds_write_addtid_b32 v36 offset:0\n"
      "ds_write_addtid_b32 v100 offset:6144\n"
      "v_mfma_f32_32x32x16_bf16 v[84:99], a[216:219], v[220:223], 0\n"
      "ds_read_b128 a[216:219], v1 offset:32\n"
      "ds_write_addtid_b32 v37 offset:512\n"
      "v_mfma_f32_32x32x16_bf16 v[84:99], a[220:223], v[224:227], v[84:99]\n"
      "ds_write_addtid_b32 v38 offset:1024\n"
      "ds_write_addtid_b32 v101 offset:6656\n"
      "ds_write_addtid_b32 v39 offset:1536\n"
      "s_nop 0\n"
      "s_waitcnt lgkmcnt(5)\n"
      "v_mfma_f32_32x32x16_bf16 v[52:67], a[224:227], v[156:159], v[52:67]\n"
      "ds_read_b128 a[220:223], v1 offset:64\n"
      "ds_write_addtid_b32 v44 offset:2048\n"
      "v_mfma_f32_32x32x16_bf16 v[68:83], a[224:227], a[116:119], v[68:83]\n"
      "ds_write_addtid_b32 v45 offset:2560\n"
      "ds_write_addtid_b32 v102 offset:7168\n"
      "v_mfma_f32_32x32x16_bf16 v[84:99], a[224:227], v[228:231], v[84:99]\n"
      "ds_write_addtid_b32 v46 offset:3072\n"
      "ds_write_addtid_b32 v47 offset:3584\n"
      "ds_write_addtid_b32 v103 offset:7680\n"
      "v_mfma_f32_4x4x4_16b_bf16 v[20:23], a[232:233], a[242:243], 0\n"
      "v_mfma_f32_4x4x4_16b_bf16 v[24:27], a[232:233], a[244:245], 0\n"
      "ds_write_addtid_b32 v28 offset:4096\n"
      "v_mfma_f32_32x32x16_bf16 v[52:67], a[228:231], v[160:163], v[52:67]\n"
      "ds_read_b128 a[224:227], v2 offset:0\n"
      "ds_read_b64_tr_b16 a[242:243], v14 offset:3584\n"
      "ds_read_b64_tr_b16 a[244:245], v14 offset:3592\n"
      "v_cvt_pk_bf16_f32 v20, v20, v24\n"
      "v_cvt_pk_bf16_f32 v21, v21, v25\n"
      "v_cvt_pk_bf16_f32 v22, v22, v26\n"
      "v_mfma_f32_32x32x16_bf16 v[68:83], a[228:231], a[120:123], v[68:83]\n"
      "ds_write_addtid_b32 v29 offset:4608\n"
      "ds_write_addtid_b32 v30 offset:5120\n"
      "ds_write_addtid_b32 v31 offset:5632\n"
      "v_cvt_pk_bf16_f32 v23, v23, v27\n"
      "v_mfma_f32_32x32x16_bf16 v[84:99], a[228:231], v[232:235], v[84:99]\n"
      "ds_write_addtid_b32 v20 offset:40192\n"
      "ds_write_addtid_b32 v21 offset:43776\n"
      "ds_write_addtid_b32 v22 offset:47360\n"
      "s_waitcnt lgkmcnt(9)\n"
      "v_mfma_f32_32x32x16_bf16 v[52:67], a[216:219], v[164:167], v[52:67]\n"
      "ds_read_b128 a[228:231], v2 offset:32\n"
      "ds_write_addtid_b32 v23 offset:50944\n"
      "ds_write_addtid_b32 v104 offset:8192\n"
      "s_waitcnt lgkmcnt(6)\n"
      "s_nop 0\n"
      "s_barrier\n"
      "v_mfma_f32_32x32x16_bf16 v[68:83], a[216:219], a[124:127], v[68:83]\n"
      "ds_write_addtid_b32 v105 offset:8704\n"
      "ds_write_addtid_b32 v106 offset:9216\n"
      "ds_write_addtid_b32 v107 offset:9728\n"
      "v_mfma_f32_32x32x16_bf16 v[84:99], a[216:219], v[236:239], v[84:99]\n"
      "ds_write_addtid_b32 v108 offset:10240\n"
      "ds_write_addtid_b32 v109 offset:10752\n"
      "ds_read2st64_b32 a[132:133], v35 offset0:0 offset1:2\n"
      "v_mfma_f32_4x4x4_16b_bf16 v[20:23], a[232:233], a[242:243], 0\n"
      "ds_read2st64_b32 a[136:137], v35 offset0:8 offset1:10\n"
      "v_mfma_f32_4x4x4_16b_bf16 v[24:27], a[232:233], a[244:245], 0\n"
      "ds_write_addtid_b32 v110 offset:11264\n"
      "ds_read2st64_b32 a[134:135], v35 offset0:4 offset1:6\n"
      "v_mfma_f32_32x32x16_bf16 v[52:67], a[220:223], v[168:171], v[52:67]\n"
      "ds_read_b128 a[216:219], v2 offset:64\n"
      "ds_write_addtid_b32 v111 offset:11776\n"
      "v_cvt_pk_bf16_f32 v20, v20, v24\n"
      "v_cvt_pk_bf16_f32 v21, v21, v25\n"
      "v_cvt_pk_bf16_f32 v22, v22, v26\n"
      "v_cvt_pk_bf16_f32 v23, v23, v27\n"
      "v_mfma_f32_32x32x16_bf16 v[68:83], a[220:223], a[128:131], v[68:83]\n"
      "ds_write_addtid_b32 v20 offset:40704\n"
      "ds_write_addtid_b32 v21 offset:44288\n"
      "ds_write_addtid_b32 v22 offset:47872\n"
      "v_mfma_f32_32x32x16_bf16 v[84:99], a[220:223], v[240:243], v[84:99]\n"
      "ds_write_addtid_b32 v23 offset:51456\n"
      "ds_write_addtid_b32 v112 offset:12288\n"
      "ds_write_addtid_b32 v113 offset:12800\n"
      "v_mfma_f32_32x32x16_bf16 v[52:67], a[224:227], v[172:175], v[52:67]\n"
      "ds_write_addtid_b32 v114 offset:13312\n"
      "ds_read2st64_b32 a[138:139], v35 offset0:12 offset1:14\n"
      "ds_write_addtid_b32 v115 offset:13824\n"
      "s_waitcnt lgkmcnt(8)\n"
      "v_mfma_f32_32x32x16_bf16 v[68:83], a[224:227], a[132:135], v[68:83]\n"
      "ds_read2st64_b32 a[140:141], v35 offset0:16 offset1:18\n"
      "ds_read2st64_b32 a[142:143], v35 offset0:20 offset1:22\n"
      "v_mfma_f32_32x32x16_bf16 v[84:99], a[224:227], v[244:247], v[84:99]\n"
      "ds_write_addtid_b32 v116 offset:14336\n"
      "ds_write_addtid_b32 v117 offset:14848\n"
      "s_waitcnt lgkmcnt(4)\n"
      "s_barrier\n"
      "v_mfma_f32_32x32x16_bf16 v[52:67], a[228:231], v[176:179], v[52:67]\n"
      "ds_read_b128 a[220:223], v0 offset:14336\n"
      "ds_write_addtid_b32 v118 offset:15360\n"
      "v_mfma_f32_32x32x16_bf16 v[68:83], a[228:231], a[136:139], v[68:83]\n"
      "ds_read_b128 a[224:227], v0 offset:14368\n"
      "ds_write_addtid_b32 v119 offset:15872\n"
      "ds_read2st64_b32 a[144:145], v35 offset0:24 offset1:26\n"
      "v_mfma_f32_32x32x16_bf16 v[84:99], a[228:231], v[248:251], v[84:99]\n"
      "ds_write_addtid_b32 v120 offset:16384\n"
      "ds_read2st64_b32 a[148:149], v35 offset0:32 offset1:34\n"
      "ds_read2st64_b32 a[146:147], v35 offset0:28 offset1:30\n"
      "v_mfma_f32_32x32x16_bf16 v[52:67], a[216:219], v[180:183], v[52:67]\n"
      "ds_read_b128 a[228:231], v0 offset:14400\n"
      "ds_write_addtid_b32 v121 offset:16896\n"
      "ds_read2st64_b32 a[152:153], v35 offset0:40 offset1:42\n"
      "s_waitcnt lgkmcnt(9)\n"
      "v_mfma_f32_32x32x16_bf16 v[68:83], a[216:219], a[140:143], v[68:83]\n"
      "ds_write_addtid_b32 v122 offset:17408\n"
      "ds_write_addtid_b32 v123 offset:17920\n"
      "v_mfma_f32_32x32x16_bf16 v[84:99], a[216:219], v[252:255], v[84:99]\n"
      "ds_write_addtid_b32 v40 offset:18432\n"
      "ds_read2st64_b32 a[150:151], v35 offset0:36 offset1:38\n"
      "v_mfma_f32_32x32x16_bf16 v[52:67], a[220:223], v[184:187], v[52:67]\n"
      "ds_read_b128 a[216:219], v1 offset:14336\n"
      "ds_write_addtid_b32 v41 offset:18944\n"
      "ds_write_addtid_b32 v42 offset:19456\n"
      "s_waitcnt lgkmcnt(7)\n"
      "v_mfma_f32_32x32x16_bf16 v[68:83], a[220:223], a[144:147], v[68:83]\n"
      "ds_write_addtid_b32 v43 offset:19968\n"
      "ds_write_addtid_b32 v48 offset:20480\n"
      "v_mfma_f32_32x32x16_bf16 v[84:99], a[220:223], a[180:183], v[84:99]\n"
      "ds_write_addtid_b32 v49 offset:20992\n"
      "ds_read2st64_b32 a[154:155], v35 offset0:44 offset1:46\n"
      "v_mfma_f32_32x32x16_bf16 v[52:67], a[224:227], v[188:191], v[52:67]\n"
      "ds_read_b128 a[220:223], v1 offset:14368\n"
      "ds_write_addtid_b32 v50 offset:21504\n"
      "ds_write_addtid_b32 v51 offset:22016\n"
      "s_waitcnt lgkmcnt(5)\n"
      "v_mfma_f32_32x32x16_bf16 v[68:83], a[224:227], a[148:151], v[68:83]\n"
      "ds_read2st64_b32 a[156:157], v35 offset0:48 offset1:50\n"
      "v_mfma_f32_32x32x16_bf16 v[84:99], a[224:227], a[184:187], v[84:99]\n"
      "ds_read2st64_b32 a[158:159], v35 offset0:52 offset1:54\n"
      "s_waitcnt lgkmcnt(2)\n"
      "s_barrier\n"
      "v_mfma_f32_32x32x16_bf16 v[52:67], a[228:231], v[192:195], v[52:67]\n"
      "ds_read_b128 a[224:227], v1 offset:14400\n"
      "v_mfma_f32_32x32x16_bf16 v[68:83], a[228:231], a[152:155], v[68:83]\n"
      "ds_read2st64_b32 a[160:161], v35 offset0:56 offset1:58\n"
      "v_mfma_f32_32x32x16_bf16 v[84:99], a[228:231], a[188:191], v[84:99]\n"
      "ds_read2st64_b32 a[162:163], v35 offset0:60 offset1:62\n"
      "v_mfma_f32_32x32x16_bf16 v[52:67], a[216:219], v[196:199], v[52:67]\n"
      "ds_read_b128 a[228:231], v2 offset:14336\n"
      "ds_read2st64_b32 a[164:165], v35 offset0:64 offset1:66\n"
      "s_waitcnt lgkmcnt(4)\n"
      "v_mfma_f32_32x32x16_bf16 v[68:83], a[216:219], a[156:159], v[68:83]\n"
      "ds_read2st64_b32 a[176:177], v35 offset0:88 offset1:90\n"
      "v_mfma_f32_32x32x16_bf16 v[84:99], a[216:219], a[192:195], v[84:99]\n"
      "ds_read2st64_b32 a[168:169], v35 offset0:72 offset1:74\n"
      "ds_read2st64_b32 a[166:167], v35 offset0:68 offset1:70\n"
      "v_mfma_f32_32x32x16_bf16 v[52:67], a[220:223], v[200:203], v[52:67]\n"
      "ds_read_b128 a[216:219], v2 offset:14368\n"
      "ds_read2st64_b32 a[172:173], v35 offset0:80 offset1:82\n"
      "s_waitcnt lgkmcnt(5)\n"
      "v_mfma_f32_32x32x16_bf16 v[68:83], a[220:223], a[160:163], v[68:83]\n"
      "v_mfma_f32_32x32x16_bf16 v[84:99], a[220:223], a[196:199], v[84:99]\n"
      "ds_read_b128 a[220:223], v2 offset:14400\n"
      "ds_read2st64_b32 a[170:171], v35 offset0:76 offset1:78\n"
      "v_mfma_f32_32x32x16_bf16 v[52:67], a[224:227], v[204:207], v[52:67]\n"
      "ds_read2st64_b32 a[178:179], v35 offset0:92 offset1:94\n"
      "s_waitcnt lgkmcnt(4)\n"
      "v_mfma_f32_32x32x16_bf16 v[68:83], a[224:227], a[164:167], v[68:83]\n"
      "ds_read2st64_b32 a[174:175], v35 offset0:84 offset1:86\n"
      "v_mfma_f32_32x32x16_bf16 v[84:99], a[224:227], a[200:203], v[84:99]\n"
      "v_mfma_f32_32x32x16_bf16 v[52:67], a[228:231], v[208:211], v[52:67]\n"
      "s_waitcnt lgkmcnt(0)\n"
      "s_barrier\n"
      "v_mfma_f32_32x32x16_bf16 v[68:83], a[228:231], a[168:171], v[68:83]\n"
      "ds_read_b64_tr_b16 a[242:243], v14 offset:29184\n"
      "ds_read_b64_tr_b16 a[244:245], v14 offset:29192\n"
      "v_mfma_f32_32x32x16_bf16 v[84:99], a[228:231], a[204:207], v[84:99]\n"
      "v_mfma_f32_32x32x16_bf16 v[52:67], a[216:219], v[212:215], v[52:67]\n"
      "ds_read_b128 a[224:227], v3 offset:29184\n"
      "s_waitcnt lgkmcnt(1)\n"
      "v_mfma_f32_4x4x4_16b_bf16 v[20:23], a[232:233], a[242:243], 0\n"
      "v_mfma_f32_4x4x4_16b_bf16 v[24:27], a[232:233], a[244:245], 0\n"
      "v_mfma_f32_32x32x16_bf16 v[52:67], a[220:223], v[216:219], v[52:67]\n"
      "v_mfma_f32_32x32x16_bf16 v[68:83], a[216:219], a[172:175], v[68:83]\n"
      "ds_read_b128 a[228:231], v4 offset:29184\n"
      "v_mfma_f32_32x32x16_bf16 v[68:83], a[220:223], a[176:179], v[68:83]\n"
      "v_mfma_f32_32x32x16_bf16 v[84:99], a[216:219], a[208:211], v[84:99]\n"
      "ds_read_b128 a[216:219], v5 offset:29184\n"
      "v_cvt_pk_bf16_f32 v20, v20, v24\n"
      "v_cvt_pk_bf16_f32 v21, v21, v25\n"
      "v_cvt_pk_bf16_f32 v22, v22, v26\n"
      "v_cvt_pk_bf16_f32 v23, v23, v27\n"
      "v_mfma_f32_32x32x16_bf16 v[84:99], a[220:223], a[212:215], v[84:99]\n"
      "ds_read_b128 a[220:223], v6 offset:29184\n"
      "ds_write_addtid_b32 v20 offset:24832\n"
      "ds_write_addtid_b32 v21 offset:28416\n"
      "s_nop 0\n"
      "s_waitcnt lgkmcnt(4)\n"
      "v_mfma_f32_16x16x32_bf16 v[100:103], a[0:3], a[224:227], 0\n"
      "ds_write_addtid_b32 v22 offset:32000\n"
      "ds_write_addtid_b32 v23 offset:35584\n"
      "v_mfma_f32_16x16x32_bf16 v[104:107], a[4:7], a[224:227], 0\n"
      "ds_write_addtid_b32 v52 offset:0\n"
      "ds_write_addtid_b32 v53 offset:3104\n"
      "v_mfma_f32_16x16x32_bf16 v[108:111], a[8:11], a[224:227], 0\n"
      "ds_read_b128 a[224:227], v7 offset:29184\n"
      "ds_read_b64_tr_b16 a[242:243], v14 offset:30208\n"
      "v_mfma_f32_16x16x32_bf16 v[100:103], a[12:15], a[228:231], v[100:103]\n"
      "ds_read_b64_tr_b16 a[244:245], v14 offset:30216\n"
      "ds_write_addtid_b32 v54 offset:6208\n"
      "v_mfma_f32_16x16x32_bf16 v[104:107], a[16:19], a[228:231], v[104:107]\n"
      "ds_write_addtid_b32 v55 offset:9312\n"
      "ds_write_addtid_b32 v56 offset:12416\n"
      "v_mfma_f32_16x16x32_bf16 v[108:111], a[20:23], a[228:231], v[108:111]\n"
      "ds_read_b128 a[228:231], v8 offset:29184\n"
      "ds_write_addtid_b32 v57 offset:15520\n"
      "s_waitcnt lgkmcnt(8)\n"
      "v_mfma_f32_16x16x32_bf16 v[100:103], a[24:27], a[216:219], v[100:103]\n"
      "ds_write_addtid_b32 v58 offset:18624\n"
      "ds_write_addtid_b32 v59 offset:21728\n"
      "v_mfma_f32_16x16x32_bf16 v[104:107], a[28:31], a[216:219], v[104:107]\n"
      "ds_write_addtid_b32 v60 offset:512\n"
      "ds_write_addtid_b32 v61 offset:3616\n"
      "v_mfma_f32_16x16x32_bf16 v[108:111], a[32:35], a[216:219], v[108:111]\n"
      "ds_read_b128 a[216:219], v9 offset:29184\n"
      "ds_write_addtid_b32 v62 offset:6720\n"
      "v_mfma_f32_16x16x32_bf16 v[100:103], a[36:39], a[220:223], v[100:103]\n"
      "ds_write_addtid_b32 v63 offset:9824\n"
      "ds_write_addtid_b32 v64 offset:12928\n"
      "s_waitcnt lgkmcnt(9)\n"
      "v_mfma_f32_4x4x4_16b_bf16 v[20:23], a[232:233], a[242:243], 0\n"
      "ds_write_addtid_b32 v65 offset:16032\n"
      "v_mfma_f32_4x4x4_16b_bf16 v[24:27], a[232:233], a[244:245], 0\n"
      "ds_write_addtid_b32 v66 offset:19136\n"
      "ds_write_addtid_b32 v67 offset:22240\n"
      "v_mfma_f32_16x16x32_bf16 v[104:107], a[40:43], a[220:223], v[104:107]\n"
      "ds_write_addtid_b32 v68 offset:1024\n"
      "v_mfma_f32_16x16x32_bf16 v[108:111], a[44:47], a[220:223], v[108:111]\n"
      "ds_read_b128 a[220:223], v10 offset:29184\n"
      "v_cvt_pk_bf16_f32 v20, v20, v24\n"
      "v_mfma_f32_16x16x32_bf16 v[100:103], a[48:51], a[224:227], v[100:103]\n"
      "ds_write_addtid_b32 v20 offset:25344\n"
      "ds_write_addtid_b32 v69 offset:4128\n"
      "v_mfma_f32_16x16x32_bf16 v[104:107], a[52:55], a[224:227], v[104:107]\n"
      "ds_write_addtid_b32 v70 offset:7232\n"
      "v_cvt_pk_bf16_f32 v21, v21, v25\n"
      "v_mfma_f32_16x16x32_bf16 v[108:111], a[56:59], a[224:227], v[108:111]\n"
      "ds_read_b128 a[224:227], v11 offset:29184\n"
      "ds_write_addtid_b32 v21 offset:28928\n"
      "v_mfma_f32_16x16x32_bf16 v[100:103], a[60:63], a[228:231], v[100:103]\n"
      "ds_write_addtid_b32 v71 offset:10336\n"
      "v_cvt_pk_bf16_f32 v22, v22, v26\n"
      "v_mfma_f32_16x16x32_bf16 v[104:107], a[64:67], a[228:231], v[104:107]\n"
      "ds_write_addtid_b32 v22 offset:32512\n"
      "ds_write_addtid_b32 v72 offset:13440\n"
      "v_mfma_f32_16x16x32_bf16 v[108:111], a[68:71], a[228:231], v[108:111]\n"
      "ds_read_b128 a[228:231], v3 offset:32256\n"
      "s_waitcnt lgkmcnt(7)\n"
      "v_mfma_f32_16x16x32_bf16 v[100:103], a[72:75], a[216:219], v[100:103]\n"
      "ds_write_addtid_b32 v73 offset:16544\n"
      "v_cvt_pk_bf16_f32 v23, v23, v27\n"
      "v_mfma_f32_16x16x32_bf16 v[104:107], a[76:79], a[216:219], v[104:107]\n"
      "ds_write_addtid_b32 v23 offset:36096\n"
      "v_mfma_f32_16x16x32_bf16 v[108:111], a[80:83], a[216:219], v[108:111]\n"
      "ds_read_b128 a[216:219], v4 offset:32256\n"
      "ds_write_addtid_b32 v74 offset:19648\n"
      "v_mfma_f32_16x16x32_bf16 v[100:103], a[84:87], a[220:223], v[100:103]\n"
      "v_mfma_f32_16x16x32_bf16 v[104:107], a[88:91], a[220:223], v[104:107]\n"
      "ds_write_addtid_b32 v75 offset:22752\n"
      "v_mfma_f32_16x16x32_bf16 v[108:111], a[92:95], a[220:223], v[108:111]\n"
      "ds_read_b128 a[220:223], v5 offset:32256\n"
      "s_waitcnt lgkmcnt(5)\n"
      "v_mfma_f32_16x16x32_bf16 v[100:103], a[96:99], a[224:227], v[100:103]\n"
      "ds_write_addtid_b32 v76 offset:1536\n"
      "v_mfma_f32_16x16x32_bf16 v[104:107], a[100:103], a[224:227], v[104:107]\n"
      "ds_write_addtid_b32 v77 offset:4640\n"
      "v_mfma_f32_16x16x32_bf16 v[108:111], a[104:107], a[224:227], v[108:111]\n"
      "ds_read_b128 a[224:227], v6 offset:32256\n"
      "v_mfma_f32_16x16x32_bf16 v[112:115], a[0:3], a[228:231], 0\n"
      "ds_write_addtid_b32 v78 offset:7744\n"
      "v_mfma_f32_16x16x32_bf16 v[116:119], a[4:7], a[228:231], 0\n"
      "ds_read_b64_tr_b16 a[242:243], v14 offset:31232\n"
      "ds_read_b64_tr_b16 a[244:245], v14 offset:31240\n"
      "v_mfma_f32_16x16x32_bf16 v[120:123], a[8:11], a[228:231], 0\n"
      "ds_read_b128 a[228:231], v7 offset:32256\n"
      "s_waitcnt lgkmcnt(6)\n"
      "v_mfma_f32_16x16x32_bf16 v[112:115], a[12:15], a[216:219], v[112:115]\n"
      "v_mfma_f32_16x16x32_bf16 v[116:119], a[16:19], a[216:219], v[116:119]\n"
      "ds_write_addtid_b32 v79 offset:10848\n"
      "v_mfma_f32_16x16x32_bf16 v[120:123], a[20:23], a[216:219], v[120:123]\n"
      "ds_read_b128 a[216:219], v8 offset:32256\n"
      "v_mfma_f32_16x16x32_bf16 v[112:115], a[24:27], a[220:223], v[112:115]\n"
      "ds_write_addtid_b32 v80 offset:13952\n"
      "v_mfma_f32_16x16x32_bf16 v[116:119], a[28:31], a[220:223], v[116:119]\n"
      "ds_write_addtid_b32 v81 offset:17056\n"
      "v_mfma_f32_16x16x32_bf16 v[120:123], a[32:35], a[220:223], v[120:123]\n"
      "ds_read_b128 a[220:223], v9 offset:32256\n"
      "s_waitcnt lgkmcnt(4)\n"
      "v_mfma_f32_4x4x4_16b_bf16 v[20:23], a[232:233], a[242:243], 0\n"
      "ds_write_addtid_b32 v82 offset:20160\n"
      "v_mfma_f32_4x4x4_16b_bf16 v[24:27], a[232:233], a[244:245], 0\n"
      "ds_write_addtid_b32 v83 offset:23264\n"
      "v_mfma_f32_16x16x32_bf16 v[112:115], a[36:39], a[224:227], v[112:115]\n"
      "ds_read_b64_tr_b16 a[242:243], v14 offset:29696\n"
      "ds_read_b64_tr_b16 a[244:245], v14 offset:29704\n"
      "v_mfma_f32_16x16x32_bf16 v[116:119], a[40:43], a[224:227], v[116:119]\n"
      "v_cvt_pk_bf16_f32 v20, v20, v24\n"
      "v_cvt_pk_bf16_f32 v21, v21, v25\n"
      "v_mfma_f32_16x16x32_bf16 v[120:123], a[44:47], a[224:227], v[120:123]\n"
      "ds_read_b128 a[224:227], v10 offset:32256\n"
      "ds_write_addtid_b32 v20 offset:25856\n"
      "v_mfma_f32_16x16x32_bf16 v[112:115], a[48:51], a[228:231], v[112:115]\n"
      "ds_read_b128 a[238:241], v11 offset:32256\n"
      "ds_write_addtid_b32 v21 offset:29440\n"
      "v_mfma_f32_16x16x32_bf16 v[116:119], a[52:55], a[228:231], v[116:119]\n"
      "v_cvt_pk_bf16_f32 v22, v22, v26\n"
      "v_cvt_pk_bf16_f32 v23, v23, v27\n"
      "v_mfma_f32_16x16x32_bf16 v[120:123], a[56:59], a[228:231], v[120:123]\n"
      "ds_write_addtid_b32 v22 offset:33024\n"
      "s_nop 0\n"
      "s_waitcnt lgkmcnt(5)\n"
      "v_mfma_f32_16x16x32_bf16 v[112:115], a[60:63], a[216:219], v[112:115]\n"
      "ds_write_addtid_b32 v23 offset:36608\n"
      "v_mfma_f32_16x16x32_bf16 v[116:119], a[64:67], a[216:219], v[116:119]\n"
      "v_mfma_f32_16x16x32_bf16 v[120:123], a[68:71], a[216:219], v[120:123]\n"
      "s_waitcnt lgkmcnt(0)\n"
      "s_barrier\n"
      "LSp0end_%=:\n"
      ".p2align 6\n"
      "LSloop_%=:\n"
      "v_mfma_f32_16x16x32_bf16 v[112:115], a[72:75], a[220:223], v[112:115]\n"
      "ds_read_b128 a[216:219], v0 offset:0\n"
      "ds_read_b128 v[28:31], v12 offset:0\n"
      "v_mfma_f32_16x16x32_bf16 v[116:119], a[76:79], a[220:223], v[116:119]\n"
      "ds_read_b128 a[246:249], v15 offset:3072\n"
      "ds_read_b128 v[32:35], v12 offset:53504\n"
      "v_mfma_f32_16x16x32_bf16 v[120:123], a[80:83], a[220:223], v[120:123]\n"
      "ds_read_b128 a[220:223], v0 offset:32\n"
      "ds_read_b128 a[250:253], v16 offset:3072\n"
      "v_mfma_f32_4x4x4_16b_bf16 v[20:23], a[232:233], a[242:243], 0\n"
      "ds_read_b128 v[44:47], v12 offset:1280\n"
      "v_mfma_f32_4x4x4_16b_bf16 v[24:27], a[232:233], a[244:245], 0\n"
      "ds_read_b128 v[48:51], v12 offset:54784\n"
      "ds_read_b128 v[36:39], v12 offset:1024\n"
      "v_mfma_f32_16x16x32_bf16 v[112:115], a[84:87], a[224:227], v[112:115]\n"
      "ds_read_b64_tr_b16 a[242:243], v14 offset:30720\n"
      "ds_read_b64_tr_b16 a[244:245], v14 offset:30728\n"
      "v_mfma_f32_16x16x32_bf16 v[116:119], a[88:91], a[224:227], v[116:119]\n"
      "ds_read_b128 v[40:43], v12 offset:54528\n"
      "ds_write_addtid_b32 v84 offset:2048\n"
      "v_mfma_f32_16x16x32_bf16 v[120:123], a[92:95], a[224:227], v[120:123]\n"
      "ds_write_addtid_b32 v85 offset:5152\n"
      "ds_write_addtid_b32 v86 offset:8256\n"
      "v_mfma_f32_16x16x32_bf16 v[112:115], a[96:99], a[238:241], v[112:115]\n"
      "ds_read_b128 a[224:227], v0 offset:64\n"
      "ds_write_addtid_b32 v87 offset:11360\n"
      "v_mfma_f32_16x16x32_bf16 v[116:119], a[100:103], a[238:241], v[116:119]\n"
      "ds_write_addtid_b32 v88 offset:14464\n"
      "ds_write_addtid_b32 v89 offset:17568\n"
      "v_cvt_pk_bf16_f32 v20, v20, v24\n"
      "v_cvt_pk_bf16_f32 v21, v21, v25\n"
      "v_cvt_pk_bf16_f32 v22, v22, v26\n"
      "v_cvt_pk_bf16_f32 v23, v23, v27\n"
      "v_mfma_f32_16x16x32_bf16 v[120:123], a[104:107], a[238:241], v[120:123]\n"
      "ds_write_addtid_b32 v20 offset:39168\n"
      "ds_write_addtid_b32 v21 offset:42752\n"
      "ds_write_addtid_b32 v22 offset:46336\n"
      "ds_write_addtid_b32 v23 offset:49920\n"
      "s_nop 0\n"
      "s_waitcnt lgkmcnt(12)\n"
      "v_mfma_f32_4x4x4_16b_bf16 v[20:23], a[232:233], a[242:243], 0\n"
      "ds_write_addtid_b32 v90 offset:20672\n"
      "v_mfma_f32_4x4x4_16b_bf16 v[24:27], a[232:233], a[244:245], 0\n"
      "ds_write_addtid_b32 v91 offset:23776\n"
      "v_add_f32 v28, v28, v44\n"
      "v_add_f32 v29, v29, v45\n"
      "v_mfma_f32_32x32x16_bf16 v[52:67], a[216:219], v[148:151], 0\n"
      "ds_read_b128 a[228:231], v1 offset:0\n"
      "ds_write_addtid_b32 v92 offset:2560\n"
      "ds_write_addtid_b32 v93 offset:5664\n"
      "v_add_f32 v30, v30, v46\n"
      "v_add_f32 v31, v31, v47\n"
      "v_cvt_pk_bf16_f32 v20, v20, v24\n"
      "v_mfma_f32_32x32x16_bf16 v[52:67], a[220:223], v[152:155], v[52:67]\n"
      "ds_write_addtid_b32 v20 offset:39680\n"
      "ds_read_b64_tr_b16 a[242:243], v14 offset:31744\n"
      "ds_read_b64_tr_b16 a[244:245], v14 offset:31752\n"
      "v_add_f32 v32, v32, v48\n"
      "v_add_f32 v33, v33, v49\n"
      "v_cvt_pk_bf16_f32 v21, v21, v25\n"
      "v_mfma_f32_32x32x16_bf16 v[68:83], a[216:219], a[108:111], 0\n"
      "buffer_store_dwordx4 a[246:249], v18, s[8:11], s60 offen offset:0 sc1\n"
      "v_mfma_f32_32x32x16_bf16 v[68:83], a[220:223], a[112:115], v[68:83]\n"
      "ds_write_addtid_b32 v94 offset:8768\n"
      "ds_write_addtid_b32 v95 offset:11872\n"
      "ds_write_addtid_b32 v96 offset:14976\n"
      "ds_write_addtid_b32 v97 offset:18080\n"
      "ds_write_addtid_b32 v98 offset:21184\n"
      "ds_write_addtid_b32 v99 offset:24288\n"
      "v_mfma_f32_32x32x16_bf16 v[84:99], a[216:219], v[220:223], 0\n"
      "ds_read_b128 a[216:219], v1 offset:32\n"
      "ds_write_addtid_b32 v21 offset:43264\n"
      "ds_read_b128 a[246:249], v17 offset:3072\n"
      "v_add_f32 v34, v34, v50\n"
      "v_add_f32 v35, v35, v51\n"
      "v_sub_f32_e64 v32, v32, v28\n"
      "v_mfma_f32_32x32x16_bf16 v[84:99], a[220:223], v[224:227], v[84:99]\n"
      "ds_read_b128 v[44:47], v12 offset:256\n"
      "ds_read_b128 v[48:51], v12 offset:53760\n"
      "v_sub_f32 v33, v33, v29\n"
      "v_sub_f32 v34, v34, v30\n"
      "v_sub_f32 v35, v35, v31\n"
      "s_waitcnt lgkmcnt(11)\n"
      "v_mfma_f32_32x32x16_bf16 v[52:67], a[224:227], v[156:159], v[52:67]\n"
      "buffer_store_dwordx4 a[250:253], v18, s[8:11], s60 offen offset:1024 sc1\n"
      "ds_read_b128 a[220:223], v1 offset:64\n"
      "v_mfma_f32_32x32x16_bf16 v[68:83], a[224:227], a[116:119], v[68:83]\n"
      "v_cvt_pk_bf16_f32 v22, v22, v26\n"
      "v_cvt_pk_bf16_f32 v23, v23, v27\n"
      "v_fmac_f32 v28, s45, v32\n"
      "v_fmac_f32 v29, s45, v33\n"
      "v_fmac_f32 v30, s45, v34\n"
      "v_fmac_f32 v31, s45, v35\n"
      "v_mfma_f32_32x32x16_bf16 v[84:99], a[224:227], v[228:231], v[84:99]\n"
      "ds_write_addtid_b32 v22 offset:46848\n"
      "ds_write_addtid_b32 v23 offset:50432\n"
      "ds_read_b128 a[250:253], v15 offset:9216\n"
      "v_fmac_f32 v28, s46, v124\n"
      "v_fmac_f32_e64 v29, s46, v125\n"
      "s_waitcnt lgkmcnt(4)\n"
      "v_mfma_f32_4x4x4_16b_bf16 v[20:23], a[232:233], a[242:243], 0\n"
      "v_fmac_f32_e64 v30, s46, v126\n"
      "v_mfma_f32_4x4x4_16b_bf16 v[24:27], a[232:233], a[244:245], 0\n"
      "v_fmac_f32_e64 v31, s46, v127\n"
      "v_fmac_f32_e64 v32, s48, v124\n"
      "v_fmac_f32_e64 v33, s48, v125\n"
      "v_mfma_f32_32x32x16_bf16 v[52:67], a[228:231], v[160:163], v[52:67]\n"
      "buffer_store_dwordx4 a[246:249], v18, s[8:11], s60 offen offset:2048 sc1\n"
      "ds_read_b128 a[224:227], v2 offset:0\n"
      "v_mfma_f32_32x32x16_bf16 v[68:83], a[228:231], a[120:123], v[68:83]\n"
      "ds_read_b64_tr_b16 a[242:243], v14 offset:32768\n"
      "ds_read_b64_tr_b16 a[244:245], v14 offset:32776\n"
      "ds_read_b128 a[246:249], v16 offset:9216\n"
      "v_fmac_f32_e64 v34, s48, v126\n"
      "v_fmac_f32 v35, s48, v127\n"
      "v_cvt_pk_bf16_f32 v28, v28, v29\n"
      "v_mfma_f32_32x32x16_bf16 v[84:99], a[228:231], v[232:235], v[84:99]\n"
      "v_cvt_pk_bf16_f32 v29, v30, v31\n"
      "v_fma_f32 v32, s47, v32, v100\n"
      "v_cvt_pk_bf16_f32 v20, v20, v24\n"
      "v_fma_f32 v33, s47, v33, v101\n"
      "v_cvt_pk_bf16_f32 v21, v21, v25\n"
      "s_waitcnt lgkmcnt(4)\n"
      "v_mfma_f32_32x32x16_bf16 v[52:67], a[216:219], v[164:167], v[52:67]\n"
      "ds_read_b128 a[228:231], v2 offset:32\n"
      "ds_write_addtid_b32 v20 offset:40192\n"
      "ds_write_b64 v13, v[28:29] offset:0\n"
      "v_fma_f32 v34, s47, v34, v102\n"
      "v_mfma_f32_32x32x16_bf16 v[68:83], a[216:219], a[124:127], v[68:83]\n"
      "buffer_store_dwordx4 a[250:253], v18, s[8:11], s61 offen offset:0 sc1\n"
      "ds_write_addtid_b32 v21 offset:43776\n"
      "v_cvt_pk_bf16_f32 v22, v22, v26\n"
      "v_cvt_pk_bf16_f32 v23, v23, v27\n"
      "v_mfma_f32_32x32x16_bf16 v[84:99], a[216:219], v[236:239], v[84:99]\n"
      "ds_write_addtid_b32 v22 offset:47360\n"
      "ds_write_addtid_b32 v23 offset:50944\n"
      "ds_read_b128 a[250:253], v17 offset:9216\n"
      "v_fma_f32 v35, s47, v35, v103\n"
      "v_cvt_pk_bf16_f32 v32, v32, v33\n"
      "v_cvt_pk_bf16_f32 v33, v34, v35\n"
      "s_waitcnt lgkmcnt(4)\n"
      "v_mfma_f32_4x4x4_16b_bf16 v[20:23], a[232:233], a[242:243], 0\n"
      "v_mfma_f32_4x4x4_16b_bf16 v[24:27], a[232:233], a[244:245], 0\n"
      "v_add_f32 v36, v36, v44\n"
      "v_add_f32 v37, v37, v45\n"
      "v_add_f32 v38, v38, v46\n"
      "v_mfma_f32_32x32x16_bf16 v[52:67], a[220:223], v[168:171], v[52:67]\n"
      "ds_read_b128 a[216:219], v2 offset:64\n"
      "ds_write_b64 v13, v[32:33] offset:12288\n"
      "v_add_f32 v39, v39, v47\n"
      "v_cvt_pk_bf16_f32 v20, v20, v24\n"
      "v_mfma_f32_32x32x16_bf16 v[68:83], a[220:223], a[128:131], v[68:83]\n"
      "ds_write_addtid_b32 v20 offset:40704\n"
      "ds_read_b128 v[28:31], v12 offset:512\n"
      "ds_read_b128 v[32:35], v12 offset:54016\n"
      "v_add_f32 v40, v40, v48\n"
      "v_add_f32_e64 v41, v41, v49\n"
      "v_cvt_pk_bf16_f32 v21, v21, v25\n"
      "v_mfma_f32_32x32x16_bf16 v[84:99], a[220:223], v[240:243], v[84:99]\n"
      "buffer_store_dwordx4 a[246:249], v18, s[8:11], s61 offen offset:1024 sc1\n"
      "v_cvt_pk_bf16_f32 v22, v22, v26\n"
      "v_cvt_pk_bf16_f32 v23, v23, v27\n"
      "v_mfma_f32_32x32x16_bf16 v[52:67], a[224:227], v[172:175], v[52:67]\n"
      "ds_write_addtid_b32 v21 offset:44288\n"
      "ds_write_addtid_b32 v22 offset:47872\n"
      "ds_write_addtid_b32 v23 offset:51456\n"
      "v_add_f32 v42, v42, v50\n"
      "v_add_f32 v43, v43, v51\n"
      "v_sub_f32 v40, v40, v36\n"
      "v_mfma_f32_32x32x16_bf16 v[68:83], a[224:227], a[132:135], v[68:83]\n"
      "ds_read_b128 v[44:47], v12 offset:1792\n"
      "ds_read_b128 v[48:51], v12 offset:55296\n"
      "v_sub_f32 v41, v41, v37\n"
      "v_sub_f32 v42, v42, v38\n"
      "s_waitcnt lgkmcnt(5)\n"
      "v_sub_f32_e64 v43, v43, v39\n"
      "v_mfma_f32_32x32x16_bf16 v[84:99], a[224:227], v[244:247], v[84:99]\n"
      "buffer_store_dwordx4 a[250:253], v18, s[8:11], s61 offen offset:2048 sc1\n"
      "s_waitcnt lgkmcnt(2)\n"
      "s_barrier\n"
      "v_mfma_f32_32x32x16_bf16 v[52:67], a[228:231], v[176:179], v[52:67]\n"
      "ds_read_b128 a[220:223], v0 offset:14336\n"
      "v_fmac_f32 v36, s45, v40\n"
      "v_fmac_f32 v37, s45, v41\n"
      "v_fmac_f32 v38, s45, v42\n"
      "v_fmac_f32 v39, s45, v43\n"
      "v_fmac_f32 v36, s46, v128\n"
      "v_fmac_f32 v37, s46, v129\n"
      "v_mfma_f32_32x32x16_bf16 v[68:83], a[228:231], a[136:139], v[68:83]\n"
      "ds_read_b128 a[224:227], v0 offset:14368\n"
      "v_fmac_f32 v38, s46, v130\n"
      "v_fmac_f32 v39, s46, v131\n"
      "v_fmac_f32 v40, s48, v128\n"
      "v_fmac_f32 v41, s48, v129\n"
      "v_fmac_f32 v42, s48, v130\n"
      "v_fmac_f32 v43, s48, v131\n"
      "v_mfma_f32_32x32x16_bf16 v[84:99], a[228:231], v[248:251], v[84:99]\n"
      "v_cvt_pk_bf16_f32 v36, v36, v37\n"
      "v_cvt_pk_bf16_f32 v37, v38, v39\n"
      "v_fma_f32 v40, s47, v40, v104\n"
      "v_fma_f32 v41, s47, v41, v105\n"
      "v_fma_f32 v42, s47, v42, v106\n"
      "v_fma_f32 v43, s47, v43, v107\n"
      "v_cvt_pk_bf16_f32 v40, v40, v41\n"
      "v_mfma_f32_32x32x16_bf16 v[52:67], a[216:219], v[180:183], v[52:67]\n"
      "ds_read_b128 a[228:231], v0 offset:14400\n"
      "ds_write_b64 v13, v[36:37] offset:64\n"
      "v_cvt_pk_bf16_f32 v41, v42, v43\n"
      "s_waitcnt lgkmcnt(2)\n"
      "v_add_f32 v28, v28, v44\n"
      "v_mfma_f32_32x32x16_bf16 v[68:83], a[216:219], a[140:143], v[68:83]\n"
      "ds_write_b64 v13, v[40:41] offset:12352\n"
      "v_add_f32 v29, v29, v45\n"
      "v_add_f32 v30, v30, v46\n"
      "v_add_f32 v31, v31, v47\n"
      "v_add_f32 v32, v32, v48\n"
      "v_mfma_f32_32x32x16_bf16 v[84:99], a[216:219], v[252:255], v[84:99]\n"
      "ds_read_b128 v[36:39], v12 offset:2048\n"
      "ds_read_b128 v[40:43], v12 offset:55552\n"
      "v_add_f32 v33, v33, v49\n"
      "v_add_f32 v34, v34, v50\n"
      "v_add_f32 v35, v35, v51\n"
      "v_sub_f32 v32, v32, v28\n"
      "v_mfma_f32_32x32x16_bf16 v[52:67], a[220:223], v[184:187], v[52:67]\n"
      "ds_read_b128 a[216:219], v1 offset:14336\n"
      "ds_read_b128 v[44:47], v12 offset:2304\n"
      "ds_read_b128 v[48:51], v12 offset:55808\n"
      "v_sub_f32 v33, v33, v29\n"
      "v_sub_f32 v34, v34, v30\n"
      "v_sub_f32_e64 v35, v35, v31\n"
      "v_mfma_f32_32x32x16_bf16 v[68:83], a[220:223], a[144:147], v[68:83]\n"
      "v_fmac_f32 v28, s45, v32\n"
      "v_fmac_f32 v29, s45, v33\n"
      "v_fmac_f32 v30, s45, v34\n"
      "v_fmac_f32 v31, s45, v35\n"
      "v_fmac_f32 v28, s46, v136\n"
      "v_fmac_f32 v29, s46, v137\n"
      "v_fmac_f32 v30, s46, v138\n"
      "v_mfma_f32_32x32x16_bf16 v[84:99], a[220:223], a[180:183], v[84:99]\n"
      "v_fmac_f32 v31, s46, v139\n"
      "v_fmac_f32 v32, s48, v136\n"
      "v_fmac_f32 v33, s48, v137\n"
      "v_fmac_f32 v34, s48, v138\n"
      "v_fmac_f32 v35, s48, v139\n"
      "v_cvt_pk_bf16_f32 v28, v28, v29\n"
      "v_cvt_pk_bf16_f32 v29, v30, v31\n"
      "v_mfma_f32_32x32x16_bf16 v[52:67], a[224:227], v[188:191], v[52:67]\n"
      "ds_read_b128 a[220:223], v1 offset:14368\n"
      "ds_write_b64 v13, v[28:29] offset:3072\n"
      "v_fma_f32 v32, s47, v32, v112\n"
      "v_fma_f32 v33, s47, v33, v113\n"
      "v_fma_f32 v34, s47, v34, v114\n"
      "v_mfma_f32_32x32x16_bf16 v[68:83], a[224:227], a[148:151], v[68:83]\n"
      "v_fma_f32 v35, s47, v35, v115\n"
      "v_cvt_pk_bf16_f32 v32, v32, v33\n"
      "v_cvt_pk_bf16_f32 v33, v34, v35\n"
      "s_waitcnt lgkmcnt(2)\n"
      "v_add_f32 v36, v36, v44\n"
      "v_add_f32 v37, v37, v45\n"
      "v_add_f32 v38, v38, v46\n"
      "v_mfma_f32_32x32x16_bf16 v[84:99], a[224:227], a[184:187], v[84:99]\n"
      "ds_write_b64 v13, v[32:33] offset:15360\n"
      "v_add_f32 v39, v39, v47\n"
      "v_add_f32 v40, v40, v48\n"
      "v_add_f32 v41, v41, v49\n"
      "v_mfma_f32_32x32x16_bf16 v[52:67], a[228:231], v[192:195], v[52:67]\n"
      "ds_read_b128 a[224:227], v1 offset:14400\n"
      "ds_read_b128 v[28:31], v12 offset:1536\n"
      "ds_read_b128 v[32:35], v12 offset:55040\n"
      "v_add_f32 v42, v42, v50\n"
      "v_add_f32 v43, v43, v51\n"
      "v_sub_f32 v40, v40, v36\n"
      "v_sub_f32_e64 v41, v41, v37\n"
      "v_mfma_f32_32x32x16_bf16 v[68:83], a[228:231], a[152:155], v[68:83]\n"
      "ds_read_b128 v[44:47], v12 offset:768\n"
      "ds_read_b128 v[48:51], v12 offset:54272\n"
      "v_sub_f32 v42, v42, v38\n"
      "v_sub_f32 v43, v43, v39\n"
      "v_fmac_f32 v36, s45, v40\n"
      "v_fmac_f32 v37, s45, v41\n"
      "v_fmac_f32 v38, s45, v42\n"
      "v_mfma_f32_32x32x16_bf16 v[84:99], a[228:231], a[188:191], v[84:99]\n"
      "v_fmac_f32 v39, s45, v43\n"
      "v_fmac_f32 v36, s46, v132\n"
      "v_fmac_f32 v37, s46, v133\n"
      "v_fmac_f32 v38, s46, v134\n"
      "v_fmac_f32 v39, s46, v135\n"
      "v_fmac_f32 v40, s48, v132\n"
      "v_fmac_f32 v41, s48, v133\n"
      "v_mfma_f32_32x32x16_bf16 v[52:67], a[216:219], v[196:199], v[52:67]\n"
      "ds_read_b128 a[228:231], v2 offset:14336\n"
      "v_fmac_f32 v42, s48, v134\n"
      "v_fmac_f32 v43, s48, v135\n"
      "v_cvt_pk_bf16_f32 v36, v36, v37\n"
      "v_cvt_pk_bf16_f32 v37, v38, v39\n"
      "s_add_u32 s58, s58, 786432\n"
      "v_fma_f32 v40, s47, v40, v108\n"
      "v_mfma_f32_32x32x16_bf16 v[68:83], a[216:219], a[156:159], v[68:83]\n"
      "ds_write_b64 v13, v[36:37] offset:128\n"
      "v_fma_f32 v41, s47, v41, v109\n"
      "v_fma_f32 v42, s47, v42, v110\n"
      "v_fma_f32 v43, s47, v43, v111\n"
      "v_cvt_pk_bf16_f32 v40, v40, v41\n"
      "v_mfma_f32_32x32x16_bf16 v[84:99], a[216:219], a[192:195], v[84:99]\n"
      "v_cvt_pk_bf16_f32 v41, v42, v43\n"
      "s_waitcnt lgkmcnt(1)\n"
      "v_add_f32 v28, v28, v44\n"
      "v_add_f32 v29, v29, v45\n"
      "v_add_f32 v30, v30, v46\n"
      "v_add_f32 v31, v31, v47\n"
      "s_add_u32 s59, s58, 196608\n"
      "v_mfma_f32_32x32x16_bf16 v[52:67], a[220:223], v[200:203], v[52:67]\n"
      "ds_read_b128 a[216:219], v2 offset:14368\n"
      "ds_write_b64 v13, v[40:41] offset:12416\n"
      "v_add_f32 v32, v32, v48\n"
      "v_add_f32 v33, v33, v49\n"
      "v_add_f32 v34, v34, v50\n"
      "v_add_f32 v35, v35, v51\n"
      "v_mfma_f32_32x32x16_bf16 v[68:83], a[220:223], a[160:163], v[68:83]\n"
      "ds_read_b128 v[36:39], v12 offset:2560\n"
      "ds_read_b128 v[40:43], v12 offset:56064\n"
      "ds_read_b128 v[44:47], v12 offset:2816\n"
      "ds_read_b128 v[48:51], v12 offset:56320\n"
      "v_sub_f32 v32, v32, v28\n"
      "v_sub_f32_e64 v33, v33, v29\n"
      "v_mfma_f32_32x32x16_bf16 v[84:99], a[220:223], a[196:199], v[84:99]\n"
      "ds_read_b128 a[220:223], v2 offset:14400\n"
      "v_sub_f32 v34, v34, v30\n"
      "v_sub_f32 v35, v35, v31\n"
      "v_fmac_f32 v28, s45, v32\n"
      "v_fmac_f32 v29, s45, v33\n"
      "s_add_u32 s60, s58, 3072\n"
      "v_mfma_f32_32x32x16_bf16 v[52:67], a[224:227], v[204:207], v[52:67]\n"
      "s_add_u32 s61, s59, 3072\n"
      "v_fmac_f32 v30, s45, v34\n"
      "v_fmac_f32 v31, s45, v35\n"
      "v_fmac_f32 v28, s46, v140\n"
      "v_fmac_f32 v29, s46, v141\n"
      "v_fmac_f32 v30, s46, v142\n"
      "v_fmac_f32 v31, s46, v143\n"
      "s_waitcnt lgkmcnt(1)\n"
      "s_barrier\n"
      "v_mfma_f32_32x32x16_bf16 v[68:83], a[224:227], a[164:167], v[68:83]\n"
      "ds_read_b64_tr_b16 a[242:243], v14 offset:0\n"
      "ds_read_b128 a[246:249], v15 offset:0\n"
      "ds_read_b64_tr_b16 a[244:245], v14 offset:8\n"
      "v_fmac_f32 v32, s48, v140\n"
      "v_fmac_f32 v33, s48, v141\n"
      "v_fmac_f32 v34, s48, v142\n"
      "v_fmac_f32 v35, s48, v143\n"
      "v_mfma_f32_32x32x16_bf16 v[84:99], a[224:227], a[200:203], v[84:99]\n"
      "ds_read_b128 a[250:253], v16 offset:0\n"
      "v_cvt_pk_bf16_f32 v28, v28, v29\n"
      "v_cvt_pk_bf16_f32 v29, v30, v31\n"
      "v_fma_f32 v32, s47, v32, v116\n"
      "v_fma_f32 v33, s47, v33, v117\n"
      "v_fma_f32 v34, s47, v34, v118\n"
      "v_mfma_f32_32x32x16_bf16 v[52:67], a[228:231], v[208:211], v[52:67]\n"
      "ds_write_b64 v13, v[28:29] offset:3136\n"
      "s_waitcnt lgkmcnt(2)\n"
      "v_fma_f32 v35, s47, v35, v119\n"
      "v_cvt_pk_bf16_f32 v32, v32, v33\n"
      "v_mfma_f32_32x32x16_bf16 v[68:83], a[228:231], a[168:171], v[68:83]\n"
      "buffer_store_dwordx4 a[246:249], v18, s[8:11], s58 offen offset:0 sc1\n"
      "s_waitcnt lgkmcnt(1)\n"
      "v_cvt_pk_bf16_f32 v33, v34, v35\n"
      "v_mfma_f32_4x4x4_16b_bf16 v[20:23], a[232:233], a[242:243], 0\n"
      "v_mfma_f32_4x4x4_16b_bf16 v[24:27], a[232:233], a[244:245], 0\n"
      "buffer_store_dwordx4 a[250:253], v18, s[8:11], s58 offen offset:1024 sc1\n"
      "v_mfma_f32_32x32x16_bf16 v[84:99], a[228:231], a[204:207], v[84:99]\n"
      "ds_read_b128 a[246:249], v17 offset:0\n"
      "ds_read_b128 a[250:253], v15 offset:6144\n"
      "ds_write_b64 v13, v[32:33] offset:15424\n"
      "v_cvt_pk_bf16_f32 v20, v20, v24\n"
      "v_mfma_f32_32x32x16_bf16 v[52:67], a[216:219], v[212:215], v[52:67]\n"
      "ds_read_b128 a[224:227], v3 offset:0\n"
      "ds_write_addtid_b32 v20 offset:24832\n"
      "v_add_f32 v36, v36, v44\n"
      "v_add_f32 v37, v37, v45\n"
      "v_add_f32 v38, v38, v46\n"
      "v_cvt_pk_bf16_f32 v21, v21, v25\n"
      "v_mfma_f32_32x32x16_bf16 v[52:67], a[220:223], v[216:219], v[52:67]\n"
      "ds_write_addtid_b32 v21 offset:28416\n"
      "s_waitcnt lgkmcnt(5)\n"
      "buffer_store_dwordx4 a[246:249], v18, s[8:11], s58 offen offset:2048 sc1\n"
      "v_mfma_f32_32x32x16_bf16 v[68:83], a[216:219], a[172:175], v[68:83]\n"
      "ds_read_b128 a[228:231], v4 offset:0\n"
      "ds_read_b128 a[246:249], v16 offset:6144\n"
      "s_waitcnt lgkmcnt(3)\n"
      "v_add_f32 v39, v39, v47\n"
      "v_add_f32 v40, v40, v48\n"
      "v_add_f32 v41, v41, v49\n"
      "v_mfma_f32_32x32x16_bf16 v[68:83], a[220:223], a[176:179], v[68:83]\n"
      "buffer_store_dwordx4 a[250:253], v18, s[8:11], s59 offen offset:0 sc1\n"
      "v_mfma_f32_32x32x16_bf16 v[84:99], a[216:219], a[208:211], v[84:99]\n"
      "ds_read_b128 a[216:219], v5 offset:0\n"
      "ds_read_b128 a[250:253], v17 offset:6144\n"
      "ds_write_addtid_b32 v52 offset:0\n"
      "v_add_f32 v42, v42, v50\n"
      "s_waitcnt lgkmcnt(3)\n"
      "v_add_f32_e64 v43, v43, v51\n"
      "v_mfma_f32_32x32x16_bf16 v[84:99], a[220:223], a[212:215], v[84:99]\n"
      "ds_read_b128 a[220:223], v6 offset:0\n"
      "ds_write_addtid_b32 v53 offset:3104\n"
      "v_sub_f32 v40, v40, v36\n"
      "v_cvt_pk_bf16_f32 v22, v22, v26\n"
      "v_sub_f32 v41, v41, v37\n"
      "v_cvt_pk_bf16_f32 v23, v23, v27\n"
      "v_mfma_f32_16x16x32_bf16 v[124:127], a[0:3], a[224:227], 0\n"
      "buffer_store_dwordx4 a[246:249], v18, s[8:11], s59 offen offset:1024 sc1\n"
      "v_mfma_f32_16x16x32_bf16 v[128:131], a[4:7], a[224:227], 0\n"
      "ds_write_addtid_b32 v22 offset:32000\n"
      "ds_write_addtid_b32 v23 offset:35584\n"
      "v_mfma_f32_16x16x32_bf16 v[132:135], a[8:11], a[224:227], 0\n"
      "ds_read_b128 a[224:227], v7 offset:0\n"
      "ds_read_b64_tr_b16 a[242:243], v14 offset:1024\n"
      "v_mfma_f32_16x16x32_bf16 v[124:127], a[12:15], a[228:231], v[124:127]\n"
      "ds_read_b64_tr_b16 a[244:245], v14 offset:1032\n"
      "ds_write_addtid_b32 v54 offset:6208\n"
      "v_mfma_f32_16x16x32_bf16 v[128:131], a[16:19], a[228:231], v[128:131]\n"
      "ds_write_addtid_b32 v55 offset:9312\n"
      "v_sub_f32 v42, v42, v38\n"
      "v_mfma_f32_16x16x32_bf16 v[132:135], a[20:23], a[228:231], v[132:135]\n"
      "ds_read_b128 a[228:231], v8 offset:0\n"
      "s_waitcnt lgkmcnt(8)\n"
      "v_mfma_f32_16x16x32_bf16 v[124:127], a[24:27], a[216:219], v[124:127]\n"
      "v_sub_f32 v43, v43, v39\n"
      "v_fmac_f32 v36, s45, v40\n"
      "v_mfma_f32_16x16x32_bf16 v[128:131], a[28:31], a[216:219], v[128:131]\n"
      "v_fmac_f32 v37, s45, v41\n"
      "v_fmac_f32 v38, s45, v42\n"
      "v_mfma_f32_16x16x32_bf16 v[132:135], a[32:35], a[216:219], v[132:135]\n"
      "ds_read_b128 a[216:219], v9 offset:0\n"
      "ds_write_addtid_b32 v56 offset:12416\n"
      "v_mfma_f32_16x16x32_bf16 v[124:127], a[36:39], a[220:223], v[124:127]\n"
      "buffer_store_dwordx4 a[250:253], v18, s[8:11], s59 offen offset:2048 sc1\n"
      "s_waitcnt lgkmcnt(2)\n"
      "v_mfma_f32_4x4x4_16b_bf16 v[20:23], a[232:233], a[242:243], 0\n"
      "v_fmac_f32 v39, s45, v43\n"
      "v_mfma_f32_4x4x4_16b_bf16 v[24:27], a[232:233], a[244:245], 0\n"
      "v_fmac_f32 v36, s46, v144\n"
      "v_fmac_f32 v37, s46, v145\n"
      "v_fmac_f32_e64 v38, s46, v146\n"
      "v_mfma_f32_16x16x32_bf16 v[128:131], a[40:43], a[220:223], v[128:131]\n"
      "ds_write_addtid_b32 v57 offset:15520\n"
      "v_cvt_pk_bf16_f32 v20, v20, v24\n"
      "v_mfma_f32_16x16x32_bf16 v[132:135], a[44:47], a[220:223], v[132:135]\n"
      "ds_read_b128 a[220:223], v10 offset:0\n"
      "ds_write_addtid_b32 v20 offset:25344\n"
      "v_mfma_f32_16x16x32_bf16 v[124:127], a[48:51], a[224:227], v[124:127]\n"
      "ds_write_addtid_b32 v58 offset:18624\n"
      "v_cvt_pk_bf16_f32 v21, v21, v25\n"
      "v_mfma_f32_16x16x32_bf16 v[128:131], a[52:55], a[224:227], v[128:131]\n"
      "ds_write_addtid_b32 v21 offset:28928\n"
      "v_fmac_f32_e64 v39, s46, v147\n"
      "v_mfma_f32_16x16x32_bf16 v[132:135], a[56:59], a[224:227], v[132:135]\n"
      "ds_read_b128 a[224:227], v11 offset:0\n"
      "ds_write_addtid_b32 v59 offset:21728\n"
      "v_mfma_f32_16x16x32_bf16 v[124:127], a[60:63], a[228:231], v[124:127]\n"
      "v_fmac_f32 v40, s48, v144\n"
      "v_fmac_f32 v41, s48, v145\n"
      "v_mfma_f32_16x16x32_bf16 v[128:131], a[64:67], a[228:231], v[128:131]\n"
      "ds_write_addtid_b32 v60 offset:512\n"
      "v_fmac_f32 v42, s48, v146\n"
      "v_mfma_f32_16x16x32_bf16 v[132:135], a[68:71], a[228:231], v[132:135]\n"
      "ds_read_b128 a[228:231], v3 offset:3072\n"
      "s_waitcnt lgkmcnt(4)\n"
      "v_mfma_f32_16x16x32_bf16 v[124:127], a[72:75], a[216:219], v[124:127]\n"
      "ds_write_addtid_b32 v61 offset:3616\n"
      "v_fmac_f32 v43, s48, v147\n"
      "v_mfma_f32_16x16x32_bf16 v[128:131], a[76:79], a[216:219], v[128:131]\n"
      "ds_write_addtid_b32 v62 offset:6720\n"
      "v_cvt_pk_bf16_f32 v36, v36, v37\n"
      "v_mfma_f32_16x16x32_bf16 v[132:135], a[80:83], a[216:219], v[132:135]\n"
      "ds_read_b128 a[216:219], v4 offset:3072\n"
      "v_cvt_pk_bf16_f32 v37, v38, v39\n"
      "v_mfma_f32_16x16x32_bf16 v[124:127], a[84:87], a[220:223], v[124:127]\n"
      "ds_write_b64 v13, v[36:37] offset:3200\n"
      "v_mfma_f32_16x16x32_bf16 v[128:131], a[88:91], a[220:223], v[128:131]\n"
      "v_fma_f32 v40, s47, v40, v120\n"
      "v_fma_f32 v41, s47, v41, v121\n"
      "v_mfma_f32_16x16x32_bf16 v[132:135], a[92:95], a[220:223], v[132:135]\n"
      "ds_read_b128 a[220:223], v5 offset:3072\n"
      "s_waitcnt lgkmcnt(3)\n"
      "v_mfma_f32_16x16x32_bf16 v[124:127], a[96:99], a[224:227], v[124:127]\n"
      "ds_write_addtid_b32 v63 offset:9824\n"
      "ds_write_addtid_b32 v64 offset:12928\n"
      "v_mfma_f32_16x16x32_bf16 v[128:131], a[100:103], a[224:227], v[128:131]\n"
      "ds_write_addtid_b32 v65 offset:16032\n"
      "v_fma_f32 v42, s47, v42, v122\n"
      "v_mfma_f32_16x16x32_bf16 v[132:135], a[104:107], a[224:227], v[132:135]\n"
      "ds_read_b128 a[224:227], v6 offset:3072\n"
      "v_fma_f32 v43, s47, v43, v123\n"
      "v_cvt_pk_bf16_f32 v40, v40, v41\n"
      "v_cvt_pk_bf16_f32 v41, v42, v43\n"
      "v_mfma_f32_16x16x32_bf16 v[136:139], a[0:3], a[228:231], 0\n"
      "ds_write_b64 v13, v[40:41] offset:15488\n"
      "v_mfma_f32_16x16x32_bf16 v[140:143], a[4:7], a[228:231], 0\n"
      "v_cvt_pk_bf16_f32 v22, v22, v26\n"
      "v_cvt_pk_bf16_f32 v23, v23, v27\n"
      "v_mfma_f32_16x16x32_bf16 v[144:147], a[8:11], a[228:231], 0\n"
      "ds_read_b128 a[228:231], v7 offset:3072\n"
      "s_nop 0\n"
      "s_waitcnt lgkmcnt(3)\n"
      "v_mfma_f32_16x16x32_bf16 v[136:139], a[12:15], a[216:219], v[136:139]\n"
      "ds_read_b64_tr_b16 a[242:243], v14 offset:2048\n"
      "ds_read_b64_tr_b16 a[244:245], v14 offset:2056\n"
      "v_mfma_f32_16x16x32_bf16 v[140:143], a[16:19], a[216:219], v[140:143]\n"
      "ds_write_addtid_b32 v22 offset:32512\n"
      "ds_write_addtid_b32 v23 offset:36096\n"
      "v_mfma_f32_16x16x32_bf16 v[144:147], a[20:23], a[216:219], v[144:147]\n"
      "ds_read_b128 a[216:219], v8 offset:3072\n"
      "ds_write_addtid_b32 v66 offset:19136\n"
      "v_mfma_f32_16x16x32_bf16 v[136:139], a[24:27], a[220:223], v[136:139]\n"
      "ds_write_addtid_b32 v67 offset:22240\n"
      "ds_write_addtid_b32 v68 offset:1024\n"
      "v_mfma_f32_16x16x32_bf16 v[140:143], a[28:31], a[220:223], v[140:143]\n"
      "ds_write_addtid_b32 v69 offset:4128\n"
      "ds_write_addtid_b32 v70 offset:7232\n"
      "v_mfma_f32_16x16x32_bf16 v[144:147], a[32:35], a[220:223], v[144:147]\n"
      "ds_read_b128 a[220:223], v9 offset:3072\n"
      "ds_write_addtid_b32 v71 offset:10336\n"
      "s_nop 0\n"
      "s_waitcnt lgkmcnt(9)\n"
      "v_mfma_f32_4x4x4_16b_bf16 v[20:23], a[232:233], a[242:243], 0\n"
      "ds_write_addtid_b32 v72 offset:13440\n"
      "ds_write_addtid_b32 v73 offset:16544\n"
      "v_mfma_f32_4x4x4_16b_bf16 v[24:27], a[232:233], a[244:245], 0\n"
      "ds_write_addtid_b32 v74 offset:19648\n"
      "ds_write_addtid_b32 v75 offset:22752\n"
      "v_mfma_f32_16x16x32_bf16 v[136:139], a[36:39], a[224:227], v[136:139]\n"
      "ds_read_b64_tr_b16 a[242:243], v14 offset:512\n"
      "ds_read_b64_tr_b16 a[244:245], v14 offset:520\n"
      "v_cvt_pk_bf16_f32 v20, v20, v24\n"
      "v_mfma_f32_16x16x32_bf16 v[140:143], a[40:43], a[224:227], v[140:143]\n"
      "ds_write_addtid_b32 v20 offset:25856\n"
      "ds_write_addtid_b32 v76 offset:1536\n"
      "v_mfma_f32_16x16x32_bf16 v[144:147], a[44:47], a[224:227], v[144:147]\n"
      "ds_read_b128 a[224:227], v10 offset:3072\n"
      "ds_write_addtid_b32 v77 offset:4640\n"
      "v_mfma_f32_16x16x32_bf16 v[136:139], a[48:51], a[228:231], v[136:139]\n"
      "ds_read_b128 a[238:241], v11 offset:3072\n"
      "ds_write_addtid_b32 v78 offset:7744\n"
      "v_mfma_f32_16x16x32_bf16 v[140:143], a[52:55], a[228:231], v[140:143]\n"
      "ds_write_addtid_b32 v79 offset:10848\n"
      "ds_write_addtid_b32 v80 offset:13952\n"
      "v_cvt_pk_bf16_f32 v21, v21, v25\n"
      "v_mfma_f32_16x16x32_bf16 v[144:147], a[56:59], a[228:231], v[144:147]\n"
      "ds_write_addtid_b32 v21 offset:29440\n"
      "ds_write_addtid_b32 v81 offset:17056\n"
      "v_cvt_pk_bf16_f32 v22, v22, v26\n"
      "v_cvt_pk_bf16_f32 v23, v23, v27\n"
      "s_nop 0\n"
      "s_waitcnt lgkmcnt(8)\n"
      "v_mfma_f32_16x16x32_bf16 v[136:139], a[60:63], a[216:219], v[136:139]\n"
      "ds_write_addtid_b32 v22 offset:33024\n"
      "ds_write_addtid_b32 v23 offset:36608\n"
      "ds_write_addtid_b32 v82 offset:20160\n"
      "ds_write_addtid_b32 v83 offset:23264\n"
      "v_mfma_f32_16x16x32_bf16 v[140:143], a[64:67], a[216:219], v[140:143]\n"
      "v_mfma_f32_16x16x32_bf16 v[144:147], a[68:71], a[216:219], v[144:147]\n"
      "s_waitcnt lgkmcnt(0)\n"
      "s_barrier\n"
      "LSmid_%=:\n"
      "v_mfma_f32_16x16x32_bf16 v[136:139], a[72:75], a[220:223], v[136:139]\n"
      "ds_read_b128 a[216:219], v0 offset:0\n"
      "ds_read_b128 v[28:31], v12 offset:0\n"
      "v_mfma_f32_16x16x32_bf16 v[140:143], a[76:79], a[220:223], v[140:143]\n"
      "ds_read_b128 a[246:249], v15 offset:3072\n"
      "ds_read_b128 v[32:35], v12 offset:53504\n"
      "v_mfma_f32_16x16x32_bf16 v[144:147], a[80:83], a[220:223], v[144:147]\n"
      "ds_read_b128 a[220:223], v0 offset:32\n"
      "ds_read_b128 a[250:253], v16 offset:3072\n"
      "v_mfma_f32_4x4x4_16b_bf16 v[20:23], a[232:233], a[242:243], 0\n"
      "ds_read_b128 v[44:47], v12 offset:1280\n"
      "v_mfma_f32_4x4x4_16b_bf16 v[24:27], a[232:233], a[244:245], 0\n"
      "ds_read_b128 v[48:51], v12 offset:54784\n"
      "ds_read_b128 v[36:39], v12 offset:1024\n"
      "v_mfma_f32_16x16x32_bf16 v[136:139], a[84:87], a[224:227], v[136:139]\n"
      "ds_read_b64_tr_b16 a[242:243], v14 offset:1536\n"
      "ds_read_b64_tr_b16 a[244:245], v14 offset:1544\n"
      "v_mfma_f32_16x16x32_bf16 v[140:143], a[88:91], a[224:227], v[140:143]\n"
      "ds_read_b128 v[40:43], v12 offset:54528\n"
      "ds_write_addtid_b32 v84 offset:2048\n"
      "v_mfma_f32_16x16x32_bf16 v[144:147], a[92:95], a[224:227], v[144:147]\n"
      "ds_write_addtid_b32 v85 offset:5152\n"
      "ds_write_addtid_b32 v86 offset:8256\n"
      "v_mfma_f32_16x16x32_bf16 v[136:139], a[96:99], a[238:241], v[136:139]\n"
      "ds_read_b128 a[224:227], v0 offset:64\n"
      "ds_write_addtid_b32 v87 offset:11360\n"
      "v_mfma_f32_16x16x32_bf16 v[140:143], a[100:103], a[238:241], v[140:143]\n"
      "ds_write_addtid_b32 v88 offset:14464\n"
      "ds_write_addtid_b32 v89 offset:17568\n"
      "v_cvt_pk_bf16_f32 v20, v20, v24\n"
      "v_cvt_pk_bf16_f32 v21, v21, v25\n"
      "v_cvt_pk_bf16_f32 v22, v22, v26\n"
      "v_cvt_pk_bf16_f32 v23, v23, v27\n"
      "v_mfma_f32_16x16x32_bf16 v[144:147], a[104:107], a[238:241], v[144:147]\n"
      "ds_write_addtid_b32 v20 offset:39168\n"
      "ds_write_addtid_b32 v21 offset:42752\n"
      "ds_write_addtid_b32 v22 offset:46336\n"
      "ds_write_addtid_b32 v23 offset:49920\n"
      "s_waitcnt lgkmcnt(12)\n"
      "v_mfma_f32_4x4x4_16b_bf16 v[20:23], a[232:233], a[242:243], 0\n"
      "ds_write_addtid_b32 v90 offset:20672\n"
      "v_mfma_f32_4x4x4_16b_bf16 v[24:27], a[232:233], a[244:245], 0\n"
      "ds_write_addtid_b32 v91 offset:23776\n"
      "v_add_f32 v28, v28, v44\n"
      "v_add_f32_e64 v29, v29, v45\n"
      "v_mfma_f32_32x32x16_bf16 v[52:67], a[216:219], v[148:151], 0\n"
      "ds_read_b128 a[228:231], v1 offset:0\n"
      "ds_write_addtid_b32 v92 offset:2560\n"
      "ds_write_addtid_b32 v93 offset:5664\n"
      "v_add_f32 v30, v30, v46\n"
      "v_add_f32 v31, v31, v47\n"
      "v_cvt_pk_bf16_f32 v20, v20, v24\n"
      "v_mfma_f32_32x32x16_bf16 v[52:67], a[220:223], v[152:155], v[52:67]\n"
      "ds_write_addtid_b32 v20 offset:39680\n"
      "ds_read_b64_tr_b16 a[242:243], v14 offset:2560\n"
      "ds_read_b64_tr_b16 a[244:245], v14 offset:2568\n"
      "v_add_f32 v32, v32, v48\n"
      "v_add_f32 v33, v33, v49\n"
      "v_cvt_pk_bf16_f32 v21, v21, v25\n"
      "v_mfma_f32_32x32x16_bf16 v[68:83], a[216:219], a[108:111], 0\n"
      "buffer_store_dwordx4 a[246:249], v18, s[8:11], s60 offen offset:0 sc1\n"
      "v_mfma_f32_32x32x16_bf16 v[68:83], a[220:223], a[112:115], v[68:83]\n"
      "ds_write_addtid_b32 v94 offset:8768\n"
      "ds_write_addtid_b32 v95 offset:11872\n"
      "ds_write_addtid_b32 v96 offset:14976\n"
      "ds_write_addtid_b32 v97 offset:18080\n"
      "ds_write_addtid_b32 v98 offset:21184\n"
      "ds_write_addtid_b32 v99 offset:24288\n"
      "v_mfma_f32_32x32x16_bf16 v[84:99], a[216:219], v[220:223], 0\n"
      "ds_read_b128 a[216:219], v1 offset:32\n"
      "ds_write_addtid_b32 v21 offset:43264\n"
      "ds_read_b128 a[246:249], v17 offset:3072\n"
      "v_add_f32 v34, v34, v50\n"
      "v_add_f32 v35, v35, v51\n"
      "v_sub_f32 v32, v32, v28\n"
      "v_mfma_f32_32x32x16_bf16 v[84:99], a[220:223], v[224:227], v[84:99]\n"
      "ds_read_b128 v[44:47], v12 offset:256\n"
      "ds_read_b128 v[48:51], v12 offset:53760\n"
      "v_sub_f32 v33, v33, v29\n"
      "v_sub_f32 v34, v34, v30\n"
      "v_sub_f32_e64 v35, v35, v31\n"
      "s_waitcnt lgkmcnt(11)\n"
      "v_mfma_f32_32x32x16_bf16 v[52:67], a[224:227], v[156:159], v[52:67]\n"
      "buffer_store_dwordx4 a[250:253], v18, s[8:11], s60 offen offset:1024 sc1\n"
      "ds_read_b128 a[220:223], v1 offset:64\n"
      "v_mfma_f32_32x32x16_bf16 v[68:83], a[224:227], a[116:119], v[68:83]\n"
      "v_cvt_pk_bf16_f32 v22, v22, v26\n"
      "v_cvt_pk_bf16_f32 v23, v23, v27\n"
      "v_fmac_f32 v28, s45, v32\n"
      "v_fmac_f32 v29, s45, v33\n"
      "v_fmac_f32 v30, s45, v34\n"
      "v_fmac_f32 v31, s45, v35\n"
      "v_mfma_f32_32x32x16_bf16 v[84:99], a[224:227], v[228:231], v[84:99]\n"
      "ds_write_addtid_b32 v22 offset:46848\n"
      "ds_write_addtid_b32 v23 offset:50432\n"
      "ds_read_b128 a[250:253], v15 offset:9216\n"
      "v_fmac_f32 v28, s46, v100\n"
      "v_fmac_f32 v29, s46, v101\n"
      "s_waitcnt lgkmcnt(4)\n"
      "v_mfma_f32_4x4x4_16b_bf16 v[20:23], a[232:233], a[242:243], 0\n"
      "v_fmac_f32 v30, s46, v102\n"
      "v_mfma_f32_4x4x4_16b_bf16 v[24:27], a[232:233], a[244:245], 0\n"
      "v_fmac_f32 v31, s46, v103\n"
      "v_fmac_f32 v32, s48, v100\n"
      "v_fmac_f32 v33, s48, v101\n"
      "v_mfma_f32_32x32x16_bf16 v[52:67], a[228:231], v[160:163], v[52:67]\n"
      "buffer_store_dwordx4 a[246:249], v18, s[8:11], s60 offen offset:2048 sc1\n"
      "ds_read_b128 a[224:227], v2 offset:0\n"
      "v_mfma_f32_32x32x16_bf16 v[68:83], a[228:231], a[120:123], v[68:83]\n"
      "ds_read_b64_tr_b16 a[242:243], v14 offset:3584\n"
      "ds_read_b64_tr_b16 a[244:245], v14 offset:3592\n"
      "ds_read_b128 a[246:249], v16 offset:9216\n"
      "v_fmac_f32 v34, s48, v102\n"
      "v_fmac_f32 v35, s48, v103\n"
      "v_cvt_pk_bf16_f32 v28, v28, v29\n"
      "v_mfma_f32_32x32x16_bf16 v[84:99], a[228:231], v[232:235], v[84:99]\n"
      "v_cvt_pk_bf16_f32 v29, v30, v31\n"
      "v_fma_f32 v32, s47, v32, v124\n"
      "v_cvt_pk_bf16_f32 v20, v20, v24\n"
      "v_fma_f32 v33, s47, v33, v125\n"
      "v_cvt_pk_bf16_f32 v21, v21, v25\n"
      "s_waitcnt lgkmcnt(4)\n"
      "v_mfma_f32_32x32x16_bf16 v[52:67], a[216:219], v[164:167], v[52:67]\n"
      "ds_read_b128 a[228:231], v2 offset:32\n"
      "ds_write_addtid_b32 v20 offset:40192\n"
      "ds_write_b64 v13, v[28:29] offset:0\n"
      "v_fma_f32 v34, s47, v34, v126\n"
      "v_mfma_f32_32x32x16_bf16 v[68:83], a[216:219], a[124:127], v[68:83]\n"
      "buffer_store_dwordx4 a[250:253], v18, s[8:11], s61 offen offset:0 sc1\n"
      "ds_write_addtid_b32 v21 offset:43776\n"
      "v_cvt_pk_bf16_f32 v22, v22, v26\n"
      "v_cvt_pk_bf16_f32 v23, v23, v27\n"
      "v_mfma_f32_32x32x16_bf16 v[84:99], a[216:219], v[236:239], v[84:99]\n"
      "ds_write_addtid_b32 v22 offset:47360\n"
      "ds_write_addtid_b32 v23 offset:50944\n"
      "ds_read_b128 a[250:253], v17 offset:9216\n"
      "v_fma_f32 v35, s47, v35, v127\n"
      "v_cvt_pk_bf16_f32 v32, v32, v33\n"
      "v_cvt_pk_bf16_f32 v33, v34, v35\n"
      "s_waitcnt lgkmcnt(4)\n"
      "v_mfma_f32_4x4x4_16b_bf16 v[20:23], a[232:233], a[242:243], 0\n"
      "v_mfma_f32_4x4x4_16b_bf16 v[24:27], a[232:233], a[244:245], 0\n"
      "v_add_f32 v36, v36, v44\n"
      "v_add_f32 v37, v37, v45\n"
      "v_add_f32 v38, v38, v46\n"
      "v_mfma_f32_32x32x16_bf16 v[52:67], a[220:223], v[168:171], v[52:67]\n"
      "ds_read_b128 a[216:219], v2 offset:64\n"
      "ds_write_b64 v13, v[32:33] offset:12288\n"
      "v_add_f32 v39, v39, v47\n"
      "v_cvt_pk_bf16_f32 v20, v20, v24\n"
      "v_mfma_f32_32x32x16_bf16 v[68:83], a[220:223], a[128:131], v[68:83]\n"
      "ds_write_addtid_b32 v20 offset:40704\n"
      "ds_read_b128 v[28:31], v12 offset:512\n"
      "ds_read_b128 v[32:35], v12 offset:54016\n"
      "v_add_f32 v40, v40, v48\n"
      "v_add_f32_e64 v41, v41, v49\n"
      "v_cvt_pk_bf16_f32 v21, v21, v25\n"
      "v_mfma_f32_32x32x16_bf16 v[84:99], a[220:223], v[240:243], v[84:99]\n"
      "buffer_store_dwordx4 a[246:249], v18, s[8:11], s61 offen offset:1024 sc1\n"
      "v_cvt_pk_bf16_f32 v22, v22, v26\n"
      "v_cvt_pk_bf16_f32 v23, v23, v27\n"
      "v_mfma_f32_32x32x16_bf16 v[52:67], a[224:227], v[172:175], v[52:67]\n"
      "ds_write_addtid_b32 v21 offset:44288\n"
      "ds_write_addtid_b32 v22 offset:47872\n"
      "ds_write_addtid_b32 v23 offset:51456\n"
      "v_add_f32 v42, v42, v50\n"
      "v_add_f32 v43, v43, v51\n"
      "v_sub_f32 v40, v40, v36\n"
      "v_mfma_f32_32x32x16_bf16 v[68:83], a[224:227], a[132:135], v[68:83]\n"
      "ds_read_b128 v[44:47], v12 offset:1792\n"
      "ds_read_b128 v[48:51], v12 offset:55296\n"
      "v_sub_f32 v41, v41, v37\n"
      "v_sub_f32 v42, v42, v38\n"
      "s_waitcnt lgkmcnt(5)\n"
      "v_sub_f32_e64 v43, v43, v39\n"
      "v_mfma_f32_32x32x16_bf16 v[84:99], a[224:227], v[244:247], v[84:99]\n"
      "buffer_store_dwordx4 a[250:253], v18, s[8:11], s61 offen offset:2048 sc1\n"
      "s_waitcnt lgkmcnt(2)\n"
      "s_barrier\n"
      "v_mfma_f32_32x32x16_bf16 v[52:67], a[228:231], v[176:179], v[52:67]\n"
      "ds_read_b128 a[220:223], v0 offset:14336\n"
      "v_fmac_f32 v36, s45, v40\n"
      "v_fmac_f32 v37, s45, v41\n"
      "v_fmac_f32 v38, s45, v42\n"
      "v_fmac_f32 v39, s45, v43\n"
      "v_fmac_f32 v36, s46, v104\n"
      "v_fmac_f32 v37, s46, v105\n"
      "v_mfma_f32_32x32x16_bf16 v[68:83], a[228:231], a[136:139], v[68:83]\n"
      "ds_read_b128 a[224:227], v0 offset:14368\n"
      "v_fmac_f32 v38, s46, v106\n"
      "v_fmac_f32 v39, s46, v107\n"
      "v_fmac_f32 v40, s48, v104\n"
      "v_fmac_f32 v41, s48, v105\n"
      "v_fmac_f32 v42, s48, v106\n"
      "v_fmac_f32 v43, s48, v107\n"
      "v_mfma_f32_32x32x16_bf16 v[84:99], a[228:231], v[248:251], v[84:99]\n"
      "v_cvt_pk_bf16_f32 v36, v36, v37\n"
      "v_cvt_pk_bf16_f32 v37, v38, v39\n"
      "v_fma_f32 v40, s47, v40, v128\n"
      "v_fma_f32 v41, s47, v41, v129\n"
      "v_fma_f32 v42, s47, v42, v130\n"
      "v_fma_f32 v43, s47, v43, v131\n"
      "v_cvt_pk_bf16_f32 v40, v40, v41\n"
      "v_mfma_f32_32x32x16_bf16 v[52:67], a[216:219], v[180:183], v[52:67]\n"
      "ds_read_b128 a[228:231], v0 offset:14400\n"
      "ds_write_b64 v13, v[36:37] offset:64\n"
      "v_cvt_pk_bf16_f32 v41, v42, v43\n"
      "s_waitcnt lgkmcnt(2)\n"
      "v_add_f32 v28, v28, v44\n"
      "v_mfma_f32_32x32x16_bf16 v[68:83], a[216:219], a[140:143], v[68:83]\n"
      "ds_write_b64 v13, v[40:41] offset:12352\n"
      "v_add_f32 v29, v29, v45\n"
      "v_add_f32 v30, v30, v46\n"
      "v_add_f32 v31, v31, v47\n"
      "v_add_f32 v32, v32, v48\n"
      "v_mfma_f32_32x32x16_bf16 v[84:99], a[216:219], v[252:255], v[84:99]\n"
      "ds_read_b128 v[36:39], v12 offset:2048\n"
      "ds_read_b128 v[40:43], v12 offset:55552\n"
      "v_add_f32 v33, v33, v49\n"
      "v_add_f32 v34, v34, v50\n"
      "v_add_f32 v35, v35, v51\n"
      "v_sub_f32 v32, v32, v28\n"
      "v_mfma_f32_32x32x16_bf16 v[52:67], a[220:223], v[184:187], v[52:67]\n"
      "ds_read_b128 a[216:219], v1 offset:14336\n"
      "ds_read_b128 v[44:47], v12 offset:2304\n"
      "ds_read_b128 v[48:51], v12 offset:55808\n"
      "v_sub_f32 v33, v33, v29\n"
      "v_sub_f32 v34, v34, v30\n"
      "v_sub_f32_e64 v35, v35, v31\n"
      "v_mfma_f32_32x32x16_bf16 v[68:83], a[220:223], a[144:147], v[68:83]\n"
      "v_fmac_f32 v28, s45, v32\n"
      "v_fmac_f32 v29, s45, v33\n"
      "v_fmac_f32 v30, s45, v34\n"
      "v_fmac_f32 v31, s45, v35\n"
      "v_fmac_f32 v28, s46, v112\n"
      "v_fmac_f32 v29, s46, v113\n"
      "v_fmac_f32 v30, s46, v114\n"
      "v_mfma_f32_32x32x16_bf16 v[84:99], a[220:223], a[180:183], v[84:99]\n"
      "v_fmac_f32 v31, s46, v115\n"
      "v_fmac_f32 v32, s48, v112\n"
      "v_fmac_f32 v33, s48, v113\n"
      "v_fmac_f32 v34, s48, v114\n"
      "v_fmac_f32 v35, s48, v115\n"
      "v_cvt_pk_bf16_f32 v28, v28, v29\n"
      "v_cvt_pk_bf16_f32 v29, v30, v31\n"
      "v_mfma_f32_32x32x16_bf16 v[52:67], a[224:227], v[188:191], v[52:67]\n"
      "ds_read_b128 a[220:223], v1 offset:14368\n"
      "ds_write_b64 v13, v[28:29] offset:3072\n"
      "v_fma_f32 v32, s47, v32, v136\n"
      "v_fma_f32 v33, s47, v33, v137\n"
      "v_fma_f32 v34, s47, v34, v138\n"
      "v_mfma_f32_32x32x16_bf16 v[68:83], a[224:227], a[148:151], v[68:83]\n"
      "v_fma_f32 v35, s47, v35, v139\n"
      "v_cvt_pk_bf16_f32 v32, v32, v33\n"
      "v_cvt_pk_bf16_f32 v33, v34, v35\n"
      "s_waitcnt lgkmcnt(2)\n"
      "v_add_f32 v36, v36, v44\n"
      "v_add_f32 v37, v37, v45\n"
      "v_add_f32 v38, v38, v46\n"
      "v_mfma_f32_32x32x16_bf16 v[84:99], a[224:227], a[184:187], v[84:99]\n"
      "ds_write_b64 v13, v[32:33] offset:15360\n"
      "v_add_f32 v39, v39, v47\n"
      "v_add_f32 v40, v40, v48\n"
      "v_add_f32 v41, v41, v49\n"
      "v_mfma_f32_32x32x16_bf16 v[52:67], a[228:231], v[192:195], v[52:67]\n"
      "ds_read_b128 a[224:227], v1 offset:14400\n"
      "ds_read_b128 v[28:31], v12 offset:1536\n"
      "ds_read_b128 v[32:35], v12 offset:55040\n"
      "v_add_f32 v42, v42, v50\n"
      "v_add_f32 v43, v43, v51\n"
      "v_sub_f32 v40, v40, v36\n"
      "v_sub_f32_e64 v41, v41, v37\n"
      "v_mfma_f32_32x32x16_bf16 v[68:83], a[228:231], a[152:155], v[68:83]\n"
      "ds_read_b128 v[44:47], v12 offset:768\n"
      "ds_read_b128 v[48:51], v12 offset:54272\n"
      "v_sub_f32 v42, v42, v38\n"
      "v_sub_f32 v43, v43, v39\n"
      "v_fmac_f32 v36, s45, v40\n"
      "v_fmac_f32 v37, s45, v41\n"
      "v_fmac_f32 v38, s45, v42\n"
      "v_mfma_f32_32x32x16_bf16 v[84:99], a[228:231], a[188:191], v[84:99]\n"
      "v_fmac_f32 v39, s45, v43\n"
      "v_fmac_f32 v36, s46, v108\n"
      "v_fmac_f32 v37, s46, v109\n"
      "v_fmac_f32 v38, s46, v110\n"
      "v_fmac_f32 v39, s46, v111\n"
      "v_fmac_f32 v40, s48, v108\n"
      "v_fmac_f32 v41, s48, v109\n"
      "v_mfma_f32_32x32x16_bf16 v[52:67], a[216:219], v[196:199], v[52:67]\n"
      "ds_read_b128 a[228:231], v2 offset:14336\n"
      "v_fmac_f32 v42, s48, v110\n"
      "v_fmac_f32 v43, s48, v111\n"
      "v_cvt_pk_bf16_f32 v36, v36, v37\n"
      "v_cvt_pk_bf16_f32 v37, v38, v39\n"
      "s_add_u32 s58, s58, 786432\n"
      "v_fma_f32 v40, s47, v40, v132\n"
      "v_mfma_f32_32x32x16_bf16 v[68:83], a[216:219], a[156:159], v[68:83]\n"
      "ds_write_b64 v13, v[36:37] offset:128\n"
      "v_fma_f32 v41, s47, v41, v133\n"
      "v_fma_f32 v42, s47, v42, v134\n"
      "v_fma_f32 v43, s47, v43, v135\n"
      "v_cvt_pk_bf16_f32 v40, v40, v41\n"
      "v_mfma_f32_32x32x16_bf16 v[84:99], a[216:219], a[192:195], v[84:99]\n"
      "v_cvt_pk_bf16_f32 v41, v42, v43\n"
      "s_waitcnt lgkmcnt(1)\n"
      "v_add_f32 v28, v28, v44\n"
      "v_add_f32 v29, v29, v45\n"
      "v_add_f32 v30, v30, v46\n"
      "v_add_f32 v31, v31, v47\n"
      "s_add_u32 s59, s58, 196608\n"
      "v_mfma_f32_32x32x16_bf16 v[52:67], a[220:223], v[200:203], v[52:67]\n"
      "ds_read_b128 a[216:219], v2 offset:14368\n"
      "ds_write_b64 v13, v[40:41] offset:12416\n"
      "v_add_f32 v32, v32, v48\n"
      "v_add_f32 v33, v33, v49\n"
      "v_add_f32 v34, v34, v50\n"
      "v_add_f32 v35, v35, v51\n"
      "v_mfma_f32_32x32x16_bf16 v[68:83], a[220:223], a[160:163], v[68:83]\n"
      "ds_read_b128 v[36:39], v12 offset:2560\n"
      "ds_read_b128 v[40:43], v12 offset:56064\n"
      "ds_read_b128 v[44:47], v12 offset:2816\n"
      "ds_read_b128 v[48:51], v12 offset:56320\n"
      "v_sub_f32 v32, v32, v28\n"
      "v_sub_f32_e64 v33, v33, v29\n"
      "v_mfma_f32_32x32x16_bf16 v[84:99], a[220:223], a[196:199], v[84:99]\n"
      "ds_read_b128 a[220:223], v2 offset:14400\n"
      "v_sub_f32 v34, v34, v30\n"
      "v_sub_f32 v35, v35, v31\n"
      "v_fmac_f32 v28, s45, v32\n"
      "v_fmac_f32 v29, s45, v33\n"
      "s_add_u32 s60, s58, 3072\n"
      "v_mfma_f32_32x32x16_bf16 v[52:67], a[224:227], v[204:207], v[52:67]\n"
      "s_add_u32 s61, s59, 3072\n"
      "v_fmac_f32 v30, s45, v34\n"
      "v_fmac_f32 v31, s45, v35\n"
      "v_fmac_f32 v28, s46, v116\n"
      "v_fmac_f32 v29, s46, v117\n"
      "v_fmac_f32 v30, s46, v118\n"
      "v_fmac_f32 v31, s46, v119\n"
      "s_waitcnt lgkmcnt(1)\n"
      "s_barrier\n"
      "v_mfma_f32_32x32x16_bf16 v[68:83], a[224:227], a[164:167], v[68:83]\n"
      "ds_read_b64_tr_b16 a[242:243], v14 offset:29184\n"
      "ds_read_b128 a[246:249], v15 offset:0\n"
      "ds_read_b64_tr_b16 a[244:245], v14 offset:29192\n"
      "v_fmac_f32 v32, s48, v116\n"
      "v_fmac_f32 v33, s48, v117\n"
      "v_fmac_f32 v34, s48, v118\n"
      "v_fmac_f32 v35, s48, v119\n"
      "v_mfma_f32_32x32x16_bf16 v[84:99], a[224:227], a[200:203], v[84:99]\n"
      "ds_read_b128 a[250:253], v16 offset:0\n"
      "v_cvt_pk_bf16_f32 v28, v28, v29\n"
      "v_cvt_pk_bf16_f32 v29, v30, v31\n"
      "v_fma_f32 v32, s47, v32, v140\n"
      "v_fma_f32 v33, s47, v33, v141\n"
      "v_fma_f32 v34, s47, v34, v142\n"
      "v_mfma_f32_32x32x16_bf16 v[52:67], a[228:231], v[208:211], v[52:67]\n"
      "ds_write_b64 v13, v[28:29] offset:3136\n"
      "s_waitcnt lgkmcnt(2)\n"
      "v_fma_f32 v35, s47, v35, v143\n"
      "v_cvt_pk_bf16_f32 v32, v32, v33\n"
      "v_mfma_f32_32x32x16_bf16 v[68:83], a[228:231], a[168:171], v[68:83]\n"
      "buffer_store_dwordx4 a[246:249], v18, s[8:11], s58 offen offset:0 sc1\n"
      "s_waitcnt lgkmcnt(1)\n"
      "v_cvt_pk_bf16_f32 v33, v34, v35\n"
      "v_mfma_f32_4x4x4_16b_bf16 v[20:23], a[232:233], a[242:243], 0\n"
      "v_mfma_f32_4x4x4_16b_bf16 v[24:27], a[232:233], a[244:245], 0\n"
      "buffer_store_dwordx4 a[250:253], v18, s[8:11], s58 offen offset:1024 sc1\n"
      "v_mfma_f32_32x32x16_bf16 v[84:99], a[228:231], a[204:207], v[84:99]\n"
      "ds_read_b128 a[246:249], v17 offset:0\n"
      "ds_read_b128 a[250:253], v15 offset:6144\n"
      "ds_write_b64 v13, v[32:33] offset:15424\n"
      "v_cvt_pk_bf16_f32 v20, v20, v24\n"
      "v_mfma_f32_32x32x16_bf16 v[52:67], a[216:219], v[212:215], v[52:67]\n"
      "ds_read_b128 a[224:227], v3 offset:29184\n"
      "ds_write_addtid_b32 v20 offset:24832\n"
      "v_add_f32 v36, v36, v44\n"
      "v_add_f32 v37, v37, v45\n"
      "v_add_f32 v38, v38, v46\n"
      "v_cvt_pk_bf16_f32 v21, v21, v25\n"
      "v_mfma_f32_32x32x16_bf16 v[52:67], a[220:223], v[216:219], v[52:67]\n"
      "ds_write_addtid_b32 v21 offset:28416\n"
      "s_waitcnt lgkmcnt(5)\n"
      "buffer_store_dwordx4 a[246:249], v18, s[8:11], s58 offen offset:2048 sc1\n"
      "v_mfma_f32_32x32x16_bf16 v[68:83], a[216:219], a[172:175], v[68:83]\n"
      "ds_read_b128 a[228:231], v4 offset:29184\n"
      "ds_read_b128 a[246:249], v16 offset:6144\n"
      "s_waitcnt lgkmcnt(3)\n"
      "v_add_f32 v39, v39, v47\n"
      "v_add_f32 v40, v40, v48\n"
      "v_add_f32 v41, v41, v49\n"
      "v_mfma_f32_32x32x16_bf16 v[68:83], a[220:223], a[176:179], v[68:83]\n"
      "buffer_store_dwordx4 a[250:253], v18, s[8:11], s59 offen offset:0 sc1\n"
      "v_mfma_f32_32x32x16_bf16 v[84:99], a[216:219], a[208:211], v[84:99]\n"
      "ds_read_b128 a[216:219], v5 offset:29184\n"
      "ds_read_b128 a[250:253], v17 offset:6144\n"
      "ds_write_addtid_b32 v52 offset:0\n"
      "v_add_f32 v42, v42, v50\n"
      "s_waitcnt lgkmcnt(3)\n"
      "v_add_f32_e64 v43, v43, v51\n"
      "v_mfma_f32_32x32x16_bf16 v[84:99], a[220:223], a[212:215], v[84:99]\n"
      "ds_read_b128 a[220:223], v6 offset:29184\n"
      "ds_write_addtid_b32 v53 offset:3104\n"
      "v_sub_f32_e64 v40, v40, v36\n"
      "v_cvt_pk_bf16_f32 v22, v22, v26\n"
      "v_sub_f32_e64 v41, v41, v37\n"
      "v_cvt_pk_bf16_f32 v23, v23, v27\n"
      "v_mfma_f32_16x16x32_bf16 v[100:103], a[0:3], a[224:227], 0\n"
      "buffer_store_dwordx4 a[246:249], v18, s[8:11], s59 offen offset:1024 sc1\n"
      "v_mfma_f32_16x16x32_bf16 v[104:107], a[4:7], a[224:227], 0\n"
      "ds_write_addtid_b32 v22 offset:32000\n"
      "ds_write_addtid_b32 v23 offset:35584\n"
      "v_mfma_f32_16x16x32_bf16 v[108:111], a[8:11], a[224:227], 0\n"
      "ds_read_b128 a[224:227], v7 offset:29184\n"
      "ds_read_b64_tr_b16 a[242:243], v14 offset:30208\n"
      "v_mfma_f32_16x16x32_bf16 v[100:103], a[12:15], a[228:231], v[100:103]\n"
      "ds_read_b64_tr_b16 a[244:245], v14 offset:30216\n"
      "ds_write_addtid_b32 v54 offset:6208\n"
      "v_mfma_f32_16x16x32_bf16 v[104:107], a[16:19], a[228:231], v[104:107]\n"
      "ds_write_addtid_b32 v55 offset:9312\n"
      "v_sub_f32 v42, v42, v38\n"
      "v_mfma_f32_16x16x32_bf16 v[108:111], a[20:23], a[228:231], v[108:111]\n"
      "ds_read_b128 a[228:231], v8 offset:29184\n"
      "s_waitcnt lgkmcnt(8)\n"
      "v_mfma_f32_16x16x32_bf16 v[100:103], a[24:27], a[216:219], v[100:103]\n"
      "v_sub_f32_e64 v43, v43, v39\n"
      "v_fmac_f32_e64 v36, s45, v40\n"
      "v_mfma_f32_16x16x32_bf16 v[104:107], a[28:31], a[216:219], v[104:107]\n"
      "v_fmac_f32 v37, s45, v41\n"
      "v_fmac_f32_e64 v38, s45, v42\n"
      "v_mfma_f32_16x16x32_bf16 v[108:111], a[32:35], a[216:219], v[108:111]\n"
      "ds_read_b128 a[216:219], v9 offset:29184\n"
      "ds_write_addtid_b32 v56 offset:12416\n"
      "v_mfma_f32_16x16x32_bf16 v[100:103], a[36:39], a[220:223], v[100:103]\n"
      "buffer_store_dwordx4 a[250:253], v18, s[8:11], s59 offen offset:2048 sc1\n"
      "s_waitcnt lgkmcnt(2)\n"
      "v_mfma_f32_4x4x4_16b_bf16 v[20:23], a[232:233], a[242:243], 0\n"
      "v_fmac_f32_e64 v39, s45, v43\n"
      "v_mfma_f32_4x4x4_16b_bf16 v[24:27], a[232:233], a[244:245], 0\n"
      "v_fmac_f32_e64 v36, s46, v120\n"
      "v_fmac_f32_e64 v37, s46, v121\n"
      "v_fmac_f32_e64 v38, s46, v122\n"
      "v_mfma_f32_16x16x32_bf16 v[104:107], a[40:43], a[220:223], v[104:107]\n"
      "ds_write_addtid_b32 v57 offset:15520\n"
      "v_cvt_pk_bf16_f32 v20, v20, v24\n"
      "v_mfma_f32_16x16x32_bf16 v[108:111], a[44:47], a[220:223], v[108:111]\n"
      "ds_read_b128 a[220:223], v10 offset:29184\n"
      "ds_write_addtid_b32 v20 offset:25344\n"
      "v_mfma_f32_16x16x32_bf16 v[100:103], a[48:51], a[224:227], v[100:103]\n"
      "ds_write_addtid_b32 v58 offset:18624\n"
      "v_cvt_pk_bf16_f32 v21, v21, v25\n"
      "v_mfma_f32_16x16x32_bf16 v[104:107], a[52:55], a[224:227], v[104:107]\n"
      "ds_write_addtid_b32 v21 offset:28928\n"
      "v_fmac_f32_e64 v39, s46, v123\n"
      "v_mfma_f32_16x16x32_bf16 v[108:111], a[56:59], a[224:227], v[108:111]\n"
      "ds_read_b128 a[224:227], v11 offset:29184\n"
      "ds_write_addtid_b32 v59 offset:21728\n"
      "v_mfma_f32_16x16x32_bf16 v[100:103], a[60:63], a[228:231], v[100:103]\n"
      "v_fmac_f32_e64 v40, s48, v120\n"
      "v_fmac_f32_e64 v41, s48, v121\n"
      "v_mfma_f32_16x16x32_bf16 v[104:107], a[64:67], a[228:231], v[104:107]\n"
      "ds_write_addtid_b32 v60 offset:512\n"
      "v_fmac_f32 v42, s48, v122\n"
      "v_mfma_f32_16x16x32_bf16 v[108:111], a[68:71], a[228:231], v[108:111]\n"
      "ds_read_b128 a[228:231], v3 offset:32256\n"
      "s_waitcnt lgkmcnt(4)\n"
      "v_mfma_f32_16x16x32_bf16 v[100:103], a[72:75], a[216:219], v[100:103]\n"
      "ds_write_addtid_b32 v61 offset:3616\n"
      "v_fmac_f32_e64 v43, s48, v123\n"
      "v_mfma_f32_16x16x32_bf16 v[104:107], a[76:79], a[216:219], v[104:107]\n"
      "ds_write_addtid_b32 v62 offset:6720\n"
      "v_cvt_pk_bf16_f32 v36, v36, v37\n"
      "v_mfma_f32_16x16x32_bf16 v[108:111], a[80:83], a[216:219], v[108:111]\n"
      "ds_read_b128 a[216:219], v4 offset:32256\n"
      "v_cvt_pk_bf16_f32 v37, v38, v39\n"
      "v_mfma_f32_16x16x32_bf16 v[100:103], a[84:87], a[220:223], v[100:103]\n"
      "ds_write_b64 v13, v[36:37] offset:3200\n"
      "v_mfma_f32_16x16x32_bf16 v[104:107], a[88:91], a[220:223], v[104:107]\n"
      "v_fma_f32 v40, s47, v40, v144\n"
      "v_fma_f32 v41, s47, v41, v145\n"
      "v_mfma_f32_16x16x32_bf16 v[108:111], a[92:95], a[220:223], v[108:111]\n"
      "ds_read_b128 a[220:223], v5 offset:32256\n"
      "s_nop 0\n"
      "s_waitcnt lgkmcnt(3)\n"
      "v_mfma_f32_16x16x32_bf16 v[100:103], a[96:99], a[224:227], v[100:103]\n"
      "ds_write_addtid_b32 v63 offset:9824\n"
      "ds_write_addtid_b32 v64 offset:12928\n"
      "v_mfma_f32_16x16x32_bf16 v[104:107], a[100:103], a[224:227], v[104:107]\n"
      "ds_write_addtid_b32 v65 offset:16032\n"
      "v_fma_f32 v42, s47, v42, v146\n"
      "v_mfma_f32_16x16x32_bf16 v[108:111], a[104:107], a[224:227], v[108:111]\n"
      "ds_read_b128 a[224:227], v6 offset:32256\n"
      "v_fma_f32 v43, s47, v43, v147\n"
      "v_cvt_pk_bf16_f32 v40, v40, v41\n"
      "v_cvt_pk_bf16_f32 v41, v42, v43\n"
      "v_mfma_f32_16x16x32_bf16 v[112:115], a[0:3], a[228:231], 0\n"
      "ds_write_b64 v13, v[40:41] offset:15488\n"
      "v_mfma_f32_16x16x32_bf16 v[116:119], a[4:7], a[228:231], 0\n"
      "v_cvt_pk_bf16_f32 v22, v22, v26\n"
      "v_cvt_pk_bf16_f32 v23, v23, v27\n"
      "v_mfma_f32_16x16x32_bf16 v[120:123], a[8:11], a[228:231], 0\n"
      "ds_read_b128 a[228:231], v7 offset:32256\n"
      "s_nop 0\n"
      "s_waitcnt lgkmcnt(3)\n"
      "v_mfma_f32_16x16x32_bf16 v[112:115], a[12:15], a[216:219], v[112:115]\n"
      "ds_read_b64_tr_b16 a[242:243], v14 offset:31232\n"
      "ds_read_b64_tr_b16 a[244:245], v14 offset:31240\n"
      "v_mfma_f32_16x16x32_bf16 v[116:119], a[16:19], a[216:219], v[116:119]\n"
      "ds_write_addtid_b32 v22 offset:32512\n"
      "ds_write_addtid_b32 v23 offset:36096\n"
      "v_mfma_f32_16x16x32_bf16 v[120:123], a[20:23], a[216:219], v[120:123]\n"
      "ds_read_b128 a[216:219], v8 offset:32256\n"
      "ds_write_addtid_b32 v66 offset:19136\n"
      "v_mfma_f32_16x16x32_bf16 v[112:115], a[24:27], a[220:223], v[112:115]\n"
      "ds_write_addtid_b32 v67 offset:22240\n"
      "ds_write_addtid_b32 v68 offset:1024\n"
      "v_mfma_f32_16x16x32_bf16 v[116:119], a[28:31], a[220:223], v[116:119]\n"
      "ds_write_addtid_b32 v69 offset:4128\n"
      "ds_write_addtid_b32 v70 offset:7232\n"
      "v_mfma_f32_16x16x32_bf16 v[120:123], a[32:35], a[220:223], v[120:123]\n"
      "ds_read_b128 a[220:223], v9 offset:32256\n"
      "ds_write_addtid_b32 v71 offset:10336\n"
      "s_nop 0\n"
      "s_waitcnt lgkmcnt(9)\n"
      "v_mfma_f32_4x4x4_16b_bf16 v[20:23], a[232:233], a[242:243], 0\n"
      "ds_write_addtid_b32 v72 offset:13440\n"
      "ds_write_addtid_b32 v73 offset:16544\n"
      "v_mfma_f32_4x4x4_16b_bf16 v[24:27], a[232:233], a[244:245], 0\n"
      "ds_write_addtid_b32 v74 offset:19648\n"
      "ds_write_addtid_b32 v75 offset:22752\n"
      "v_mfma_f32_16x16x32_bf16 v[112:115], a[36:39], a[224:227], v[112:115]\n"
      "ds_read_b64_tr_b16 a[242:243], v14 offset:29696\n"
      "ds_read_b64_tr_b16 a[244:245], v14 offset:29704\n"
      "v_cvt_pk_bf16_f32 v20, v20, v24\n"
      "v_mfma_f32_16x16x32_bf16 v[116:119], a[40:43], a[224:227], v[116:119]\n"
      "ds_write_addtid_b32 v20 offset:25856\n"
      "ds_write_addtid_b32 v76 offset:1536\n"
      "v_mfma_f32_16x16x32_bf16 v[120:123], a[44:47], a[224:227], v[120:123]\n"
      "ds_read_b128 a[224:227], v10 offset:32256\n"
      "ds_write_addtid_b32 v77 offset:4640\n"
      "v_mfma_f32_16x16x32_bf16 v[112:115], a[48:51], a[228:231], v[112:115]\n"
      "ds_read_b128 a[238:241], v11 offset:32256\n"
      "ds_write_addtid_b32 v78 offset:7744\n"
      "v_mfma_f32_16x16x32_bf16 v[116:119], a[52:55], a[228:231], v[116:119]\n"
      "ds_write_addtid_b32 v79 offset:10848\n"
      "ds_write_addtid_b32 v80 offset:13952\n"
      "v_cvt_pk_bf16_f32 v21, v21, v25\n"
      "v_mfma_f32_16x16x32_bf16 v[120:123], a[56:59], a[228:231], v[120:123]\n"
      "ds_write_addtid_b32 v21 offset:29440\n"
      "ds_write_addtid_b32 v81 offset:17056\n"
      "v_cvt_pk_bf16_f32 v22, v22, v26\n"
      "v_cvt_pk_bf16_f32 v23, v23, v27\n"
      "s_waitcnt lgkmcnt(8)\n"
      "v_mfma_f32_16x16x32_bf16 v[112:115], a[60:63], a[216:219], v[112:115]\n"
      "ds_write_addtid_b32 v22 offset:33024\n"
      "ds_write_addtid_b32 v23 offset:36608\n"
      "ds_write_addtid_b32 v82 offset:20160\n"
      "ds_write_addtid_b32 v83 offset:23264\n"
      "v_mfma_f32_16x16x32_bf16 v[116:119], a[64:67], a[216:219], v[116:119]\n"
      "v_mfma_f32_16x16x32_bf16 v[120:123], a[68:71], a[216:219], v[120:123]\n"
      "s_waitcnt lgkmcnt(0)\n"
      "s_barrier\n"
      "s_add_u32 s55, s55, 2\n"
      "s_cmp_lt_u32 s55, 6\n"
      "s_cbranch_scc1 LSloop_%=\n"
      "LSlast_%=:\n"
      "v_mfma_f32_16x16x32_bf16 v[112:115], a[72:75], a[220:223], v[112:115]\n"
      "ds_read_b128 a[216:219], v0 offset:0\n"
      "ds_read_b128 v[28:31], v12 offset:0\n"
      "v_mfma_f32_16x16x32_bf16 v[116:119], a[76:79], a[220:223], v[116:119]\n"
      "ds_read_b128 a[246:249], v15 offset:3072\n"
      "ds_read_b128 v[32:35], v12 offset:53504\n"
      "v_mfma_f32_16x16x32_bf16 v[120:123], a[80:83], a[220:223], v[120:123]\n"
      "ds_read_b128 a[220:223], v0 offset:32\n"
      "ds_read_b128 a[250:253], v16 offset:3072\n"
      "v_mfma_f32_4x4x4_16b_bf16 v[20:23], a[232:233], a[242:243], 0\n"
      "ds_read_b128 v[44:47], v12 offset:1280\n"
      "v_mfma_f32_4x4x4_16b_bf16 v[24:27], a[232:233], a[244:245], 0\n"
      "ds_read_b128 v[48:51], v12 offset:54784\n"
      "v_mfma_f32_16x16x32_bf16 v[112:115], a[84:87], a[224:227], v[112:115]\n"
      "ds_read_b64_tr_b16 a[242:243], v14 offset:30720\n"
      "ds_read_b64_tr_b16 a[244:245], v14 offset:30728\n"
      "v_mfma_f32_16x16x32_bf16 v[116:119], a[88:91], a[224:227], v[116:119]\n"
      "ds_read_b128 v[36:39], v12 offset:1024\n"
      "ds_read_b128 v[40:43], v12 offset:54528\n"
      "v_mfma_f32_16x16x32_bf16 v[120:123], a[92:95], a[224:227], v[120:123]\n"
      "ds_write_addtid_b32 v84 offset:2048\n"
      "ds_write_addtid_b32 v85 offset:5152\n"
      "v_mfma_f32_16x16x32_bf16 v[112:115], a[96:99], a[238:241], v[112:115]\n"
      "ds_read_b128 a[224:227], v0 offset:64\n"
      "v_cvt_pk_bf16_f32 v20, v20, v24\n"
      "v_mfma_f32_16x16x32_bf16 v[116:119], a[100:103], a[238:241], v[116:119]\n"
      "v_cvt_pk_bf16_f32 v21, v21, v25\n"
      "v_cvt_pk_bf16_f32 v22, v22, v26\n"
      "v_cvt_pk_bf16_f32 v23, v23, v27\n"
      "v_mfma_f32_16x16x32_bf16 v[120:123], a[104:107], a[238:241], v[120:123]\n"
      "ds_write_addtid_b32 v20 offset:39168\n"
      "ds_write_addtid_b32 v21 offset:42752\n"
      "ds_write_addtid_b32 v22 offset:46336\n"
      "ds_write_addtid_b32 v23 offset:49920\n"
      "s_nop 0\n"
      "s_waitcnt lgkmcnt(9)\n"
      "v_mfma_f32_4x4x4_16b_bf16 v[20:23], a[232:233], a[242:243], 0\n"
      "ds_write_addtid_b32 v86 offset:8256\n"
      "v_mfma_f32_4x4x4_16b_bf16 v[24:27], a[232:233], a[244:245], 0\n"
      "ds_write_addtid_b32 v87 offset:11360\n"
      "ds_write_addtid_b32 v88 offset:14464\n"
      "v_mfma_f32_32x32x16_bf16 v[52:67], a[216:219], v[148:151], 0\n"
      "ds_read_b128 a[228:231], v1 offset:0\n"
      "ds_write_addtid_b32 v89 offset:17568\n"
      "ds_write_addtid_b32 v90 offset:20672\n"
      "v_add_f32 v28, v28, v44\n"
      "v_add_f32 v29, v29, v45\n"
      "v_add_f32_e64 v30, v30, v46\n"
      "v_mfma_f32_32x32x16_bf16 v[52:67], a[220:223], v[152:155], v[52:67]\n"
      "ds_read_b64_tr_b16 a[242:243], v14 offset:31744\n"
      "ds_read_b64_tr_b16 a[244:245], v14 offset:31752\n"
      "ds_write_addtid_b32 v91 offset:23776\n"
      "v_add_f32 v31, v31, v47\n"
      "v_add_f32 v32, v32, v48\n"
      "v_add_f32_e64 v33, v33, v49\n"
      "v_mfma_f32_32x32x16_bf16 v[68:83], a[216:219], a[108:111], 0\n"
      "buffer_store_dwordx4 a[246:249], v18, s[8:11], s60 offen offset:0 sc1\n"
      "v_mfma_f32_32x32x16_bf16 v[68:83], a[220:223], a[112:115], v[68:83]\n"
      "ds_write_addtid_b32 v92 offset:2560\n"
      "ds_write_addtid_b32 v93 offset:5664\n"
      "ds_write_addtid_b32 v94 offset:8768\n"
      "ds_write_addtid_b32 v95 offset:11872\n"
      "ds_write_addtid_b32 v96 offset:14976\n"
      "ds_write_addtid_b32 v97 offset:18080\n"
      "ds_write_addtid_b32 v98 offset:21184\n"
      "ds_write_addtid_b32 v99 offset:24288\n"
      "v_mfma_f32_32x32x16_bf16 v[84:99], a[216:219], v[220:223], 0\n"
      "ds_read_b128 a[216:219], v1 offset:32\n"
      "v_add_f32 v34, v34, v50\n"
      "v_add_f32 v35, v35, v51\n"
      "v_sub_f32 v32, v32, v28\n"
      "v_sub_f32 v33, v33, v29\n"
      "v_cvt_pk_bf16_f32 v20, v20, v24\n"
      "v_mfma_f32_32x32x16_bf16 v[84:99], a[220:223], v[224:227], v[84:99]\n"
      "ds_write_addtid_b32 v20 offset:39680\n"
      "ds_read_b128 a[246:249], v17 offset:3072\n"
      "ds_read_b128 v[44:47], v12 offset:256\n"
      "v_sub_f32 v34, v34, v30\n"
      "v_sub_f32_e64 v35, v35, v31\n"
      "s_waitcnt lgkmcnt(12)\n"
      "v_mfma_f32_32x32x16_bf16 v[52:67], a[224:227], v[156:159], v[52:67]\n"
      "buffer_store_dwordx4 a[250:253], v18, s[8:11], s60 offen offset:1024 sc1\n"
      "ds_read_b128 a[220:223], v1 offset:64\n"
      "v_mfma_f32_32x32x16_bf16 v[68:83], a[224:227], a[116:119], v[68:83]\n"
      "ds_read_b128 a[250:253], v15 offset:9216\n"
      "ds_read_b128 v[48:51], v12 offset:53760\n"
      "v_cvt_pk_bf16_f32 v21, v21, v25\n"
      "v_cvt_pk_bf16_f32 v22, v22, v26\n"
      "v_cvt_pk_bf16_f32 v23, v23, v27\n"
      "v_fmac_f32 v28, s45, v32\n"
      "v_fmac_f32 v29, s45, v33\n"
      "v_mfma_f32_32x32x16_bf16 v[84:99], a[224:227], v[228:231], v[84:99]\n"
      "ds_write_addtid_b32 v21 offset:43264\n"
      "ds_write_addtid_b32 v22 offset:46848\n"
      "ds_write_addtid_b32 v23 offset:50432\n"
      "v_fmac_f32 v30, s45, v34\n"
      "v_fmac_f32 v31, s45, v35\n"
      "v_fmac_f32 v28, s46, v124\n"
      "v_fmac_f32 v29, s46, v125\n"
      "v_mfma_f32_4x4x4_16b_bf16 v[20:23], a[232:233], a[242:243], 0\n"
      "v_fmac_f32 v30, s46, v126\n"
      "v_mfma_f32_4x4x4_16b_bf16 v[24:27], a[232:233], a[244:245], 0\n"
      "v_fmac_f32 v31, s46, v127\n"
      "v_fmac_f32 v32, s48, v124\n"
      "v_fmac_f32 v33, s48, v125\n"
      "v_mfma_f32_32x32x16_bf16 v[52:67], a[228:231], v[160:163], v[52:67]\n"
      "ds_read_b128 a[224:227], v2 offset:0\n"
      "ds_read_b64_tr_b16 a[242:243], v14 offset:32768\n"
      "ds_read_b64_tr_b16 a[244:245], v14 offset:32776\n"
      "s_waitcnt lgkmcnt(7)\n"
      "v_fmac_f32 v34, s48, v126\n"
      "v_fmac_f32_e64 v35, s48, v127\n"
      "v_cvt_pk_bf16_f32 v28, v28, v29\n"
      "v_mfma_f32_32x32x16_bf16 v[68:83], a[228:231], a[120:123], v[68:83]\n"
      "buffer_store_dwordx4 a[246:249], v18, s[8:11], s60 offen offset:2048 sc1\n"
      "v_cvt_pk_bf16_f32 v29, v30, v31\n"
      "v_mfma_f32_32x32x16_bf16 v[84:99], a[228:231], v[232:235], v[84:99]\n"
      "ds_read_b128 a[246:249], v16 offset:9216\n"
      "ds_write_b64 v13, v[28:29] offset:0\n"
      "v_fma_f32 v32, s47, v32, v100\n"
      "v_fma_f32 v33, s47, v33, v101\n"
      "v_cvt_pk_bf16_f32 v20, v20, v24\n"
      "v_mfma_f32_32x32x16_bf16 v[52:67], a[216:219], v[164:167], v[52:67]\n"
      "ds_read_b128 a[228:231], v2 offset:32\n"
      "ds_write_addtid_b32 v20 offset:40192\n"
      "v_fma_f32 v34, s47, v34, v102\n"
      "v_fma_f32 v35, s47, v35, v103\n"
      "v_cvt_pk_bf16_f32 v21, v21, v25\n"
      "v_cvt_pk_bf16_f32 v32, v32, v33\n"
      "v_mfma_f32_32x32x16_bf16 v[68:83], a[216:219], a[124:127], v[68:83]\n"
      "buffer_store_dwordx4 a[250:253], v18, s[8:11], s61 offen offset:0 sc1\n"
      "ds_write_addtid_b32 v21 offset:43776\n"
      "v_cvt_pk_bf16_f32 v22, v22, v26\n"
      "v_cvt_pk_bf16_f32 v23, v23, v27\n"
      "v_mfma_f32_32x32x16_bf16 v[84:99], a[216:219], v[236:239], v[84:99]\n"
      "ds_write_addtid_b32 v22 offset:47360\n"
      "ds_write_addtid_b32 v23 offset:50944\n"
      "ds_read_b128 a[250:253], v17 offset:9216\n"
      "v_cvt_pk_bf16_f32 v33, v34, v35\n"
      "v_add_f32 v36, v36, v44\n"
      "v_add_f32 v37, v37, v45\n"
      "s_waitcnt lgkmcnt(4)\n"
      "v_mfma_f32_4x4x4_16b_bf16 v[20:23], a[232:233], a[242:243], 0\n"
      "v_add_f32 v38, v38, v46\n"
      "v_mfma_f32_4x4x4_16b_bf16 v[24:27], a[232:233], a[244:245], 0\n"
      "v_add_f32 v39, v39, v47\n"
      "v_add_f32 v40, v40, v48\n"
      "v_add_f32_e64 v41, v41, v49\n"
      "v_mfma_f32_32x32x16_bf16 v[52:67], a[220:223], v[168:171], v[52:67]\n"
      "ds_read_b128 a[216:219], v2 offset:64\n"
      "ds_write_b64 v13, v[32:33] offset:12288\n"
      "v_cvt_pk_bf16_f32 v20, v20, v24\n"
      "v_add_f32 v42, v42, v50\n"
      "v_mfma_f32_32x32x16_bf16 v[68:83], a[220:223], a[128:131], v[68:83]\n"
      "ds_write_addtid_b32 v20 offset:40704\n"
      "ds_read_b128 v[28:31], v12 offset:512\n"
      "ds_read_b128 v[32:35], v12 offset:54016\n"
      "v_add_f32 v43, v43, v51\n"
      "v_cvt_pk_bf16_f32 v21, v21, v25\n"
      "v_sub_f32_e64 v40, v40, v36\n"
      "v_mfma_f32_32x32x16_bf16 v[84:99], a[220:223], v[240:243], v[84:99]\n"
      "buffer_store_dwordx4 a[246:249], v18, s[8:11], s61 offen offset:1024 sc1\n"
      "v_cvt_pk_bf16_f32 v22, v22, v26\n"
      "v_cvt_pk_bf16_f32 v23, v23, v27\n"
      "v_mfma_f32_32x32x16_bf16 v[52:67], a[224:227], v[172:175], v[52:67]\n"
      "ds_write_addtid_b32 v21 offset:44288\n"
      "ds_write_addtid_b32 v22 offset:47872\n"
      "ds_write_addtid_b32 v23 offset:51456\n"
      "v_sub_f32 v41, v41, v37\n"
      "v_sub_f32 v42, v42, v38\n"
      "v_sub_f32 v43, v43, v39\n"
      "v_mfma_f32_32x32x16_bf16 v[68:83], a[224:227], a[132:135], v[68:83]\n"
      "ds_read_b128 v[44:47], v12 offset:1792\n"
      "ds_read_b128 v[48:51], v12 offset:55296\n"
      "v_fmac_f32 v36, s45, v40\n"
      "s_waitcnt lgkmcnt(5)\n"
      "v_fmac_f32 v37, s45, v41\n"
      "v_fmac_f32 v38, s45, v42\n"
      "v_mfma_f32_32x32x16_bf16 v[84:99], a[224:227], v[244:247], v[84:99]\n"
      "buffer_store_dwordx4 a[250:253], v18, s[8:11], s61 offen offset:2048 sc1\n"
      "v_fmac_f32 v39, s45, v43\n"
      "s_waitcnt lgkmcnt(2)\n"
      "s_barrier\n"
      "v_mfma_f32_32x32x16_bf16 v[52:67], a[228:231], v[176:179], v[52:67]\n"
      "ds_read_b128 a[220:223], v0 offset:14336\n"
      "v_fmac_f32 v36, s46, v128\n"
      "v_fmac_f32 v37, s46, v129\n"
      "v_fmac_f32 v38, s46, v130\n"
      "v_fmac_f32 v39, s46, v131\n"
      "v_fmac_f32 v40, s48, v128\n"
      "v_fmac_f32 v41, s48, v129\n"
      "v_mfma_f32_32x32x16_bf16 v[68:83], a[228:231], a[136:139], v[68:83]\n"
      "ds_read_b128 a[224:227], v0 offset:14368\n"
      "v_fmac_f32 v42, s48, v130\n"
      "v_fmac_f32 v43, s48, v131\n"
      "v_cvt_pk_bf16_f32 v36, v36, v37\n"
      "v_cvt_pk_bf16_f32 v37, v38, v39\n"
      "v_fma_f32 v40, s47, v40, v104\n"
      "v_fma_f32 v41, s47, v41, v105\n"
      "v_mfma_f32_32x32x16_bf16 v[84:99], a[228:231], v[248:251], v[84:99]\n"
      "ds_write_b64 v13, v[36:37] offset:64\n"
      "v_fma_f32 v42, s47, v42, v106\n"
      "v_fma_f32 v43, s47, v43, v107\n"
      "v_cvt_pk_bf16_f32 v40, v40, v41\n"
      "v_cvt_pk_bf16_f32 v41, v42, v43\n"
      "v_mfma_f32_32x32x16_bf16 v[52:67], a[216:219], v[180:183], v[52:67]\n"
      "ds_read_b128 a[228:231], v0 offset:14400\n"
      "ds_write_b64 v13, v[40:41] offset:12352\n"
      "s_waitcnt lgkmcnt(3)\n"
      "v_add_f32 v28, v28, v44\n"
      "v_add_f32 v29, v29, v45\n"
      "v_mfma_f32_32x32x16_bf16 v[68:83], a[216:219], a[140:143], v[68:83]\n"
      "ds_read_b128 v[36:39], v12 offset:2048\n"
      "ds_read_b128 v[40:43], v12 offset:55552\n"
      "v_add_f32 v30, v30, v46\n"
      "v_add_f32 v31, v31, v47\n"
      "v_add_f32 v32, v32, v48\n"
      "v_add_f32_e64 v33, v33, v49\n"
      "v_mfma_f32_32x32x16_bf16 v[84:99], a[216:219], v[252:255], v[84:99]\n"
      "v_add_f32 v34, v34, v50\n"
      "v_add_f32 v35, v35, v51\n"
      "v_sub_f32 v32, v32, v28\n"
      "v_sub_f32 v33, v33, v29\n"
      "v_sub_f32 v34, v34, v30\n"
      "v_sub_f32 v35, v35, v31\n"
      "v_fmac_f32_e64 v28, s45, v32\n"
      "v_mfma_f32_32x32x16_bf16 v[52:67], a[220:223], v[184:187], v[52:67]\n"
      "ds_read_b128 a[216:219], v1 offset:14336\n"
      "ds_read_b128 v[44:47], v12 offset:2304\n"
      "ds_read_b128 v[48:51], v12 offset:55808\n"
      "v_fmac_f32 v29, s45, v33\n"
      "v_fmac_f32 v30, s45, v34\n"
      "v_fmac_f32 v31, s45, v35\n"
      "v_fmac_f32 v28, s46, v136\n"
      "v_mfma_f32_32x32x16_bf16 v[68:83], a[220:223], a[144:147], v[68:83]\n"
      "v_fmac_f32 v29, s46, v137\n"
      "v_fmac_f32 v30, s46, v138\n"
      "v_fmac_f32 v31, s46, v139\n"
      "v_fmac_f32 v32, s48, v136\n"
      "v_fmac_f32 v33, s48, v137\n"
      "s_add_u32 s50, s62, 6291456\n"
      "v_fmac_f32 v34, s48, v138\n"
      "v_mfma_f32_32x32x16_bf16 v[84:99], a[220:223], a[180:183], v[84:99]\n"
      "v_fmac_f32_e64 v35, s48, v139\n"
      "v_cvt_pk_bf16_f32 v28, v28, v29\n"
      "v_cvt_pk_bf16_f32 v29, v30, v31\n"
      "v_fma_f32 v32, s47, v32, v112\n"
      "v_fma_f32 v33, s47, v33, v113\n"
      "s_add_u32 s51, s62, 6294528\n"
      "v_fma_f32 v34, s47, v34, v114\n"
      "v_mfma_f32_32x32x16_bf16 v[52:67], a[224:227], v[188:191], v[52:67]\n"
      "ds_read_b128 a[220:223], v1 offset:14368\n"
      "ds_write_b64 v13, v[28:29] offset:3072\n"
      "v_fma_f32 v35, s47, v35, v115\n"
      "v_cvt_pk_bf16_f32 v32, v32, v33\n"
      "v_cvt_pk_bf16_f32 v33, v34, v35\n"
      "v_mfma_f32_32x32x16_bf16 v[68:83], a[224:227], a[148:151], v[68:83]\n"
      "ds_write_b64 v13, v[32:33] offset:15360\n"
      "s_waitcnt lgkmcnt(3)\n"
      "v_add_f32 v36, v36, v44\n"
      "v_add_f32 v37, v37, v45\n"
      "v_add_f32 v38, v38, v46\n"
      "v_mfma_f32_32x32x16_bf16 v[84:99], a[224:227], a[184:187], v[84:99]\n"
      "ds_read_b128 v[28:31], v12 offset:1536\n"
      "ds_read_b128 v[32:35], v12 offset:55040\n"
      "v_add_f32 v39, v39, v47\n"
      "v_add_f32 v40, v40, v48\n"
      "v_add_f32 v41, v41, v49\n"
      "v_add_f32 v42, v42, v50\n"
      "v_mfma_f32_32x32x16_bf16 v[52:67], a[228:231], v[192:195], v[52:67]\n"
      "ds_read_b128 a[224:227], v1 offset:14400\n"
      "v_add_f32 v43, v43, v51\n"
      "v_sub_f32 v40, v40, v36\n"
      "v_sub_f32 v41, v41, v37\n"
      "v_sub_f32 v42, v42, v38\n"
      "v_sub_f32 v43, v43, v39\n"
      "v_fmac_f32 v36, s45, v40\n"
      "v_mfma_f32_32x32x16_bf16 v[68:83], a[228:231], a[152:155], v[68:83]\n"
      "ds_read_b128 v[44:47], v12 offset:768\n"
      "ds_read_b128 v[48:51], v12 offset:54272\n"
      "v_fmac_f32 v37, s45, v41\n"
      "v_fmac_f32 v38, s45, v42\n"
      "v_fmac_f32 v39, s45, v43\n"
      "v_fmac_f32 v36, s46, v132\n"
      "v_fmac_f32 v37, s46, v133\n"
      "s_add_u32 s52, s62, 7077888\n"
      "v_mfma_f32_32x32x16_bf16 v[84:99], a[228:231], a[188:191], v[84:99]\n"
      "v_fmac_f32 v38, s46, v134\n"
      "v_fmac_f32 v39, s46, v135\n"
      "v_fmac_f32 v40, s48, v132\n"
      "v_fmac_f32 v41, s48, v133\n"
      "v_fmac_f32 v42, s48, v134\n"
      "s_add_u32 s53, s62, 7080960\n"
      "v_fmac_f32_e64 v43, s48, v135\n"
      "v_mfma_f32_32x32x16_bf16 v[52:67], a[216:219], v[196:199], v[52:67]\n"
      "ds_read_b128 a[228:231], v2 offset:14336\n"
      "v_cvt_pk_bf16_f32 v36, v36, v37\n"
      "v_cvt_pk_bf16_f32 v37, v38, v39\n"
      "v_fma_f32 v40, s47, v40, v108\n"
      "v_fma_f32 v41, s47, v41, v109\n"
      "v_fma_f32 v42, s47, v42, v110\n"
      "v_mfma_f32_32x32x16_bf16 v[68:83], a[216:219], a[156:159], v[68:83]\n"
      "ds_write_b64 v13, v[36:37] offset:128\n"
      "v_fma_f32 v43, s47, v43, v111\n"
      "v_cvt_pk_bf16_f32 v40, v40, v41\n"
      "v_cvt_pk_bf16_f32 v41, v42, v43\n"
      "s_waitcnt lgkmcnt(2)\n"
      "v_add_f32 v28, v28, v44\n"
      "v_mfma_f32_32x32x16_bf16 v[84:99], a[216:219], a[192:195], v[84:99]\n"
      "ds_write_b64 v13, v[40:41] offset:12416\n"
      "v_add_f32 v29, v29, v45\n"
      "v_add_f32 v30, v30, v46\n"
      "v_add_f32 v31, v31, v47\n"
      "v_add_f32 v32, v32, v48\n"
      "v_mfma_f32_32x32x16_bf16 v[52:67], a[220:223], v[200:203], v[52:67]\n"
      "ds_read_b128 a[216:219], v2 offset:14368\n"
      "ds_read_b128 v[36:39], v12 offset:2560\n"
      "ds_read_b128 v[40:43], v12 offset:56064\n"
      "v_add_f32 v33, v33, v49\n"
      "v_add_f32 v34, v34, v50\n"
      "v_add_f32_e64 v35, v35, v51\n"
      "v_mfma_f32_32x32x16_bf16 v[68:83], a[220:223], a[160:163], v[68:83]\n"
      "ds_read_b128 v[44:47], v12 offset:2816\n"
      "ds_read_b128 v[48:51], v12 offset:56320\n"
      "v_sub_f32 v32, v32, v28\n"
      "v_sub_f32 v33, v33, v29\n"
      "v_sub_f32 v34, v34, v30\n"
      "v_sub_f32 v35, v35, v31\n"
      "v_fmac_f32 v28, s45, v32\n"
      "v_mfma_f32_32x32x16_bf16 v[84:99], a[220:223], a[196:199], v[84:99]\n"
      "ds_read_b128 a[220:223], v2 offset:14400\n"
      "v_fmac_f32 v29, s45, v33\n"
      "v_fmac_f32 v30, s45, v34\n"
      "v_fmac_f32 v31, s45, v35\n"
      "v_fmac_f32 v28, s46, v140\n"
      "v_fmac_f32 v29, s46, v141\n"
      "v_fmac_f32 v30, s46, v142\n"
      "v_mfma_f32_32x32x16_bf16 v[52:67], a[224:227], v[204:207], v[52:67]\n"
      "v_fmac_f32 v31, s46, v143\n"
      "v_fmac_f32 v32, s48, v140\n"
      "v_fmac_f32 v33, s48, v141\n"
      "v_fmac_f32 v34, s48, v142\n"
      "v_fmac_f32 v35, s48, v143\n"
      "v_cvt_pk_bf16_f32 v28, v28, v29\n"
      "s_waitcnt lgkmcnt(1)\n"
      "s_barrier\n"
      "v_mfma_f32_32x32x16_bf16 v[68:83], a[224:227], a[164:167], v[68:83]\n"
      "ds_read_b128 a[246:249], v15 offset:0\n"
      "ds_read_b128 a[250:253], v16 offset:0\n"
      "ds_read_b128 a[242:245], v17 offset:0\n"
      "v_cvt_pk_bf16_f32 v29, v30, v31\n"
      "v_fma_f32 v32, s47, v32, v116\n"
      "v_fma_f32 v33, s47, v33, v117\n"
      "v_fma_f32 v34, s47, v34, v118\n"
      "v_fma_f32 v35, s47, v35, v119\n"
      "v_mfma_f32_32x32x16_bf16 v[84:99], a[224:227], a[200:203], v[84:99]\n"
      "ds_write_b64 v13, v[28:29] offset:3136\n"
      "v_cvt_pk_bf16_f32 v32, v32, v33\n"
      "v_cvt_pk_bf16_f32 v33, v34, v35\n"
      "v_add_f32 v36, v36, v44\n"
      "v_mfma_f32_32x32x16_bf16 v[52:67], a[228:231], v[208:211], v[52:67]\n"
      "ds_write_b64 v13, v[32:33] offset:15424\n"
      "v_add_f32 v37, v37, v45\n"
      "s_waitcnt lgkmcnt(4)\n"
      "v_add_f32 v38, v38, v46\n"
      "v_add_f32 v39, v39, v47\n"
      "v_add_f32 v40, v40, v48\n"
      "v_mfma_f32_32x32x16_bf16 v[68:83], a[228:231], a[168:171], v[68:83]\n"
      "buffer_store_dwordx4 a[246:249], v18, s[8:11], s50 offen offset:0 sc1\n"
      "v_mfma_f32_32x32x16_bf16 v[84:99], a[228:231], a[204:207], v[84:99]\n"
      "v_add_f32 v41, v41, v49\n"
      "v_add_f32 v42, v42, v50\n"
      "v_add_f32 v43, v43, v51\n"
      "v_sub_f32 v40, v40, v36\n"
      "s_waitcnt lgkmcnt(1)\n"
      "v_sub_f32 v41, v41, v37\n"
      "v_sub_f32_e64 v42, v42, v38\n"
      "v_mfma_f32_32x32x16_bf16 v[52:67], a[216:219], v[212:215], v[52:67]\n"
      "buffer_store_dwordx4 a[250:253], v18, s[8:11], s50 offen offset:1024 sc1\n"
      "ds_read_b128 a[224:227], v3 offset:0\n"
      "v_mfma_f32_32x32x16_bf16 v[52:67], a[220:223], v[216:219], v[52:67]\n"
      "v_sub_f32 v43, v43, v39\n"
      "v_fmac_f32 v36, s45, v40\n"
      "v_fmac_f32 v37, s45, v41\n"
      "v_fmac_f32 v38, s45, v42\n"
      "v_fmac_f32_e64 v39, s45, v43\n"
      "v_fmac_f32_e64 v36, s46, v144\n"
      "v_mfma_f32_32x32x16_bf16 v[68:83], a[216:219], a[172:175], v[68:83]\n"
      "ds_read_b128 a[228:231], v4 offset:0\n"
      "v_fmac_f32_e64 v37, s46, v145\n"
      "v_fmac_f32_e64 v38, s46, v146\n"
      "v_fmac_f32_e64 v39, s46, v147\n"
      "v_fmac_f32_e64 v40, s48, v144\n"
      "v_fmac_f32_e64 v41, s48, v145\n"
      "v_mfma_f32_32x32x16_bf16 v[68:83], a[220:223], a[176:179], v[68:83]\n"
      "buffer_store_dwordx4 a[242:245], v18, s[8:11], s50 offen offset:2048 sc1\n"
      "ds_write_addtid_b32 v52 offset:0\n"
      "ds_write_addtid_b32 v53 offset:3104\n"
      "v_mfma_f32_32x32x16_bf16 v[84:99], a[216:219], a[208:211], v[84:99]\n"
      "ds_read_b128 a[216:219], v5 offset:0\n"
      "ds_write_addtid_b32 v54 offset:6208\n"
      "ds_write_addtid_b32 v55 offset:9312\n"
      "v_fmac_f32_e64 v42, s48, v146\n"
      "v_fmac_f32 v43, s48, v147\n"
      "v_cvt_pk_bf16_f32 v36, v36, v37\n"
      "v_mfma_f32_32x32x16_bf16 v[84:99], a[220:223], a[212:215], v[84:99]\n"
      "ds_read_b128 a[220:223], v6 offset:0\n"
      "ds_write_addtid_b32 v56 offset:12416\n"
      "ds_write_addtid_b32 v57 offset:15520\n"
      "v_cvt_pk_bf16_f32 v37, v38, v39\n"
      "v_fma_f32 v40, s47, v40, v120\n"
      "s_waitcnt lgkmcnt(8)\n"
      "v_mfma_f32_16x16x32_bf16 v[124:127], a[0:3], a[224:227], 0\n"
      "ds_write_b64 v13, v[36:37] offset:3200\n"
      "v_mfma_f32_16x16x32_bf16 v[128:131], a[4:7], a[224:227], 0\n"
      "v_fma_f32 v41, s47, v41, v121\n"
      "v_fma_f32 v42, s47, v42, v122\n"
      "v_mfma_f32_16x16x32_bf16 v[132:135], a[8:11], a[224:227], 0\n"
      "ds_read_b128 a[224:227], v7 offset:0\n"
      "ds_write_addtid_b32 v58 offset:18624\n"
      "v_mfma_f32_16x16x32_bf16 v[124:127], a[12:15], a[228:231], v[124:127]\n"
      "ds_write_addtid_b32 v59 offset:21728\n"
      "ds_write_addtid_b32 v60 offset:512\n"
      "v_mfma_f32_16x16x32_bf16 v[128:131], a[16:19], a[228:231], v[128:131]\n"
      "ds_write_addtid_b32 v61 offset:3616\n"
      "ds_write_addtid_b32 v62 offset:6720\n"
      "v_mfma_f32_16x16x32_bf16 v[132:135], a[20:23], a[228:231], v[132:135]\n"
      "ds_read_b128 a[228:231], v8 offset:0\n"
      "ds_write_addtid_b32 v63 offset:9824\n"
      "s_nop 0\n"
      "s_waitcnt lgkmcnt(8)\n"
      "v_mfma_f32_16x16x32_bf16 v[124:127], a[24:27], a[216:219], v[124:127]\n"
      "ds_write_addtid_b32 v64 offset:12928\n"
      "ds_write_addtid_b32 v65 offset:16032\n"
      "v_mfma_f32_16x16x32_bf16 v[128:131], a[28:31], a[216:219], v[128:131]\n"
      "ds_write_addtid_b32 v66 offset:19136\n"
      "ds_write_addtid_b32 v67 offset:22240\n"
      "v_mfma_f32_16x16x32_bf16 v[132:135], a[32:35], a[216:219], v[132:135]\n"
      "ds_read_b128 a[216:219], v9 offset:0\n"
      "ds_write_addtid_b32 v68 offset:1024\n"
      "v_mfma_f32_16x16x32_bf16 v[124:127], a[36:39], a[220:223], v[124:127]\n"
      "ds_write_addtid_b32 v69 offset:4128\n"
      "ds_write_addtid_b32 v70 offset:7232\n"
      "v_mfma_f32_16x16x32_bf16 v[128:131], a[40:43], a[220:223], v[128:131]\n"
      "ds_write_addtid_b32 v71 offset:10336\n"
      "ds_write_addtid_b32 v72 offset:13440\n"
      "v_fma_f32 v43, s47, v43, v123\n"
      "v_mfma_f32_16x16x32_bf16 v[132:135], a[44:47], a[220:223], v[132:135]\n"
      "ds_read_b128 a[220:223], v10 offset:0\n"
      "ds_write_addtid_b32 v73 offset:16544\n"
      "s_nop 0\n"
      "s_waitcnt lgkmcnt(10)\n"
      "v_mfma_f32_16x16x32_bf16 v[124:127], a[48:51], a[224:227], v[124:127]\n"
      "ds_write_addtid_b32 v74 offset:19648\n"
      "ds_write_addtid_b32 v75 offset:22752\n"
      "v_mfma_f32_16x16x32_bf16 v[128:131], a[52:55], a[224:227], v[128:131]\n"
      "ds_write_addtid_b32 v76 offset:1536\n"
      "ds_write_addtid_b32 v77 offset:4640\n"
      "v_cvt_pk_bf16_f32 v40, v40, v41\n"
      "v_mfma_f32_16x16x32_bf16 v[132:135], a[56:59], a[224:227], v[132:135]\n"
      "ds_read_b128 a[224:227], v11 offset:0\n"
      "ds_write_addtid_b32 v78 offset:7744\n"
      "v_mfma_f32_16x16x32_bf16 v[124:127], a[60:63], a[228:231], v[124:127]\n"
      "ds_write_addtid_b32 v79 offset:10848\n"
      "ds_write_addtid_b32 v80 offset:13952\n"
      "v_cvt_pk_bf16_f32 v41, v42, v43\n"
      "v_mfma_f32_16x16x32_bf16 v[128:131], a[64:67], a[228:231], v[128:131]\n"
      "ds_write_b64 v13, v[40:41] offset:15488\n"
      "v_mfma_f32_16x16x32_bf16 v[132:135], a[68:71], a[228:231], v[132:135]\n"
      "ds_read_b128 a[228:231], v3 offset:3072\n"
      "s_nop 0\n"
      "s_waitcnt lgkmcnt(8)\n"
      "v_mfma_f32_16x16x32_bf16 v[124:127], a[72:75], a[216:219], v[124:127]\n"
      "ds_write_addtid_b32 v81 offset:17056\n"
      "ds_write_addtid_b32 v82 offset:20160\n"
      "v_mfma_f32_16x16x32_bf16 v[128:131], a[76:79], a[216:219], v[128:131]\n"
      "ds_write_addtid_b32 v83 offset:23264\n"
      "ds_write_addtid_b32 v84 offset:2048\n"
      "v_mfma_f32_16x16x32_bf16 v[132:135], a[80:83], a[216:219], v[132:135]\n"
      "ds_read_b128 a[216:219], v4 offset:3072\n"
      "v_mfma_f32_16x16x32_bf16 v[124:127], a[84:87], a[220:223], v[124:127]\n"
      "ds_write_addtid_b32 v85 offset:5152\n"
      "ds_write_addtid_b32 v86 offset:8256\n"
      "v_mfma_f32_16x16x32_bf16 v[128:131], a[88:91], a[220:223], v[128:131]\n"
      "ds_write_addtid_b32 v87 offset:11360\n"
      "ds_write_addtid_b32 v88 offset:14464\n"
      "ds_write_addtid_b32 v89 offset:17568\n"
      "ds_write_addtid_b32 v90 offset:20672\n"
      "ds_write_addtid_b32 v91 offset:23776\n"
      "ds_write_addtid_b32 v92 offset:2560\n"
      "ds_write_addtid_b32 v93 offset:5664\n"
      "ds_write_addtid_b32 v94 offset:8768\n"
      "ds_write_addtid_b32 v95 offset:11872\n"
      "ds_write_addtid_b32 v96 offset:14976\n"
      "ds_write_addtid_b32 v97 offset:18080\n"
      "ds_write_addtid_b32 v98 offset:21184\n"
      "ds_write_addtid_b32 v99 offset:24288\n"
      "v_mfma_f32_16x16x32_bf16 v[132:135], a[92:95], a[220:223], v[132:135]\n"
      "ds_read_b128 a[220:223], v5 offset:3072\n"
      "s_waitcnt lgkmcnt(15)\n"
      "v_mfma_f32_16x16x32_bf16 v[124:127], a[96:99], a[224:227], v[124:127]\n"
      "s_waitcnt lgkmcnt(1)\n"
      "s_nop 0\n"
      "s_barrier\n"
      "v_mfma_f32_16x16x32_bf16 v[128:131], a[100:103], a[224:227], v[128:131]\n"
      "ds_read_b128 v[148:151], v12 offset:0\n"
      "ds_read_b128 a[108:111], v15 offset:3072\n"
      "v_mfma_f32_16x16x32_bf16 v[132:135], a[104:107], a[224:227], v[132:135]\n"
      "ds_read_b128 a[224:227], v6 offset:3072\n"
      "ds_read_b128 v[152:155], v12 offset:53504\n"
      "v_mfma_f32_16x16x32_bf16 v[136:139], a[0:3], a[228:231], 0\n"
      "ds_read_b128 a[112:115], v16 offset:3072\n"
      "v_mfma_f32_16x16x32_bf16 v[140:143], a[4:7], a[228:231], 0\n"
      "ds_read_b128 a[116:119], v17 offset:3072\n"
      "v_mfma_f32_16x16x32_bf16 v[144:147], a[8:11], a[228:231], 0\n"
      "ds_read_b128 a[228:231], v7 offset:3072\n"
      "v_mfma_f32_16x16x32_bf16 v[136:139], a[12:15], a[216:219], v[136:139]\n"
      "ds_read_b128 v[156:159], v12 offset:1280\n"
      "v_mfma_f32_16x16x32_bf16 v[140:143], a[16:19], a[216:219], v[140:143]\n"
      "ds_read_b128 v[160:163], v12 offset:54784\n"
      "s_nop 0\n"
      "s_waitcnt lgkmcnt(7)\n"
      "v_mfma_f32_16x16x32_bf16 v[144:147], a[20:23], a[216:219], v[144:147]\n"
      "ds_read_b128 a[216:219], v8 offset:3072\n"
      "v_mfma_f32_16x16x32_bf16 v[136:139], a[24:27], a[220:223], v[136:139]\n"
      "ds_read_b128 v[164:167], v12 offset:1024\n"
      "v_mfma_f32_16x16x32_bf16 v[140:143], a[28:31], a[220:223], v[140:143]\n"
      "ds_read_b128 v[168:171], v12 offset:54528\n"
      "v_mfma_f32_16x16x32_bf16 v[144:147], a[32:35], a[220:223], v[144:147]\n"
      "ds_read_b128 a[220:223], v9 offset:3072\n"
      "s_waitcnt lgkmcnt(7)\n"
      "v_mfma_f32_16x16x32_bf16 v[136:139], a[36:39], a[224:227], v[136:139]\n"
      "buffer_store_dwordx4 a[108:111], v18, s[8:11], s51 offen offset:0 sc1\n"
      "v_mfma_f32_16x16x32_bf16 v[140:143], a[40:43], a[224:227], v[140:143]\n"
      "s_waitcnt lgkmcnt(4)\n"
      "v_add_f32 v148, v148, v156\n"
      "v_add_f32 v149, v149, v157\n"
      "v_add_f32 v150, v150, v158\n"
      "v_add_f32 v151, v151, v159\n"
      "v_add_f32_e64 v152, v152, v160\n"
      "v_mfma_f32_16x16x32_bf16 v[144:147], a[44:47], a[224:227], v[144:147]\n"
      "ds_read_b128 a[224:227], v10 offset:3072\n"
      "v_add_f32 v153, v153, v161\n"
      "v_add_f32 v154, v154, v162\n"
      "v_add_f32 v155, v155, v163\n"
      "v_sub_f32 v152, v152, v148\n"
      "v_sub_f32 v153, v153, v149\n"
      "v_mfma_f32_16x16x32_bf16 v[136:139], a[48:51], a[228:231], v[136:139]\n"
      "ds_read_b128 a[238:241], v11 offset:3072\n"
      "ds_read_b128 v[172:175], v12 offset:256\n"
      "ds_read_b128 v[176:179], v12 offset:53760\n"
      "v_sub_f32 v154, v154, v150\n"
      "v_sub_f32 v155, v155, v151\n"
      "v_fmac_f32 v148, s45, v152\n"
      "v_mfma_f32_16x16x32_bf16 v[140:143], a[52:55], a[228:231], v[140:143]\n"
      "ds_read_b128 v[180:183], v12 offset:2048\n"
      "v_fmac_f32 v149, s45, v153\n"
      "v_fmac_f32 v150, s45, v154\n"
      "v_mfma_f32_16x16x32_bf16 v[144:147], a[56:59], a[228:231], v[144:147]\n"
      "ds_read_b128 v[184:187], v12 offset:55552\n"
      "ds_read_b128 v[188:191], v12 offset:2304\n"
      "ds_read_b128 v[192:195], v12 offset:55808\n"
      "s_waitcnt lgkmcnt(8)\n"
      "v_mfma_f32_16x16x32_bf16 v[136:139], a[60:63], a[216:219], v[136:139]\n"
      "v_fmac_f32 v151, s45, v155\n"
      "v_fmac_f32 v148, s46, v100\n"
      "v_fmac_f32 v149, s46, v101\n"
      "v_mfma_f32_16x16x32_bf16 v[140:143], a[64:67], a[216:219], v[140:143]\n"
      "ds_read_b128 v[196:199], v12 offset:512\n"
      "ds_read_b128 v[200:203], v12 offset:54016\n"
      "ds_read_b128 v[204:207], v12 offset:1792\n"
      "v_mfma_f32_16x16x32_bf16 v[144:147], a[68:71], a[216:219], v[144:147]\n"
      "ds_read_b128 v[208:211], v12 offset:55296\n"
      "ds_read_b128 v[212:215], v12 offset:1536\n"
      "v_fmac_f32 v150, s46, v102\n"
      "v_mfma_f32_16x16x32_bf16 v[136:139], a[72:75], a[220:223], v[136:139]\n"
      "v_fmac_f32 v151, s46, v103\n"
      "v_fmac_f32 v152, s48, v100\n"
      "v_mfma_f32_16x16x32_bf16 v[140:143], a[76:79], a[220:223], v[140:143]\n"
      "ds_read_b128 v[216:219], v12 offset:55040\n"
      "v_fmac_f32 v153, s48, v101\n"
      "v_fmac_f32_e64 v154, s48, v102\n"
      "v_mfma_f32_16x16x32_bf16 v[144:147], a[80:83], a[220:223], v[144:147]\n"
      "ds_read_b128 v[220:223], v12 offset:768\n"
      "ds_read_b128 v[224:227], v12 offset:54272\n"
      "v_fmac_f32 v155, s48, v103\n"
      "s_waitcnt lgkmcnt(8)\n"
      "v_mfma_f32_16x16x32_bf16 v[136:139], a[84:87], a[224:227], v[136:139]\n"
      "v_cvt_pk_bf16_f32 v148, v148, v149\n"
      "v_cvt_pk_bf16_f32 v149, v150, v151\n"
      "v_fma_f32 v152, s47, v152, v124\n"
      "v_mfma_f32_16x16x32_bf16 v[140:143], a[88:91], a[224:227], v[140:143]\n"
      "buffer_store_dwordx4 a[112:115], v18, s[8:11], s51 offen offset:1024 sc1\n"
      "v_mfma_f32_16x16x32_bf16 v[144:147], a[92:95], a[224:227], v[144:147]\n"
      "ds_read_b128 v[228:231], v12 offset:2560\n"
      "ds_read_b128 v[232:235], v12 offset:56064\n"
      "v_mfma_f32_16x16x32_bf16 v[136:139], a[96:99], a[238:241], v[136:139]\n"
      "ds_write_b64 v13, v[148:149] offset:0\n"
      "v_mfma_f32_16x16x32_bf16 v[140:143], a[100:103], a[238:241], v[140:143]\n"
      "ds_read_b128 v[236:239], v12 offset:2816\n"
      "ds_read_b128 v[240:243], v12 offset:56320\n"
      "v_mfma_f32_16x16x32_bf16 v[144:147], a[104:107], a[238:241], v[144:147]\n"
      "v_fma_f32 v153, s47, v153, v125\n"
      "v_fma_f32 v154, s47, v154, v126\n"
      "buffer_store_dwordx4 a[116:119], v18, s[8:11], s51 offen offset:2048 sc1\n"
      "v_fma_f32 v155, s47, v155, v127\n"
      "v_cvt_pk_bf16_f32 v152, v152, v153\n"
      "v_cvt_pk_bf16_f32 v153, v154, v155\n"
      "v_add_f32 v164, v164, v172\n"
      "v_add_f32 v165, v165, v173\n"
      "v_add_f32 v166, v166, v174\n"
      "v_add_f32 v167, v167, v175\n"
      "v_add_f32 v168, v168, v176\n"
      "v_add_f32 v169, v169, v177\n"
      "v_pk_add_f32 v[170:171], v[170:171], v[178:179]\n"
      "v_pk_add_f32 v[168:169], v[168:169], v[164:165] neg_lo:[0,1] neg_hi:[0,1]\n"
      "v_pk_add_f32 v[170:171], v[170:171], v[166:167] neg_lo:[0,1] neg_hi:[0,1]\n"
      "ds_write_b64 v13, v[152:153] offset:12288\n"
      "v_pk_fma_f32 v[164:165], v[168:169], s[44:45], v[164:165] op_sel:[0,1,0] op_sel_hi:[1,1,1]\n"
      "v_pk_fma_f32 v[166:167], v[170:171], s[44:45], v[166:167] op_sel:[0,1,0] op_sel_hi:[1,1,1]\n"
      "v_pk_fma_f32 v[164:165], v[104:105], s[46:47], v[164:165] op_sel:[0,0,0] op_sel_hi:[1,0,1]\n"
      "v_pk_fma_f32 v[166:167], v[106:107], s[46:47], v[166:167] op_sel:[0,0,0] op_sel_hi:[1,0,1]\n"
      "v_pk_fma_f32 v[168:169], v[104:105], s[48:49], v[168:169] op_sel:[0,0,0] op_sel_hi:[1,0,1]\n"
      "v_pk_fma_f32 v[170:171], v[106:107], s[48:49], v[170:171] op_sel:[0,0,0] op_sel_hi:[1,0,1]\n"
      "v_cvt_pk_bf16_f32 v164, v164, v165\n"
      "v_cvt_pk_bf16_f32 v165, v166, v167\n"
      "v_pk_fma_f32 v[168:169], v[168:169], s[46:47], v[128:129] op_sel:[0,1,0] op_sel_hi:[1,1,1]\n"
      "v_pk_fma_f32 v[170:171], v[170:171], s[46:47], v[130:131] op_sel:[0,1,0] op_sel_hi:[1,1,1]\n"
      "v_cvt_pk_bf16_f32 v168, v168, v169\n"
      "v_cvt_pk_bf16_f32 v169, v170, v171\n"
      "v_add_f32 v180, v180, v188\n"
      "ds_write_b64 v13, v[164:165] offset:64\n"
      "ds_write_b64 v13, v[168:169] offset:12352\n"
      "v_add_f32 v181, v181, v189\n"
      "v_pk_add_f32 v[182:183], v[182:183], v[190:191]\n"
      "v_pk_add_f32 v[184:185], v[184:185], v[192:193]\n"
      "v_pk_add_f32 v[186:187], v[186:187], v[194:195]\n"
      "v_pk_add_f32 v[184:185], v[184:185], v[180:181] neg_lo:[0,1] neg_hi:[0,1]\n"
      "v_pk_add_f32 v[186:187], v[186:187], v[182:183] neg_lo:[0,1] neg_hi:[0,1]\n"
      "v_pk_fma_f32 v[180:181], v[184:185], s[44:45], v[180:181] op_sel:[0,1,0] op_sel_hi:[1,1,1]\n"
      "v_pk_fma_f32 v[182:183], v[186:187], s[44:45], v[182:183] op_sel:[0,1,0] op_sel_hi:[1,1,1]\n"
      "v_pk_fma_f32 v[180:181], v[108:109], s[46:47], v[180:181] op_sel:[0,0,0] op_sel_hi:[1,0,1]\n"
      "v_pk_fma_f32 v[182:183], v[110:111], s[46:47], v[182:183] op_sel:[0,0,0] op_sel_hi:[1,0,1]\n"
      "v_pk_fma_f32 v[184:185], v[108:109], s[48:49], v[184:185] op_sel:[0,0,0] op_sel_hi:[1,0,1]\n"
      "v_pk_fma_f32 v[186:187], v[110:111], s[48:49], v[186:187] op_sel:[0,0,0] op_sel_hi:[1,0,1]\n"
      "v_cvt_pk_bf16_f32 v180, v180, v181\n"
      "v_cvt_pk_bf16_f32 v181, v182, v183\n"
      "v_pk_fma_f32 v[184:185], v[184:185], s[46:47], v[132:133] op_sel:[0,1,0] op_sel_hi:[1,1,1]\n"
      "v_pk_fma_f32 v[186:187], v[186:187], s[46:47], v[134:135] op_sel:[0,1,0] op_sel_hi:[1,1,1]\n"
      "v_cvt_pk_bf16_f32 v184, v184, v185\n"
      "v_cvt_pk_bf16_f32 v185, v186, v187\n"
      "ds_write_b64 v13, v[180:181] offset:128\n"
      "ds_write_b64 v13, v[184:185] offset:12416\n"
      "s_nop 0\n"
      "s_waitcnt lgkmcnt(2)\n"
      "v_pk_add_f32 v[196:197], v[196:197], v[204:205]\n"
      "v_pk_add_f32 v[198:199], v[198:199], v[206:207]\n"
      "v_pk_add_f32 v[200:201], v[200:201], v[208:209]\n"
      "v_pk_add_f32 v[202:203], v[202:203], v[210:211]\n"
      "v_pk_add_f32 v[200:201], v[200:201], v[196:197] neg_lo:[0,1] neg_hi:[0,1]\n"
      "v_pk_add_f32 v[202:203], v[202:203], v[198:199] neg_lo:[0,1] neg_hi:[0,1]\n"
      "v_pk_fma_f32 v[196:197], v[200:201], s[44:45], v[196:197] op_sel:[0,1,0] op_sel_hi:[1,1,1]\n"
      "v_fmac_f32_e64 v198, s45, v202\n"
      "s_waitcnt lgkmcnt(0)\n"
      "s_barrier\n"
      "ds_read_b128 a[120:123], v15 offset:0\n"
      "v_fmac_f32_e64 v199, s45, v203\n"
      "v_pk_fma_f32 v[196:197], v[112:113], s[46:47], v[196:197] op_sel:[0,0,0] op_sel_hi:[1,0,1]\n"
      "v_pk_fma_f32 v[198:199], v[114:115], s[46:47], v[198:199] op_sel:[0,0,0] op_sel_hi:[1,0,1]\n"
      "v_pk_fma_f32 v[200:201], v[112:113], s[48:49], v[200:201] op_sel:[0,0,0] op_sel_hi:[1,0,1]\n"
      "v_pk_fma_f32 v[202:203], v[114:115], s[48:49], v[202:203] op_sel:[0,0,0] op_sel_hi:[1,0,1]\n"
      "ds_read_b128 a[124:127], v16 offset:0\n"
      "ds_read_b128 a[128:131], v17 offset:0\n"
      "v_cvt_pk_bf16_f32 v196, v196, v197\n"
      "v_cvt_pk_bf16_f32 v197, v198, v199\n"
      "v_pk_add_f32 v[212:213], v[212:213], v[220:221]\n"
      "v_pk_add_f32 v[214:215], v[214:215], v[222:223]\n"
      "v_add_f32 v216, v216, v224\n"
      "ds_write_b64 v13, v[196:197] offset:3072\n"
      "v_add_f32_e64 v217, v217, v225\n"
      "v_pk_add_f32 v[218:219], v[218:219], v[226:227]\n"
      "s_waitcnt lgkmcnt(3)\n"
      "v_pk_add_f32 v[216:217], v[216:217], v[212:213] neg_lo:[0,1] neg_hi:[0,1]\n"
      "v_pk_add_f32 v[218:219], v[218:219], v[214:215] neg_lo:[0,1] neg_hi:[0,1]\n"
      "v_pk_fma_f32 v[212:213], v[216:217], s[44:45], v[212:213] op_sel:[0,1,0] op_sel_hi:[1,1,1]\n"
      "v_pk_fma_f32 v[214:215], v[218:219], s[44:45], v[214:215] op_sel:[0,1,0] op_sel_hi:[1,1,1]\n"
      "v_pk_fma_f32 v[212:213], v[116:117], s[46:47], v[212:213] op_sel:[0,0,0] op_sel_hi:[1,0,1]\n"
      "v_pk_fma_f32 v[214:215], v[118:119], s[46:47], v[214:215] op_sel:[0,0,0] op_sel_hi:[1,0,1]\n"
      "v_pk_fma_f32 v[216:217], v[116:117], s[48:49], v[216:217] op_sel:[0,0,0] op_sel_hi:[1,0,1]\n"
      "v_pk_fma_f32 v[218:219], v[118:119], s[48:49], v[218:219] op_sel:[0,0,0] op_sel_hi:[1,0,1]\n"
      "v_cvt_pk_bf16_f32 v212, v212, v213\n"
      "v_cvt_pk_bf16_f32 v213, v214, v215\n"
      "v_pk_add_f32 v[228:229], v[228:229], v[236:237]\n"
      "v_pk_add_f32 v[230:231], v[230:231], v[238:239]\n"
      "ds_write_b64 v13, v[212:213] offset:3136\n"
      "v_pk_add_f32 v[232:233], v[232:233], v[240:241]\n"
      "v_pk_add_f32 v[234:235], v[234:235], v[242:243]\n"
      "v_pk_add_f32 v[232:233], v[232:233], v[228:229] neg_lo:[0,1] neg_hi:[0,1]\n"
      "v_pk_add_f32 v[234:235], v[234:235], v[230:231] neg_lo:[0,1] neg_hi:[0,1]\n"
      "v_pk_fma_f32 v[228:229], v[232:233], s[44:45], v[228:229] op_sel:[0,1,0] op_sel_hi:[1,1,1]\n"
      "v_pk_fma_f32 v[230:231], v[234:235], s[44:45], v[230:231] op_sel:[0,1,0] op_sel_hi:[1,1,1]\n"
      "v_pk_fma_f32 v[228:229], v[120:121], s[46:47], v[228:229] op_sel:[0,0,0] op_sel_hi:[1,0,1]\n"
      "v_pk_fma_f32 v[230:231], v[122:123], s[46:47], v[230:231] op_sel:[0,0,0] op_sel_hi:[1,0,1]\n"
      "v_pk_fma_f32 v[232:233], v[120:121], s[48:49], v[232:233] op_sel:[0,0,0] op_sel_hi:[1,0,1]\n"
      "v_pk_fma_f32 v[234:235], v[122:123], s[48:49], v[234:235] op_sel:[0,0,0] op_sel_hi:[1,0,1]\n"
      "v_cvt_pk_bf16_f32 v228, v228, v229\n"
      "v_cvt_pk_bf16_f32 v229, v230, v231\n"
      "v_pk_fma_f32 v[200:201], v[200:201], s[46:47], v[136:137] op_sel:[0,1,0] op_sel_hi:[1,1,1]\n"
      "v_pk_fma_f32 v[202:203], v[202:203], s[46:47], v[138:139] op_sel:[0,1,0] op_sel_hi:[1,1,1]\n"
      "v_cvt_pk_bf16_f32 v200, v200, v201\n"
      "v_cvt_pk_bf16_f32 v201, v202, v203\n"
      "v_pk_fma_f32 v[216:217], v[216:217], s[46:47], v[140:141] op_sel:[0,1,0] op_sel_hi:[1,1,1]\n"
      "buffer_store_dwordx4 a[120:123], v18, s[8:11], s52 offen offset:0 sc1\n"
      "ds_write_b64 v13, v[228:229] offset:3200\n"
      "ds_write_b64 v13, v[200:201] offset:15360\n"
      "s_nop 0\n"
      "s_waitcnt lgkmcnt(2)\n"
      "v_pk_fma_f32 v[218:219], v[218:219], s[46:47], v[142:143] op_sel:[0,1,0] op_sel_hi:[1,1,1]\n"
      "v_cvt_pk_bf16_f32 v216, v216, v217\n"
      "v_cvt_pk_bf16_f32 v217, v218, v219\n"
      "v_pk_fma_f32 v[232:233], v[232:233], s[46:47], v[144:145] op_sel:[0,1,0] op_sel_hi:[1,1,1]\n"
      "buffer_store_dwordx4 a[124:127], v18, s[8:11], s52 offen offset:1024 sc1\n"
      "ds_write_b64 v13, v[216:217] offset:15424\n"
      "buffer_store_dwordx4 a[128:131], v18, s[8:11], s52 offen offset:2048 sc1\n"
      "v_pk_fma_f32 v[234:235], v[234:235], s[46:47], v[146:147] op_sel:[0,1,0] op_sel_hi:[1,1,1]\n"
      "v_cvt_pk_bf16_f32 v232, v232, v233\n"
      "v_cvt_pk_bf16_f32 v233, v234, v235\n"
      "ds_write_b64 v13, v[232:233] offset:15488\n"
      "s_waitcnt lgkmcnt(0)\n"
      "s_barrier\n"
      "ds_read_b128 a[132:135], v15 offset:3072\n"
      "ds_read_b128 a[136:139], v16 offset:3072\n"
      "ds_read_b128 a[140:143], v17 offset:3072\n"
      "s_waitcnt lgkmcnt(2)\n"
      "buffer_store_dwordx4 a[132:135], v18, s[8:11], s53 offen offset:0 sc1\n"
      "s_waitcnt lgkmcnt(1)\n"
      "buffer_store_dwordx4 a[136:139], v18, s[8:11], s53 offen offset:1024 sc1\n"
      "s_waitcnt lgkmcnt(0)\n"
      "buffer_store_dwordx4 a[140:143], v18, s[8:11], s53 offen offset:2048 sc1\n"
      "LSend_%=:\n"
      "Lfin_%=:\n"
      "s_waitcnt vmcnt(0) lgkmcnt(0)\n"
      "s_endpgm\n"
      ".rept 512\n"
      "s_nop 0\n"
      ".endr\n"
      "Lpad_%=:\n"
      ".Lwg_end:\n"
      : "+{v0}"(tid)
      : "{s[0:1]}"(__builtin_amdgcn_kernarg_segment_ptr()), "{s2}"(blockIdx.x)
      : "v1", "v2", "v3", "v4", "v5", "v6", "v7", "v8", "v9", "v10", "v11", "v12", "v13", "v14", "v15", "v16", "v17", "v18", "v19", "v20", "v21", "v22", "v23", "v24", "v25", "v26", "v27", "v28", "v29", "v30", "v31", "v32", "v33", "v34", "v35", "v36", "v37", "v38", "v39", "v40", "v41", "v42", "v43", "v44", "v45", "v46", "v47", "v48", "v49", "v50", "v51", "v52", "v53", "v54", "v55", "v56", "v57", "v58", "v59", "v60", "v61", "v62", "v63", "v64", "v65", "v66", "v67", "v68", "v69", "v70", "v71", "v72", "v73", "v74", "v75", "v76", "v77", "v78", "v79", "v80", "v81", "v82", "v83", "v84", "v85", "v86", "v87", "v88", "v89", "v90", "v91", "v92", "v93", "v94", "v95", "v96", "v97", "v98", "v99", "v100", "v101", "v102", "v103", "v104", "v105", "v106", "v107", "v108", "v109", "v110", "v111", "v112", "v113", "v114", "v115", "v116", "v117", "v118", "v119", "v120", "v121", "v122", "v123", "v124", "v125", "v126", "v127", "v128", "v129", "v130", "v131", "v132", "v133", "v134", "v135", "v136", "v137", "v138", "v139", "v140", "v141", "v142", "v143", "v144", "v145", "v146", "v147", "v148", "v149", "v150", "v151", "v152", "v153", "v154", "v155", "v156", "v157", "v158", "v159", "v160", "v161", "v162", "v163", "v164", "v165", "v166", "v167", "v168", "v169", "v170", "v171", "v172", "v173", "v174", "v175", "v176", "v177", "v178", "v179", "v180", "v181", "v182", "v183", "v184", "v185", "v186", "v187", "v188", "v189", "v190", "v191", "v192", "v193", "v194", "v195", "v196", "v197", "v198", "v199", "v200", "v201", "v202", "v203", "v204", "v205", "v206", "v207", "v208", "v209", "v210", "v211", "v212", "v213", "v214", "v215", "v216", "v217", "v218", "v219", "v220", "v221", "v222", "v223", "v224", "v225", "v226", "v227", "v228", "v229", "v230", "v231", "v232", "v233", "v234", "v235", "v236", "v237", "v238", "v239", "v240", "v241", "v242", "v243", "v244", "v245", "v246", "v247", "v248", "v249", "v250", "v251", "v252", "v253", "v254", "v255", "a0", "a1", "a2", "a3", "a4", "a5", "a6", "a7", "a8", "a9", "a10", "a11", "a12", "a13", "a14", "a15", "a16", "a17", "a18", "a19", "a20", "a21", "a22", "a23", "a24", "a25", "a26", "a27", "a28", "a29", "a30", "a31", "a32", "a33", "a34", "a35", "a36", "a37", "a38", "a39", "a40", "a41", "a42", "a43", "a44", "a45", "a46", "a47", "a48", "a49", "a50", "a51", "a52", "a53", "a54", "a55", "a56", "a57", "a58", "a59", "a60", "a61", "a62", "a63", "a64", "a65", "a66", "a67", "a68", "a69", "a70", "a71", "a72", "a73", "a74", "a75", "a76", "a77", "a78", "a79", "a80", "a81", "a82", "a83", "a84", "a85", "a86", "a87", "a88", "a89", "a90", "a91", "a92", "a93", "a94", "a95", "a96", "a97", "a98", "a99", "a100", "a101", "a102", "a103", "a104", "a105", "a106", "a107", "a108", "a109", "a110", "a111", "a112", "a113", "a114", "a115", "a116", "a117", "a118", "a119", "a120", "a121", "a122", "a123", "a124", "a125", "a126", "a127", "a128", "a129", "a130", "a131", "a132", "a133", "a134", "a135", "a136", "a137", "a138", "a139", "a140", "a141", "a142", "a143", "a144", "a145", "a146", "a147", "a148", "a149", "a150", "a151", "a152", "a153", "a154", "a155", "a156", "a157", "a158", "a159", "a160", "a161", "a162", "a163", "a164", "a165", "a166", "a167", "a168", "a169", "a170", "a171", "a172", "a173", "a174", "a175", "a176", "a177", "a178", "a179", "a180", "a181", "a182", "a183", "a184", "a185", "a186", "a187", "a188", "a189", "a190", "a191", "a192", "a193", "a194", "a195", "a196", "a197", "a198", "a199", "a200", "a201", "a202", "a203", "a204", "a205", "a206", "a207", "a208", "a209", "a210", "a211", "a212", "a213", "a214", "a215", "a216", "a217", "a218", "a219", "a220", "a221", "a222", "a223", "a224", "a225", "a226", "a227", "a228", "a229", "a230", "a231", "a232", "a233", "a234", "a235", "a236", "a237", "a238", "a239", "a240", "a241", "a242", "a243", "a244", "a245", "a246", "a247", "a248", "a249", "a250", "a251", "a252", "a253", "a254", "a255", "s3", "s4", "s5", "s6", "s7", "s8", "s9", "s10", "s11", "s12", "s13", "s14", "s15", "s16", "s17", "s18", "s19", "s20", "s21", "s22", "s23", "s24", "s25", "s26", "s27", "s28", "s29", "s30", "s31", "s33", "s34", "s35", "s36", "s37", "s38", "s39", "s40", "s41", "s42", "s43", "s44", "s45", "s46", "s47", "s48", "s49", "s50", "s51", "s52", "s53", "s54", "s55", "s56", "s57", "s58", "s59", "s60", "s61", "s62", "s63", "s64", "s65", "s66", "s67", "s68", "s69", "s70", "s71", "s72", "s73", "s74", "s75", "s76", "s77", "s78", "s79", "s80", "s81", "s82", "s83", "s84", "s85", "s86", "s87", "s88", "s89", "s90", "s91", "s92", "s93", "s94", "s95", "s96", "s97", "s98", "s99", "scc", "vcc", "memory");
  asm volatile("" :: "v"((unsigned)(uintptr_t)smem) : "memory");
}

extern "C" void kernel_launch(void* const* d_in, const int* in_sizes, int n_in, void* d_out,
                              int out_size, void* d_ws, size_t ws_size, hipStream_t stream) {
  conv_kernel<<<256, 256, 0, stream>>>((const char*)d_in[0], (const char*)d_in[1], (char*)d_out, (char*)d_ws);
}
```

```cpp
#include <hip/hip_runtime.h>
#include <hip/hip_bf16.h>
#include <stdint.h>

__global__ void __launch_bounds__(256, 1)
conv_kernel(const char* __restrict__ x, const char* __restrict__ w, char* __restrict__ out, char* __restrict__ dbg) {
  __shared__ __attribute__((aligned(1024))) char smem[163840];
  unsigned tid = threadIdx.x;
  asm volatile(
      "s_load_dwordx2 s[12:13], s[0:1], 8\n"
      "s_load_dwordx2 s[4:5], s[0:1], 0\n"
      "s_load_dwordx2 s[8:9], s[0:1], 16\n"
      "v_readfirstlane_b32 s16, v0\n"
      "s_getpc_b64 s[26:27]\n"
      ".Lwg_pc:\n"
      "v_and_b32 v37, 3, v0\n"
      "s_lshr_b32 s23, s2, 3\n"
      "s_lshl_b32 s28, s16, 3\n"
      "v_lshlrev_b32 v40, 7, v37\n"
      "s_lshl_b32 s29, s23, 11\n"
      "s_mov_b32 m0, 150016\n"
      "s_add_u32 s28, s28, s29\n"
      "v_and_b32 v36, 63, v0\n"
      "v_add_u32 v40, s28, v40\n"
      "s_lshr_b32 s16, s16, 6\n"
      "v_min_u32 v40, .Lwg_end-.Lwg_pc-4, v40\n"
      "s_and_b32 s18, s16, 1\n"
      "global_load_lds_dword v40, s[26:27]\n"
      "v_lshlrev_b32 v92, 4, v36\n"
      "s_lshr_b32 s17, s16, 1\n"
      "s_and_b32 s28, s2, 7\n"
      "s_mul_i32 s19, s28, 6291456\n"
      "s_and_b32 s28, s23, 1\n"
      "s_mul_i32 s20, s28, 98304\n"
      "s_lshr_b32 s28, s23, 1\n"
      "s_mul_i32 s29, s28, 6144\n"
      "s_add_u32 s21, s20, s29\n"
      "s_add_u32 s6, s19, 6492160\n"
      "s_min_u32 s6, s6, 50335744\n"
      "s_mov_b32 s7, 0x20000\n"
      "s_mov_b32 s10, 50331648\n"
      "s_mov_b32 s11, 0x20000\n"
      "s_mov_b32 s14, 165888\n"
      "s_mov_b32 s15, 0x20000\n"
      "s_mul_i32 s63, s18, 3072\n"
      "s_add_u32 s54, s19, s20\n"
      "s_sub_u32 s54, s54, 196608\n"
      "s_lshl_b32 s64, s18, 8\n"
      "s_mul_i32 s28, s17, 24832\n"
      "s_add_u32 s64, s64, s28\n"
      "s_mov_b32 s55, 0\n"
      "v_bitop3_b32 v93, v36, v92, 32 bitop3:0x6c\n"
      "v_xor_b32 v94, 32, v92\n"
      "v_bfe_u32 v38, v36, 2, 2\n"
      "v_lshrrev_b32 v39, 4, v36\n"
      "s_cmp_eq_u32 s17, 1\n"
      "s_cbranch_scc1 LBD_%=\n"
      "v_lshlrev_b32 v40, 3, v92\n"
      "s_mul_i32 s53, s18, 2688\n"
      "s_mul_i32 s34, s23, 5184\n"
      "v_min_u32 v40, 2560, v40\n"
      "s_add_u32 s53, s53, s34\n"
      "v_lshlrev_b32 v43, 4, v37\n"
      "s_mul_i32 s26, s17, 0x8080\n"
      "s_sub_u32 s26, 0xbf80, s26\n"
      "s_mov_b32 s27, 0\n"
      "s_lshl_b32 s34, s17, 7\n"
      "s_mov_b32 s29, 0\n"
      "s_sub_u32 s28, s26, s34\n"
      "s_mov_b32 s31, 0\n"
      "s_sub_u32 s30, s28, s34\n"
      "v_lshlrev_b64 v[44:45], v43, s[26:27]\n"
      "v_lshlrev_b64 v[46:47], v43, s[28:29]\n"
      "s_xor_b32 s28, s28, 0x8000\n"
      "v_lshlrev_b64 v[48:49], v43, s[30:31]\n"
      "v_lshlrev_b64 v[50:51], v43, s[28:29]\n"
      "v_accvgpr_write_b32 a244, v44\n"
      "v_accvgpr_write_b32 a245, v45\n"
      "v_accvgpr_write_b32 a246, v46\n"
      "v_accvgpr_write_b32 a247, v47\n"
      "v_accvgpr_write_b32 a248, v48\n"
      "v_accvgpr_write_b32 a249, v49\n"
      "v_accvgpr_write_b32 a250, v50\n"
      "v_accvgpr_write_b32 a251, v51\n"
      "s_waitcnt lgkmcnt(0)\n"
      "s_and_b32 s13, s13, 0xffff\n"
      "buffer_load_dword v40, s[12:15], s53 offen lds\n"
      "s_add_u32 m0, s63, 30976\n"
      "s_add_u32 s52, s63, 55296\n"
      "buffer_load_dwordx4 v92, s[12:15], s52 offen offset:0 lds\n"
      "buffer_load_dwordx4 v93, s[12:15], s52 offen offset:1024 lds\n"
      "buffer_load_dwordx4 v94, s[12:15], s52 offen offset:2048 lds\n"
      "s_add_u32 m0, s63, 0\n"
      "s_add_u32 s52, s63, 110592\n"
      "buffer_load_dwordx4 v92, s[12:15], s52 offen offset:0 lds\n"
      "buffer_load_dwordx4 v93, s[12:15], s52 offen offset:1024 lds\n"
      "buffer_load_dwordx4 v94, s[12:15], s52 offen offset:2048 lds\n"
      "s_branch LS_%=\n"
      "LBD_%=:\n"
      "s_sub_u32 s26, s21, s20\n"
      "s_lshl_b32 s37, s18, 11\n"
      "s_add_u32 s26, s26, s37\n"
      "s_add_u32 s27, s26, 3712\n"
      "v_add_u32 v44, s27, v92\n"
      "s_cmp_eq_u32 s18, 1\n"
      "s_cselect_b32 s28, 16, 48\n"
      "s_cselect_b32 s29, 0, 32\n"
      "s_cselect_b32 s27, 48, 64\n"
      "v_cmp_le_u32 vcc, s28, v36\n"
      "v_cmp_gt_u32 s[30:31], s29, v36\n"
      "v_xor_b32 v45, 32, v44\n"
      "s_cmp_eq_u32 s26, 0\n"
      "s_cselect_b32 s28, 24, 0\n"
      "v_cndmask_b32 v10, v44, v45, vcc\n"
      "v_cndmask_b32 v46, v44, v45, s[30:31]\n"
      "v_cmp_le_u32 vcc, s28, v36\n"
      "v_cmp_gt_u32 s[30:31], s27, v36\n"
      "s_cmp_eq_u32 s26, 94208\n"
      "s_cselect_b32 s27, 24, 64\n"
      "v_cmp_gt_u32 s[34:35], s27, v36\n"
      "v_subrev_u32 v45, 256, v44\n"
      "v_cndmask_b32 v8, 2.0, v10, vcc\n"
      "v_cndmask_b32 v9, v45, v46, s[30:31]\n"
      "v_cndmask_b32 v11, 2.0, v9, s[34:35]\n"
      "s_add_u32 s34, s19, s21\n"
      "s_cmp_eq_u32 s19, 0\n"
      "s_cselect_b32 s36, 0x80000000, s54\n"
      "s_waitcnt lgkmcnt(0)\n"
      "s_load_dword s44, s[4:5], s34\n"
      "s_and_b32 s13, s13, 0xffff\n"
      "s_add_u32 m0, s63, 24832\n"
      "s_add_u32 s52, s63, 0\n"
      "buffer_load_dwordx4 v92, s[12:15], s52 offen offset:0 lds\n"
      "buffer_load_dwordx4 v93, s[12:15], s52 offen offset:1024 lds\n"
      "buffer_load_dwordx4 v94, s[12:15], s52 offen offset:2048 lds\n"
      "s_add_u32 s4, s4, -4096\n"
      "s_addc_u32 s5, s5, -1\n"
      "s_and_b32 s5, s5, 0xffff\n"
      "s_add_u32 s53, s54, 196608\n"
      "s_add_u32 m0, s37, 82432\n"
      "s_nop 0\n"
      "buffer_load_dwordx4 v8, s[4:7], s53 offen offset:0 nt lds\n"
      "buffer_load_dwordx4 v9, s[4:7], s53 offen offset:1024 nt lds\n"
      "s_add_u32 s53, s54, 393216\n"
      "s_add_u32 m0, s37, 86536\n"
      "s_nop 0\n"
      "buffer_load_dwordx4 v8, s[4:7], s53 offen offset:0 nt lds\n"
      "buffer_load_dwordx4 v9, s[4:7], s53 offen offset:1024 nt lds\n"
      "s_add_u32 s53, s54, 589824\n"
      "s_add_u32 m0, s37, 90760\n"
      "s_nop 0\n"
      "buffer_load_dwordx4 v8, s[4:7], s53 offen offset:0 nt lds\n"
      "buffer_load_dwordx4 v9, s[4:7], s53 offen offset:1024 nt lds\n"
      "s_add_u32 s53, s54, 786432\n"
      "s_add_u32 m0, s37, 95104\n"
      "s_nop 0\n"
      "buffer_load_dwordx4 v8, s[4:7], s53 offen offset:0 nt lds\n"
      "buffer_load_dwordx4 v9, s[4:7], s53 offen offset:1024 nt lds\n"
      "s_add_u32 s53, s36, 0\n"
      "s_add_u32 m0, s37, 150272\n"
      "s_nop 0\n"
      "buffer_load_dwordx4 v8, s[4:7], s53 offen offset:0 nt lds\n"
      "buffer_load_dwordx4 v9, s[4:7], s53 offen offset:1024 nt lds\n"
      "v_lshlrev_b32 v43, 4, v37\n"
      "v_lshlrev_b32 v44, 2, v37\n"
      "v_and_b32 v43, 16, v43\n"
      "v_and_or_b32 v43, v44, 8, v43\n"
      "v_mul_u32_u24 v44, 192, v38\n"
      "v_mul_u32_u24 v45, 1536, v39\n"
      "s_lshl_b32 s27, s18, 5\n"
      "v_lshlrev_b32 v46, 1, v36\n"
      "v_add3_u32 v43, v43, v44, v45\n"
      "v_bitop3_b32 v46, v46, s27, 32 bitop3:0x6c\n"
      "v_add_u32 v95, v43, v46\n"
      "v_add_u32 v96, 57344, v95\n"
      "v_add_u32 v97, 114688, v95\n"
      "v_lshlrev_b32 v43, 4, v37\n"
      "s_mul_i32 s26, s17, 0x8080\n"
      "s_sub_u32 s26, 0xbf80, s26\n"
      "s_mov_b32 s27, 0\n"
      "s_lshl_b32 s34, s17, 7\n"
      "s_mov_b32 s29, 0\n"
      "s_sub_u32 s28, s26, s34\n"
      "s_mov_b32 s31, 0\n"
      "s_sub_u32 s30, s28, s34\n"
      "v_lshlrev_b64 v[44:45], v43, s[26:27]\n"
      "v_lshlrev_b64 v[46:47], v43, s[28:29]\n"
      "s_xor_b32 s28, s28, 0x8000\n"
      "v_lshlrev_b64 v[48:49], v43, s[30:31]\n"
      "v_lshlrev_b64 v[50:51], v43, s[28:29]\n"
      "v_accvgpr_write_b32 a244, v44\n"
      "v_accvgpr_write_b32 a245, v45\n"
      "v_accvgpr_write_b32 a246, v46\n"
      "v_accvgpr_write_b32 a247, v47\n"
      "v_accvgpr_write_b32 a248, v48\n"
      "v_accvgpr_write_b32 a249, v49\n"
      "v_accvgpr_write_b32 a250, v50\n"
      "v_accvgpr_write_b32 a251, v51\n"
      "s_lshl_b32 s26, s18, 3\n"
      "v_and_b32 v42, 15, v36\n"
      "v_xor_b32 v42, s26, v42\n"
      "s_lshl_b32 s26, s17, 13\n"
      "s_add_u32 s26, s26, 24832\n"
      "s_mul_i32 s27, s17, 12672\n"
      "v_mul_u32_u24 v44, 192, v42\n"
      "v_lshlrev_b32 v43, 3, v42\n"
      "v_and_b32 v45, 48, v36\n"
      "s_add_u32 s27, s27, 82432\n"
      "v_bitop3_b32 v46, v43, v45, 32 bitop3:0x6c\n"
      "v_add3_u32 v0, v46, v44, s26\n"
      "v_add3_u32 v3, v46, v44, s27\n"
      "v_add_u32 v43, 16, v43\n"
      "v_add_u32 v44, 384, v44\n"
      "v_bitop3_b32 v46, v43, v45, 32 bitop3:0x6c\n"
      "v_add3_u32 v1, v46, v44, s26\n"
      "v_add3_u32 v4, v46, v44, s27\n"
      "v_add_u32 v43, 16, v43\n"
      "v_add_u32 v44, 384, v44\n"
      "v_bitop3_b32 v46, v43, v45, 32 bitop3:0x6c\n"
      "v_add3_u32 v2, v46, v44, s26\n"
      "v_add3_u32 v5, v46, v44, s27\n"
      "v_lshlrev_b32 v43, 31, v36\n"
      "v_and_b32 v44, 2, v36\n"
      "v_or_b32 v43, 0x3f803f80, v43\n"
      "v_mul_u32_u24 v45, 0xc00080, v44\n"
      "v_lshl_add_u32 v44, v44, 22, v43\n"
      "v_add_u32 v45, v45, v43\n"
      "v_accvgpr_write_b32 a216, v44\n"
      "v_accvgpr_write_b32 a217, v45\n"
      "v_lshlrev_b32 v43, 3, v38\n"
      "s_mov_b32 s26, 806420736\n"
      "v_bfe_u32 v43, s26, v43, 8\n"
      "v_lshlrev_b32 v44, 12, v38\n"
      "s_lshl_b32 s28, s18, 8\n"
      "s_add_u32 s28, s28, 82432\n"
      "v_lshl_add_u32 v43, v43, 3, v44\n"
      "v_lshl_add_u32 v44, v39, 2, v37\n"
      "v_lshl_add_u32 v43, v44, 4, v43\n"
      "v_add_u32 v7, s28, v43\n"
      "s_mul_i32 s26, s17, 70144\n"
      "s_lshl_b32 s27, s18, 8\n"
      "s_sub_u32 s26, 70144, s26\n"
      "s_add_u32 s26, s26, s27\n"
      "v_lshl_add_u32 v6, v36, 2, s26\n"
      "v_lshrrev_b32 v43, 3, v36\n"
      "v_and_b32 v44, 7, v36\n"
      "v_mul_u32_u24 v45, 384, v43\n"
      "v_lshl_add_u32 v18, v44, 4, v45\n"
      "v_and_b32 v44, 2, v36\n"
      "v_lshlrev_b32 v43, 3, v43\n"
      "v_lshlrev_b32 v45, 4, v44\n"
      "v_xor_b32 v43, v43, v45\n"
      "v_lshl_add_u32 v43, v44, 7, v43\n"
      "v_and_b32 v44, 1, v36\n"
      "s_lshl_b32 s26, s18, 11\n"
      "v_lshl_add_u32 v43, v44, 6, v43\n"
      "s_add_u32 s26, s26, 12288\n"
      "v_bfe_u32 v44, v36, 2, 1\n"
      "v_add_u32 v43, s26, v43\n"
      "v_mul_i32_i24 v45, 8196, v44\n"
      "s_mov_b32 s27, 0\n"
      "v_add3_u32 v12, v43, v45, s27\n"
      "v_add_u32 v15, 128, v12\n"
      "v_mul_i32_i24 v45, -4092, v44\n"
      "s_mov_b32 s27, 4096\n"
      "v_add3_u32 v13, v43, v45, s27\n"
      "v_add_u32 v16, 128, v13\n"
      "v_mul_i32_i24 v45, -4100, v44\n"
      "s_mov_b32 s27, 8200\n"
      "v_add3_u32 v14, v43, v45, s27\n"
      "v_add_u32 v17, 128, v14\n"
      "s_mul_i32 s62, s18, 393216\n"
      "s_add_u32 s62, s62, s19\n"
      "s_add_u32 s62, s62, s21\n"
      "s_sub_u32 s62, s62, 786432\n"
      "s_mov_b32 s58, 0x80000000\n"
      "s_and_b32 s9, s9, 0xffff\n"
      "s_mov_b32 s45, 0xbf2aaaab\n"
      "s_mov_b32 s46, 0xbe800000\n"
      "s_mov_b32 s47, 0x3f2aaaab\n"
      "s_mov_b32 s48, 0xc0c00000\n"
      "s_mov_b32 s49, 0x3f000000\n"
      "s_add_u32 m0, s63, 116224\n"
      "s_add_u32 s52, s63, 6144\n"
      "buffer_load_dwordx4 v92, s[12:15], s52 offen offset:0 lds\n"
      "buffer_load_dwordx4 v93, s[12:15], s52 offen offset:1024 lds\n"
      "buffer_load_dwordx4 v94, s[12:15], s52 offen offset:2048 lds\n"
      "LDpro_%=:\n"
      "s_waitcnt vmcnt(13)\n"
      "s_barrier\n"
      "ds_read_b64_tr_b16 a[0:1], v95 offset:24832\n"
      "ds_read_b64_tr_b16 a[2:3], v95 offset:25600\n"
      "ds_read_b64_tr_b16 a[224:225], v95 offset:0\n"
      "ds_read_b64_tr_b16 a[226:227], v95 offset:768\n"
      "ds_read_b64_tr_b16 a[220:221], v95 offset:30976\n"
      "ds_read_b64_tr_b16 a[222:223], v95 offset:31744\n"
      "ds_read_b64_tr_b16 a[4:5], v95 offset:24896\n"
      "ds_read_b64_tr_b16 a[6:7], v95 offset:25664\n"
      "ds_read_b64_tr_b16 a[232:233], v95 offset:64\n"
      "ds_read_b64_tr_b16 a[234:235], v95 offset:832\n"
      "ds_read_b64_tr_b16 a[228:229], v95 offset:31040\n"
      "ds_read_b64_tr_b16 a[230:231], v95 offset:31808\n"
      "s_add_u32 m0, s63, 18432\n"
      "s_add_u32 s52, s63, 122880\n"
      "s_nop 0\n"
      "s_waitcnt lgkmcnt(10)\n"
      "v_mfma_f32_4x4x4_16b_bf16 v[28:31], a[244:245], a[0:1], 0\n"
      "v_mfma_f32_4x4x4_16b_bf16 v[32:35], a[244:245], a[2:3], 0\n"
      "buffer_load_dwordx4 v92, s[12:15], s52 offen offset:0 lds\n"
      "s_waitcnt lgkmcnt(8)\n"
      "v_mfma_f32_4x4x4_16b_bf16 v[28:31], a[248:249], a[224:225], v[28:31]\n"
      "buffer_load_dwordx4 v93, s[12:15], s52 offen offset:1024 lds\n"
      "v_mfma_f32_4x4x4_16b_bf16 v[32:35], a[248:249], a[226:227], v[32:35]\n"
      "buffer_load_dwordx4 v94, s[12:15], s52 offen offset:2048 lds\n"
      "s_waitcnt lgkmcnt(6)\n"
      "v_mfma_f32_4x4x4_16b_bf16 v[36:39], a[246:247], a[220:221], v[28:31]\n"
      "ds_read_b64_tr_b16 a[8:9], v95 offset:24960\n"
      "v_mfma_f32_4x4x4_16b_bf16 v[40:43], a[246:247], a[222:223], v[32:35]\n"
      "ds_read_b64_tr_b16 a[10:11], v95 offset:25728\n"
      "v_mfma_f32_4x4x4_16b_bf16 v[28:31], a[250:251], a[220:221], v[28:31]\n"
      "ds_read_b64_tr_b16 a[240:241], v95 offset:128\n"
      "v_mfma_f32_4x4x4_16b_bf16 v[32:35], a[250:251], a[222:223], v[32:35]\n"
      "ds_read_b64_tr_b16 a[242:243], v95 offset:896\n"
      "ds_read_b64_tr_b16 a[236:237], v95 offset:31104\n"
      "ds_read_b64_tr_b16 a[238:239], v95 offset:31872\n"
      "s_add_u32 m0, s63, 53504\n"
      "s_add_u32 s52, s63, 129024\n"
      "s_nop 0\n"
      "s_waitcnt lgkmcnt(6)\n"
      "v_mfma_f32_4x4x4_16b_bf16 v[76:79], a[244:245], a[4:5], 0\n"
      "v_mfma_f32_4x4x4_16b_bf16 v[80:83], a[244:245], a[6:7], 0\n"
      "buffer_load_dwordx4 v92, s[12:15], s52 offen offset:0 lds\n"
      "v_mfma_f32_4x4x4_16b_bf16 v[76:79], a[248:249], a[232:233], v[76:79]\n"
      "buffer_load_dwordx4 v93, s[12:15], s52 offen offset:1024 lds\n"
      "v_mfma_f32_4x4x4_16b_bf16 v[80:83], a[248:249], a[234:235], v[80:83]\n"
      "buffer_load_dwordx4 v94, s[12:15], s52 offen offset:2048 lds\n"
      "v_mfma_f32_4x4x4_16b_bf16 v[84:87], a[246:247], a[228:229], v[76:79]\n"
      "v_mfma_f32_4x4x4_16b_bf16 v[88:91], a[246:247], a[230:231], v[80:83]\n"
      "v_mfma_f32_4x4x4_16b_bf16 v[76:79], a[250:251], a[228:229], v[76:79]\n"
      "v_mfma_f32_4x4x4_16b_bf16 v[80:83], a[250:251], a[230:231], v[80:83]\n"
      "v_cvt_pk_bf16_f32 v252, v36, v40\n"
      "v_cvt_pk_bf16_f32 v253, v37, v41\n"
      "v_cvt_pk_bf16_f32 v254, v38, v42\n"
      "v_cvt_pk_bf16_f32 v255, v39, v43\n"
      "v_cvt_pk_bf16_f32 v248, v28, v32\n"
      "v_cvt_pk_bf16_f32 v249, v29, v33\n"
      "v_cvt_pk_bf16_f32 v250, v30, v34\n"
      "v_cvt_pk_bf16_f32 v251, v31, v35\n"
      "s_nop 0\n"
      "s_waitcnt lgkmcnt(0)\n"
      "v_mfma_f32_4x4x4_16b_bf16 v[28:31], a[244:245], a[8:9], 0\n"
      "v_mfma_f32_4x4x4_16b_bf16 v[32:35], a[244:245], a[10:11], 0\n"
      "s_nop 0\n"
      "v_mfma_f32_4x4x4_16b_bf16 v[28:31], a[248:249], a[240:241], v[28:31]\n"
      "v_mfma_f32_4x4x4_16b_bf16 v[32:35], a[248:249], a[242:243], v[32:35]\n"
      "s_nop 0\n"
      "v_mfma_f32_4x4x4_16b_bf16 v[36:39], a[246:247], a[236:237], v[28:31]\n"
      "v_mfma_f32_4x4x4_16b_bf16 v[40:43], a[246:247], a[238:239], v[32:35]\n"
      "v_mfma_f32_4x4x4_16b_bf16 v[28:31], a[250:251], a[236:237], v[28:31]\n"
      "v_mfma_f32_4x4x4_16b_bf16 v[32:35], a[250:251], a[238:239], v[32:35]\n"
      "v_cvt_pk_bf16_f32 v244, v84, v88\n"
      "v_cvt_pk_bf16_f32 v245, v85, v89\n"
      "v_cvt_pk_bf16_f32 v246, v86, v90\n"
      "v_cvt_pk_bf16_f32 v247, v87, v91\n"
      "v_cvt_pk_bf16_f32 v240, v76, v80\n"
      "v_cvt_pk_bf16_f32 v241, v77, v81\n"
      "v_cvt_pk_bf16_f32 v242, v78, v82\n"
      "v_cvt_pk_bf16_f32 v243, v79, v83\n"
      "s_waitcnt vmcnt(6)\n"
      "s_barrier\n"
      "ds_read_b64_tr_b16 a[12:13], v97 offset:1536\n"
      "ds_read_b64_tr_b16 a[14:15], v97 offset:2304\n"
      "ds_read_b64_tr_b16 a[224:225], v97 offset:13824\n"
      "ds_read_b64_tr_b16 a[226:227], v97 offset:14592\n"
      "ds_read_b64_tr_b16 a[220:221], v97 offset:7680\n"
      "ds_read_b64_tr_b16 a[222:223], v97 offset:8448\n"
      "s_add_u32 m0, s63, 71936\n"
      "s_add_u32 s52, s63, 135168\n"
      "s_waitcnt lgkmcnt(4)\n"
      "v_mfma_f32_4x4x4_16b_bf16 v[76:79], a[244:245], a[12:13], 0\n"
      "v_mfma_f32_4x4x4_16b_bf16 v[80:83], a[244:245], a[14:15], 0\n"
      "buffer_load_dwordx4 v92, s[12:15], s52 offen offset:0 lds\n"
      "s_waitcnt lgkmcnt(2)\n"
      "v_mfma_f32_4x4x4_16b_bf16 v[76:79], a[248:249], a[224:225], v[76:79]\n"
      "buffer_load_dwordx4 v93, s[12:15], s52 offen offset:1024 lds\n"
      "v_mfma_f32_4x4x4_16b_bf16 v[80:83], a[248:249], a[226:227], v[80:83]\n"
      "buffer_load_dwordx4 v94, s[12:15], s52 offen offset:2048 lds\n"
      "s_nop 0\n"
      "s_waitcnt lgkmcnt(0)\n"
      "v_mfma_f32_4x4x4_16b_bf16 v[84:87], a[246:247], a[220:221], v[76:79]\n"
      "ds_read_b64_tr_b16 a[16:17], v97 offset:1600\n"
      "v_mfma_f32_4x4x4_16b_bf16 v[88:91], a[246:247], a[222:223], v[80:83]\n"
      "ds_read_b64_tr_b16 a[18:19], v97 offset:2368\n"
      "v_mfma_f32_4x4x4_16b_bf16 v[76:79], a[250:251], a[220:221], v[76:79]\n"
      "ds_read_b64_tr_b16 a[232:233], v97 offset:13888\n"
      "v_mfma_f32_4x4x4_16b_bf16 v[80:83], a[250:251], a[222:223], v[80:83]\n"
      "ds_read_b64_tr_b16 a[234:235], v97 offset:14656\n"
      "ds_read_b64_tr_b16 a[228:229], v97 offset:7744\n"
      "ds_read_b64_tr_b16 a[230:231], v97 offset:8512\n"
      "ds_read_b64_tr_b16 a[20:21], v97 offset:1664\n"
      "ds_read_b64_tr_b16 a[22:23], v97 offset:2432\n"
      "ds_read_b64_tr_b16 a[240:241], v97 offset:13952\n"
      "ds_read_b64_tr_b16 a[242:243], v97 offset:14720\n"
      "ds_read_b64_tr_b16 a[236:237], v97 offset:7808\n"
      "ds_read_b64_tr_b16 a[238:239], v97 offset:8576\n"
      "ds_read_b64_tr_b16 a[188:189], v7 offset:2048\n"
      "ds_read_b64_tr_b16 a[190:191], v7 offset:2056\n"
      "ds_read_b128 a[192:195], v3 offset:55168\n"
      "v_cvt_pk_bf16_f32 v236, v36, v40\n"
      "v_cvt_pk_bf16_f32 v237, v37, v41\n"
      "v_cvt_pk_bf16_f32 v238, v38, v42\n"
      "v_cvt_pk_bf16_f32 v239, v39, v43\n"
      "v_cvt_pk_bf16_f32 v232, v28, v32\n"
      "v_cvt_pk_bf16_f32 v233, v29, v33\n"
      "v_cvt_pk_bf16_f32 v234, v30, v34\n"
      "v_cvt_pk_bf16_f32 v235, v31, v35\n"
      "s_waitcnt vmcnt(6)\n"
      "s_barrier\n"
      "s_nop 0\n"
      "s_waitcnt lgkmcnt(8)\n"
      "v_mfma_f32_4x4x4_16b_bf16 v[28:31], a[244:245], a[16:17], 0\n"
      "v_mfma_f32_4x4x4_16b_bf16 v[32:35], a[244:245], a[18:19], 0\n"
      "ds_read_b64_tr_b16 a[24:25], v95 offset:6144\n"
      "v_mfma_f32_4x4x4_16b_bf16 v[28:31], a[248:249], a[232:233], v[28:31]\n"
      "ds_read_b64_tr_b16 a[26:27], v95 offset:6912\n"
      "v_mfma_f32_4x4x4_16b_bf16 v[32:35], a[248:249], a[234:235], v[32:35]\n"
      "ds_read_b64_tr_b16 a[224:225], v95 offset:18432\n"
      "v_mfma_f32_4x4x4_16b_bf16 v[36:39], a[246:247], a[228:229], v[28:31]\n"
      "ds_read_b64_tr_b16 a[226:227], v95 offset:19200\n"
      "v_mfma_f32_4x4x4_16b_bf16 v[40:43], a[246:247], a[230:231], v[32:35]\n"
      "ds_read_b64_tr_b16 a[220:221], v95 offset:12288\n"
      "v_mfma_f32_4x4x4_16b_bf16 v[28:31], a[250:251], a[228:229], v[28:31]\n"
      "ds_read_b64_tr_b16 a[222:223], v95 offset:13056\n"
      "v_mfma_f32_4x4x4_16b_bf16 v[32:35], a[250:251], a[230:231], v[32:35]\n"
      "ds_read_b64_tr_b16 a[184:185], v7 offset:2560\n"
      "s_waitcnt lgkmcnt(7)\n"
      "v_mfma_f32_4x4x4_16b_bf16 v[44:47], a[216:217], a[188:189], 0\n"
      "ds_read_b64_tr_b16 a[186:187], v7 offset:2568\n"
      "v_mfma_f32_4x4x4_16b_bf16 v[48:51], a[216:217], a[190:191], 0\n"
      "ds_read_b128 a[196:199], v3 offset:55232\n"
      "v_cvt_pk_bf16_f32 v228, v84, v88\n"
      "v_cvt_pk_bf16_f32 v229, v85, v89\n"
      "v_cvt_pk_bf16_f32 v230, v86, v90\n"
      "v_cvt_pk_bf16_f32 v231, v87, v91\n"
      "v_cvt_pk_bf16_f32 v224, v76, v80\n"
      "v_cvt_pk_bf16_f32 v225, v77, v81\n"
      "v_cvt_pk_bf16_f32 v226, v78, v82\n"
      "v_cvt_pk_bf16_f32 v227, v79, v83\n"
      "s_add_u32 m0, s63, 0\n"
      "s_add_u32 s52, s63, 141312\n"
      "v_mfma_f32_4x4x4_16b_bf16 v[76:79], a[244:245], a[20:21], 0\n"
      "v_mfma_f32_4x4x4_16b_bf16 v[80:83], a[244:245], a[22:23], 0\n"
      "buffer_load_dwordx4 v92, s[12:15], s52 offen offset:0 lds\n"
      "v_mfma_f32_4x4x4_16b_bf16 v[76:79], a[248:249], a[240:241], v[76:79]\n"
      "buffer_load_dwordx4 v93, s[12:15], s52 offen offset:1024 lds\n"
      "v_mfma_f32_4x4x4_16b_bf16 v[80:83], a[248:249], a[242:243], v[80:83]\n"
      "buffer_load_dwordx4 v94, s[12:15], s52 offen offset:2048 lds\n"
      "v_mfma_f32_4x4x4_16b_bf16 v[84:87], a[246:247], a[236:237], v[76:79]\n"
      "ds_read_b64_tr_b16 a[28:29], v95 offset:6208\n"
      "v_mfma_f32_4x4x4_16b_bf16 v[88:91], a[246:247], a[238:239], v[80:83]\n"
      "ds_read_b64_tr_b16 a[30:31], v95 offset:6976\n"
      "v_mfma_f32_4x4x4_16b_bf16 v[76:79], a[250:251], a[236:237], v[76:79]\n"
      "ds_read_b64_tr_b16 a[232:233], v95 offset:18496\n"
      "v_mfma_f32_4x4x4_16b_bf16 v[80:83], a[250:251], a[238:239], v[80:83]\n"
      "ds_read_b64_tr_b16 a[234:235], v95 offset:19264\n"
      "s_nop 0\n"
      "s_waitcnt lgkmcnt(4)\n"
      "v_mfma_f32_4x4x4_16b_bf16 v[112:115], a[216:217], a[184:185], 0\n"
      "ds_read_b64_tr_b16 a[228:229], v95 offset:12352\n"
      "v_mfma_f32_4x4x4_16b_bf16 v[116:119], a[216:217], a[186:187], 0\n"
      "ds_read_b64_tr_b16 a[230:231], v95 offset:13120\n"
      "ds_read_b64_tr_b16 a[180:181], v7 offset:3072\n"
      "ds_read_b64_tr_b16 a[182:183], v7 offset:3080\n"
      "v_cvt_pk_bf16_f32 v220, v36, v40\n"
      "v_cvt_pk_bf16_f32 v221, v37, v41\n"
      "v_cvt_pk_bf16_f32 v222, v38, v42\n"
      "v_cvt_pk_bf16_f32 v223, v39, v43\n"
      "v_cvt_pk_bf16_f32 v216, v28, v32\n"
      "v_cvt_pk_bf16_f32 v217, v29, v33\n"
      "v_mfma_f32_16x16x32_bf16 v[100:103], a[0:3], a[192:195], 0\n"
      "v_cvt_pk_bf16_f32 v218, v30, v34\n"
      "v_cvt_pk_bf16_f32 v219, v31, v35\n"
      "v_mfma_f32_16x16x32_bf16 v[104:107], a[4:7], a[192:195], 0\n"
      "v_cvt_pk_bf16_f32 v44, v44, v48\n"
      "v_cvt_pk_bf16_f32 v45, v45, v49\n"
      "v_mfma_f32_16x16x32_bf16 v[108:111], a[8:11], a[192:195], 0\n"
      "v_cvt_pk_bf16_f32 v46, v46, v50\n"
      "v_cvt_pk_bf16_f32 v47, v47, v51\n"
      "s_add_u32 s53, s54, 199680\n"
      "s_add_u32 m0, s37, 99328\n"
      "v_mfma_f32_4x4x4_16b_bf16 v[28:31], a[244:245], a[24:25], 0\n"
      "v_mfma_f32_4x4x4_16b_bf16 v[32:35], a[244:245], a[26:27], 0\n"
      "buffer_load_dwordx4 v10, s[4:7], s53 offen offset:0 nt lds\n"
      "buffer_load_dwordx4 v11, s[4:7], s53 offen offset:1024 nt lds\n"
      "v_mfma_f32_4x4x4_16b_bf16 v[28:31], a[248:249], a[224:225], v[28:31]\n"
      "s_add_u32 s53, s54, 396288\n"
      "s_add_u32 m0, s37, 103432\n"
      "v_mfma_f32_4x4x4_16b_bf16 v[32:35], a[248:249], a[226:227], v[32:35]\n"
      "buffer_load_dwordx4 v10, s[4:7], s53 offen offset:0 nt lds\n"
      "buffer_load_dwordx4 v11, s[4:7], s53 offen offset:1024 nt lds\n"
      "v_mfma_f32_4x4x4_16b_bf16 v[36:39], a[246:247], a[220:221], v[28:31]\n"
      "ds_read_b64_tr_b16 a[32:33], v95 offset:6272\n"
      "v_mfma_f32_4x4x4_16b_bf16 v[40:43], a[246:247], a[222:223], v[32:35]\n"
      "ds_read_b64_tr_b16 a[34:35], v95 offset:7040\n"
      "v_mfma_f32_4x4x4_16b_bf16 v[28:31], a[250:251], a[220:221], v[28:31]\n"
      "ds_read_b64_tr_b16 a[240:241], v95 offset:18560\n"
      "v_mfma_f32_4x4x4_16b_bf16 v[32:35], a[250:251], a[222:223], v[32:35]\n"
      "ds_read_b64_tr_b16 a[242:243], v95 offset:19328\n"
      "s_nop 0\n"
      "s_waitcnt lgkmcnt(4)\n"
      "v_mfma_f32_4x4x4_16b_bf16 v[20:23], a[216:217], a[180:181], 0\n"
      "ds_read_b64_tr_b16 a[236:237], v95 offset:12416\n"
      "v_mfma_f32_4x4x4_16b_bf16 v[24:27], a[216:217], a[182:183], 0\n"
      "ds_read_b64_tr_b16 a[238:239], v95 offset:13184\n"
      "ds_read_b64_tr_b16 a[176:177], v7 offset:3584\n"
      "ds_read_b64_tr_b16 a[178:179], v7 offset:3592\n"
      "s_mov_b32 m0, s64\n"
      "s_nop 0\n"
      "ds_write_addtid_b32 v44 offset:2048\n"
      "ds_write_addtid_b32 v45 offset:6144\n"
      "ds_write_addtid_b32 v46 offset:10240\n"
      "ds_write_addtid_b32 v47 offset:14336\n"
      "v_cvt_pk_bf16_f32 v212, v84, v88\n"
      "v_cvt_pk_bf16_f32 v213, v85, v89\n"
      "v_cvt_pk_bf16_f32 v214, v86, v90\n"
      "v_cvt_pk_bf16_f32 v215, v87, v91\n"
      "v_cvt_pk_bf16_f32 v208, v76, v80\n"
      "v_cvt_pk_bf16_f32 v209, v77, v81\n"
      "v_mfma_f32_16x16x32_bf16 v[100:103], a[12:15], a[196:199], v[100:103]\n"
      "v_cvt_pk_bf16_f32 v210, v78, v82\n"
      "v_cvt_pk_bf16_f32 v211, v79, v83\n"
      "v_mfma_f32_16x16x32_bf16 v[104:107], a[16:19], a[196:199], v[104:107]\n"
      "v_cvt_pk_bf16_f32 v112, v112, v116\n"
      "v_cvt_pk_bf16_f32 v113, v113, v117\n"
      "v_mfma_f32_16x16x32_bf16 v[108:111], a[20:23], a[196:199], v[108:111]\n"
      "v_cvt_pk_bf16_f32 v114, v114, v118\n"
      "v_cvt_pk_bf16_f32 v115, v115, v119\n"
      "s_waitcnt lgkmcnt(0)\n"
      "s_waitcnt vmcnt(10)\n"
      "s_nop 0\n"
      "s_barrier\n"
      "s_add_u32 s53, s54, 592896\n"
      "s_add_u32 m0, s37, 107656\n"
      "v_mfma_f32_4x4x4_16b_bf16 v[76:79], a[244:245], a[28:29], 0\n"
      "v_mfma_f32_4x4x4_16b_bf16 v[80:83], a[244:245], a[30:31], 0\n"
      "buffer_load_dwordx4 v10, s[4:7], s53 offen offset:0 nt lds\n"
      "buffer_load_dwordx4 v11, s[4:7], s53 offen offset:1024 nt lds\n"
      "v_mfma_f32_4x4x4_16b_bf16 v[76:79], a[248:249], a[232:233], v[76:79]\n"
      "s_add_u32 s53, s54, 789504\n"
      "s_add_u32 m0, s37, 112000\n"
      "v_mfma_f32_4x4x4_16b_bf16 v[80:83], a[248:249], a[234:235], v[80:83]\n"
      "buffer_load_dwordx4 v10, s[4:7], s53 offen offset:0 nt lds\n"
      "buffer_load_dwordx4 v11, s[4:7], s53 offen offset:1024 nt lds\n"
      "v_mfma_f32_4x4x4_16b_bf16 v[84:87], a[246:247], a[228:229], v[76:79]\n"
      "ds_read_b64_tr_b16 a[36:37], v95 offset:41216\n"
      "v_mfma_f32_4x4x4_16b_bf16 v[88:91], a[246:247], a[230:231], v[80:83]\n"
      "ds_read_b64_tr_b16 a[38:39], v95 offset:41984\n"
      "v_mfma_f32_4x4x4_16b_bf16 v[76:79], a[250:251], a[228:229], v[76:79]\n"
      "ds_read_b64_tr_b16 a[224:225], v95 offset:53504\n"
      "v_mfma_f32_4x4x4_16b_bf16 v[80:83], a[250:251], a[230:231], v[80:83]\n"
      "ds_read_b64_tr_b16 a[226:227], v95 offset:54272\n"
      "v_mfma_f32_4x4x4_16b_bf16 v[44:47], a[216:217], a[176:177], 0\n"
      "ds_read_b64_tr_b16 a[220:221], v95 offset:47360\n"
      "v_mfma_f32_4x4x4_16b_bf16 v[48:51], a[216:217], a[178:179], 0\n"
      "ds_read_b64_tr_b16 a[222:223], v95 offset:48128\n"
      "s_mov_b32 m0, s64\n"
      "s_nop 0\n"
      "ds_write_addtid_b32 v112 offset:2560\n"
      "ds_write_addtid_b32 v113 offset:6656\n"
      "ds_write_addtid_b32 v114 offset:10752\n"
      "ds_write_addtid_b32 v115 offset:14848\n"
      "ds_read_b128 a[200:203], v3 offset:55296\n"
      "v_cvt_pk_bf16_f32 v204, v36, v40\n"
      "v_cvt_pk_bf16_f32 v205, v37, v41\n"
      "v_cvt_pk_bf16_f32 v206, v38, v42\n"
      "v_cvt_pk_bf16_f32 v207, v39, v43\n"
      "v_cvt_pk_bf16_f32 v200, v28, v32\n"
      "v_cvt_pk_bf16_f32 v201, v29, v33\n"
      "v_cvt_pk_bf16_f32 v202, v30, v34\n"
      "v_cvt_pk_bf16_f32 v203, v31, v35\n"
      "v_cvt_pk_bf16_f32 v20, v20, v24\n"
      "v_cvt_pk_bf16_f32 v21, v21, v25\n"
      "v_cvt_pk_bf16_f32 v22, v22, v26\n"
      "v_cvt_pk_bf16_f32 v23, v23, v27\n"
      "s_add_u32 s53, s36, 3072\n"
      "s_add_u32 m0, s37, 154368\n"
      "v_mfma_f32_4x4x4_16b_bf16 v[28:31], a[244:245], a[32:33], 0\n"
      "v_mfma_f32_4x4x4_16b_bf16 v[32:35], a[244:245], a[34:35], 0\n"
      "buffer_load_dwordx4 v10, s[4:7], s53 offen offset:0 nt lds\n"
      "buffer_load_dwordx4 v11, s[4:7], s53 offen offset:1024 nt lds\n"
      "v_mfma_f32_4x4x4_16b_bf16 v[28:31], a[248:249], a[240:241], v[28:31]\n"
      "ds_read_b64_tr_b16 a[40:41], v95 offset:41280\n"
      "v_mfma_f32_4x4x4_16b_bf16 v[32:35], a[248:249], a[242:243], v[32:35]\n"
      "ds_read_b64_tr_b16 a[42:43], v95 offset:42048\n"
      "v_mfma_f32_4x4x4_16b_bf16 v[36:39], a[246:247], a[236:237], v[28:31]\n"
      "ds_read_b64_tr_b16 a[232:233], v95 offset:53568\n"
      "v_mfma_f32_4x4x4_16b_bf16 v[40:43], a[246:247], a[238:239], v[32:35]\n"
      "ds_read_b64_tr_b16 a[234:235], v95 offset:54336\n"
      "v_mfma_f32_4x4x4_16b_bf16 v[28:31], a[250:251], a[236:237], v[28:31]\n"
      "ds_read_b64_tr_b16 a[228:229], v95 offset:47424\n"
      "v_mfma_f32_4x4x4_16b_bf16 v[32:35], a[250:251], a[238:239], v[32:35]\n"
      "ds_read_b64_tr_b16 a[230:231], v95 offset:48192\n"
      "s_mov_b32 m0, s64\n"
      "s_nop 0\n"
      "ds_write_addtid_b32 v20 offset:3072\n"
      "ds_write_addtid_b32 v21 offset:7168\n"
      "ds_write_addtid_b32 v22 offset:11264\n"
      "ds_write_addtid_b32 v23 offset:15360\n"
      "v_cvt_pk_bf16_f32 v196, v84, v88\n"
      "v_cvt_pk_bf16_f32 v197, v85, v89\n"
      "v_cvt_pk_bf16_f32 v198, v86, v90\n"
      "v_cvt_pk_bf16_f32 v199, v87, v91\n"
      "v_cvt_pk_bf16_f32 v192, v76, v80\n"
      "v_cvt_pk_bf16_f32 v193, v77, v81\n"
      "v_cvt_pk_bf16_f32 v194, v78, v82\n"
      "v_cvt_pk_bf16_f32 v195, v79, v83\n"
      "v_cvt_pk_bf16_f32 v44, v44, v48\n"
      "v_cvt_pk_bf16_f32 v45, v45, v49\n"
      "v_cvt_pk_bf16_f32 v46, v46, v50\n"
      "v_cvt_pk_bf16_f32 v47, v47, v51\n"
      "s_nop 0\n"
      "s_waitcnt lgkmcnt(4)\n"
      "v_mfma_f32_4x4x4_16b_bf16 v[76:79], a[244:245], a[36:37], 0\n"
      "v_mfma_f32_4x4x4_16b_bf16 v[80:83], a[244:245], a[38:39], 0\n"
      "ds_read_b64_tr_b16 a[44:45], v95 offset:41344\n"
      "v_mfma_f32_4x4x4_16b_bf16 v[76:79], a[248:249], a[224:225], v[76:79]\n"
      "ds_read_b64_tr_b16 a[46:47], v95 offset:42112\n"
      "v_mfma_f32_4x4x4_16b_bf16 v[80:83], a[248:249], a[226:227], v[80:83]\n"
      "ds_read_b64_tr_b16 a[240:241], v95 offset:53632\n"
      "v_mfma_f32_4x4x4_16b_bf16 v[84:87], a[246:247], a[220:221], v[76:79]\n"
      "ds_read_b64_tr_b16 a[242:243], v95 offset:54400\n"
      "v_mfma_f32_4x4x4_16b_bf16 v[88:91], a[246:247], a[222:223], v[80:83]\n"
      "ds_read_b64_tr_b16 a[236:237], v95 offset:47488\n"
      "v_mfma_f32_4x4x4_16b_bf16 v[76:79], a[250:251], a[220:221], v[76:79]\n"
      "ds_read_b64_tr_b16 a[238:239], v95 offset:48256\n"
      "v_mfma_f32_4x4x4_16b_bf16 v[80:83], a[250:251], a[222:223], v[80:83]\n"
      "ds_write_addtid_b32 v44 offset:3584\n"
      "ds_write_addtid_b32 v45 offset:7680\n"
      "ds_write_addtid_b32 v46 offset:11776\n"
      "ds_write_addtid_b32 v47 offset:15872\n"
      "v_cvt_pk_bf16_f32 v188, v36, v40\n"
      "v_cvt_pk_bf16_f32 v189, v37, v41\n"
      "v_mfma_f32_16x16x32_bf16 v[100:103], a[24:27], a[200:203], v[100:103]\n"
      "v_cvt_pk_bf16_f32 v190, v38, v42\n"
      "v_cvt_pk_bf16_f32 v191, v39, v43\n"
      "v_mfma_f32_16x16x32_bf16 v[104:107], a[28:31], a[200:203], v[104:107]\n"
      "v_cvt_pk_bf16_f32 v184, v28, v32\n"
      "v_cvt_pk_bf16_f32 v185, v29, v33\n"
      "v_mfma_f32_16x16x32_bf16 v[108:111], a[32:35], a[200:203], v[108:111]\n"
      "v_cvt_pk_bf16_f32 v186, v30, v34\n"
      "v_cvt_pk_bf16_f32 v187, v31, v35\n"
      "s_waitcnt lgkmcnt(0)\n"
      "s_waitcnt vmcnt(13)\n"
      "s_nop 0\n"
      "s_barrier\n"
      "s_add_u32 m0, s63, 18432\n"
      "s_add_u32 s52, s63, 147456\n"
      "v_mfma_f32_4x4x4_16b_bf16 v[28:31], a[244:245], a[40:41], 0\n"
      "v_mfma_f32_4x4x4_16b_bf16 v[32:35], a[244:245], a[42:43], 0\n"
      "buffer_load_dwordx4 v92, s[12:15], s52 offen offset:0 lds\n"
      "v_mfma_f32_4x4x4_16b_bf16 v[28:31], a[248:249], a[232:233], v[28:31]\n"
      "buffer_load_dwordx4 v93, s[12:15], s52 offen offset:1024 lds\n"
      "v_mfma_f32_4x4x4_16b_bf16 v[32:35], a[248:249], a[234:235], v[32:35]\n"
      "buffer_load_dwordx4 v94, s[12:15], s52 offen offset:2048 lds\n"
      "v_mfma_f32_4x4x4_16b_bf16 v[36:39], a[246:247], a[228:229], v[28:31]\n"
      "ds_read_b64_tr_b16 a[48:49], v96 offset:2304\n"
      "v_mfma_f32_4x4x4_16b_bf16 v[40:43], a[246:247], a[230:231], v[32:35]\n"
      "ds_read_b64_tr_b16 a[50:51], v96 offset:3072\n"
      "v_mfma_f32_4x4x4_16b_bf16 v[28:31], a[250:251], a[228:229], v[28:31]\n"
      "ds_read_b64_tr_b16 a[224:225], v96 offset:14592\n"
      "v_mfma_f32_4x4x4_16b_bf16 v[32:35], a[250:251], a[230:231], v[32:35]\n"
      "ds_read_b64_tr_b16 a[226:227], v96 offset:15360\n"
      "ds_read_b64_tr_b16 a[220:221], v96 offset:8448\n"
      "ds_read_b64_tr_b16 a[222:223], v96 offset:9216\n"
      "ds_read_b128 a[204:207], v4 offset:55168\n"
      "ds_read_b128 a[208:211], v0 offset:0\n"
      "ds_read_b128 a[212:215], v0 offset:4096\n"
      "v_cvt_pk_bf16_f32 v180, v84, v88\n"
      "v_cvt_pk_bf16_f32 v181, v85, v89\n"
      "v_cvt_pk_bf16_f32 v182, v86, v90\n"
      "v_cvt_pk_bf16_f32 v183, v87, v91\n"
      "v_cvt_pk_bf16_f32 v176, v76, v80\n"
      "v_cvt_pk_bf16_f32 v177, v77, v81\n"
      "v_cvt_pk_bf16_f32 v178, v78, v82\n"
      "v_cvt_pk_bf16_f32 v179, v79, v83\n"
      "v_mfma_f32_4x4x4_16b_bf16 v[76:79], a[244:245], a[44:45], 0\n"
      "v_mfma_f32_4x4x4_16b_bf16 v[80:83], a[244:245], a[46:47], 0\n"
      "ds_read_b64_tr_b16 a[52:53], v96 offset:2368\n"
      "v_mfma_f32_4x4x4_16b_bf16 v[76:79], a[248:249], a[240:241], v[76:79]\n"
      "ds_read_b64_tr_b16 a[54:55], v96 offset:3136\n"
      "v_mfma_f32_4x4x4_16b_bf16 v[80:83], a[248:249], a[242:243], v[80:83]\n"
      "ds_read_b64_tr_b16 a[232:233], v96 offset:14656\n"
      "v_mfma_f32_4x4x4_16b_bf16 v[84:87], a[246:247], a[236:237], v[76:79]\n"
      "ds_read_b64_tr_b16 a[234:235], v96 offset:15424\n"
      "v_mfma_f32_4x4x4_16b_bf16 v[88:91], a[246:247], a[238:239], v[80:83]\n"
      "ds_read_b64_tr_b16 a[228:229], v96 offset:8512\n"
      "v_mfma_f32_4x4x4_16b_bf16 v[76:79], a[250:251], a[236:237], v[76:79]\n"
      "ds_read_b64_tr_b16 a[230:231], v96 offset:9280\n"
      "v_mfma_f32_4x4x4_16b_bf16 v[80:83], a[250:251], a[238:239], v[80:83]\n"
      "ds_read_b128 a[192:195], v0 offset:64\n"
      "ds_read_b128 a[196:199], v0 offset:4160\n"
      "ds_read_b128 a[200:203], v0 offset:128\n"
      "v_cvt_pk_bf16_f32 v172, v36, v40\n"
      "v_cvt_pk_bf16_f32 v173, v37, v41\n"
      "v_cvt_pk_bf16_f32 v174, v38, v42\n"
      "v_cvt_pk_bf16_f32 v175, v39, v43\n"
      "v_cvt_pk_bf16_f32 v168, v28, v32\n"
      "v_cvt_pk_bf16_f32 v169, v29, v33\n"
      "v_cvt_pk_bf16_f32 v170, v30, v34\n"
      "v_cvt_pk_bf16_f32 v171, v31, v35\n"
      "s_nop 0\n"
      "s_waitcnt lgkmcnt(6)\n"
      "v_mfma_f32_4x4x4_16b_bf16 v[28:31], a[244:245], a[48:49], 0\n"
      "v_mfma_f32_4x4x4_16b_bf16 v[32:35], a[244:245], a[50:51], 0\n"
      "ds_read_b64_tr_b16 a[56:57], v96 offset:2432\n"
      "v_mfma_f32_4x4x4_16b_bf16 v[28:31], a[248:249], a[224:225], v[28:31]\n"
      "ds_read_b64_tr_b16 a[58:59], v96 offset:3200\n"
      "v_mfma_f32_4x4x4_16b_bf16 v[32:35], a[248:249], a[226:227], v[32:35]\n"
      "ds_read_b64_tr_b16 a[240:241], v96 offset:14720\n"
      "v_mfma_f32_4x4x4_16b_bf16 v[36:39], a[246:247], a[220:221], v[28:31]\n"
      "ds_read_b64_tr_b16 a[242:243], v96 offset:15488\n"
      "v_mfma_f32_4x4x4_16b_bf16 v[40:43], a[246:247], a[222:223], v[32:35]\n"
      "ds_read_b64_tr_b16 a[236:237], v96 offset:8576\n"
      "v_mfma_f32_4x4x4_16b_bf16 v[28:31], a[250:251], a[220:221], v[28:31]\n"
      "ds_read_b64_tr_b16 a[238:239], v96 offset:9344\n"
      "v_mfma_f32_4x4x4_16b_bf16 v[32:35], a[250:251], a[222:223], v[32:35]\n"
      "v_mfma_f32_16x16x32_bf16 v[100:103], a[36:39], a[204:207], v[100:103]\n"
      "v_cvt_pk_bf16_f32 v164, v84, v88\n"
      "v_cvt_pk_bf16_f32 v165, v85, v89\n"
      "v_mfma_f32_16x16x32_bf16 v[104:107], a[40:43], a[204:207], v[104:107]\n"
      "v_cvt_pk_bf16_f32 v166, v86, v90\n"
      "v_cvt_pk_bf16_f32 v167, v87, v91\n"
      "v_mfma_f32_16x16x32_bf16 v[108:111], a[44:47], a[204:207], v[108:111]\n"
      "v_cvt_pk_bf16_f32 v160, v76, v80\n"
      "v_cvt_pk_bf16_f32 v161, v77, v81\n"
      "v_mfma_f32_16x16x32_bf16 v[52:55], v[252:255], a[208:211], 0\n"
      "v_cvt_pk_bf16_f32 v162, v78, v82\n"
      "v_cvt_pk_bf16_f32 v163, v79, v83\n"
      "s_waitcnt vmcnt(13)\n"
      "s_barrier\n"
      "s_add_u32 m0, s63, 53504\n"
      "s_add_u32 s52, s63, 153600\n"
      "v_mfma_f32_4x4x4_16b_bf16 v[76:79], a[244:245], a[52:53], 0\n"
      "v_mfma_f32_4x4x4_16b_bf16 v[80:83], a[244:245], a[54:55], 0\n"
      "buffer_load_dwordx4 v92, s[12:15], s52 offen offset:0 lds\n"
      "s_nop 0\n"
      "s_waitcnt lgkmcnt(0)\n"
      "v_mfma_f32_4x4x4_16b_bf16 v[76:79], a[248:249], a[232:233], v[76:79]\n"
      "buffer_load_dwordx4 v93, s[12:15], s52 offen offset:1024 lds\n"
      "v_mfma_f32_4x4x4_16b_bf16 v[80:83], a[248:249], a[234:235], v[80:83]\n"
      "buffer_load_dwordx4 v94, s[12:15], s52 offen offset:2048 lds\n"
      "v_mfma_f32_4x4x4_16b_bf16 v[84:87], a[246:247], a[228:229], v[76:79]\n"
      "ds_read_b64_tr_b16 a[60:61], v97 offset:19968\n"
      "v_mfma_f32_4x4x4_16b_bf16 v[88:91], a[246:247], a[230:231], v[80:83]\n"
      "ds_read_b64_tr_b16 a[62:63], v97 offset:20736\n"
      "v_mfma_f32_4x4x4_16b_bf16 v[76:79], a[250:251], a[228:229], v[76:79]\n"
      "ds_read_b64_tr_b16 a[224:225], v95 offset:0\n"
      "v_mfma_f32_4x4x4_16b_bf16 v[80:83], a[250:251], a[230:231], v[80:83]\n"
      "ds_read_b64_tr_b16 a[226:227], v95 offset:768\n"
      "ds_read_b64_tr_b16 a[220:221], v97 offset:26112\n"
      "ds_read_b64_tr_b16 a[222:223], v97 offset:26880\n"
      "ds_read_b128 a[204:207], v0 offset:4224\n"
      "v_mfma_f32_16x16x32_bf16 v[56:59], v[244:247], a[208:211], 0\n"
      "v_cvt_pk_bf16_f32 v156, v36, v40\n"
      "v_cvt_pk_bf16_f32 v157, v37, v41\n"
      "v_mfma_f32_16x16x32_bf16 v[60:63], v[236:239], a[208:211], 0\n"
      "v_cvt_pk_bf16_f32 v158, v38, v42\n"
      "v_cvt_pk_bf16_f32 v159, v39, v43\n"
      "v_mfma_f32_16x16x32_bf16 v[64:67], v[248:251], a[212:215], 0\n"
      "v_cvt_pk_bf16_f32 v152, v28, v32\n"
      "v_cvt_pk_bf16_f32 v153, v29, v33\n"
      "v_mfma_f32_16x16x32_bf16 v[68:71], v[240:243], a[212:215], 0\n"
      "v_cvt_pk_bf16_f32 v154, v30, v34\n"
      "v_cvt_pk_bf16_f32 v155, v31, v35\n"
      "v_mfma_f32_4x4x4_16b_bf16 v[28:31], a[244:245], a[56:57], 0\n"
      "v_mfma_f32_4x4x4_16b_bf16 v[32:35], a[244:245], a[58:59], 0\n"
      "ds_read_b64_tr_b16 a[64:65], v97 offset:20032\n"
      "v_mfma_f32_4x4x4_16b_bf16 v[28:31], a[248:249], a[240:241], v[28:31]\n"
      "ds_read_b64_tr_b16 a[66:67], v97 offset:20800\n"
      "v_mfma_f32_4x4x4_16b_bf16 v[32:35], a[248:249], a[242:243], v[32:35]\n"
      "ds_read_b64_tr_b16 a[232:233], v95 offset:64\n"
      "v_mfma_f32_4x4x4_16b_bf16 v[36:39], a[246:247], a[236:237], v[28:31]\n"
      "ds_read_b64_tr_b16 a[234:235], v95 offset:832\n"
      "v_mfma_f32_4x4x4_16b_bf16 v[40:43], a[246:247], a[238:239], v[32:35]\n"
      "ds_read_b64_tr_b16 a[228:229], v97 offset:26176\n"
      "v_mfma_f32_4x4x4_16b_bf16 v[28:31], a[250:251], a[236:237], v[28:31]\n"
      "ds_read_b64_tr_b16 a[230:231], v97 offset:26944\n"
      "v_mfma_f32_4x4x4_16b_bf16 v[32:35], a[250:251], a[238:239], v[32:35]\n"
      "ds_read_b128 a[208:211], v1 offset:0\n"
      "v_mfma_f32_16x16x32_bf16 v[72:75], v[232:235], a[212:215], 0\n"
      "v_cvt_pk_bf16_f32 v148, v84, v88\n"
      "v_cvt_pk_bf16_f32 v149, v85, v89\n"
      "v_mfma_f32_16x16x32_bf16 v[52:55], v[228:231], a[192:195], v[52:55]\n"
      "v_cvt_pk_bf16_f32 v150, v86, v90\n"
      "v_cvt_pk_bf16_f32 v151, v87, v91\n"
      "v_mfma_f32_16x16x32_bf16 v[56:59], v[220:223], a[192:195], v[56:59]\n"
      "v_cvt_pk_bf16_f32 v144, v76, v80\n"
      "v_cvt_pk_bf16_f32 v145, v77, v81\n"
      "v_mfma_f32_16x16x32_bf16 v[60:63], v[212:215], a[192:195], v[60:63]\n"
      "v_cvt_pk_bf16_f32 v146, v78, v82\n"
      "v_cvt_pk_bf16_f32 v147, v79, v83\n"
      "s_nop 0\n"
      "s_waitcnt lgkmcnt(1)\n"
      "v_mfma_f32_4x4x4_16b_bf16 v[76:79], a[244:245], a[60:61], 0\n"
      "v_mfma_f32_4x4x4_16b_bf16 v[80:83], a[244:245], a[62:63], 0\n"
      "ds_read_b64_tr_b16 a[68:69], v97 offset:20096\n"
      "v_mfma_f32_4x4x4_16b_bf16 v[76:79], a[248:249], a[224:225], v[76:79]\n"
      "ds_read_b64_tr_b16 a[70:71], v97 offset:20864\n"
      "v_mfma_f32_4x4x4_16b_bf16 v[80:83], a[248:249], a[226:227], v[80:83]\n"
      "ds_read_b64_tr_b16 a[240:241], v95 offset:128\n"
      "v_mfma_f32_4x4x4_16b_bf16 v[84:87], a[246:247], a[220:221], v[76:79]\n"
      "ds_read_b64_tr_b16 a[242:243], v95 offset:896\n"
      "v_mfma_f32_4x4x4_16b_bf16 v[88:91], a[246:247], a[222:223], v[80:83]\n"
      "ds_read_b64_tr_b16 a[236:237], v97 offset:26240\n"
      "v_mfma_f32_4x4x4_16b_bf16 v[76:79], a[250:251], a[220:221], v[76:79]\n"
      "ds_read_b64_tr_b16 a[238:239], v97 offset:27008\n"
      "v_mfma_f32_4x4x4_16b_bf16 v[80:83], a[250:251], a[222:223], v[80:83]\n"
      "ds_read_b128 a[212:215], v1 offset:4096\n"
      "ds_read_b128 a[192:195], v4 offset:55232\n"
      "v_mfma_f32_16x16x32_bf16 v[64:67], v[224:227], a[196:199], v[64:67]\n"
      "v_cvt_pk_bf16_f32 v140, v36, v40\n"
      "v_cvt_pk_bf16_f32 v141, v37, v41\n"
      "v_mfma_f32_16x16x32_bf16 v[68:71], v[216:219], a[196:199], v[68:71]\n"
      "v_cvt_pk_bf16_f32 v142, v38, v42\n"
      "v_cvt_pk_bf16_f32 v143, v39, v43\n"
      "v_mfma_f32_16x16x32_bf16 v[72:75], v[208:211], a[196:199], v[72:75]\n"
      "v_cvt_pk_bf16_f32 v136, v28, v32\n"
      "v_cvt_pk_bf16_f32 v137, v29, v33\n"
      "v_mfma_f32_16x16x32_bf16 v[52:55], v[204:207], a[200:203], v[52:55]\n"
      "v_cvt_pk_bf16_f32 v138, v30, v34\n"
      "v_cvt_pk_bf16_f32 v139, v31, v35\n"
      "s_waitcnt vmcnt(3)\n"
      "s_barrier\n"
      "s_add_u32 m0, s63, 71936\n"
      "s_add_u32 s52, s63, 159744\n"
      "v_mfma_f32_4x4x4_16b_bf16 v[28:31], a[244:245], a[64:65], 0\n"
      "v_mfma_f32_4x4x4_16b_bf16 v[32:35], a[244:245], a[66:67], 0\n"
      "buffer_load_dwordx4 v92, s[12:15], s52 offen offset:0 lds\n"
      "v_mfma_f32_4x4x4_16b_bf16 v[28:31], a[248:249], a[232:233], v[28:31]\n"
      "buffer_load_dwordx4 v93, s[12:15], s52 offen offset:1024 lds\n"
      "v_mfma_f32_4x4x4_16b_bf16 v[32:35], a[248:249], a[234:235], v[32:35]\n"
      "buffer_load_dwordx4 v94, s[12:15], s52 offen offset:2048 lds\n"
      "v_mfma_f32_4x4x4_16b_bf16 v[36:39], a[246:247], a[228:229], v[28:31]\n"
      "ds_read_b64_tr_b16 a[72:73], v95 offset:6144\n"
      "v_mfma_f32_4x4x4_16b_bf16 v[40:43], a[246:247], a[230:231], v[32:35]\n"
      "ds_read_b64_tr_b16 a[74:75], v95 offset:6912\n"
      "v_mfma_f32_4x4x4_16b_bf16 v[28:31], a[250:251], a[228:229], v[28:31]\n"
      "ds_read_b64_tr_b16 a[224:225], v95 offset:18432\n"
      "v_mfma_f32_4x4x4_16b_bf16 v[32:35], a[250:251], a[230:231], v[32:35]\n"
      "ds_read_b64_tr_b16 a[226:227], v95 offset:19200\n"
      "ds_read_b64_tr_b16 a[220:221], v95 offset:12288\n"
      "ds_read_b64_tr_b16 a[222:223], v95 offset:13056\n"
      "ds_read_b128 a[196:199], v1 offset:64\n"
      "v_mfma_f32_16x16x32_bf16 v[56:59], v[196:199], a[200:203], v[56:59]\n"
      "v_cvt_pk_bf16_f32 v132, v84, v88\n"
      "v_cvt_pk_bf16_f32 v133, v85, v89\n"
      "v_mfma_f32_16x16x32_bf16 v[60:63], v[188:191], a[200:203], v[60:63]\n"
      "v_cvt_pk_bf16_f32 v134, v86, v90\n"
      "v_cvt_pk_bf16_f32 v135, v87, v91\n"
      "v_mfma_f32_16x16x32_bf16 v[64:67], v[200:203], a[204:207], v[64:67]\n"
      "v_cvt_pk_bf16_f32 v128, v76, v80\n"
      "v_cvt_pk_bf16_f32 v129, v77, v81\n"
      "v_mfma_f32_16x16x32_bf16 v[68:71], v[192:195], a[204:207], v[68:71]\n"
      "v_cvt_pk_bf16_f32 v130, v78, v82\n"
      "v_cvt_pk_bf16_f32 v131, v79, v83\n"
      "s_nop 0\n"
      "s_waitcnt lgkmcnt(2)\n"
      "v_mfma_f32_4x4x4_16b_bf16 v[76:79], a[244:245], a[68:69], 0\n"
      "v_mfma_f32_4x4x4_16b_bf16 v[80:83], a[244:245], a[70:71], 0\n"
      "ds_read_b64_tr_b16 a[76:77], v95 offset:6208\n"
      "v_mfma_f32_4x4x4_16b_bf16 v[76:79], a[248:249], a[240:241], v[76:79]\n"
      "ds_read_b64_tr_b16 a[78:79], v95 offset:6976\n"
      "v_mfma_f32_4x4x4_16b_bf16 v[80:83], a[248:249], a[242:243], v[80:83]\n"
      "ds_read_b64_tr_b16 a[232:233], v95 offset:18496\n"
      "v_mfma_f32_4x4x4_16b_bf16 v[84:87], a[246:247], a[236:237], v[76:79]\n"
      "ds_read_b64_tr_b16 a[234:235], v95 offset:19264\n"
      "v_mfma_f32_4x4x4_16b_bf16 v[88:91], a[246:247], a[238:239], v[80:83]\n"
      "ds_read_b64_tr_b16 a[228:229], v95 offset:12352\n"
      "v_mfma_f32_4x4x4_16b_bf16 v[76:79], a[250:251], a[236:237], v[76:79]\n"
      "ds_read_b64_tr_b16 a[230:231], v95 offset:13120\n"
      "v_mfma_f32_4x4x4_16b_bf16 v[80:83], a[250:251], a[238:239], v[80:83]\n"
      "ds_read_b128 a[200:203], v1 offset:4160\n"
      "v_mfma_f32_16x16x32_bf16 v[72:75], v[184:187], a[204:207], v[72:75]\n"
      "v_cvt_pk_bf16_f32 v124, v36, v40\n"
      "v_cvt_pk_bf16_f32 v125, v37, v41\n"
      "v_mfma_f32_16x16x32_bf16 v[52:55], v[180:183], a[208:211], v[52:55]\n"
      "v_cvt_pk_bf16_f32 v126, v38, v42\n"
      "v_cvt_pk_bf16_f32 v127, v39, v43\n"
      "v_mfma_f32_16x16x32_bf16 v[56:59], v[172:175], a[208:211], v[56:59]\n"
      "v_cvt_pk_bf16_f32 v28, v28, v32\n"
      "v_cvt_pk_bf16_f32 v29, v29, v33\n"
      "v_mfma_f32_16x16x32_bf16 v[60:63], v[164:167], a[208:211], v[60:63]\n"
      "v_cvt_pk_bf16_f32 v30, v30, v34\n"
      "v_cvt_pk_bf16_f32 v31, v31, v35\n"
      "v_mfma_f32_16x16x32_bf16 v[64:67], v[176:179], a[212:215], v[64:67]\n"
      "v_accvgpr_write_b32 a188, v28\n"
      "v_accvgpr_write_b32 a189, v29\n"
      "v_mfma_f32_16x16x32_bf16 v[68:71], v[168:171], a[212:215], v[68:71]\n"
      "v_accvgpr_write_b32 a190, v30\n"
      "v_accvgpr_write_b32 a191, v31\n"
      "s_add_u32 s53, s54, 983040\n"
      "s_add_u32 m0, s37, 116224\n"
      "v_mfma_f32_4x4x4_16b_bf16 v[28:31], a[244:245], a[72:73], 0\n"
      "v_mfma_f32_4x4x4_16b_bf16 v[32:35], a[244:245], a[74:75], 0\n"
      "buffer_load_dwordx4 v8, s[4:7], s53 offen offset:0 nt lds\n"
      "buffer_load_dwordx4 v9, s[4:7], s53 offen offset:1024 nt lds\n"
      "v_mfma_f32_4x4x4_16b_bf16 v[28:31], a[248:249], a[224:225], v[28:31]\n"
      "ds_read_b64_tr_b16 a[80:81], v95 offset:6272\n"
      "v_mfma_f32_4x4x4_16b_bf16 v[32:35], a[248:249], a[226:227], v[32:35]\n"
      "ds_read_b64_tr_b16 a[82:83], v95 offset:7040\n"
      "s_nop 0\n"
      "s_waitcnt lgkmcnt(2)\n"
      "v_mfma_f32_4x4x4_16b_bf16 v[36:39], a[246:247], a[220:221], v[28:31]\n"
      "ds_read_b64_tr_b16 a[240:241], v95 offset:18560\n"
      "v_mfma_f32_4x4x4_16b_bf16 v[40:43], a[246:247], a[222:223], v[32:35]\n"
      "ds_read_b64_tr_b16 a[242:243], v95 offset:19328\n"
      "v_mfma_f32_4x4x4_16b_bf16 v[28:31], a[250:251], a[220:221], v[28:31]\n"
      "ds_read_b64_tr_b16 a[236:237], v95 offset:12416\n"
      "v_mfma_f32_4x4x4_16b_bf16 v[32:35], a[250:251], a[222:223], v[32:35]\n"
      "ds_read_b64_tr_b16 a[238:239], v95 offset:13184\n"
      "ds_read_b128 a[204:207], v4 offset:55296\n"
      "ds_read_b128 a[208:211], v1 offset:128\n"
      "v_cvt_pk_bf16_f32 v84, v84, v88\n"
      "v_cvt_pk_bf16_f32 v85, v85, v89\n"
      "v_cvt_pk_bf16_f32 v86, v86, v90\n"
      "v_cvt_pk_bf16_f32 v87, v87, v91\n"
      "v_mfma_f32_16x16x32_bf16 v[72:75], v[160:163], a[212:215], v[72:75]\n"
      "v_accvgpr_write_b32 a184, v84\n"
      "v_accvgpr_write_b32 a185, v85\n"
      "v_mfma_f32_16x16x32_bf16 v[100:103], a[48:51], a[192:195], v[100:103]\n"
      "v_accvgpr_write_b32 a186, v86\n"
      "v_accvgpr_write_b32 a187, v87\n"
      "v_mfma_f32_16x16x32_bf16 v[104:107], a[52:55], a[192:195], v[104:107]\n"
      "v_cvt_pk_bf16_f32 v76, v76, v80\n"
      "v_cvt_pk_bf16_f32 v77, v77, v81\n"
      "v_mfma_f32_16x16x32_bf16 v[108:111], a[56:59], a[192:195], v[108:111]\n"
      "v_cvt_pk_bf16_f32 v78, v78, v82\n"
      "v_cvt_pk_bf16_f32 v79, v79, v83\n"
      "v_mfma_f32_16x16x32_bf16 v[52:55], v[156:159], a[196:199], v[52:55]\n"
      "v_accvgpr_write_b32 a180, v76\n"
      "v_accvgpr_write_b32 a181, v77\n"
      "v_mfma_f32_16x16x32_bf16 v[56:59], v[148:151], a[196:199], v[56:59]\n"
      "v_accvgpr_write_b32 a182, v78\n"
      "v_accvgpr_write_b32 a183, v79\n"
      "s_waitcnt vmcnt(5)\n"
      "s_barrier\n"
      "s_add_u32 s53, s54, 1179648\n"
      "s_add_u32 m0, s37, 120328\n"
      "v_mfma_f32_4x4x4_16b_bf16 v[76:79], a[244:245], a[76:77], 0\n"
      "v_mfma_f32_4x4x4_16b_bf16 v[80:83], a[244:245], a[78:79], 0\n"
      "buffer_load_dwordx4 v8, s[4:7], s53 offen offset:0 nt lds\n"
      "buffer_load_dwordx4 v9, s[4:7], s53 offen offset:1024 nt lds\n"
      "v_mfma_f32_4x4x4_16b_bf16 v[76:79], a[248:249], a[232:233], v[76:79]\n"
      "ds_read_b64_tr_b16 a[84:85], v95 offset:41216\n"
      "v_mfma_f32_4x4x4_16b_bf16 v[80:83], a[248:249], a[234:235], v[80:83]\n"
      "ds_read_b64_tr_b16 a[86:87], v95 offset:41984\n"
      "v_mfma_f32_4x4x4_16b_bf16 v[84:87], a[246:247], a[228:229], v[76:79]\n"
      "ds_read_b64_tr_b16 a[224:225], v95 offset:53504\n"
      "v_mfma_f32_4x4x4_16b_bf16 v[88:91], a[246:247], a[230:231], v[80:83]\n"
      "ds_read_b64_tr_b16 a[226:227], v95 offset:54272\n"
      "v_mfma_f32_4x4x4_16b_bf16 v[76:79], a[250:251], a[228:229], v[76:79]\n"
      "ds_read_b64_tr_b16 a[220:221], v95 offset:47360\n"
      "v_mfma_f32_4x4x4_16b_bf16 v[80:83], a[250:251], a[230:231], v[80:83]\n"
      "ds_read_b64_tr_b16 a[222:223], v95 offset:48128\n"
      "ds_read_b128 a[212:215], v1 offset:4224\n"
      "ds_read_b128 a[192:195], v5 offset:55168\n"
      "v_cvt_pk_bf16_f32 v36, v36, v40\n"
      "v_cvt_pk_bf16_f32 v37, v37, v41\n"
      "v_cvt_pk_bf16_f32 v38, v38, v42\n"
      "v_cvt_pk_bf16_f32 v39, v39, v43\n"
      "v_mfma_f32_16x16x32_bf16 v[60:63], v[140:143], a[196:199], v[60:63]\n"
      "v_accvgpr_write_b32 a176, v36\n"
      "v_accvgpr_write_b32 a177, v37\n"
      "v_mfma_f32_16x16x32_bf16 v[64:67], v[152:155], a[200:203], v[64:67]\n"
      "v_accvgpr_write_b32 a178, v38\n"
      "v_accvgpr_write_b32 a179, v39\n"
      "v_mfma_f32_16x16x32_bf16 v[68:71], v[144:147], a[200:203], v[68:71]\n"
      "v_cvt_pk_bf16_f32 v28, v28, v32\n"
      "v_cvt_pk_bf16_f32 v29, v29, v33\n"
      "v_mfma_f32_16x16x32_bf16 v[72:75], v[136:139], a[200:203], v[72:75]\n"
      "v_cvt_pk_bf16_f32 v30, v30, v34\n"
      "v_cvt_pk_bf16_f32 v31, v31, v35\n"
      "s_nop 0\n"
      "s_waitcnt lgkmcnt(1)\n"
      "v_mfma_f32_16x16x32_bf16 v[100:103], a[60:63], a[204:207], v[100:103]\n"
      "v_accvgpr_write_b32 a172, v28\n"
      "v_accvgpr_write_b32 a173, v29\n"
      "v_mfma_f32_16x16x32_bf16 v[104:107], a[64:67], a[204:207], v[104:107]\n"
      "v_accvgpr_write_b32 a174, v30\n"
      "v_accvgpr_write_b32 a175, v31\n"
      "s_add_u32 s53, s54, 1376256\n"
      "s_add_u32 m0, s37, 124552\n"
      "v_mfma_f32_4x4x4_16b_bf16 v[28:31], a[244:245], a[80:81], 0\n"
      "v_mfma_f32_4x4x4_16b_bf16 v[32:35], a[244:245], a[82:83], 0\n"
      "buffer_load_dwordx4 v8, s[4:7], s53 offen offset:0 nt lds\n"
      "buffer_load_dwordx4 v9, s[4:7], s53 offen offset:1024 nt lds\n"
      "v_mfma_f32_4x4x4_16b_bf16 v[28:31], a[248:249], a[240:241], v[28:31]\n"
      "ds_read_b64_tr_b16 a[88:89], v95 offset:41280\n"
      "v_mfma_f32_4x4x4_16b_bf16 v[32:35], a[248:249], a[242:243], v[32:35]\n"
      "ds_read_b64_tr_b16 a[90:91], v95 offset:42048\n"
      "v_mfma_f32_4x4x4_16b_bf16 v[36:39], a[246:247], a[236:237], v[28:31]\n"
      "ds_read_b64_tr_b16 a[232:233], v95 offset:53568\n"
      "v_mfma_f32_4x4x4_16b_bf16 v[40:43], a[246:247], a[238:239], v[32:35]\n"
      "ds_read_b64_tr_b16 a[234:235], v95 offset:54336\n"
      "v_mfma_f32_4x4x4_16b_bf16 v[28:31], a[250:251], a[236:237], v[28:31]\n"
      "ds_read_b64_tr_b16 a[228:229], v95 offset:47424\n"
      "v_mfma_f32_4x4x4_16b_bf16 v[32:35], a[250:251], a[238:239], v[32:35]\n"
      "ds_read_b64_tr_b16 a[230:231], v95 offset:48192\n"
      "ds_read_b128 a[196:199], v2 offset:0\n"
      "ds_read_b128 a[200:203], v2 offset:4096\n"
      "v_cvt_pk_bf16_f32 v84, v84, v88\n"
      "v_cvt_pk_bf16_f32 v85, v85, v89\n"
      "v_cvt_pk_bf16_f32 v86, v86, v90\n"
      "v_cvt_pk_bf16_f32 v87, v87, v91\n"
      "v_mfma_f32_16x16x32_bf16 v[108:111], a[68:71], a[204:207], v[108:111]\n"
      "v_accvgpr_write_b32 a168, v84\n"
      "v_accvgpr_write_b32 a169, v85\n"
      "v_mfma_f32_16x16x32_bf16 v[52:55], v[132:135], a[208:211], v[52:55]\n"
      "v_accvgpr_write_b32 a170, v86\n"
      "v_accvgpr_write_b32 a171, v87\n"
      "v_mfma_f32_16x16x32_bf16 v[56:59], v[124:127], a[208:211], v[56:59]\n"
      "v_cvt_pk_bf16_f32 v76, v76, v80\n"
      "v_cvt_pk_bf16_f32 v77, v77, v81\n"
      "v_mfma_f32_16x16x32_bf16 v[60:63], a[184:187], a[208:211], v[60:63]\n"
      "v_cvt_pk_bf16_f32 v78, v78, v82\n"
      "v_cvt_pk_bf16_f32 v79, v79, v83\n"
      "v_mfma_f32_16x16x32_bf16 v[64:67], v[128:131], a[212:215], v[64:67]\n"
      "v_accvgpr_write_b32 a164, v76\n"
      "v_accvgpr_write_b32 a165, v77\n"
      "v_mfma_f32_16x16x32_bf16 v[68:71], a[188:191], a[212:215], v[68:71]\n"
      "v_accvgpr_write_b32 a166, v78\n"
      "v_accvgpr_write_b32 a167, v79\n"
      "s_add_u32 s53, s54, 1572864\n"
      "s_add_u32 m0, s37, 128896\n"
      "v_mfma_f32_4x4x4_16b_bf16 v[76:79], a[244:245], a[84:85], 0\n"
      "v_mfma_f32_4x4x4_16b_bf16 v[80:83], a[244:245], a[86:87], 0\n"
      "buffer_load_dwordx4 v8, s[4:7], s53 offen offset:0 nt lds\n"
      "buffer_load_dwordx4 v9, s[4:7], s53 offen offset:1024 nt lds\n"
      "v_mfma_f32_4x4x4_16b_bf16 v[76:79], a[248:249], a[224:225], v[76:79]\n"
      "ds_read_b64_tr_b16 a[92:93], v95 offset:41344\n"
      "v_mfma_f32_4x4x4_16b_bf16 v[80:83], a[248:249], a[226:227], v[80:83]\n"
      "ds_read_b64_tr_b16 a[94:95], v95 offset:42112\n"
      "v_mfma_f32_4x4x4_16b_bf16 v[84:87], a[246:247], a[220:221], v[76:79]\n"
      "ds_read_b64_tr_b16 a[240:241], v95 offset:53632\n"
      "v_mfma_f32_4x4x4_16b_bf16 v[88:91], a[246:247], a[222:223], v[80:83]\n"
      "ds_read_b64_tr_b16 a[242:243], v95 offset:54400\n"
      "v_mfma_f32_4x4x4_16b_bf16 v[76:79], a[250:251], a[220:221], v[76:79]\n"
      "ds_read_b64_tr_b16 a[236:237], v95 offset:47488\n"
      "v_mfma_f32_4x4x4_16b_bf16 v[80:83], a[250:251], a[222:223], v[80:83]\n"
      "ds_read_b64_tr_b16 a[238:239], v95 offset:48256\n"
      "v_cvt_pk_bf16_f32 v36, v36, v40\n"
      "v_cvt_pk_bf16_f32 v37, v37, v41\n"
      "v_cvt_pk_bf16_f32 v38, v38, v42\n"
      "v_cvt_pk_bf16_f32 v39, v39, v43\n"
      "v_accvgpr_write_b32 a160, v36\n"
      "v_accvgpr_write_b32 a161, v37\n"
      "v_accvgpr_write_b32 a162, v38\n"
      "v_accvgpr_write_b32 a163, v39\n"
      "v_mfma_f32_16x16x32_bf16 v[72:75], a[180:183], a[212:215], v[72:75]\n"
      "v_cvt_pk_bf16_f32 v28, v28, v32\n"
      "v_cvt_pk_bf16_f32 v29, v29, v33\n"
      "s_nop 0\n"
      "s_waitcnt lgkmcnt(3)\n"
      "v_mfma_f32_16x16x32_bf16 v[100:103], a[72:75], a[192:195], v[100:103]\n"
      "v_cvt_pk_bf16_f32 v30, v30, v34\n"
      "v_cvt_pk_bf16_f32 v31, v31, v35\n"
      "v_mfma_f32_16x16x32_bf16 v[104:107], a[76:79], a[192:195], v[104:107]\n"
      "v_accvgpr_write_b32 a156, v28\n"
      "v_accvgpr_write_b32 a157, v29\n"
      "v_mfma_f32_16x16x32_bf16 v[108:111], a[80:83], a[192:195], v[108:111]\n"
      "v_accvgpr_write_b32 a158, v30\n"
      "v_accvgpr_write_b32 a159, v31\n"
      "s_waitcnt vmcnt(8)\n"
      "s_barrier\n"
      "s_add_u32 s53, s54, 986112\n"
      "s_add_u32 m0, s37, 133120\n"
      "v_mfma_f32_4x4x4_16b_bf16 v[28:31], a[244:245], a[88:89], 0\n"
      "v_mfma_f32_4x4x4_16b_bf16 v[32:35], a[244:245], a[90:91], 0\n"
      "buffer_load_dwordx4 v10, s[4:7], s53 offen offset:0 nt lds\n"
      "buffer_load_dwordx4 v11, s[4:7], s53 offen offset:1024 nt lds\n"
      "v_mfma_f32_4x4x4_16b_bf16 v[28:31], a[248:249], a[232:233], v[28:31]\n"
      "ds_read_b64_tr_b16 a[96:97], v96 offset:2304\n"
      "v_mfma_f32_4x4x4_16b_bf16 v[32:35], a[248:249], a[234:235], v[32:35]\n"
      "ds_read_b64_tr_b16 a[98:99], v96 offset:3072\n"
      "v_mfma_f32_4x4x4_16b_bf16 v[36:39], a[246:247], a[228:229], v[28:31]\n"
      "ds_read_b64_tr_b16 a[224:225], v96 offset:14592\n"
      "v_mfma_f32_4x4x4_16b_bf16 v[40:43], a[246:247], a[230:231], v[32:35]\n"
      "ds_read_b64_tr_b16 a[226:227], v96 offset:15360\n"
      "v_mfma_f32_4x4x4_16b_bf16 v[28:31], a[250:251], a[228:229], v[28:31]\n"
      "ds_read_b64_tr_b16 a[220:221], v96 offset:8448\n"
      "v_mfma_f32_4x4x4_16b_bf16 v[32:35], a[250:251], a[230:231], v[32:35]\n"
      "ds_read_b64_tr_b16 a[222:223], v96 offset:9216\n"
      "ds_read_b128 a[204:207], v5 offset:55232\n"
      "v_cvt_pk_bf16_f32 v84, v84, v88\n"
      "v_cvt_pk_bf16_f32 v85, v85, v89\n"
      "v_cvt_pk_bf16_f32 v86, v86, v90\n"
      "v_cvt_pk_bf16_f32 v87, v87, v91\n"
      "v_mfma_f32_16x16x32_bf16 v[52:55], a[176:179], a[196:199], v[52:55]\n"
      "v_accvgpr_write_b32 a152, v84\n"
      "v_accvgpr_write_b32 a153, v85\n"
      "v_mfma_f32_16x16x32_bf16 v[56:59], a[168:171], a[196:199], v[56:59]\n"
      "v_accvgpr_write_b32 a154, v86\n"
      "v_accvgpr_write_b32 a155, v87\n"
      "v_mfma_f32_16x16x32_bf16 v[60:63], a[160:163], a[196:199], v[60:63]\n"
      "v_cvt_pk_bf16_f32 v76, v76, v80\n"
      "v_cvt_pk_bf16_f32 v77, v77, v81\n"
      "v_mfma_f32_16x16x32_bf16 v[64:67], a[172:175], a[200:203], v[64:67]\n"
      "v_cvt_pk_bf16_f32 v78, v78, v82\n"
      "v_cvt_pk_bf16_f32 v79, v79, v83\n"
      "v_mfma_f32_16x16x32_bf16 v[68:71], a[164:167], a[200:203], v[68:71]\n"
      "v_accvgpr_write_b32 a148, v76\n"
      "v_accvgpr_write_b32 a149, v77\n"
      "v_mfma_f32_16x16x32_bf16 v[72:75], a[156:159], a[200:203], v[72:75]\n"
      "v_accvgpr_write_b32 a150, v78\n"
      "v_accvgpr_write_b32 a151, v79\n"
      "s_add_u32 s53, s54, 1182720\n"
      "s_add_u32 m0, s37, 137224\n"
      "v_mfma_f32_4x4x4_16b_bf16 v[76:79], a[244:245], a[92:93], 0\n"
      "v_mfma_f32_4x4x4_16b_bf16 v[80:83], a[244:245], a[94:95], 0\n"
      "buffer_load_dwordx4 v10, s[4:7], s53 offen offset:0 nt lds\n"
      "buffer_load_dwordx4 v11, s[4:7], s53 offen offset:1024 nt lds\n"
      "s_nop 0\n"
      "s_waitcnt lgkmcnt(0)\n"
      "v_mfma_f32_4x4x4_16b_bf16 v[76:79], a[248:249], a[240:241], v[76:79]\n"
      "ds_read_b64_tr_b16 a[100:101], v96 offset:2368\n"
      "v_mfma_f32_4x4x4_16b_bf16 v[80:83], a[248:249], a[242:243], v[80:83]\n"
      "ds_read_b64_tr_b16 a[102:103], v96 offset:3136\n"
      "v_mfma_f32_4x4x4_16b_bf16 v[84:87], a[246:247], a[236:237], v[76:79]\n"
      "ds_read_b64_tr_b16 a[232:233], v96 offset:14656\n"
      "v_mfma_f32_4x4x4_16b_bf16 v[88:91], a[246:247], a[238:239], v[80:83]\n"
      "ds_read_b64_tr_b16 a[234:235], v96 offset:15424\n"
      "v_mfma_f32_4x4x4_16b_bf16 v[76:79], a[250:251], a[236:237], v[76:79]\n"
      "ds_read_b64_tr_b16 a[228:229], v96 offset:8512\n"
      "v_mfma_f32_4x4x4_16b_bf16 v[80:83], a[250:251], a[238:239], v[80:83]\n"
      "ds_read_b64_tr_b16 a[230:231], v96 offset:9280\n"
      "ds_read_b128 a[208:211], v2 offset:64\n"
      "ds_read_b128 a[212:215], v2 offset:4160\n"
      "v_cvt_pk_bf16_f32 v36, v36, v40\n"
      "v_cvt_pk_bf16_f32 v37, v37, v41\n"
      "v_cvt_pk_bf16_f32 v38, v38, v42\n"
      "v_cvt_pk_bf16_f32 v39, v39, v43\n"
      "v_accvgpr_write_b32 a144, v36\n"
      "v_accvgpr_write_b32 a145, v37\n"
      "v_accvgpr_write_b32 a146, v38\n"
      "v_accvgpr_write_b32 a147, v39\n"
      "v_cvt_pk_bf16_f32 v28, v28, v32\n"
      "v_cvt_pk_bf16_f32 v29, v29, v33\n"
      "v_cvt_pk_bf16_f32 v30, v30, v34\n"
      "v_cvt_pk_bf16_f32 v31, v31, v35\n"
      "v_accvgpr_write_b32 a140, v28\n"
      "v_accvgpr_write_b32 a141, v29\n"
      "v_accvgpr_write_b32 a142, v30\n"
      "v_accvgpr_write_b32 a143, v31\n"
      "s_add_u32 s53, s54, 1379328\n"
      "s_add_u32 m0, s37, 141448\n"
      "v_mfma_f32_4x4x4_16b_bf16 v[28:31], a[244:245], a[96:97], 0\n"
      "v_mfma_f32_4x4x4_16b_bf16 v[32:35], a[244:245], a[98:99], 0\n"
      "buffer_load_dwordx4 v10, s[4:7], s53 offen offset:0 nt lds\n"
      "buffer_load_dwordx4 v11, s[4:7], s53 offen offset:1024 nt lds\n"
      "v_mfma_f32_4x4x4_16b_bf16 v[28:31], a[248:249], a[224:225], v[28:31]\n"
      "ds_read_b64_tr_b16 a[104:105], v96 offset:2432\n"
      "v_mfma_f32_4x4x4_16b_bf16 v[32:35], a[248:249], a[226:227], v[32:35]\n"
      "ds_read_b64_tr_b16 a[106:107], v96 offset:3200\n"
      "v_mfma_f32_4x4x4_16b_bf16 v[36:39], a[246:247], a[220:221], v[28:31]\n"
      "ds_read_b64_tr_b16 a[240:241], v96 offset:14720\n"
      "v_mfma_f32_4x4x4_16b_bf16 v[40:43], a[246:247], a[222:223], v[32:35]\n"
      "ds_read_b64_tr_b16 a[242:243], v96 offset:15488\n"
      "v_mfma_f32_4x4x4_16b_bf16 v[28:31], a[250:251], a[220:221], v[28:31]\n"
      "ds_read_b64_tr_b16 a[236:237], v96 offset:8576\n"
      "v_mfma_f32_4x4x4_16b_bf16 v[32:35], a[250:251], a[222:223], v[32:35]\n"
      "ds_read_b64_tr_b16 a[238:239], v96 offset:9344\n"
      "v_cvt_pk_bf16_f32 v84, v84, v88\n"
      "v_cvt_pk_bf16_f32 v85, v85, v89\n"
      "v_cvt_pk_bf16_f32 v86, v86, v90\n"
      "v_cvt_pk_bf16_f32 v87, v87, v91\n"
      "v_accvgpr_write_b32 a136, v84\n"
      "v_accvgpr_write_b32 a137, v85\n"
      "v_accvgpr_write_b32 a138, v86\n"
      "v_accvgpr_write_b32 a139, v87\n"
      "v_cvt_pk_bf16_f32 v76, v76, v80\n"
      "v_cvt_pk_bf16_f32 v77, v77, v81\n"
      "v_mfma_f32_16x16x32_bf16 v[100:103], a[84:87], a[204:207], v[100:103]\n"
      "v_cvt_pk_bf16_f32 v78, v78, v82\n"
      "v_cvt_pk_bf16_f32 v79, v79, v83\n"
      "v_mfma_f32_16x16x32_bf16 v[104:107], a[88:91], a[204:207], v[104:107]\n"
      "v_accvgpr_write_b32 a132, v76\n"
      "v_accvgpr_write_b32 a133, v77\n"
      "v_mfma_f32_16x16x32_bf16 v[108:111], a[92:95], a[204:207], v[108:111]\n"
      "v_accvgpr_write_b32 a134, v78\n"
      "v_accvgpr_write_b32 a135, v79\n"
      "s_add_u32 s53, s54, 1575936\n"
      "s_add_u32 m0, s37, 145792\n"
      "s_waitcnt lgkmcnt(0)\n"
      "v_mfma_f32_4x4x4_16b_bf16 v[76:79], a[244:245], a[100:101], 0\n"
      "v_mfma_f32_4x4x4_16b_bf16 v[80:83], a[244:245], a[102:103], 0\n"
      "buffer_load_dwordx4 v10, s[4:7], s53 offen offset:0 nt lds\n"
      "buffer_load_dwordx4 v11, s[4:7], s53 offen offset:1024 nt lds\n"
      "v_mfma_f32_4x4x4_16b_bf16 v[76:79], a[248:249], a[232:233], v[76:79]\n"
      "ds_read_b128 a[192:195], v5 offset:55296\n"
      "v_mfma_f32_4x4x4_16b_bf16 v[80:83], a[248:249], a[234:235], v[80:83]\n"
      "v_mfma_f32_4x4x4_16b_bf16 v[84:87], a[246:247], a[228:229], v[76:79]\n"
      "s_nop 0\n"
      "v_mfma_f32_4x4x4_16b_bf16 v[88:91], a[246:247], a[230:231], v[80:83]\n"
      "v_mfma_f32_4x4x4_16b_bf16 v[76:79], a[250:251], a[228:229], v[76:79]\n"
      "v_mfma_f32_4x4x4_16b_bf16 v[80:83], a[250:251], a[230:231], v[80:83]\n"
      "v_cvt_pk_bf16_f32 v36, v36, v40\n"
      "v_cvt_pk_bf16_f32 v37, v37, v41\n"
      "v_cvt_pk_bf16_f32 v38, v38, v42\n"
      "v_cvt_pk_bf16_f32 v39, v39, v43\n"
      "v_mfma_f32_16x16x32_bf16 v[52:55], a[152:155], a[208:211], v[52:55]\n"
      "v_accvgpr_write_b32 a128, v36\n"
      "v_accvgpr_write_b32 a129, v37\n"
      "v_mfma_f32_16x16x32_bf16 v[56:59], a[144:147], a[208:211], v[56:59]\n"
      "v_accvgpr_write_b32 a130, v38\n"
      "v_accvgpr_write_b32 a131, v39\n"
      "v_mfma_f32_16x16x32_bf16 v[60:63], a[136:139], a[208:211], v[60:63]\n"
      "v_cvt_pk_bf16_f32 v28, v28, v32\n"
      "v_cvt_pk_bf16_f32 v29, v29, v33\n"
      "v_mfma_f32_16x16x32_bf16 v[64:67], a[148:151], a[212:215], v[64:67]\n"
      "v_cvt_pk_bf16_f32 v30, v30, v34\n"
      "v_cvt_pk_bf16_f32 v31, v31, v35\n"
      "v_mfma_f32_16x16x32_bf16 v[68:71], a[140:143], a[212:215], v[68:71]\n"
      "v_accvgpr_write_b32 a124, v28\n"
      "v_accvgpr_write_b32 a125, v29\n"
      "v_mfma_f32_16x16x32_bf16 v[72:75], a[132:135], a[212:215], v[72:75]\n"
      "v_accvgpr_write_b32 a126, v30\n"
      "v_accvgpr_write_b32 a127, v31\n"
      "v_mfma_f32_4x4x4_16b_bf16 v[28:31], a[244:245], a[104:105], 0\n"
      "v_mfma_f32_4x4x4_16b_bf16 v[32:35], a[244:245], a[106:107], 0\n"
      "ds_read_b128 a[196:199], v2 offset:128\n"
      "v_mfma_f32_4x4x4_16b_bf16 v[28:31], a[248:249], a[240:241], v[28:31]\n"
      "ds_read_b128 a[200:203], v2 offset:4224\n"
      "v_mfma_f32_4x4x4_16b_bf16 v[32:35], a[248:249], a[242:243], v[32:35]\n"
      "v_mfma_f32_4x4x4_16b_bf16 v[36:39], a[246:247], a[236:237], v[28:31]\n"
      "s_nop 0\n"
      "s_nop 0\n"
      "v_mfma_f32_4x4x4_16b_bf16 v[40:43], a[246:247], a[238:239], v[32:35]\n"
      "v_mfma_f32_4x4x4_16b_bf16 v[28:31], a[250:251], a[236:237], v[28:31]\n"
      "v_mfma_f32_4x4x4_16b_bf16 v[32:35], a[250:251], a[238:239], v[32:35]\n"
      "v_cvt_pk_bf16_f32 v84, v84, v88\n"
      "v_cvt_pk_bf16_f32 v85, v85, v89\n"
      "v_cvt_pk_bf16_f32 v86, v86, v90\n"
      "v_cvt_pk_bf16_f32 v87, v87, v91\n"
      "v_accvgpr_write_b32 a120, v84\n"
      "v_accvgpr_write_b32 a121, v85\n"
      "v_accvgpr_write_b32 a122, v86\n"
      "v_accvgpr_write_b32 a123, v87\n"
      "v_cvt_pk_bf16_f32 v76, v76, v80\n"
      "v_cvt_pk_bf16_f32 v77, v77, v81\n"
      "v_cvt_pk_bf16_f32 v78, v78, v82\n"
      "v_cvt_pk_bf16_f32 v79, v79, v83\n"
      "v_accvgpr_write_b32 a116, v76\n"
      "v_accvgpr_write_b32 a117, v77\n"
      "v_accvgpr_write_b32 a118, v78\n"
      "v_accvgpr_write_b32 a119, v79\n"
      "s_waitcnt lgkmcnt(0)\n"
      "s_waitcnt vmcnt(14)\n"
      "s_barrier\n"
      "s_mov_b32 m0, s64\n"
      "s_nop 0\n"
      "v_mfma_f32_16x16x32_bf16 v[100:103], a[96:99], a[192:195], v[100:103]\n"
      "ds_read_b128 a[204:207], v3 offset:59264\n"
      "ds_read_b64_tr_b16 a[220:221], v7 offset:18944\n"
      "ds_read_b64_tr_b16 a[222:223], v7 offset:18952\n"
      "v_cvt_pk_bf16_f32 v36, v36, v40\n"
      "v_cvt_pk_bf16_f32 v37, v37, v41\n"
      "v_cvt_pk_bf16_f32 v38, v38, v42\n"
      "v_cvt_pk_bf16_f32 v39, v39, v43\n"
      "v_accvgpr_write_b32 a112, v36\n"
      "v_mfma_f32_16x16x32_bf16 v[104:107], a[100:103], a[192:195], v[104:107]\n"
      "ds_read_b128 a[208:211], v3 offset:59328\n"
      "s_add_u32 s53, s54, 1769472\n"
      "s_add_u32 m0, s37, 82432\n"
      "v_accvgpr_write_b32 a113, v37\n"
      "v_accvgpr_write_b32 a114, v38\n"
      "v_mfma_f32_16x16x32_bf16 v[108:111], a[104:107], a[192:195], v[108:111]\n"
      "buffer_load_dwordx4 v8, s[4:7], s53 offen offset:0 nt lds\n"
      "buffer_load_dwordx4 v9, s[4:7], s53 offen offset:1024 nt lds\n"
      "s_mov_b32 m0, s64\n"
      "v_accvgpr_write_b32 a115, v39\n"
      "v_mfma_f32_16x16x32_bf16 v[52:55], a[128:131], a[196:199], v[52:55]\n"
      "ds_read_b128 a[212:215], v3 offset:59392\n"
      "v_cvt_pk_bf16_f32 v28, v28, v32\n"
      "v_cvt_pk_bf16_f32 v29, v29, v33\n"
      "v_cvt_pk_bf16_f32 v30, v30, v34\n"
      "v_cvt_pk_bf16_f32 v31, v31, v35\n"
      "v_accvgpr_write_b32 a108, v28\n"
      "v_accvgpr_write_b32 a109, v29\n"
      "v_accvgpr_write_b32 a110, v30\n"
      "v_accvgpr_write_b32 a111, v31\n"
      "v_mfma_f32_16x16x32_bf16 v[56:59], a[120:123], a[196:199], v[56:59]\n"
      "v_mfma_f32_16x16x32_bf16 v[60:63], a[112:115], a[196:199], v[60:63]\n"
      "v_mfma_f32_16x16x32_bf16 v[64:67], a[124:127], a[200:203], v[64:67]\n"
      "ds_read_b128 a[192:195], v4 offset:59264\n"
      "ds_read_b64_tr_b16 a[224:225], v7 offset:19456\n"
      "s_add_u32 s53, s54, 1966080\n"
      "s_add_u32 m0, s37, 86536\n"
      "v_mfma_f32_16x16x32_bf16 v[68:71], a[116:119], a[200:203], v[68:71]\n"
      "buffer_load_dwordx4 v8, s[4:7], s53 offen offset:0 nt lds\n"
      "buffer_load_dwordx4 v9, s[4:7], s53 offen offset:1024 nt lds\n"
      "s_mov_b32 m0, s64\n"
      "v_mfma_f32_16x16x32_bf16 v[72:75], a[108:111], a[200:203], v[72:75]\n"
      "ds_read_b64_tr_b16 a[226:227], v7 offset:19464\n"
      "s_waitcnt lgkmcnt(3)\n"
      "v_mfma_f32_4x4x4_16b_bf16 v[20:23], a[216:217], a[220:221], 0\n"
      "v_add_f32_e64 v52, v52, v64\n"
      "v_mfma_f32_4x4x4_16b_bf16 v[24:27], a[216:217], a[222:223], 0\n"
      "v_add_f32 v53, v53, v65\n"
      "v_add_f32 v54, v54, v66\n"
      "v_add_f32_e64 v55, v55, v67\n"
      "v_mfma_f32_16x16x32_bf16 v[112:115], a[0:3], a[204:207], 0\n"
      "ds_read_b128 a[196:199], v4 offset:59328\n"
      "ds_write_addtid_b32 v52 offset:45312\n"
      "v_mfma_f32_16x16x32_bf16 v[116:119], a[4:7], a[204:207], 0\n"
      "ds_write_addtid_b32 v53 offset:45824\n"
      "ds_write_addtid_b32 v54 offset:46336\n"
      "v_mfma_f32_16x16x32_bf16 v[120:123], a[8:11], a[204:207], 0\n"
      "ds_write_addtid_b32 v55 offset:46848\n"
      "v_cvt_pk_bf16_f32 v20, v20, v24\n"
      "v_mfma_f32_16x16x32_bf16 v[112:115], a[12:15], a[208:211], v[112:115]\n"
      "ds_read_b128 a[200:203], v4 offset:59392\n"
      "ds_write_addtid_b32 v20 offset:18432\n"
      "v_mfma_f32_16x16x32_bf16 v[116:119], a[16:19], a[208:211], v[116:119]\n"
      "v_cvt_pk_bf16_f32 v21, v21, v25\n"
      "v_cvt_pk_bf16_f32 v22, v22, v26\n"
      "v_mfma_f32_16x16x32_bf16 v[120:123], a[20:23], a[208:211], v[120:123]\n"
      "ds_write_addtid_b32 v21 offset:22528\n"
      "ds_write_addtid_b32 v22 offset:26624\n"
      "v_mfma_f32_16x16x32_bf16 v[112:115], a[24:27], a[212:215], v[112:115]\n"
      "ds_read_b128 a[204:207], v5 offset:59264\n"
      "ds_read_b64_tr_b16 a[228:229], v7 offset:19968\n"
      "v_mfma_f32_16x16x32_bf16 v[116:119], a[28:31], a[212:215], v[116:119]\n"
      "ds_read_b64_tr_b16 a[230:231], v7 offset:19976\n"
      "v_cvt_pk_bf16_f32 v23, v23, v27\n"
      "v_mfma_f32_16x16x32_bf16 v[120:123], a[32:35], a[212:215], v[120:123]\n"
      "ds_write_addtid_b32 v23 offset:30720\n"
      "s_waitcnt lgkmcnt(6)\n"
      "v_mfma_f32_4x4x4_16b_bf16 v[28:31], a[216:217], a[224:225], 0\n"
      "v_add_f32 v56, v56, v68\n"
      "v_mfma_f32_4x4x4_16b_bf16 v[32:35], a[216:217], a[226:227], 0\n"
      "v_add_f32 v57, v57, v69\n"
      "v_add_f32 v58, v58, v70\n"
      "v_add_f32_e64 v59, v59, v71\n"
      "v_mfma_f32_16x16x32_bf16 v[112:115], a[36:39], a[192:195], v[112:115]\n"
      "ds_read_b128 a[208:211], v5 offset:59328\n"
      "ds_write_addtid_b32 v56 offset:47360\n"
      "v_mfma_f32_16x16x32_bf16 v[116:119], a[40:43], a[192:195], v[116:119]\n"
      "ds_write_addtid_b32 v57 offset:47872\n"
      "ds_write_addtid_b32 v58 offset:48384\n"
      "v_mfma_f32_16x16x32_bf16 v[120:123], a[44:47], a[192:195], v[120:123]\n"
      "ds_write_addtid_b32 v59 offset:48896\n"
      "v_cvt_pk_bf16_f32 v28, v28, v32\n"
      "v_mfma_f32_16x16x32_bf16 v[112:115], a[48:51], a[196:199], v[112:115]\n"
      "ds_read_b128 a[212:215], v5 offset:59392\n"
      "ds_write_addtid_b32 v28 offset:18944\n"
      "v_mfma_f32_16x16x32_bf16 v[116:119], a[52:55], a[196:199], v[116:119]\n"
      "v_cvt_pk_bf16_f32 v29, v29, v33\n"
      "v_cvt_pk_bf16_f32 v30, v30, v34\n"
      "v_mfma_f32_16x16x32_bf16 v[120:123], a[56:59], a[196:199], v[120:123]\n"
      "ds_write_addtid_b32 v29 offset:23040\n"
      "ds_write_addtid_b32 v30 offset:27136\n"
      "v_mfma_f32_16x16x32_bf16 v[112:115], a[60:63], a[200:203], v[112:115]\n"
      "ds_read_b128 a[192:195], v3 offset:0\n"
      "ds_read_b64_tr_b16 a[232:233], v7 offset:20480\n"
      "v_mfma_f32_16x16x32_bf16 v[116:119], a[64:67], a[200:203], v[116:119]\n"
      "ds_read_b64_tr_b16 a[234:235], v7 offset:20488\n"
      "v_cvt_pk_bf16_f32 v31, v31, v35\n"
      "v_mfma_f32_16x16x32_bf16 v[120:123], a[68:71], a[200:203], v[120:123]\n"
      "ds_write_addtid_b32 v31 offset:31232\n"
      "s_waitcnt lgkmcnt(6)\n"
      "v_mfma_f32_4x4x4_16b_bf16 v[20:23], a[216:217], a[228:229], 0\n"
      "v_add_f32 v60, v60, v72\n"
      "v_mfma_f32_4x4x4_16b_bf16 v[24:27], a[216:217], a[230:231], 0\n"
      "v_add_f32 v61, v61, v73\n"
      "v_add_f32 v62, v62, v74\n"
      "v_add_f32_e64 v63, v63, v75\n"
      "v_mfma_f32_16x16x32_bf16 v[112:115], a[72:75], a[204:207], v[112:115]\n"
      "ds_read_b128 a[196:199], v3 offset:64\n"
      "ds_write_addtid_b32 v60 offset:49408\n"
      "v_mfma_f32_16x16x32_bf16 v[116:119], a[76:79], a[204:207], v[116:119]\n"
      "ds_write_addtid_b32 v61 offset:49920\n"
      "ds_write_addtid_b32 v62 offset:50432\n"
      "v_mfma_f32_16x16x32_bf16 v[120:123], a[80:83], a[204:207], v[120:123]\n"
      "ds_write_addtid_b32 v63 offset:50944\n"
      "v_cvt_pk_bf16_f32 v20, v20, v24\n"
      "v_mfma_f32_16x16x32_bf16 v[112:115], a[84:87], a[208:211], v[112:115]\n"
      "ds_read_b128 a[200:203], v3 offset:128\n"
      "ds_write_addtid_b32 v20 offset:19456\n"
      "v_mfma_f32_16x16x32_bf16 v[116:119], a[88:91], a[208:211], v[116:119]\n"
      "v_cvt_pk_bf16_f32 v21, v21, v25\n"
      "v_cvt_pk_bf16_f32 v22, v22, v26\n"
      "v_mfma_f32_16x16x32_bf16 v[120:123], a[92:95], a[208:211], v[120:123]\n"
      "ds_write_addtid_b32 v21 offset:23552\n"
      "ds_write_addtid_b32 v22 offset:27648\n"
      "v_mfma_f32_16x16x32_bf16 v[112:115], a[96:99], a[212:215], v[112:115]\n"
      "ds_read_b128 a[204:207], v4 offset:0\n"
      "v_cvt_pk_bf16_f32 v23, v23, v27\n"
      "v_mfma_f32_16x16x32_bf16 v[116:119], a[100:103], a[212:215], v[116:119]\n"
      "ds_write_addtid_b32 v23 offset:31744\n"
      "v_mfma_f32_16x16x32_bf16 v[120:123], a[104:107], a[212:215], v[120:123]\n"
      "s_nop 0\n"
      "s_waitcnt lgkmcnt(4)\n"
      "v_mfma_f32_4x4x4_16b_bf16 v[28:31], a[216:217], a[232:233], 0\n"
      "v_mfma_f32_4x4x4_16b_bf16 v[32:35], a[216:217], a[234:235], 0\n"
      "v_mfma_f32_16x16x32_bf16 v[76:79], a[0:3], a[192:195], 0\n"
      "ds_read_b128 a[208:211], v4 offset:64\n"
      "v_mfma_f32_16x16x32_bf16 v[80:83], a[4:7], a[192:195], 0\n"
      "v_mfma_f32_16x16x32_bf16 v[84:87], a[8:11], a[192:195], 0\n"
      "v_mfma_f32_16x16x32_bf16 v[76:79], a[12:15], a[196:199], v[76:79]\n"
      "ds_read_b128 a[212:215], v4 offset:128\n"
      "v_cvt_pk_bf16_f32 v28, v28, v32\n"
      "v_mfma_f32_16x16x32_bf16 v[80:83], a[16:19], a[196:199], v[80:83]\n"
      "ds_write_addtid_b32 v28 offset:19968\n"
      "v_cvt_pk_bf16_f32 v29, v29, v33\n"
      "v_mfma_f32_16x16x32_bf16 v[84:87], a[20:23], a[196:199], v[84:87]\n"
      "ds_write_addtid_b32 v29 offset:24064\n"
      "v_cvt_pk_bf16_f32 v30, v30, v34\n"
      "v_mfma_f32_16x16x32_bf16 v[76:79], a[24:27], a[200:203], v[76:79]\n"
      "ds_read_b128 a[192:195], v5 offset:0\n"
      "ds_write_addtid_b32 v30 offset:28160\n"
      "v_mfma_f32_16x16x32_bf16 v[80:83], a[28:31], a[200:203], v[80:83]\n"
      "v_cvt_pk_bf16_f32 v31, v31, v35\n"
      "v_mfma_f32_16x16x32_bf16 v[84:87], a[32:35], a[200:203], v[84:87]\n"
      "ds_write_addtid_b32 v31 offset:32256\n"
      "s_nop 0\n"
      "s_waitcnt lgkmcnt(5)\n"
      "v_mfma_f32_16x16x32_bf16 v[76:79], a[36:39], a[204:207], v[76:79]\n"
      "ds_read_b128 a[196:199], v5 offset:64\n"
      "v_mfma_f32_16x16x32_bf16 v[80:83], a[40:43], a[204:207], v[80:83]\n"
      "v_mfma_f32_16x16x32_bf16 v[84:87], a[44:47], a[204:207], v[84:87]\n"
      "v_mfma_f32_16x16x32_bf16 v[76:79], a[48:51], a[208:211], v[76:79]\n"
      "ds_read_b128 a[200:203], v5 offset:128\n"
      "v_mfma_f32_16x16x32_bf16 v[80:83], a[52:55], a[208:211], v[80:83]\n"
      "v_mfma_f32_16x16x32_bf16 v[84:87], a[56:59], a[208:211], v[84:87]\n"
      "v_mfma_f32_16x16x32_bf16 v[76:79], a[60:63], a[212:215], v[76:79]\n"
      "v_fma_f32 v40, -2.0, v64, v52\n"
      "v_fma_f32 v41, -2.0, v65, v53\n"
      "v_fma_f32 v42, -2.0, v66, v54\n"
      "v_fma_f32 v43, -2.0, v67, v55\n"
      "v_mfma_f32_16x16x32_bf16 v[80:83], a[64:67], a[212:215], v[80:83]\n"
      "v_fma_f32 v44, -2.0, v68, v56\n"
      "v_fma_f32 v45, -2.0, v69, v57\n"
      "v_fma_f32 v46, -2.0, v70, v58\n"
      "v_fma_f32 v47, -2.0, v71, v59\n"
      "v_mfma_f32_16x16x32_bf16 v[84:87], a[68:71], a[212:215], v[84:87]\n"
      "v_fma_f32 v48, -2.0, v72, v60\n"
      "v_fma_f32 v49, -2.0, v73, v61\n"
      "v_fma_f32 v50, -2.0, v74, v62\n"
      "v_fma_f32 v51, -2.0, v75, v63\n"
      "s_waitcnt lgkmcnt(0)\n"
      "s_waitcnt vmcnt(10)\n"
      "s_barrier\n"
      "s_add_u32 s54, s54, 1966080\n"
      ".p2align 6\n"
      "LDloop_%=:\n"
      "v_mfma_f32_16x16x32_bf16 v[76:79], a[72:75], a[192:195], v[76:79]\n"
      "ds_read_b128 a[204:207], v0 offset:16384\n"
      "ds_read2st64_b32 v[28:29], v6 offset0:0 offset1:2\n"
      "ds_read2st64_b32 v[30:31], v6 offset0:4 offset1:6\n"
      "v_mfma_f32_16x16x32_bf16 v[80:83], a[76:79], a[192:195], v[80:83]\n"
      "ds_read_b128 a[208:211], v0 offset:20480\n"
      "ds_read2st64_b32 v[32:33], v6 offset0:8 offset1:10\n"
      "v_mfma_f32_16x16x32_bf16 v[84:87], a[80:83], a[192:195], v[84:87]\n"
      "s_add_u32 s58, s58, 3072\n"
      "s_add_u32 s59, s58, 196608\n"
      "ds_read2st64_b32 v[34:35], v6 offset0:12 offset1:14\n"
      "v_mfma_f32_16x16x32_bf16 v[76:79], a[84:87], a[196:199], v[76:79]\n"
      "ds_read_b128 a[212:215], v0 offset:16448\n"
      "v_mfma_f32_16x16x32_bf16 v[80:83], a[88:91], a[196:199], v[80:83]\n"
      "ds_read2st64_b32 v[36:37], v6 offset0:16 offset1:18\n"
      "ds_read2st64_b32 v[38:39], v6 offset0:20 offset1:22\n"
      "v_mfma_f32_16x16x32_bf16 v[84:87], a[92:95], a[196:199], v[84:87]\n"
      "v_mfma_f32_16x16x32_bf16 v[76:79], a[96:99], a[200:203], v[76:79]\n"
      "ds_read_b128 a[192:195], v0 offset:20544\n"
      "s_waitcnt lgkmcnt(7)\n"
      "v_fmac_f32 v40, s46, v28\n"
      "v_fmac_f32 v41, s46, v29\n"
      "v_fmac_f32 v42, s46, v30\n"
      "v_fmac_f32_e64 v43, s46, v31\n"
      "v_mfma_f32_16x16x32_bf16 v[80:83], a[100:103], a[200:203], v[80:83]\n"
      "ds_read2st64_b32 a[220:221], v12 offset0:181 offset1:183\n"
      "v_fma_f32 v28, 0.5, v28, v100\n"
      "v_fma_f32 v29, 0.5, v29, v101\n"
      "v_mfma_f32_16x16x32_bf16 v[84:87], a[104:107], a[200:203], v[84:87]\n"
      "ds_read_b64_tr_b16 a[244:245], v7 offset:37376\n"
      "ds_read2st64_b32 a[222:223], v15 offset0:181 offset1:183\n"
      "v_mfma_f32_16x16x32_bf16 v[52:55], v[252:255], a[204:207], 0\n"
      "ds_read_b128 a[196:199], v0 offset:16512\n"
      "ds_read_b64_tr_b16 a[246:247], v7 offset:37384\n"
      "v_mfma_f32_16x16x32_bf16 v[56:59], v[244:247], a[204:207], 0\n"
      "v_fma_f32 v30, 0.5, v30, v102\n"
      "v_fma_f32 v31, 0.5, v31, v103\n"
      "v_fmac_f32 v40, s48, v76\n"
      "v_mfma_f32_16x16x32_bf16 v[60:63], v[236:239], a[204:207], 0\n"
      "v_fmac_f32 v41, s48, v77\n"
      "v_fmac_f32 v42, s48, v78\n"
      "s_waitcnt lgkmcnt(5)\n"
      "v_mfma_f32_16x16x32_bf16 v[64:67], v[248:251], a[208:211], 0\n"
      "ds_read_b128 a[200:203], v0 offset:20608\n"
      "v_fmac_f32 v43, s48, v79\n"
      "v_fmac_f32 v28, s45, v40\n"
      "v_mfma_f32_16x16x32_bf16 v[68:71], v[240:243], a[208:211], 0\n"
      "v_fmac_f32 v29, s45, v41\n"
      "v_fmac_f32 v30, s45, v42\n"
      "v_mfma_f32_16x16x32_bf16 v[72:75], v[232:235], a[208:211], 0\n"
      "v_fmac_f32_e64 v31, s45, v43\n"
      "v_fma_f32 v40, s47, v40, -v76\n"
      "v_fma_f32 v41, s47, v41, -v77\n"
      "v_mfma_f32_16x16x32_bf16 v[52:55], v[228:231], a[212:215], v[52:55]\n"
      "ds_read_b128 a[204:207], v1 offset:16384\n"
      "v_fma_f32 v42, s47, v42, -v78\n"
      "v_mfma_f32_16x16x32_bf16 v[56:59], v[220:223], a[212:215], v[56:59]\n"
      "v_fma_f32 v43, s47, v43, -v79\n"
      "v_cvt_pk_bf16_f32 v28, v28, v29\n"
      "v_mfma_f32_16x16x32_bf16 v[60:63], v[212:215], a[212:215], v[60:63]\n"
      "ds_write_addtid_b32 v28 offset:32768\n"
      "ds_read2st64_b32 a[224:225], v13 offset0:181 offset1:183\n"
      "ds_read2st64_b32 a[226:227], v16 offset0:181 offset1:183\n"
      "v_mfma_f32_16x16x32_bf16 v[64:67], v[224:227], a[192:195], v[64:67]\n"
      "ds_read_b128 a[208:211], v1 offset:20480\n"
      "v_cvt_pk_bf16_f32 v30, v30, v31\n"
      "v_mfma_f32_16x16x32_bf16 v[68:71], v[216:219], a[192:195], v[68:71]\n"
      "ds_write_addtid_b32 v30 offset:33280\n"
      "v_cvt_pk_bf16_f32 v40, v40, v41\n"
      "v_cvt_pk_bf16_f32 v42, v42, v43\n"
      "s_waitcnt lgkmcnt(5)\n"
      "v_mfma_f32_4x4x4_16b_bf16 v[20:23], a[216:217], a[244:245], 0\n"
      "ds_write_addtid_b32 v40 offset:34816\n"
      "v_mfma_f32_4x4x4_16b_bf16 v[24:27], a[216:217], a[246:247], 0\n"
      "ds_write_addtid_b32 v42 offset:35328\n"
      "v_fmac_f32 v44, s46, v32\n"
      "v_fmac_f32 v45, s46, v33\n"
      "v_mfma_f32_16x16x32_bf16 v[72:75], v[208:211], a[192:195], v[72:75]\n"
      "v_fmac_f32 v46, s46, v34\n"
      "v_cvt_pk_bf16_f32 v20, v20, v24\n"
      "v_mfma_f32_16x16x32_bf16 v[52:55], v[204:207], a[196:199], v[52:55]\n"
      "buffer_store_dwordx4 a[220:223], v18, s[8:11], s58 offen offset:0 sc1\n"
      "ds_read_b128 a[212:215], v1 offset:16448\n"
      "v_mfma_f32_16x16x32_bf16 v[56:59], v[196:199], a[196:199], v[56:59]\n"
      "s_add_u32 s54, s54, -193536\n"
      "s_add_u32 m0, s37, 99328\n"
      "v_mfma_f32_16x16x32_bf16 v[60:63], v[188:191], a[196:199], v[60:63]\n"
      "buffer_load_dwordx4 v10, s[4:7], s54 offen offset:0 nt lds\n"
      "buffer_load_dwordx4 v11, s[4:7], s54 offen offset:1024 nt lds\n"
      "s_mov_b32 m0, s64\n"
      "v_mfma_f32_16x16x32_bf16 v[64:67], v[200:203], a[200:203], v[64:67]\n"
      "ds_read_b128 a[192:195], v1 offset:20544\n"
      "ds_write_addtid_b32 v20 offset:3584\n"
      "v_mfma_f32_16x16x32_bf16 v[68:71], v[192:195], a[200:203], v[68:71]\n"
      "v_cvt_pk_bf16_f32 v21, v21, v25\n"
      "v_fmac_f32 v47, s46, v35\n"
      "v_cvt_pk_bf16_f32 v22, v22, v26\n"
      "v_mfma_f32_16x16x32_bf16 v[72:75], v[184:187], a[200:203], v[72:75]\n"
      "ds_write_addtid_b32 v21 offset:7680\n"
      "ds_read2st64_b32 a[228:229], v14 offset0:181 offset1:183\n"
      "v_mfma_f32_16x16x32_bf16 v[52:55], v[180:183], a[204:207], v[52:55]\n"
      "ds_read_b128 a[196:199], v1 offset:16512\n"
      "ds_write_addtid_b32 v22 offset:11776\n"
      "v_mfma_f32_16x16x32_bf16 v[56:59], v[172:175], a[204:207], v[56:59]\n"
      "ds_read2st64_b32 a[230:231], v17 offset0:181 offset1:183\n"
      "v_cvt_pk_bf16_f32 v23, v23, v27\n"
      "v_fma_f32 v32, 0.5, v32, v104\n"
      "v_mfma_f32_16x16x32_bf16 v[60:63], v[164:167], a[204:207], v[60:63]\n"
      "ds_write_addtid_b32 v23 offset:15872\n"
      "v_fma_f32 v33, 0.5, v33, v105\n"
      "s_nop 0\n"
      "s_waitcnt lgkmcnt(6)\n"
      "v_mfma_f32_16x16x32_bf16 v[64:67], v[176:179], a[208:211], v[64:67]\n"
      "ds_read_b128 a[200:203], v1 offset:20608\n"
      "v_fma_f32 v34, 0.5, v34, v106\n"
      "v_fma_f32 v35, 0.5, v35, v107\n"
      "v_mfma_f32_16x16x32_bf16 v[68:71], v[168:171], a[208:211], v[68:71]\n"
      "buffer_store_dwordx4 a[224:227], v18, s[8:11], s58 offen offset:128 sc1\n"
      "v_mfma_f32_16x16x32_bf16 v[72:75], v[160:163], a[208:211], v[72:75]\n"
      "v_fmac_f32 v44, s48, v80\n"
      "v_fmac_f32 v45, s48, v81\n"
      "v_mfma_f32_16x16x32_bf16 v[52:55], v[156:159], a[212:215], v[52:55]\n"
      "ds_read_b128 a[204:207], v2 offset:16384\n"
      "v_fmac_f32_e64 v46, s48, v82\n"
      "v_mfma_f32_16x16x32_bf16 v[56:59], v[148:151], a[212:215], v[56:59]\n"
      "ds_read2st64_b32 a[232:233], v12 offset0:4 offset1:6\n"
      "ds_read2st64_b32 a[234:235], v15 offset0:4 offset1:6\n"
      "v_fmac_f32_e64 v47, s48, v83\n"
      "v_mfma_f32_16x16x32_bf16 v[60:63], v[140:143], a[212:215], v[60:63]\n"
      "s_add_u32 s54, s54, 196608\n"
      "s_add_u32 m0, s37, 103432\n"
      "v_mfma_f32_16x16x32_bf16 v[64:67], v[152:155], a[192:195], v[64:67]\n"
      "buffer_load_dwordx4 v10, s[4:7], s54 offen offset:0 nt lds\n"
      "buffer_load_dwordx4 v11, s[4:7], s54 offen offset:1024 nt lds\n"
      "s_mov_b32 m0, s64\n"
      "ds_read_b128 a[208:211], v2 offset:20480\n"
      "v_mfma_f32_16x16x32_bf16 v[68:71], v[144:147], a[192:195], v[68:71]\n"
      "v_fmac_f32 v32, s45, v44\n"
      "v_fmac_f32_e64 v33, s45, v45\n"
      "v_fmac_f32_e64 v34, s45, v46\n"
      "v_mfma_f32_16x16x32_bf16 v[72:75], v[136:139], a[192:195], v[72:75]\n"
      "v_fmac_f32 v35, s45, v47\n"
      "v_fma_f32 v44, s47, v44, -v80\n"
      "s_waitcnt lgkmcnt(1)\n"
      "v_mfma_f32_16x16x32_bf16 v[52:55], v[132:135], a[196:199], v[52:55]\n"
      "ds_read_b128 a[212:215], v2 offset:16448\n"
      "v_fma_f32 v45, s47, v45, -v81\n"
      "v_mfma_f32_16x16x32_bf16 v[56:59], v[124:127], a[196:199], v[56:59]\n"
      "buffer_store_dwordx4 a[228:231], v18, s[8:11], s58 offen offset:256 sc1\n"
      "v_mfma_f32_16x16x32_bf16 v[60:63], a[184:187], a[196:199], v[60:63]\n"
      "v_fma_f32 v46, s47, v46, -v82\n"
      "v_fma_f32 v47, s47, v47, -v83\n"
      "v_mfma_f32_16x16x32_bf16 v[64:67], v[128:131], a[200:203], v[64:67]\n"
      "ds_read_b128 a[192:195], v2 offset:20544\n"
      "v_cvt_pk_bf16_f32 v32, v32, v33\n"
      "v_mfma_f32_16x16x32_bf16 v[68:71], a[188:191], a[200:203], v[68:71]\n"
      "ds_write_addtid_b32 v32 offset:40964\n"
      "v_cvt_pk_bf16_f32 v34, v34, v35\n"
      "v_cvt_pk_bf16_f32 v44, v44, v45\n"
      "v_mfma_f32_16x16x32_bf16 v[72:75], a[180:183], a[200:203], v[72:75]\n"
      "ds_write_addtid_b32 v34 offset:41476\n"
      "ds_write_addtid_b32 v44 offset:43012\n"
      "ds_read2st64_b32 a[236:237], v13 offset0:4 offset1:6\n"
      "v_cvt_pk_bf16_f32 v46, v46, v47\n"
      "v_mfma_f32_16x16x32_bf16 v[52:55], a[176:179], a[204:207], v[52:55]\n"
      "ds_read_b128 a[196:199], v2 offset:16512\n"
      "ds_write_addtid_b32 v46 offset:43524\n"
      "v_mfma_f32_16x16x32_bf16 v[56:59], a[168:171], a[204:207], v[56:59]\n"
      "ds_read2st64_b32 a[238:239], v16 offset0:4 offset1:6\n"
      "v_fmac_f32 v48, s46, v36\n"
      "v_mfma_f32_16x16x32_bf16 v[60:63], a[160:163], a[204:207], v[60:63]\n"
      "v_fmac_f32_e64 v49, s46, v37\n"
      "v_fmac_f32_e64 v50, s46, v38\n"
      "s_waitcnt lgkmcnt(6)\n"
      "v_mfma_f32_16x16x32_bf16 v[64:67], a[172:175], a[208:211], v[64:67]\n"
      "buffer_store_dwordx4 a[232:235], v18, s[8:11], s59 offen offset:0 sc1\n"
      "ds_read_b128 a[200:203], v2 offset:20608\n"
      "v_mfma_f32_16x16x32_bf16 v[68:71], a[164:167], a[208:211], v[68:71]\n"
      "v_fmac_f32_e64 v51, s46, v39\n"
      "v_fma_f32 v36, 0.5, v36, v108\n"
      "v_fma_f32 v37, 0.5, v37, v109\n"
      "v_mfma_f32_16x16x32_bf16 v[72:75], a[156:159], a[208:211], v[72:75]\n"
      "v_fma_f32 v38, 0.5, v38, v110\n"
      "v_fma_f32 v39, 0.5, v39, v111\n"
      "v_fmac_f32_e64 v48, s48, v84\n"
      "v_mfma_f32_16x16x32_bf16 v[52:55], a[152:155], a[212:215], v[52:55]\n"
      "ds_read_b128 a[204:207], v3 offset:16896\n"
      "v_fmac_f32_e64 v49, s48, v85\n"
      "v_mfma_f32_16x16x32_bf16 v[56:59], a[144:147], a[212:215], v[56:59]\n"
      "v_fmac_f32_e64 v50, s48, v86\n"
      "v_fmac_f32_e64 v51, s48, v87\n"
      "v_mfma_f32_16x16x32_bf16 v[60:63], a[136:139], a[212:215], v[60:63]\n"
      "v_fmac_f32_e64 v36, s45, v48\n"
      "v_fmac_f32_e64 v37, s45, v49\n"
      "v_mfma_f32_16x16x32_bf16 v[64:67], a[148:151], a[192:195], v[64:67]\n"
      "ds_read_b128 a[208:211], v3 offset:16960\n"
      "v_fmac_f32_e64 v38, s45, v50\n"
      "v_mfma_f32_16x16x32_bf16 v[68:71], a[140:143], a[192:195], v[68:71]\n"
      "v_fmac_f32 v39, s45, v51\n"
      "v_fma_f32 v48, s47, v48, -v84\n"
      "v_mfma_f32_16x16x32_bf16 v[72:75], a[132:135], a[192:195], v[72:75]\n"
      "ds_read2st64_b32 a[240:241], v14 offset0:4 offset1:6\n"
      "ds_read2st64_b32 a[242:243], v17 offset0:4 offset1:6\n"
      "v_fma_f32 v49, s47, v49, -v85\n"
      "v_fma_f32 v50, s47, v50, -v86\n"
      "s_waitcnt lgkmcnt(3)\n"
      "v_mfma_f32_16x16x32_bf16 v[52:55], a[128:131], a[196:199], v[52:55]\n"
      "ds_read_b128 a[212:215], v3 offset:17024\n"
      "v_fma_f32 v51, s47, v51, -v87\n"
      "v_mfma_f32_16x16x32_bf16 v[56:59], a[120:123], a[196:199], v[56:59]\n"
      "buffer_store_dwordx4 a[236:239], v18, s[8:11], s59 offen offset:128 sc1\n"
      "v_mfma_f32_16x16x32_bf16 v[60:63], a[112:115], a[196:199], v[60:63]\n"
      "v_cvt_pk_bf16_f32 v36, v36, v37\n"
      "v_cvt_pk_bf16_f32 v38, v38, v39\n"
      "v_mfma_f32_16x16x32_bf16 v[64:67], a[124:127], a[200:203], v[64:67]\n"
      "ds_read_b128 a[192:195], v4 offset:16896\n"
      "ds_write_addtid_b32 v36 offset:36864\n"
      "ds_write_addtid_b32 v38 offset:37376\n"
      "s_add_u32 s54, s54, 193536\n"
      "s_add_u32 m0, s37, 90760\n"
      "v_mfma_f32_16x16x32_bf16 v[68:71], a[116:119], a[200:203], v[68:71]\n"
      "buffer_load_dwordx4 v8, s[4:7], s54 offen offset:0 nt lds\n"
      "buffer_load_dwordx4 v9, s[4:7], s54 offen offset:1024 nt lds\n"
      "s_mov_b32 m0, s64\n"
      "v_mfma_f32_16x16x32_bf16 v[72:75], a[108:111], a[200:203], v[72:75]\n"
      "v_add_f32 v52, v52, v64\n"
      "v_add_f32 v53, v53, v65\n"
      "v_mfma_f32_16x16x32_bf16 v[88:91], a[0:3], a[204:207], 0\n"
      "ds_read_b128 a[196:199], v4 offset:16960\n"
      "ds_write_addtid_b32 v52 offset:51456\n"
      "ds_write_addtid_b32 v53 offset:51968\n"
      "v_mfma_f32_16x16x32_bf16 v[92:95], a[4:7], a[204:207], 0\n"
      "v_add_f32 v54, v54, v66\n"
      "v_add_f32 v55, v55, v67\n"
      "v_mfma_f32_16x16x32_bf16 v[96:99], a[8:11], a[204:207], 0\n"
      "ds_write_addtid_b32 v54 offset:52480\n"
      "ds_write_addtid_b32 v55 offset:52992\n"
      "v_add_f32_e64 v56, v56, v68\n"
      "s_waitcnt lgkmcnt(5)\n"
      "v_mfma_f32_16x16x32_bf16 v[88:91], a[12:15], a[208:211], v[88:91]\n"
      "ds_read_b128 a[200:203], v4 offset:17024\n"
      "ds_write_addtid_b32 v56 offset:53504\n"
      "v_mfma_f32_16x16x32_bf16 v[92:95], a[16:19], a[208:211], v[92:95]\n"
      "v_add_f32 v57, v57, v69\n"
      "v_cvt_pk_bf16_f32 v48, v48, v49\n"
      "v_add_f32 v58, v58, v70\n"
      "v_mfma_f32_16x16x32_bf16 v[96:99], a[20:23], a[208:211], v[96:99]\n"
      "ds_write_addtid_b32 v48 offset:38912\n"
      "ds_write_addtid_b32 v57 offset:54016\n"
      "v_mfma_f32_16x16x32_bf16 v[88:91], a[24:27], a[212:215], v[88:91]\n"
      "ds_read_b128 a[204:207], v5 offset:16896\n"
      "ds_write_addtid_b32 v58 offset:54528\n"
      "v_mfma_f32_16x16x32_bf16 v[92:95], a[28:31], a[212:215], v[92:95]\n"
      "buffer_store_dwordx4 a[240:243], v18, s[8:11], s59 offen offset:256 sc1\n"
      "v_mfma_f32_16x16x32_bf16 v[96:99], a[32:35], a[212:215], v[96:99]\n"
      "v_add_f32 v59, v59, v71\n"
      "v_add_f32 v60, v60, v72\n"
      "v_cvt_pk_bf16_f32 v50, v50, v51\n"
      "v_mfma_f32_16x16x32_bf16 v[88:91], a[36:39], a[192:195], v[88:91]\n"
      "ds_read_b128 a[208:211], v5 offset:16960\n"
      "ds_write_addtid_b32 v50 offset:39424\n"
      "v_mfma_f32_16x16x32_bf16 v[92:95], a[40:43], a[192:195], v[92:95]\n"
      "ds_write_addtid_b32 v59 offset:55040\n"
      "ds_write_addtid_b32 v60 offset:55552\n"
      "v_add_f32 v61, v61, v73\n"
      "v_mfma_f32_16x16x32_bf16 v[96:99], a[44:47], a[192:195], v[96:99]\n"
      "ds_write_addtid_b32 v61 offset:56064\n"
      "v_add_f32 v62, v62, v74\n"
      "v_add_f32_e64 v63, v63, v75\n"
      "s_waitcnt lgkmcnt(5)\n"
      "v_mfma_f32_16x16x32_bf16 v[88:91], a[48:51], a[196:199], v[88:91]\n"
      "ds_read_b128 a[212:215], v5 offset:17024\n"
      "s_add_u32 s54, s54, 196608\n"
      "s_add_u32 m0, s37, 95104\n"
      "v_mfma_f32_16x16x32_bf16 v[92:95], a[52:55], a[196:199], v[92:95]\n"
      "buffer_load_dwordx4 v8, s[4:7], s54 offen offset:0 nt lds\n"
      "buffer_load_dwordx4 v9, s[4:7], s54 offen offset:1024 nt lds\n"
      "s_mov_b32 m0, s64\n"
      "v_mfma_f32_16x16x32_bf16 v[96:99], a[56:59], a[196:199], v[96:99]\n"
      "ds_write_addtid_b32 v62 offset:56576\n"
      "ds_write_addtid_b32 v63 offset:57088\n"
      "v_mfma_f32_16x16x32_bf16 v[88:91], a[60:63], a[200:203], v[88:91]\n"
      "v_fma_f32 v40, -2.0, v64, v52\n"
      "v_fma_f32 v41, -2.0, v65, v53\n"
      "v_fma_f32 v42, -2.0, v66, v54\n"
      "v_fma_f32 v43, -2.0, v67, v55\n"
      "v_fma_f32 v44, -2.0, v68, v56\n"
      "v_mfma_f32_16x16x32_bf16 v[92:95], a[64:67], a[200:203], v[92:95]\n"
      "v_fma_f32 v45, -2.0, v69, v57\n"
      "v_fma_f32 v46, -2.0, v70, v58\n"
      "v_fma_f32 v47, -2.0, v71, v59\n"
      "v_fma_f32 v48, -2.0, v72, v60\n"
      "v_mfma_f32_16x16x32_bf16 v[96:99], a[68:71], a[200:203], v[96:99]\n"
      "v_fma_f32 v49, -2.0, v73, v61\n"
      "v_fma_f32 v50, -2.0, v74, v62\n"
      "v_fma_f32 v51, -2.0, v75, v63\n"
      "s_waitcnt lgkmcnt(0)\n"
      "s_waitcnt vmcnt(14)\n"
      "s_barrier\n"
      "LDmid_%=:\n"
      "v_mfma_f32_16x16x32_bf16 v[88:91], a[72:75], a[204:207], v[88:91]\n"
      "ds_read_b128 a[192:195], v0 offset:0\n"
      "ds_read2st64_b32 v[28:29], v6 offset0:24 offset1:26\n"
      "ds_read2st64_b32 v[30:31], v6 offset0:28 offset1:30\n"
      "v_mfma_f32_16x16x32_bf16 v[92:95], a[76:79], a[204:207], v[92:95]\n"
      "ds_read_b128 a[196:199], v0 offset:4096\n"
      "ds_read2st64_b32 v[32:33], v6 offset0:32 offset1:34\n"
      "v_mfma_f32_16x16x32_bf16 v[96:99], a[80:83], a[204:207], v[96:99]\n"
      "s_add_u32 s62, s62, 786432\n"
      "s_mov_b32 s58, s62\n"
      "ds_read2st64_b32 v[34:35], v6 offset0:36 offset1:38\n"
      "v_mfma_f32_16x16x32_bf16 v[88:91], a[84:87], a[208:211], v[88:91]\n"
      "ds_read_b128 a[200:203], v0 offset:64\n"
      "s_add_u32 s59, s58, 196608\n"
      "v_mfma_f32_16x16x32_bf16 v[92:95], a[88:91], a[208:211], v[92:95]\n"
      "ds_read2st64_b32 v[36:37], v6 offset0:40 offset1:42\n"
      "ds_read2st64_b32 v[38:39], v6 offset0:44 offset1:46\n"
      "v_mfma_f32_16x16x32_bf16 v[96:99], a[92:95], a[208:211], v[96:99]\n"
      "v_mfma_f32_16x16x32_bf16 v[88:91], a[96:99], a[212:215], v[88:91]\n"
      "ds_read_b128 a[204:207], v0 offset:4160\n"
      "s_waitcnt lgkmcnt(7)\n"
      "v_fmac_f32 v40, s46, v28\n"
      "v_fmac_f32 v41, s46, v29\n"
      "v_fmac_f32 v42, s46, v30\n"
      "v_mfma_f32_16x16x32_bf16 v[92:95], a[100:103], a[212:215], v[92:95]\n"
      "ds_read2st64_b32 a[220:221], v12 offset0:177 offset1:179\n"
      "ds_read2st64_b32 a[222:223], v15 offset0:177 offset1:179\n"
      "v_fmac_f32_e64 v43, s46, v31\n"
      "v_mfma_f32_16x16x32_bf16 v[96:99], a[104:107], a[212:215], v[96:99]\n"
      "ds_read_b64_tr_b16 a[244:245], v7 offset:54272\n"
      "ds_read_b64_tr_b16 a[246:247], v7 offset:54280\n"
      "v_mfma_f32_16x16x32_bf16 v[52:55], v[252:255], a[192:195], 0\n"
      "ds_read_b128 a[208:211], v0 offset:128\n"
      "v_fma_f32 v28, 0.5, v28, v112\n"
      "v_mfma_f32_16x16x32_bf16 v[56:59], v[244:247], a[192:195], 0\n"
      "v_fma_f32 v29, 0.5, v29, v113\n"
      "v_fma_f32 v30, 0.5, v30, v114\n"
      "v_fma_f32 v31, 0.5, v31, v115\n"
      "v_mfma_f32_16x16x32_bf16 v[60:63], v[236:239], a[192:195], 0\n"
      "v_fmac_f32 v40, s48, v88\n"
      "v_fmac_f32 v41, s48, v89\n"
      "s_waitcnt lgkmcnt(6)\n"
      "v_mfma_f32_16x16x32_bf16 v[64:67], v[248:251], a[196:199], 0\n"
      "ds_read_b128 a[212:215], v0 offset:4224\n"
      "v_fmac_f32 v42, s48, v90\n"
      "v_fmac_f32_e64 v43, s48, v91\n"
      "v_mfma_f32_16x16x32_bf16 v[68:71], v[240:243], a[196:199], 0\n"
      "v_fmac_f32_e64 v28, s45, v40\n"
      "v_fmac_f32_e64 v29, s45, v41\n"
      "v_mfma_f32_16x16x32_bf16 v[72:75], v[232:235], a[196:199], 0\n"
      "v_fmac_f32_e64 v30, s45, v42\n"
      "v_fmac_f32_e64 v31, s45, v43\n"
      "v_fma_f32 v40, s47, v40, -v88\n"
      "v_mfma_f32_16x16x32_bf16 v[52:55], v[228:231], a[200:203], v[52:55]\n"
      "ds_read_b128 a[192:195], v1 offset:0\n"
      "v_fma_f32 v41, s47, v41, -v89\n"
      "v_mfma_f32_16x16x32_bf16 v[56:59], v[220:223], a[200:203], v[56:59]\n"
      "v_fma_f32 v42, s47, v42, -v90\n"
      "v_fma_f32 v43, s47, v43, -v91\n"
      "v_mfma_f32_16x16x32_bf16 v[60:63], v[212:215], a[200:203], v[60:63]\n"
      "ds_read2st64_b32 a[224:225], v13 offset0:177 offset1:179\n"
      "ds_read2st64_b32 a[226:227], v16 offset0:177 offset1:179\n"
      "v_cvt_pk_bf16_f32 v28, v28, v29\n"
      "s_nop 0\n"
      "s_waitcnt lgkmcnt(3)\n"
      "v_mfma_f32_16x16x32_bf16 v[64:67], v[224:227], a[204:207], v[64:67]\n"
      "ds_read_b128 a[196:199], v1 offset:4096\n"
      "ds_write_addtid_b32 v28 offset:33792\n"
      "v_mfma_f32_16x16x32_bf16 v[68:71], v[216:219], a[204:207], v[68:71]\n"
      "v_cvt_pk_bf16_f32 v30, v30, v31\n"
      "v_cvt_pk_bf16_f32 v40, v40, v41\n"
      "v_cvt_pk_bf16_f32 v42, v42, v43\n"
      "v_mfma_f32_4x4x4_16b_bf16 v[20:23], a[216:217], a[244:245], 0\n"
      "v_mfma_f32_4x4x4_16b_bf16 v[24:27], a[216:217], a[246:247], 0\n"
      "ds_write_addtid_b32 v30 offset:34304\n"
      "ds_write_addtid_b32 v40 offset:35840\n"
      "ds_write_addtid_b32 v42 offset:36352\n"
      "v_fmac_f32_e64 v44, s46, v32\n"
      "v_mfma_f32_16x16x32_bf16 v[72:75], v[208:211], a[204:207], v[72:75]\n"
      "v_cvt_pk_bf16_f32 v20, v20, v24\n"
      "v_fmac_f32_e64 v45, s46, v33\n"
      "v_mfma_f32_16x16x32_bf16 v[52:55], v[204:207], a[208:211], v[52:55]\n"
      "buffer_store_dwordx4 a[220:223], v18, s[8:11], s58 offen offset:0 sc1\n"
      "ds_read_b128 a[200:203], v1 offset:64\n"
      "v_mfma_f32_16x16x32_bf16 v[56:59], v[196:199], a[208:211], v[56:59]\n"
      "s_add_u32 s54, s54, -193536\n"
      "s_add_u32 m0, s37, 107656\n"
      "v_mfma_f32_16x16x32_bf16 v[60:63], v[188:191], a[208:211], v[60:63]\n"
      "buffer_load_dwordx4 v10, s[4:7], s54 offen offset:0 nt lds\n"
      "buffer_load_dwordx4 v11, s[4:7], s54 offen offset:1024 nt lds\n"
      "s_mov_b32 m0, s64\n"
      "v_mfma_f32_16x16x32_bf16 v[64:67], v[200:203], a[212:215], v[64:67]\n"
      "ds_read_b128 a[204:207], v1 offset:4160\n"
      "ds_write_addtid_b32 v20 offset:19968\n"
      "v_mfma_f32_16x16x32_bf16 v[68:71], v[192:195], a[212:215], v[68:71]\n"
      "v_cvt_pk_bf16_f32 v21, v21, v25\n"
      "v_fmac_f32 v46, s46, v34\n"
      "v_cvt_pk_bf16_f32 v22, v22, v26\n"
      "v_mfma_f32_16x16x32_bf16 v[72:75], v[184:187], a[212:215], v[72:75]\n"
      "ds_write_addtid_b32 v21 offset:24064\n"
      "ds_read2st64_b32 a[228:229], v14 offset0:177 offset1:179\n"
      "s_nop 0\n"
      "s_waitcnt lgkmcnt(4)\n"
      "v_mfma_f32_16x16x32_bf16 v[52:55], v[180:183], a[192:195], v[52:55]\n"
      "ds_read_b128 a[208:211], v1 offset:128\n"
      "ds_write_addtid_b32 v22 offset:28160\n"
      "ds_read2st64_b32 a[230:231], v17 offset0:177 offset1:179\n"
      "v_mfma_f32_16x16x32_bf16 v[56:59], v[172:175], a[192:195], v[56:59]\n"
      "v_cvt_pk_bf16_f32 v23, v23, v27\n"
      "v_fmac_f32_e64 v47, s46, v35\n"
      "v_mfma_f32_16x16x32_bf16 v[60:63], v[164:167], a[192:195], v[60:63]\n"
      "ds_write_addtid_b32 v23 offset:32256\n"
      "v_fma_f32 v32, 0.5, v32, v116\n"
      "v_fma_f32 v33, 0.5, v33, v117\n"
      "v_mfma_f32_16x16x32_bf16 v[64:67], v[176:179], a[196:199], v[64:67]\n"
      "ds_read_b128 a[212:215], v1 offset:4224\n"
      "v_fma_f32 v34, 0.5, v34, v118\n"
      "v_mfma_f32_16x16x32_bf16 v[68:71], v[168:171], a[196:199], v[68:71]\n"
      "buffer_store_dwordx4 a[224:227], v18, s[8:11], s58 offen offset:128 sc1\n"
      "v_mfma_f32_16x16x32_bf16 v[72:75], v[160:163], a[196:199], v[72:75]\n"
      "v_fma_f32 v35, 0.5, v35, v119\n"
      "v_fmac_f32 v44, s48, v92\n"
      "v_fmac_f32 v45, s48, v93\n"
      "v_mfma_f32_16x16x32_bf16 v[52:55], v[156:159], a[200:203], v[52:55]\n"
      "ds_read_b128 a[192:195], v2 offset:0\n"
      "v_fmac_f32_e64 v46, s48, v94\n"
      "v_mfma_f32_16x16x32_bf16 v[56:59], v[148:151], a[200:203], v[56:59]\n"
      "ds_read2st64_b32 a[232:233], v12 offset0:0 offset1:2\n"
      "ds_read2st64_b32 a[234:235], v15 offset0:0 offset1:2\n"
      "v_fmac_f32_e64 v47, s48, v95\n"
      "v_mfma_f32_16x16x32_bf16 v[60:63], v[140:143], a[200:203], v[60:63]\n"
      "s_add_u32 s54, s54, 196608\n"
      "s_add_u32 m0, s37, 112000\n"
      "s_waitcnt lgkmcnt(3)\n"
      "v_mfma_f32_16x16x32_bf16 v[64:67], v[152:155], a[204:207], v[64:67]\n"
      "buffer_load_dwordx4 v10, s[4:7], s54 offen offset:0 nt lds\n"
      "buffer_load_dwordx4 v11, s[4:7], s54 offen offset:1024 nt lds\n"
      "s_mov_b32 m0, s64\n"
      "ds_read_b128 a[196:199], v2 offset:4096\n"
      "v_mfma_f32_16x16x32_bf16 v[68:71], v[144:147], a[204:207], v[68:71]\n"
      "v_fmac_f32_e64 v32, s45, v44\n"
      "v_fmac_f32_e64 v33, s45, v45\n"
      "v_fmac_f32_e64 v34, s45, v46\n"
      "v_mfma_f32_16x16x32_bf16 v[72:75], v[136:139], a[204:207], v[72:75]\n"
      "v_fmac_f32_e64 v35, s45, v47\n"
      "v_fma_f32 v44, s47, v44, -v92\n"
      "v_mfma_f32_16x16x32_bf16 v[52:55], v[132:135], a[208:211], v[52:55]\n"
      "ds_read_b128 a[200:203], v2 offset:64\n"
      "v_fma_f32 v45, s47, v45, -v93\n"
      "v_mfma_f32_16x16x32_bf16 v[56:59], v[124:127], a[208:211], v[56:59]\n"
      "buffer_store_dwordx4 a[228:231], v18, s[8:11], s58 offen offset:256 sc1\n"
      "v_mfma_f32_16x16x32_bf16 v[60:63], a[184:187], a[208:211], v[60:63]\n"
      "v_fma_f32 v46, s47, v46, -v94\n"
      "v_fma_f32 v47, s47, v47, -v95\n"
      "v_mfma_f32_16x16x32_bf16 v[64:67], v[128:131], a[212:215], v[64:67]\n"
      "ds_read_b128 a[204:207], v2 offset:4160\n"
      "v_cvt_pk_bf16_f32 v32, v32, v33\n"
      "v_mfma_f32_16x16x32_bf16 v[68:71], a[188:191], a[212:215], v[68:71]\n"
      "ds_write_addtid_b32 v32 offset:41988\n"
      "v_cvt_pk_bf16_f32 v34, v34, v35\n"
      "v_cvt_pk_bf16_f32 v44, v44, v45\n"
      "v_mfma_f32_16x16x32_bf16 v[72:75], a[180:183], a[212:215], v[72:75]\n"
      "ds_write_addtid_b32 v34 offset:42500\n"
      "ds_write_addtid_b32 v44 offset:44036\n"
      "ds_read2st64_b32 a[236:237], v13 offset0:0 offset1:2\n"
      "v_cvt_pk_bf16_f32 v46, v46, v47\n"
      "s_waitcnt lgkmcnt(5)\n"
      "v_mfma_f32_16x16x32_bf16 v[52:55], a[176:179], a[192:195], v[52:55]\n"
      "ds_read_b128 a[208:211], v2 offset:128\n"
      "ds_write_addtid_b32 v46 offset:44548\n"
      "v_mfma_f32_16x16x32_bf16 v[56:59], a[168:171], a[192:195], v[56:59]\n"
      "ds_read2st64_b32 a[238:239], v16 offset0:0 offset1:2\n"
      "v_fmac_f32 v48, s46, v36\n"
      "v_mfma_f32_16x16x32_bf16 v[60:63], a[160:163], a[192:195], v[60:63]\n"
      "v_fmac_f32 v49, s46, v37\n"
      "v_fmac_f32 v50, s46, v38\n"
      "v_mfma_f32_16x16x32_bf16 v[64:67], a[172:175], a[196:199], v[64:67]\n"
      "buffer_store_dwordx4 a[232:235], v18, s[8:11], s59 offen offset:0 sc1\n"
      "ds_read_b128 a[212:215], v2 offset:4224\n"
      "v_mfma_f32_16x16x32_bf16 v[68:71], a[164:167], a[196:199], v[68:71]\n"
      "v_fmac_f32_e64 v51, s46, v39\n"
      "v_fma_f32 v36, 0.5, v36, v120\n"
      "v_fma_f32 v37, 0.5, v37, v121\n"
      "v_mfma_f32_16x16x32_bf16 v[72:75], a[156:159], a[196:199], v[72:75]\n"
      "v_fma_f32 v38, 0.5, v38, v122\n"
      "v_fma_f32 v39, 0.5, v39, v123\n"
      "v_mfma_f32_16x16x32_bf16 v[52:55], a[152:155], a[200:203], v[52:55]\n"
      "ds_read_b128 a[192:195], v3 offset:33792\n"
      "v_fmac_f32 v48, s48, v96\n"
      "v_fmac_f32 v49, s48, v97\n"
      "v_mfma_f32_16x16x32_bf16 v[56:59], a[144:147], a[200:203], v[56:59]\n"
      "v_fmac_f32 v50, s48, v98\n"
      "v_fmac_f32 v51, s48, v99\n"
      "v_mfma_f32_16x16x32_bf16 v[60:63], a[136:139], a[200:203], v[60:63]\n"
      "v_fmac_f32 v36, s45, v48\n"
      "v_fmac_f32 v37, s45, v49\n"
      "s_waitcnt lgkmcnt(1)\n"
      "v_mfma_f32_16x16x32_bf16 v[64:67], a[148:151], a[204:207], v[64:67]\n"
      "ds_read_b128 a[196:199], v3 offset:33856\n"
      "v_fmac_f32 v38, s45, v50\n"
      "v_mfma_f32_16x16x32_bf16 v[68:71], a[140:143], a[204:207], v[68:71]\n"
      "v_fmac_f32_e64 v39, s45, v51\n"
      "v_fma_f32 v48, s47, v48, -v96\n"
      "v_mfma_f32_16x16x32_bf16 v[72:75], a[132:135], a[204:207], v[72:75]\n"
      "ds_read2st64_b32 a[240:241], v14 offset0:0 offset1:2\n"
      "ds_read2st64_b32 a[242:243], v17 offset0:0 offset1:2\n"
      "v_fma_f32 v49, s47, v49, -v97\n"
      "v_fma_f32 v50, s47, v50, -v98\n"
      "v_mfma_f32_16x16x32_bf16 v[52:55], a[128:131], a[208:211], v[52:55]\n"
      "ds_read_b128 a[200:203], v3 offset:33920\n"
      "v_fma_f32 v51, s47, v51, -v99\n"
      "v_mfma_f32_16x16x32_bf16 v[56:59], a[120:123], a[208:211], v[56:59]\n"
      "buffer_store_dwordx4 a[236:239], v18, s[8:11], s59 offen offset:128 sc1\n"
      "v_mfma_f32_16x16x32_bf16 v[60:63], a[112:115], a[208:211], v[60:63]\n"
      "v_cvt_pk_bf16_f32 v36, v36, v37\n"
      "v_cvt_pk_bf16_f32 v38, v38, v39\n"
      "v_mfma_f32_16x16x32_bf16 v[64:67], a[124:127], a[212:215], v[64:67]\n"
      "ds_read_b128 a[204:207], v4 offset:33792\n"
      "ds_write_addtid_b32 v36 offset:37888\n"
      "s_add_u32 s54, s54, 193536\n"
      "s_add_u32 m0, s37, 116224\n"
      "v_mfma_f32_16x16x32_bf16 v[68:71], a[116:119], a[212:215], v[68:71]\n"
      "buffer_load_dwordx4 v8, s[4:7], s54 offen offset:0 nt lds\n"
      "buffer_load_dwordx4 v9, s[4:7], s54 offen offset:1024 nt lds\n"
      "s_mov_b32 m0, s64\n"
      "s_nop 0\n"
      "ds_write_addtid_b32 v38 offset:38400\n"
      "v_mfma_f32_16x16x32_bf16 v[72:75], a[108:111], a[212:215], v[72:75]\n"
      "v_add_f32 v52, v52, v64\n"
      "v_add_f32 v53, v53, v65\n"
      "s_waitcnt lgkmcnt(3)\n"
      "v_mfma_f32_16x16x32_bf16 v[100:103], a[0:3], a[192:195], 0\n"
      "ds_read_b128 a[208:211], v4 offset:33856\n"
      "ds_write_addtid_b32 v52 offset:45312\n"
      "ds_write_addtid_b32 v53 offset:45824\n"
      "v_mfma_f32_16x16x32_bf16 v[104:107], a[4:7], a[192:195], 0\n"
      "v_add_f32 v54, v54, v66\n"
      "v_add_f32_e64 v55, v55, v67\n"
      "v_mfma_f32_16x16x32_bf16 v[108:111], a[8:11], a[192:195], 0\n"
      "ds_write_addtid_b32 v54 offset:46336\n"
      "ds_write_addtid_b32 v55 offset:46848\n"
      "v_add_f32_e64 v56, v56, v68\n"
      "v_mfma_f32_16x16x32_bf16 v[100:103], a[12:15], a[196:199], v[100:103]\n"
      "ds_read_b128 a[212:215], v4 offset:33920\n"
      "ds_write_addtid_b32 v56 offset:47360\n"
      "v_mfma_f32_16x16x32_bf16 v[104:107], a[16:19], a[196:199], v[104:107]\n"
      "v_add_f32_e64 v57, v57, v69\n"
      "v_cvt_pk_bf16_f32 v48, v48, v49\n"
      "v_mfma_f32_16x16x32_bf16 v[108:111], a[20:23], a[196:199], v[108:111]\n"
      "ds_write_addtid_b32 v48 offset:39936\n"
      "ds_write_addtid_b32 v57 offset:47872\n"
      "v_add_f32_e64 v58, v58, v70\n"
      "v_mfma_f32_16x16x32_bf16 v[100:103], a[24:27], a[200:203], v[100:103]\n"
      "ds_read_b128 a[192:195], v5 offset:33792\n"
      "ds_write_addtid_b32 v58 offset:48384\n"
      "v_mfma_f32_16x16x32_bf16 v[104:107], a[28:31], a[200:203], v[104:107]\n"
      "buffer_store_dwordx4 a[240:243], v18, s[8:11], s59 offen offset:256 sc1\n"
      "v_mfma_f32_16x16x32_bf16 v[108:111], a[32:35], a[200:203], v[108:111]\n"
      "v_add_f32 v59, v59, v71\n"
      "v_add_f32_e64 v60, v60, v72\n"
      "v_cvt_pk_bf16_f32 v50, v50, v51\n"
      "s_waitcnt lgkmcnt(2)\n"
      "v_mfma_f32_16x16x32_bf16 v[100:103], a[36:39], a[204:207], v[100:103]\n"
      "ds_read_b128 a[196:199], v5 offset:33856\n"
      "ds_write_addtid_b32 v50 offset:40448\n"
      "v_mfma_f32_16x16x32_bf16 v[104:107], a[40:43], a[204:207], v[104:107]\n"
      "ds_write_addtid_b32 v59 offset:48896\n"
      "ds_write_addtid_b32 v60 offset:49408\n"
      "v_add_f32_e64 v61, v61, v73\n"
      "v_mfma_f32_16x16x32_bf16 v[108:111], a[44:47], a[204:207], v[108:111]\n"
      "ds_write_addtid_b32 v61 offset:49920\n"
      "v_add_f32_e64 v62, v62, v74\n"
      "v_add_f32 v63, v63, v75\n"
      "v_mfma_f32_16x16x32_bf16 v[100:103], a[48:51], a[208:211], v[100:103]\n"
      "ds_read_b128 a[200:203], v5 offset:33920\n"
      "s_add_u32 s54, s54, 196608\n"
      "s_add_u32 m0, s37, 120328\n"
      "v_mfma_f32_16x16x32_bf16 v[104:107], a[52:55], a[208:211], v[104:107]\n"
      "buffer_load_dwordx4 v8, s[4:7], s54 offen offset:0 nt lds\n"
      "buffer_load_dwordx4 v9, s[4:7], s54 offen offset:1024 nt lds\n"
      "s_mov_b32 m0, s64\n"
      "v_mfma_f32_16x16x32_bf16 v[108:111], a[56:59], a[208:211], v[108:111]\n"
      "ds_write_addtid_b32 v62 offset:50432\n"
      "ds_write_addtid_b32 v63 offset:50944\n"
      "v_mfma_f32_16x16x32_bf16 v[100:103], a[60:63], a[212:215], v[100:103]\n"
      "v_fma_f32 v40, -2.0, v64, v52\n"
      "v_fma_f32 v41, -2.0, v65, v53\n"
      "v_fma_f32 v42, -2.0, v66, v54\n"
      "v_fma_f32 v43, -2.0, v67, v55\n"
      "v_fma_f32 v44, -2.0, v68, v56\n"
      "v_mfma_f32_16x16x32_bf16 v[104:107], a[64:67], a[212:215], v[104:107]\n"
      "v_fma_f32 v45, -2.0, v69, v57\n"
      "v_fma_f32 v46, -2.0, v70, v58\n"
      "v_fma_f32 v47, -2.0, v71, v59\n"
      "v_fma_f32 v48, -2.0, v72, v60\n"
      "v_mfma_f32_16x16x32_bf16 v[108:111], a[68:71], a[212:215], v[108:111]\n"
      "v_fma_f32 v49, -2.0, v73, v61\n"
      "v_fma_f32 v50, -2.0, v74, v62\n"
      "v_fma_f32 v51, -2.0, v75, v63\n"
      "s_waitcnt lgkmcnt(0)\n"
      "s_waitcnt vmcnt(14)\n"
      "s_nop 0\n"
      "s_barrier\n"
      "v_mfma_f32_16x16x32_bf16 v[100:103], a[72:75], a[192:195], v[100:103]\n"
      "ds_read_b128 a[204:207], v0 offset:16384\n"
      "ds_read2st64_b32 v[28:29], v6 offset0:0 offset1:2\n"
      "ds_read2st64_b32 v[30:31], v6 offset0:4 offset1:6\n"
      "v_mfma_f32_16x16x32_bf16 v[104:107], a[76:79], a[192:195], v[104:107]\n"
      "ds_read_b128 a[208:211], v0 offset:20480\n"
      "ds_read2st64_b32 v[32:33], v6 offset0:8 offset1:10\n"
      "v_mfma_f32_16x16x32_bf16 v[108:111], a[80:83], a[192:195], v[108:111]\n"
      "s_add_u32 s58, s58, 3072\n"
      "s_add_u32 s59, s58, 196608\n"
      "ds_read2st64_b32 v[34:35], v6 offset0:12 offset1:14\n"
      "v_mfma_f32_16x16x32_bf16 v[100:103], a[84:87], a[196:199], v[100:103]\n"
      "ds_read_b128 a[212:215], v0 offset:16448\n"
      "v_mfma_f32_16x16x32_bf16 v[104:107], a[88:91], a[196:199], v[104:107]\n"
      "ds_read2st64_b32 v[36:37], v6 offset0:16 offset1:18\n"
      "ds_read2st64_b32 v[38:39], v6 offset0:20 offset1:22\n"
      "v_mfma_f32_16x16x32_bf16 v[108:111], a[92:95], a[196:199], v[108:111]\n"
      "v_mfma_f32_16x16x32_bf16 v[100:103], a[96:99], a[200:203], v[100:103]\n"
      "ds_read_b128 a[192:195], v0 offset:20544\n"
      "s_waitcnt lgkmcnt(7)\n"
      "v_fmac_f32 v40, s46, v28\n"
      "v_fmac_f32_e64 v41, s46, v29\n"
      "v_fmac_f32_e64 v42, s46, v30\n"
      "v_fmac_f32_e64 v43, s46, v31\n"
      "v_mfma_f32_16x16x32_bf16 v[104:107], a[100:103], a[200:203], v[104:107]\n"
      "ds_read2st64_b32 a[220:221], v12 offset0:181 offset1:183\n"
      "v_fma_f32 v28, 0.5, v28, v76\n"
      "v_fma_f32 v29, 0.5, v29, v77\n"
      "v_mfma_f32_16x16x32_bf16 v[108:111], a[104:107], a[200:203], v[108:111]\n"
      "ds_read_b64_tr_b16 a[244:245], v7 offset:3584\n"
      "ds_read2st64_b32 a[222:223], v15 offset0:181 offset1:183\n"
      "v_mfma_f32_16x16x32_bf16 v[52:55], v[252:255], a[204:207], 0\n"
      "ds_read_b128 a[196:199], v0 offset:16512\n"
      "ds_read_b64_tr_b16 a[246:247], v7 offset:3592\n"
      "v_mfma_f32_16x16x32_bf16 v[56:59], v[244:247], a[204:207], 0\n"
      "v_fma_f32 v30, 0.5, v30, v78\n"
      "v_fma_f32 v31, 0.5, v31, v79\n"
      "v_fmac_f32 v40, s48, v100\n"
      "v_mfma_f32_16x16x32_bf16 v[60:63], v[236:239], a[204:207], 0\n"
      "v_fmac_f32_e64 v41, s48, v101\n"
      "v_fmac_f32_e64 v42, s48, v102\n"
      "s_waitcnt lgkmcnt(5)\n"
      "v_mfma_f32_16x16x32_bf16 v[64:67], v[248:251], a[208:211], 0\n"
      "ds_read_b128 a[200:203], v0 offset:20608\n"
      "v_fmac_f32_e64 v43, s48, v103\n"
      "v_fmac_f32_e64 v28, s45, v40\n"
      "v_mfma_f32_16x16x32_bf16 v[68:71], v[240:243], a[208:211], 0\n"
      "v_fmac_f32_e64 v29, s45, v41\n"
      "v_fmac_f32_e64 v30, s45, v42\n"
      "v_mfma_f32_16x16x32_bf16 v[72:75], v[232:235], a[208:211], 0\n"
      "v_fmac_f32_e64 v31, s45, v43\n"
      "v_fma_f32 v40, s47, v40, -v100\n"
      "v_fma_f32 v41, s47, v41, -v101\n"
      "v_mfma_f32_16x16x32_bf16 v[52:55], v[228:231], a[212:215], v[52:55]\n"
      "ds_read_b128 a[204:207], v1 offset:16384\n"
      "v_fma_f32 v42, s47, v42, -v102\n"
      "v_mfma_f32_16x16x32_bf16 v[56:59], v[220:223], a[212:215], v[56:59]\n"
      "v_fma_f32 v43, s47, v43, -v103\n"
      "v_cvt_pk_bf16_f32 v28, v28, v29\n"
      "v_mfma_f32_16x16x32_bf16 v[60:63], v[212:215], a[212:215], v[60:63]\n"
      "ds_write_addtid_b32 v28 offset:32768\n"
      "ds_read2st64_b32 a[224:225], v13 offset0:181 offset1:183\n"
      "ds_read2st64_b32 a[226:227], v16 offset0:181 offset1:183\n"
      "v_mfma_f32_16x16x32_bf16 v[64:67], v[224:227], a[192:195], v[64:67]\n"
      "ds_read_b128 a[208:211], v1 offset:20480\n"
      "v_cvt_pk_bf16_f32 v30, v30, v31\n"
      "v_mfma_f32_16x16x32_bf16 v[68:71], v[216:219], a[192:195], v[68:71]\n"
      "ds_write_addtid_b32 v30 offset:33280\n"
      "v_cvt_pk_bf16_f32 v40, v40, v41\n"
      "v_cvt_pk_bf16_f32 v42, v42, v43\n"
      "s_waitcnt lgkmcnt(5)\n"
      "v_mfma_f32_4x4x4_16b_bf16 v[20:23], a[216:217], a[244:245], 0\n"
      "ds_write_addtid_b32 v40 offset:34816\n"
      "v_mfma_f32_4x4x4_16b_bf16 v[24:27], a[216:217], a[246:247], 0\n"
      "ds_write_addtid_b32 v42 offset:35328\n"
      "v_fmac_f32 v44, s46, v32\n"
      "v_fmac_f32 v45, s46, v33\n"
      "v_mfma_f32_16x16x32_bf16 v[72:75], v[208:211], a[192:195], v[72:75]\n"
      "v_fmac_f32 v46, s46, v34\n"
      "v_cvt_pk_bf16_f32 v20, v20, v24\n"
      "v_mfma_f32_16x16x32_bf16 v[52:55], v[204:207], a[196:199], v[52:55]\n"
      "buffer_store_dwordx4 a[220:223], v18, s[8:11], s58 offen offset:0 sc1\n"
      "ds_read_b128 a[212:215], v1 offset:16448\n"
      "v_mfma_f32_16x16x32_bf16 v[56:59], v[196:199], a[196:199], v[56:59]\n"
      "s_add_u32 s54, s54, -193536\n"
      "s_add_u32 m0, s37, 133120\n"
      "v_mfma_f32_16x16x32_bf16 v[60:63], v[188:191], a[196:199], v[60:63]\n"
      "buffer_load_dwordx4 v10, s[4:7], s54 offen offset:0 nt lds\n"
      "buffer_load_dwordx4 v11, s[4:7], s54 offen offset:1024 nt lds\n"
      "s_mov_b32 m0, s64\n"
      "v_mfma_f32_16x16x32_bf16 v[64:67], v[200:203], a[200:203], v[64:67]\n"
      "ds_read_b128 a[192:195], v1 offset:20544\n"
      "ds_write_addtid_b32 v20 offset:3584\n"
      "v_mfma_f32_16x16x32_bf16 v[68:71], v[192:195], a[200:203], v[68:71]\n"
      "v_cvt_pk_bf16_f32 v21, v21, v25\n"
      "v_fmac_f32 v47, s46, v35\n"
      "v_cvt_pk_bf16_f32 v22, v22, v26\n"
      "v_mfma_f32_16x16x32_bf16 v[72:75], v[184:187], a[200:203], v[72:75]\n"
      "ds_write_addtid_b32 v21 offset:7680\n"
      "ds_read2st64_b32 a[228:229], v14 offset0:181 offset1:183\n"
      "v_mfma_f32_16x16x32_bf16 v[52:55], v[180:183], a[204:207], v[52:55]\n"
      "ds_read_b128 a[196:199], v1 offset:16512\n"
      "ds_write_addtid_b32 v22 offset:11776\n"
      "v_mfma_f32_16x16x32_bf16 v[56:59], v[172:175], a[204:207], v[56:59]\n"
      "ds_read2st64_b32 a[230:231], v17 offset0:181 offset1:183\n"
      "v_cvt_pk_bf16_f32 v23, v23, v27\n"
      "v_fma_f32 v32, 0.5, v32, v80\n"
      "v_mfma_f32_16x16x32_bf16 v[60:63], v[164:167], a[204:207], v[60:63]\n"
      "ds_write_addtid_b32 v23 offset:15872\n"
      "v_fma_f32 v33, 0.5, v33, v81\n"
      "s_nop 0\n"
      "s_waitcnt lgkmcnt(6)\n"
      "v_mfma_f32_16x16x32_bf16 v[64:67], v[176:179], a[208:211], v[64:67]\n"
      "ds_read_b128 a[200:203], v1 offset:20608\n"
      "v_fma_f32 v34, 0.5, v34, v82\n"
      "v_fma_f32 v35, 0.5, v35, v83\n"
      "v_mfma_f32_16x16x32_bf16 v[68:71], v[168:171], a[208:211], v[68:71]\n"
      "buffer_store_dwordx4 a[224:227], v18, s[8:11], s58 offen offset:128 sc1\n"
      "v_mfma_f32_16x16x32_bf16 v[72:75], v[160:163], a[208:211], v[72:75]\n"
      "v_fmac_f32 v44, s48, v104\n"
      "v_fmac_f32 v45, s48, v105\n"
      "v_mfma_f32_16x16x32_bf16 v[52:55], v[156:159], a[212:215], v[52:55]\n"
      "ds_read_b128 a[204:207], v2 offset:16384\n"
      "v_fmac_f32_e64 v46, s48, v106\n"
      "v_mfma_f32_16x16x32_bf16 v[56:59], v[148:151], a[212:215], v[56:59]\n"
      "ds_read2st64_b32 a[232:233], v12 offset0:4 offset1:6\n"
      "ds_read2st64_b32 a[234:235], v15 offset0:4 offset1:6\n"
      "v_fmac_f32_e64 v47, s48, v107\n"
      "v_mfma_f32_16x16x32_bf16 v[60:63], v[140:143], a[212:215], v[60:63]\n"
      "s_add_u32 s54, s54, 196608\n"
      "s_add_u32 m0, s37, 137224\n"
      "v_mfma_f32_16x16x32_bf16 v[64:67], v[152:155], a[192:195], v[64:67]\n"
      "buffer_load_dwordx4 v10, s[4:7], s54 offen offset:0 nt lds\n"
      "buffer_load_dwordx4 v11, s[4:7], s54 offen offset:1024 nt lds\n"
      "s_mov_b32 m0, s64\n"
      "ds_read_b128 a[208:211], v2 offset:20480\n"
      "v_mfma_f32_16x16x32_bf16 v[68:71], v[144:147], a[192:195], v[68:71]\n"
      "v_fmac_f32 v32, s45, v44\n"
      "v_fmac_f32_e64 v33, s45, v45\n"
      "v_fmac_f32_e64 v34, s45, v46\n"
      "v_mfma_f32_16x16x32_bf16 v[72:75], v[136:139], a[192:195], v[72:75]\n"
      "v_fmac_f32 v35, s45, v47\n"
      "v_fma_f32 v44, s47, v44, -v104\n"
      "s_waitcnt lgkmcnt(1)\n"
      "v_mfma_f32_16x16x32_bf16 v[52:55], v[132:135], a[196:199], v[52:55]\n"
      "ds_read_b128 a[212:215], v2 offset:16448\n"
      "v_fma_f32 v45, s47, v45, -v105\n"
      "v_mfma_f32_16x16x32_bf16 v[56:59], v[124:127], a[196:199], v[56:59]\n"
      "buffer_store_dwordx4 a[228:231], v18, s[8:11], s58 offen offset:256 sc1\n"
      "v_mfma_f32_16x16x32_bf16 v[60:63], a[184:187], a[196:199], v[60:63]\n"
      "v_fma_f32 v46, s47, v46, -v106\n"
      "v_fma_f32 v47, s47, v47, -v107\n"
      "v_mfma_f32_16x16x32_bf16 v[64:67], v[128:131], a[200:203], v[64:67]\n"
      "ds_read_b128 a[192:195], v2 offset:20544\n"
      "v_cvt_pk_bf16_f32 v32, v32, v33\n"
      "v_mfma_f32_16x16x32_bf16 v[68:71], a[188:191], a[200:203], v[68:71]\n"
      "ds_write_addtid_b32 v32 offset:40964\n"
      "v_cvt_pk_bf16_f32 v34, v34, v35\n"
      "v_cvt_pk_bf16_f32 v44, v44, v45\n"
      "v_mfma_f32_16x16x32_bf16 v[72:75], a[180:183], a[200:203], v[72:75]\n"
      "ds_write_addtid_b32 v34 offset:41476\n"
      "ds_write_addtid_b32 v44 offset:43012\n"
      "ds_read2st64_b32 a[236:237], v13 offset0:4 offset1:6\n"
      "v_cvt_pk_bf16_f32 v46, v46, v47\n"
      "v_mfma_f32_16x16x32_bf16 v[52:55], a[176:179], a[204:207], v[52:55]\n"
      "ds_read_b128 a[196:199], v2 offset:16512\n"
      "ds_write_addtid_b32 v46 offset:43524\n"
      "v_mfma_f32_16x16x32_bf16 v[56:59], a[168:171], a[204:207], v[56:59]\n"
      "ds_read2st64_b32 a[238:239], v16 offset0:4 offset1:6\n"
      "v_fmac_f32 v48, s46, v36\n"
      "v_mfma_f32_16x16x32_bf16 v[60:63], a[160:163], a[204:207], v[60:63]\n"
      "v_fmac_f32_e64 v49, s46, v37\n"
      "v_fmac_f32_e64 v50, s46, v38\n"
      "s_waitcnt lgkmcnt(6)\n"
      "v_mfma_f32_16x16x32_bf16 v[64:67], a[172:175], a[208:211], v[64:67]\n"
      "buffer_store_dwordx4 a[232:235], v18, s[8:11], s59 offen offset:0 sc1\n"
      "ds_read_b128 a[200:203], v2 offset:20608\n"
      "v_mfma_f32_16x16x32_bf16 v[68:71], a[164:167], a[208:211], v[68:71]\n"
      "v_fmac_f32_e64 v51, s46, v39\n"
      "v_fma_f32 v36, 0.5, v36, v84\n"
      "v_fma_f32 v37, 0.5, v37, v85\n"
      "v_mfma_f32_16x16x32_bf16 v[72:75], a[156:159], a[208:211], v[72:75]\n"
      "v_fma_f32 v38, 0.5, v38, v86\n"
      "v_fma_f32 v39, 0.5, v39, v87\n"
      "v_fmac_f32_e64 v48, s48, v108\n"
      "v_mfma_f32_16x16x32_bf16 v[52:55], a[152:155], a[212:215], v[52:55]\n"
      "ds_read_b128 a[204:207], v3 offset:50688\n"
      "v_fmac_f32_e64 v49, s48, v109\n"
      "v_mfma_f32_16x16x32_bf16 v[56:59], a[144:147], a[212:215], v[56:59]\n"
      "v_fmac_f32_e64 v50, s48, v110\n"
      "v_fmac_f32_e64 v51, s48, v111\n"
      "v_mfma_f32_16x16x32_bf16 v[60:63], a[136:139], a[212:215], v[60:63]\n"
      "v_fmac_f32_e64 v36, s45, v48\n"
      "v_fmac_f32_e64 v37, s45, v49\n"
      "v_mfma_f32_16x16x32_bf16 v[64:67], a[148:151], a[192:195], v[64:67]\n"
      "ds_read_b128 a[208:211], v3 offset:50752\n"
      "v_fmac_f32_e64 v38, s45, v50\n"
      "v_mfma_f32_16x16x32_bf16 v[68:71], a[140:143], a[192:195], v[68:71]\n"
      "v_fmac_f32 v39, s45, v51\n"
      "v_fma_f32 v48, s47, v48, -v108\n"
      "v_mfma_f32_16x16x32_bf16 v[72:75], a[132:135], a[192:195], v[72:75]\n"
      "ds_read2st64_b32 a[240:241], v14 offset0:4 offset1:6\n"
      "ds_read2st64_b32 a[242:243], v17 offset0:4 offset1:6\n"
      "v_fma_f32 v49, s47, v49, -v109\n"
      "v_fma_f32 v50, s47, v50, -v110\n"
      "s_waitcnt lgkmcnt(3)\n"
      "v_mfma_f32_16x16x32_bf16 v[52:55], a[128:131], a[196:199], v[52:55]\n"
      "ds_read_b128 a[212:215], v3 offset:50816\n"
      "v_fma_f32 v51, s47, v51, -v111\n"
      "v_mfma_f32_16x16x32_bf16 v[56:59], a[120:123], a[196:199], v[56:59]\n"
      "buffer_store_dwordx4 a[236:239], v18, s[8:11], s59 offen offset:128 sc1\n"
      "v_mfma_f32_16x16x32_bf16 v[60:63], a[112:115], a[196:199], v[60:63]\n"
      "v_cvt_pk_bf16_f32 v36, v36, v37\n"
      "v_cvt_pk_bf16_f32 v38, v38, v39\n"
      "v_mfma_f32_16x16x32_bf16 v[64:67], a[124:127], a[200:203], v[64:67]\n"
      "ds_read_b128 a[192:195], v4 offset:50688\n"
      "ds_write_addtid_b32 v36 offset:36864\n"
      "ds_write_addtid_b32 v38 offset:37376\n"
      "s_add_u32 s54, s54, 193536\n"
      "s_add_u32 m0, s37, 124552\n"
      "v_mfma_f32_16x16x32_bf16 v[68:71], a[116:119], a[200:203], v[68:71]\n"
      "buffer_load_dwordx4 v8, s[4:7], s54 offen offset:0 nt lds\n"
      "buffer_load_dwordx4 v9, s[4:7], s54 offen offset:1024 nt lds\n"
      "s_mov_b32 m0, s64\n"
      "v_mfma_f32_16x16x32_bf16 v[72:75], a[108:111], a[200:203], v[72:75]\n"
      "v_add_f32 v52, v52, v64\n"
      "v_add_f32 v53, v53, v65\n"
      "v_mfma_f32_16x16x32_bf16 v[112:115], a[0:3], a[204:207], 0\n"
      "ds_read_b128 a[196:199], v4 offset:50752\n"
      "ds_write_addtid_b32 v52 offset:51456\n"
      "ds_write_addtid_b32 v53 offset:51968\n"
      "v_mfma_f32_16x16x32_bf16 v[116:119], a[4:7], a[204:207], 0\n"
      "v_add_f32 v54, v54, v66\n"
      "v_add_f32 v55, v55, v67\n"
      "v_mfma_f32_16x16x32_bf16 v[120:123], a[8:11], a[204:207], 0\n"
      "ds_write_addtid_b32 v54 offset:52480\n"
      "ds_write_addtid_b32 v55 offset:52992\n"
      "v_add_f32_e64 v56, v56, v68\n"
      "s_waitcnt lgkmcnt(5)\n"
      "v_mfma_f32_16x16x32_bf16 v[112:115], a[12:15], a[208:211], v[112:115]\n"
      "ds_read_b128 a[200:203], v4 offset:50816\n"
      "ds_write_addtid_b32 v56 offset:53504\n"
      "v_mfma_f32_16x16x32_bf16 v[116:119], a[16:19], a[208:211], v[116:119]\n"
      "v_add_f32 v57, v57, v69\n"
      "v_cvt_pk_bf16_f32 v48, v48, v49\n"
      "v_add_f32 v58, v58, v70\n"
      "v_mfma_f32_16x16x32_bf16 v[120:123], a[20:23], a[208:211], v[120:123]\n"
      "ds_write_addtid_b32 v48 offset:38912\n"
      "ds_write_addtid_b32 v57 offset:54016\n"
      "v_mfma_f32_16x16x32_bf16 v[112:115], a[24:27], a[212:215], v[112:115]\n"
      "ds_read_b128 a[204:207], v5 offset:50688\n"
      "ds_write_addtid_b32 v58 offset:54528\n"
      "v_mfma_f32_16x16x32_bf16 v[116:119], a[28:31], a[212:215], v[116:119]\n"
      "buffer_store_dwordx4 a[240:243], v18, s[8:11], s59 offen offset:256 sc1\n"
      "v_mfma_f32_16x16x32_bf16 v[120:123], a[32:35], a[212:215], v[120:123]\n"
      "v_add_f32 v59, v59, v71\n"
      "v_add_f32 v60, v60, v72\n"
      "v_cvt_pk_bf16_f32 v50, v50, v51\n"
      "v_mfma_f32_16x16x32_bf16 v[112:115], a[36:39], a[192:195], v[112:115]\n"
      "ds_read_b128 a[208:211], v5 offset:50752\n"
      "ds_write_addtid_b32 v50 offset:39424\n"
      "v_mfma_f32_16x16x32_bf16 v[116:119], a[40:43], a[192:195], v[116:119]\n"
      "ds_write_addtid_b32 v59 offset:55040\n"
      "ds_write_addtid_b32 v60 offset:55552\n"
      "v_add_f32 v61, v61, v73\n"
      "v_mfma_f32_16x16x32_bf16 v[120:123], a[44:47], a[192:195], v[120:123]\n"
      "ds_write_addtid_b32 v61 offset:56064\n"
      "v_add_f32 v62, v62, v74\n"
      "v_add_f32_e64 v63, v63, v75\n"
      "s_waitcnt lgkmcnt(5)\n"
      "v_mfma_f32_16x16x32_bf16 v[112:115], a[48:51], a[196:199], v[112:115]\n"
      "ds_read_b128 a[212:215], v5 offset:50816\n"
      "s_add_u32 s54, s54, 196608\n"
      "s_add_u32 m0, s37, 128896\n"
      "v_mfma_f32_16x16x32_bf16 v[116:119], a[52:55], a[196:199], v[116:119]\n"
      "buffer_load_dwordx4 v8, s[4:7], s54 offen offset:0 nt lds\n"
      "buffer_load_dwordx4 v9, s[4:7], s54 offen offset:1024 nt lds\n"
      "s_mov_b32 m0, s64\n"
      "v_mfma_f32_16x16x32_bf16 v[120:123], a[56:59], a[196:199], v[120:123]\n"
      "ds_write_addtid_b32 v62 offset:56576\n"
      "ds_write_addtid_b32 v63 offset:57088\n"
      "v_mfma_f32_16x16x32_bf16 v[112:115], a[60:63], a[200:203], v[112:115]\n"
      "v_fma_f32 v40, -2.0, v64, v52\n"
      "v_fma_f32 v41, -2.0, v65, v53\n"
      "v_fma_f32 v42, -2.0, v66, v54\n"
      "v_fma_f32 v43, -2.0, v67, v55\n"
      "v_fma_f32 v44, -2.0, v68, v56\n"
      "v_mfma_f32_16x16x32_bf16 v[116:119], a[64:67], a[200:203], v[116:119]\n"
      "v_fma_f32 v45, -2.0, v69, v57\n"
      "v_fma_f32 v46, -2.0, v70, v58\n"
      "v_fma_f32 v47, -2.0, v71, v59\n"
      "v_fma_f32 v48, -2.0, v72, v60\n"
      "v_mfma_f32_16x16x32_bf16 v[120:123], a[68:71], a[200:203], v[120:123]\n"
      "v_fma_f32 v49, -2.0, v73, v61\n"
      "v_fma_f32 v50, -2.0, v74, v62\n"
      "v_fma_f32 v51, -2.0, v75, v63\n"
      "s_waitcnt lgkmcnt(0)\n"
      "s_waitcnt vmcnt(14)\n"
      "s_barrier\n"
      "v_mfma_f32_16x16x32_bf16 v[112:115], a[72:75], a[204:207], v[112:115]\n"
      "ds_read_b128 a[192:195], v0 offset:0\n"
      "ds_read2st64_b32 v[28:29], v6 offset0:24 offset1:26\n"
      "ds_read2st64_b32 v[30:31], v6 offset0:28 offset1:30\n"
      "v_mfma_f32_16x16x32_bf16 v[116:119], a[76:79], a[204:207], v[116:119]\n"
      "ds_read_b128 a[196:199], v0 offset:4096\n"
      "ds_read2st64_b32 v[32:33], v6 offset0:32 offset1:34\n"
      "v_mfma_f32_16x16x32_bf16 v[120:123], a[80:83], a[204:207], v[120:123]\n"
      "s_add_u32 s62, s62, 786432\n"
      "s_mov_b32 s58, s62\n"
      "ds_read2st64_b32 v[34:35], v6 offset0:36 offset1:38\n"
      "v_mfma_f32_16x16x32_bf16 v[112:115], a[84:87], a[208:211], v[112:115]\n"
      "ds_read_b128 a[200:203], v0 offset:64\n"
      "s_add_u32 s59, s58, 196608\n"
      "v_mfma_f32_16x16x32_bf16 v[116:119], a[88:91], a[208:211], v[116:119]\n"
      "ds_read2st64_b32 v[36:37], v6 offset0:40 offset1:42\n"
      "ds_read2st64_b32 v[38:39], v6 offset0:44 offset1:46\n"
      "v_mfma_f32_16x16x32_bf16 v[120:123], a[92:95], a[208:211], v[120:123]\n"
      "v_mfma_f32_16x16x32_bf16 v[112:115], a[96:99], a[212:215], v[112:115]\n"
      "ds_read_b128 a[204:207], v0 offset:4160\n"
      "s_waitcnt lgkmcnt(7)\n"
      "v_fmac_f32 v40, s46, v28\n"
      "v_fmac_f32 v41, s46, v29\n"
      "v_fmac_f32 v42, s46, v30\n"
      "v_mfma_f32_16x16x32_bf16 v[116:119], a[100:103], a[212:215], v[116:119]\n"
      "ds_read2st64_b32 a[220:221], v12 offset0:177 offset1:179\n"
      "ds_read2st64_b32 a[222:223], v15 offset0:177 offset1:179\n"
      "v_fmac_f32_e64 v43, s46, v31\n"
      "v_mfma_f32_16x16x32_bf16 v[120:123], a[104:107], a[212:215], v[120:123]\n"
      "ds_read_b64_tr_b16 a[244:245], v7 offset:20480\n"
      "ds_read_b64_tr_b16 a[246:247], v7 offset:20488\n"
      "v_mfma_f32_16x16x32_bf16 v[52:55], v[252:255], a[192:195], 0\n"
      "ds_read_b128 a[208:211], v0 offset:128\n"
      "v_fma_f32 v28, 0.5, v28, v88\n"
      "v_mfma_f32_16x16x32_bf16 v[56:59], v[244:247], a[192:195], 0\n"
      "v_fma_f32 v29, 0.5, v29, v89\n"
      "v_fma_f32 v30, 0.5, v30, v90\n"
      "v_fma_f32 v31, 0.5, v31, v91\n"
      "v_mfma_f32_16x16x32_bf16 v[60:63], v[236:239], a[192:195], 0\n"
      "v_fmac_f32 v40, s48, v112\n"
      "v_fmac_f32 v41, s48, v113\n"
      "s_waitcnt lgkmcnt(6)\n"
      "v_mfma_f32_16x16x32_bf16 v[64:67], v[248:251], a[196:199], 0\n"
      "ds_read_b128 a[212:215], v0 offset:4224\n"
      "v_fmac_f32 v42, s48, v114\n"
      "v_fmac_f32_e64 v43, s48, v115\n"
      "v_mfma_f32_16x16x32_bf16 v[68:71], v[240:243], a[196:199], 0\n"
      "v_fmac_f32_e64 v28, s45, v40\n"
      "v_fmac_f32_e64 v29, s45, v41\n"
      "v_mfma_f32_16x16x32_bf16 v[72:75], v[232:235], a[196:199], 0\n"
      "v_fmac_f32_e64 v30, s45, v42\n"
      "v_fmac_f32_e64 v31, s45, v43\n"
      "v_fma_f32 v40, s47, v40, -v112\n"
      "v_mfma_f32_16x16x32_bf16 v[52:55], v[228:231], a[200:203], v[52:55]\n"
      "ds_read_b128 a[192:195], v1 offset:0\n"
      "v_fma_f32 v41, s47, v41, -v113\n"
      "v_mfma_f32_16x16x32_bf16 v[56:59], v[220:223], a[200:203], v[56:59]\n"
      "v_fma_f32 v42, s47, v42, -v114\n"
      "v_fma_f32 v43, s47, v43, -v115\n"
      "v_mfma_f32_16x16x32_bf16 v[60:63], v[212:215], a[200:203], v[60:63]\n"
      "ds_read2st64_b32 a[224:225], v13 offset0:177 offset1:179\n"
      "ds_read2st64_b32 a[226:227], v16 offset0:177 offset1:179\n"
      "v_cvt_pk_bf16_f32 v28, v28, v29\n"
      "s_nop 0\n"
      "s_waitcnt lgkmcnt(3)\n"
      "v_mfma_f32_16x16x32_bf16 v[64:67], v[224:227], a[204:207], v[64:67]\n"
      "ds_read_b128 a[196:199], v1 offset:4096\n"
      "ds_write_addtid_b32 v28 offset:33792\n"
      "v_mfma_f32_16x16x32_bf16 v[68:71], v[216:219], a[204:207], v[68:71]\n"
      "v_cvt_pk_bf16_f32 v30, v30, v31\n"
      "v_cvt_pk_bf16_f32 v40, v40, v41\n"
      "v_cvt_pk_bf16_f32 v42, v42, v43\n"
      "v_mfma_f32_4x4x4_16b_bf16 v[20:23], a[216:217], a[244:245], 0\n"
      "v_mfma_f32_4x4x4_16b_bf16 v[24:27], a[216:217], a[246:247], 0\n"
      "ds_write_addtid_b32 v30 offset:34304\n"
      "ds_write_addtid_b32 v40 offset:35840\n"
      "ds_write_addtid_b32 v42 offset:36352\n"
      "v_fmac_f32_e64 v44, s46, v32\n"
      "v_mfma_f32_16x16x32_bf16 v[72:75], v[208:211], a[204:207], v[72:75]\n"
      "v_cvt_pk_bf16_f32 v20, v20, v24\n"
      "v_fmac_f32_e64 v45, s46, v33\n"
      "v_mfma_f32_16x16x32_bf16 v[52:55], v[204:207], a[208:211], v[52:55]\n"
      "buffer_store_dwordx4 a[220:223], v18, s[8:11], s58 offen offset:0 sc1\n"
      "ds_read_b128 a[200:203], v1 offset:64\n"
      "v_mfma_f32_16x16x32_bf16 v[56:59], v[196:199], a[208:211], v[56:59]\n"
      "s_add_u32 s54, s54, -193536\n"
      "s_add_u32 m0, s37, 141448\n"
      "v_mfma_f32_16x16x32_bf16 v[60:63], v[188:191], a[208:211], v[60:63]\n"
      "buffer_load_dwordx4 v10, s[4:7], s54 offen offset:0 nt lds\n"
      "buffer_load_dwordx4 v11, s[4:7], s54 offen offset:1024 nt lds\n"
      "s_mov_b32 m0, s64\n"
      "v_mfma_f32_16x16x32_bf16 v[64:67], v[200:203], a[212:215], v[64:67]\n"
      "ds_read_b128 a[204:207], v1 offset:4160\n"
      "ds_write_addtid_b32 v20 offset:19968\n"
      "v_mfma_f32_16x16x32_bf16 v[68:71], v[192:195], a[212:215], v[68:71]\n"
      "v_cvt_pk_bf16_f32 v21, v21, v25\n"
      "v_fmac_f32 v46, s46, v34\n"
      "v_cvt_pk_bf16_f32 v22, v22, v26\n"
      "v_mfma_f32_16x16x32_bf16 v[72:75], v[184:187], a[212:215], v[72:75]\n"
      "ds_write_addtid_b32 v21 offset:24064\n"
      "ds_read2st64_b32 a[228:229], v14 offset0:177 offset1:179\n"
      "s_nop 0\n"
      "s_waitcnt lgkmcnt(4)\n"
      "v_mfma_f32_16x16x32_bf16 v[52:55], v[180:183], a[192:195], v[52:55]\n"
      "ds_read_b128 a[208:211], v1 offset:128\n"
      "ds_write_addtid_b32 v22 offset:28160\n"
      "ds_read2st64_b32 a[230:231], v17 offset0:177 offset1:179\n"
      "v_mfma_f32_16x16x32_bf16 v[56:59], v[172:175], a[192:195], v[56:59]\n"
      "v_cvt_pk_bf16_f32 v23, v23, v27\n"
      "v_fmac_f32_e64 v47, s46, v35\n"
      "v_mfma_f32_16x16x32_bf16 v[60:63], v[164:167], a[192:195], v[60:63]\n"
      "ds_write_addtid_b32 v23 offset:32256\n"
      "v_fma_f32 v32, 0.5, v32, v92\n"
      "v_fma_f32 v33, 0.5, v33, v93\n"
      "v_mfma_f32_16x16x32_bf16 v[64:67], v[176:179], a[196:199], v[64:67]\n"
      "ds_read_b128 a[212:215], v1 offset:4224\n"
      "v_fma_f32 v34, 0.5, v34, v94\n"
      "v_mfma_f32_16x16x32_bf16 v[68:71], v[168:171], a[196:199], v[68:71]\n"
      "buffer_store_dwordx4 a[224:227], v18, s[8:11], s58 offen offset:128 sc1\n"
      "v_mfma_f32_16x16x32_bf16 v[72:75], v[160:163], a[196:199], v[72:75]\n"
      "v_fma_f32 v35, 0.5, v35, v95\n"
      "v_fmac_f32 v44, s48, v116\n"
      "v_fmac_f32 v45, s48, v117\n"
      "v_mfma_f32_16x16x32_bf16 v[52:55], v[156:159], a[200:203], v[52:55]\n"
      "ds_read_b128 a[192:195], v2 offset:0\n"
      "v_fmac_f32_e64 v46, s48, v118\n"
      "v_mfma_f32_16x16x32_bf16 v[56:59], v[148:151], a[200:203], v[56:59]\n"
      "ds_read2st64_b32 a[232:233], v12 offset0:0 offset1:2\n"
      "ds_read2st64_b32 a[234:235], v15 offset0:0 offset1:2\n"
      "v_fmac_f32_e64 v47, s48, v119\n"
      "v_mfma_f32_16x16x32_bf16 v[60:63], v[140:143], a[200:203], v[60:63]\n"
      "s_add_u32 s54, s54, 196608\n"
      "s_add_u32 m0, s37, 145792\n"
      "s_waitcnt lgkmcnt(3)\n"
      "v_mfma_f32_16x16x32_bf16 v[64:67], v[152:155], a[204:207], v[64:67]\n"
      "buffer_load_dwordx4 v10, s[4:7], s54 offen offset:0 nt lds\n"
      "buffer_load_dwordx4 v11, s[4:7], s54 offen offset:1024 nt lds\n"
      "s_mov_b32 m0, s64\n"
      "ds_read_b128 a[196:199], v2 offset:4096\n"
      "v_mfma_f32_16x16x32_bf16 v[68:71], v[144:147], a[204:207], v[68:71]\n"
      "v_fmac_f32_e64 v32, s45, v44\n"
      "v_fmac_f32_e64 v33, s45, v45\n"
      "v_fmac_f32_e64 v34, s45, v46\n"
      "v_mfma_f32_16x16x32_bf16 v[72:75], v[136:139], a[204:207], v[72:75]\n"
      "v_fmac_f32_e64 v35, s45, v47\n"
      "v_fma_f32 v44, s47, v44, -v116\n"
      "v_mfma_f32_16x16x32_bf16 v[52:55], v[132:135], a[208:211], v[52:55]\n"
      "ds_read_b128 a[200:203], v2 offset:64\n"
      "v_fma_f32 v45, s47, v45, -v117\n"
      "v_mfma_f32_16x16x32_bf16 v[56:59], v[124:127], a[208:211], v[56:59]\n"
      "buffer_store_dwordx4 a[228:231], v18, s[8:11], s58 offen offset:256 sc1\n"
      "v_mfma_f32_16x16x32_bf16 v[60:63], a[184:187], a[208:211], v[60:63]\n"
      "v_fma_f32 v46, s47, v46, -v118\n"
      "v_fma_f32 v47, s47, v47, -v119\n"
      "v_mfma_f32_16x16x32_bf16 v[64:67], v[128:131], a[212:215], v[64:67]\n"
      "ds_read_b128 a[204:207], v2 offset:4160\n"
      "v_cvt_pk_bf16_f32 v32, v32, v33\n"
      "v_mfma_f32_16x16x32_bf16 v[68:71], a[188:191], a[212:215], v[68:71]\n"
      "ds_write_addtid_b32 v32 offset:41988\n"
      "v_cvt_pk_bf16_f32 v34, v34, v35\n"
      "v_cvt_pk_bf16_f32 v44, v44, v45\n"
      "v_mfma_f32_16x16x32_bf16 v[72:75], a[180:183], a[212:215], v[72:75]\n"
      "ds_write_addtid_b32 v34 offset:42500\n"
      "ds_write_addtid_b32 v44 offset:44036\n"
      "ds_read2st64_b32 a[236:237], v13 offset0:0 offset1:2\n"
      "v_cvt_pk_bf16_f32 v46, v46, v47\n"
      "s_waitcnt lgkmcnt(5)\n"
      "v_mfma_f32_16x16x32_bf16 v[52:55], a[176:179], a[192:195], v[52:55]\n"
      "ds_read_b128 a[208:211], v2 offset:128\n"
      "ds_write_addtid_b32 v46 offset:44548\n"
      "v_mfma_f32_16x16x32_bf16 v[56:59], a[168:171], a[192:195], v[56:59]\n"
      "ds_read2st64_b32 a[238:239], v16 offset0:0 offset1:2\n"
      "v_fmac_f32 v48, s46, v36\n"
      "v_mfma_f32_16x16x32_bf16 v[60:63], a[160:163], a[192:195], v[60:63]\n"
      "v_fmac_f32 v49, s46, v37\n"
      "v_fmac_f32 v50, s46, v38\n"
      "v_mfma_f32_16x16x32_bf16 v[64:67], a[172:175], a[196:199], v[64:67]\n"
      "buffer_store_dwordx4 a[232:235], v18, s[8:11], s59 offen offset:0 sc1\n"
      "ds_read_b128 a[212:215], v2 offset:4224\n"
      "v_mfma_f32_16x16x32_bf16 v[68:71], a[164:167], a[196:199], v[68:71]\n"
      "v_fmac_f32_e64 v51, s46, v39\n"
      "v_fma_f32 v36, 0.5, v36, v96\n"
      "v_fma_f32 v37, 0.5, v37, v97\n"
      "v_mfma_f32_16x16x32_bf16 v[72:75], a[156:159], a[196:199], v[72:75]\n"
      "v_fma_f32 v38, 0.5, v38, v98\n"
      "v_fma_f32 v39, 0.5, v39, v99\n"
      "v_mfma_f32_16x16x32_bf16 v[52:55], a[152:155], a[200:203], v[52:55]\n"
      "ds_read_b128 a[192:195], v3 offset:0\n"
      "v_fmac_f32 v48, s48, v120\n"
      "v_fmac_f32 v49, s48, v121\n"
      "v_mfma_f32_16x16x32_bf16 v[56:59], a[144:147], a[200:203], v[56:59]\n"
      "v_fmac_f32 v50, s48, v122\n"
      "v_fmac_f32 v51, s48, v123\n"
      "v_mfma_f32_16x16x32_bf16 v[60:63], a[136:139], a[200:203], v[60:63]\n"
      "v_fmac_f32 v36, s45, v48\n"
      "v_fmac_f32 v37, s45, v49\n"
      "s_waitcnt lgkmcnt(1)\n"
      "v_mfma_f32_16x16x32_bf16 v[64:67], a[148:151], a[204:207], v[64:67]\n"
      "ds_read_b128 a[196:199], v3 offset:64\n"
      "v_fmac_f32 v38, s45, v50\n"
      "v_mfma_f32_16x16x32_bf16 v[68:71], a[140:143], a[204:207], v[68:71]\n"
      "v_fmac_f32_e64 v39, s45, v51\n"
      "v_fma_f32 v48, s47, v48, -v120\n"
      "v_mfma_f32_16x16x32_bf16 v[72:75], a[132:135], a[204:207], v[72:75]\n"
      "ds_read2st64_b32 a[240:241], v14 offset0:0 offset1:2\n"
      "ds_read2st64_b32 a[242:243], v17 offset0:0 offset1:2\n"
      "v_fma_f32 v49, s47, v49, -v121\n"
      "v_fma_f32 v50, s47, v50, -v122\n"
      "v_mfma_f32_16x16x32_bf16 v[52:55], a[128:131], a[208:211], v[52:55]\n"
      "ds_read_b128 a[200:203], v3 offset:128\n"
      "v_fma_f32 v51, s47, v51, -v123\n"
      "v_mfma_f32_16x16x32_bf16 v[56:59], a[120:123], a[208:211], v[56:59]\n"
      "buffer_store_dwordx4 a[236:239], v18, s[8:11], s59 offen offset:128 sc1\n"
      "v_mfma_f32_16x16x32_bf16 v[60:63], a[112:115], a[208:211], v[60:63]\n"
      "v_cvt_pk_bf16_f32 v36, v36, v37\n"
      "v_cvt_pk_bf16_f32 v38, v38, v39\n"
      "v_mfma_f32_16x16x32_bf16 v[64:67], a[124:127], a[212:215], v[64:67]\n"
      "ds_read_b128 a[204:207], v4 offset:0\n"
      "ds_write_addtid_b32 v36 offset:37888\n"
      "s_add_u32 s54, s54, 193536\n"
      "s_add_u32 m0, s37, 82432\n"
      "v_mfma_f32_16x16x32_bf16 v[68:71], a[116:119], a[212:215], v[68:71]\n"
      "buffer_load_dwordx4 v8, s[4:7], s54 offen offset:0 nt lds\n"
      "buffer_load_dwordx4 v9, s[4:7], s54 offen offset:1024 nt lds\n"
      "s_mov_b32 m0, s64\n"
      "s_nop 0\n"
      "ds_write_addtid_b32 v38 offset:38400\n"
      "v_mfma_f32_16x16x32_bf16 v[72:75], a[108:111], a[212:215], v[72:75]\n"
      "v_add_f32 v52, v52, v64\n"
      "v_add_f32 v53, v53, v65\n"
      "s_waitcnt lgkmcnt(3)\n"
      "v_mfma_f32_16x16x32_bf16 v[76:79], a[0:3], a[192:195], 0\n"
      "ds_read_b128 a[208:211], v4 offset:64\n"
      "ds_write_addtid_b32 v52 offset:45312\n"
      "ds_write_addtid_b32 v53 offset:45824\n"
      "v_mfma_f32_16x16x32_bf16 v[80:83], a[4:7], a[192:195], 0\n"
      "v_add_f32 v54, v54, v66\n"
      "v_add_f32_e64 v55, v55, v67\n"
      "v_mfma_f32_16x16x32_bf16 v[84:87], a[8:11], a[192:195], 0\n"
      "ds_write_addtid_b32 v54 offset:46336\n"
      "ds_write_addtid_b32 v55 offset:46848\n"
      "v_add_f32_e64 v56, v56, v68\n"
      "v_mfma_f32_16x16x32_bf16 v[76:79], a[12:15], a[196:199], v[76:79]\n"
      "ds_read_b128 a[212:215], v4 offset:128\n"
      "ds_write_addtid_b32 v56 offset:47360\n"
      "v_mfma_f32_16x16x32_bf16 v[80:83], a[16:19], a[196:199], v[80:83]\n"
      "v_add_f32_e64 v57, v57, v69\n"
      "v_cvt_pk_bf16_f32 v48, v48, v49\n"
      "v_mfma_f32_16x16x32_bf16 v[84:87], a[20:23], a[196:199], v[84:87]\n"
      "ds_write_addtid_b32 v48 offset:39936\n"
      "ds_write_addtid_b32 v57 offset:47872\n"
      "v_add_f32_e64 v58, v58, v70\n"
      "v_mfma_f32_16x16x32_bf16 v[76:79], a[24:27], a[200:203], v[76:79]\n"
      "ds_read_b128 a[192:195], v5 offset:0\n"
      "ds_write_addtid_b32 v58 offset:48384\n"
      "v_mfma_f32_16x16x32_bf16 v[80:83], a[28:31], a[200:203], v[80:83]\n"
      "buffer_store_dwordx4 a[240:243], v18, s[8:11], s59 offen offset:256 sc1\n"
      "v_mfma_f32_16x16x32_bf16 v[84:87], a[32:35], a[200:203], v[84:87]\n"
      "v_add_f32 v59, v59, v71\n"
      "v_add_f32_e64 v60, v60, v72\n"
      "v_cvt_pk_bf16_f32 v50, v50, v51\n"
      "s_waitcnt lgkmcnt(2)\n"
      "v_mfma_f32_16x16x32_bf16 v[76:79], a[36:39], a[204:207], v[76:79]\n"
      "ds_read_b128 a[196:199], v5 offset:64\n"
      "ds_write_addtid_b32 v50 offset:40448\n"
      "v_mfma_f32_16x16x32_bf16 v[80:83], a[40:43], a[204:207], v[80:83]\n"
      "ds_write_addtid_b32 v59 offset:48896\n"
      "ds_write_addtid_b32 v60 offset:49408\n"
      "v_add_f32_e64 v61, v61, v73\n"
      "v_mfma_f32_16x16x32_bf16 v[84:87], a[44:47], a[204:207], v[84:87]\n"
      "ds_write_addtid_b32 v61 offset:49920\n"
      "v_add_f32_e64 v62, v62, v74\n"
      "v_add_f32 v63, v63, v75\n"
      "v_mfma_f32_16x16x32_bf16 v[76:79], a[48:51], a[208:211], v[76:79]\n"
      "ds_read_b128 a[200:203], v5 offset:128\n"
      "s_add_u32 s54, s54, 196608\n"
      "s_add_u32 m0, s37, 86536\n"
      "v_mfma_f32_16x16x32_bf16 v[80:83], a[52:55], a[208:211], v[80:83]\n"
      "buffer_load_dwordx4 v8, s[4:7], s54 offen offset:0 nt lds\n"
      "buffer_load_dwordx4 v9, s[4:7], s54 offen offset:1024 nt lds\n"
      "s_mov_b32 m0, s64\n"
      "v_mfma_f32_16x16x32_bf16 v[84:87], a[56:59], a[208:211], v[84:87]\n"
      "ds_write_addtid_b32 v62 offset:50432\n"
      "ds_write_addtid_b32 v63 offset:50944\n"
      "v_mfma_f32_16x16x32_bf16 v[76:79], a[60:63], a[212:215], v[76:79]\n"
      "v_fma_f32 v40, -2.0, v64, v52\n"
      "v_fma_f32 v41, -2.0, v65, v53\n"
      "v_fma_f32 v42, -2.0, v66, v54\n"
      "v_fma_f32 v43, -2.0, v67, v55\n"
      "v_fma_f32 v44, -2.0, v68, v56\n"
      "v_mfma_f32_16x16x32_bf16 v[80:83], a[64:67], a[212:215], v[80:83]\n"
      "v_fma_f32 v45, -2.0, v69, v57\n"
      "v_fma_f32 v46, -2.0, v70, v58\n"
      "v_fma_f32 v47, -2.0, v71, v59\n"
      "v_fma_f32 v48, -2.0, v72, v60\n"
      "v_mfma_f32_16x16x32_bf16 v[84:87], a[68:71], a[212:215], v[84:87]\n"
      "v_fma_f32 v49, -2.0, v73, v61\n"
      "v_fma_f32 v50, -2.0, v74, v62\n"
      "v_fma_f32 v51, -2.0, v75, v63\n"
      "s_waitcnt lgkmcnt(0)\n"
      "s_waitcnt vmcnt(14)\n"
      "s_barrier\n"
      "s_add_u32 s55, s55, 2\n"
      "s_cmp_lt_u32 s55, 6\n"
      "s_cbranch_scc1 LDloop_%=\n"
      "LDlast_%=:\n"
      "v_mfma_f32_16x16x32_bf16 v[76:79], a[72:75], a[192:195], v[76:79]\n"
      "ds_read_b128 a[204:207], v0 offset:16384\n"
      "ds_read2st64_b32 v[28:29], v6 offset0:0 offset1:2\n"
      "ds_read2st64_b32 v[30:31], v6 offset0:4 offset1:6\n"
      "v_mfma_f32_16x16x32_bf16 v[80:83], a[76:79], a[192:195], v[80:83]\n"
      "ds_read_b128 a[208:211], v0 offset:20480\n"
      "ds_read2st64_b32 v[32:33], v6 offset0:8 offset1:10\n"
      "v_mfma_f32_16x16x32_bf16 v[84:87], a[80:83], a[192:195], v[84:87]\n"
      "s_add_u32 s58, s58, 3072\n"
      "s_add_u32 s59, s58, 196608\n"
      "v_mfma_f32_16x16x32_bf16 v[76:79], a[84:87], a[196:199], v[76:79]\n"
      "ds_read_b128 a[212:215], v0 offset:16448\n"
      "v_mfma_f32_16x16x32_bf16 v[80:83], a[88:91], a[196:199], v[80:83]\n"
      "ds_read2st64_b32 v[34:35], v6 offset0:12 offset1:14\n"
      "ds_read_b64_tr_b16 a[244:245], v7 offset:36864\n"
      "ds_read2st64_b32 v[36:37], v6 offset0:16 offset1:18\n"
      "v_mfma_f32_16x16x32_bf16 v[84:87], a[92:95], a[196:199], v[84:87]\n"
      "v_mfma_f32_16x16x32_bf16 v[76:79], a[96:99], a[200:203], v[76:79]\n"
      "ds_read_b128 a[192:195], v0 offset:20544\n"
      "ds_read_b64_tr_b16 a[246:247], v7 offset:36872\n"
      "s_waitcnt lgkmcnt(8)\n"
      "v_fmac_f32 v40, s46, v28\n"
      "v_fmac_f32 v41, s46, v29\n"
      "v_fmac_f32 v42, s46, v30\n"
      "v_mfma_f32_16x16x32_bf16 v[80:83], a[100:103], a[200:203], v[80:83]\n"
      "ds_read2st64_b32 a[220:221], v12 offset0:181 offset1:183\n"
      "ds_read2st64_b32 v[38:39], v6 offset0:20 offset1:22\n"
      "v_mfma_f32_16x16x32_bf16 v[84:87], a[104:107], a[200:203], v[84:87]\n"
      "v_fmac_f32_e64 v43, s46, v31\n"
      "v_fma_f32 v28, 0.5, v28, v100\n"
      "v_fma_f32 v29, 0.5, v29, v101\n"
      "v_mfma_f32_16x16x32_bf16 v[52:55], v[252:255], a[204:207], 0\n"
      "ds_read_b128 a[196:199], v0 offset:16512\n"
      "ds_read2st64_b32 a[222:223], v15 offset0:181 offset1:183\n"
      "v_mfma_f32_16x16x32_bf16 v[56:59], v[244:247], a[204:207], 0\n"
      "v_fma_f32 v30, 0.5, v30, v102\n"
      "v_fma_f32 v31, 0.5, v31, v103\n"
      "v_mfma_f32_16x16x32_bf16 v[60:63], v[236:239], a[204:207], 0\n"
      "v_fmac_f32 v40, s48, v76\n"
      "v_fmac_f32 v41, s48, v77\n"
      "v_fmac_f32 v42, s48, v78\n"
      "s_waitcnt lgkmcnt(4)\n"
      "v_mfma_f32_16x16x32_bf16 v[64:67], v[248:251], a[208:211], 0\n"
      "ds_read_b128 a[200:203], v0 offset:20608\n"
      "v_fmac_f32 v43, s48, v79\n"
      "v_mfma_f32_16x16x32_bf16 v[68:71], v[240:243], a[208:211], 0\n"
      "v_fmac_f32 v28, s45, v40\n"
      "v_fmac_f32 v29, s45, v41\n"
      "v_fmac_f32 v30, s45, v42\n"
      "v_mfma_f32_16x16x32_bf16 v[72:75], v[232:235], a[208:211], 0\n"
      "v_fmac_f32_e64 v31, s45, v43\n"
      "v_fma_f32 v40, s47, v40, -v76\n"
      "v_mfma_f32_16x16x32_bf16 v[52:55], v[228:231], a[212:215], v[52:55]\n"
      "ds_read_b128 a[204:207], v1 offset:16384\n"
      "v_fma_f32 v41, s47, v41, -v77\n"
      "v_mfma_f32_4x4x4_16b_bf16 v[20:23], a[216:217], a[244:245], 0\n"
      "v_fma_f32 v42, s47, v42, -v78\n"
      "v_mfma_f32_4x4x4_16b_bf16 v[24:27], a[216:217], a[246:247], 0\n"
      "v_fma_f32 v43, s47, v43, -v79\n"
      "v_cvt_pk_bf16_f32 v28, v28, v29\n"
      "v_cvt_pk_bf16_f32 v30, v30, v31\n"
      "v_mfma_f32_16x16x32_bf16 v[56:59], v[220:223], a[212:215], v[56:59]\n"
      "ds_write_addtid_b32 v28 offset:32768\n"
      "ds_write_addtid_b32 v30 offset:33280\n"
      "v_mfma_f32_16x16x32_bf16 v[60:63], v[212:215], a[212:215], v[60:63]\n"
      "ds_read2st64_b32 a[224:225], v13 offset0:181 offset1:183\n"
      "ds_read2st64_b32 a[226:227], v16 offset0:181 offset1:183\n"
      "v_cvt_pk_bf16_f32 v20, v20, v24\n"
      "v_mfma_f32_16x16x32_bf16 v[64:67], v[224:227], a[192:195], v[64:67]\n"
      "ds_read_b128 a[208:211], v1 offset:20480\n"
      "ds_write_addtid_b32 v20 offset:3072\n"
      "v_mfma_f32_16x16x32_bf16 v[68:71], v[216:219], a[192:195], v[68:71]\n"
      "v_cvt_pk_bf16_f32 v21, v21, v25\n"
      "v_cvt_pk_bf16_f32 v22, v22, v26\n"
      "v_mfma_f32_16x16x32_bf16 v[72:75], v[208:211], a[192:195], v[72:75]\n"
      "ds_write_addtid_b32 v21 offset:7168\n"
      "ds_write_addtid_b32 v22 offset:11264\n"
      "v_cvt_pk_bf16_f32 v40, v40, v41\n"
      "s_nop 0\n"
      "s_waitcnt lgkmcnt(6)\n"
      "v_mfma_f32_16x16x32_bf16 v[52:55], v[204:207], a[196:199], v[52:55]\n"
      "buffer_store_dwordx4 a[220:223], v18, s[8:11], s58 offen offset:0 sc1\n"
      "ds_read_b128 a[212:215], v1 offset:16448\n"
      "v_mfma_f32_16x16x32_bf16 v[56:59], v[196:199], a[196:199], v[56:59]\n"
      "ds_write_addtid_b32 v40 offset:34816\n"
      "s_add_u32 s54, s54, -193536\n"
      "s_add_u32 m0, s37, 99328\n"
      "v_mfma_f32_16x16x32_bf16 v[60:63], v[188:191], a[196:199], v[60:63]\n"
      "buffer_load_dwordx4 v10, s[4:7], s54 offen offset:0 nt lds\n"
      "buffer_load_dwordx4 v11, s[4:7], s54 offen offset:1024 nt lds\n"
      "s_mov_b32 m0, s64\n"
      "v_mfma_f32_16x16x32_bf16 v[64:67], v[200:203], a[200:203], v[64:67]\n"
      "ds_read_b128 a[192:195], v1 offset:20544\n"
      "v_cvt_pk_bf16_f32 v23, v23, v27\n"
      "v_mfma_f32_16x16x32_bf16 v[68:71], v[192:195], a[200:203], v[68:71]\n"
      "ds_write_addtid_b32 v23 offset:15360\n"
      "v_cvt_pk_bf16_f32 v42, v42, v43\n"
      "v_fmac_f32 v44, s46, v32\n"
      "v_mfma_f32_16x16x32_bf16 v[72:75], v[184:187], a[200:203], v[72:75]\n"
      "ds_write_addtid_b32 v42 offset:35328\n"
      "ds_read2st64_b32 a[228:229], v14 offset0:181 offset1:183\n"
      "v_mfma_f32_16x16x32_bf16 v[52:55], v[180:183], a[204:207], v[52:55]\n"
      "ds_read_b128 a[196:199], v1 offset:16512\n"
      "ds_read2st64_b32 a[230:231], v17 offset0:181 offset1:183\n"
      "v_mfma_f32_16x16x32_bf16 v[56:59], v[172:175], a[204:207], v[56:59]\n"
      "v_fmac_f32 v45, s46, v33\n"
      "v_fmac_f32 v46, s46, v34\n"
      "v_mfma_f32_16x16x32_bf16 v[60:63], v[164:167], a[204:207], v[60:63]\n"
      "v_fmac_f32 v47, s46, v35\n"
      "v_fma_f32 v32, 0.5, v32, v104\n"
      "s_waitcnt lgkmcnt(4)\n"
      "v_mfma_f32_16x16x32_bf16 v[64:67], v[176:179], a[208:211], v[64:67]\n"
      "ds_read_b128 a[200:203], v1 offset:20608\n"
      "v_fma_f32 v33, 0.5, v33, v105\n"
      "v_mfma_f32_16x16x32_bf16 v[68:71], v[168:171], a[208:211], v[68:71]\n"
      "buffer_store_dwordx4 a[224:227], v18, s[8:11], s58 offen offset:128 sc1\n"
      "v_mfma_f32_16x16x32_bf16 v[72:75], v[160:163], a[208:211], v[72:75]\n"
      "ds_read_b64_tr_b16 a[248:249], v7 offset:37376\n"
      "ds_read_b64_tr_b16 a[250:251], v7 offset:37384\n"
      "v_fma_f32 v34, 0.5, v34, v106\n"
      "v_mfma_f32_16x16x32_bf16 v[52:55], v[156:159], a[212:215], v[52:55]\n"
      "ds_read_b128 a[204:207], v2 offset:16384\n"
      "v_fma_f32 v35, 0.5, v35, v107\n"
      "v_mfma_f32_16x16x32_bf16 v[56:59], v[148:151], a[212:215], v[56:59]\n"
      "ds_read2st64_b32 a[232:233], v12 offset0:4 offset1:6\n"
      "ds_read2st64_b32 a[234:235], v15 offset0:4 offset1:6\n"
      "v_fmac_f32 v44, s48, v80\n"
      "v_mfma_f32_16x16x32_bf16 v[60:63], v[140:143], a[212:215], v[60:63]\n"
      "v_fmac_f32 v45, s48, v81\n"
      "v_fmac_f32_e64 v46, s48, v82\n"
      "v_mfma_f32_16x16x32_bf16 v[64:67], v[152:155], a[192:195], v[64:67]\n"
      "ds_read_b128 a[208:211], v2 offset:20480\n"
      "v_fmac_f32_e64 v47, s48, v83\n"
      "v_mfma_f32_16x16x32_bf16 v[68:71], v[144:147], a[192:195], v[68:71]\n"
      "v_fmac_f32 v32, s45, v44\n"
      "v_fmac_f32_e64 v33, s45, v45\n"
      "v_fmac_f32_e64 v34, s45, v46\n"
      "v_mfma_f32_16x16x32_bf16 v[72:75], v[136:139], a[192:195], v[72:75]\n"
      "v_fmac_f32_e64 v35, s45, v47\n"
      "v_fma_f32 v44, s47, v44, -v80\n"
      "s_waitcnt lgkmcnt(2)\n"
      "v_mfma_f32_16x16x32_bf16 v[52:55], v[132:135], a[196:199], v[52:55]\n"
      "ds_read_b128 a[212:215], v2 offset:16448\n"
      "v_fma_f32 v45, s47, v45, -v81\n"
      "v_mfma_f32_16x16x32_bf16 v[56:59], v[124:127], a[196:199], v[56:59]\n"
      "buffer_store_dwordx4 a[228:231], v18, s[8:11], s58 offen offset:256 sc1\n"
      "v_mfma_f32_16x16x32_bf16 v[60:63], a[184:187], a[196:199], v[60:63]\n"
      "v_fma_f32 v46, s47, v46, -v82\n"
      "v_fma_f32 v47, s47, v47, -v83\n"
      "v_mfma_f32_16x16x32_bf16 v[64:67], v[128:131], a[200:203], v[64:67]\n"
      "ds_read_b128 a[192:195], v2 offset:20544\n"
      "v_cvt_pk_bf16_f32 v32, v32, v33\n"
      "v_mfma_f32_16x16x32_bf16 v[68:71], a[188:191], a[200:203], v[68:71]\n"
      "ds_write_addtid_b32 v32 offset:40964\n"
      "v_cvt_pk_bf16_f32 v34, v34, v35\n"
      "v_cvt_pk_bf16_f32 v44, v44, v45\n"
      "v_mfma_f32_4x4x4_16b_bf16 v[20:23], a[216:217], a[248:249], 0\n"
      "v_cvt_pk_bf16_f32 v46, v46, v47\n"
      "v_mfma_f32_4x4x4_16b_bf16 v[24:27], a[216:217], a[250:251], 0\n"
      "v_fmac_f32_e64 v48, s46, v36\n"
      "v_fmac_f32_e64 v49, s46, v37\n"
      "v_fmac_f32_e64 v50, s46, v38\n"
      "v_mfma_f32_16x16x32_bf16 v[72:75], a[180:183], a[200:203], v[72:75]\n"
      "ds_write_addtid_b32 v34 offset:41476\n"
      "ds_write_addtid_b32 v44 offset:43012\n"
      "v_mfma_f32_16x16x32_bf16 v[52:55], a[176:179], a[204:207], v[52:55]\n"
      "ds_read_b128 a[196:199], v2 offset:16512\n"
      "ds_write_addtid_b32 v46 offset:43524\n"
      "v_mfma_f32_16x16x32_bf16 v[56:59], a[168:171], a[204:207], v[56:59]\n"
      "ds_read2st64_b32 a[236:237], v13 offset0:4 offset1:6\n"
      "ds_read2st64_b32 a[238:239], v16 offset0:4 offset1:6\n"
      "v_cvt_pk_bf16_f32 v20, v20, v24\n"
      "v_mfma_f32_16x16x32_bf16 v[60:63], a[160:163], a[204:207], v[60:63]\n"
      "ds_write_addtid_b32 v20 offset:3584\n"
      "v_cvt_pk_bf16_f32 v21, v21, v25\n"
      "s_waitcnt lgkmcnt(7)\n"
      "v_mfma_f32_16x16x32_bf16 v[64:67], a[172:175], a[208:211], v[64:67]\n"
      "buffer_store_dwordx4 a[232:235], v18, s[8:11], s59 offen offset:0 sc1\n"
      "ds_read_b128 a[200:203], v2 offset:20608\n"
      "v_mfma_f32_16x16x32_bf16 v[68:71], a[164:167], a[208:211], v[68:71]\n"
      "ds_write_addtid_b32 v21 offset:7680\n"
      "v_fmac_f32 v51, s46, v39\n"
      "v_fma_f32 v36, 0.5, v36, v108\n"
      "v_mfma_f32_16x16x32_bf16 v[72:75], a[156:159], a[208:211], v[72:75]\n"
      "v_cvt_pk_bf16_f32 v22, v22, v26\n"
      "v_fma_f32 v37, 0.5, v37, v109\n"
      "v_cvt_pk_bf16_f32 v23, v23, v27\n"
      "v_mfma_f32_16x16x32_bf16 v[52:55], a[152:155], a[212:215], v[52:55]\n"
      "ds_read_b128 a[204:207], v3 offset:16896\n"
      "ds_write_addtid_b32 v22 offset:11776\n"
      "v_mfma_f32_16x16x32_bf16 v[56:59], a[144:147], a[212:215], v[56:59]\n"
      "ds_write_addtid_b32 v23 offset:15872\n"
      "v_fma_f32 v38, 0.5, v38, v110\n"
      "v_mfma_f32_16x16x32_bf16 v[60:63], a[136:139], a[212:215], v[60:63]\n"
      "v_fma_f32 v39, 0.5, v39, v111\n"
      "v_fmac_f32 v48, s48, v84\n"
      "v_fmac_f32 v49, s48, v85\n"
      "v_mfma_f32_16x16x32_bf16 v[64:67], a[148:151], a[192:195], v[64:67]\n"
      "ds_read_b128 a[208:211], v3 offset:16960\n"
      "v_fmac_f32 v50, s48, v86\n"
      "v_mfma_f32_16x16x32_bf16 v[68:71], a[140:143], a[192:195], v[68:71]\n"
      "v_fmac_f32 v51, s48, v87\n"
      "v_fmac_f32 v36, s45, v48\n"
      "v_mfma_f32_16x16x32_bf16 v[72:75], a[132:135], a[192:195], v[72:75]\n"
      "ds_read2st64_b32 a[240:241], v14 offset0:4 offset1:6\n"
      "ds_read2st64_b32 a[242:243], v17 offset0:4 offset1:6\n"
      "v_fmac_f32 v37, s45, v49\n"
      "s_waitcnt lgkmcnt(3)\n"
      "v_mfma_f32_16x16x32_bf16 v[52:55], a[128:131], a[196:199], v[52:55]\n"
      "ds_read_b128 a[212:215], v3 offset:17024\n"
      "v_fmac_f32 v38, s45, v50\n"
      "v_mfma_f32_16x16x32_bf16 v[56:59], a[120:123], a[196:199], v[56:59]\n"
      "buffer_store_dwordx4 a[236:239], v18, s[8:11], s59 offen offset:128 sc1\n"
      "v_mfma_f32_16x16x32_bf16 v[60:63], a[112:115], a[196:199], v[60:63]\n"
      "v_fmac_f32_e64 v39, s45, v51\n"
      "v_fma_f32 v48, s47, v48, -v84\n"
      "v_mfma_f32_16x16x32_bf16 v[64:67], a[124:127], a[200:203], v[64:67]\n"
      "ds_read_b128 a[192:195], v4 offset:16896\n"
      "v_fma_f32 v49, s47, v49, -v85\n"
      "v_mfma_f32_16x16x32_bf16 v[68:71], a[116:119], a[200:203], v[68:71]\n"
      "v_fma_f32 v50, s47, v50, -v86\n"
      "v_fma_f32 v51, s47, v51, -v87\n"
      "v_mfma_f32_16x16x32_bf16 v[72:75], a[108:111], a[200:203], v[72:75]\n"
      "v_cvt_pk_bf16_f32 v36, v36, v37\n"
      "v_cvt_pk_bf16_f32 v38, v38, v39\n"
      "v_add_f32 v52, v52, v64\n"
      "v_add_f32 v53, v53, v65\n"
      "v_mfma_f32_16x16x32_bf16 v[88:91], a[0:3], a[204:207], 0\n"
      "ds_read_b128 a[196:199], v4 offset:16960\n"
      "ds_write_addtid_b32 v36 offset:36864\n"
      "v_mfma_f32_16x16x32_bf16 v[92:95], a[4:7], a[204:207], 0\n"
      "ds_write_addtid_b32 v38 offset:37376\n"
      "ds_write_addtid_b32 v52 offset:51456\n"
      "ds_write_addtid_b32 v53 offset:51968\n"
      "v_mfma_f32_16x16x32_bf16 v[96:99], a[8:11], a[204:207], 0\n"
      "v_add_f32 v54, v54, v66\n"
      "v_add_f32_e64 v55, v55, v67\n"
      "s_waitcnt lgkmcnt(5)\n"
      "v_mfma_f32_16x16x32_bf16 v[88:91], a[12:15], a[208:211], v[88:91]\n"
      "ds_read_b128 a[200:203], v4 offset:17024\n"
      "ds_write_addtid_b32 v54 offset:52480\n"
      "v_mfma_f32_16x16x32_bf16 v[92:95], a[16:19], a[208:211], v[92:95]\n"
      "ds_write_addtid_b32 v55 offset:52992\n"
      "v_add_f32 v56, v56, v68\n"
      "v_add_f32 v57, v57, v69\n"
      "v_mfma_f32_16x16x32_bf16 v[96:99], a[20:23], a[208:211], v[96:99]\n"
      "ds_write_addtid_b32 v56 offset:53504\n"
      "ds_write_addtid_b32 v57 offset:54016\n"
      "v_mfma_f32_16x16x32_bf16 v[88:91], a[24:27], a[212:215], v[88:91]\n"
      "ds_read_b128 a[204:207], v5 offset:16896\n"
      "v_add_f32_e64 v58, v58, v70\n"
      "v_mfma_f32_16x16x32_bf16 v[92:95], a[28:31], a[212:215], v[92:95]\n"
      "buffer_store_dwordx4 a[240:243], v18, s[8:11], s59 offen offset:256 sc1\n"
      "v_mfma_f32_16x16x32_bf16 v[96:99], a[32:35], a[212:215], v[96:99]\n"
      "ds_write_addtid_b32 v58 offset:54528\n"
      "v_cvt_pk_bf16_f32 v48, v48, v49\n"
      "v_add_f32_e64 v59, v59, v71\n"
      "v_mfma_f32_16x16x32_bf16 v[88:91], a[36:39], a[192:195], v[88:91]\n"
      "ds_read_b128 a[208:211], v5 offset:16960\n"
      "ds_write_addtid_b32 v48 offset:38912\n"
      "v_cvt_pk_bf16_f32 v50, v50, v51\n"
      "v_mfma_f32_16x16x32_bf16 v[92:95], a[40:43], a[192:195], v[92:95]\n"
      "ds_write_addtid_b32 v50 offset:39424\n"
      "ds_write_addtid_b32 v59 offset:55040\n"
      "v_mfma_f32_16x16x32_bf16 v[96:99], a[44:47], a[192:195], v[96:99]\n"
      "v_add_f32 v60, v60, v72\n"
      "v_add_f32 v61, v61, v73\n"
      "v_add_f32 v62, v62, v74\n"
      "v_add_f32_e64 v63, v63, v75\n"
      "s_waitcnt lgkmcnt(4)\n"
      "v_mfma_f32_16x16x32_bf16 v[88:91], a[48:51], a[196:199], v[88:91]\n"
      "ds_read_b128 a[212:215], v5 offset:17024\n"
      "ds_write_addtid_b32 v60 offset:55552\n"
      "v_mfma_f32_16x16x32_bf16 v[92:95], a[52:55], a[196:199], v[92:95]\n"
      "ds_write_addtid_b32 v61 offset:56064\n"
      "ds_write_addtid_b32 v62 offset:56576\n"
      "v_mfma_f32_16x16x32_bf16 v[96:99], a[56:59], a[196:199], v[96:99]\n"
      "ds_write_addtid_b32 v63 offset:57088\n"
      "v_mfma_f32_16x16x32_bf16 v[88:91], a[60:63], a[200:203], v[88:91]\n"
      "v_fma_f32 v40, -2.0, v64, v52\n"
      "v_fma_f32 v41, -2.0, v65, v53\n"
      "v_fma_f32 v42, -2.0, v66, v54\n"
      "v_fma_f32 v43, -2.0, v67, v55\n"
      "v_fma_f32 v44, -2.0, v68, v56\n"
      "v_mfma_f32_16x16x32_bf16 v[92:95], a[64:67], a[200:203], v[92:95]\n"
      "v_fma_f32 v45, -2.0, v69, v57\n"
      "v_fma_f32 v46, -2.0, v70, v58\n"
      "v_fma_f32 v47, -2.0, v71, v59\n"
      "v_fma_f32 v48, -2.0, v72, v60\n"
      "v_mfma_f32_16x16x32_bf16 v[96:99], a[68:71], a[200:203], v[96:99]\n"
      "v_fma_f32 v49, -2.0, v73, v61\n"
      "v_fma_f32 v50, -2.0, v74, v62\n"
      "v_fma_f32 v51, -2.0, v75, v63\n"
      "s_waitcnt lgkmcnt(0)\n"
      "s_waitcnt vmcnt(8)\n"
      "s_barrier\n"
      "v_mfma_f32_16x16x32_bf16 v[88:91], a[72:75], a[204:207], v[88:91]\n"
      "ds_read_b128 a[192:195], v0 offset:0\n"
      "ds_read2st64_b32 v[28:29], v6 offset0:24 offset1:26\n"
      "ds_read2st64_b32 v[30:31], v6 offset0:28 offset1:30\n"
      "v_mfma_f32_16x16x32_bf16 v[92:95], a[76:79], a[204:207], v[92:95]\n"
      "ds_read_b128 a[196:199], v0 offset:4096\n"
      "ds_read2st64_b32 v[32:33], v6 offset0:32 offset1:34\n"
      "v_mfma_f32_16x16x32_bf16 v[96:99], a[80:83], a[204:207], v[96:99]\n"
      "s_add_u32 s62, s62, 786432\n"
      "s_mov_b32 s58, s62\n"
      "v_mfma_f32_16x16x32_bf16 v[88:91], a[84:87], a[208:211], v[88:91]\n"
      "ds_read_b128 a[200:203], v0 offset:64\n"
      "s_add_u32 s59, s58, 196608\n"
      "ds_read2st64_b32 v[34:35], v6 offset0:36 offset1:38\n"
      "v_mfma_f32_16x16x32_bf16 v[92:95], a[88:91], a[208:211], v[92:95]\n"
      "ds_read_b64_tr_b16 a[244:245], v7 offset:53760\n"
      "ds_read2st64_b32 v[36:37], v6 offset0:40 offset1:42\n"
      "v_mfma_f32_16x16x32_bf16 v[96:99], a[92:95], a[208:211], v[96:99]\n"
      "v_mfma_f32_16x16x32_bf16 v[88:91], a[96:99], a[212:215], v[88:91]\n"
      "ds_read_b128 a[204:207], v0 offset:4160\n"
      "s_waitcnt lgkmcnt(7)\n"
      "v_fmac_f32 v40, s46, v28\n"
      "v_fmac_f32 v41, s46, v29\n"
      "v_fmac_f32 v42, s46, v30\n"
      "v_mfma_f32_16x16x32_bf16 v[92:95], a[100:103], a[212:215], v[92:95]\n"
      "ds_read2st64_b32 a[220:221], v12 offset0:177 offset1:179\n"
      "ds_read_b64_tr_b16 a[246:247], v7 offset:53768\n"
      "v_mfma_f32_16x16x32_bf16 v[96:99], a[104:107], a[212:215], v[96:99]\n"
      "ds_read2st64_b32 a[222:223], v15 offset0:177 offset1:179\n"
      "ds_read2st64_b32 v[38:39], v6 offset0:44 offset1:46\n"
      "v_mfma_f32_16x16x32_bf16 v[52:55], v[252:255], a[192:195], 0\n"
      "ds_read_b128 a[208:211], v0 offset:128\n"
      "v_fmac_f32_e64 v43, s46, v31\n"
      "v_fma_f32 v28, 0.5, v28, v112\n"
      "v_mfma_f32_16x16x32_bf16 v[56:59], v[244:247], a[192:195], 0\n"
      "v_fma_f32 v29, 0.5, v29, v113\n"
      "v_fma_f32 v30, 0.5, v30, v114\n"
      "v_mfma_f32_16x16x32_bf16 v[60:63], v[236:239], a[192:195], 0\n"
      "v_fma_f32 v31, 0.5, v31, v115\n"
      "v_fmac_f32 v40, s48, v88\n"
      "s_waitcnt lgkmcnt(6)\n"
      "v_mfma_f32_16x16x32_bf16 v[64:67], v[248:251], a[196:199], 0\n"
      "ds_read_b128 a[212:215], v0 offset:4224\n"
      "v_fmac_f32 v41, s48, v89\n"
      "v_fmac_f32 v42, s48, v90\n"
      "v_mfma_f32_16x16x32_bf16 v[68:71], v[240:243], a[196:199], 0\n"
      "v_fmac_f32_e64 v43, s48, v91\n"
      "v_fmac_f32 v28, s45, v40\n"
      "v_mfma_f32_16x16x32_bf16 v[72:75], v[232:235], a[196:199], 0\n"
      "v_fmac_f32_e64 v29, s45, v41\n"
      "v_fmac_f32_e64 v30, s45, v42\n"
      "v_mfma_f32_16x16x32_bf16 v[52:55], v[228:231], a[200:203], v[52:55]\n"
      "ds_read_b128 a[192:195], v1 offset:0\n"
      "v_fmac_f32_e64 v31, s45, v43\n"
      "v_fma_f32 v40, s47, v40, -v88\n"
      "s_waitcnt lgkmcnt(2)\n"
      "v_mfma_f32_4x4x4_16b_bf16 v[20:23], a[216:217], a[244:245], 0\n"
      "v_fma_f32 v41, s47, v41, -v89\n"
      "v_mfma_f32_4x4x4_16b_bf16 v[24:27], a[216:217], a[246:247], 0\n"
      "v_fma_f32 v42, s47, v42, -v90\n"
      "v_fma_f32 v43, s47, v43, -v91\n"
      "v_cvt_pk_bf16_f32 v28, v28, v29\n"
      "v_mfma_f32_16x16x32_bf16 v[56:59], v[220:223], a[200:203], v[56:59]\n"
      "ds_write_addtid_b32 v28 offset:33792\n"
      "v_cvt_pk_bf16_f32 v20, v20, v24\n"
      "v_mfma_f32_16x16x32_bf16 v[60:63], v[212:215], a[200:203], v[60:63]\n"
      "ds_write_addtid_b32 v20 offset:19456\n"
      "ds_read2st64_b32 a[224:225], v13 offset0:177 offset1:179\n"
      "v_mfma_f32_16x16x32_bf16 v[64:67], v[224:227], a[204:207], v[64:67]\n"
      "ds_read_b128 a[196:199], v1 offset:4096\n"
      "ds_read2st64_b32 a[226:227], v16 offset0:177 offset1:179\n"
      "v_mfma_f32_16x16x32_bf16 v[68:71], v[216:219], a[204:207], v[68:71]\n"
      "v_cvt_pk_bf16_f32 v21, v21, v25\n"
      "v_cvt_pk_bf16_f32 v30, v30, v31\n"
      "v_cvt_pk_bf16_f32 v22, v22, v26\n"
      "v_mfma_f32_16x16x32_bf16 v[72:75], v[208:211], a[204:207], v[72:75]\n"
      "ds_write_addtid_b32 v30 offset:34304\n"
      "ds_write_addtid_b32 v21 offset:23552\n"
      "v_mfma_f32_16x16x32_bf16 v[52:55], v[204:207], a[208:211], v[52:55]\n"
      "buffer_store_dwordx4 a[220:223], v18, s[8:11], s58 offen offset:0 sc1\n"
      "ds_read_b128 a[200:203], v1 offset:64\n"
      "v_mfma_f32_16x16x32_bf16 v[56:59], v[196:199], a[208:211], v[56:59]\n"
      "ds_write_addtid_b32 v22 offset:27648\n"
      "v_cvt_pk_bf16_f32 v40, v40, v41\n"
      "v_cvt_pk_bf16_f32 v42, v42, v43\n"
      "v_mfma_f32_16x16x32_bf16 v[60:63], v[188:191], a[208:211], v[60:63]\n"
      "ds_write_addtid_b32 v40 offset:35840\n"
      "ds_write_addtid_b32 v42 offset:36352\n"
      "s_waitcnt lgkmcnt(4)\n"
      "v_mfma_f32_16x16x32_bf16 v[64:67], v[200:203], a[212:215], v[64:67]\n"
      "ds_read_b128 a[204:207], v1 offset:4160\n"
      "v_cvt_pk_bf16_f32 v23, v23, v27\n"
      "v_mfma_f32_16x16x32_bf16 v[68:71], v[192:195], a[212:215], v[68:71]\n"
      "ds_write_addtid_b32 v23 offset:31744\n"
      "v_fmac_f32 v44, s46, v32\n"
      "v_mfma_f32_16x16x32_bf16 v[72:75], v[184:187], a[212:215], v[72:75]\n"
      "ds_read2st64_b32 a[228:229], v14 offset0:177 offset1:179\n"
      "ds_read2st64_b32 a[230:231], v17 offset0:177 offset1:179\n"
      "v_mfma_f32_16x16x32_bf16 v[52:55], v[180:183], a[192:195], v[52:55]\n"
      "ds_read_b128 a[208:211], v1 offset:128\n"
      "v_fmac_f32 v45, s46, v33\n"
      "v_mfma_f32_16x16x32_bf16 v[56:59], v[172:175], a[192:195], v[56:59]\n"
      "v_fmac_f32 v46, s46, v34\n"
      "v_fmac_f32_e64 v47, s46, v35\n"
      "v_fma_f32 v32, 0.5, v32, v116\n"
      "v_mfma_f32_16x16x32_bf16 v[60:63], v[164:167], a[192:195], v[60:63]\n"
      "v_fma_f32 v33, 0.5, v33, v117\n"
      "v_fma_f32 v34, 0.5, v34, v118\n"
      "v_mfma_f32_16x16x32_bf16 v[64:67], v[176:179], a[196:199], v[64:67]\n"
      "ds_read_b128 a[212:215], v1 offset:4224\n"
      "v_fma_f32 v35, 0.5, v35, v119\n"
      "v_mfma_f32_16x16x32_bf16 v[68:71], v[168:171], a[196:199], v[68:71]\n"
      "buffer_store_dwordx4 a[224:227], v18, s[8:11], s58 offen offset:128 sc1\n"
      "v_mfma_f32_16x16x32_bf16 v[72:75], v[160:163], a[196:199], v[72:75]\n"
      "ds_read_b64_tr_b16 a[248:249], v7 offset:54272\n"
      "ds_read_b64_tr_b16 a[250:251], v7 offset:54280\n"
      "v_fmac_f32 v44, s48, v92\n"
      "s_waitcnt lgkmcnt(3)\n"
      "v_mfma_f32_16x16x32_bf16 v[52:55], v[156:159], a[200:203], v[52:55]\n"
      "ds_read_b128 a[192:195], v2 offset:0\n"
      "v_fmac_f32_e64 v45, s48, v93\n"
      "v_mfma_f32_16x16x32_bf16 v[56:59], v[148:151], a[200:203], v[56:59]\n"
      "ds_read2st64_b32 a[232:233], v12 offset0:0 offset1:2\n"
      "ds_read2st64_b32 a[234:235], v15 offset0:0 offset1:2\n"
      "v_fmac_f32 v46, s48, v94\n"
      "v_mfma_f32_16x16x32_bf16 v[60:63], v[140:143], a[200:203], v[60:63]\n"
      "v_fmac_f32 v47, s48, v95\n"
      "v_fmac_f32 v32, s45, v44\n"
      "v_mfma_f32_16x16x32_bf16 v[64:67], v[152:155], a[204:207], v[64:67]\n"
      "ds_read_b128 a[196:199], v2 offset:4096\n"
      "v_fmac_f32 v33, s45, v45\n"
      "v_mfma_f32_16x16x32_bf16 v[68:71], v[144:147], a[204:207], v[68:71]\n"
      "v_fmac_f32 v34, s45, v46\n"
      "v_fmac_f32 v35, s45, v47\n"
      "v_fma_f32 v44, s47, v44, -v92\n"
      "v_mfma_f32_16x16x32_bf16 v[72:75], v[136:139], a[204:207], v[72:75]\n"
      "v_fma_f32 v45, s47, v45, -v93\n"
      "v_fma_f32 v46, s47, v46, -v94\n"
      "v_mfma_f32_16x16x32_bf16 v[52:55], v[132:135], a[208:211], v[52:55]\n"
      "ds_read_b128 a[200:203], v2 offset:64\n"
      "v_fma_f32 v47, s47, v47, -v95\n"
      "v_mfma_f32_16x16x32_bf16 v[56:59], v[124:127], a[208:211], v[56:59]\n"
      "buffer_store_dwordx4 a[228:231], v18, s[8:11], s58 offen offset:256 sc1\n"
      "v_mfma_f32_16x16x32_bf16 v[60:63], a[184:187], a[208:211], v[60:63]\n"
      "v_cvt_pk_bf16_f32 v32, v32, v33\n"
      "s_nop 0\n"
      "s_waitcnt lgkmcnt(1)\n"
      "v_mfma_f32_16x16x32_bf16 v[64:67], v[128:131], a[212:215], v[64:67]\n"
      "ds_read_b128 a[204:207], v2 offset:4160\n"
      "ds_write_addtid_b32 v32 offset:41988\n"
      "v_cvt_pk_bf16_f32 v34, v34, v35\n"
      "v_mfma_f32_16x16x32_bf16 v[68:71], a[188:191], a[212:215], v[68:71]\n"
      "ds_write_addtid_b32 v34 offset:42500\n"
      "v_cvt_pk_bf16_f32 v44, v44, v45\n"
      "v_cvt_pk_bf16_f32 v46, v46, v47\n"
      "v_mfma_f32_4x4x4_16b_bf16 v[20:23], a[216:217], a[248:249], 0\n"
      "v_fmac_f32 v48, s46, v36\n"
      "v_mfma_f32_4x4x4_16b_bf16 v[24:27], a[216:217], a[250:251], 0\n"
      "v_fmac_f32 v49, s46, v37\n"
      "v_fmac_f32 v50, s46, v38\n"
      "v_fmac_f32 v51, s46, v39\n"
      "v_mfma_f32_16x16x32_bf16 v[72:75], a[180:183], a[212:215], v[72:75]\n"
      "ds_write_addtid_b32 v44 offset:44036\n"
      "ds_write_addtid_b32 v46 offset:44548\n"
      "v_mfma_f32_16x16x32_bf16 v[52:55], a[176:179], a[192:195], v[52:55]\n"
      "ds_read_b128 a[208:211], v2 offset:128\n"
      "ds_read2st64_b32 a[236:237], v13 offset0:0 offset1:2\n"
      "v_mfma_f32_16x16x32_bf16 v[56:59], a[168:171], a[192:195], v[56:59]\n"
      "ds_read2st64_b32 a[238:239], v16 offset0:0 offset1:2\n"
      "v_cvt_pk_bf16_f32 v20, v20, v24\n"
      "v_mfma_f32_16x16x32_bf16 v[60:63], a[160:163], a[192:195], v[60:63]\n"
      "ds_write_addtid_b32 v20 offset:19968\n"
      "v_cvt_pk_bf16_f32 v21, v21, v25\n"
      "v_fma_f32 v36, 0.5, v36, v120\n"
      "v_mfma_f32_16x16x32_bf16 v[64:67], a[172:175], a[196:199], v[64:67]\n"
      "buffer_store_dwordx4 a[232:235], v18, s[8:11], s59 offen offset:0 sc1\n"
      "ds_read_b128 a[212:215], v2 offset:4224\n"
      "v_mfma_f32_16x16x32_bf16 v[68:71], a[164:167], a[196:199], v[68:71]\n"
      "ds_write_addtid_b32 v21 offset:24064\n"
      "v_fma_f32 v37, 0.5, v37, v121\n"
      "v_mfma_f32_16x16x32_bf16 v[72:75], a[156:159], a[196:199], v[72:75]\n"
      "v_cvt_pk_bf16_f32 v22, v22, v26\n"
      "v_fma_f32 v38, 0.5, v38, v122\n"
      "v_cvt_pk_bf16_f32 v23, v23, v27\n"
      "s_waitcnt lgkmcnt(2)\n"
      "v_mfma_f32_16x16x32_bf16 v[52:55], a[152:155], a[200:203], v[52:55]\n"
      "ds_read_b128 a[192:195], v3 offset:33792\n"
      "ds_write_addtid_b32 v22 offset:28160\n"
      "v_mfma_f32_16x16x32_bf16 v[56:59], a[144:147], a[200:203], v[56:59]\n"
      "ds_write_addtid_b32 v23 offset:32256\n"
      "v_fma_f32 v39, 0.5, v39, v123\n"
      "v_mfma_f32_16x16x32_bf16 v[60:63], a[136:139], a[200:203], v[60:63]\n"
      "v_fmac_f32 v48, s48, v96\n"
      "v_fmac_f32 v49, s48, v97\n"
      "v_fmac_f32 v50, s48, v98\n"
      "v_mfma_f32_16x16x32_bf16 v[64:67], a[148:151], a[204:207], v[64:67]\n"
      "ds_read_b128 a[196:199], v3 offset:33856\n"
      "v_fmac_f32 v51, s48, v99\n"
      "v_mfma_f32_16x16x32_bf16 v[68:71], a[140:143], a[204:207], v[68:71]\n"
      "v_fmac_f32 v36, s45, v48\n"
      "v_fmac_f32 v37, s45, v49\n"
      "v_mfma_f32_16x16x32_bf16 v[72:75], a[132:135], a[204:207], v[72:75]\n"
      "ds_read2st64_b32 a[240:241], v14 offset0:0 offset1:2\n"
      "ds_read2st64_b32 a[242:243], v17 offset0:0 offset1:2\n"
      "v_fmac_f32 v38, s45, v50\n"
      "v_mfma_f32_16x16x32_bf16 v[52:55], a[128:131], a[208:211], v[52:55]\n"
      "ds_read_b128 a[200:203], v3 offset:33920\n"
      "v_fmac_f32 v39, s45, v51\n"
      "v_mfma_f32_16x16x32_bf16 v[56:59], a[120:123], a[208:211], v[56:59]\n"
      "buffer_store_dwordx4 a[236:239], v18, s[8:11], s59 offen offset:128 sc1\n"
      "v_mfma_f32_16x16x32_bf16 v[60:63], a[112:115], a[208:211], v[60:63]\n"
      "v_fma_f32 v48, s47, v48, -v96\n"
      "v_fma_f32 v49, s47, v49, -v97\n"
      "s_waitcnt lgkmcnt(2)\n"
      "v_mfma_f32_16x16x32_bf16 v[64:67], a[124:127], a[212:215], v[64:67]\n"
      "ds_read_b128 a[204:207], v4 offset:33792\n"
      "v_fma_f32 v50, s47, v50, -v98\n"
      "v_mfma_f32_16x16x32_bf16 v[68:71], a[116:119], a[212:215], v[68:71]\n"
      "v_fma_f32 v51, s47, v51, -v99\n"
      "v_cvt_pk_bf16_f32 v36, v36, v37\n"
      "v_mfma_f32_16x16x32_bf16 v[72:75], a[108:111], a[212:215], v[72:75]\n"
      "ds_write_addtid_b32 v36 offset:37888\n"
      "v_cvt_pk_bf16_f32 v38, v38, v39\n"
      "v_add_f32 v52, v52, v64\n"
      "v_add_f32 v53, v53, v65\n"
      "v_mfma_f32_16x16x32_bf16 v[100:103], a[0:3], a[192:195], 0\n"
      "ds_read_b128 a[208:211], v4 offset:33856\n"
      "ds_write_addtid_b32 v38 offset:38400\n"
      "v_mfma_f32_16x16x32_bf16 v[104:107], a[4:7], a[192:195], 0\n"
      "ds_write_addtid_b32 v52 offset:45312\n"
      "ds_write_addtid_b32 v53 offset:45824\n"
      "v_mfma_f32_16x16x32_bf16 v[108:111], a[8:11], a[192:195], 0\n"
      "v_add_f32 v54, v54, v66\n"
      "v_add_f32 v55, v55, v67\n"
      "v_add_f32_e64 v56, v56, v68\n"
      "v_mfma_f32_16x16x32_bf16 v[100:103], a[12:15], a[196:199], v[100:103]\n"
      "ds_read_b128 a[212:215], v4 offset:33920\n"
      "ds_write_addtid_b32 v54 offset:46336\n"
      "v_mfma_f32_16x16x32_bf16 v[104:107], a[16:19], a[196:199], v[104:107]\n"
      "ds_write_addtid_b32 v55 offset:46848\n"
      "ds_write_addtid_b32 v56 offset:47360\n"
      "v_mfma_f32_16x16x32_bf16 v[108:111], a[20:23], a[196:199], v[108:111]\n"
      "v_add_f32 v57, v57, v69\n"
      "v_add_f32_e64 v58, v58, v70\n"
      "v_cvt_pk_bf16_f32 v48, v48, v49\n"
      "s_waitcnt lgkmcnt(4)\n"
      "v_mfma_f32_16x16x32_bf16 v[100:103], a[24:27], a[200:203], v[100:103]\n"
      "ds_read_b128 a[192:195], v5 offset:33792\n"
      "ds_write_addtid_b32 v48 offset:39936\n"
      "v_mfma_f32_16x16x32_bf16 v[104:107], a[28:31], a[200:203], v[104:107]\n"
      "buffer_store_dwordx4 a[240:243], v18, s[8:11], s59 offen offset:256 sc1\n"
      "v_mfma_f32_16x16x32_bf16 v[108:111], a[32:35], a[200:203], v[108:111]\n"
      "ds_write_addtid_b32 v57 offset:47872\n"
      "ds_write_addtid_b32 v58 offset:48384\n"
      "v_mfma_f32_16x16x32_bf16 v[100:103], a[36:39], a[204:207], v[100:103]\n"
      "ds_read_b128 a[196:199], v5 offset:33856\n"
      "v_cvt_pk_bf16_f32 v50, v50, v51\n"
      "v_add_f32 v59, v59, v71\n"
      "v_mfma_f32_16x16x32_bf16 v[104:107], a[40:43], a[204:207], v[104:107]\n"
      "ds_write_addtid_b32 v50 offset:40448\n"
      "ds_write_addtid_b32 v59 offset:48896\n"
      "v_mfma_f32_16x16x32_bf16 v[108:111], a[44:47], a[204:207], v[108:111]\n"
      "v_add_f32 v60, v60, v72\n"
      "v_add_f32_e64 v61, v61, v73\n"
      "v_add_f32_e64 v62, v62, v74\n"
      "v_add_f32_e64 v63, v63, v75\n"
      "v_mfma_f32_16x16x32_bf16 v[100:103], a[48:51], a[208:211], v[100:103]\n"
      "ds_read_b128 a[200:203], v5 offset:33920\n"
      "ds_write_addtid_b32 v60 offset:49408\n"
      "v_mfma_f32_16x16x32_bf16 v[104:107], a[52:55], a[208:211], v[104:107]\n"
      "ds_write_addtid_b32 v61 offset:49920\n"
      "ds_write_addtid_b32 v62 offset:50432\n"
      "v_mfma_f32_16x16x32_bf16 v[108:111], a[56:59], a[208:211], v[108:111]\n"
      "ds_write_addtid_b32 v63 offset:50944\n"
      "s_waitcnt lgkmcnt(6)\n"
      "v_mfma_f32_16x16x32_bf16 v[100:103], a[60:63], a[212:215], v[100:103]\n"
      "v_fma_f32 v40, -2.0, v64, v52\n"
      "v_fma_f32 v41, -2.0, v65, v53\n"
      "v_fma_f32 v42, -2.0, v66, v54\n"
      "v_fma_f32 v43, -2.0, v67, v55\n"
      "v_mfma_f32_16x16x32_bf16 v[104:107], a[64:67], a[212:215], v[104:107]\n"
      "v_fma_f32 v44, -2.0, v68, v56\n"
      "v_fma_f32 v45, -2.0, v69, v57\n"
      "v_fma_f32 v46, -2.0, v70, v58\n"
      "v_fma_f32 v47, -2.0, v71, v59\n"
      "v_mfma_f32_16x16x32_bf16 v[108:111], a[68:71], a[212:215], v[108:111]\n"
      "v_fma_f32 v48, -2.0, v72, v60\n"
      "v_fma_f32 v49, -2.0, v73, v61\n"
      "v_fma_f32 v50, -2.0, v74, v62\n"
      "v_fma_f32 v51, -2.0, v75, v63\n"
      "s_waitcnt lgkmcnt(0)\n"
      "s_waitcnt vmcnt(11)\n"
      "s_barrier\n"
      "v_mfma_f32_16x16x32_bf16 v[100:103], a[72:75], a[192:195], v[100:103]\n"
      "ds_read_b128 a[204:207], v0 offset:16384\n"
      "ds_read2st64_b32 v[28:29], v6 offset0:0 offset1:2\n"
      "v_mfma_f32_16x16x32_bf16 v[104:107], a[76:79], a[192:195], v[104:107]\n"
      "ds_read_b128 a[208:211], v0 offset:20480\n"
      "ds_read2st64_b32 v[30:31], v6 offset0:4 offset1:6\n"
      "s_add_u32 s58, s58, 3072\n"
      "v_mfma_f32_16x16x32_bf16 v[108:111], a[80:83], a[192:195], v[108:111]\n"
      "v_mfma_f32_16x16x32_bf16 v[100:103], a[84:87], a[196:199], v[100:103]\n"
      "ds_read_b128 a[212:215], v0 offset:16448\n"
      "ds_read2st64_b32 v[32:33], v6 offset0:8 offset1:10\n"
      "v_mfma_f32_16x16x32_bf16 v[104:107], a[88:91], a[196:199], v[104:107]\n"
      "ds_read2st64_b32 v[34:35], v6 offset0:12 offset1:14\n"
      "ds_read2st64_b32 v[36:37], v6 offset0:16 offset1:18\n"
      "v_mfma_f32_16x16x32_bf16 v[108:111], a[92:95], a[196:199], v[108:111]\n"
      "v_mfma_f32_16x16x32_bf16 v[100:103], a[96:99], a[200:203], v[100:103]\n"
      "ds_read_b128 a[192:195], v0 offset:20544\n"
      "ds_read2st64_b32 a[220:221], v12 offset0:181 offset1:183\n"
      "v_mfma_f32_16x16x32_bf16 v[104:107], a[100:103], a[200:203], v[104:107]\n"
      "ds_read2st64_b32 a[222:223], v15 offset0:181 offset1:183\n"
      "ds_read2st64_b32 v[38:39], v6 offset0:20 offset1:22\n"
      "s_waitcnt lgkmcnt(8)\n"
      "v_fmac_f32 v40, s46, v28\n"
      "v_fmac_f32_e64 v41, s46, v29\n"
      "v_mfma_f32_16x16x32_bf16 v[108:111], a[104:107], a[200:203], v[108:111]\n"
      "v_fmac_f32 v42, s46, v30\n"
      "v_fmac_f32 v43, s46, v31\n"
      "v_mfma_f32_16x16x32_bf16 v[52:55], v[252:255], a[204:207], 0\n"
      "ds_read_b128 a[196:199], v0 offset:16512\n"
      "v_fma_f32 v28, 0.5, v28, v76\n"
      "v_mfma_f32_16x16x32_bf16 v[56:59], v[244:247], a[204:207], 0\n"
      "v_fma_f32 v29, 0.5, v29, v77\n"
      "v_fma_f32 v30, 0.5, v30, v78\n"
      "v_mfma_f32_16x16x32_bf16 v[60:63], v[236:239], a[204:207], 0\n"
      "v_fma_f32 v31, 0.5, v31, v79\n"
      "v_fmac_f32 v40, s48, v100\n"
      "v_mfma_f32_16x16x32_bf16 v[64:67], v[248:251], a[208:211], 0\n"
      "ds_read_b128 a[200:203], v0 offset:20608\n"
      "v_fmac_f32 v41, s48, v101\n"
      "v_mfma_f32_16x16x32_bf16 v[68:71], v[240:243], a[208:211], 0\n"
      "v_fmac_f32 v42, s48, v102\n"
      "v_fmac_f32 v43, s48, v103\n"
      "v_mfma_f32_16x16x32_bf16 v[72:75], v[232:235], a[208:211], 0\n"
      "v_fmac_f32 v28, s45, v40\n"
      "v_fmac_f32 v29, s45, v41\n"
      "s_waitcnt lgkmcnt(2)\n"
      "v_mfma_f32_16x16x32_bf16 v[52:55], v[228:231], a[212:215], v[52:55]\n"
      "ds_read_b128 a[204:207], v1 offset:16384\n"
      "v_fmac_f32 v30, s45, v42\n"
      "v_mfma_f32_16x16x32_bf16 v[56:59], v[220:223], a[212:215], v[56:59]\n"
      "v_fmac_f32_e64 v31, s45, v43\n"
      "v_fma_f32 v40, s47, v40, -v100\n"
      "v_mfma_f32_16x16x32_bf16 v[60:63], v[212:215], a[212:215], v[60:63]\n"
      "v_fma_f32 v41, s47, v41, -v101\n"
      "v_fma_f32 v42, s47, v42, -v102\n"
      "v_mfma_f32_16x16x32_bf16 v[64:67], v[224:227], a[192:195], v[64:67]\n"
      "ds_read_b128 a[208:211], v1 offset:20480\n"
      "v_fma_f32 v43, s47, v43, -v103\n"
      "v_mfma_f32_16x16x32_bf16 v[68:71], v[216:219], a[192:195], v[68:71]\n"
      "v_cvt_pk_bf16_f32 v28, v28, v29\n"
      "v_cvt_pk_bf16_f32 v30, v30, v31\n"
      "v_mfma_f32_16x16x32_bf16 v[72:75], v[208:211], a[192:195], v[72:75]\n"
      "buffer_store_dwordx4 a[220:223], v18, s[8:11], s58 offen offset:0 sc1\n"
      "s_nop 0\n"
      "s_waitcnt lgkmcnt(1)\n"
      "v_mfma_f32_16x16x32_bf16 v[52:55], v[204:207], a[196:199], v[52:55]\n"
      "ds_read_b128 a[212:215], v1 offset:16448\n"
      "ds_write_addtid_b32 v28 offset:32768\n"
      "v_mfma_f32_16x16x32_bf16 v[56:59], v[196:199], a[196:199], v[56:59]\n"
      "ds_write_addtid_b32 v30 offset:33280\n"
      "ds_read2st64_b32 a[224:225], v13 offset0:181 offset1:183\n"
      "v_cvt_pk_bf16_f32 v40, v40, v41\n"
      "v_mfma_f32_16x16x32_bf16 v[60:63], v[188:191], a[196:199], v[60:63]\n"
      "ds_write_addtid_b32 v40 offset:34816\n"
      "ds_read2st64_b32 a[226:227], v16 offset0:181 offset1:183\n"
      "v_mfma_f32_16x16x32_bf16 v[64:67], v[200:203], a[200:203], v[64:67]\n"
      "ds_read_b128 a[192:195], v1 offset:20544\n"
      "v_cvt_pk_bf16_f32 v42, v42, v43\n"
      "v_mfma_f32_16x16x32_bf16 v[68:71], v[192:195], a[200:203], v[68:71]\n"
      "ds_write_addtid_b32 v42 offset:35328\n"
      "v_fmac_f32_e64 v44, s46, v32\n"
      "v_mfma_f32_16x16x32_bf16 v[72:75], v[184:187], a[200:203], v[72:75]\n"
      "v_fmac_f32_e64 v45, s46, v33\n"
      "v_fmac_f32_e64 v46, s46, v34\n"
      "v_mfma_f32_16x16x32_bf16 v[52:55], v[180:183], a[204:207], v[52:55]\n"
      "ds_read_b128 a[196:199], v1 offset:16512\n"
      "v_fmac_f32 v47, s46, v35\n"
      "v_mfma_f32_16x16x32_bf16 v[56:59], v[172:175], a[204:207], v[56:59]\n"
      "v_fma_f32 v32, 0.5, v32, v80\n"
      "v_fma_f32 v33, 0.5, v33, v81\n"
      "v_mfma_f32_16x16x32_bf16 v[60:63], v[164:167], a[204:207], v[60:63]\n"
      "v_fma_f32 v34, 0.5, v34, v82\n"
      "s_waitcnt lgkmcnt(3)\n"
      "v_mfma_f32_16x16x32_bf16 v[64:67], v[176:179], a[208:211], v[64:67]\n"
      "ds_read_b128 a[200:203], v1 offset:20608\n"
      "v_fma_f32 v35, 0.5, v35, v83\n"
      "v_mfma_f32_16x16x32_bf16 v[68:71], v[168:171], a[208:211], v[68:71]\n"
      "v_fmac_f32 v44, s48, v104\n"
      "v_fmac_f32 v45, s48, v105\n"
      "v_mfma_f32_16x16x32_bf16 v[72:75], v[160:163], a[208:211], v[72:75]\n"
      "v_fmac_f32 v46, s48, v106\n"
      "v_fmac_f32 v47, s48, v107\n"
      "v_mfma_f32_16x16x32_bf16 v[52:55], v[156:159], a[212:215], v[52:55]\n"
      "ds_read_b128 a[204:207], v2 offset:16384\n"
      "v_fmac_f32_e64 v32, s45, v44\n"
      "v_mfma_f32_16x16x32_bf16 v[56:59], v[148:151], a[212:215], v[56:59]\n"
      "v_fmac_f32 v33, s45, v45\n"
      "v_fmac_f32 v34, s45, v46\n"
      "v_mfma_f32_16x16x32_bf16 v[60:63], v[140:143], a[212:215], v[60:63]\n"
      "v_fmac_f32 v35, s45, v47\n"
      "s_waitcnt lgkmcnt(1)\n"
      "v_mfma_f32_16x16x32_bf16 v[64:67], v[152:155], a[192:195], v[64:67]\n"
      "buffer_store_dwordx4 a[224:227], v18, s[8:11], s58 offen offset:128 sc1\n"
      "ds_read_b128 a[208:211], v2 offset:20480\n"
      "v_mfma_f32_16x16x32_bf16 v[68:71], v[144:147], a[192:195], v[68:71]\n"
      "v_fma_f32 v44, s47, v44, -v104\n"
      "v_fma_f32 v45, s47, v45, -v105\n"
      "v_mfma_f32_16x16x32_bf16 v[72:75], v[136:139], a[192:195], v[72:75]\n"
      "ds_read2st64_b32 a[228:229], v14 offset0:181 offset1:183\n"
      "ds_read2st64_b32 a[230:231], v17 offset0:181 offset1:183\n"
      "v_fma_f32 v46, s47, v46, -v106\n"
      "v_mfma_f32_16x16x32_bf16 v[52:55], v[132:135], a[196:199], v[52:55]\n"
      "ds_read_b128 a[212:215], v2 offset:16448\n"
      "v_fma_f32 v47, s47, v47, -v107\n"
      "v_mfma_f32_16x16x32_bf16 v[56:59], v[124:127], a[196:199], v[56:59]\n"
      "v_cvt_pk_bf16_f32 v32, v32, v33\n"
      "v_cvt_pk_bf16_f32 v34, v34, v35\n"
      "v_mfma_f32_16x16x32_bf16 v[60:63], a[184:187], a[196:199], v[60:63]\n"
      "ds_write_addtid_b32 v32 offset:40964\n"
      "ds_write_addtid_b32 v34 offset:41476\n"
      "v_mfma_f32_16x16x32_bf16 v[64:67], v[128:131], a[200:203], v[64:67]\n"
      "ds_read_b128 a[192:195], v2 offset:20544\n"
      "v_cvt_pk_bf16_f32 v44, v44, v45\n"
      "v_mfma_f32_16x16x32_bf16 v[68:71], a[188:191], a[200:203], v[68:71]\n"
      "ds_write_addtid_b32 v44 offset:43012\n"
      "v_cvt_pk_bf16_f32 v46, v46, v47\n"
      "v_mfma_f32_16x16x32_bf16 v[72:75], a[180:183], a[200:203], v[72:75]\n"
      "ds_write_addtid_b32 v46 offset:43524\n"
      "v_fmac_f32 v48, s46, v36\n"
      "s_waitcnt lgkmcnt(6)\n"
      "v_mfma_f32_16x16x32_bf16 v[52:55], a[176:179], a[204:207], v[52:55]\n"
      "ds_read_b128 a[196:199], v2 offset:16512\n"
      "v_fmac_f32 v49, s46, v37\n"
      "v_mfma_f32_16x16x32_bf16 v[56:59], a[168:171], a[204:207], v[56:59]\n"
      "v_fmac_f32 v50, s46, v38\n"
      "v_fmac_f32_e64 v51, s46, v39\n"
      "v_mfma_f32_16x16x32_bf16 v[60:63], a[160:163], a[204:207], v[60:63]\n"
      "v_fma_f32 v36, 0.5, v36, v84\n"
      "v_fma_f32 v37, 0.5, v37, v85\n"
      "v_mfma_f32_16x16x32_bf16 v[64:67], a[172:175], a[208:211], v[64:67]\n"
      "ds_read_b128 a[200:203], v2 offset:20608\n"
      "v_fma_f32 v38, 0.5, v38, v86\n"
      "v_mfma_f32_16x16x32_bf16 v[68:71], a[164:167], a[208:211], v[68:71]\n"
      "v_fma_f32 v39, 0.5, v39, v87\n"
      "v_fmac_f32 v48, s48, v108\n"
      "v_mfma_f32_16x16x32_bf16 v[72:75], a[156:159], a[208:211], v[72:75]\n"
      "v_fmac_f32_e64 v49, s48, v109\n"
      "s_waitcnt lgkmcnt(1)\n"
      "v_mfma_f32_16x16x32_bf16 v[52:55], a[152:155], a[212:215], v[52:55]\n"
      "ds_read_b128 a[204:207], v3 offset:50688\n"
      "v_fmac_f32 v50, s48, v110\n"
      "v_mfma_f32_16x16x32_bf16 v[56:59], a[144:147], a[212:215], v[56:59]\n"
      "buffer_store_dwordx4 a[228:231], v18, s[8:11], s58 offen offset:256 sc1\n"
      "v_mfma_f32_16x16x32_bf16 v[60:63], a[136:139], a[212:215], v[60:63]\n"
      "v_fmac_f32 v51, s48, v111\n"
      "v_fmac_f32_e64 v36, s45, v48\n"
      "v_mfma_f32_16x16x32_bf16 v[64:67], a[148:151], a[192:195], v[64:67]\n"
      "ds_read_b128 a[208:211], v3 offset:50752\n"
      "v_fmac_f32 v37, s45, v49\n"
      "v_mfma_f32_16x16x32_bf16 v[68:71], a[140:143], a[192:195], v[68:71]\n"
      "v_fmac_f32 v38, s45, v50\n"
      "v_fmac_f32_e64 v39, s45, v51\n"
      "v_mfma_f32_16x16x32_bf16 v[72:75], a[132:135], a[192:195], v[72:75]\n"
      "v_fma_f32 v48, s47, v48, -v108\n"
      "v_fma_f32 v49, s47, v49, -v109\n"
      "v_mfma_f32_16x16x32_bf16 v[52:55], a[128:131], a[196:199], v[52:55]\n"
      "ds_read_b128 a[212:215], v3 offset:50816\n"
      "v_fma_f32 v50, s47, v50, -v110\n"
      "v_mfma_f32_16x16x32_bf16 v[56:59], a[120:123], a[196:199], v[56:59]\n"
      "v_fma_f32 v51, s47, v51, -v111\n"
      "v_cvt_pk_bf16_f32 v36, v36, v37\n"
      "v_mfma_f32_16x16x32_bf16 v[60:63], a[112:115], a[196:199], v[60:63]\n"
      "ds_write_addtid_b32 v36 offset:36864\n"
      "s_nop 0\n"
      "s_waitcnt lgkmcnt(2)\n"
      "v_mfma_f32_16x16x32_bf16 v[64:67], a[124:127], a[200:203], v[64:67]\n"
      "ds_read_b128 a[192:195], v4 offset:50688\n"
      "v_cvt_pk_bf16_f32 v38, v38, v39\n"
      "v_mfma_f32_16x16x32_bf16 v[68:71], a[116:119], a[200:203], v[68:71]\n"
      "ds_write_addtid_b32 v38 offset:37376\n"
      "v_cvt_pk_bf16_f32 v48, v48, v49\n"
      "v_mfma_f32_16x16x32_bf16 v[72:75], a[108:111], a[200:203], v[72:75]\n"
      "ds_write_addtid_b32 v48 offset:38912\n"
      "v_cvt_pk_bf16_f32 v50, v50, v51\n"
      "v_add_f32 v52, v52, v64\n"
      "v_mfma_f32_16x16x32_bf16 v[112:115], a[0:3], a[204:207], 0\n"
      "ds_read_b128 a[196:199], v4 offset:50752\n"
      "ds_write_addtid_b32 v50 offset:39424\n"
      "v_mfma_f32_16x16x32_bf16 v[116:119], a[4:7], a[204:207], 0\n"
      "ds_write_addtid_b32 v52 offset:51456\n"
      "v_add_f32 v53, v53, v65\n"
      "v_mfma_f32_16x16x32_bf16 v[120:123], a[8:11], a[204:207], 0\n"
      "ds_write_addtid_b32 v53 offset:51968\n"
      "v_add_f32 v54, v54, v66\n"
      "v_mfma_f32_16x16x32_bf16 v[112:115], a[12:15], a[208:211], v[112:115]\n"
      "ds_read_b128 a[200:203], v4 offset:50816\n"
      "ds_write_addtid_b32 v54 offset:52480\n"
      "v_mfma_f32_16x16x32_bf16 v[116:119], a[16:19], a[208:211], v[116:119]\n"
      "v_add_f32 v55, v55, v67\n"
      "v_add_f32_e64 v56, v56, v68\n"
      "v_mfma_f32_16x16x32_bf16 v[120:123], a[20:23], a[208:211], v[120:123]\n"
      "ds_write_addtid_b32 v55 offset:52992\n"
      "ds_write_addtid_b32 v56 offset:53504\n"
      "s_waitcnt lgkmcnt(5)\n"
      "v_mfma_f32_16x16x32_bf16 v[112:115], a[24:27], a[212:215], v[112:115]\n"
      "ds_read_b128 a[204:207], v5 offset:50688\n"
      "v_add_f32 v57, v57, v69\n"
      "v_mfma_f32_16x16x32_bf16 v[116:119], a[28:31], a[212:215], v[116:119]\n"
      "ds_write_addtid_b32 v57 offset:54016\n"
      "v_add_f32_e64 v58, v58, v70\n"
      "v_mfma_f32_16x16x32_bf16 v[120:123], a[32:35], a[212:215], v[120:123]\n"
      "ds_write_addtid_b32 v58 offset:54528\n"
      "v_add_f32 v59, v59, v71\n"
      "v_mfma_f32_16x16x32_bf16 v[112:115], a[36:39], a[192:195], v[112:115]\n"
      "ds_read_b128 a[208:211], v5 offset:50752\n"
      "ds_write_addtid_b32 v59 offset:55040\n"
      "v_mfma_f32_16x16x32_bf16 v[116:119], a[40:43], a[192:195], v[116:119]\n"
      "v_add_f32 v60, v60, v72\n"
      "v_add_f32_e64 v61, v61, v73\n"
      "v_mfma_f32_16x16x32_bf16 v[120:123], a[44:47], a[192:195], v[120:123]\n"
      "ds_write_addtid_b32 v60 offset:55552\n"
      "ds_write_addtid_b32 v61 offset:56064\n"
      "v_add_f32_e64 v62, v62, v74\n"
      "v_add_f32_e64 v63, v63, v75\n"
      "v_mfma_f32_16x16x32_bf16 v[112:115], a[48:51], a[196:199], v[112:115]\n"
      "ds_read_b128 a[212:215], v5 offset:50816\n"
      "ds_write_addtid_b32 v62 offset:56576\n"
      "v_mfma_f32_16x16x32_bf16 v[116:119], a[52:55], a[196:199], v[116:119]\n"
      "ds_write_addtid_b32 v63 offset:57088\n"
      "v_mfma_f32_16x16x32_bf16 v[120:123], a[56:59], a[196:199], v[120:123]\n"
      "s_nop 0\n"
      "s_waitcnt lgkmcnt(5)\n"
      "v_mfma_f32_16x16x32_bf16 v[112:115], a[60:63], a[200:203], v[112:115]\n"
      "v_fma_f32 v40, -2.0, v64, v52\n"
      "v_fma_f32 v41, -2.0, v65, v53\n"
      "v_fma_f32 v42, -2.0, v66, v54\n"
      "v_fma_f32 v43, -2.0, v67, v55\n"
      "v_fma_f32 v44, -2.0, v68, v56\n"
      "v_mfma_f32_16x16x32_bf16 v[116:119], a[64:67], a[200:203], v[116:119]\n"
      "v_fma_f32 v45, -2.0, v69, v57\n"
      "v_fma_f32 v46, -2.0, v70, v58\n"
      "v_fma_f32 v47, -2.0, v71, v59\n"
      "v_fma_f32 v48, -2.0, v72, v60\n"
      "v_mfma_f32_16x16x32_bf16 v[120:123], a[68:71], a[200:203], v[120:123]\n"
      "v_fma_f32 v49, -2.0, v73, v61\n"
      "v_fma_f32 v50, -2.0, v74, v62\n"
      "v_fma_f32 v51, -2.0, v75, v63\n"
      "s_waitcnt lgkmcnt(0)\n"
      "s_barrier\n"
      "v_mfma_f32_16x16x32_bf16 v[112:115], a[72:75], a[204:207], v[112:115]\n"
      "ds_read2st64_b32 v[28:29], v6 offset0:24 offset1:26\n"
      "ds_read2st64_b32 a[220:221], v12 offset0:177 offset1:179\n"
      "s_add_u32 s62, s62, 786432\n"
      "v_mfma_f32_16x16x32_bf16 v[116:119], a[76:79], a[204:207], v[116:119]\n"
      "ds_read2st64_b32 v[30:31], v6 offset0:28 offset1:30\n"
      "ds_read2st64_b32 a[224:225], v13 offset0:177 offset1:179\n"
      "v_mfma_f32_16x16x32_bf16 v[120:123], a[80:83], a[204:207], v[120:123]\n"
      "ds_read2st64_b32 a[222:223], v15 offset0:177 offset1:179\n"
      "ds_read2st64_b32 v[32:33], v6 offset0:32 offset1:34\n"
      "s_mov_b32 s58, s62\n"
      "v_mfma_f32_16x16x32_bf16 v[112:115], a[84:87], a[208:211], v[112:115]\n"
      "ds_read2st64_b32 v[34:35], v6 offset0:36 offset1:38\n"
      "v_mfma_f32_16x16x32_bf16 v[116:119], a[88:91], a[208:211], v[116:119]\n"
      "ds_read2st64_b32 v[36:37], v6 offset0:40 offset1:42\n"
      "ds_read2st64_b32 v[38:39], v6 offset0:44 offset1:46\n"
      "ds_read2st64_b32 a[226:227], v16 offset0:177 offset1:179\n"
      "v_mfma_f32_16x16x32_bf16 v[120:123], a[92:95], a[208:211], v[120:123]\n"
      "v_mfma_f32_16x16x32_bf16 v[112:115], a[96:99], a[212:215], v[112:115]\n"
      "s_waitcnt lgkmcnt(5)\n"
      "buffer_store_dwordx4 a[220:223], v18, s[8:11], s58 offen offset:0 sc1\n"
      "ds_read2st64_b32 a[228:229], v14 offset0:177 offset1:179\n"
      "ds_read2st64_b32 a[230:231], v17 offset0:177 offset1:179\n"
      "v_mfma_f32_16x16x32_bf16 v[116:119], a[100:103], a[212:215], v[116:119]\n"
      "v_fmac_f32 v40, s46, v28\n"
      "v_fmac_f32_e64 v41, s46, v29\n"
      "v_mfma_f32_16x16x32_bf16 v[120:123], a[104:107], a[212:215], v[120:123]\n"
      "s_waitcnt lgkmcnt(2)\n"
      "buffer_store_dwordx4 a[224:227], v18, s[8:11], s58 offen offset:128 sc1\n"
      "v_fmac_f32_e64 v42, s46, v30\n"
      "v_fmac_f32 v43, s46, v31\n"
      "v_fma_f32 v28, 0.5, v28, v88\n"
      "v_fma_f32 v29, 0.5, v29, v89\n"
      "v_fma_f32 v30, 0.5, v30, v90\n"
      "v_fma_f32 v31, 0.5, v31, v91\n"
      "v_pk_fma_f32 v[40:41], v[112:113], s[48:49], v[40:41] op_sel:[0,0,0] op_sel_hi:[1,0,1]\n"
      "s_waitcnt lgkmcnt(0)\n"
      "buffer_store_dwordx4 a[228:231], v18, s[8:11], s58 offen offset:256 sc1\n"
      "v_pk_fma_f32 v[42:43], v[114:115], s[48:49], v[42:43] op_sel:[0,0,0] op_sel_hi:[1,0,1]\n"
      "v_pk_fma_f32 v[28:29], v[40:41], s[44:45], v[28:29] op_sel:[0,1,0] op_sel_hi:[1,1,1]\n"
      "v_pk_fma_f32 v[30:31], v[42:43], s[44:45], v[30:31] op_sel:[0,1,0] op_sel_hi:[1,1,1]\n"
      "v_pk_fma_f32 v[40:41], v[40:41], s[46:47], v[112:113] op_sel:[0,1,0] op_sel_hi:[1,1,1] neg_lo:[0,0,1] neg_hi:[0,0,1]\n"
      "v_pk_fma_f32 v[42:43], v[42:43], s[46:47], v[114:115] op_sel:[0,1,0] op_sel_hi:[1,1,1] neg_lo:[0,0,1] neg_hi:[0,0,1]\n"
      "v_cvt_pk_bf16_f32 v28, v28, v29\n"
      "v_cvt_pk_bf16_f32 v30, v30, v31\n"
      "v_cvt_pk_bf16_f32 v40, v40, v41\n"
      "v_cvt_pk_bf16_f32 v42, v42, v43\n"
      "v_pk_fma_f32 v[44:45], v[32:33], s[46:47], v[44:45] op_sel:[0,0,0] op_sel_hi:[1,0,1]\n"
      "ds_write_addtid_b32 v28 offset:33792\n"
      "ds_write_addtid_b32 v30 offset:34304\n"
      "ds_write_addtid_b32 v40 offset:35840\n"
      "v_pk_fma_f32 v[46:47], v[34:35], s[46:47], v[46:47] op_sel:[0,0,0] op_sel_hi:[1,0,1]\n"
      "v_fma_f32 v32, 0.5, v32, v92\n"
      "ds_write_addtid_b32 v42 offset:36352\n"
      "v_fma_f32 v33, 0.5, v33, v93\n"
      "v_pk_fma_f32 v[34:35], v[34:35], s[48:49], v[94:95] op_sel:[0,1,0] op_sel_hi:[1,1,1]\n"
      "v_pk_fma_f32 v[44:45], v[116:117], s[48:49], v[44:45] op_sel:[0,0,0] op_sel_hi:[1,0,1]\n"
      "v_pk_fma_f32 v[46:47], v[118:119], s[48:49], v[46:47] op_sel:[0,0,0] op_sel_hi:[1,0,1]\n"
      "v_pk_fma_f32 v[32:33], v[44:45], s[44:45], v[32:33] op_sel:[0,1,0] op_sel_hi:[1,1,1]\n"
      "v_pk_fma_f32 v[34:35], v[46:47], s[44:45], v[34:35] op_sel:[0,1,0] op_sel_hi:[1,1,1]\n"
      "v_pk_fma_f32 v[44:45], v[44:45], s[46:47], v[116:117] op_sel:[0,1,0] op_sel_hi:[1,1,1] neg_lo:[0,0,1] neg_hi:[0,0,1]\n"
      "v_pk_fma_f32 v[46:47], v[46:47], s[46:47], v[118:119] op_sel:[0,1,0] op_sel_hi:[1,1,1] neg_lo:[0,0,1] neg_hi:[0,0,1]\n"
      "v_cvt_pk_bf16_f32 v32, v32, v33\n"
      "v_cvt_pk_bf16_f32 v34, v34, v35\n"
      "v_cvt_pk_bf16_f32 v44, v44, v45\n"
      "v_cvt_pk_bf16_f32 v46, v46, v47\n"
      "v_pk_fma_f32 v[48:49], v[36:37], s[46:47], v[48:49] op_sel:[0,0,0] op_sel_hi:[1,0,1]\n"
      "ds_write_addtid_b32 v32 offset:41988\n"
      "ds_write_addtid_b32 v34 offset:42500\n"
      "ds_write_addtid_b32 v44 offset:44036\n"
      "v_pk_fma_f32 v[50:51], v[38:39], s[46:47], v[50:51] op_sel:[0,0,0] op_sel_hi:[1,0,1]\n"
      "v_fma_f32 v36, 0.5, v36, v96\n"
      "ds_write_addtid_b32 v46 offset:44548\n"
      "v_fma_f32 v37, 0.5, v37, v97\n"
      "v_pk_fma_f32 v[38:39], v[38:39], s[48:49], v[98:99] op_sel:[0,1,0] op_sel_hi:[1,1,1]\n"
      "v_pk_fma_f32 v[48:49], v[120:121], s[48:49], v[48:49] op_sel:[0,0,0] op_sel_hi:[1,0,1]\n"
      "v_pk_fma_f32 v[50:51], v[122:123], s[48:49], v[50:51] op_sel:[0,0,0] op_sel_hi:[1,0,1]\n"
      "v_pk_fma_f32 v[36:37], v[48:49], s[44:45], v[36:37] op_sel:[0,1,0] op_sel_hi:[1,1,1]\n"
      "v_pk_fma_f32 v[38:39], v[50:51], s[44:45], v[38:39] op_sel:[0,1,0] op_sel_hi:[1,1,1]\n"
      "v_pk_fma_f32 v[48:49], v[48:49], s[46:47], v[120:121] op_sel:[0,1,0] op_sel_hi:[1,1,1] neg_lo:[0,0,1] neg_hi:[0,0,1]\n"
      "v_pk_fma_f32 v[50:51], v[50:51], s[46:47], v[122:123] op_sel:[0,1,0] op_sel_hi:[1,1,1] neg_lo:[0,0,1] neg_hi:[0,0,1]\n"
      "v_cvt_pk_bf16_f32 v36, v36, v37\n"
      "v_cvt_pk_bf16_f32 v38, v38, v39\n"
      "v_cvt_pk_bf16_f32 v48, v48, v49\n"
      "v_cvt_pk_bf16_f32 v50, v50, v51\n"
      "ds_write_addtid_b32 v36 offset:37888\n"
      "ds_write_addtid_b32 v38 offset:38400\n"
      "ds_write_addtid_b32 v48 offset:39936\n"
      "ds_write_addtid_b32 v50 offset:40448\n"
      "s_waitcnt lgkmcnt(0)\n"
      "s_barrier\n"
      "ds_read2st64_b32 a[220:221], v12 offset0:181 offset1:183\n"
      "ds_read2st64_b32 a[222:223], v15 offset0:181 offset1:183\n"
      "ds_read2st64_b32 a[224:225], v13 offset0:181 offset1:183\n"
      "ds_read2st64_b32 a[226:227], v16 offset0:181 offset1:183\n"
      "ds_read2st64_b32 a[228:229], v14 offset0:181 offset1:183\n"
      "ds_read2st64_b32 a[230:231], v17 offset0:181 offset1:183\n"
      "s_add_u32 s58, s58, 3072\n"
      "s_waitcnt lgkmcnt(4)\n"
      "buffer_store_dwordx4 a[220:223], v18, s[8:11], s58 offen offset:0 sc1\n"
      "s_waitcnt lgkmcnt(2)\n"
      "buffer_store_dwordx4 a[224:227], v18, s[8:11], s58 offen offset:128 sc1\n"
      "s_waitcnt lgkmcnt(0)\n"
      "buffer_store_dwordx4 a[228:231], v18, s[8:11], s58 offen offset:256 sc1\n"
      "LDend_%=:\n"
      "s_branch Lfin_%=\n"
      ".p2align 6\n"
      "LS_%=:\n"
      "v_lshlrev_b32 v43, 4, v37\n"
      "v_lshlrev_b32 v44, 2, v37\n"
      "v_and_b32 v43, 16, v43\n"
      "v_and_or_b32 v43, v44, 8, v43\n"
      "v_mul_u32_u24 v44, 192, v38\n"
      "v_mul_u32_u24 v45, 1536, v39\n"
      "s_lshl_b32 s27, s18, 5\n"
      "v_lshlrev_b32 v46, 1, v36\n"
      "v_add3_u32 v43, v43, v44, v45\n"
      "v_bitop3_b32 v46, v46, s27, 32 bitop3:0x6c\n"
      "v_add_u32 v95, v43, v46\n"
      "v_add_u32 v96, 57344, v95\n"
      "v_add_u32 v97, 114688, v95\n"
      "s_lshl_b32 s26, s18, 3\n"
      "v_and_b32 v42, 15, v36\n"
      "v_xor_b32 v42, s26, v42\n"
      "s_lshl_b32 s26, s17, 13\n"
      "s_add_u32 s26, s26, 24832\n"
      "s_mul_i32 s27, s17, 12672\n"
      "v_mul_u32_u24 v44, 192, v42\n"
      "v_lshlrev_b32 v43, 3, v42\n"
      "v_and_b32 v45, 48, v36\n"
      "s_add_u32 s27, s27, 82432\n"
      "v_bitop3_b32 v46, v43, v45, 32 bitop3:0x6c\n"
      "v_add3_u32 v0, v46, v44, s26\n"
      "v_add3_u32 v3, v46, v44, s27\n"
      "v_add_u32 v43, 16, v43\n"
      "v_add_u32 v44, 384, v44\n"
      "v_bitop3_b32 v46, v43, v45, 32 bitop3:0x6c\n"
      "v_add3_u32 v1, v46, v44, s26\n"
      "v_add3_u32 v4, v46, v44, s27\n"
      "v_add_u32 v43, 16, v43\n"
      "v_add_u32 v44, 384, v44\n"
      "v_bitop3_b32 v46, v43, v45, 32 bitop3:0x6c\n"
      "v_add3_u32 v2, v46, v44, s26\n"
      "v_add3_u32 v5, v46, v44, s27\n"
      "v_lshlrev_b32 v43, 31, v36\n"
      "v_and_b32 v44, 2, v36\n"
      "v_or_b32 v43, 0x3f803f80, v43\n"
      "v_mul_u32_u24 v45, 0xc00080, v44\n"
      "v_lshl_add_u32 v44, v44, 22, v43\n"
      "v_add_u32 v45, v45, v43\n"
      "v_accvgpr_write_b32 a216, v44\n"
      "v_accvgpr_write_b32 a217, v45\n"
      "v_lshlrev_b32 v43, 3, v38\n"
      "s_mov_b32 s26, 806420736\n"
      "v_bfe_u32 v43, s26, v43, 8\n"
      "v_lshlrev_b32 v44, 12, v38\n"
      "s_lshl_b32 s28, s18, 8\n"
      "s_add_u32 s28, s28, 82432\n"
      "v_lshl_add_u32 v43, v43, 3, v44\n"
      "v_lshl_add_u32 v44, v39, 2, v37\n"
      "v_lshl_add_u32 v43, v44, 4, v43\n"
      "v_add_u32 v7, s28, v43\n"
      "s_mul_i32 s26, s17, 70144\n"
      "s_lshl_b32 s27, s18, 8\n"
      "s_sub_u32 s26, 70144, s26\n"
      "s_add_u32 s26, s26, s27\n"
      "v_lshl_add_u32 v6, v36, 2, s26\n"
      "v_lshrrev_b32 v43, 3, v36\n"
      "v_and_b32 v44, 7, v36\n"
      "v_mul_u32_u24 v45, 384, v43\n"
      "v_lshl_add_u32 v11, v44, 4, v45\n"
      "v_and_b32 v44, 2, v36\n"
      "v_lshlrev_b32 v43, 3, v43\n"
      "v_lshlrev_b32 v45, 4, v44\n"
      "v_xor_b32 v43, v43, v45\n"
      "v_lshl_add_u32 v43, v44, 7, v43\n"
      "v_and_b32 v44, 1, v36\n"
      "s_lshl_b32 s26, s18, 11\n"
      "v_lshl_add_u32 v43, v44, 6, v43\n"
      "s_add_u32 s26, s26, 12288\n"
      "v_bfe_u32 v44, v36, 2, 1\n"
      "v_add_u32 v43, s26, v43\n"
      "v_mul_i32_i24 v45, 8196, v44\n"
      "s_mov_b32 s27, 0\n"
      "v_add3_u32 v8, v43, v45, s27\n"
      "v_mul_i32_i24 v45, -4092, v44\n"
      "s_mov_b32 s27, 4096\n"
      "v_add3_u32 v9, v43, v45, s27\n"
      "v_mul_i32_i24 v45, -4100, v44\n"
      "s_mov_b32 s27, 8200\n"
      "v_add3_u32 v10, v43, v45, s27\n"
      "s_mul_i32 s62, s18, 393216\n"
      "s_add_u32 s62, s62, s19\n"
      "s_add_u32 s62, s62, s21\n"
      "s_sub_u32 s62, s62, 786432\n"
      "s_mov_b32 s58, 0x80000000\n"
      "s_and_b32 s9, s9, 0xffff\n"
      "s_mov_b32 s45, 0xbeaaaaab\n"
      "s_mov_b32 s46, 0xbe800000\n"
      "s_mov_b32 s47, 0x3eaaaaab\n"
      "s_add_u32 m0, s63, 122368\n"
      "s_add_u32 s52, s63, 61440\n"
      "buffer_load_dwordx4 v92, s[12:15], s52 offen offset:0 lds\n"
      "buffer_load_dwordx4 v93, s[12:15], s52 offen offset:1024 lds\n"
      "buffer_load_dwordx4 v94, s[12:15], s52 offen offset:2048 lds\n"
      "s_add_u32 m0, s63, 128512\n"
      "s_add_u32 s52, s63, 116736\n"
      "buffer_load_dwordx4 v92, s[12:15], s52 offen offset:0 lds\n"
      "buffer_load_dwordx4 v93, s[12:15], s52 offen offset:1024 lds\n"
      "buffer_load_dwordx4 v94, s[12:15], s52 offen offset:2048 lds\n"
      "LSpro_%=:\n"
      "s_waitcnt vmcnt(6)\n"
      "s_nop 0\n"
      "s_barrier\n"
      "ds_read_b64_tr_b16 a[224:225], v95 offset:24832\n"
      "ds_read_b64_tr_b16 a[226:227], v95 offset:25600\n"
      "ds_read_b64_tr_b16 a[0:1], v95 offset:0\n"
      "ds_read_b64_tr_b16 a[2:3], v95 offset:768\n"
      "ds_read_b64_tr_b16 a[220:221], v95 offset:30976\n"
      "ds_read_b64_tr_b16 a[222:223], v95 offset:31744\n"
      "ds_read_b64_tr_b16 a[232:233], v95 offset:24896\n"
      "ds_read_b64_tr_b16 a[234:235], v95 offset:25664\n"
      "ds_read_b64_tr_b16 a[4:5], v95 offset:64\n"
      "ds_read_b64_tr_b16 a[6:7], v95 offset:832\n"
      "ds_read_b64_tr_b16 a[228:229], v95 offset:31040\n"
      "ds_read_b64_tr_b16 a[230:231], v95 offset:31808\n"
      "s_add_u32 m0, s63, 6144\n"
      "s_add_u32 s52, s63, 12288\n"
      "s_waitcnt lgkmcnt(10)\n"
      "v_mfma_f32_4x4x4_16b_bf16 v[28:31], a[244:245], a[224:225], 0\n"
      "v_mfma_f32_4x4x4_16b_bf16 v[32:35], a[244:245], a[226:227], 0\n"
      "buffer_load_dwordx4 v92, s[12:15], s52 offen offset:0 lds\n"
      "s_waitcnt lgkmcnt(8)\n"
      "v_mfma_f32_4x4x4_16b_bf16 v[28:31], a[248:249], a[0:1], v[28:31]\n"
      "buffer_load_dwordx4 v93, s[12:15], s52 offen offset:1024 lds\n"
      "v_mfma_f32_4x4x4_16b_bf16 v[32:35], a[248:249], a[2:3], v[32:35]\n"
      "buffer_load_dwordx4 v94, s[12:15], s52 offen offset:2048 lds\n"
      "s_waitcnt lgkmcnt(6)\n"
      "v_mfma_f32_4x4x4_16b_bf16 v[36:39], a[246:247], a[220:221], v[28:31]\n"
      "ds_read_b64_tr_b16 a[240:241], v95 offset:24960\n"
      "v_mfma_f32_4x4x4_16b_bf16 v[40:43], a[246:247], a[222:223], v[32:35]\n"
      "ds_read_b64_tr_b16 a[242:243], v95 offset:25728\n"
      "v_mfma_f32_4x4x4_16b_bf16 v[28:31], a[250:251], a[220:221], v[28:31]\n"
      "ds_read_b64_tr_b16 a[8:9], v95 offset:128\n"
      "v_mfma_f32_4x4x4_16b_bf16 v[32:35], a[250:251], a[222:223], v[32:35]\n"
      "ds_read_b64_tr_b16 a[10:11], v95 offset:896\n"
      "ds_read_b64_tr_b16 a[236:237], v95 offset:31104\n"
      "ds_read_b64_tr_b16 a[238:239], v95 offset:31872\n"
      "s_add_u32 m0, s63, 12288\n"
      "s_add_u32 s52, s63, 67584\n"
      "s_waitcnt lgkmcnt(6)\n"
      "v_mfma_f32_4x4x4_16b_bf16 v[76:79], a[244:245], a[232:233], 0\n"
      "v_mfma_f32_4x4x4_16b_bf16 v[80:83], a[244:245], a[234:235], 0\n"
      "buffer_load_dwordx4 v92, s[12:15], s52 offen offset:0 lds\n"
      "v_mfma_f32_4x4x4_16b_bf16 v[76:79], a[248:249], a[4:5], v[76:79]\n"
      "buffer_load_dwordx4 v93, s[12:15], s52 offen offset:1024 lds\n"
      "v_mfma_f32_4x4x4_16b_bf16 v[80:83], a[248:249], a[6:7], v[80:83]\n"
      "buffer_load_dwordx4 v94, s[12:15], s52 offen offset:2048 lds\n"
      "v_mfma_f32_4x4x4_16b_bf16 v[84:87], a[246:247], a[228:229], v[76:79]\n"
      "v_mfma_f32_4x4x4_16b_bf16 v[88:91], a[246:247], a[230:231], v[80:83]\n"
      "v_mfma_f32_4x4x4_16b_bf16 v[76:79], a[250:251], a[228:229], v[76:79]\n"
      "v_mfma_f32_4x4x4_16b_bf16 v[80:83], a[250:251], a[230:231], v[80:83]\n"
      "v_cvt_pk_bf16_f32 v252, v36, v40\n"
      "v_cvt_pk_bf16_f32 v253, v37, v41\n"
      "v_cvt_pk_bf16_f32 v254, v38, v42\n"
      "v_cvt_pk_bf16_f32 v255, v39, v43\n"
      "v_cvt_pk_bf16_f32 v248, v28, v32\n"
      "v_cvt_pk_bf16_f32 v249, v29, v33\n"
      "v_cvt_pk_bf16_f32 v250, v30, v34\n"
      "v_cvt_pk_bf16_f32 v251, v31, v35\n"
      "s_add_u32 m0, s63, 41216\n"
      "s_add_u32 s52, s63, 18432\n"
      "s_nop 0\n"
      "s_waitcnt lgkmcnt(0)\n"
      "v_mfma_f32_4x4x4_16b_bf16 v[28:31], a[244:245], a[240:241], 0\n"
      "v_mfma_f32_4x4x4_16b_bf16 v[32:35], a[244:245], a[242:243], 0\n"
      "buffer_load_dwordx4 v92, s[12:15], s52 offen offset:0 lds\n"
      "v_mfma_f32_4x4x4_16b_bf16 v[28:31], a[248:249], a[8:9], v[28:31]\n"
      "buffer_load_dwordx4 v93, s[12:15], s52 offen offset:1024 lds\n"
      "v_mfma_f32_4x4x4_16b_bf16 v[32:35], a[248:249], a[10:11], v[32:35]\n"
      "buffer_load_dwordx4 v94, s[12:15], s52 offen offset:2048 lds\n"
      "v_mfma_f32_4x4x4_16b_bf16 v[36:39], a[246:247], a[236:237], v[28:31]\n"
      "v_mfma_f32_4x4x4_16b_bf16 v[40:43], a[246:247], a[238:239], v[32:35]\n"
      "v_mfma_f32_4x4x4_16b_bf16 v[28:31], a[250:251], a[236:237], v[28:31]\n"
      "v_mfma_f32_4x4x4_16b_bf16 v[32:35], a[250:251], a[238:239], v[32:35]\n"
      "v_cvt_pk_bf16_f32 v244, v84, v88\n"
      "v_cvt_pk_bf16_f32 v245, v85, v89\n"
      "v_cvt_pk_bf16_f32 v246, v86, v90\n"
      "v_cvt_pk_bf16_f32 v247, v87, v91\n"
      "v_cvt_pk_bf16_f32 v240, v76, v80\n"
      "v_cvt_pk_bf16_f32 v241, v77, v81\n"
      "v_cvt_pk_bf16_f32 v242, v78, v82\n"
      "v_cvt_pk_bf16_f32 v243, v79, v83\n"
      "s_waitcnt vmcnt(9)\n"
      "s_barrier\n"
      "ds_read_b64_tr_b16 a[224:225], v97 offset:1536\n"
      "ds_read_b64_tr_b16 a[226:227], v97 offset:2304\n"
      "ds_read_b64_tr_b16 a[12:13], v97 offset:13824\n"
      "ds_read_b64_tr_b16 a[14:15], v97 offset:14592\n"
      "ds_read_b64_tr_b16 a[220:221], v97 offset:7680\n"
      "ds_read_b64_tr_b16 a[222:223], v97 offset:8448\n"
      "s_add_u32 m0, s63, 47360\n"
      "s_add_u32 s52, s63, 73728\n"
      "s_waitcnt lgkmcnt(4)\n"
      "v_mfma_f32_4x4x4_16b_bf16 v[76:79], a[244:245], a[224:225], 0\n"
      "v_mfma_f32_4x4x4_16b_bf16 v[80:83], a[244:245], a[226:227], 0\n"
      "buffer_load_dwordx4 v92, s[12:15], s52 offen offset:0 lds\n"
      "s_waitcnt lgkmcnt(2)\n"
      "v_mfma_f32_4x4x4_16b_bf16 v[76:79], a[248:249], a[12:13], v[76:79]\n"
      "buffer_load_dwordx4 v93, s[12:15], s52 offen offset:1024 lds\n"
      "v_mfma_f32_4x4x4_16b_bf16 v[80:83], a[248:249], a[14:15], v[80:83]\n"
      "buffer_load_dwordx4 v94, s[12:15], s52 offen offset:2048 lds\n"
      "s_nop 0\n"
      "s_waitcnt lgkmcnt(0)\n"
      "v_mfma_f32_4x4x4_16b_bf16 v[84:87], a[246:247], a[220:221], v[76:79]\n"
      "ds_read_b64_tr_b16 a[232:233], v97 offset:1600\n"
      "v_mfma_f32_4x4x4_16b_bf16 v[88:91], a[246:247], a[222:223], v[80:83]\n"
      "ds_read_b64_tr_b16 a[234:235], v97 offset:2368\n"
      "v_mfma_f32_4x4x4_16b_bf16 v[76:79], a[250:251], a[220:221], v[76:79]\n"
      "ds_read_b64_tr_b16 a[16:17], v97 offset:13888\n"
      "v_mfma_f32_4x4x4_16b_bf16 v[80:83], a[250:251], a[222:223], v[80:83]\n"
      "ds_read_b64_tr_b16 a[18:19], v97 offset:14656\n"
      "ds_read_b64_tr_b16 a[228:229], v97 offset:7744\n"
      "ds_read_b64_tr_b16 a[230:231], v97 offset:8512\n"
      "ds_read_b64_tr_b16 a[240:241], v97 offset:1664\n"
      "ds_read_b64_tr_b16 a[242:243], v97 offset:2432\n"
      "ds_read_b64_tr_b16 a[20:21], v97 offset:13952\n"
      "ds_read_b64_tr_b16 a[22:23], v97 offset:14720\n"
      "ds_read_b64_tr_b16 a[236:237], v97 offset:7808\n"
      "ds_read_b64_tr_b16 a[238:239], v97 offset:8576\n"
      "ds_read_b64_tr_b16 a[188:189], v7 offset:0\n"
      "ds_read_b64_tr_b16 a[190:191], v7 offset:8\n"
      "ds_read_b128 a[192:195], v3 offset:0\n"
      "v_cvt_pk_bf16_f32 v236, v36, v40\n"
      "v_cvt_pk_bf16_f32 v237, v37, v41\n"
      "v_cvt_pk_bf16_f32 v238, v38, v42\n"
      "v_cvt_pk_bf16_f32 v239, v39, v43\n"
      "v_cvt_pk_bf16_f32 v232, v28, v32\n"
      "v_cvt_pk_bf16_f32 v233, v29, v33\n"
      "v_cvt_pk_bf16_f32 v234, v30, v34\n"
      "v_cvt_pk_bf16_f32 v235, v31, v35\n"
      "s_waitcnt vmcnt(6)\n"
      "s_barrier\n"
      "s_add_u32 m0, s63, 59648\n"
      "s_add_u32 s52, s63, 24576\n"
      "s_nop 0\n"
      "s_waitcnt lgkmcnt(7)\n"
      "v_mfma_f32_4x4x4_16b_bf16 v[28:31], a[244:245], a[232:233], 0\n"
      "v_mfma_f32_4x4x4_16b_bf16 v[32:35], a[244:245], a[234:235], 0\n"
      "buffer_load_dwordx4 v92, s[12:15], s52 offen offset:0 lds\n"
      "v_mfma_f32_4x4x4_16b_bf16 v[28:31], a[248:249], a[16:17], v[28:31]\n"
      "buffer_load_dwordx4 v93, s[12:15], s52 offen offset:1024 lds\n"
      "v_mfma_f32_4x4x4_16b_bf16 v[32:35], a[248:249], a[18:19], v[32:35]\n"
      "buffer_load_dwordx4 v94, s[12:15], s52 offen offset:2048 lds\n"
      "v_mfma_f32_4x4x4_16b_bf16 v[36:39], a[246:247], a[228:229], v[28:31]\n"
      "ds_read_b64_tr_b16 a[224:225], v95 offset:6144\n"
      "v_mfma_f32_4x4x4_16b_bf16 v[40:43], a[246:247], a[230:231], v[32:35]\n"
      "ds_read_b64_tr_b16 a[226:227], v95 offset:6912\n"
      "v_mfma_f32_4x4x4_16b_bf16 v[28:31], a[250:251], a[228:229], v[28:31]\n"
      "ds_read_b64_tr_b16 a[24:25], v95 offset:18432\n"
      "v_mfma_f32_4x4x4_16b_bf16 v[32:35], a[250:251], a[230:231], v[32:35]\n"
      "ds_read_b64_tr_b16 a[26:27], v95 offset:19200\n"
      "s_nop 0\n"
      "s_waitcnt lgkmcnt(4)\n"
      "v_mfma_f32_4x4x4_16b_bf16 v[44:47], a[216:217], a[188:189], 0\n"
      "ds_read_b64_tr_b16 a[220:221], v95 offset:12288\n"
      "v_mfma_f32_4x4x4_16b_bf16 v[48:51], a[216:217], a[190:191], 0\n"
      "ds_read_b64_tr_b16 a[222:223], v95 offset:13056\n"
      "ds_read_b64_tr_b16 a[184:185], v7 offset:512\n"
      "ds_read_b64_tr_b16 a[186:187], v7 offset:520\n"
      "ds_read_b128 a[196:199], v3 offset:64\n"
      "v_cvt_pk_bf16_f32 v228, v84, v88\n"
      "v_cvt_pk_bf16_f32 v229, v85, v89\n"
      "v_cvt_pk_bf16_f32 v230, v86, v90\n"
      "v_cvt_pk_bf16_f32 v231, v87, v91\n"
      "v_cvt_pk_bf16_f32 v224, v76, v80\n"
      "v_cvt_pk_bf16_f32 v225, v77, v81\n"
      "v_cvt_pk_bf16_f32 v226, v78, v82\n"
      "v_cvt_pk_bf16_f32 v227, v79, v83\n"
      "s_add_u32 m0, s63, 65792\n"
      "s_add_u32 s52, s63, 79872\n"
      "v_mfma_f32_4x4x4_16b_bf16 v[76:79], a[244:245], a[240:241], 0\n"
      "v_mfma_f32_4x4x4_16b_bf16 v[80:83], a[244:245], a[242:243], 0\n"
      "buffer_load_dwordx4 v92, s[12:15], s52 offen offset:0 lds\n"
      "v_mfma_f32_4x4x4_16b_bf16 v[76:79], a[248:249], a[20:21], v[76:79]\n"
      "buffer_load_dwordx4 v93, s[12:15], s52 offen offset:1024 lds\n"
      "v_mfma_f32_4x4x4_16b_bf16 v[80:83], a[248:249], a[22:23], v[80:83]\n"
      "buffer_load_dwordx4 v94, s[12:15], s52 offen offset:2048 lds\n"
      "v_mfma_f32_4x4x4_16b_bf16 v[84:87], a[246:247], a[236:237], v[76:79]\n"
      "ds_read_b64_tr_b16 a[232:233], v95 offset:6208\n"
      "v_mfma_f32_4x4x4_16b_bf16 v[88:91], a[246:247], a[238:239], v[80:83]\n"
      "ds_read_b64_tr_b16 a[234:235], v95 offset:6976\n"
      "v_mfma_f32_4x4x4_16b_bf16 v[76:79], a[250:251], a[236:237], v[76:79]\n"
      "ds_read_b64_tr_b16 a[28:29], v95 offset:18496\n"
      "v_mfma_f32_4x4x4_16b_bf16 v[80:83], a[250:251], a[238:239], v[80:83]\n"
      "ds_read_b64_tr_b16 a[30:31], v95 offset:19264\n"
      "s_nop 0\n"
      "s_waitcnt lgkmcnt(4)\n"
      "v_mfma_f32_4x4x4_16b_bf16 v[112:115], a[216:217], a[184:185], 0\n"
      "ds_read_b64_tr_b16 a[228:229], v95 offset:12352\n"
      "v_mfma_f32_4x4x4_16b_bf16 v[116:119], a[216:217], a[186:187], 0\n"
      "ds_read_b64_tr_b16 a[230:231], v95 offset:13120\n"
      "ds_read_b64_tr_b16 a[180:181], v7 offset:1024\n"
      "ds_read_b64_tr_b16 a[182:183], v7 offset:1032\n"
      "v_cvt_pk_bf16_f32 v220, v36, v40\n"
      "v_cvt_pk_bf16_f32 v221, v37, v41\n"
      "v_cvt_pk_bf16_f32 v222, v38, v42\n"
      "v_cvt_pk_bf16_f32 v223, v39, v43\n"
      "v_cvt_pk_bf16_f32 v216, v28, v32\n"
      "v_cvt_pk_bf16_f32 v217, v29, v33\n"
      "v_mfma_f32_16x16x32_bf16 v[100:103], a[0:3], a[192:195], 0\n"
      "v_cvt_pk_bf16_f32 v218, v30, v34\n"
      "v_cvt_pk_bf16_f32 v219, v31, v35\n"
      "v_mfma_f32_16x16x32_bf16 v[104:107], a[4:7], a[192:195], 0\n"
      "v_cvt_pk_bf16_f32 v44, v44, v48\n"
      "v_cvt_pk_bf16_f32 v45, v45, v49\n"
      "v_mfma_f32_16x16x32_bf16 v[108:111], a[8:11], a[192:195], 0\n"
      "v_cvt_pk_bf16_f32 v46, v46, v50\n"
      "v_cvt_pk_bf16_f32 v47, v47, v51\n"
      "s_add_u32 m0, s63, 134656\n"
      "s_add_u32 s52, s63, 30720\n"
      "v_mfma_f32_4x4x4_16b_bf16 v[28:31], a[244:245], a[224:225], 0\n"
      "v_mfma_f32_4x4x4_16b_bf16 v[32:35], a[244:245], a[226:227], 0\n"
      "buffer_load_dwordx4 v92, s[12:15], s52 offen offset:0 lds\n"
      "v_mfma_f32_4x4x4_16b_bf16 v[28:31], a[248:249], a[24:25], v[28:31]\n"
      "buffer_load_dwordx4 v93, s[12:15], s52 offen offset:1024 lds\n"
      "v_mfma_f32_4x4x4_16b_bf16 v[32:35], a[248:249], a[26:27], v[32:35]\n"
      "buffer_load_dwordx4 v94, s[12:15], s52 offen offset:2048 lds\n"
      "v_mfma_f32_4x4x4_16b_bf16 v[36:39], a[246:247], a[220:221], v[28:31]\n"
      "ds_read_b64_tr_b16 a[240:241], v95 offset:6272\n"
      "v_mfma_f32_4x4x4_16b_bf16 v[40:43], a[246:247], a[222:223], v[32:35]\n"
      "ds_read_b64_tr_b16 a[242:243], v95 offset:7040\n"
      "v_mfma_f32_4x4x4_16b_bf16 v[28:31], a[250:251], a[220:221], v[28:31]\n"
      "ds_read_b64_tr_b16 a[32:33], v95 offset:18560\n"
      "v_mfma_f32_4x4x4_16b_bf16 v[32:35], a[250:251], a[222:223], v[32:35]\n"
      "ds_read_b64_tr_b16 a[34:35], v95 offset:19328\n"
      "s_nop 0\n"
      "s_waitcnt lgkmcnt(4)\n"
      "v_mfma_f32_4x4x4_16b_bf16 v[20:23], a[216:217], a[180:181], 0\n"
      "ds_read_b64_tr_b16 a[236:237], v95 offset:12416\n"
      "v_mfma_f32_4x4x4_16b_bf16 v[24:27], a[216:217], a[182:183], 0\n"
      "ds_read_b64_tr_b16 a[238:239], v95 offset:13184\n"
      "ds_read_b64_tr_b16 a[176:177], v7 offset:1536\n"
      "ds_read_b64_tr_b16 a[178:179], v7 offset:1544\n"
      "s_mov_b32 m0, s64\n"
      "s_nop 0\n"
      "ds_write_addtid_b32 v44 offset:24832\n"
      "ds_write_addtid_b32 v45 offset:28928\n"
      "ds_write_addtid_b32 v46 offset:33024\n"
      "ds_write_addtid_b32 v47 offset:37120\n"
      "v_cvt_pk_bf16_f32 v212, v84, v88\n"
      "v_cvt_pk_bf16_f32 v213, v85, v89\n"
      "v_cvt_pk_bf16_f32 v214, v86, v90\n"
      "v_cvt_pk_bf16_f32 v215, v87, v91\n"
      "v_cvt_pk_bf16_f32 v208, v76, v80\n"
      "v_cvt_pk_bf16_f32 v209, v77, v81\n"
      "v_mfma_f32_16x16x32_bf16 v[100:103], a[12:15], a[196:199], v[100:103]\n"
      "v_cvt_pk_bf16_f32 v210, v78, v82\n"
      "v_cvt_pk_bf16_f32 v211, v79, v83\n"
      "v_mfma_f32_16x16x32_bf16 v[104:107], a[16:19], a[196:199], v[104:107]\n"
      "v_cvt_pk_bf16_f32 v112, v112, v116\n"
      "v_cvt_pk_bf16_f32 v113, v113, v117\n"
      "v_mfma_f32_16x16x32_bf16 v[108:111], a[20:23], a[196:199], v[108:111]\n"
      "v_cvt_pk_bf16_f32 v114, v114, v118\n"
      "v_cvt_pk_bf16_f32 v115, v115, v119\n"
      "s_waitcnt lgkmcnt(0)\n"
      "s_waitcnt vmcnt(9)\n"
      "s_nop 0\n"
      "s_barrier\n"
      "s_add_u32 m0, s63, 140800\n"
      "s_add_u32 s52, s63, 86016\n"
      "v_mfma_f32_4x4x4_16b_bf16 v[76:79], a[244:245], a[232:233], 0\n"
      "v_mfma_f32_4x4x4_16b_bf16 v[80:83], a[244:245], a[234:235], 0\n"
      "buffer_load_dwordx4 v92, s[12:15], s52 offen offset:0 lds\n"
      "v_mfma_f32_4x4x4_16b_bf16 v[76:79], a[248:249], a[28:29], v[76:79]\n"
      "buffer_load_dwordx4 v93, s[12:15], s52 offen offset:1024 lds\n"
      "v_mfma_f32_4x4x4_16b_bf16 v[80:83], a[248:249], a[30:31], v[80:83]\n"
      "buffer_load_dwordx4 v94, s[12:15], s52 offen offset:2048 lds\n"
      "v_mfma_f32_4x4x4_16b_bf16 v[84:87], a[246:247], a[228:229], v[76:79]\n"
      "ds_read_b64_tr_b16 a[224:225], v95 offset:41216\n"
      "v_mfma_f32_4x4x4_16b_bf16 v[88:91], a[246:247], a[230:231], v[80:83]\n"
      "ds_read_b64_tr_b16 a[226:227], v95 offset:41984\n"
      "v_mfma_f32_4x4x4_16b_bf16 v[76:79], a[250:251], a[228:229], v[76:79]\n"
      "ds_read_b64_tr_b16 a[36:37], v95 offset:53504\n"
      "v_mfma_f32_4x4x4_16b_bf16 v[80:83], a[250:251], a[230:231], v[80:83]\n"
      "ds_read_b64_tr_b16 a[38:39], v95 offset:54272\n"
      "v_mfma_f32_4x4x4_16b_bf16 v[44:47], a[216:217], a[176:177], 0\n"
      "ds_read_b64_tr_b16 a[220:221], v95 offset:47360\n"
      "v_mfma_f32_4x4x4_16b_bf16 v[48:51], a[216:217], a[178:179], 0\n"
      "ds_read_b64_tr_b16 a[222:223], v95 offset:48128\n"
      "s_mov_b32 m0, s64\n"
      "s_nop 0\n"
      "ds_write_addtid_b32 v112 offset:25344\n"
      "ds_write_addtid_b32 v113 offset:29440\n"
      "ds_write_addtid_b32 v114 offset:33536\n"
      "ds_write_addtid_b32 v115 offset:37632\n"
      "ds_read_b128 a[200:203], v3 offset:128\n"
      "v_cvt_pk_bf16_f32 v204, v36, v40\n"
      "v_cvt_pk_bf16_f32 v205, v37, v41\n"
      "v_cvt_pk_bf16_f32 v206, v38, v42\n"
      "v_cvt_pk_bf16_f32 v207, v39, v43\n"
      "v_cvt_pk_bf16_f32 v200, v28, v32\n"
      "v_cvt_pk_bf16_f32 v201, v29, v33\n"
      "v_cvt_pk_bf16_f32 v202, v30, v34\n"
      "v_cvt_pk_bf16_f32 v203, v31, v35\n"
      "v_cvt_pk_bf16_f32 v20, v20, v24\n"
      "v_cvt_pk_bf16_f32 v21, v21, v25\n"
      "v_cvt_pk_bf16_f32 v22, v22, v26\n"
      "v_cvt_pk_bf16_f32 v23, v23, v27\n"
      "v_mfma_f32_4x4x4_16b_bf16 v[28:31], a[244:245], a[240:241], 0\n"
      "v_mfma_f32_4x4x4_16b_bf16 v[32:35], a[244:245], a[242:243], 0\n"
      "ds_read_b64_tr_b16 a[232:233], v95 offset:41280\n"
      "v_mfma_f32_4x4x4_16b_bf16 v[28:31], a[248:249], a[32:33], v[28:31]\n"
      "ds_read_b64_tr_b16 a[234:235], v95 offset:42048\n"
      "v_mfma_f32_4x4x4_16b_bf16 v[32:35], a[248:249], a[34:35], v[32:35]\n"
      "ds_read_b64_tr_b16 a[40:41], v95 offset:53568\n"
      "v_mfma_f32_4x4x4_16b_bf16 v[36:39], a[246:247], a[236:237], v[28:31]\n"
      "ds_read_b64_tr_b16 a[42:43], v95 offset:54336\n"
      "v_mfma_f32_4x4x4_16b_bf16 v[40:43], a[246:247], a[238:239], v[32:35]\n"
      "ds_read_b64_tr_b16 a[228:229], v95 offset:47424\n"
      "v_mfma_f32_4x4x4_16b_bf16 v[28:31], a[250:251], a[236:237], v[28:31]\n"
      "ds_read_b64_tr_b16 a[230:231], v95 offset:48192\n"
      "v_mfma_f32_4x4x4_16b_bf16 v[32:35], a[250:251], a[238:239], v[32:35]\n"
      "ds_write_addtid_b32 v20 offset:25856\n"
      "ds_write_addtid_b32 v21 offset:29952\n"
      "ds_write_addtid_b32 v22 offset:34048\n"
      "ds_write_addtid_b32 v23 offset:38144\n"
      "v_cvt_pk_bf16_f32 v196, v84, v88\n"
      "v_cvt_pk_bf16_f32 v197, v85, v89\n"
      "v_cvt_pk_bf16_f32 v198, v86, v90\n"
      "v_cvt_pk_bf16_f32 v199, v87, v91\n"
      "v_cvt_pk_bf16_f32 v192, v76, v80\n"
      "v_cvt_pk_bf16_f32 v193, v77, v81\n"
      "v_cvt_pk_bf16_f32 v194, v78, v82\n"
      "v_cvt_pk_bf16_f32 v195, v79, v83\n"
      "v_cvt_pk_bf16_f32 v44, v44, v48\n"
      "v_cvt_pk_bf16_f32 v45, v45, v49\n"
      "v_cvt_pk_bf16_f32 v46, v46, v50\n"
      "v_cvt_pk_bf16_f32 v47, v47, v51\n"
      "s_nop 0\n"
      "s_waitcnt lgkmcnt(4)\n"
      "v_mfma_f32_4x4x4_16b_bf16 v[76:79], a[244:245], a[224:225], 0\n"
      "v_mfma_f32_4x4x4_16b_bf16 v[80:83], a[244:245], a[226:227], 0\n"
      "ds_read_b64_tr_b16 a[240:241], v95 offset:41344\n"
      "v_mfma_f32_4x4x4_16b_bf16 v[76:79], a[248:249], a[36:37], v[76:79]\n"
      "ds_read_b64_tr_b16 a[242:243], v95 offset:42112\n"
      "v_mfma_f32_4x4x4_16b_bf16 v[80:83], a[248:249], a[38:39], v[80:83]\n"
      "ds_read_b64_tr_b16 a[44:45], v95 offset:53632\n"
      "v_mfma_f32_4x4x4_16b_bf16 v[84:87], a[246:247], a[220:221], v[76:79]\n"
      "ds_read_b64_tr_b16 a[46:47], v95 offset:54400\n"
      "v_mfma_f32_4x4x4_16b_bf16 v[88:91], a[246:247], a[222:223], v[80:83]\n"
      "ds_read_b64_tr_b16 a[236:237], v95 offset:47488\n"
      "v_mfma_f32_4x4x4_16b_bf16 v[76:79], a[250:251], a[220:221], v[76:79]\n"
      "ds_read_b64_tr_b16 a[238:239], v95 offset:48256\n"
      "v_mfma_f32_4x4x4_16b_bf16 v[80:83], a[250:251], a[222:223], v[80:83]\n"
      "ds_write_addtid_b32 v44 offset:26368\n"
      "ds_write_addtid_b32 v45 offset:30464\n"
      "ds_write_addtid_b32 v46 offset:34560\n"
      "ds_write_addtid_b32 v47 offset:38656\n"
      "v_cvt_pk_bf16_f32 v188, v36, v40\n"
      "v_cvt_pk_bf16_f32 v189, v37, v41\n"
      "v_mfma_f32_16x16x32_bf16 v[100:103], a[24:27], a[200:203], v[100:103]\n"
      "v_cvt_pk_bf16_f32 v190, v38, v42\n"
      "v_cvt_pk_bf16_f32 v191, v39, v43\n"
      "v_mfma_f32_16x16x32_bf16 v[104:107], a[28:31], a[200:203], v[104:107]\n"
      "v_cvt_pk_bf16_f32 v184, v28, v32\n"
      "v_cvt_pk_bf16_f32 v185, v29, v33\n"
      "v_mfma_f32_16x16x32_bf16 v[108:111], a[32:35], a[200:203], v[108:111]\n"
      "v_cvt_pk_bf16_f32 v186, v30, v34\n"
      "v_cvt_pk_bf16_f32 v187, v31, v35\n"
      "s_waitcnt lgkmcnt(0)\n"
      "s_waitcnt vmcnt(6)\n"
      "s_nop 0\n"
      "s_barrier\n"
      "s_add_u32 m0, s63, 6144\n"
      "s_add_u32 s52, s63, 36864\n"
      "v_mfma_f32_4x4x4_16b_bf16 v[28:31], a[244:245], a[232:233], 0\n"
      "v_mfma_f32_4x4x4_16b_bf16 v[32:35], a[244:245], a[234:235], 0\n"
      "buffer_load_dwordx4 v92, s[12:15], s52 offen offset:0 lds\n"
      "v_mfma_f32_4x4x4_16b_bf16 v[28:31], a[248:249], a[40:41], v[28:31]\n"
      "buffer_load_dwordx4 v93, s[12:15], s52 offen offset:1024 lds\n"
      "v_mfma_f32_4x4x4_16b_bf16 v[32:35], a[248:249], a[42:43], v[32:35]\n"
      "buffer_load_dwordx4 v94, s[12:15], s52 offen offset:2048 lds\n"
      "v_mfma_f32_4x4x4_16b_bf16 v[36:39], a[246:247], a[228:229], v[28:31]\n"
      "ds_read_b64_tr_b16 a[224:225], v96 offset:2304\n"
      "v_mfma_f32_4x4x4_16b_bf16 v[40:43], a[246:247], a[230:231], v[32:35]\n"
      "ds_read_b64_tr_b16 a[226:227], v96 offset:3072\n"
      "v_mfma_f32_4x4x4_16b_bf16 v[28:31], a[250:251], a[228:229], v[28:31]\n"
      "ds_read_b64_tr_b16 a[48:49], v96 offset:14592\n"
      "v_mfma_f32_4x4x4_16b_bf16 v[32:35], a[250:251], a[230:231], v[32:35]\n"
      "ds_read_b64_tr_b16 a[50:51], v96 offset:15360\n"
      "ds_read_b64_tr_b16 a[220:221], v96 offset:8448\n"
      "ds_read_b64_tr_b16 a[222:223], v96 offset:9216\n"
      "ds_read_b128 a[204:207], v4 offset:0\n"
      "ds_read_b128 a[208:211], v0 offset:0\n"
      "ds_read_b128 a[212:215], v0 offset:4096\n"
      "v_cvt_pk_bf16_f32 v180, v84, v88\n"
      "v_cvt_pk_bf16_f32 v181, v85, v89\n"
      "v_cvt_pk_bf16_f32 v182, v86, v90\n"
      "v_cvt_pk_bf16_f32 v183, v87, v91\n"
      "v_cvt_pk_bf16_f32 v176, v76, v80\n"
      "v_cvt_pk_bf16_f32 v177, v77, v81\n"
      "v_cvt_pk_bf16_f32 v178, v78, v82\n"
      "v_cvt_pk_bf16_f32 v179, v79, v83\n"
      "s_add_u32 m0, s63, 12288\n"
      "s_add_u32 s52, s63, 92160\n"
      "v_mfma_f32_4x4x4_16b_bf16 v[76:79], a[244:245], a[240:241], 0\n"
      "v_mfma_f32_4x4x4_16b_bf16 v[80:83], a[244:245], a[242:243], 0\n"
      "buffer_load_dwordx4 v92, s[12:15], s52 offen offset:0 lds\n"
      "v_mfma_f32_4x4x4_16b_bf16 v[76:79], a[248:249], a[44:45], v[76:79]\n"
      "buffer_load_dwordx4 v93, s[12:15], s52 offen offset:1024 lds\n"
      "v_mfma_f32_4x4x4_16b_bf16 v[80:83], a[248:249], a[46:47], v[80:83]\n"
      "buffer_load_dwordx4 v94, s[12:15], s52 offen offset:2048 lds\n"
      "v_mfma_f32_4x4x4_16b_bf16 v[84:87], a[246:247], a[236:237], v[76:79]\n"
      "ds_read_b64_tr_b16 a[232:233], v96 offset:2368\n"
      "v_mfma_f32_4x4x4_16b_bf16 v[88:91], a[246:247], a[238:239], v[80:83]\n"
      "ds_read_b64_tr_b16 a[234:235], v96 offset:3136\n"
      "v_mfma_f32_4x4x4_16b_bf16 v[76:79], a[250:251], a[236:237], v[76:79]\n"
      "ds_read_b64_tr_b16 a[52:53], v96 offset:14656\n"
      "v_mfma_f32_4x4x4_16b_bf16 v[80:83], a[250:251], a[238:239], v[80:83]\n"
      "ds_read_b64_tr_b16 a[54:55], v96 offset:15424\n"
      "ds_read_b64_tr_b16 a[228:229], v96 offset:8512\n"
      "ds_read_b64_tr_b16 a[230:231], v96 offset:9280\n"
      "ds_read_b128 a[192:195], v0 offset:64\n"
      "ds_read_b128 a[196:199], v0 offset:4160\n"
      "ds_read_b128 a[200:203], v0 offset:128\n"
      "v_cvt_pk_bf16_f32 v172, v36, v40\n"
      "v_cvt_pk_bf16_f32 v173, v37, v41\n"
      "v_cvt_pk_bf16_f32 v174, v38, v42\n"
      "v_cvt_pk_bf16_f32 v175, v39, v43\n"
      "v_cvt_pk_bf16_f32 v168, v28, v32\n"
      "v_cvt_pk_bf16_f32 v169, v29, v33\n"
      "v_cvt_pk_bf16_f32 v170, v30, v34\n"
      "v_cvt_pk_bf16_f32 v171, v31, v35\n"
      "s_nop 0\n"
      "s_waitcnt lgkmcnt(7)\n"
      "v_mfma_f32_4x4x4_16b_bf16 v[28:31], a[244:245], a[224:225], 0\n"
      "v_mfma_f32_4x4x4_16b_bf16 v[32:35], a[244:245], a[226:227], 0\n"
      "ds_read_b64_tr_b16 a[240:241], v96 offset:2432\n"
      "v_mfma_f32_4x4x4_16b_bf16 v[28:31], a[248:249], a[48:49], v[28:31]\n"
      "ds_read_b64_tr_b16 a[242:243], v96 offset:3200\n"
      "v_mfma_f32_4x4x4_16b_bf16 v[32:35], a[248:249], a[50:51], v[32:35]\n"
      "ds_read_b64_tr_b16 a[56:57], v96 offset:14720\n"
      "v_mfma_f32_4x4x4_16b_bf16 v[36:39], a[246:247], a[220:221], v[28:31]\n"
      "ds_read_b64_tr_b16 a[58:59], v96 offset:15488\n"
      "v_mfma_f32_4x4x4_16b_bf16 v[40:43], a[246:247], a[222:223], v[32:35]\n"
      "ds_read_b64_tr_b16 a[236:237], v96 offset:8576\n"
      "v_mfma_f32_4x4x4_16b_bf16 v[28:31], a[250:251], a[220:221], v[28:31]\n"
      "ds_read_b64_tr_b16 a[238:239], v96 offset:9344\n"
      "v_mfma_f32_4x4x4_16b_bf16 v[32:35], a[250:251], a[222:223], v[32:35]\n"
      "v_mfma_f32_16x16x32_bf16 v[100:103], a[36:39], a[204:207], v[100:103]\n"
      "v_cvt_pk_bf16_f32 v164, v84, v88\n"
      "v_cvt_pk_bf16_f32 v165, v85, v89\n"
      "v_mfma_f32_16x16x32_bf16 v[104:107], a[40:43], a[204:207], v[104:107]\n"
      "v_cvt_pk_bf16_f32 v166, v86, v90\n"
      "v_cvt_pk_bf16_f32 v167, v87, v91\n"
      "v_mfma_f32_16x16x32_bf16 v[108:111], a[44:47], a[204:207], v[108:111]\n"
      "v_cvt_pk_bf16_f32 v160, v76, v80\n"
      "v_cvt_pk_bf16_f32 v161, v77, v81\n"
      "v_mfma_f32_16x16x32_bf16 v[52:55], v[252:255], a[208:211], 0\n"
      "v_cvt_pk_bf16_f32 v162, v78, v82\n"
      "v_cvt_pk_bf16_f32 v163, v79, v83\n"
      "s_waitcnt vmcnt(6)\n"
      "s_barrier\n"
      "s_add_u32 m0, s63, 41216\n"
      "s_add_u32 s52, s63, 43008\n"
      "v_mfma_f32_4x4x4_16b_bf16 v[76:79], a[244:245], a[232:233], 0\n"
      "v_mfma_f32_4x4x4_16b_bf16 v[80:83], a[244:245], a[234:235], 0\n"
      "buffer_load_dwordx4 v92, s[12:15], s52 offen offset:0 lds\n"
      "s_nop 0\n"
      "s_waitcnt lgkmcnt(0)\n"
      "v_mfma_f32_4x4x4_16b_bf16 v[76:79], a[248:249], a[52:53], v[76:79]\n"
      "buffer_load_dwordx4 v93, s[12:15], s52 offen offset:1024 lds\n"
      "v_mfma_f32_4x4x4_16b_bf16 v[80:83], a[248:249], a[54:55], v[80:83]\n"
      "buffer_load_dwordx4 v94, s[12:15], s52 offen offset:2048 lds\n"
      "v_mfma_f32_4x4x4_16b_bf16 v[84:87], a[246:247], a[228:229], v[76:79]\n"
      "ds_read_b64_tr_b16 a[224:225], v97 offset:19968\n"
      "v_mfma_f32_4x4x4_16b_bf16 v[88:91], a[246:247], a[230:231], v[80:83]\n"
      "ds_read_b64_tr_b16 a[226:227], v97 offset:20736\n"
      "v_mfma_f32_4x4x4_16b_bf16 v[76:79], a[250:251], a[228:229], v[76:79]\n"
      "ds_read_b64_tr_b16 a[60:61], v95 offset:0\n"
      "v_mfma_f32_4x4x4_16b_bf16 v[80:83], a[250:251], a[230:231], v[80:83]\n"
      "ds_read_b64_tr_b16 a[62:63], v95 offset:768\n"
      "ds_read_b64_tr_b16 a[220:221], v97 offset:26112\n"
      "ds_read_b64_tr_b16 a[222:223], v97 offset:26880\n"
      "ds_read_b128 a[204:207], v0 offset:4224\n"
      "v_mfma_f32_16x16x32_bf16 v[56:59], v[244:247], a[208:211], 0\n"
      "v_cvt_pk_bf16_f32 v156, v36, v40\n"
      "v_cvt_pk_bf16_f32 v157, v37, v41\n"
      "v_mfma_f32_16x16x32_bf16 v[60:63], v[236:239], a[208:211], 0\n"
      "v_cvt_pk_bf16_f32 v158, v38, v42\n"
      "v_cvt_pk_bf16_f32 v159, v39, v43\n"
      "v_mfma_f32_16x16x32_bf16 v[64:67], v[248:251], a[212:215], 0\n"
      "v_cvt_pk_bf16_f32 v152, v28, v32\n"
      "v_cvt_pk_bf16_f32 v153, v29, v33\n"
      "v_mfma_f32_16x16x32_bf16 v[68:71], v[240:243], a[212:215], 0\n"
      "v_cvt_pk_bf16_f32 v154, v30, v34\n"
      "v_cvt_pk_bf16_f32 v155, v31, v35\n"
      "s_add_u32 m0, s63, 47360\n"
      "s_add_u32 s52, s63, 98304\n"
      "v_mfma_f32_4x4x4_16b_bf16 v[28:31], a[244:245], a[240:241], 0\n"
      "v_mfma_f32_4x4x4_16b_bf16 v[32:35], a[244:245], a[242:243], 0\n"
      "buffer_load_dwordx4 v92, s[12:15], s52 offen offset:0 lds\n"
      "v_mfma_f32_4x4x4_16b_bf16 v[28:31], a[248:249], a[56:57], v[28:31]\n"
      "buffer_load_dwordx4 v93, s[12:15], s52 offen offset:1024 lds\n"
      "v_mfma_f32_4x4x4_16b_bf16 v[32:35], a[248:249], a[58:59], v[32:35]\n"
      "buffer_load_dwordx4 v94, s[12:15], s52 offen offset:2048 lds\n"
      "v_mfma_f32_4x4x4_16b_bf16 v[36:39], a[246:247], a[236:237], v[28:31]\n"
      "ds_read_b64_tr_b16 a[232:233], v97 offset:20032\n"
      "v_mfma_f32_4x4x4_16b_bf16 v[40:43], a[246:247], a[238:239], v[32:35]\n"
      "ds_read_b64_tr_b16 a[234:235], v97 offset:20800\n"
      "v_mfma_f32_4x4x4_16b_bf16 v[28:31], a[250:251], a[236:237], v[28:31]\n"
      "ds_read_b64_tr_b16 a[64:65], v95 offset:64\n"
      "v_mfma_f32_4x4x4_16b_bf16 v[32:35], a[250:251], a[238:239], v[32:35]\n"
      "ds_read_b64_tr_b16 a[66:67], v95 offset:832\n"
      "ds_read_b64_tr_b16 a[228:229], v97 offset:26176\n"
      "ds_read_b64_tr_b16 a[230:231], v97 offset:26944\n"
      "ds_read_b128 a[208:211], v1 offset:0\n"
      "v_mfma_f32_16x16x32_bf16 v[72:75], v[232:235], a[212:215], 0\n"
      "v_cvt_pk_bf16_f32 v148, v84, v88\n"
      "v_cvt_pk_bf16_f32 v149, v85, v89\n"
      "v_mfma_f32_16x16x32_bf16 v[52:55], v[228:231], a[192:195], v[52:55]\n"
      "v_cvt_pk_bf16_f32 v150, v86, v90\n"
      "v_cvt_pk_bf16_f32 v151, v87, v91\n"
      "v_mfma_f32_16x16x32_bf16 v[56:59], v[220:223], a[192:195], v[56:59]\n"
      "v_cvt_pk_bf16_f32 v144, v76, v80\n"
      "v_cvt_pk_bf16_f32 v145, v77, v81\n"
      "v_mfma_f32_16x16x32_bf16 v[60:63], v[212:215], a[192:195], v[60:63]\n"
      "v_cvt_pk_bf16_f32 v146, v78, v82\n"
      "v_cvt_pk_bf16_f32 v147, v79, v83\n"
      "s_nop 0\n"
      "s_waitcnt lgkmcnt(2)\n"
      "v_mfma_f32_4x4x4_16b_bf16 v[76:79], a[244:245], a[224:225], 0\n"
      "v_mfma_f32_4x4x4_16b_bf16 v[80:83], a[244:245], a[226:227], 0\n"
      "ds_read_b64_tr_b16 a[240:241], v97 offset:20096\n"
      "v_mfma_f32_4x4x4_16b_bf16 v[76:79], a[248:249], a[60:61], v[76:79]\n"
      "ds_read_b64_tr_b16 a[242:243], v97 offset:20864\n"
      "v_mfma_f32_4x4x4_16b_bf16 v[80:83], a[248:249], a[62:63], v[80:83]\n"
      "ds_read_b64_tr_b16 a[68:69], v95 offset:128\n"
      "v_mfma_f32_4x4x4_16b_bf16 v[84:87], a[246:247], a[220:221], v[76:79]\n"
      "ds_read_b64_tr_b16 a[70:71], v95 offset:896\n"
      "v_mfma_f32_4x4x4_16b_bf16 v[88:91], a[246:247], a[222:223], v[80:83]\n"
      "ds_read_b64_tr_b16 a[236:237], v97 offset:26240\n"
      "v_mfma_f32_4x4x4_16b_bf16 v[76:79], a[250:251], a[220:221], v[76:79]\n"
      "ds_read_b64_tr_b16 a[238:239], v97 offset:27008\n"
      "v_mfma_f32_4x4x4_16b_bf16 v[80:83], a[250:251], a[222:223], v[80:83]\n"
      "ds_read_b128 a[212:215], v1 offset:4096\n"
      "ds_read_b128 a[192:195], v4 offset:64\n"
      "v_mfma_f32_16x16x32_bf16 v[64:67], v[224:227], a[196:199], v[64:67]\n"
      "v_cvt_pk_bf16_f32 v140, v36, v40\n"
      "v_cvt_pk_bf16_f32 v141, v37, v41\n"
      "v_mfma_f32_16x16x32_bf16 v[68:71], v[216:219], a[196:199], v[68:71]\n"
      "v_cvt_pk_bf16_f32 v142, v38, v42\n"
      "v_cvt_pk_bf16_f32 v143, v39, v43\n"
      "v_mfma_f32_16x16x32_bf16 v[72:75], v[208:211], a[196:199], v[72:75]\n"
      "v_cvt_pk_bf16_f32 v136, v28, v32\n"
      "v_cvt_pk_bf16_f32 v137, v29, v33\n"
      "v_mfma_f32_16x16x32_bf16 v[52:55], v[204:207], a[200:203], v[52:55]\n"
      "v_cvt_pk_bf16_f32 v138, v30, v34\n"
      "v_cvt_pk_bf16_f32 v139, v31, v35\n"
      "s_waitcnt vmcnt(6)\n"
      "s_barrier\n"
      "s_add_u32 m0, s63, 59648\n"
      "s_add_u32 s52, s63, 49152\n"
      "v_mfma_f32_4x4x4_16b_bf16 v[28:31], a[244:245], a[232:233], 0\n"
      "v_mfma_f32_4x4x4_16b_bf16 v[32:35], a[244:245], a[234:235], 0\n"
      "buffer_load_dwordx4 v92, s[12:15], s52 offen offset:0 lds\n"
      "v_mfma_f32_4x4x4_16b_bf16 v[28:31], a[248:249], a[64:65], v[28:31]\n"
      "buffer_load_dwordx4 v93, s[12:15], s52 offen offset:1024 lds\n"
      "v_mfma_f32_4x4x4_16b_bf16 v[32:35], a[248:249], a[66:67], v[32:35]\n"
      "buffer_load_dwordx4 v94, s[12:15], s52 offen offset:2048 lds\n"
      "s_nop 0\n"
      "s_waitcnt lgkmcnt(0)\n"
      "v_mfma_f32_4x4x4_16b_bf16 v[36:39], a[246:247], a[228:229], v[28:31]\n"
      "ds_read_b64_tr_b16 a[224:225], v95 offset:6144\n"
      "v_mfma_f32_4x4x4_16b_bf16 v[40:43], a[246:247], a[230:231], v[32:35]\n"
      "ds_read_b64_tr_b16 a[226:227], v95 offset:6912\n"
      "v_mfma_f32_4x4x4_16b_bf16 v[28:31], a[250:251], a[228:229], v[28:31]\n"
      "ds_read_b64_tr_b16 a[72:73], v95 offset:18432\n"
      "v_mfma_f32_4x4x4_16b_bf16 v[32:35], a[250:251], a[230:231], v[32:35]\n"
      "ds_read_b64_tr_b16 a[74:75], v95 offset:19200\n"
      "ds_read_b64_tr_b16 a[220:221], v95 offset:12288\n"
      "ds_read_b64_tr_b16 a[222:223], v95 offset:13056\n"
      "ds_read_b128 a[196:199], v1 offset:64\n"
      "v_mfma_f32_16x16x32_bf16 v[56:59], v[196:199], a[200:203], v[56:59]\n"
      "v_cvt_pk_bf16_f32 v132, v84, v88\n"
      "v_cvt_pk_bf16_f32 v133, v85, v89\n"
      "v_mfma_f32_16x16x32_bf16 v[60:63], v[188:191], a[200:203], v[60:63]\n"
      "v_cvt_pk_bf16_f32 v134, v86, v90\n"
      "v_cvt_pk_bf16_f32 v135, v87, v91\n"
      "v_mfma_f32_16x16x32_bf16 v[64:67], v[200:203], a[204:207], v[64:67]\n"
      "v_cvt_pk_bf16_f32 v128, v76, v80\n"
      "v_cvt_pk_bf16_f32 v129, v77, v81\n"
      "v_mfma_f32_16x16x32_bf16 v[68:71], v[192:195], a[204:207], v[68:71]\n"
      "v_cvt_pk_bf16_f32 v130, v78, v82\n"
      "v_cvt_pk_bf16_f32 v131, v79, v83\n"
      "s_add_u32 m0, s63, 65792\n"
      "s_add_u32 s52, s63, 104448\n"
      "v_mfma_f32_4x4x4_16b_bf16 v[76:79], a[244:245], a[240:241], 0\n"
      "v_mfma_f32_4x4x4_16b_bf16 v[80:83], a[244:245], a[242:243], 0\n"
      "buffer_load_dwordx4 v92, s[12:15], s52 offen offset:0 lds\n"
      "v_mfma_f32_4x4x4_16b_bf16 v[76:79], a[248:249], a[68:69], v[76:79]\n"
      "buffer_load_dwordx4 v93, s[12:15], s52 offen offset:1024 lds\n"
      "v_mfma_f32_4x4x4_16b_bf16 v[80:83], a[248:249], a[70:71], v[80:83]\n"
      "buffer_load_dwordx4 v94, s[12:15], s52 offen offset:2048 lds\n"
      "v_mfma_f32_4x4x4_16b_bf16 v[84:87], a[246:247], a[236:237], v[76:79]\n"
      "ds_read_b64_tr_b16 a[232:233], v95 offset:6208\n"
      "v_mfma_f32_4x4x4_16b_bf16 v[88:91], a[246:247], a[238:239], v[80:83]\n"
      "ds_read_b64_tr_b16 a[234:235], v95 offset:6976\n"
      "v_mfma_f32_4x4x4_16b_bf16 v[76:79], a[250:251], a[236:237], v[76:79]\n"
      "ds_read_b64_tr_b16 a[76:77], v95 offset:18496\n"
      "v_mfma_f32_4x4x4_16b_bf16 v[80:83], a[250:251], a[238:239], v[80:83]\n"
      "ds_read_b64_tr_b16 a[78:79], v95 offset:19264\n"
      "ds_read_b64_tr_b16 a[228:229], v95 offset:12352\n"
      "ds_read_b64_tr_b16 a[230:231], v95 offset:13120\n"
      "ds_read_b128 a[200:203], v1 offset:4160\n"
      "v_mfma_f32_16x16x32_bf16 v[72:75], v[184:187], a[204:207], v[72:75]\n"
      "v_cvt_pk_bf16_f32 v124, v36, v40\n"
      "v_cvt_pk_bf16_f32 v125, v37, v41\n"
      "v_mfma_f32_16x16x32_bf16 v[52:55], v[180:183], a[208:211], v[52:55]\n"
      "v_cvt_pk_bf16_f32 v126, v38, v42\n"
      "v_cvt_pk_bf16_f32 v127, v39, v43\n"
      "v_mfma_f32_16x16x32_bf16 v[56:59], v[172:175], a[208:211], v[56:59]\n"
      "v_cvt_pk_bf16_f32 v28, v28, v32\n"
      "v_cvt_pk_bf16_f32 v29, v29, v33\n"
      "v_mfma_f32_16x16x32_bf16 v[60:63], v[164:167], a[208:211], v[60:63]\n"
      "v_cvt_pk_bf16_f32 v30, v30, v34\n"
      "v_cvt_pk_bf16_f32 v31, v31, v35\n"
      "v_mfma_f32_16x16x32_bf16 v[64:67], v[176:179], a[212:215], v[64:67]\n"
      "v_accvgpr_write_b32 a188, v28\n"
      "v_accvgpr_write_b32 a189, v29\n"
      "v_mfma_f32_16x16x32_bf16 v[68:71], v[168:171], a[212:215], v[68:71]\n"
      "v_accvgpr_write_b32 a190, v30\n"
      "v_accvgpr_write_b32 a191, v31\n"
      "s_nop 0\n"
      "s_waitcnt lgkmcnt(0)\n"
      "v_mfma_f32_4x4x4_16b_bf16 v[28:31], a[244:245], a[224:225], 0\n"
      "v_mfma_f32_4x4x4_16b_bf16 v[32:35], a[244:245], a[226:227], 0\n"
      "ds_read_b64_tr_b16 a[240:241], v95 offset:6272\n"
      "v_mfma_f32_4x4x4_16b_bf16 v[28:31], a[248:249], a[72:73], v[28:31]\n"
      "ds_read_b64_tr_b16 a[242:243], v95 offset:7040\n"
      "v_mfma_f32_4x4x4_16b_bf16 v[32:35], a[248:249], a[74:75], v[32:35]\n"
      "ds_read_b64_tr_b16 a[80:81], v95 offset:18560\n"
      "v_mfma_f32_4x4x4_16b_bf16 v[36:39], a[246:247], a[220:221], v[28:31]\n"
      "ds_read_b64_tr_b16 a[82:83], v95 offset:19328\n"
      "v_mfma_f32_4x4x4_16b_bf16 v[40:43], a[246:247], a[222:223], v[32:35]\n"
      "ds_read_b64_tr_b16 a[236:237], v95 offset:12416\n"
      "v_mfma_f32_4x4x4_16b_bf16 v[28:31], a[250:251], a[220:221], v[28:31]\n"
      "ds_read_b64_tr_b16 a[238:239], v95 offset:13184\n"
      "v_mfma_f32_4x4x4_16b_bf16 v[32:35], a[250:251], a[222:223], v[32:35]\n"
      "ds_read_b128 a[204:207], v4 offset:128\n"
      "ds_read_b128 a[208:211], v1 offset:128\n"
      "v_cvt_pk_bf16_f32 v84, v84, v88\n"
      "v_cvt_pk_bf16_f32 v85, v85, v89\n"
      "v_cvt_pk_bf16_f32 v86, v86, v90\n"
      "v_cvt_pk_bf16_f32 v87, v87, v91\n"
      "v_mfma_f32_16x16x32_bf16 v[72:75], v[160:163], a[212:215], v[72:75]\n"
      "v_accvgpr_write_b32 a184, v84\n"
      "v_accvgpr_write_b32 a185, v85\n"
      "v_mfma_f32_16x16x32_bf16 v[100:103], a[48:51], a[192:195], v[100:103]\n"
      "v_accvgpr_write_b32 a186, v86\n"
      "v_accvgpr_write_b32 a187, v87\n"
      "v_mfma_f32_16x16x32_bf16 v[104:107], a[52:55], a[192:195], v[104:107]\n"
      "v_cvt_pk_bf16_f32 v76, v76, v80\n"
      "v_cvt_pk_bf16_f32 v77, v77, v81\n"
      "v_mfma_f32_16x16x32_bf16 v[108:111], a[56:59], a[192:195], v[108:111]\n"
      "v_cvt_pk_bf16_f32 v78, v78, v82\n"
      "v_cvt_pk_bf16_f32 v79, v79, v83\n"
      "v_mfma_f32_16x16x32_bf16 v[52:55], v[156:159], a[196:199], v[52:55]\n"
      "v_accvgpr_write_b32 a180, v76\n"
      "v_accvgpr_write_b32 a181, v77\n"
      "v_mfma_f32_16x16x32_bf16 v[56:59], v[148:151], a[196:199], v[56:59]\n"
      "v_accvgpr_write_b32 a182, v78\n"
      "v_accvgpr_write_b32 a183, v79\n"
      "s_waitcnt vmcnt(6)\n"
      "s_barrier\n"
      "v_mfma_f32_4x4x4_16b_bf16 v[76:79], a[244:245], a[232:233], 0\n"
      "v_mfma_f32_4x4x4_16b_bf16 v[80:83], a[244:245], a[234:235], 0\n"
      "ds_read_b64_tr_b16 a[224:225], v95 offset:41216\n"
      "v_mfma_f32_4x4x4_16b_bf16 v[76:79], a[248:249], a[76:77], v[76:79]\n"
      "ds_read_b64_tr_b16 a[226:227], v95 offset:41984\n"
      "v_mfma_f32_4x4x4_16b_bf16 v[80:83], a[248:249], a[78:79], v[80:83]\n"
      "ds_read_b64_tr_b16 a[84:85], v95 offset:53504\n"
      "v_mfma_f32_4x4x4_16b_bf16 v[84:87], a[246:247], a[228:229], v[76:79]\n"
      "ds_read_b64_tr_b16 a[86:87], v95 offset:54272\n"
      "v_mfma_f32_4x4x4_16b_bf16 v[88:91], a[246:247], a[230:231], v[80:83]\n"
      "ds_read_b64_tr_b16 a[220:221], v95 offset:47360\n"
      "v_mfma_f32_4x4x4_16b_bf16 v[76:79], a[250:251], a[228:229], v[76:79]\n"
      "ds_read_b64_tr_b16 a[222:223], v95 offset:48128\n"
      "v_mfma_f32_4x4x4_16b_bf16 v[80:83], a[250:251], a[230:231], v[80:83]\n"
      "ds_read_b128 a[212:215], v1 offset:4224\n"
      "ds_read_b128 a[192:195], v5 offset:0\n"
      "v_cvt_pk_bf16_f32 v36, v36, v40\n"
      "v_cvt_pk_bf16_f32 v37, v37, v41\n"
      "v_cvt_pk_bf16_f32 v38, v38, v42\n"
      "v_cvt_pk_bf16_f32 v39, v39, v43\n"
      "v_mfma_f32_16x16x32_bf16 v[60:63], v[140:143], a[196:199], v[60:63]\n"
      "v_accvgpr_write_b32 a176, v36\n"
      "v_accvgpr_write_b32 a177, v37\n"
      "v_mfma_f32_16x16x32_bf16 v[64:67], v[152:155], a[200:203], v[64:67]\n"
      "v_accvgpr_write_b32 a178, v38\n"
      "v_accvgpr_write_b32 a179, v39\n"
      "v_mfma_f32_16x16x32_bf16 v[68:71], v[144:147], a[200:203], v[68:71]\n"
      "v_cvt_pk_bf16_f32 v28, v28, v32\n"
      "v_cvt_pk_bf16_f32 v29, v29, v33\n"
      "v_mfma_f32_16x16x32_bf16 v[72:75], v[136:139], a[200:203], v[72:75]\n"
      "v_cvt_pk_bf16_f32 v30, v30, v34\n"
      "v_cvt_pk_bf16_f32 v31, v31, v35\n"
      "s_nop 0\n"
      "s_waitcnt lgkmcnt(1)\n"
      "v_mfma_f32_16x16x32_bf16 v[100:103], a[60:63], a[204:207], v[100:103]\n"
      "v_accvgpr_write_b32 a172, v28\n"
      "v_accvgpr_write_b32 a173, v29\n"
      "v_mfma_f32_16x16x32_bf16 v[104:107], a[64:67], a[204:207], v[104:107]\n"
      "v_accvgpr_write_b32 a174, v30\n"
      "v_accvgpr_write_b32 a175, v31\n"
      "v_mfma_f32_4x4x4_16b_bf16 v[28:31], a[244:245], a[240:241], 0\n"
      "v_mfma_f32_4x4x4_16b_bf16 v[32:35], a[244:245], a[242:243], 0\n"
      "ds_read_b64_tr_b16 a[232:233], v95 offset:41280\n"
      "v_mfma_f32_4x4x4_16b_bf16 v[28:31], a[248:249], a[80:81], v[28:31]\n"
      "ds_read_b64_tr_b16 a[234:235], v95 offset:42048\n"
      "v_mfma_f32_4x4x4_16b_bf16 v[32:35], a[248:249], a[82:83], v[32:35]\n"
      "ds_read_b64_tr_b16 a[88:89], v95 offset:53568\n"
      "v_mfma_f32_4x4x4_16b_bf16 v[36:39], a[246:247], a[236:237], v[28:31]\n"
      "ds_read_b64_tr_b16 a[90:91], v95 offset:54336\n"
      "v_mfma_f32_4x4x4_16b_bf16 v[40:43], a[246:247], a[238:239], v[32:35]\n"
      "ds_read_b64_tr_b16 a[228:229], v95 offset:47424\n"
      "v_mfma_f32_4x4x4_16b_bf16 v[28:31], a[250:251], a[236:237], v[28:31]\n"
      "ds_read_b64_tr_b16 a[230:231], v95 offset:48192\n"
      "v_mfma_f32_4x4x4_16b_bf16 v[32:35], a[250:251], a[238:239], v[32:35]\n"
      "ds_read_b128 a[196:199], v2 offset:0\n"
      "ds_read_b128 a[200:203], v2 offset:4096\n"
      "v_cvt_pk_bf16_f32 v84, v84, v88\n"
      "v_cvt_pk_bf16_f32 v85, v85, v89\n"
      "v_cvt_pk_bf16_f32 v86, v86, v90\n"
      "v_cvt_pk_bf16_f32 v87, v87, v91\n"
      "v_mfma_f32_16x16x32_bf16 v[108:111], a[68:71], a[204:207], v[108:111]\n"
      "v_accvgpr_write_b32 a168, v84\n"
      "v_accvgpr_write_b32 a169, v85\n"
      "v_mfma_f32_16x16x32_bf16 v[52:55], v[132:135], a[208:211], v[52:55]\n"
      "v_accvgpr_write_b32 a170, v86\n"
      "v_accvgpr_write_b32 a171, v87\n"
      "v_mfma_f32_16x16x32_bf16 v[56:59], v[124:127], a[208:211], v[56:59]\n"
      "v_cvt_pk_bf16_f32 v76, v76, v80\n"
      "v_cvt_pk_bf16_f32 v77, v77, v81\n"
      "v_mfma_f32_16x16x32_bf16 v[60:63], a[184:187], a[208:211], v[60:63]\n"
      "v_cvt_pk_bf16_f32 v78, v78, v82\n"
      "v_cvt_pk_bf16_f32 v79, v79, v83\n"
      "v_mfma_f32_16x16x32_bf16 v[64:67], v[128:131], a[212:215], v[64:67]\n"
      "v_accvgpr_write_b32 a164, v76\n"
      "v_accvgpr_write_b32 a165, v77\n"
      "v_mfma_f32_16x16x32_bf16 v[68:71], a[188:191], a[212:215], v[68:71]\n"
      "v_accvgpr_write_b32 a166, v78\n"
      "v_accvgpr_write_b32 a167, v79\n"
      "v_mfma_f32_4x4x4_16b_bf16 v[76:79], a[244:245], a[224:225], 0\n"
      "v_mfma_f32_4x4x4_16b_bf16 v[80:83], a[244:245], a[226:227], 0\n"
      "ds_read_b64_tr_b16 a[240:241], v95 offset:41344\n"
      "v_mfma_f32_4x4x4_16b_bf16 v[76:79], a[248:249], a[84:85], v[76:79]\n"
      "ds_read_b64_tr_b16 a[242:243], v95 offset:42112\n"
      "v_mfma_f32_4x4x4_16b_bf16 v[80:83], a[248:249], a[86:87], v[80:83]\n"
      "ds_read_b64_tr_b16 a[92:93], v95 offset:53632\n"
      "v_mfma_f32_4x4x4_16b_bf16 v[84:87], a[246:247], a[220:221], v[76:79]\n"
      "ds_read_b64_tr_b16 a[94:95], v95 offset:54400\n"
      "v_mfma_f32_4x4x4_16b_bf16 v[88:91], a[246:247], a[222:223], v[80:83]\n"
      "ds_read_b64_tr_b16 a[236:237], v95 offset:47488\n"
      "v_mfma_f32_4x4x4_16b_bf16 v[76:79], a[250:251], a[220:221], v[76:79]\n"
      "ds_read_b64_tr_b16 a[238:239], v95 offset:48256\n"
      "v_mfma_f32_4x4x4_16b_bf16 v[80:83], a[250:251], a[222:223], v[80:83]\n"
      "v_cvt_pk_bf16_f32 v36, v36, v40\n"
      "v_cvt_pk_bf16_f32 v37, v37, v41\n"
      "v_cvt_pk_bf16_f32 v38, v38, v42\n"
      "v_cvt_pk_bf16_f32 v39, v39, v43\n"
      "v_accvgpr_write_b32 a160, v36\n"
      "v_accvgpr_write_b32 a161, v37\n"
      "v_accvgpr_write_b32 a162, v38\n"
      "v_accvgpr_write_b32 a163, v39\n"
      "v_mfma_f32_16x16x32_bf16 v[72:75], a[180:183], a[212:215], v[72:75]\n"
      "v_cvt_pk_bf16_f32 v28, v28, v32\n"
      "v_cvt_pk_bf16_f32 v29, v29, v33\n"
      "s_nop 0\n"
      "s_waitcnt lgkmcnt(2)\n"
      "v_mfma_f32_16x16x32_bf16 v[100:103], a[72:75], a[192:195], v[100:103]\n"
      "v_cvt_pk_bf16_f32 v30, v30, v34\n"
      "v_cvt_pk_bf16_f32 v31, v31, v35\n"
      "v_mfma_f32_16x16x32_bf16 v[104:107], a[76:79], a[192:195], v[104:107]\n"
      "v_accvgpr_write_b32 a156, v28\n"
      "v_accvgpr_write_b32 a157, v29\n"
      "v_mfma_f32_16x16x32_bf16 v[108:111], a[80:83], a[192:195], v[108:111]\n"
      "v_accvgpr_write_b32 a158, v30\n"
      "v_accvgpr_write_b32 a159, v31\n"
      "s_waitcnt vmcnt(0)\n"
      "s_barrier\n"
      "v_mfma_f32_4x4x4_16b_bf16 v[28:31], a[244:245], a[232:233], 0\n"
      "v_mfma_f32_4x4x4_16b_bf16 v[32:35], a[244:245], a[234:235], 0\n"
      "ds_read_b64_tr_b16 a[224:225], v96 offset:2304\n"
      "v_mfma_f32_4x4x4_16b_bf16 v[28:31], a[248:249], a[88:89], v[28:31]\n"
      "ds_read_b64_tr_b16 a[226:227], v96 offset:3072\n"
      "v_mfma_f32_4x4x4_16b_bf16 v[32:35], a[248:249], a[90:91], v[32:35]\n"
      "ds_read_b64_tr_b16 a[96:97], v96 offset:14592\n"
      "v_mfma_f32_4x4x4_16b_bf16 v[36:39], a[246:247], a[228:229], v[28:31]\n"
      "ds_read_b64_tr_b16 a[98:99], v96 offset:15360\n"
      "v_mfma_f32_4x4x4_16b_bf16 v[40:43], a[246:247], a[230:231], v[32:35]\n"
      "ds_read_b64_tr_b16 a[220:221], v96 offset:8448\n"
      "v_mfma_f32_4x4x4_16b_bf16 v[28:31], a[250:251], a[228:229], v[28:31]\n"
      "ds_read_b64_tr_b16 a[222:223], v96 offset:9216\n"
      "v_mfma_f32_4x4x4_16b_bf16 v[32:35], a[250:251], a[230:231], v[32:35]\n"
      "ds_read_b128 a[204:207], v5 offset:64\n"
      "v_cvt_pk_bf16_f32 v84, v84, v88\n"
      "v_cvt_pk_bf16_f32 v85, v85, v89\n"
      "v_cvt_pk_bf16_f32 v86, v86, v90\n"
      "v_cvt_pk_bf16_f32 v87, v87, v91\n"
      "v_mfma_f32_16x16x32_bf16 v[52:55], a[176:179], a[196:199], v[52:55]\n"
      "v_accvgpr_write_b32 a152, v84\n"
      "v_accvgpr_write_b32 a153, v85\n"
      "v_mfma_f32_16x16x32_bf16 v[56:59], a[168:171], a[196:199], v[56:59]\n"
      "v_accvgpr_write_b32 a154, v86\n"
      "v_accvgpr_write_b32 a155, v87\n"
      "v_mfma_f32_16x16x32_bf16 v[60:63], a[160:163], a[196:199], v[60:63]\n"
      "v_cvt_pk_bf16_f32 v76, v76, v80\n"
      "v_cvt_pk_bf16_f32 v77, v77, v81\n"
      "v_mfma_f32_16x16x32_bf16 v[64:67], a[172:175], a[200:203], v[64:67]\n"
      "v_cvt_pk_bf16_f32 v78, v78, v82\n"
      "v_cvt_pk_bf16_f32 v79, v79, v83\n"
      "v_mfma_f32_16x16x32_bf16 v[68:71], a[164:167], a[200:203], v[68:71]\n"
      "v_accvgpr_write_b32 a148, v76\n"
      "v_accvgpr_write_b32 a149, v77\n"
      "v_mfma_f32_16x16x32_bf16 v[72:75], a[156:159], a[200:203], v[72:75]\n"
      "v_accvgpr_write_b32 a150, v78\n"
      "v_accvgpr_write_b32 a151, v79\n"
      "v_mfma_f32_4x4x4_16b_bf16 v[76:79], a[244:245], a[240:241], 0\n"
      "v_mfma_f32_4x4x4_16b_bf16 v[80:83], a[244:245], a[242:243], 0\n"
      "ds_read_b64_tr_b16 a[232:233], v96 offset:2368\n"
      "v_mfma_f32_4x4x4_16b_bf16 v[76:79], a[248:249], a[92:93], v[76:79]\n"
      "ds_read_b64_tr_b16 a[234:235], v96 offset:3136\n"
      "v_mfma_f32_4x4x4_16b_bf16 v[80:83], a[248:249], a[94:95], v[80:83]\n"
      "ds_read_b64_tr_b16 a[100:101], v96 offset:14656\n"
      "s_nop 0\n"
      "s_waitcnt lgkmcnt(3)\n"
      "v_mfma_f32_4x4x4_16b_bf16 v[84:87], a[246:247], a[236:237], v[76:79]\n"
      "ds_read_b64_tr_b16 a[102:103], v96 offset:15424\n"
      "v_mfma_f32_4x4x4_16b_bf16 v[88:91], a[246:247], a[238:239], v[80:83]\n"
      "ds_read_b64_tr_b16 a[228:229], v96 offset:8512\n"
      "v_mfma_f32_4x4x4_16b_bf16 v[76:79], a[250:251], a[236:237], v[76:79]\n"
      "ds_read_b64_tr_b16 a[230:231], v96 offset:9280\n"
      "v_mfma_f32_4x4x4_16b_bf16 v[80:83], a[250:251], a[238:239], v[80:83]\n"
      "ds_read_b128 a[208:211], v2 offset:64\n"
      "ds_read_b128 a[212:215], v2 offset:4160\n"
      "v_cvt_pk_bf16_f32 v36, v36, v40\n"
      "v_cvt_pk_bf16_f32 v37, v37, v41\n"
      "v_cvt_pk_bf16_f32 v38, v38, v42\n"
      "v_cvt_pk_bf16_f32 v39, v39, v43\n"
      "v_accvgpr_write_b32 a144, v36\n"
      "v_accvgpr_write_b32 a145, v37\n"
      "v_accvgpr_write_b32 a146, v38\n"
      "v_accvgpr_write_b32 a147, v39\n"
      "v_cvt_pk_bf16_f32 v28, v28, v32\n"
      "v_cvt_pk_bf16_f32 v29, v29, v33\n"
      "v_cvt_pk_bf16_f32 v30, v30, v34\n"
      "v_cvt_pk_bf16_f32 v31, v31, v35\n"
      "v_accvgpr_write_b32 a140, v28\n"
      "v_accvgpr_write_b32 a141, v29\n"
      "v_accvgpr_write_b32 a142, v30\n"
      "v_accvgpr_write_b32 a143, v31\n"
      "v_mfma_f32_4x4x4_16b_bf16 v[28:31], a[244:245], a[224:225], 0\n"
      "v_mfma_f32_4x4x4_16b_bf16 v[32:35], a[244:245], a[226:227], 0\n"
      "ds_read_b64_tr_b16 a[240:241], v96 offset:2432\n"
      "v_mfma_f32_4x4x4_16b_bf16 v[28:31], a[248:249], a[96:97], v[28:31]\n"
      "ds_read_b64_tr_b16 a[242:243], v96 offset:3200\n"
      "v_mfma_f32_4x4x4_16b_bf16 v[32:35], a[248:249], a[98:99], v[32:35]\n"
      "ds_read_b64_tr_b16 a[104:105], v96 offset:14720\n"
      "v_mfma_f32_4x4x4_16b_bf16 v[36:39], a[246:247], a[220:221], v[28:31]\n"
      "ds_read_b64_tr_b16 a[106:107], v96 offset:15488\n"
      "v_mfma_f32_4x4x4_16b_bf16 v[40:43], a[246:247], a[222:223], v[32:35]\n"
      "ds_read_b64_tr_b16 a[236:237], v96 offset:8576\n"
      "v_mfma_f32_4x4x4_16b_bf16 v[28:31], a[250:251], a[220:221], v[28:31]\n"
      "ds_read_b64_tr_b16 a[238:239], v96 offset:9344\n"
      "v_mfma_f32_4x4x4_16b_bf16 v[32:35], a[250:251], a[222:223], v[32:35]\n"
      "v_cvt_pk_bf16_f32 v84, v84, v88\n"
      "v_cvt_pk_bf16_f32 v85, v85, v89\n"
      "v_cvt_pk_bf16_f32 v86, v86, v90\n"
      "v_cvt_pk_bf16_f32 v87, v87, v91\n"
      "v_accvgpr_write_b32 a136, v84\n"
      "v_accvgpr_write_b32 a137, v85\n"
      "v_accvgpr_write_b32 a138, v86\n"
      "v_accvgpr_write_b32 a139, v87\n"
      "v_cvt_pk_bf16_f32 v76, v76, v80\n"
      "v_cvt_pk_bf16_f32 v77, v77, v81\n"
      "v_mfma_f32_16x16x32_bf16 v[100:103], a[84:87], a[204:207], v[100:103]\n"
      "v_cvt_pk_bf16_f32 v78, v78, v82\n"
      "v_cvt_pk_bf16_f32 v79, v79, v83\n"
      "v_mfma_f32_16x16x32_bf16 v[104:107], a[88:91], a[204:207], v[104:107]\n"
      "v_accvgpr_write_b32 a132, v76\n"
      "v_accvgpr_write_b32 a133, v77\n"
      "v_mfma_f32_16x16x32_bf16 v[108:111], a[92:95], a[204:207], v[108:111]\n"
      "v_accvgpr_write_b32 a134, v78\n"
      "v_accvgpr_write_b32 a135, v79\n"
      "s_waitcnt lgkmcnt(0)\n"
      "v_mfma_f32_4x4x4_16b_bf16 v[76:79], a[244:245], a[232:233], 0\n"
      "v_mfma_f32_4x4x4_16b_bf16 v[80:83], a[244:245], a[234:235], 0\n"
      "ds_read_b128 a[192:195], v5 offset:128\n"
      "v_mfma_f32_4x4x4_16b_bf16 v[76:79], a[248:249], a[100:101], v[76:79]\n"
      "v_mfma_f32_4x4x4_16b_bf16 v[80:83], a[248:249], a[102:103], v[80:83]\n"
      "s_nop 0\n"
      "v_mfma_f32_4x4x4_16b_bf16 v[84:87], a[246:247], a[228:229], v[76:79]\n"
      "v_mfma_f32_4x4x4_16b_bf16 v[88:91], a[246:247], a[230:231], v[80:83]\n"
      "v_mfma_f32_4x4x4_16b_bf16 v[76:79], a[250:251], a[228:229], v[76:79]\n"
      "v_mfma_f32_4x4x4_16b_bf16 v[80:83], a[250:251], a[230:231], v[80:83]\n"
      "v_cvt_pk_bf16_f32 v36, v36, v40\n"
      "v_cvt_pk_bf16_f32 v37, v37, v41\n"
      "v_cvt_pk_bf16_f32 v38, v38, v42\n"
      "v_cvt_pk_bf16_f32 v39, v39, v43\n"
      "v_mfma_f32_16x16x32_bf16 v[52:55], a[152:155], a[208:211], v[52:55]\n"
      "v_accvgpr_write_b32 a128, v36\n"
      "v_accvgpr_write_b32 a129, v37\n"
      "v_mfma_f32_16x16x32_bf16 v[56:59], a[144:147], a[208:211], v[56:59]\n"
      "v_accvgpr_write_b32 a130, v38\n"
      "v_accvgpr_write_b32 a131, v39\n"
      "v_mfma_f32_16x16x32_bf16 v[60:63], a[136:139], a[208:211], v[60:63]\n"
      "v_cvt_pk_bf16_f32 v28, v28, v32\n"
      "v_cvt_pk_bf16_f32 v29, v29, v33\n"
      "v_mfma_f32_16x16x32_bf16 v[64:67], a[148:151], a[212:215], v[64:67]\n"
      "v_cvt_pk_bf16_f32 v30, v30, v34\n"
      "v_cvt_pk_bf16_f32 v31, v31, v35\n"
      "v_mfma_f32_16x16x32_bf16 v[68:71], a[140:143], a[212:215], v[68:71]\n"
      "v_accvgpr_write_b32 a124, v28\n"
      "v_accvgpr_write_b32 a125, v29\n"
      "v_mfma_f32_16x16x32_bf16 v[72:75], a[132:135], a[212:215], v[72:75]\n"
      "v_accvgpr_write_b32 a126, v30\n"
      "v_accvgpr_write_b32 a127, v31\n"
      "v_mfma_f32_4x4x4_16b_bf16 v[28:31], a[244:245], a[240:241], 0\n"
      "v_mfma_f32_4x4x4_16b_bf16 v[32:35], a[244:245], a[242:243], 0\n"
      "ds_read_b128 a[196:199], v2 offset:128\n"
      "v_mfma_f32_4x4x4_16b_bf16 v[28:31], a[248:249], a[104:105], v[28:31]\n"
      "ds_read_b128 a[200:203], v2 offset:4224\n"
      "v_mfma_f32_4x4x4_16b_bf16 v[32:35], a[248:249], a[106:107], v[32:35]\n"
      "v_mfma_f32_4x4x4_16b_bf16 v[36:39], a[246:247], a[236:237], v[28:31]\n"
      "s_nop 0\n"
      "s_nop 0\n"
      "v_mfma_f32_4x4x4_16b_bf16 v[40:43], a[246:247], a[238:239], v[32:35]\n"
      "v_mfma_f32_4x4x4_16b_bf16 v[28:31], a[250:251], a[236:237], v[28:31]\n"
      "v_mfma_f32_4x4x4_16b_bf16 v[32:35], a[250:251], a[238:239], v[32:35]\n"
      "v_cvt_pk_bf16_f32 v84, v84, v88\n"
      "v_cvt_pk_bf16_f32 v85, v85, v89\n"
      "v_cvt_pk_bf16_f32 v86, v86, v90\n"
      "v_cvt_pk_bf16_f32 v87, v87, v91\n"
      "v_accvgpr_write_b32 a120, v84\n"
      "v_accvgpr_write_b32 a121, v85\n"
      "v_accvgpr_write_b32 a122, v86\n"
      "v_accvgpr_write_b32 a123, v87\n"
      "v_cvt_pk_bf16_f32 v76, v76, v80\n"
      "v_cvt_pk_bf16_f32 v77, v77, v81\n"
      "v_cvt_pk_bf16_f32 v78, v78, v82\n"
      "v_cvt_pk_bf16_f32 v79, v79, v83\n"
      "v_accvgpr_write_b32 a116, v76\n"
      "v_accvgpr_write_b32 a117, v77\n"
      "v_accvgpr_write_b32 a118, v78\n"
      "v_accvgpr_write_b32 a119, v79\n"
      "s_waitcnt lgkmcnt(0)\n"
      "s_barrier\n"
      "s_mov_b32 m0, s64\n"
      "s_nop 0\n"
      "v_mfma_f32_16x16x32_bf16 v[100:103], a[96:99], a[192:195], v[100:103]\n"
      "ds_read_b128 a[204:207], v3 offset:16896\n"
      "ds_read_b64_tr_b16 a[220:221], v7 offset:16896\n"
      "ds_read_b64_tr_b16 a[222:223], v7 offset:16904\n"
      "v_cvt_pk_bf16_f32 v36, v36, v40\n"
      "v_cvt_pk_bf16_f32 v37, v37, v41\n"
      "v_mfma_f32_16x16x32_bf16 v[104:107], a[100:103], a[192:195], v[104:107]\n"
      "ds_read_b128 a[208:211], v3 offset:16960\n"
      "v_cvt_pk_bf16_f32 v38, v38, v42\n"
      "v_cvt_pk_bf16_f32 v39, v39, v43\n"
      "v_accvgpr_write_b32 a112, v36\n"
      "v_mfma_f32_16x16x32_bf16 v[108:111], a[104:107], a[192:195], v[108:111]\n"
      "v_accvgpr_write_b32 a113, v37\n"
      "v_accvgpr_write_b32 a114, v38\n"
      "v_accvgpr_write_b32 a115, v39\n"
      "v_cvt_pk_bf16_f32 v28, v28, v32\n"
      "v_cvt_pk_bf16_f32 v29, v29, v33\n"
      "v_cvt_pk_bf16_f32 v30, v30, v34\n"
      "v_mfma_f32_16x16x32_bf16 v[52:55], a[128:131], a[196:199], v[52:55]\n"
      "ds_read_b128 a[212:215], v3 offset:17024\n"
      "v_cvt_pk_bf16_f32 v31, v31, v35\n"
      "v_accvgpr_write_b32 a108, v28\n"
      "v_accvgpr_write_b32 a109, v29\n"
      "v_accvgpr_write_b32 a110, v30\n"
      "v_accvgpr_write_b32 a111, v31\n"
      "v_mfma_f32_16x16x32_bf16 v[56:59], a[120:123], a[196:199], v[56:59]\n"
      "v_mfma_f32_16x16x32_bf16 v[60:63], a[112:115], a[196:199], v[60:63]\n"
      "v_mfma_f32_16x16x32_bf16 v[64:67], a[124:127], a[200:203], v[64:67]\n"
      "ds_read_b128 a[192:195], v4 offset:16896\n"
      "ds_read_b64_tr_b16 a[224:225], v7 offset:17408\n"
      "v_mfma_f32_16x16x32_bf16 v[68:71], a[116:119], a[200:203], v[68:71]\n"
      "ds_read_b64_tr_b16 a[226:227], v7 offset:17416\n"
      "v_mfma_f32_16x16x32_bf16 v[72:75], a[108:111], a[200:203], v[72:75]\n"
      "s_waitcnt lgkmcnt(3)\n"
      "v_mfma_f32_4x4x4_16b_bf16 v[20:23], a[216:217], a[220:221], 0\n"
      "v_mfma_f32_4x4x4_16b_bf16 v[24:27], a[216:217], a[222:223], 0\n"
      "v_sub_f32 v52, v64, v52\n"
      "v_sub_f32 v53, v65, v53\n"
      "v_sub_f32 v54, v66, v54\n"
      "v_sub_f32 v55, v67, v55\n"
      "v_sub_f32 v56, v68, v56\n"
      "v_mfma_f32_16x16x32_bf16 v[112:115], a[0:3], a[204:207], 0\n"
      "ds_read_b128 a[196:199], v4 offset:16960\n"
      "ds_write_addtid_b32 v52 offset:0\n"
      "v_mfma_f32_16x16x32_bf16 v[116:119], a[4:7], a[204:207], 0\n"
      "ds_write_addtid_b32 v53 offset:512\n"
      "ds_write_addtid_b32 v54 offset:1024\n"
      "v_mfma_f32_16x16x32_bf16 v[120:123], a[8:11], a[204:207], 0\n"
      "ds_write_addtid_b32 v55 offset:1536\n"
      "ds_write_addtid_b32 v56 offset:2048\n"
      "v_mfma_f32_16x16x32_bf16 v[112:115], a[12:15], a[208:211], v[112:115]\n"
      "ds_read_b128 a[200:203], v4 offset:17024\n"
      "v_cvt_pk_bf16_f32 v20, v20, v24\n"
      "v_mfma_f32_16x16x32_bf16 v[116:119], a[16:19], a[208:211], v[116:119]\n"
      "ds_write_addtid_b32 v20 offset:41216\n"
      "v_cvt_pk_bf16_f32 v21, v21, v25\n"
      "v_mfma_f32_16x16x32_bf16 v[120:123], a[20:23], a[208:211], v[120:123]\n"
      "ds_write_addtid_b32 v21 offset:45312\n"
      "v_add_f32_e64 v64, v64, v100\n"
      "v_mfma_f32_16x16x32_bf16 v[112:115], a[24:27], a[212:215], v[112:115]\n"
      "ds_read_b128 a[204:207], v5 offset:16896\n"
      "ds_read_b64_tr_b16 a[228:229], v7 offset:17920\n"
      "v_mfma_f32_16x16x32_bf16 v[116:119], a[28:31], a[212:215], v[116:119]\n"
      "ds_read_b64_tr_b16 a[230:231], v7 offset:17928\n"
      "v_cvt_pk_bf16_f32 v22, v22, v26\n"
      "v_mfma_f32_16x16x32_bf16 v[120:123], a[32:35], a[212:215], v[120:123]\n"
      "ds_write_addtid_b32 v22 offset:49408\n"
      "v_cvt_pk_bf16_f32 v23, v23, v27\n"
      "s_waitcnt lgkmcnt(5)\n"
      "v_mfma_f32_4x4x4_16b_bf16 v[28:31], a[216:217], a[224:225], 0\n"
      "ds_write_addtid_b32 v23 offset:53504\n"
      "v_mfma_f32_4x4x4_16b_bf16 v[32:35], a[216:217], a[226:227], 0\n"
      "v_add_f32 v65, v65, v101\n"
      "v_sub_f32 v57, v69, v57\n"
      "v_add_f32 v66, v66, v102\n"
      "v_mfma_f32_16x16x32_bf16 v[112:115], a[36:39], a[192:195], v[112:115]\n"
      "ds_read_b128 a[208:211], v5 offset:16960\n"
      "ds_write_addtid_b32 v57 offset:2560\n"
      "v_mfma_f32_16x16x32_bf16 v[116:119], a[40:43], a[192:195], v[116:119]\n"
      "v_cvt_pk_bf16_f32 v28, v28, v32\n"
      "v_sub_f32_e64 v58, v70, v58\n"
      "v_mfma_f32_16x16x32_bf16 v[120:123], a[44:47], a[192:195], v[120:123]\n"
      "ds_write_addtid_b32 v28 offset:41728\n"
      "ds_write_addtid_b32 v58 offset:3072\n"
      "v_mfma_f32_16x16x32_bf16 v[112:115], a[48:51], a[196:199], v[112:115]\n"
      "ds_read_b128 a[212:215], v5 offset:17024\n"
      "v_cvt_pk_bf16_f32 v29, v29, v33\n"
      "v_mfma_f32_16x16x32_bf16 v[116:119], a[52:55], a[196:199], v[116:119]\n"
      "ds_write_addtid_b32 v29 offset:45824\n"
      "v_add_f32_e64 v67, v67, v103\n"
      "v_mfma_f32_16x16x32_bf16 v[120:123], a[56:59], a[196:199], v[120:123]\n"
      "v_cvt_pk_bf16_f32 v30, v30, v34\n"
      "v_sub_f32_e64 v59, v71, v59\n"
      "v_mfma_f32_16x16x32_bf16 v[112:115], a[60:63], a[200:203], v[112:115]\n"
      "ds_read_b128 a[192:195], v3 offset:33792\n"
      "ds_read_b64_tr_b16 a[232:233], v7 offset:18432\n"
      "v_mfma_f32_16x16x32_bf16 v[116:119], a[64:67], a[200:203], v[116:119]\n"
      "ds_write_addtid_b32 v30 offset:49920\n"
      "ds_read_b64_tr_b16 a[234:235], v7 offset:18440\n"
      "v_mfma_f32_16x16x32_bf16 v[120:123], a[68:71], a[200:203], v[120:123]\n"
      "ds_write_addtid_b32 v59 offset:3584\n"
      "v_cvt_pk_bf16_f32 v31, v31, v35\n"
      "s_waitcnt lgkmcnt(5)\n"
      "v_mfma_f32_4x4x4_16b_bf16 v[20:23], a[216:217], a[228:229], 0\n"
      "ds_write_addtid_b32 v31 offset:54016\n"
      "v_mfma_f32_4x4x4_16b_bf16 v[24:27], a[216:217], a[230:231], 0\n"
      "v_add_f32 v68, v68, v104\n"
      "v_sub_f32 v60, v72, v60\n"
      "v_add_f32 v69, v69, v105\n"
      "v_mfma_f32_16x16x32_bf16 v[112:115], a[72:75], a[204:207], v[112:115]\n"
      "ds_read_b128 a[196:199], v3 offset:33856\n"
      "ds_write_addtid_b32 v60 offset:4096\n"
      "v_mfma_f32_16x16x32_bf16 v[116:119], a[76:79], a[204:207], v[116:119]\n"
      "v_cvt_pk_bf16_f32 v20, v20, v24\n"
      "v_cvt_pk_bf16_f32 v21, v21, v25\n"
      "v_mfma_f32_16x16x32_bf16 v[120:123], a[80:83], a[204:207], v[120:123]\n"
      "ds_write_addtid_b32 v20 offset:42240\n"
      "ds_write_addtid_b32 v21 offset:46336\n"
      "v_mfma_f32_16x16x32_bf16 v[112:115], a[84:87], a[208:211], v[112:115]\n"
      "ds_read_b128 a[200:203], v3 offset:33920\n"
      "v_cvt_pk_bf16_f32 v22, v22, v26\n"
      "v_mfma_f32_16x16x32_bf16 v[116:119], a[88:91], a[208:211], v[116:119]\n"
      "ds_write_addtid_b32 v22 offset:50432\n"
      "v_sub_f32 v61, v73, v61\n"
      "v_mfma_f32_16x16x32_bf16 v[120:123], a[92:95], a[208:211], v[120:123]\n"
      "ds_write_addtid_b32 v61 offset:4608\n"
      "v_add_f32 v70, v70, v106\n"
      "v_mfma_f32_16x16x32_bf16 v[112:115], a[96:99], a[212:215], v[112:115]\n"
      "ds_read_b128 a[204:207], v4 offset:33792\n"
      "v_cvt_pk_bf16_f32 v23, v23, v27\n"
      "v_mfma_f32_16x16x32_bf16 v[116:119], a[100:103], a[212:215], v[116:119]\n"
      "ds_write_addtid_b32 v23 offset:54528\n"
      "v_add_f32 v71, v71, v107\n"
      "v_mfma_f32_16x16x32_bf16 v[120:123], a[104:107], a[212:215], v[120:123]\n"
      "v_sub_f32_e64 v62, v74, v62\n"
      "s_waitcnt lgkmcnt(4)\n"
      "v_mfma_f32_4x4x4_16b_bf16 v[28:31], a[216:217], a[232:233], 0\n"
      "ds_write_addtid_b32 v62 offset:5120\n"
      "v_mfma_f32_4x4x4_16b_bf16 v[32:35], a[216:217], a[234:235], 0\n"
      "v_add_f32_e64 v72, v72, v108\n"
      "v_sub_f32_e64 v63, v75, v63\n"
      "v_add_f32_e64 v73, v73, v109\n"
      "v_mfma_f32_16x16x32_bf16 v[76:79], a[0:3], a[192:195], 0\n"
      "ds_read_b128 a[208:211], v4 offset:33856\n"
      "ds_write_addtid_b32 v63 offset:5632\n"
      "v_mfma_f32_16x16x32_bf16 v[80:83], a[4:7], a[192:195], 0\n"
      "v_cvt_pk_bf16_f32 v28, v28, v32\n"
      "v_cvt_pk_bf16_f32 v29, v29, v33\n"
      "v_mfma_f32_16x16x32_bf16 v[84:87], a[8:11], a[192:195], 0\n"
      "ds_write_addtid_b32 v28 offset:42752\n"
      "ds_write_addtid_b32 v29 offset:46848\n"
      "v_mfma_f32_16x16x32_bf16 v[76:79], a[12:15], a[196:199], v[76:79]\n"
      "ds_read_b128 a[212:215], v4 offset:33920\n"
      "v_cvt_pk_bf16_f32 v30, v30, v34\n"
      "v_mfma_f32_16x16x32_bf16 v[80:83], a[16:19], a[196:199], v[80:83]\n"
      "ds_write_addtid_b32 v30 offset:50944\n"
      "v_cvt_pk_bf16_f32 v31, v31, v35\n"
      "v_mfma_f32_16x16x32_bf16 v[84:87], a[20:23], a[196:199], v[84:87]\n"
      "ds_write_addtid_b32 v31 offset:55040\n"
      "v_add_f32_e64 v74, v74, v110\n"
      "v_mfma_f32_16x16x32_bf16 v[76:79], a[24:27], a[200:203], v[76:79]\n"
      "ds_read_b128 a[192:195], v5 offset:33792\n"
      "v_add_f32 v75, v75, v111\n"
      "v_mfma_f32_16x16x32_bf16 v[80:83], a[28:31], a[200:203], v[80:83]\n"
      "v_mfma_f32_16x16x32_bf16 v[84:87], a[32:35], a[200:203], v[84:87]\n"
      "s_waitcnt lgkmcnt(3)\n"
      "v_mfma_f32_16x16x32_bf16 v[76:79], a[36:39], a[204:207], v[76:79]\n"
      "ds_read_b128 a[196:199], v5 offset:33856\n"
      "v_mfma_f32_16x16x32_bf16 v[80:83], a[40:43], a[204:207], v[80:83]\n"
      "v_mfma_f32_16x16x32_bf16 v[84:87], a[44:47], a[204:207], v[84:87]\n"
      "v_mfma_f32_16x16x32_bf16 v[76:79], a[48:51], a[208:211], v[76:79]\n"
      "ds_read_b128 a[200:203], v5 offset:33920\n"
      "v_mfma_f32_16x16x32_bf16 v[80:83], a[52:55], a[208:211], v[80:83]\n"
      "v_mfma_f32_16x16x32_bf16 v[84:87], a[56:59], a[208:211], v[84:87]\n"
      "v_mfma_f32_16x16x32_bf16 v[76:79], a[60:63], a[212:215], v[76:79]\n"
      "v_fma_f32 v40, 0.5, v52, -v64\n"
      "v_fma_f32 v41, 0.5, v53, -v65\n"
      "v_fma_f32 v42, 0.5, v54, -v66\n"
      "v_fma_f32 v43, 0.5, v55, -v67\n"
      "v_mfma_f32_16x16x32_bf16 v[80:83], a[64:67], a[212:215], v[80:83]\n"
      "v_fma_f32 v44, 0.5, v56, -v68\n"
      "v_fma_f32 v45, 0.5, v57, -v69\n"
      "v_fma_f32 v46, 0.5, v58, -v70\n"
      "v_fma_f32 v47, 0.5, v59, -v71\n"
      "v_mfma_f32_16x16x32_bf16 v[84:87], a[68:71], a[212:215], v[84:87]\n"
      "v_fma_f32 v48, 0.5, v60, -v72\n"
      "v_fma_f32 v49, 0.5, v61, -v73\n"
      "v_fma_f32 v50, 0.5, v62, -v74\n"
      "v_fma_f32 v51, 0.5, v63, -v75\n"
      "s_waitcnt lgkmcnt(0)\n"
      "s_barrier\n"
      ".p2align 6\n"
      "LSloop_%=:\n"
      "v_mfma_f32_16x16x32_bf16 v[76:79], a[72:75], a[192:195], v[76:79]\n"
      "ds_read_b128 a[204:207], v0 offset:16384\n"
      "ds_read2st64_b32 v[28:29], v6 offset0:0 offset1:2\n"
      "v_mfma_f32_16x16x32_bf16 v[80:83], a[76:79], a[192:195], v[80:83]\n"
      "ds_read_b128 a[208:211], v0 offset:20480\n"
      "ds_read_b64_tr_b16 a[220:221], v7 offset:33792\n"
      "v_mfma_f32_16x16x32_bf16 v[84:87], a[80:83], a[192:195], v[84:87]\n"
      "ds_read2st64_b32 v[30:31], v6 offset0:4 offset1:6\n"
      "ds_read2st64_b32 v[32:33], v6 offset0:8 offset1:10\n"
      "v_mfma_f32_16x16x32_bf16 v[76:79], a[84:87], a[196:199], v[76:79]\n"
      "ds_read_b128 a[212:215], v0 offset:16448\n"
      "v_mfma_f32_16x16x32_bf16 v[80:83], a[88:91], a[196:199], v[80:83]\n"
      "v_mfma_f32_16x16x32_bf16 v[84:87], a[92:95], a[196:199], v[84:87]\n"
      "ds_read_b64_tr_b16 a[222:223], v7 offset:33800\n"
      "v_mfma_f32_16x16x32_bf16 v[76:79], a[96:99], a[200:203], v[76:79]\n"
      "ds_read_b128 a[192:195], v0 offset:20544\n"
      "ds_read2st64_b32 v[34:35], v6 offset0:12 offset1:14\n"
      "v_mfma_f32_16x16x32_bf16 v[80:83], a[100:103], a[200:203], v[80:83]\n"
      "ds_read_b64_tr_b16 a[224:225], v7 offset:34304\n"
      "ds_read2st64_b32 v[36:37], v6 offset0:16 offset1:18\n"
      "v_mfma_f32_16x16x32_bf16 v[84:87], a[104:107], a[200:203], v[84:87]\n"
      "ds_read_b64_tr_b16 a[226:227], v7 offset:34312\n"
      "ds_read2st64_b32 v[38:39], v6 offset0:20 offset1:22\n"
      "s_waitcnt lgkmcnt(9)\n"
      "v_sub_f32 v28, v28, v40\n"
      "v_sub_f32 v29, v29, v41\n"
      "v_sub_f32 v30, v30, v42\n"
      "v_mfma_f32_16x16x32_bf16 v[52:55], v[252:255], a[204:207], v[112:115]\n"
      "ds_read_b128 a[196:199], v0 offset:16512\n"
      "v_sub_f32 v31, v31, v43\n"
      "v_fmac_f32 v28, s46, v100\n"
      "v_fmac_f32 v29, s46, v101\n"
      "v_fmac_f32 v30, s46, v102\n"
      "s_waitcnt lgkmcnt(7)\n"
      "v_mfma_f32_4x4x4_16b_bf16 v[12:15], a[216:217], a[220:221], 0\n"
      "v_fmac_f32 v31, s46, v103\n"
      "v_mfma_f32_4x4x4_16b_bf16 v[16:19], a[216:217], a[222:223], 0\n"
      "v_fmac_f32 v40, s45, v28\n"
      "v_fmac_f32 v41, s45, v29\n"
      "v_fmac_f32 v42, s45, v30\n"
      "v_mfma_f32_16x16x32_bf16 v[56:59], v[244:247], a[204:207], v[116:119]\n"
      "v_fmac_f32 v43, s45, v31\n"
      "v_cvt_pk_bf16_f32 v12, v12, v16\n"
      "v_mfma_f32_16x16x32_bf16 v[60:63], v[236:239], a[204:207], v[120:123]\n"
      "ds_write_addtid_b32 v12 offset:24832\n"
      "v_fma_f32 v28, s47, v28, v76\n"
      "v_mfma_f32_16x16x32_bf16 v[64:67], v[248:251], a[208:211], v[112:115]\n"
      "ds_read_b128 a[200:203], v0 offset:20608\n"
      "v_fma_f32 v29, s47, v29, v77\n"
      "v_mfma_f32_16x16x32_bf16 v[68:71], v[240:243], a[208:211], v[116:119]\n"
      "v_cvt_pk_bf16_f32 v13, v13, v17\n"
      "v_fma_f32 v30, s47, v30, v78\n"
      "v_cvt_pk_bf16_f32 v14, v14, v18\n"
      "v_mfma_f32_16x16x32_bf16 v[72:75], v[232:235], a[208:211], v[120:123]\n"
      "ds_write_addtid_b32 v13 offset:28928\n"
      "ds_write_addtid_b32 v14 offset:33024\n"
      "v_mfma_f32_16x16x32_bf16 v[52:55], v[228:231], a[212:215], v[52:55]\n"
      "ds_read_b128 a[204:207], v1 offset:16384\n"
      "v_fma_f32 v31, s47, v31, v79\n"
      "v_mfma_f32_16x16x32_bf16 v[56:59], v[220:223], a[212:215], v[56:59]\n"
      "ds_read_b64_tr_b16 a[228:229], v7 offset:34816\n"
      "ds_read_b64_tr_b16 a[230:231], v7 offset:34824\n"
      "v_mfma_f32_16x16x32_bf16 v[60:63], v[212:215], a[212:215], v[60:63]\n"
      "v_cvt_pk_bf16_f32 v40, v40, v41\n"
      "v_cvt_pk_bf16_f32 v42, v42, v43\n"
      "v_cvt_pk_bf16_f32 v15, v15, v19\n"
      "s_nop 0\n"
      "s_waitcnt lgkmcnt(5)\n"
      "v_mfma_f32_4x4x4_16b_bf16 v[20:23], a[216:217], a[224:225], 0\n"
      "ds_write_addtid_b32 v40 offset:12288\n"
      "v_mfma_f32_4x4x4_16b_bf16 v[24:27], a[216:217], a[226:227], 0\n"
      "ds_write_addtid_b32 v42 offset:12800\n"
      "ds_write_addtid_b32 v15 offset:37120\n"
      "v_cvt_pk_bf16_f32 v28, v28, v29\n"
      "v_mfma_f32_16x16x32_bf16 v[64:67], v[224:227], a[192:195], v[64:67]\n"
      "ds_read_b128 a[208:211], v1 offset:20480\n"
      "ds_write_addtid_b32 v28 offset:14336\n"
      "v_mfma_f32_16x16x32_bf16 v[68:71], v[216:219], a[192:195], v[68:71]\n"
      "v_cvt_pk_bf16_f32 v30, v30, v31\n"
      "v_cvt_pk_bf16_f32 v20, v20, v24\n"
      "v_mfma_f32_16x16x32_bf16 v[72:75], v[208:211], a[192:195], v[72:75]\n"
      "ds_write_addtid_b32 v20 offset:25344\n"
      "ds_write_addtid_b32 v30 offset:14848\n"
      "v_mfma_f32_16x16x32_bf16 v[52:55], v[204:207], a[196:199], v[52:55]\n"
      "ds_read_b128 a[212:215], v1 offset:16448\n"
      "v_sub_f32 v32, v32, v44\n"
      "v_mfma_f32_16x16x32_bf16 v[56:59], v[196:199], a[196:199], v[56:59]\n"
      "v_cvt_pk_bf16_f32 v21, v21, v25\n"
      "v_sub_f32 v33, v33, v45\n"
      "v_mfma_f32_16x16x32_bf16 v[60:63], v[188:191], a[196:199], v[60:63]\n"
      "ds_write_addtid_b32 v21 offset:29440\n"
      "v_sub_f32 v34, v34, v46\n"
      "v_mfma_f32_16x16x32_bf16 v[64:67], v[200:203], a[200:203], v[64:67]\n"
      "ds_read_b128 a[192:195], v1 offset:20544\n"
      "v_cvt_pk_bf16_f32 v22, v22, v26\n"
      "v_mfma_f32_16x16x32_bf16 v[68:71], v[192:195], a[200:203], v[68:71]\n"
      "ds_write_addtid_b32 v22 offset:33536\n"
      "v_sub_f32 v35, v35, v47\n"
      "v_mfma_f32_16x16x32_bf16 v[72:75], v[184:187], a[200:203], v[72:75]\n"
      "v_cvt_pk_bf16_f32 v23, v23, v27\n"
      "v_fmac_f32 v32, s46, v104\n"
      "v_fmac_f32_e64 v33, s46, v105\n"
      "s_waitcnt lgkmcnt(3)\n"
      "v_mfma_f32_16x16x32_bf16 v[52:55], v[180:183], a[204:207], v[52:55]\n"
      "ds_read_b128 a[196:199], v1 offset:16512\n"
      "ds_read_b64_tr_b16 a[232:233], v7 offset:35328\n"
      "v_mfma_f32_4x4x4_16b_bf16 v[12:15], a[216:217], a[228:229], 0\n"
      "ds_write_addtid_b32 v23 offset:37632\n"
      "v_mfma_f32_4x4x4_16b_bf16 v[16:19], a[216:217], a[230:231], 0\n"
      "ds_read_b64_tr_b16 a[234:235], v7 offset:35336\n"
      "v_fmac_f32 v34, s46, v106\n"
      "v_fmac_f32 v35, s46, v107\n"
      "v_mfma_f32_16x16x32_bf16 v[56:59], v[172:175], a[204:207], v[56:59]\n"
      "v_fmac_f32_e64 v44, s45, v32\n"
      "v_cvt_pk_bf16_f32 v12, v12, v16\n"
      "v_mfma_f32_16x16x32_bf16 v[60:63], v[164:167], a[204:207], v[60:63]\n"
      "ds_write_addtid_b32 v12 offset:25856\n"
      "v_fmac_f32 v45, s45, v33\n"
      "v_mfma_f32_16x16x32_bf16 v[64:67], v[176:179], a[208:211], v[64:67]\n"
      "ds_read_b128 a[200:203], v1 offset:20608\n"
      "v_cvt_pk_bf16_f32 v13, v13, v17\n"
      "v_mfma_f32_16x16x32_bf16 v[68:71], v[168:171], a[208:211], v[68:71]\n"
      "ds_write_addtid_b32 v13 offset:29952\n"
      "v_fmac_f32 v46, s45, v34\n"
      "v_fmac_f32_e64 v47, s45, v35\n"
      "v_mfma_f32_16x16x32_bf16 v[72:75], v[160:163], a[208:211], v[72:75]\n"
      "v_cvt_pk_bf16_f32 v14, v14, v18\n"
      "v_fma_f32 v32, s47, v32, v80\n"
      "v_mfma_f32_16x16x32_bf16 v[52:55], v[156:159], a[212:215], v[52:55]\n"
      "ds_read_b128 a[204:207], v2 offset:16384\n"
      "ds_write_addtid_b32 v14 offset:34048\n"
      "v_mfma_f32_16x16x32_bf16 v[56:59], v[148:151], a[212:215], v[56:59]\n"
      "v_fma_f32 v33, s47, v33, v81\n"
      "v_cvt_pk_bf16_f32 v15, v15, v19\n"
      "v_mfma_f32_16x16x32_bf16 v[60:63], v[140:143], a[212:215], v[60:63]\n"
      "ds_write_addtid_b32 v15 offset:38144\n"
      "v_fma_f32 v34, s47, v34, v82\n"
      "v_fma_f32 v35, s47, v35, v83\n"
      "s_nop 0\n"
      "s_waitcnt lgkmcnt(3)\n"
      "v_mfma_f32_16x16x32_bf16 v[64:67], v[152:155], a[192:195], v[64:67]\n"
      "ds_read_b128 a[208:211], v2 offset:20480\n"
      "ds_read_b64_tr_b16 a[220:221], v7 offset:35840\n"
      "v_mfma_f32_16x16x32_bf16 v[68:71], v[144:147], a[192:195], v[68:71]\n"
      "ds_read_b64_tr_b16 a[222:223], v7 offset:35848\n"
      "v_cvt_pk_bf16_f32 v44, v44, v45\n"
      "v_mfma_f32_16x16x32_bf16 v[72:75], v[136:139], a[192:195], v[72:75]\n"
      "ds_write_addtid_b32 v44 offset:20484\n"
      "v_cvt_pk_bf16_f32 v46, v46, v47\n"
      "v_cvt_pk_bf16_f32 v32, v32, v33\n"
      "v_mfma_f32_4x4x4_16b_bf16 v[20:23], a[216:217], a[232:233], 0\n"
      "v_cvt_pk_bf16_f32 v34, v34, v35\n"
      "v_mfma_f32_4x4x4_16b_bf16 v[24:27], a[216:217], a[234:235], 0\n"
      "v_sub_f32 v36, v36, v48\n"
      "v_sub_f32 v37, v37, v49\n"
      "v_sub_f32_e64 v38, v38, v50\n"
      "v_mfma_f32_16x16x32_bf16 v[52:55], v[132:135], a[196:199], v[52:55]\n"
      "ds_read_b128 a[212:215], v2 offset:16448\n"
      "ds_write_addtid_b32 v46 offset:20996\n"
      "v_mfma_f32_16x16x32_bf16 v[56:59], v[124:127], a[196:199], v[56:59]\n"
      "ds_write_addtid_b32 v32 offset:22532\n"
      "ds_write_addtid_b32 v34 offset:23044\n"
      "v_mfma_f32_16x16x32_bf16 v[60:63], a[184:187], a[196:199], v[60:63]\n"
      "v_cvt_pk_bf16_f32 v20, v20, v24\n"
      "v_cvt_pk_bf16_f32 v21, v21, v25\n"
      "v_mfma_f32_16x16x32_bf16 v[64:67], v[128:131], a[200:203], v[64:67]\n"
      "ds_read_b128 a[192:195], v2 offset:20544\n"
      "ds_write_addtid_b32 v20 offset:26368\n"
      "v_mfma_f32_16x16x32_bf16 v[68:71], a[188:191], a[200:203], v[68:71]\n"
      "ds_write_addtid_b32 v21 offset:30464\n"
      "v_sub_f32 v39, v39, v51\n"
      "v_mfma_f32_16x16x32_bf16 v[72:75], a[180:183], a[200:203], v[72:75]\n"
      "v_cvt_pk_bf16_f32 v22, v22, v26\n"
      "v_fmac_f32_e64 v36, s46, v108\n"
      "s_waitcnt lgkmcnt(4)\n"
      "v_mfma_f32_16x16x32_bf16 v[52:55], a[176:179], a[204:207], v[52:55]\n"
      "ds_read_b128 a[196:199], v2 offset:16512\n"
      "ds_write_addtid_b32 v22 offset:34560\n"
      "v_mfma_f32_16x16x32_bf16 v[56:59], a[168:171], a[204:207], v[56:59]\n"
      "ds_read_b64_tr_b16 a[224:225], v7 offset:36352\n"
      "ds_read_b64_tr_b16 a[226:227], v7 offset:36360\n"
      "v_mfma_f32_16x16x32_bf16 v[60:63], a[160:163], a[204:207], v[60:63]\n"
      "v_cvt_pk_bf16_f32 v23, v23, v27\n"
      "v_fmac_f32 v37, s46, v109\n"
      "v_fmac_f32 v38, s46, v110\n"
      "v_mfma_f32_4x4x4_16b_bf16 v[12:15], a[216:217], a[220:221], 0\n"
      "v_fmac_f32 v39, s46, v111\n"
      "v_mfma_f32_4x4x4_16b_bf16 v[16:19], a[216:217], a[222:223], 0\n"
      "v_fmac_f32 v48, s45, v36\n"
      "v_fmac_f32_e64 v49, s45, v37\n"
      "v_fmac_f32_e64 v50, s45, v38\n"
      "v_mfma_f32_16x16x32_bf16 v[64:67], a[172:175], a[208:211], v[64:67]\n"
      "ds_read_b128 a[200:203], v2 offset:20608\n"
      "ds_write_addtid_b32 v23 offset:38656\n"
      "v_mfma_f32_16x16x32_bf16 v[68:71], a[164:167], a[208:211], v[68:71]\n"
      "v_cvt_pk_bf16_f32 v12, v12, v16\n"
      "v_cvt_pk_bf16_f32 v13, v13, v17\n"
      "v_mfma_f32_16x16x32_bf16 v[72:75], a[156:159], a[208:211], v[72:75]\n"
      "ds_write_addtid_b32 v12 offset:26880\n"
      "ds_write_addtid_b32 v13 offset:30976\n"
      "v_mfma_f32_16x16x32_bf16 v[52:55], a[152:155], a[212:215], v[52:55]\n"
      "ds_read_b128 a[204:207], v3 offset:50688\n"
      "v_fmac_f32 v51, s45, v39\n"
      "v_mfma_f32_16x16x32_bf16 v[56:59], a[144:147], a[212:215], v[56:59]\n"
      "v_fma_f32 v36, s47, v36, v84\n"
      "v_cvt_pk_bf16_f32 v14, v14, v18\n"
      "v_mfma_f32_16x16x32_bf16 v[60:63], a[136:139], a[212:215], v[60:63]\n"
      "ds_write_addtid_b32 v14 offset:35072\n"
      "v_fma_f32 v37, s47, v37, v85\n"
      "v_cvt_pk_bf16_f32 v15, v15, v19\n"
      "s_waitcnt lgkmcnt(4)\n"
      "v_mfma_f32_16x16x32_bf16 v[64:67], a[148:151], a[192:195], v[64:67]\n"
      "ds_read_b128 a[208:211], v3 offset:50752\n"
      "ds_write_addtid_b32 v15 offset:39168\n"
      "v_mfma_f32_16x16x32_bf16 v[68:71], a[140:143], a[192:195], v[68:71]\n"
      "ds_read_b64_tr_b16 a[228:229], v7 offset:36864\n"
      "ds_read_b64_tr_b16 a[230:231], v7 offset:36872\n"
      "v_mfma_f32_16x16x32_bf16 v[72:75], a[132:135], a[192:195], v[72:75]\n"
      "v_fma_f32 v38, s47, v38, v86\n"
      "v_fma_f32 v39, s47, v39, v87\n"
      "v_cvt_pk_bf16_f32 v48, v48, v49\n"
      "v_mfma_f32_16x16x32_bf16 v[52:55], a[128:131], a[196:199], v[52:55]\n"
      "ds_read_b128 a[212:215], v3 offset:50816\n"
      "ds_write_addtid_b32 v48 offset:16384\n"
      "v_mfma_f32_4x4x4_16b_bf16 v[20:23], a[216:217], a[224:225], 0\n"
      "v_cvt_pk_bf16_f32 v50, v50, v51\n"
      "v_mfma_f32_4x4x4_16b_bf16 v[24:27], a[216:217], a[226:227], 0\n"
      "v_cvt_pk_bf16_f32 v36, v36, v37\n"
      "v_cvt_pk_bf16_f32 v38, v38, v39\n"
      "v_mfma_f32_16x16x32_bf16 v[56:59], a[120:123], a[196:199], v[56:59]\n"
      "ds_write_addtid_b32 v50 offset:16896\n"
      "ds_write_addtid_b32 v36 offset:18432\n"
      "v_cvt_pk_bf16_f32 v20, v20, v24\n"
      "v_mfma_f32_16x16x32_bf16 v[60:63], a[112:115], a[196:199], v[60:63]\n"
      "ds_write_addtid_b32 v20 offset:27392\n"
      "ds_write_addtid_b32 v38 offset:18944\n"
      "v_mfma_f32_16x16x32_bf16 v[64:67], a[124:127], a[200:203], v[64:67]\n"
      "ds_read_b128 a[192:195], v4 offset:50688\n"
      "v_cvt_pk_bf16_f32 v21, v21, v25\n"
      "v_mfma_f32_16x16x32_bf16 v[68:71], a[116:119], a[200:203], v[68:71]\n"
      "ds_write_addtid_b32 v21 offset:31488\n"
      "v_cvt_pk_bf16_f32 v22, v22, v26\n"
      "v_mfma_f32_16x16x32_bf16 v[72:75], a[108:111], a[200:203], v[72:75]\n"
      "ds_write_addtid_b32 v22 offset:35584\n"
      "v_cvt_pk_bf16_f32 v23, v23, v27\n"
      "v_sub_f32 v52, v64, v52\n"
      "s_waitcnt lgkmcnt(5)\n"
      "v_mfma_f32_16x16x32_bf16 v[88:91], a[0:3], a[204:207], 0\n"
      "ds_read_b128 a[196:199], v4 offset:50752\n"
      "ds_write_addtid_b32 v52 offset:6144\n"
      "v_mfma_f32_16x16x32_bf16 v[92:95], a[4:7], a[204:207], 0\n"
      "ds_write_addtid_b32 v23 offset:39680\n"
      "v_sub_f32 v53, v65, v53\n"
      "v_mfma_f32_16x16x32_bf16 v[96:99], a[8:11], a[204:207], 0\n"
      "ds_write_addtid_b32 v53 offset:6656\n"
      "v_sub_f32 v54, v66, v54\n"
      "v_sub_f32_e64 v55, v67, v55\n"
      "v_mfma_f32_16x16x32_bf16 v[88:91], a[12:15], a[208:211], v[88:91]\n"
      "ds_read_b128 a[200:203], v4 offset:50816\n"
      "ds_write_addtid_b32 v54 offset:7168\n"
      "v_mfma_f32_4x4x4_16b_bf16 v[12:15], a[216:217], a[228:229], 0\n"
      "ds_write_addtid_b32 v55 offset:7680\n"
      "v_mfma_f32_4x4x4_16b_bf16 v[16:19], a[216:217], a[230:231], 0\n"
      "v_sub_f32 v56, v68, v56\n"
      "v_sub_f32 v57, v69, v57\n"
      "v_sub_f32_e64 v58, v70, v58\n"
      "v_mfma_f32_16x16x32_bf16 v[92:95], a[16:19], a[208:211], v[92:95]\n"
      "ds_write_addtid_b32 v56 offset:8192\n"
      "ds_write_addtid_b32 v57 offset:8704\n"
      "v_mfma_f32_16x16x32_bf16 v[96:99], a[20:23], a[208:211], v[96:99]\n"
      "ds_write_addtid_b32 v58 offset:9216\n"
      "v_sub_f32 v59, v71, v59\n"
      "v_mfma_f32_16x16x32_bf16 v[88:91], a[24:27], a[212:215], v[88:91]\n"
      "ds_read_b128 a[204:207], v5 offset:50688\n"
      "ds_write_addtid_b32 v59 offset:9728\n"
      "v_mfma_f32_16x16x32_bf16 v[92:95], a[28:31], a[212:215], v[92:95]\n"
      "v_cvt_pk_bf16_f32 v12, v12, v16\n"
      "v_sub_f32 v60, v72, v60\n"
      "v_mfma_f32_16x16x32_bf16 v[96:99], a[32:35], a[212:215], v[96:99]\n"
      "ds_write_addtid_b32 v12 offset:27904\n"
      "ds_write_addtid_b32 v60 offset:10240\n"
      "v_cvt_pk_bf16_f32 v13, v13, v17\n"
      "s_waitcnt lgkmcnt(5)\n"
      "v_mfma_f32_16x16x32_bf16 v[88:91], a[36:39], a[192:195], v[88:91]\n"
      "ds_read_b128 a[208:211], v5 offset:50752\n"
      "ds_write_addtid_b32 v13 offset:32000\n"
      "v_mfma_f32_16x16x32_bf16 v[92:95], a[40:43], a[192:195], v[92:95]\n"
      "v_sub_f32 v61, v73, v61\n"
      "v_sub_f32 v62, v74, v62\n"
      "v_mfma_f32_16x16x32_bf16 v[96:99], a[44:47], a[192:195], v[96:99]\n"
      "ds_write_addtid_b32 v61 offset:10752\n"
      "ds_write_addtid_b32 v62 offset:11264\n"
      "v_sub_f32 v63, v75, v63\n"
      "v_mfma_f32_16x16x32_bf16 v[88:91], a[48:51], a[196:199], v[88:91]\n"
      "ds_read_b128 a[212:215], v5 offset:50816\n"
      "ds_write_addtid_b32 v63 offset:11776\n"
      "v_cvt_pk_bf16_f32 v14, v14, v18\n"
      "v_cvt_pk_bf16_f32 v15, v15, v19\n"
      "v_mfma_f32_16x16x32_bf16 v[92:95], a[52:55], a[196:199], v[92:95]\n"
      "ds_write_addtid_b32 v14 offset:36096\n"
      "ds_write_addtid_b32 v15 offset:40192\n"
      "v_mfma_f32_16x16x32_bf16 v[96:99], a[56:59], a[196:199], v[96:99]\n"
      "v_mfma_f32_16x16x32_bf16 v[88:91], a[60:63], a[200:203], v[88:91]\n"
      "v_fma_f32 v40, 0.5, v52, -v64\n"
      "v_fma_f32 v41, 0.5, v53, -v65\n"
      "v_fma_f32 v42, 0.5, v54, -v66\n"
      "v_fma_f32 v43, 0.5, v55, -v67\n"
      "v_mfma_f32_16x16x32_bf16 v[92:95], a[64:67], a[200:203], v[92:95]\n"
      "v_fma_f32 v44, 0.5, v56, -v68\n"
      "v_fma_f32 v45, 0.5, v57, -v69\n"
      "v_fma_f32 v46, 0.5, v58, -v70\n"
      "v_fma_f32 v47, 0.5, v59, -v71\n"
      "v_mfma_f32_16x16x32_bf16 v[96:99], a[68:71], a[200:203], v[96:99]\n"
      "v_fma_f32 v48, 0.5, v60, -v72\n"
      "v_fma_f32 v49, 0.5, v61, -v73\n"
      "v_fma_f32 v50, 0.5, v62, -v74\n"
      "v_fma_f32 v51, 0.5, v63, -v75\n"
      "s_waitcnt lgkmcnt(0)\n"
      "s_barrier\n"
      "LSmid_%=:\n"
      "v_mfma_f32_16x16x32_bf16 v[88:91], a[72:75], a[204:207], v[88:91]\n"
      "ds_read_b128 a[192:195], v0 offset:0\n"
      "ds_read2st64_b32 v[28:29], v6 offset0:24 offset1:26\n"
      "v_mfma_f32_16x16x32_bf16 v[92:95], a[76:79], a[204:207], v[92:95]\n"
      "ds_read_b128 a[196:199], v0 offset:4096\n"
      "ds_read_b64_tr_b16 a[220:221], v7 offset:50688\n"
      "v_mfma_f32_16x16x32_bf16 v[96:99], a[80:83], a[204:207], v[96:99]\n"
      "ds_read2st64_b32 v[30:31], v6 offset0:28 offset1:30\n"
      "ds_read2st64_b32 v[32:33], v6 offset0:32 offset1:34\n"
      "v_mfma_f32_16x16x32_bf16 v[88:91], a[84:87], a[208:211], v[88:91]\n"
      "ds_read_b128 a[200:203], v0 offset:64\n"
      "v_mfma_f32_16x16x32_bf16 v[92:95], a[88:91], a[208:211], v[92:95]\n"
      "v_mfma_f32_16x16x32_bf16 v[96:99], a[92:95], a[208:211], v[96:99]\n"
      "ds_read_b64_tr_b16 a[222:223], v7 offset:50696\n"
      "v_mfma_f32_16x16x32_bf16 v[88:91], a[96:99], a[212:215], v[88:91]\n"
      "ds_read_b128 a[204:207], v0 offset:4160\n"
      "ds_read2st64_b32 v[34:35], v6 offset0:36 offset1:38\n"
      "v_mfma_f32_16x16x32_bf16 v[92:95], a[100:103], a[212:215], v[92:95]\n"
      "ds_read_b64_tr_b16 a[224:225], v7 offset:51200\n"
      "ds_read2st64_b32 v[36:37], v6 offset0:40 offset1:42\n"
      "v_mfma_f32_16x16x32_bf16 v[96:99], a[104:107], a[212:215], v[96:99]\n"
      "ds_read_b64_tr_b16 a[226:227], v7 offset:51208\n"
      "s_waitcnt lgkmcnt(9)\n"
      "v_mfma_f32_16x16x32_bf16 v[52:55], v[252:255], a[192:195], v[76:79]\n"
      "ds_read_b128 a[208:211], v0 offset:128\n"
      "s_waitcnt lgkmcnt(8)\n"
      "v_sub_f32 v28, v28, v40\n"
      "v_sub_f32 v29, v29, v41\n"
      "v_sub_f32 v30, v30, v42\n"
      "v_sub_f32 v31, v31, v43\n"
      "s_waitcnt lgkmcnt(6)\n"
      "v_mfma_f32_4x4x4_16b_bf16 v[12:15], a[216:217], a[220:221], 0\n"
      "ds_read2st64_b32 v[38:39], v6 offset0:44 offset1:46\n"
      "v_fmac_f32 v28, s46, v112\n"
      "v_fmac_f32 v29, s46, v113\n"
      "v_fmac_f32 v30, s46, v114\n"
      "v_fmac_f32 v31, s46, v115\n"
      "v_mfma_f32_4x4x4_16b_bf16 v[16:19], a[216:217], a[222:223], 0\n"
      "v_fmac_f32 v40, s45, v28\n"
      "v_fmac_f32 v41, s45, v29\n"
      "v_fmac_f32 v42, s45, v30\n"
      "v_mfma_f32_16x16x32_bf16 v[56:59], v[244:247], a[192:195], v[80:83]\n"
      "v_fmac_f32_e64 v43, s45, v31\n"
      "v_cvt_pk_bf16_f32 v12, v12, v16\n"
      "v_mfma_f32_16x16x32_bf16 v[60:63], v[236:239], a[192:195], v[84:87]\n"
      "ds_write_addtid_b32 v12 offset:41216\n"
      "v_fma_f32 v28, s47, v28, v88\n"
      "v_mfma_f32_16x16x32_bf16 v[64:67], v[248:251], a[196:199], v[76:79]\n"
      "ds_read_b128 a[212:215], v0 offset:4224\n"
      "v_fma_f32 v29, s47, v29, v89\n"
      "v_mfma_f32_16x16x32_bf16 v[68:71], v[240:243], a[196:199], v[80:83]\n"
      "v_cvt_pk_bf16_f32 v13, v13, v17\n"
      "v_fma_f32 v30, s47, v30, v90\n"
      "v_cvt_pk_bf16_f32 v14, v14, v18\n"
      "v_mfma_f32_16x16x32_bf16 v[72:75], v[232:235], a[196:199], v[84:87]\n"
      "ds_write_addtid_b32 v13 offset:45312\n"
      "ds_write_addtid_b32 v14 offset:49408\n"
      "v_mfma_f32_16x16x32_bf16 v[52:55], v[228:231], a[200:203], v[52:55]\n"
      "ds_read_b128 a[192:195], v1 offset:0\n"
      "v_fma_f32 v31, s47, v31, v91\n"
      "v_mfma_f32_16x16x32_bf16 v[56:59], v[220:223], a[200:203], v[56:59]\n"
      "ds_read_b64_tr_b16 a[228:229], v7 offset:51712\n"
      "ds_read_b64_tr_b16 a[230:231], v7 offset:51720\n"
      "v_mfma_f32_16x16x32_bf16 v[60:63], v[212:215], a[200:203], v[60:63]\n"
      "v_cvt_pk_bf16_f32 v40, v40, v41\n"
      "v_cvt_pk_bf16_f32 v42, v42, v43\n"
      "v_cvt_pk_bf16_f32 v15, v15, v19\n"
      "s_nop 0\n"
      "s_waitcnt lgkmcnt(5)\n"
      "v_mfma_f32_4x4x4_16b_bf16 v[20:23], a[216:217], a[224:225], 0\n"
      "ds_write_addtid_b32 v40 offset:13312\n"
      "v_mfma_f32_4x4x4_16b_bf16 v[24:27], a[216:217], a[226:227], 0\n"
      "ds_write_addtid_b32 v42 offset:13824\n"
      "ds_write_addtid_b32 v15 offset:53504\n"
      "v_cvt_pk_bf16_f32 v28, v28, v29\n"
      "v_mfma_f32_16x16x32_bf16 v[64:67], v[224:227], a[204:207], v[64:67]\n"
      "ds_read_b128 a[196:199], v1 offset:4096\n"
      "ds_write_addtid_b32 v28 offset:15360\n"
      "v_mfma_f32_16x16x32_bf16 v[68:71], v[216:219], a[204:207], v[68:71]\n"
      "v_cvt_pk_bf16_f32 v30, v30, v31\n"
      "v_cvt_pk_bf16_f32 v20, v20, v24\n"
      "v_mfma_f32_16x16x32_bf16 v[72:75], v[208:211], a[204:207], v[72:75]\n"
      "ds_write_addtid_b32 v20 offset:41728\n"
      "ds_write_addtid_b32 v30 offset:15872\n"
      "v_mfma_f32_16x16x32_bf16 v[52:55], v[204:207], a[208:211], v[52:55]\n"
      "ds_read_b128 a[200:203], v1 offset:64\n"
      "v_sub_f32 v32, v32, v44\n"
      "v_mfma_f32_16x16x32_bf16 v[56:59], v[196:199], a[208:211], v[56:59]\n"
      "v_cvt_pk_bf16_f32 v21, v21, v25\n"
      "v_sub_f32 v33, v33, v45\n"
      "v_mfma_f32_16x16x32_bf16 v[60:63], v[188:191], a[208:211], v[60:63]\n"
      "ds_write_addtid_b32 v21 offset:45824\n"
      "v_sub_f32 v34, v34, v46\n"
      "v_mfma_f32_16x16x32_bf16 v[64:67], v[200:203], a[212:215], v[64:67]\n"
      "ds_read_b128 a[204:207], v1 offset:4160\n"
      "v_cvt_pk_bf16_f32 v22, v22, v26\n"
      "v_mfma_f32_16x16x32_bf16 v[68:71], v[192:195], a[212:215], v[68:71]\n"
      "ds_write_addtid_b32 v22 offset:49920\n"
      "v_sub_f32 v35, v35, v47\n"
      "v_mfma_f32_16x16x32_bf16 v[72:75], v[184:187], a[212:215], v[72:75]\n"
      "v_cvt_pk_bf16_f32 v23, v23, v27\n"
      "v_fmac_f32 v32, s46, v116\n"
      "v_fmac_f32_e64 v33, s46, v117\n"
      "s_waitcnt lgkmcnt(3)\n"
      "v_mfma_f32_16x16x32_bf16 v[52:55], v[180:183], a[192:195], v[52:55]\n"
      "ds_read_b128 a[208:211], v1 offset:128\n"
      "ds_read_b64_tr_b16 a[232:233], v7 offset:52224\n"
      "v_mfma_f32_4x4x4_16b_bf16 v[12:15], a[216:217], a[228:229], 0\n"
      "ds_write_addtid_b32 v23 offset:54016\n"
      "v_mfma_f32_4x4x4_16b_bf16 v[16:19], a[216:217], a[230:231], 0\n"
      "ds_read_b64_tr_b16 a[234:235], v7 offset:52232\n"
      "v_fmac_f32 v34, s46, v118\n"
      "v_fmac_f32 v35, s46, v119\n"
      "v_mfma_f32_16x16x32_bf16 v[56:59], v[172:175], a[192:195], v[56:59]\n"
      "v_fmac_f32 v44, s45, v32\n"
      "v_cvt_pk_bf16_f32 v12, v12, v16\n"
      "v_mfma_f32_16x16x32_bf16 v[60:63], v[164:167], a[192:195], v[60:63]\n"
      "ds_write_addtid_b32 v12 offset:42240\n"
      "v_fmac_f32 v45, s45, v33\n"
      "v_mfma_f32_16x16x32_bf16 v[64:67], v[176:179], a[196:199], v[64:67]\n"
      "ds_read_b128 a[212:215], v1 offset:4224\n"
      "v_cvt_pk_bf16_f32 v13, v13, v17\n"
      "v_mfma_f32_16x16x32_bf16 v[68:71], v[168:171], a[196:199], v[68:71]\n"
      "ds_write_addtid_b32 v13 offset:46336\n"
      "v_fmac_f32 v46, s45, v34\n"
      "v_fmac_f32 v47, s45, v35\n"
      "v_mfma_f32_16x16x32_bf16 v[72:75], v[160:163], a[196:199], v[72:75]\n"
      "v_cvt_pk_bf16_f32 v14, v14, v18\n"
      "v_fma_f32 v32, s47, v32, v92\n"
      "v_mfma_f32_16x16x32_bf16 v[52:55], v[156:159], a[200:203], v[52:55]\n"
      "ds_read_b128 a[192:195], v2 offset:0\n"
      "ds_write_addtid_b32 v14 offset:50432\n"
      "v_mfma_f32_16x16x32_bf16 v[56:59], v[148:151], a[200:203], v[56:59]\n"
      "v_fma_f32 v33, s47, v33, v93\n"
      "v_cvt_pk_bf16_f32 v15, v15, v19\n"
      "v_mfma_f32_16x16x32_bf16 v[60:63], v[140:143], a[200:203], v[60:63]\n"
      "ds_write_addtid_b32 v15 offset:54528\n"
      "v_fma_f32 v34, s47, v34, v94\n"
      "v_fma_f32 v35, s47, v35, v95\n"
      "s_nop 0\n"
      "s_waitcnt lgkmcnt(3)\n"
      "v_mfma_f32_16x16x32_bf16 v[64:67], v[152:155], a[204:207], v[64:67]\n"
      "ds_read_b128 a[196:199], v2 offset:4096\n"
      "ds_read_b64_tr_b16 a[220:221], v7 offset:52736\n"
      "v_mfma_f32_16x16x32_bf16 v[68:71], v[144:147], a[204:207], v[68:71]\n"
      "ds_read_b64_tr_b16 a[222:223], v7 offset:52744\n"
      "v_cvt_pk_bf16_f32 v44, v44, v45\n"
      "v_mfma_f32_16x16x32_bf16 v[72:75], v[136:139], a[204:207], v[72:75]\n"
      "ds_write_addtid_b32 v44 offset:21508\n"
      "v_cvt_pk_bf16_f32 v46, v46, v47\n"
      "v_cvt_pk_bf16_f32 v32, v32, v33\n"
      "v_mfma_f32_4x4x4_16b_bf16 v[20:23], a[216:217], a[232:233], 0\n"
      "v_cvt_pk_bf16_f32 v34, v34, v35\n"
      "v_mfma_f32_4x4x4_16b_bf16 v[24:27], a[216:217], a[234:235], 0\n"
      "v_sub_f32_e64 v36, v36, v48\n"
      "v_sub_f32_e64 v37, v37, v49\n"
      "v_sub_f32_e64 v38, v38, v50\n"
      "v_mfma_f32_16x16x32_bf16 v[52:55], v[132:135], a[208:211], v[52:55]\n"
      "ds_read_b128 a[200:203], v2 offset:64\n"
      "ds_write_addtid_b32 v46 offset:22020\n"
      "v_mfma_f32_16x16x32_bf16 v[56:59], v[124:127], a[208:211], v[56:59]\n"
      "ds_write_addtid_b32 v32 offset:23556\n"
      "ds_write_addtid_b32 v34 offset:24068\n"
      "v_mfma_f32_16x16x32_bf16 v[60:63], a[184:187], a[208:211], v[60:63]\n"
      "v_cvt_pk_bf16_f32 v20, v20, v24\n"
      "v_cvt_pk_bf16_f32 v21, v21, v25\n"
      "v_mfma_f32_16x16x32_bf16 v[64:67], v[128:131], a[212:215], v[64:67]\n"
      "ds_read_b128 a[204:207], v2 offset:4160\n"
      "ds_write_addtid_b32 v20 offset:42752\n"
      "v_mfma_f32_16x16x32_bf16 v[68:71], a[188:191], a[212:215], v[68:71]\n"
      "ds_write_addtid_b32 v21 offset:46848\n"
      "v_sub_f32 v39, v39, v51\n"
      "v_mfma_f32_16x16x32_bf16 v[72:75], a[180:183], a[212:215], v[72:75]\n"
      "v_cvt_pk_bf16_f32 v22, v22, v26\n"
      "v_fmac_f32_e64 v36, s46, v120\n"
      "s_waitcnt lgkmcnt(4)\n"
      "v_mfma_f32_16x16x32_bf16 v[52:55], a[176:179], a[192:195], v[52:55]\n"
      "ds_read_b128 a[208:211], v2 offset:128\n"
      "ds_write_addtid_b32 v22 offset:50944\n"
      "v_mfma_f32_16x16x32_bf16 v[56:59], a[168:171], a[192:195], v[56:59]\n"
      "ds_read_b64_tr_b16 a[224:225], v7 offset:53248\n"
      "ds_read_b64_tr_b16 a[226:227], v7 offset:53256\n"
      "v_mfma_f32_16x16x32_bf16 v[60:63], a[160:163], a[192:195], v[60:63]\n"
      "v_cvt_pk_bf16_f32 v23, v23, v27\n"
      "v_fmac_f32_e64 v37, s46, v121\n"
      "v_fmac_f32_e64 v38, s46, v122\n"
      "v_mfma_f32_4x4x4_16b_bf16 v[12:15], a[216:217], a[220:221], 0\n"
      "v_fmac_f32_e64 v39, s46, v123\n"
      "v_mfma_f32_4x4x4_16b_bf16 v[16:19], a[216:217], a[222:223], 0\n"
      "v_fmac_f32_e64 v48, s45, v36\n"
      "v_fmac_f32_e64 v49, s45, v37\n"
      "v_fmac_f32_e64 v50, s45, v38\n"
      "v_mfma_f32_16x16x32_bf16 v[64:67], a[172:175], a[196:199], v[64:67]\n"
      "ds_read_b128 a[212:215], v2 offset:4224\n"
      "ds_write_addtid_b32 v23 offset:55040\n"
      "v_mfma_f32_16x16x32_bf16 v[68:71], a[164:167], a[196:199], v[68:71]\n"
      "v_cvt_pk_bf16_f32 v12, v12, v16\n"
      "v_cvt_pk_bf16_f32 v13, v13, v17\n"
      "v_mfma_f32_16x16x32_bf16 v[72:75], a[156:159], a[196:199], v[72:75]\n"
      "ds_write_addtid_b32 v12 offset:43264\n"
      "ds_write_addtid_b32 v13 offset:47360\n"
      "v_mfma_f32_16x16x32_bf16 v[52:55], a[152:155], a[200:203], v[52:55]\n"
      "ds_read_b128 a[192:195], v3 offset:0\n"
      "v_fmac_f32 v51, s45, v39\n"
      "v_mfma_f32_16x16x32_bf16 v[56:59], a[144:147], a[200:203], v[56:59]\n"
      "v_fma_f32 v36, s47, v36, v96\n"
      "v_cvt_pk_bf16_f32 v14, v14, v18\n"
      "v_mfma_f32_16x16x32_bf16 v[60:63], a[136:139], a[200:203], v[60:63]\n"
      "ds_write_addtid_b32 v14 offset:51456\n"
      "v_fma_f32 v37, s47, v37, v97\n"
      "v_cvt_pk_bf16_f32 v15, v15, v19\n"
      "s_waitcnt lgkmcnt(5)\n"
      "v_mfma_f32_16x16x32_bf16 v[64:67], a[148:151], a[204:207], v[64:67]\n"
      "ds_read_b128 a[196:199], v3 offset:64\n"
      "ds_write_addtid_b32 v15 offset:55552\n"
      "v_mfma_f32_16x16x32_bf16 v[68:71], a[140:143], a[204:207], v[68:71]\n"
      "ds_read_b64_tr_b16 a[228:229], v7 offset:53760\n"
      "ds_read_b64_tr_b16 a[230:231], v7 offset:53768\n"
      "v_mfma_f32_16x16x32_bf16 v[72:75], a[132:135], a[204:207], v[72:75]\n"
      "v_fma_f32 v38, s47, v38, v98\n"
      "v_fma_f32 v39, s47, v39, v99\n"
      "v_cvt_pk_bf16_f32 v48, v48, v49\n"
      "v_mfma_f32_16x16x32_bf16 v[52:55], a[128:131], a[208:211], v[52:55]\n"
      "ds_read_b128 a[200:203], v3 offset:128\n"
      "ds_write_addtid_b32 v48 offset:17408\n"
      "v_mfma_f32_4x4x4_16b_bf16 v[20:23], a[216:217], a[224:225], 0\n"
      "v_cvt_pk_bf16_f32 v50, v50, v51\n"
      "v_mfma_f32_4x4x4_16b_bf16 v[24:27], a[216:217], a[226:227], 0\n"
      "v_cvt_pk_bf16_f32 v36, v36, v37\n"
      "v_cvt_pk_bf16_f32 v38, v38, v39\n"
      "v_mfma_f32_16x16x32_bf16 v[56:59], a[120:123], a[208:211], v[56:59]\n"
      "ds_write_addtid_b32 v50 offset:17920\n"
      "ds_write_addtid_b32 v36 offset:19456\n"
      "v_cvt_pk_bf16_f32 v20, v20, v24\n"
      "v_mfma_f32_16x16x32_bf16 v[60:63], a[112:115], a[208:211], v[60:63]\n"
      "ds_write_addtid_b32 v20 offset:43776\n"
      "ds_write_addtid_b32 v38 offset:19968\n"
      "v_mfma_f32_16x16x32_bf16 v[64:67], a[124:127], a[212:215], v[64:67]\n"
      "ds_read_b128 a[204:207], v4 offset:0\n"
      "v_cvt_pk_bf16_f32 v21, v21, v25\n"
      "v_mfma_f32_16x16x32_bf16 v[68:71], a[116:119], a[212:215], v[68:71]\n"
      "ds_write_addtid_b32 v21 offset:47872\n"
      "v_cvt_pk_bf16_f32 v22, v22, v26\n"
      "v_mfma_f32_16x16x32_bf16 v[72:75], a[108:111], a[212:215], v[72:75]\n"
      "ds_write_addtid_b32 v22 offset:51968\n"
      "v_cvt_pk_bf16_f32 v23, v23, v27\n"
      "v_sub_f32 v52, v64, v52\n"
      "s_waitcnt lgkmcnt(5)\n"
      "v_mfma_f32_16x16x32_bf16 v[100:103], a[0:3], a[192:195], 0\n"
      "ds_read_b128 a[208:211], v4 offset:64\n"
      "ds_write_addtid_b32 v52 offset:0\n"
      "v_mfma_f32_16x16x32_bf16 v[104:107], a[4:7], a[192:195], 0\n"
      "ds_write_addtid_b32 v23 offset:56064\n"
      "v_sub_f32 v53, v65, v53\n"
      "v_mfma_f32_16x16x32_bf16 v[108:111], a[8:11], a[192:195], 0\n"
      "ds_write_addtid_b32 v53 offset:512\n"
      "v_sub_f32 v54, v66, v54\n"
      "v_sub_f32_e64 v55, v67, v55\n"
      "v_mfma_f32_16x16x32_bf16 v[100:103], a[12:15], a[196:199], v[100:103]\n"
      "ds_read_b128 a[212:215], v4 offset:128\n"
      "ds_write_addtid_b32 v54 offset:1024\n"
      "v_mfma_f32_4x4x4_16b_bf16 v[12:15], a[216:217], a[228:229], 0\n"
      "ds_write_addtid_b32 v55 offset:1536\n"
      "v_mfma_f32_4x4x4_16b_bf16 v[16:19], a[216:217], a[230:231], 0\n"
      "v_sub_f32 v56, v68, v56\n"
      "v_sub_f32 v57, v69, v57\n"
      "v_sub_f32_e64 v58, v70, v58\n"
      "v_mfma_f32_16x16x32_bf16 v[104:107], a[16:19], a[196:199], v[104:107]\n"
      "ds_write_addtid_b32 v56 offset:2048\n"
      "ds_write_addtid_b32 v57 offset:2560\n"
      "v_mfma_f32_16x16x32_bf16 v[108:111], a[20:23], a[196:199], v[108:111]\n"
      "ds_write_addtid_b32 v58 offset:3072\n"
      "v_sub_f32 v59, v71, v59\n"
      "v_mfma_f32_16x16x32_bf16 v[100:103], a[24:27], a[200:203], v[100:103]\n"
      "ds_read_b128 a[192:195], v5 offset:0\n"
      "ds_write_addtid_b32 v59 offset:3584\n"
      "v_mfma_f32_16x16x32_bf16 v[104:107], a[28:31], a[200:203], v[104:107]\n"
      "v_cvt_pk_bf16_f32 v12, v12, v16\n"
      "v_sub_f32 v60, v72, v60\n"
      "v_mfma_f32_16x16x32_bf16 v[108:111], a[32:35], a[200:203], v[108:111]\n"
      "ds_write_addtid_b32 v12 offset:44288\n"
      "ds_write_addtid_b32 v60 offset:4096\n"
      "v_cvt_pk_bf16_f32 v13, v13, v17\n"
      "s_waitcnt lgkmcnt(5)\n"
      "v_mfma_f32_16x16x32_bf16 v[100:103], a[36:39], a[204:207], v[100:103]\n"
      "ds_read_b128 a[196:199], v5 offset:64\n"
      "ds_write_addtid_b32 v13 offset:48384\n"
      "v_mfma_f32_16x16x32_bf16 v[104:107], a[40:43], a[204:207], v[104:107]\n"
      "v_sub_f32 v61, v73, v61\n"
      "v_sub_f32 v62, v74, v62\n"
      "v_mfma_f32_16x16x32_bf16 v[108:111], a[44:47], a[204:207], v[108:111]\n"
      "ds_write_addtid_b32 v61 offset:4608\n"
      "ds_write_addtid_b32 v62 offset:5120\n"
      "v_sub_f32 v63, v75, v63\n"
      "v_mfma_f32_16x16x32_bf16 v[100:103], a[48:51], a[208:211], v[100:103]\n"
      "ds_read_b128 a[200:203], v5 offset:128\n"
      "ds_write_addtid_b32 v63 offset:5632\n"
      "v_cvt_pk_bf16_f32 v14, v14, v18\n"
      "v_cvt_pk_bf16_f32 v15, v15, v19\n"
      "v_mfma_f32_16x16x32_bf16 v[104:107], a[52:55], a[208:211], v[104:107]\n"
      "ds_write_addtid_b32 v14 offset:52480\n"
      "ds_write_addtid_b32 v15 offset:56576\n"
      "v_mfma_f32_16x16x32_bf16 v[108:111], a[56:59], a[208:211], v[108:111]\n"
      "v_mfma_f32_16x16x32_bf16 v[100:103], a[60:63], a[212:215], v[100:103]\n"
      "v_fma_f32 v40, 0.5, v52, -v64\n"
      "v_fma_f32 v41, 0.5, v53, -v65\n"
      "v_fma_f32 v42, 0.5, v54, -v66\n"
      "v_fma_f32 v43, 0.5, v55, -v67\n"
      "v_mfma_f32_16x16x32_bf16 v[104:107], a[64:67], a[212:215], v[104:107]\n"
      "v_fma_f32 v44, 0.5, v56, -v68\n"
      "v_fma_f32 v45, 0.5, v57, -v69\n"
      "v_fma_f32 v46, 0.5, v58, -v70\n"
      "v_fma_f32 v47, 0.5, v59, -v71\n"
      "v_mfma_f32_16x16x32_bf16 v[108:111], a[68:71], a[212:215], v[108:111]\n"
      "v_fma_f32 v48, 0.5, v60, -v72\n"
      "v_fma_f32 v49, 0.5, v61, -v73\n"
      "v_fma_f32 v50, 0.5, v62, -v74\n"
      "v_fma_f32 v51, 0.5, v63, -v75\n"
      "s_waitcnt lgkmcnt(0)\n"
      "s_barrier\n"
      "v_mfma_f32_16x16x32_bf16 v[100:103], a[72:75], a[192:195], v[100:103]\n"
      "ds_read_b128 a[204:207], v0 offset:16384\n"
      "ds_read2st64_b32 v[28:29], v6 offset0:0 offset1:2\n"
      "v_mfma_f32_16x16x32_bf16 v[104:107], a[76:79], a[192:195], v[104:107]\n"
      "ds_read_b128 a[208:211], v0 offset:20480\n"
      "ds_read_b64_tr_b16 a[220:221], v7 offset:0\n"
      "v_mfma_f32_16x16x32_bf16 v[108:111], a[80:83], a[192:195], v[108:111]\n"
      "ds_read2st64_b32 v[30:31], v6 offset0:4 offset1:6\n"
      "ds_read2st64_b32 v[32:33], v6 offset0:8 offset1:10\n"
      "v_mfma_f32_16x16x32_bf16 v[100:103], a[84:87], a[196:199], v[100:103]\n"
      "ds_read_b128 a[212:215], v0 offset:16448\n"
      "v_mfma_f32_16x16x32_bf16 v[104:107], a[88:91], a[196:199], v[104:107]\n"
      "v_mfma_f32_16x16x32_bf16 v[108:111], a[92:95], a[196:199], v[108:111]\n"
      "ds_read_b64_tr_b16 a[222:223], v7 offset:8\n"
      "v_mfma_f32_16x16x32_bf16 v[100:103], a[96:99], a[200:203], v[100:103]\n"
      "ds_read_b128 a[192:195], v0 offset:20544\n"
      "ds_read2st64_b32 v[34:35], v6 offset0:12 offset1:14\n"
      "v_mfma_f32_16x16x32_bf16 v[104:107], a[100:103], a[200:203], v[104:107]\n"
      "ds_read_b64_tr_b16 a[224:225], v7 offset:512\n"
      "ds_read2st64_b32 v[36:37], v6 offset0:16 offset1:18\n"
      "v_mfma_f32_16x16x32_bf16 v[108:111], a[104:107], a[200:203], v[108:111]\n"
      "ds_read_b64_tr_b16 a[226:227], v7 offset:520\n"
      "ds_read2st64_b32 v[38:39], v6 offset0:20 offset1:22\n"
      "s_waitcnt lgkmcnt(9)\n"
      "v_sub_f32 v28, v28, v40\n"
      "v_sub_f32 v29, v29, v41\n"
      "v_sub_f32 v30, v30, v42\n"
      "v_mfma_f32_16x16x32_bf16 v[52:55], v[252:255], a[204:207], v[88:91]\n"
      "ds_read_b128 a[196:199], v0 offset:16512\n"
      "v_sub_f32 v31, v31, v43\n"
      "v_fmac_f32 v28, s46, v76\n"
      "v_fmac_f32 v29, s46, v77\n"
      "v_fmac_f32 v30, s46, v78\n"
      "s_waitcnt lgkmcnt(7)\n"
      "v_mfma_f32_4x4x4_16b_bf16 v[12:15], a[216:217], a[220:221], 0\n"
      "v_fmac_f32 v31, s46, v79\n"
      "v_mfma_f32_4x4x4_16b_bf16 v[16:19], a[216:217], a[222:223], 0\n"
      "v_fmac_f32 v40, s45, v28\n"
      "v_fmac_f32 v41, s45, v29\n"
      "v_fmac_f32 v42, s45, v30\n"
      "v_mfma_f32_16x16x32_bf16 v[56:59], v[244:247], a[204:207], v[92:95]\n"
      "v_fmac_f32 v43, s45, v31\n"
      "v_cvt_pk_bf16_f32 v12, v12, v16\n"
      "v_mfma_f32_16x16x32_bf16 v[60:63], v[236:239], a[204:207], v[96:99]\n"
      "ds_write_addtid_b32 v12 offset:24832\n"
      "v_fma_f32 v28, s47, v28, v100\n"
      "v_mfma_f32_16x16x32_bf16 v[64:67], v[248:251], a[208:211], v[88:91]\n"
      "ds_read_b128 a[200:203], v0 offset:20608\n"
      "v_fma_f32 v29, s47, v29, v101\n"
      "v_mfma_f32_16x16x32_bf16 v[68:71], v[240:243], a[208:211], v[92:95]\n"
      "v_cvt_pk_bf16_f32 v13, v13, v17\n"
      "v_fma_f32 v30, s47, v30, v102\n"
      "v_cvt_pk_bf16_f32 v14, v14, v18\n"
      "v_mfma_f32_16x16x32_bf16 v[72:75], v[232:235], a[208:211], v[96:99]\n"
      "ds_write_addtid_b32 v13 offset:28928\n"
      "ds_write_addtid_b32 v14 offset:33024\n"
      "v_mfma_f32_16x16x32_bf16 v[52:55], v[228:231], a[212:215], v[52:55]\n"
      "ds_read_b128 a[204:207], v1 offset:16384\n"
      "v_fma_f32 v31, s47, v31, v103\n"
      "v_mfma_f32_16x16x32_bf16 v[56:59], v[220:223], a[212:215], v[56:59]\n"
      "ds_read_b64_tr_b16 a[228:229], v7 offset:1024\n"
      "ds_read_b64_tr_b16 a[230:231], v7 offset:1032\n"
      "v_mfma_f32_16x16x32_bf16 v[60:63], v[212:215], a[212:215], v[60:63]\n"
      "v_cvt_pk_bf16_f32 v40, v40, v41\n"
      "v_cvt_pk_bf16_f32 v42, v42, v43\n"
      "v_cvt_pk_bf16_f32 v15, v15, v19\n"
      "s_nop 0\n"
      "s_waitcnt lgkmcnt(5)\n"
      "v_mfma_f32_4x4x4_16b_bf16 v[20:23], a[216:217], a[224:225], 0\n"
      "ds_write_addtid_b32 v40 offset:12288\n"
      "v_mfma_f32_4x4x4_16b_bf16 v[24:27], a[216:217], a[226:227], 0\n"
      "ds_write_addtid_b32 v42 offset:12800\n"
      "ds_write_addtid_b32 v15 offset:37120\n"
      "v_cvt_pk_bf16_f32 v28, v28, v29\n"
      "v_mfma_f32_16x16x32_bf16 v[64:67], v[224:227], a[192:195], v[64:67]\n"
      "ds_read_b128 a[208:211], v1 offset:20480\n"
      "ds_write_addtid_b32 v28 offset:14336\n"
      "v_mfma_f32_16x16x32_bf16 v[68:71], v[216:219], a[192:195], v[68:71]\n"
      "v_cvt_pk_bf16_f32 v30, v30, v31\n"
      "v_cvt_pk_bf16_f32 v20, v20, v24\n"
      "v_mfma_f32_16x16x32_bf16 v[72:75], v[208:211], a[192:195], v[72:75]\n"
      "ds_write_addtid_b32 v20 offset:25344\n"
      "ds_write_addtid_b32 v30 offset:14848\n"
      "v_mfma_f32_16x16x32_bf16 v[52:55], v[204:207], a[196:199], v[52:55]\n"
      "ds_read_b128 a[212:215], v1 offset:16448\n"
      "v_sub_f32 v32, v32, v44\n"
      "v_mfma_f32_16x16x32_bf16 v[56:59], v[196:199], a[196:199], v[56:59]\n"
      "v_cvt_pk_bf16_f32 v21, v21, v25\n"
      "v_sub_f32 v33, v33, v45\n"
      "v_mfma_f32_16x16x32_bf16 v[60:63], v[188:191], a[196:199], v[60:63]\n"
      "ds_write_addtid_b32 v21 offset:29440\n"
      "v_sub_f32_e64 v34, v34, v46\n"
      "v_mfma_f32_16x16x32_bf16 v[64:67], v[200:203], a[200:203], v[64:67]\n"
      "ds_read_b128 a[192:195], v1 offset:20544\n"
      "v_cvt_pk_bf16_f32 v22, v22, v26\n"
      "v_mfma_f32_16x16x32_bf16 v[68:71], v[192:195], a[200:203], v[68:71]\n"
      "ds_write_addtid_b32 v22 offset:33536\n"
      "v_sub_f32 v35, v35, v47\n"
      "v_mfma_f32_16x16x32_bf16 v[72:75], v[184:187], a[200:203], v[72:75]\n"
      "v_cvt_pk_bf16_f32 v23, v23, v27\n"
      "v_fmac_f32 v32, s46, v80\n"
      "v_fmac_f32 v33, s46, v81\n"
      "s_waitcnt lgkmcnt(3)\n"
      "v_mfma_f32_16x16x32_bf16 v[52:55], v[180:183], a[204:207], v[52:55]\n"
      "ds_read_b128 a[196:199], v1 offset:16512\n"
      "ds_read_b64_tr_b16 a[232:233], v7 offset:1536\n"
      "v_mfma_f32_4x4x4_16b_bf16 v[12:15], a[216:217], a[228:229], 0\n"
      "ds_write_addtid_b32 v23 offset:37632\n"
      "v_mfma_f32_4x4x4_16b_bf16 v[16:19], a[216:217], a[230:231], 0\n"
      "ds_read_b64_tr_b16 a[234:235], v7 offset:1544\n"
      "v_fmac_f32 v34, s46, v82\n"
      "v_fmac_f32 v35, s46, v83\n"
      "v_mfma_f32_16x16x32_bf16 v[56:59], v[172:175], a[204:207], v[56:59]\n"
      "v_fmac_f32 v44, s45, v32\n"
      "v_cvt_pk_bf16_f32 v12, v12, v16\n"
      "v_mfma_f32_16x16x32_bf16 v[60:63], v[164:167], a[204:207], v[60:63]\n"
      "ds_write_addtid_b32 v12 offset:25856\n"
      "v_fmac_f32 v45, s45, v33\n"
      "v_mfma_f32_16x16x32_bf16 v[64:67], v[176:179], a[208:211], v[64:67]\n"
      "ds_read_b128 a[200:203], v1 offset:20608\n"
      "v_cvt_pk_bf16_f32 v13, v13, v17\n"
      "v_mfma_f32_16x16x32_bf16 v[68:71], v[168:171], a[208:211], v[68:71]\n"
      "ds_write_addtid_b32 v13 offset:29952\n"
      "v_fmac_f32_e64 v46, s45, v34\n"
      "v_fmac_f32_e64 v47, s45, v35\n"
      "v_mfma_f32_16x16x32_bf16 v[72:75], v[160:163], a[208:211], v[72:75]\n"
      "v_cvt_pk_bf16_f32 v14, v14, v18\n"
      "v_fma_f32 v32, s47, v32, v104\n"
      "v_mfma_f32_16x16x32_bf16 v[52:55], v[156:159], a[212:215], v[52:55]\n"
      "ds_read_b128 a[204:207], v2 offset:16384\n"
      "ds_write_addtid_b32 v14 offset:34048\n"
      "v_mfma_f32_16x16x32_bf16 v[56:59], v[148:151], a[212:215], v[56:59]\n"
      "v_fma_f32 v33, s47, v33, v105\n"
      "v_cvt_pk_bf16_f32 v15, v15, v19\n"
      "v_mfma_f32_16x16x32_bf16 v[60:63], v[140:143], a[212:215], v[60:63]\n"
      "ds_write_addtid_b32 v15 offset:38144\n"
      "v_fma_f32 v34, s47, v34, v106\n"
      "v_fma_f32 v35, s47, v35, v107\n"
      "s_nop 0\n"
      "s_waitcnt lgkmcnt(3)\n"
      "v_mfma_f32_16x16x32_bf16 v[64:67], v[152:155], a[192:195], v[64:67]\n"
      "ds_read_b128 a[208:211], v2 offset:20480\n"
      "ds_read_b64_tr_b16 a[220:221], v7 offset:2048\n"
      "v_mfma_f32_16x16x32_bf16 v[68:71], v[144:147], a[192:195], v[68:71]\n"
      "ds_read_b64_tr_b16 a[222:223], v7 offset:2056\n"
      "v_cvt_pk_bf16_f32 v44, v44, v45\n"
      "v_mfma_f32_16x16x32_bf16 v[72:75], v[136:139], a[192:195], v[72:75]\n"
      "ds_write_addtid_b32 v44 offset:20484\n"
      "v_cvt_pk_bf16_f32 v46, v46, v47\n"
      "v_cvt_pk_bf16_f32 v32, v32, v33\n"
      "v_mfma_f32_4x4x4_16b_bf16 v[20:23], a[216:217], a[232:233], 0\n"
      "v_cvt_pk_bf16_f32 v34, v34, v35\n"
      "v_mfma_f32_4x4x4_16b_bf16 v[24:27], a[216:217], a[234:235], 0\n"
      "v_sub_f32_e64 v36, v36, v48\n"
      "v_sub_f32_e64 v37, v37, v49\n"
      "v_sub_f32_e64 v38, v38, v50\n"
      "v_mfma_f32_16x16x32_bf16 v[52:55], v[132:135], a[196:199], v[52:55]\n"
      "ds_read_b128 a[212:215], v2 offset:16448\n"
      "ds_write_addtid_b32 v46 offset:20996\n"
      "v_mfma_f32_16x16x32_bf16 v[56:59], v[124:127], a[196:199], v[56:59]\n"
      "ds_write_addtid_b32 v32 offset:22532\n"
      "ds_write_addtid_b32 v34 offset:23044\n"
      "v_mfma_f32_16x16x32_bf16 v[60:63], a[184:187], a[196:199], v[60:63]\n"
      "v_cvt_pk_bf16_f32 v20, v20, v24\n"
      "v_cvt_pk_bf16_f32 v21, v21, v25\n"
      "v_mfma_f32_16x16x32_bf16 v[64:67], v[128:131], a[200:203], v[64:67]\n"
      "ds_read_b128 a[192:195], v2 offset:20544\n"
      "ds_write_addtid_b32 v20 offset:26368\n"
      "v_mfma_f32_16x16x32_bf16 v[68:71], a[188:191], a[200:203], v[68:71]\n"
      "ds_write_addtid_b32 v21 offset:30464\n"
      "v_sub_f32 v39, v39, v51\n"
      "v_mfma_f32_16x16x32_bf16 v[72:75], a[180:183], a[200:203], v[72:75]\n"
      "v_cvt_pk_bf16_f32 v22, v22, v26\n"
      "v_fmac_f32_e64 v36, s46, v84\n"
      "s_waitcnt lgkmcnt(4)\n"
      "v_mfma_f32_16x16x32_bf16 v[52:55], a[176:179], a[204:207], v[52:55]\n"
      "ds_read_b128 a[196:199], v2 offset:16512\n"
      "ds_write_addtid_b32 v22 offset:34560\n"
      "v_mfma_f32_16x16x32_bf16 v[56:59], a[168:171], a[204:207], v[56:59]\n"
      "ds_read_b64_tr_b16 a[224:225], v7 offset:2560\n"
      "ds_read_b64_tr_b16 a[226:227], v7 offset:2568\n"
      "v_mfma_f32_16x16x32_bf16 v[60:63], a[160:163], a[204:207], v[60:63]\n"
      "v_cvt_pk_bf16_f32 v23, v23, v27\n"
      "v_fmac_f32_e64 v37, s46, v85\n"
      "v_fmac_f32_e64 v38, s46, v86\n"
      "v_mfma_f32_4x4x4_16b_bf16 v[12:15], a[216:217], a[220:221], 0\n"
      "v_fmac_f32_e64 v39, s46, v87\n"
      "v_mfma_f32_4x4x4_16b_bf16 v[16:19], a[216:217], a[222:223], 0\n"
      "v_fmac_f32_e64 v48, s45, v36\n"
      "v_fmac_f32_e64 v49, s45, v37\n"
      "v_fmac_f32_e64 v50, s45, v38\n"
      "v_mfma_f32_16x16x32_bf16 v[64:67], a[172:175], a[208:211], v[64:67]\n"
      "ds_read_b128 a[200:203], v2 offset:20608\n"
      "ds_write_addtid_b32 v23 offset:38656\n"
      "v_mfma_f32_16x16x32_bf16 v[68:71], a[164:167], a[208:211], v[68:71]\n"
      "v_cvt_pk_bf16_f32 v12, v12, v16\n"
      "v_cvt_pk_bf16_f32 v13, v13, v17\n"
      "v_mfma_f32_16x16x32_bf16 v[72:75], a[156:159], a[208:211], v[72:75]\n"
      "ds_write_addtid_b32 v12 offset:26880\n"
      "ds_write_addtid_b32 v13 offset:30976\n"
      "v_mfma_f32_16x16x32_bf16 v[52:55], a[152:155], a[212:215], v[52:55]\n"
      "ds_read_b128 a[204:207], v3 offset:16896\n"
      "v_fmac_f32 v51, s45, v39\n"
      "v_mfma_f32_16x16x32_bf16 v[56:59], a[144:147], a[212:215], v[56:59]\n"
      "v_fma_f32 v36, s47, v36, v108\n"
      "v_cvt_pk_bf16_f32 v14, v14, v18\n"
      "v_mfma_f32_16x16x32_bf16 v[60:63], a[136:139], a[212:215], v[60:63]\n"
      "ds_write_addtid_b32 v14 offset:35072\n"
      "v_fma_f32 v37, s47, v37, v109\n"
      "v_cvt_pk_bf16_f32 v15, v15, v19\n"
      "s_waitcnt lgkmcnt(4)\n"
      "v_mfma_f32_16x16x32_bf16 v[64:67], a[148:151], a[192:195], v[64:67]\n"
      "ds_read_b128 a[208:211], v3 offset:16960\n"
      "ds_write_addtid_b32 v15 offset:39168\n"
      "v_mfma_f32_16x16x32_bf16 v[68:71], a[140:143], a[192:195], v[68:71]\n"
      "ds_read_b64_tr_b16 a[228:229], v7 offset:3072\n"
      "ds_read_b64_tr_b16 a[230:231], v7 offset:3080\n"
      "v_mfma_f32_16x16x32_bf16 v[72:75], a[132:135], a[192:195], v[72:75]\n"
      "v_fma_f32 v38, s47, v38, v110\n"
      "v_fma_f32 v39, s47, v39, v111\n"
      "v_cvt_pk_bf16_f32 v48, v48, v49\n"
      "v_mfma_f32_16x16x32_bf16 v[52:55], a[128:131], a[196:199], v[52:55]\n"
      "ds_read_b128 a[212:215], v3 offset:17024\n"
      "ds_write_addtid_b32 v48 offset:16384\n"
      "v_mfma_f32_4x4x4_16b_bf16 v[20:23], a[216:217], a[224:225], 0\n"
      "v_cvt_pk_bf16_f32 v50, v50, v51\n"
      "v_mfma_f32_4x4x4_16b_bf16 v[24:27], a[216:217], a[226:227], 0\n"
      "v_cvt_pk_bf16_f32 v36, v36, v37\n"
      "v_cvt_pk_bf16_f32 v38, v38, v39\n"
      "v_mfma_f32_16x16x32_bf16 v[56:59], a[120:123], a[196:199], v[56:59]\n"
      "ds_write_addtid_b32 v50 offset:16896\n"
      "ds_write_addtid_b32 v36 offset:18432\n"
      "v_cvt_pk_bf16_f32 v20, v20, v24\n"
      "v_mfma_f32_16x16x32_bf16 v[60:63], a[112:115], a[196:199], v[60:63]\n"
      "ds_write_addtid_b32 v20 offset:27392\n"
      "ds_write_addtid_b32 v38 offset:18944\n"
      "v_mfma_f32_16x16x32_bf16 v[64:67], a[124:127], a[200:203], v[64:67]\n"
      "ds_read_b128 a[192:195], v4 offset:16896\n"
      "v_cvt_pk_bf16_f32 v21, v21, v25\n"
      "v_mfma_f32_16x16x32_bf16 v[68:71], a[116:119], a[200:203], v[68:71]\n"
      "ds_write_addtid_b32 v21 offset:31488\n"
      "v_cvt_pk_bf16_f32 v22, v22, v26\n"
      "v_mfma_f32_16x16x32_bf16 v[72:75], a[108:111], a[200:203], v[72:75]\n"
      "ds_write_addtid_b32 v22 offset:35584\n"
      "v_cvt_pk_bf16_f32 v23, v23, v27\n"
      "v_sub_f32 v52, v64, v52\n"
      "s_waitcnt lgkmcnt(5)\n"
      "v_mfma_f32_16x16x32_bf16 v[112:115], a[0:3], a[204:207], 0\n"
      "ds_read_b128 a[196:199], v4 offset:16960\n"
      "ds_write_addtid_b32 v52 offset:6144\n"
      "v_mfma_f32_16x16x32_bf16 v[116:119], a[4:7], a[204:207], 0\n"
      "ds_write_addtid_b32 v23 offset:39680\n"
      "v_sub_f32 v53, v65, v53\n"
      "v_mfma_f32_16x16x32_bf16 v[120:123], a[8:11], a[204:207], 0\n"
      "ds_write_addtid_b32 v53 offset:6656\n"
      "v_sub_f32 v54, v66, v54\n"
      "v_sub_f32_e64 v55, v67, v55\n"
      "v_mfma_f32_16x16x32_bf16 v[112:115], a[12:15], a[208:211], v[112:115]\n"
      "ds_read_b128 a[200:203], v4 offset:17024\n"
      "ds_write_addtid_b32 v54 offset:7168\n"
      "v_mfma_f32_4x4x4_16b_bf16 v[12:15], a[216:217], a[228:229], 0\n"
      "ds_write_addtid_b32 v55 offset:7680\n"
      "v_mfma_f32_4x4x4_16b_bf16 v[16:19], a[216:217], a[230:231], 0\n"
      "v_sub_f32 v56, v68, v56\n"
      "v_sub_f32 v57, v69, v57\n"
      "v_sub_f32_e64 v58, v70, v58\n"
      "v_mfma_f32_16x16x32_bf16 v[116:119], a[16:19], a[208:211], v[116:119]\n"
      "ds_write_addtid_b32 v56 offset:8192\n"
      "ds_write_addtid_b32 v57 offset:8704\n"
      "v_mfma_f32_16x16x32_bf16 v[120:123], a[20:23], a[208:211], v[120:123]\n"
      "ds_write_addtid_b32 v58 offset:9216\n"
      "v_sub_f32 v59, v71, v59\n"
      "v_mfma_f32_16x16x32_bf16 v[112:115], a[24:27], a[212:215], v[112:115]\n"
      "ds_read_b128 a[204:207], v5 offset:16896\n"
      "ds_write_addtid_b32 v59 offset:9728\n"
      "v_mfma_f32_16x16x32_bf16 v[116:119], a[28:31], a[212:215], v[116:119]\n"
      "v_cvt_pk_bf16_f32 v12, v12, v16\n"
      "v_sub_f32 v60, v72, v60\n"
      "v_mfma_f32_16x16x32_bf16 v[120:123], a[32:35], a[212:215], v[120:123]\n"
      "ds_write_addtid_b32 v12 offset:27904\n"
      "ds_write_addtid_b32 v60 offset:10240\n"
      "v_cvt_pk_bf16_f32 v13, v13, v17\n"
      "s_waitcnt lgkmcnt(5)\n"
      "v_mfma_f32_16x16x32_bf16 v[112:115], a[36:39], a[192:195], v[112:115]\n"
      "ds_read_b128 a[208:211], v5 offset:16960\n"
      "ds_write_addtid_b32 v13 offset:32000\n"
      "v_mfma_f32_16x16x32_bf16 v[116:119], a[40:43], a[192:195], v[116:119]\n"
      "v_sub_f32 v61, v73, v61\n"
      "v_sub_f32 v62, v74, v62\n"
      "v_mfma_f32_16x16x32_bf16 v[120:123], a[44:47], a[192:195], v[120:123]\n"
      "ds_write_addtid_b32 v61 offset:10752\n"
      "ds_write_addtid_b32 v62 offset:11264\n"
      "v_sub_f32 v63, v75, v63\n"
      "v_mfma_f32_16x16x32_bf16 v[112:115], a[48:51], a[196:199], v[112:115]\n"
      "ds_read_b128 a[212:215], v5 offset:17024\n"
      "ds_write_addtid_b32 v63 offset:11776\n"
      "v_cvt_pk_bf16_f32 v14, v14, v18\n"
      "v_cvt_pk_bf16_f32 v15, v15, v19\n"
      "v_mfma_f32_16x16x32_bf16 v[116:119], a[52:55], a[196:199], v[116:119]\n"
      "ds_write_addtid_b32 v14 offset:36096\n"
      "ds_write_addtid_b32 v15 offset:40192\n"
      "v_mfma_f32_16x16x32_bf16 v[120:123], a[56:59], a[196:199], v[120:123]\n"
      "v_mfma_f32_16x16x32_bf16 v[112:115], a[60:63], a[200:203], v[112:115]\n"
      "v_fma_f32 v40, 0.5, v52, -v64\n"
      "v_fma_f32 v41, 0.5, v53, -v65\n"
      "v_fma_f32 v42, 0.5, v54, -v66\n"
      "v_fma_f32 v43, 0.5, v55, -v67\n"
      "v_mfma_f32_16x16x32_bf16 v[116:119], a[64:67], a[200:203], v[116:119]\n"
      "v_fma_f32 v44, 0.5, v56, -v68\n"
      "v_fma_f32 v45, 0.5, v57, -v69\n"
      "v_fma_f32 v46, 0.5, v58, -v70\n"
      "v_fma_f32 v47, 0.5, v59, -v71\n"
      "v_mfma_f32_16x16x32_bf16 v[120:123], a[68:71], a[200:203], v[120:123]\n"
      "v_fma_f32 v48, 0.5, v60, -v72\n"
      "v_fma_f32 v49, 0.5, v61, -v73\n"
      "v_fma_f32 v50, 0.5, v62, -v74\n"
      "v_fma_f32 v51, 0.5, v63, -v75\n"
      "s_waitcnt lgkmcnt(0)\n"
      "s_barrier\n"
      "v_mfma_f32_16x16x32_bf16 v[112:115], a[72:75], a[204:207], v[112:115]\n"
      "ds_read_b128 a[192:195], v0 offset:0\n"
      "ds_read2st64_b32 v[28:29], v6 offset0:24 offset1:26\n"
      "v_mfma_f32_16x16x32_bf16 v[116:119], a[76:79], a[204:207], v[116:119]\n"
      "ds_read_b128 a[196:199], v0 offset:4096\n"
      "ds_read_b64_tr_b16 a[220:221], v7 offset:16896\n"
      "v_mfma_f32_16x16x32_bf16 v[120:123], a[80:83], a[204:207], v[120:123]\n"
      "ds_read2st64_b32 v[30:31], v6 offset0:28 offset1:30\n"
      "ds_read2st64_b32 v[32:33], v6 offset0:32 offset1:34\n"
      "v_mfma_f32_16x16x32_bf16 v[112:115], a[84:87], a[208:211], v[112:115]\n"
      "ds_read_b128 a[200:203], v0 offset:64\n"
      "v_mfma_f32_16x16x32_bf16 v[116:119], a[88:91], a[208:211], v[116:119]\n"
      "v_mfma_f32_16x16x32_bf16 v[120:123], a[92:95], a[208:211], v[120:123]\n"
      "ds_read_b64_tr_b16 a[222:223], v7 offset:16904\n"
      "v_mfma_f32_16x16x32_bf16 v[112:115], a[96:99], a[212:215], v[112:115]\n"
      "ds_read_b128 a[204:207], v0 offset:4160\n"
      "ds_read2st64_b32 v[34:35], v6 offset0:36 offset1:38\n"
      "v_mfma_f32_16x16x32_bf16 v[116:119], a[100:103], a[212:215], v[116:119]\n"
      "ds_read_b64_tr_b16 a[224:225], v7 offset:17408\n"
      "ds_read2st64_b32 v[36:37], v6 offset0:40 offset1:42\n"
      "v_mfma_f32_16x16x32_bf16 v[120:123], a[104:107], a[212:215], v[120:123]\n"
      "ds_read_b64_tr_b16 a[226:227], v7 offset:17416\n"
      "s_waitcnt lgkmcnt(9)\n"
      "v_mfma_f32_16x16x32_bf16 v[52:55], v[252:255], a[192:195], v[100:103]\n"
      "ds_read_b128 a[208:211], v0 offset:128\n"
      "s_waitcnt lgkmcnt(8)\n"
      "v_sub_f32 v28, v28, v40\n"
      "v_sub_f32 v29, v29, v41\n"
      "v_sub_f32 v30, v30, v42\n"
      "v_sub_f32 v31, v31, v43\n"
      "s_waitcnt lgkmcnt(6)\n"
      "v_mfma_f32_4x4x4_16b_bf16 v[12:15], a[216:217], a[220:221], 0\n"
      "ds_read2st64_b32 v[38:39], v6 offset0:44 offset1:46\n"
      "v_fmac_f32 v28, s46, v88\n"
      "v_fmac_f32 v29, s46, v89\n"
      "v_fmac_f32 v30, s46, v90\n"
      "v_fmac_f32 v31, s46, v91\n"
      "v_mfma_f32_4x4x4_16b_bf16 v[16:19], a[216:217], a[222:223], 0\n"
      "v_fmac_f32 v40, s45, v28\n"
      "v_fmac_f32 v41, s45, v29\n"
      "v_fmac_f32 v42, s45, v30\n"
      "v_mfma_f32_16x16x32_bf16 v[56:59], v[244:247], a[192:195], v[104:107]\n"
      "v_fmac_f32_e64 v43, s45, v31\n"
      "v_cvt_pk_bf16_f32 v12, v12, v16\n"
      "v_mfma_f32_16x16x32_bf16 v[60:63], v[236:239], a[192:195], v[108:111]\n"
      "ds_write_addtid_b32 v12 offset:41216\n"
      "v_fma_f32 v28, s47, v28, v112\n"
      "v_mfma_f32_16x16x32_bf16 v[64:67], v[248:251], a[196:199], v[100:103]\n"
      "ds_read_b128 a[212:215], v0 offset:4224\n"
      "v_fma_f32 v29, s47, v29, v113\n"
      "v_mfma_f32_16x16x32_bf16 v[68:71], v[240:243], a[196:199], v[104:107]\n"
      "v_cvt_pk_bf16_f32 v13, v13, v17\n"
      "v_fma_f32 v30, s47, v30, v114\n"
      "v_cvt_pk_bf16_f32 v14, v14, v18\n"
      "v_mfma_f32_16x16x32_bf16 v[72:75], v[232:235], a[196:199], v[108:111]\n"
      "ds_write_addtid_b32 v13 offset:45312\n"
      "ds_write_addtid_b32 v14 offset:49408\n"
      "v_mfma_f32_16x16x32_bf16 v[52:55], v[228:231], a[200:203], v[52:55]\n"
      "ds_read_b128 a[192:195], v1 offset:0\n"
      "v_fma_f32 v31, s47, v31, v115\n"
      "v_mfma_f32_16x16x32_bf16 v[56:59], v[220:223], a[200:203], v[56:59]\n"
      "ds_read_b64_tr_b16 a[228:229], v7 offset:17920\n"
      "ds_read_b64_tr_b16 a[230:231], v7 offset:17928\n"
      "v_mfma_f32_16x16x32_bf16 v[60:63], v[212:215], a[200:203], v[60:63]\n"
      "v_cvt_pk_bf16_f32 v40, v40, v41\n"
      "v_cvt_pk_bf16_f32 v42, v42, v43\n"
      "v_cvt_pk_bf16_f32 v15, v15, v19\n"
      "s_nop 0\n"
      "s_waitcnt lgkmcnt(5)\n"
      "v_mfma_f32_4x4x4_16b_bf16 v[20:23], a[216:217], a[224:225], 0\n"
      "ds_write_addtid_b32 v40 offset:13312\n"
      "v_mfma_f32_4x4x4_16b_bf16 v[24:27], a[216:217], a[226:227], 0\n"
      "ds_write_addtid_b32 v42 offset:13824\n"
      "ds_write_addtid_b32 v15 offset:53504\n"
      "v_cvt_pk_bf16_f32 v28, v28, v29\n"
      "v_mfma_f32_16x16x32_bf16 v[64:67], v[224:227], a[204:207], v[64:67]\n"
      "ds_read_b128 a[196:199], v1 offset:4096\n"
      "ds_write_addtid_b32 v28 offset:15360\n"
      "v_mfma_f32_16x16x32_bf16 v[68:71], v[216:219], a[204:207], v[68:71]\n"
      "v_cvt_pk_bf16_f32 v30, v30, v31\n"
      "v_cvt_pk_bf16_f32 v20, v20, v24\n"
      "v_mfma_f32_16x16x32_bf16 v[72:75], v[208:211], a[204:207], v[72:75]\n"
      "ds_write_addtid_b32 v20 offset:41728\n"
      "ds_write_addtid_b32 v30 offset:15872\n"
      "v_mfma_f32_16x16x32_bf16 v[52:55], v[204:207], a[208:211], v[52:55]\n"
      "ds_read_b128 a[200:203], v1 offset:64\n"
      "v_sub_f32 v32, v32, v44\n"
      "v_mfma_f32_16x16x32_bf16 v[56:59], v[196:199], a[208:211], v[56:59]\n"
      "v_cvt_pk_bf16_f32 v21, v21, v25\n"
      "v_sub_f32 v33, v33, v45\n"
      "v_mfma_f32_16x16x32_bf16 v[60:63], v[188:191], a[208:211], v[60:63]\n"
      "ds_write_addtid_b32 v21 offset:45824\n"
      "v_sub_f32 v34, v34, v46\n"
      "v_mfma_f32_16x16x32_bf16 v[64:67], v[200:203], a[212:215], v[64:67]\n"
      "ds_read_b128 a[204:207], v1 offset:4160\n"
      "v_cvt_pk_bf16_f32 v22, v22, v26\n"
      "v_mfma_f32_16x16x32_bf16 v[68:71], v[192:195], a[212:215], v[68:71]\n"
      "ds_write_addtid_b32 v22 offset:49920\n"
      "v_sub_f32 v35, v35, v47\n"
      "v_mfma_f32_16x16x32_bf16 v[72:75], v[184:187], a[212:215], v[72:75]\n"
      "v_cvt_pk_bf16_f32 v23, v23, v27\n"
      "v_fmac_f32 v32, s46, v92\n"
      "v_fmac_f32_e64 v33, s46, v93\n"
      "s_waitcnt lgkmcnt(3)\n"
      "v_mfma_f32_16x16x32_bf16 v[52:55], v[180:183], a[192:195], v[52:55]\n"
      "ds_read_b128 a[208:211], v1 offset:128\n"
      "ds_read_b64_tr_b16 a[232:233], v7 offset:18432\n"
      "v_mfma_f32_4x4x4_16b_bf16 v[12:15], a[216:217], a[228:229], 0\n"
      "ds_write_addtid_b32 v23 offset:54016\n"
      "v_mfma_f32_4x4x4_16b_bf16 v[16:19], a[216:217], a[230:231], 0\n"
      "ds_read_b64_tr_b16 a[234:235], v7 offset:18440\n"
      "v_fmac_f32 v34, s46, v94\n"
      "v_fmac_f32 v35, s46, v95\n"
      "v_mfma_f32_16x16x32_bf16 v[56:59], v[172:175], a[192:195], v[56:59]\n"
      "v_fmac_f32 v44, s45, v32\n"
      "v_cvt_pk_bf16_f32 v12, v12, v16\n"
      "v_mfma_f32_16x16x32_bf16 v[60:63], v[164:167], a[192:195], v[60:63]\n"
      "ds_write_addtid_b32 v12 offset:42240\n"
      "v_fmac_f32 v45, s45, v33\n"
      "v_mfma_f32_16x16x32_bf16 v[64:67], v[176:179], a[196:199], v[64:67]\n"
      "ds_read_b128 a[212:215], v1 offset:4224\n"
      "v_cvt_pk_bf16_f32 v13, v13, v17\n"
      "v_mfma_f32_16x16x32_bf16 v[68:71], v[168:171], a[196:199], v[68:71]\n"
      "ds_write_addtid_b32 v13 offset:46336\n"
      "v_fmac_f32 v46, s45, v34\n"
      "v_fmac_f32 v47, s45, v35\n"
      "v_mfma_f32_16x16x32_bf16 v[72:75], v[160:163], a[196:199], v[72:75]\n"
      "v_cvt_pk_bf16_f32 v14, v14, v18\n"
      "v_fma_f32 v32, s47, v32, v116\n"
      "v_mfma_f32_16x16x32_bf16 v[52:55], v[156:159], a[200:203], v[52:55]\n"
      "ds_read_b128 a[192:195], v2 offset:0\n"
      "ds_write_addtid_b32 v14 offset:50432\n"
      "v_mfma_f32_16x16x32_bf16 v[56:59], v[148:151], a[200:203], v[56:59]\n"
      "v_fma_f32 v33, s47, v33, v117\n"
      "v_cvt_pk_bf16_f32 v15, v15, v19\n"
      "v_mfma_f32_16x16x32_bf16 v[60:63], v[140:143], a[200:203], v[60:63]\n"
      "ds_write_addtid_b32 v15 offset:54528\n"
      "v_fma_f32 v34, s47, v34, v118\n"
      "v_fma_f32 v35, s47, v35, v119\n"
      "s_nop 0\n"
      "s_waitcnt lgkmcnt(3)\n"
      "v_mfma_f32_16x16x32_bf16 v[64:67], v[152:155], a[204:207], v[64:67]\n"
      "ds_read_b128 a[196:199], v2 offset:4096\n"
      "ds_read_b64_tr_b16 a[220:221], v7 offset:18944\n"
      "v_mfma_f32_16x16x32_bf16 v[68:71], v[144:147], a[204:207], v[68:71]\n"
      "ds_read_b64_tr_b16 a[222:223], v7 offset:18952\n"
      "v_cvt_pk_bf16_f32 v44, v44, v45\n"
      "v_mfma_f32_16x16x32_bf16 v[72:75], v[136:139], a[204:207], v[72:75]\n"
      "ds_write_addtid_b32 v44 offset:21508\n"
      "v_cvt_pk_bf16_f32 v46, v46, v47\n"
      "v_cvt_pk_bf16_f32 v32, v32, v33\n"
      "v_mfma_f32_4x4x4_16b_bf16 v[20:23], a[216:217], a[232:233], 0\n"
      "v_cvt_pk_bf16_f32 v34, v34, v35\n"
      "v_mfma_f32_4x4x4_16b_bf16 v[24:27], a[216:217], a[234:235], 0\n"
      "v_sub_f32_e64 v36, v36, v48\n"
      "v_sub_f32_e64 v37, v37, v49\n"
      "v_sub_f32_e64 v38, v38, v50\n"
      "v_mfma_f32_16x16x32_bf16 v[52:55], v[132:135], a[208:211], v[52:55]\n"
      "ds_read_b128 a[200:203], v2 offset:64\n"
      "ds_write_addtid_b32 v46 offset:22020\n"
      "v_mfma_f32_16x16x32_bf16 v[56:59], v[124:127], a[208:211], v[56:59]\n"
      "ds_write_addtid_b32 v32 offset:23556\n"
      "ds_write_addtid_b32 v34 offset:24068\n"
      "v_mfma_f32_16x16x32_bf16 v[60:63], a[184:187], a[208:211], v[60:63]\n"
      "v_cvt_pk_bf16_f32 v20, v20, v24\n"
      "v_cvt_pk_bf16_f32 v21, v21, v25\n"
      "v_mfma_f32_16x16x32_bf16 v[64:67], v[128:131], a[212:215], v[64:67]\n"
      "ds_read_b128 a[204:207], v2 offset:4160\n"
      "ds_write_addtid_b32 v20 offset:42752\n"
      "v_mfma_f32_16x16x32_bf16 v[68:71], a[188:191], a[212:215], v[68:71]\n"
      "ds_write_addtid_b32 v21 offset:46848\n"
      "v_sub_f32 v39, v39, v51\n"
      "v_mfma_f32_16x16x32_bf16 v[72:75], a[180:183], a[212:215], v[72:75]\n"
      "v_cvt_pk_bf16_f32 v22, v22, v26\n"
      "v_fmac_f32_e64 v36, s46, v96\n"
      "s_waitcnt lgkmcnt(4)\n"
      "v_mfma_f32_16x16x32_bf16 v[52:55], a[176:179], a[192:195], v[52:55]\n"
      "ds_read_b128 a[208:211], v2 offset:128\n"
      "ds_write_addtid_b32 v22 offset:50944\n"
      "v_mfma_f32_16x16x32_bf16 v[56:59], a[168:171], a[192:195], v[56:59]\n"
      "ds_read_b64_tr_b16 a[224:225], v7 offset:19456\n"
      "ds_read_b64_tr_b16 a[226:227], v7 offset:19464\n"
      "v_mfma_f32_16x16x32_bf16 v[60:63], a[160:163], a[192:195], v[60:63]\n"
      "v_cvt_pk_bf16_f32 v23, v23, v27\n"
      "v_fmac_f32_e64 v37, s46, v97\n"
      "v_fmac_f32_e64 v38, s46, v98\n"
      "v_mfma_f32_4x4x4_16b_bf16 v[12:15], a[216:217], a[220:221], 0\n"
      "v_fmac_f32_e64 v39, s46, v99\n"
      "v_mfma_f32_4x4x4_16b_bf16 v[16:19], a[216:217], a[222:223], 0\n"
      "v_fmac_f32_e64 v48, s45, v36\n"
      "v_fmac_f32_e64 v49, s45, v37\n"
      "v_fmac_f32_e64 v50, s45, v38\n"
      "v_mfma_f32_16x16x32_bf16 v[64:67], a[172:175], a[196:199], v[64:67]\n"
      "ds_read_b128 a[212:215], v2 offset:4224\n"
      "ds_write_addtid_b32 v23 offset:55040\n"
      "v_mfma_f32_16x16x32_bf16 v[68:71], a[164:167], a[196:199], v[68:71]\n"
      "v_cvt_pk_bf16_f32 v12, v12, v16\n"
      "v_cvt_pk_bf16_f32 v13, v13, v17\n"
      "v_mfma_f32_16x16x32_bf16 v[72:75], a[156:159], a[196:199], v[72:75]\n"
      "ds_write_addtid_b32 v12 offset:43264\n"
      "ds_write_addtid_b32 v13 offset:47360\n"
      "v_mfma_f32_16x16x32_bf16 v[52:55], a[152:155], a[200:203], v[52:55]\n"
      "ds_read_b128 a[192:195], v3 offset:33792\n"
      "v_fmac_f32 v51, s45, v39\n"
      "v_mfma_f32_16x16x32_bf16 v[56:59], a[144:147], a[200:203], v[56:59]\n"
      "v_fma_f32 v36, s47, v36, v120\n"
      "v_cvt_pk_bf16_f32 v14, v14, v18\n"
      "v_mfma_f32_16x16x32_bf16 v[60:63], a[136:139], a[200:203], v[60:63]\n"
      "ds_write_addtid_b32 v14 offset:51456\n"
      "v_fma_f32 v37, s47, v37, v121\n"
      "v_cvt_pk_bf16_f32 v15, v15, v19\n"
      "s_waitcnt lgkmcnt(5)\n"
      "v_mfma_f32_16x16x32_bf16 v[64:67], a[148:151], a[204:207], v[64:67]\n"
      "ds_read_b128 a[196:199], v3 offset:33856\n"
      "ds_write_addtid_b32 v15 offset:55552\n"
      "v_mfma_f32_16x16x32_bf16 v[68:71], a[140:143], a[204:207], v[68:71]\n"
      "ds_read_b64_tr_b16 a[228:229], v7 offset:19968\n"
      "ds_read_b64_tr_b16 a[230:231], v7 offset:19976\n"
      "v_mfma_f32_16x16x32_bf16 v[72:75], a[132:135], a[204:207], v[72:75]\n"
      "v_fma_f32 v38, s47, v38, v122\n"
      "v_fma_f32 v39, s47, v39, v123\n"
      "v_cvt_pk_bf16_f32 v48, v48, v49\n"
      "v_mfma_f32_16x16x32_bf16 v[52:55], a[128:131], a[208:211], v[52:55]\n"
      "ds_read_b128 a[200:203], v3 offset:33920\n"
      "ds_write_addtid_b32 v48 offset:17408\n"
      "v_mfma_f32_4x4x4_16b_bf16 v[20:23], a[216:217], a[224:225], 0\n"
      "v_cvt_pk_bf16_f32 v50, v50, v51\n"
      "v_mfma_f32_4x4x4_16b_bf16 v[24:27], a[216:217], a[226:227], 0\n"
      "v_cvt_pk_bf16_f32 v36, v36, v37\n"
      "v_cvt_pk_bf16_f32 v38, v38, v39\n"
      "v_mfma_f32_16x16x32_bf16 v[56:59], a[120:123], a[208:211], v[56:59]\n"
      "ds_write_addtid_b32 v50 offset:17920\n"
      "ds_write_addtid_b32 v36 offset:19456\n"
      "v_cvt_pk_bf16_f32 v20, v20, v24\n"
      "v_mfma_f32_16x16x32_bf16 v[60:63], a[112:115], a[208:211], v[60:63]\n"
      "ds_write_addtid_b32 v20 offset:43776\n"
      "ds_write_addtid_b32 v38 offset:19968\n"
      "v_mfma_f32_16x16x32_bf16 v[64:67], a[124:127], a[212:215], v[64:67]\n"
      "ds_read_b128 a[204:207], v4 offset:33792\n"
      "v_cvt_pk_bf16_f32 v21, v21, v25\n"
      "v_mfma_f32_16x16x32_bf16 v[68:71], a[116:119], a[212:215], v[68:71]\n"
      "ds_write_addtid_b32 v21 offset:47872\n"
      "v_cvt_pk_bf16_f32 v22, v22, v26\n"
      "v_mfma_f32_16x16x32_bf16 v[72:75], a[108:111], a[212:215], v[72:75]\n"
      "ds_write_addtid_b32 v22 offset:51968\n"
      "v_cvt_pk_bf16_f32 v23, v23, v27\n"
      "v_sub_f32 v52, v64, v52\n"
      "s_waitcnt lgkmcnt(5)\n"
      "v_mfma_f32_16x16x32_bf16 v[76:79], a[0:3], a[192:195], 0\n"
      "ds_read_b128 a[208:211], v4 offset:33856\n"
      "ds_write_addtid_b32 v52 offset:0\n"
      "v_mfma_f32_16x16x32_bf16 v[80:83], a[4:7], a[192:195], 0\n"
      "ds_write_addtid_b32 v23 offset:56064\n"
      "v_sub_f32 v53, v65, v53\n"
      "v_mfma_f32_16x16x32_bf16 v[84:87], a[8:11], a[192:195], 0\n"
      "ds_write_addtid_b32 v53 offset:512\n"
      "v_sub_f32 v54, v66, v54\n"
      "v_sub_f32_e64 v55, v67, v55\n"
      "v_mfma_f32_16x16x32_bf16 v[76:79], a[12:15], a[196:199], v[76:79]\n"
      "ds_read_b128 a[212:215], v4 offset:33920\n"
      "ds_write_addtid_b32 v54 offset:1024\n"
      "v_mfma_f32_4x4x4_16b_bf16 v[12:15], a[216:217], a[228:229], 0\n"
      "ds_write_addtid_b32 v55 offset:1536\n"
      "v_mfma_f32_4x4x4_16b_bf16 v[16:19], a[216:217], a[230:231], 0\n"
      "v_sub_f32 v56, v68, v56\n"
      "v_sub_f32 v57, v69, v57\n"
      "v_sub_f32_e64 v58, v70, v58\n"
      "v_mfma_f32_16x16x32_bf16 v[80:83], a[16:19], a[196:199], v[80:83]\n"
      "ds_write_addtid_b32 v56 offset:2048\n"
      "ds_write_addtid_b32 v57 offset:2560\n"
      "v_mfma_f32_16x16x32_bf16 v[84:87], a[20:23], a[196:199], v[84:87]\n"
      "ds_write_addtid_b32 v58 offset:3072\n"
      "v_sub_f32 v59, v71, v59\n"
      "v_mfma_f32_16x16x32_bf16 v[76:79], a[24:27], a[200:203], v[76:79]\n"
      "ds_read_b128 a[192:195], v5 offset:33792\n"
      "ds_write_addtid_b32 v59 offset:3584\n"
      "v_mfma_f32_16x16x32_bf16 v[80:83], a[28:31], a[200:203], v[80:83]\n"
      "v_cvt_pk_bf16_f32 v12, v12, v16\n"
      "v_sub_f32 v60, v72, v60\n"
      "v_mfma_f32_16x16x32_bf16 v[84:87], a[32:35], a[200:203], v[84:87]\n"
      "ds_write_addtid_b32 v12 offset:44288\n"
      "ds_write_addtid_b32 v60 offset:4096\n"
      "v_cvt_pk_bf16_f32 v13, v13, v17\n"
      "s_waitcnt lgkmcnt(5)\n"
      "v_mfma_f32_16x16x32_bf16 v[76:79], a[36:39], a[204:207], v[76:79]\n"
      "ds_read_b128 a[196:199], v5 offset:33856\n"
      "ds_write_addtid_b32 v13 offset:48384\n"
      "v_mfma_f32_16x16x32_bf16 v[80:83], a[40:43], a[204:207], v[80:83]\n"
      "v_sub_f32 v61, v73, v61\n"
      "v_sub_f32 v62, v74, v62\n"
      "v_mfma_f32_16x16x32_bf16 v[84:87], a[44:47], a[204:207], v[84:87]\n"
      "ds_write_addtid_b32 v61 offset:4608\n"
      "ds_write_addtid_b32 v62 offset:5120\n"
      "v_sub_f32 v63, v75, v63\n"
      "v_mfma_f32_16x16x32_bf16 v[76:79], a[48:51], a[208:211], v[76:79]\n"
      "ds_read_b128 a[200:203], v5 offset:33920\n"
      "ds_write_addtid_b32 v63 offset:5632\n"
      "v_cvt_pk_bf16_f32 v14, v14, v18\n"
      "v_cvt_pk_bf16_f32 v15, v15, v19\n"
      "v_mfma_f32_16x16x32_bf16 v[80:83], a[52:55], a[208:211], v[80:83]\n"
      "ds_write_addtid_b32 v14 offset:52480\n"
      "ds_write_addtid_b32 v15 offset:56576\n"
      "v_mfma_f32_16x16x32_bf16 v[84:87], a[56:59], a[208:211], v[84:87]\n"
      "v_mfma_f32_16x16x32_bf16 v[76:79], a[60:63], a[212:215], v[76:79]\n"
      "v_fma_f32 v40, 0.5, v52, -v64\n"
      "v_fma_f32 v41, 0.5, v53, -v65\n"
      "v_fma_f32 v42, 0.5, v54, -v66\n"
      "v_fma_f32 v43, 0.5, v55, -v67\n"
      "v_mfma_f32_16x16x32_bf16 v[80:83], a[64:67], a[212:215], v[80:83]\n"
      "v_fma_f32 v44, 0.5, v56, -v68\n"
      "v_fma_f32 v45, 0.5, v57, -v69\n"
      "v_fma_f32 v46, 0.5, v58, -v70\n"
      "v_fma_f32 v47, 0.5, v59, -v71\n"
      "v_mfma_f32_16x16x32_bf16 v[84:87], a[68:71], a[212:215], v[84:87]\n"
      "v_fma_f32 v48, 0.5, v60, -v72\n"
      "v_fma_f32 v49, 0.5, v61, -v73\n"
      "v_fma_f32 v50, 0.5, v62, -v74\n"
      "v_fma_f32 v51, 0.5, v63, -v75\n"
      "s_waitcnt lgkmcnt(0)\n"
      "s_nop 0\n"
      "s_barrier\n"
      "s_add_u32 s55, s55, 2\n"
      "s_cmp_lt_u32 s55, 6\n"
      "s_cbranch_scc1 LSloop_%=\n"
      "LSlast_%=:\n"
      "v_mfma_f32_16x16x32_bf16 v[76:79], a[72:75], a[192:195], v[76:79]\n"
      "ds_read_b128 a[204:207], v0 offset:16384\n"
      "ds_read2st64_b32 v[28:29], v6 offset0:0 offset1:2\n"
      "v_mfma_f32_16x16x32_bf16 v[80:83], a[76:79], a[192:195], v[80:83]\n"
      "ds_read_b128 a[208:211], v0 offset:20480\n"
      "ds_read_b64_tr_b16 a[220:221], v7 offset:33792\n"
      "v_mfma_f32_16x16x32_bf16 v[84:87], a[80:83], a[192:195], v[84:87]\n"
      "ds_read2st64_b32 v[30:31], v6 offset0:4 offset1:6\n"
      "ds_read2st64_b32 v[32:33], v6 offset0:8 offset1:10\n"
      "v_mfma_f32_16x16x32_bf16 v[76:79], a[84:87], a[196:199], v[76:79]\n"
      "ds_read_b128 a[212:215], v0 offset:16448\n"
      "v_mfma_f32_16x16x32_bf16 v[80:83], a[88:91], a[196:199], v[80:83]\n"
      "ds_read_b64_tr_b16 a[222:223], v7 offset:33800\n"
      "v_mfma_f32_16x16x32_bf16 v[84:87], a[92:95], a[196:199], v[84:87]\n"
      "v_mfma_f32_16x16x32_bf16 v[76:79], a[96:99], a[200:203], v[76:79]\n"
      "ds_read_b128 a[192:195], v0 offset:20544\n"
      "ds_read2st64_b32 v[34:35], v6 offset0:12 offset1:14\n"
      "v_mfma_f32_16x16x32_bf16 v[80:83], a[100:103], a[200:203], v[80:83]\n"
      "ds_read2st64_b32 v[36:37], v6 offset0:16 offset1:18\n"
      "v_mfma_f32_16x16x32_bf16 v[84:87], a[104:107], a[200:203], v[84:87]\n"
      "ds_read2st64_b32 v[38:39], v6 offset0:20 offset1:22\n"
      "s_waitcnt lgkmcnt(8)\n"
      "v_mfma_f32_16x16x32_bf16 v[52:55], v[252:255], a[204:207], v[112:115]\n"
      "ds_read_b128 a[196:199], v0 offset:16512\n"
      "s_waitcnt lgkmcnt(7)\n"
      "v_sub_f32 v28, v28, v40\n"
      "v_sub_f32 v29, v29, v41\n"
      "v_sub_f32 v30, v30, v42\n"
      "v_sub_f32 v31, v31, v43\n"
      "s_waitcnt lgkmcnt(5)\n"
      "v_mfma_f32_4x4x4_16b_bf16 v[12:15], a[216:217], a[220:221], 0\n"
      "ds_read_b64_tr_b16 a[224:225], v7 offset:34304\n"
      "ds_read_b64_tr_b16 a[226:227], v7 offset:34312\n"
      "v_fmac_f32 v28, s46, v100\n"
      "v_fmac_f32 v29, s46, v101\n"
      "v_fmac_f32 v30, s46, v102\n"
      "v_mfma_f32_4x4x4_16b_bf16 v[16:19], a[216:217], a[222:223], 0\n"
      "v_fmac_f32 v31, s46, v103\n"
      "v_fmac_f32 v40, s45, v28\n"
      "v_fmac_f32 v41, s45, v29\n"
      "v_mfma_f32_16x16x32_bf16 v[56:59], v[244:247], a[204:207], v[116:119]\n"
      "v_fmac_f32 v42, s45, v30\n"
      "v_cvt_pk_bf16_f32 v12, v12, v16\n"
      "v_mfma_f32_16x16x32_bf16 v[60:63], v[236:239], a[204:207], v[120:123]\n"
      "ds_write_addtid_b32 v12 offset:24832\n"
      "v_fmac_f32_e64 v43, s45, v31\n"
      "v_mfma_f32_16x16x32_bf16 v[64:67], v[248:251], a[208:211], v[112:115]\n"
      "ds_read_b128 a[200:203], v0 offset:20608\n"
      "v_fma_f32 v28, s47, v28, v76\n"
      "v_mfma_f32_16x16x32_bf16 v[68:71], v[240:243], a[208:211], v[116:119]\n"
      "v_cvt_pk_bf16_f32 v13, v13, v17\n"
      "v_fma_f32 v29, s47, v29, v77\n"
      "v_cvt_pk_bf16_f32 v14, v14, v18\n"
      "v_mfma_f32_16x16x32_bf16 v[72:75], v[232:235], a[208:211], v[120:123]\n"
      "ds_write_addtid_b32 v13 offset:28928\n"
      "ds_write_addtid_b32 v14 offset:33024\n"
      "v_mfma_f32_16x16x32_bf16 v[52:55], v[228:231], a[212:215], v[52:55]\n"
      "ds_read_b128 a[204:207], v1 offset:16384\n"
      "v_fma_f32 v30, s47, v30, v78\n"
      "v_mfma_f32_16x16x32_bf16 v[56:59], v[220:223], a[212:215], v[56:59]\n"
      "v_fma_f32 v31, s47, v31, v79\n"
      "v_cvt_pk_bf16_f32 v15, v15, v19\n"
      "v_mfma_f32_16x16x32_bf16 v[60:63], v[212:215], a[212:215], v[60:63]\n"
      "ds_write_addtid_b32 v15 offset:37120\n"
      "v_cvt_pk_bf16_f32 v40, v40, v41\n"
      "s_nop 0\n"
      "s_waitcnt lgkmcnt(4)\n"
      "v_mfma_f32_16x16x32_bf16 v[64:67], v[224:227], a[192:195], v[64:67]\n"
      "ds_read_b128 a[208:211], v1 offset:20480\n"
      "ds_write_addtid_b32 v40 offset:12288\n"
      "v_cvt_pk_bf16_f32 v42, v42, v43\n"
      "v_mfma_f32_16x16x32_bf16 v[68:71], v[216:219], a[192:195], v[68:71]\n"
      "ds_write_addtid_b32 v42 offset:12800\n"
      "v_cvt_pk_bf16_f32 v28, v28, v29\n"
      "v_mfma_f32_16x16x32_bf16 v[72:75], v[208:211], a[192:195], v[72:75]\n"
      "ds_write_addtid_b32 v28 offset:14336\n"
      "ds_read_b64_tr_b16 a[228:229], v7 offset:34816\n"
      "v_mfma_f32_4x4x4_16b_bf16 v[20:23], a[216:217], a[224:225], 0\n"
      "ds_read_b64_tr_b16 a[230:231], v7 offset:34824\n"
      "v_mfma_f32_4x4x4_16b_bf16 v[24:27], a[216:217], a[226:227], 0\n"
      "v_cvt_pk_bf16_f32 v30, v30, v31\n"
      "v_sub_f32 v32, v32, v44\n"
      "v_sub_f32 v33, v33, v45\n"
      "v_mfma_f32_16x16x32_bf16 v[52:55], v[204:207], a[196:199], v[52:55]\n"
      "ds_read_b128 a[212:215], v1 offset:16448\n"
      "ds_write_addtid_b32 v30 offset:14848\n"
      "v_mfma_f32_16x16x32_bf16 v[56:59], v[196:199], a[196:199], v[56:59]\n"
      "v_cvt_pk_bf16_f32 v20, v20, v24\n"
      "v_sub_f32_e64 v34, v34, v46\n"
      "v_mfma_f32_16x16x32_bf16 v[60:63], v[188:191], a[196:199], v[60:63]\n"
      "ds_write_addtid_b32 v20 offset:25344\n"
      "v_cvt_pk_bf16_f32 v21, v21, v25\n"
      "v_mfma_f32_16x16x32_bf16 v[64:67], v[200:203], a[200:203], v[64:67]\n"
      "ds_read_b128 a[192:195], v1 offset:20544\n"
      "ds_write_addtid_b32 v21 offset:29440\n"
      "v_mfma_f32_16x16x32_bf16 v[68:71], v[192:195], a[200:203], v[68:71]\n"
      "v_sub_f32 v35, v35, v47\n"
      "v_fmac_f32 v32, s46, v104\n"
      "v_mfma_f32_16x16x32_bf16 v[72:75], v[184:187], a[200:203], v[72:75]\n"
      "v_cvt_pk_bf16_f32 v22, v22, v26\n"
      "v_fmac_f32 v33, s46, v105\n"
      "s_waitcnt lgkmcnt(3)\n"
      "v_mfma_f32_16x16x32_bf16 v[52:55], v[180:183], a[204:207], v[52:55]\n"
      "ds_read_b128 a[196:199], v1 offset:16512\n"
      "ds_write_addtid_b32 v22 offset:33536\n"
      "v_mfma_f32_16x16x32_bf16 v[56:59], v[172:175], a[204:207], v[56:59]\n"
      "v_fmac_f32_e64 v34, s46, v106\n"
      "v_cvt_pk_bf16_f32 v23, v23, v27\n"
      "v_mfma_f32_16x16x32_bf16 v[60:63], v[164:167], a[204:207], v[60:63]\n"
      "ds_write_addtid_b32 v23 offset:37632\n"
      "v_fmac_f32 v35, s46, v107\n"
      "v_mfma_f32_16x16x32_bf16 v[64:67], v[176:179], a[208:211], v[64:67]\n"
      "ds_read_b128 a[200:203], v1 offset:20608\n"
      "v_fmac_f32 v44, s45, v32\n"
      "v_mfma_f32_16x16x32_bf16 v[68:71], v[168:171], a[208:211], v[68:71]\n"
      "ds_read_b64_tr_b16 a[232:233], v7 offset:35328\n"
      "ds_read_b64_tr_b16 a[234:235], v7 offset:35336\n"
      "v_fmac_f32 v45, s45, v33\n"
      "v_mfma_f32_4x4x4_16b_bf16 v[12:15], a[216:217], a[228:229], 0\n"
      "v_fmac_f32 v46, s45, v34\n"
      "v_mfma_f32_4x4x4_16b_bf16 v[16:19], a[216:217], a[230:231], 0\n"
      "v_fmac_f32_e64 v47, s45, v35\n"
      "v_fma_f32 v32, s47, v32, v80\n"
      "v_fma_f32 v33, s47, v33, v81\n"
      "v_mfma_f32_16x16x32_bf16 v[72:75], v[160:163], a[208:211], v[72:75]\n"
      "v_fma_f32 v34, s47, v34, v82\n"
      "v_cvt_pk_bf16_f32 v12, v12, v16\n"
      "v_mfma_f32_16x16x32_bf16 v[52:55], v[156:159], a[212:215], v[52:55]\n"
      "ds_read_b128 a[204:207], v2 offset:16384\n"
      "ds_write_addtid_b32 v12 offset:25856\n"
      "v_mfma_f32_16x16x32_bf16 v[56:59], v[148:151], a[212:215], v[56:59]\n"
      "v_cvt_pk_bf16_f32 v13, v13, v17\n"
      "v_fma_f32 v35, s47, v35, v83\n"
      "v_mfma_f32_16x16x32_bf16 v[60:63], v[140:143], a[212:215], v[60:63]\n"
      "ds_write_addtid_b32 v13 offset:29952\n"
      "v_cvt_pk_bf16_f32 v14, v14, v18\n"
      "s_nop 0\n"
      "s_waitcnt lgkmcnt(3)\n"
      "v_mfma_f32_16x16x32_bf16 v[64:67], v[152:155], a[192:195], v[64:67]\n"
      "ds_read_b128 a[208:211], v2 offset:20480\n"
      "ds_write_addtid_b32 v14 offset:34048\n"
      "v_mfma_f32_16x16x32_bf16 v[68:71], v[144:147], a[192:195], v[68:71]\n"
      "v_cvt_pk_bf16_f32 v44, v44, v45\n"
      "v_cvt_pk_bf16_f32 v46, v46, v47\n"
      "v_mfma_f32_16x16x32_bf16 v[72:75], v[136:139], a[192:195], v[72:75]\n"
      "ds_write_addtid_b32 v44 offset:20484\n"
      "ds_write_addtid_b32 v46 offset:20996\n"
      "v_mfma_f32_16x16x32_bf16 v[52:55], v[132:135], a[196:199], v[52:55]\n"
      "ds_read_b128 a[212:215], v2 offset:16448\n"
      "v_cvt_pk_bf16_f32 v15, v15, v19\n"
      "v_cvt_pk_bf16_f32 v32, v32, v33\n"
      "v_mfma_f32_16x16x32_bf16 v[56:59], v[124:127], a[196:199], v[56:59]\n"
      "ds_write_addtid_b32 v32 offset:22532\n"
      "ds_write_addtid_b32 v15 offset:38144\n"
      "v_mfma_f32_16x16x32_bf16 v[60:63], a[184:187], a[196:199], v[60:63]\n"
      "v_cvt_pk_bf16_f32 v34, v34, v35\n"
      "v_sub_f32_e64 v36, v36, v48\n"
      "v_mfma_f32_16x16x32_bf16 v[64:67], v[128:131], a[200:203], v[64:67]\n"
      "ds_read_b128 a[192:195], v2 offset:20544\n"
      "ds_write_addtid_b32 v34 offset:23044\n"
      "v_mfma_f32_4x4x4_16b_bf16 v[20:23], a[216:217], a[232:233], 0\n"
      "ds_read_b64_tr_b16 a[220:221], v7 offset:35840\n"
      "v_mfma_f32_4x4x4_16b_bf16 v[24:27], a[216:217], a[234:235], 0\n"
      "ds_read_b64_tr_b16 a[222:223], v7 offset:35848\n"
      "v_sub_f32 v37, v37, v49\n"
      "v_sub_f32 v38, v38, v50\n"
      "v_mfma_f32_16x16x32_bf16 v[68:71], a[188:191], a[200:203], v[68:71]\n"
      "v_sub_f32_e64 v39, v39, v51\n"
      "v_cvt_pk_bf16_f32 v20, v20, v24\n"
      "v_mfma_f32_16x16x32_bf16 v[72:75], a[180:183], a[200:203], v[72:75]\n"
      "ds_write_addtid_b32 v20 offset:26368\n"
      "s_waitcnt lgkmcnt(5)\n"
      "v_mfma_f32_16x16x32_bf16 v[52:55], a[176:179], a[204:207], v[52:55]\n"
      "ds_read_b128 a[196:199], v2 offset:16512\n"
      "v_fmac_f32 v36, s46, v108\n"
      "v_mfma_f32_16x16x32_bf16 v[56:59], a[168:171], a[204:207], v[56:59]\n"
      "v_cvt_pk_bf16_f32 v21, v21, v25\n"
      "v_fmac_f32 v37, s46, v109\n"
      "v_mfma_f32_16x16x32_bf16 v[60:63], a[160:163], a[204:207], v[60:63]\n"
      "ds_write_addtid_b32 v21 offset:30464\n"
      "v_fmac_f32 v38, s46, v110\n"
      "v_cvt_pk_bf16_f32 v22, v22, v26\n"
      "v_mfma_f32_16x16x32_bf16 v[64:67], a[172:175], a[208:211], v[64:67]\n"
      "ds_read_b128 a[200:203], v2 offset:20608\n"
      "ds_write_addtid_b32 v22 offset:34560\n"
      "v_mfma_f32_16x16x32_bf16 v[68:71], a[164:167], a[208:211], v[68:71]\n"
      "v_fmac_f32 v39, s46, v111\n"
      "v_fmac_f32 v48, s45, v36\n"
      "v_mfma_f32_16x16x32_bf16 v[72:75], a[156:159], a[208:211], v[72:75]\n"
      "v_cvt_pk_bf16_f32 v23, v23, v27\n"
      "v_fmac_f32 v49, s45, v37\n"
      "v_mfma_f32_16x16x32_bf16 v[52:55], a[152:155], a[212:215], v[52:55]\n"
      "ds_read_b128 a[204:207], v3 offset:50688\n"
      "ds_write_addtid_b32 v23 offset:38656\n"
      "v_mfma_f32_16x16x32_bf16 v[56:59], a[144:147], a[212:215], v[56:59]\n"
      "v_fmac_f32 v50, s45, v38\n"
      "v_fmac_f32 v51, s45, v39\n"
      "v_fma_f32 v36, s47, v36, v84\n"
      "v_mfma_f32_16x16x32_bf16 v[60:63], a[136:139], a[212:215], v[60:63]\n"
      "ds_read_b64_tr_b16 a[224:225], v7 offset:36352\n"
      "ds_read_b64_tr_b16 a[226:227], v7 offset:36360\n"
      "s_waitcnt lgkmcnt(4)\n"
      "v_mfma_f32_4x4x4_16b_bf16 v[12:15], a[216:217], a[220:221], 0\n"
      "v_fma_f32 v37, s47, v37, v85\n"
      "v_mfma_f32_4x4x4_16b_bf16 v[16:19], a[216:217], a[222:223], 0\n"
      "v_fma_f32 v38, s47, v38, v86\n"
      "v_fma_f32 v39, s47, v39, v87\n"
      "v_cvt_pk_bf16_f32 v48, v48, v49\n"
      "v_mfma_f32_16x16x32_bf16 v[64:67], a[148:151], a[192:195], v[64:67]\n"
      "ds_read_b128 a[208:211], v3 offset:50752\n"
      "ds_write_addtid_b32 v48 offset:16384\n"
      "v_mfma_f32_16x16x32_bf16 v[68:71], a[140:143], a[192:195], v[68:71]\n"
      "v_cvt_pk_bf16_f32 v12, v12, v16\n"
      "v_cvt_pk_bf16_f32 v13, v13, v17\n"
      "v_mfma_f32_16x16x32_bf16 v[72:75], a[132:135], a[192:195], v[72:75]\n"
      "ds_write_addtid_b32 v12 offset:26880\n"
      "ds_write_addtid_b32 v13 offset:30976\n"
      "v_mfma_f32_16x16x32_bf16 v[52:55], a[128:131], a[196:199], v[52:55]\n"
      "ds_read_b128 a[212:215], v3 offset:50816\n"
      "v_cvt_pk_bf16_f32 v14, v14, v18\n"
      "v_mfma_f32_16x16x32_bf16 v[56:59], a[120:123], a[196:199], v[56:59]\n"
      "ds_write_addtid_b32 v14 offset:35072\n"
      "v_cvt_pk_bf16_f32 v50, v50, v51\n"
      "v_mfma_f32_16x16x32_bf16 v[60:63], a[112:115], a[196:199], v[60:63]\n"
      "ds_write_addtid_b32 v50 offset:16896\n"
      "v_cvt_pk_bf16_f32 v36, v36, v37\n"
      "v_mfma_f32_16x16x32_bf16 v[64:67], a[124:127], a[200:203], v[64:67]\n"
      "ds_read_b128 a[192:195], v4 offset:50688\n"
      "ds_write_addtid_b32 v36 offset:18432\n"
      "v_cvt_pk_bf16_f32 v15, v15, v19\n"
      "v_mfma_f32_16x16x32_bf16 v[68:71], a[116:119], a[200:203], v[68:71]\n"
      "ds_write_addtid_b32 v15 offset:39168\n"
      "v_cvt_pk_bf16_f32 v38, v38, v39\n"
      "v_mfma_f32_16x16x32_bf16 v[72:75], a[108:111], a[200:203], v[72:75]\n"
      "ds_write_addtid_b32 v38 offset:18944\n"
      "v_sub_f32 v52, v64, v52\n"
      "s_waitcnt lgkmcnt(5)\n"
      "v_mfma_f32_16x16x32_bf16 v[88:91], a[0:3], a[204:207], 0\n"
      "ds_read_b128 a[196:199], v4 offset:50752\n"
      "ds_write_addtid_b32 v52 offset:6144\n"
      "v_mfma_f32_16x16x32_bf16 v[92:95], a[4:7], a[204:207], 0\n"
      "v_sub_f32 v53, v65, v53\n"
      "v_sub_f32 v54, v66, v54\n"
      "v_mfma_f32_4x4x4_16b_bf16 v[20:23], a[216:217], a[224:225], 0\n"
      "v_sub_f32 v55, v67, v55\n"
      "v_sub_f32 v56, v68, v56\n"
      "v_mfma_f32_4x4x4_16b_bf16 v[24:27], a[216:217], a[226:227], 0\n"
      "v_sub_f32 v57, v69, v57\n"
      "v_sub_f32 v58, v70, v58\n"
      "v_sub_f32_e64 v59, v71, v59\n"
      "v_mfma_f32_16x16x32_bf16 v[96:99], a[8:11], a[204:207], 0\n"
      "ds_write_addtid_b32 v53 offset:6656\n"
      "ds_write_addtid_b32 v54 offset:7168\n"
      "v_mfma_f32_16x16x32_bf16 v[88:91], a[12:15], a[208:211], v[88:91]\n"
      "ds_read_b128 a[200:203], v4 offset:50816\n"
      "ds_write_addtid_b32 v55 offset:7680\n"
      "v_mfma_f32_16x16x32_bf16 v[92:95], a[16:19], a[208:211], v[92:95]\n"
      "ds_write_addtid_b32 v56 offset:8192\n"
      "ds_write_addtid_b32 v57 offset:8704\n"
      "v_mfma_f32_16x16x32_bf16 v[96:99], a[20:23], a[208:211], v[96:99]\n"
      "ds_write_addtid_b32 v58 offset:9216\n"
      "ds_write_addtid_b32 v59 offset:9728\n"
      "v_mfma_f32_16x16x32_bf16 v[88:91], a[24:27], a[212:215], v[88:91]\n"
      "ds_read_b128 a[204:207], v5 offset:50688\n"
      "v_cvt_pk_bf16_f32 v20, v20, v24\n"
      "v_mfma_f32_16x16x32_bf16 v[92:95], a[28:31], a[212:215], v[92:95]\n"
      "ds_write_addtid_b32 v20 offset:27392\n"
      "v_cvt_pk_bf16_f32 v21, v21, v25\n"
      "v_mfma_f32_16x16x32_bf16 v[96:99], a[32:35], a[212:215], v[96:99]\n"
      "ds_write_addtid_b32 v21 offset:31488\n"
      "v_sub_f32 v60, v72, v60\n"
      "s_waitcnt lgkmcnt(7)\n"
      "v_mfma_f32_16x16x32_bf16 v[88:91], a[36:39], a[192:195], v[88:91]\n"
      "ds_read_b128 a[208:211], v5 offset:50752\n"
      "ds_write_addtid_b32 v60 offset:10240\n"
      "v_mfma_f32_16x16x32_bf16 v[92:95], a[40:43], a[192:195], v[92:95]\n"
      "v_cvt_pk_bf16_f32 v22, v22, v26\n"
      "v_cvt_pk_bf16_f32 v23, v23, v27\n"
      "v_sub_f32 v61, v73, v61\n"
      "v_mfma_f32_16x16x32_bf16 v[96:99], a[44:47], a[192:195], v[96:99]\n"
      "ds_write_addtid_b32 v22 offset:35584\n"
      "ds_write_addtid_b32 v23 offset:39680\n"
      "v_sub_f32 v62, v74, v62\n"
      "v_sub_f32_e64 v63, v75, v63\n"
      "v_mfma_f32_16x16x32_bf16 v[88:91], a[48:51], a[196:199], v[88:91]\n"
      "ds_read_b128 a[212:215], v5 offset:50816\n"
      "ds_write_addtid_b32 v61 offset:10752\n"
      "v_mfma_f32_16x16x32_bf16 v[92:95], a[52:55], a[196:199], v[92:95]\n"
      "ds_write_addtid_b32 v62 offset:11264\n"
      "ds_write_addtid_b32 v63 offset:11776\n"
      "v_mfma_f32_16x16x32_bf16 v[96:99], a[56:59], a[196:199], v[96:99]\n"
      "v_mfma_f32_16x16x32_bf16 v[88:91], a[60:63], a[200:203], v[88:91]\n"
      "v_fma_f32 v40, 0.5, v52, -v64\n"
      "v_fma_f32 v41, 0.5, v53, -v65\n"
      "v_fma_f32 v42, 0.5, v54, -v66\n"
      "v_fma_f32 v43, 0.5, v55, -v67\n"
      "v_mfma_f32_16x16x32_bf16 v[92:95], a[64:67], a[200:203], v[92:95]\n"
      "v_fma_f32 v44, 0.5, v56, -v68\n"
      "v_fma_f32 v45, 0.5, v57, -v69\n"
      "v_fma_f32 v46, 0.5, v58, -v70\n"
      "v_fma_f32 v47, 0.5, v59, -v71\n"
      "v_mfma_f32_16x16x32_bf16 v[96:99], a[68:71], a[200:203], v[96:99]\n"
      "v_fma_f32 v48, 0.5, v60, -v72\n"
      "v_fma_f32 v49, 0.5, v61, -v73\n"
      "v_fma_f32 v50, 0.5, v62, -v74\n"
      "v_fma_f32 v51, 0.5, v63, -v75\n"
      "s_waitcnt lgkmcnt(0)\n"
      "s_barrier\n"
      "v_mfma_f32_16x16x32_bf16 v[88:91], a[72:75], a[204:207], v[88:91]\n"
      "ds_read_b128 a[192:195], v0 offset:0\n"
      "ds_read2st64_b32 v[28:29], v6 offset0:24 offset1:26\n"
      "v_mfma_f32_16x16x32_bf16 v[92:95], a[76:79], a[204:207], v[92:95]\n"
      "ds_read_b128 a[196:199], v0 offset:4096\n"
      "ds_read_b64_tr_b16 a[220:221], v7 offset:50688\n"
      "v_mfma_f32_16x16x32_bf16 v[96:99], a[80:83], a[204:207], v[96:99]\n"
      "ds_read2st64_b32 v[30:31], v6 offset0:28 offset1:30\n"
      "ds_read2st64_b32 v[32:33], v6 offset0:32 offset1:34\n"
      "v_mfma_f32_16x16x32_bf16 v[88:91], a[84:87], a[208:211], v[88:91]\n"
      "ds_read_b128 a[200:203], v0 offset:64\n"
      "v_mfma_f32_16x16x32_bf16 v[92:95], a[88:91], a[208:211], v[92:95]\n"
      "v_mfma_f32_16x16x32_bf16 v[96:99], a[92:95], a[208:211], v[96:99]\n"
      "ds_read_b64_tr_b16 a[222:223], v7 offset:50696\n"
      "ds_read2st64_b32 v[34:35], v6 offset0:36 offset1:38\n"
      "v_mfma_f32_16x16x32_bf16 v[88:91], a[96:99], a[212:215], v[88:91]\n"
      "ds_read_b128 a[204:207], v0 offset:4160\n"
      "ds_read2st64_b32 v[36:37], v6 offset0:40 offset1:42\n"
      "v_mfma_f32_16x16x32_bf16 v[92:95], a[100:103], a[212:215], v[92:95]\n"
      "v_mfma_f32_16x16x32_bf16 v[96:99], a[104:107], a[212:215], v[96:99]\n"
      "ds_read2st64_b32 v[38:39], v6 offset0:44 offset1:46\n"
      "s_waitcnt lgkmcnt(8)\n"
      "v_mfma_f32_16x16x32_bf16 v[52:55], v[252:255], a[192:195], v[76:79]\n"
      "ds_read_b128 a[208:211], v0 offset:128\n"
      "s_waitcnt lgkmcnt(7)\n"
      "v_sub_f32 v28, v28, v40\n"
      "v_sub_f32 v29, v29, v41\n"
      "v_sub_f32 v30, v30, v42\n"
      "v_sub_f32 v31, v31, v43\n"
      "s_waitcnt lgkmcnt(5)\n"
      "v_mfma_f32_4x4x4_16b_bf16 v[12:15], a[216:217], a[220:221], 0\n"
      "ds_read_b64_tr_b16 a[224:225], v7 offset:51200\n"
      "ds_read_b64_tr_b16 a[226:227], v7 offset:51208\n"
      "v_fmac_f32 v28, s46, v112\n"
      "v_fmac_f32 v29, s46, v113\n"
      "v_fmac_f32 v30, s46, v114\n"
      "v_mfma_f32_4x4x4_16b_bf16 v[16:19], a[216:217], a[222:223], 0\n"
      "v_fmac_f32 v31, s46, v115\n"
      "v_fmac_f32 v40, s45, v28\n"
      "v_fmac_f32 v41, s45, v29\n"
      "v_mfma_f32_16x16x32_bf16 v[56:59], v[244:247], a[192:195], v[80:83]\n"
      "v_fmac_f32 v42, s45, v30\n"
      "v_cvt_pk_bf16_f32 v12, v12, v16\n"
      "v_mfma_f32_16x16x32_bf16 v[60:63], v[236:239], a[192:195], v[84:87]\n"
      "ds_write_addtid_b32 v12 offset:41216\n"
      "v_fmac_f32_e64 v43, s45, v31\n"
      "v_mfma_f32_16x16x32_bf16 v[64:67], v[248:251], a[196:199], v[76:79]\n"
      "ds_read_b128 a[212:215], v0 offset:4224\n"
      "v_fma_f32 v28, s47, v28, v88\n"
      "v_mfma_f32_16x16x32_bf16 v[68:71], v[240:243], a[196:199], v[80:83]\n"
      "v_cvt_pk_bf16_f32 v13, v13, v17\n"
      "v_fma_f32 v29, s47, v29, v89\n"
      "v_cvt_pk_bf16_f32 v14, v14, v18\n"
      "v_mfma_f32_16x16x32_bf16 v[72:75], v[232:235], a[196:199], v[84:87]\n"
      "ds_write_addtid_b32 v13 offset:45312\n"
      "ds_write_addtid_b32 v14 offset:49408\n"
      "v_mfma_f32_16x16x32_bf16 v[52:55], v[228:231], a[200:203], v[52:55]\n"
      "ds_read_b128 a[192:195], v1 offset:0\n"
      "v_fma_f32 v30, s47, v30, v90\n"
      "v_mfma_f32_16x16x32_bf16 v[56:59], v[220:223], a[200:203], v[56:59]\n"
      "v_fma_f32 v31, s47, v31, v91\n"
      "v_cvt_pk_bf16_f32 v15, v15, v19\n"
      "v_mfma_f32_16x16x32_bf16 v[60:63], v[212:215], a[200:203], v[60:63]\n"
      "ds_write_addtid_b32 v15 offset:53504\n"
      "v_cvt_pk_bf16_f32 v40, v40, v41\n"
      "s_nop 0\n"
      "s_waitcnt lgkmcnt(4)\n"
      "v_mfma_f32_16x16x32_bf16 v[64:67], v[224:227], a[204:207], v[64:67]\n"
      "ds_read_b128 a[196:199], v1 offset:4096\n"
      "ds_write_addtid_b32 v40 offset:13312\n"
      "v_cvt_pk_bf16_f32 v42, v42, v43\n"
      "v_mfma_f32_16x16x32_bf16 v[68:71], v[216:219], a[204:207], v[68:71]\n"
      "ds_write_addtid_b32 v42 offset:13824\n"
      "v_cvt_pk_bf16_f32 v28, v28, v29\n"
      "v_mfma_f32_16x16x32_bf16 v[72:75], v[208:211], a[204:207], v[72:75]\n"
      "ds_write_addtid_b32 v28 offset:15360\n"
      "ds_read_b64_tr_b16 a[228:229], v7 offset:51712\n"
      "v_mfma_f32_4x4x4_16b_bf16 v[20:23], a[216:217], a[224:225], 0\n"
      "ds_read_b64_tr_b16 a[230:231], v7 offset:51720\n"
      "v_mfma_f32_4x4x4_16b_bf16 v[24:27], a[216:217], a[226:227], 0\n"
      "v_cvt_pk_bf16_f32 v30, v30, v31\n"
      "v_sub_f32 v32, v32, v44\n"
      "v_sub_f32 v33, v33, v45\n"
      "v_mfma_f32_16x16x32_bf16 v[52:55], v[204:207], a[208:211], v[52:55]\n"
      "ds_read_b128 a[200:203], v1 offset:64\n"
      "ds_write_addtid_b32 v30 offset:15872\n"
      "v_mfma_f32_16x16x32_bf16 v[56:59], v[196:199], a[208:211], v[56:59]\n"
      "v_cvt_pk_bf16_f32 v20, v20, v24\n"
      "v_sub_f32_e64 v34, v34, v46\n"
      "v_mfma_f32_16x16x32_bf16 v[60:63], v[188:191], a[208:211], v[60:63]\n"
      "ds_write_addtid_b32 v20 offset:41728\n"
      "v_cvt_pk_bf16_f32 v21, v21, v25\n"
      "v_mfma_f32_16x16x32_bf16 v[64:67], v[200:203], a[212:215], v[64:67]\n"
      "ds_read_b128 a[204:207], v1 offset:4160\n"
      "ds_write_addtid_b32 v21 offset:45824\n"
      "v_mfma_f32_16x16x32_bf16 v[68:71], v[192:195], a[212:215], v[68:71]\n"
      "v_sub_f32 v35, v35, v47\n"
      "v_fmac_f32 v32, s46, v116\n"
      "v_mfma_f32_16x16x32_bf16 v[72:75], v[184:187], a[212:215], v[72:75]\n"
      "v_cvt_pk_bf16_f32 v22, v22, v26\n"
      "v_fmac_f32 v33, s46, v117\n"
      "s_waitcnt lgkmcnt(3)\n"
      "v_mfma_f32_16x16x32_bf16 v[52:55], v[180:183], a[192:195], v[52:55]\n"
      "ds_read_b128 a[208:211], v1 offset:128\n"
      "ds_write_addtid_b32 v22 offset:49920\n"
      "v_mfma_f32_16x16x32_bf16 v[56:59], v[172:175], a[192:195], v[56:59]\n"
      "v_fmac_f32_e64 v34, s46, v118\n"
      "v_cvt_pk_bf16_f32 v23, v23, v27\n"
      "v_mfma_f32_16x16x32_bf16 v[60:63], v[164:167], a[192:195], v[60:63]\n"
      "ds_write_addtid_b32 v23 offset:54016\n"
      "v_fmac_f32 v35, s46, v119\n"
      "v_mfma_f32_16x16x32_bf16 v[64:67], v[176:179], a[196:199], v[64:67]\n"
      "ds_read_b128 a[212:215], v1 offset:4224\n"
      "v_fmac_f32 v44, s45, v32\n"
      "v_mfma_f32_16x16x32_bf16 v[68:71], v[168:171], a[196:199], v[68:71]\n"
      "ds_read_b64_tr_b16 a[232:233], v7 offset:52224\n"
      "ds_read_b64_tr_b16 a[234:235], v7 offset:52232\n"
      "v_fmac_f32 v45, s45, v33\n"
      "v_mfma_f32_4x4x4_16b_bf16 v[12:15], a[216:217], a[228:229], 0\n"
      "v_fmac_f32 v46, s45, v34\n"
      "v_mfma_f32_4x4x4_16b_bf16 v[16:19], a[216:217], a[230:231], 0\n"
      "v_fmac_f32_e64 v47, s45, v35\n"
      "v_fma_f32 v32, s47, v32, v92\n"
      "v_fma_f32 v33, s47, v33, v93\n"
      "v_mfma_f32_16x16x32_bf16 v[72:75], v[160:163], a[196:199], v[72:75]\n"
      "v_fma_f32 v34, s47, v34, v94\n"
      "v_cvt_pk_bf16_f32 v12, v12, v16\n"
      "v_mfma_f32_16x16x32_bf16 v[52:55], v[156:159], a[200:203], v[52:55]\n"
      "ds_read_b128 a[192:195], v2 offset:0\n"
      "ds_write_addtid_b32 v12 offset:42240\n"
      "v_mfma_f32_16x16x32_bf16 v[56:59], v[148:151], a[200:203], v[56:59]\n"
      "v_cvt_pk_bf16_f32 v13, v13, v17\n"
      "v_fma_f32 v35, s47, v35, v95\n"
      "v_mfma_f32_16x16x32_bf16 v[60:63], v[140:143], a[200:203], v[60:63]\n"
      "ds_write_addtid_b32 v13 offset:46336\n"
      "v_cvt_pk_bf16_f32 v14, v14, v18\n"
      "s_nop 0\n"
      "s_waitcnt lgkmcnt(3)\n"
      "v_mfma_f32_16x16x32_bf16 v[64:67], v[152:155], a[204:207], v[64:67]\n"
      "ds_read_b128 a[196:199], v2 offset:4096\n"
      "ds_write_addtid_b32 v14 offset:50432\n"
      "v_mfma_f32_16x16x32_bf16 v[68:71], v[144:147], a[204:207], v[68:71]\n"
      "v_cvt_pk_bf16_f32 v44, v44, v45\n"
      "v_cvt_pk_bf16_f32 v46, v46, v47\n"
      "v_mfma_f32_16x16x32_bf16 v[72:75], v[136:139], a[204:207], v[72:75]\n"
      "ds_write_addtid_b32 v44 offset:21508\n"
      "ds_write_addtid_b32 v46 offset:22020\n"
      "v_mfma_f32_16x16x32_bf16 v[52:55], v[132:135], a[208:211], v[52:55]\n"
      "ds_read_b128 a[200:203], v2 offset:64\n"
      "v_cvt_pk_bf16_f32 v15, v15, v19\n"
      "v_cvt_pk_bf16_f32 v32, v32, v33\n"
      "v_mfma_f32_16x16x32_bf16 v[56:59], v[124:127], a[208:211], v[56:59]\n"
      "ds_write_addtid_b32 v32 offset:23556\n"
      "ds_write_addtid_b32 v15 offset:54528\n"
      "v_mfma_f32_16x16x32_bf16 v[60:63], a[184:187], a[208:211], v[60:63]\n"
      "v_cvt_pk_bf16_f32 v34, v34, v35\n"
      "v_sub_f32_e64 v36, v36, v48\n"
      "v_mfma_f32_16x16x32_bf16 v[64:67], v[128:131], a[212:215], v[64:67]\n"
      "ds_read_b128 a[204:207], v2 offset:4160\n"
      "ds_write_addtid_b32 v34 offset:24068\n"
      "v_mfma_f32_4x4x4_16b_bf16 v[20:23], a[216:217], a[232:233], 0\n"
      "ds_read_b64_tr_b16 a[220:221], v7 offset:52736\n"
      "v_mfma_f32_4x4x4_16b_bf16 v[24:27], a[216:217], a[234:235], 0\n"
      "ds_read_b64_tr_b16 a[222:223], v7 offset:52744\n"
      "v_sub_f32 v37, v37, v49\n"
      "v_sub_f32 v38, v38, v50\n"
      "v_mfma_f32_16x16x32_bf16 v[68:71], a[188:191], a[212:215], v[68:71]\n"
      "v_sub_f32 v39, v39, v51\n"
      "v_cvt_pk_bf16_f32 v20, v20, v24\n"
      "v_mfma_f32_16x16x32_bf16 v[72:75], a[180:183], a[212:215], v[72:75]\n"
      "ds_write_addtid_b32 v20 offset:42752\n"
      "s_waitcnt lgkmcnt(5)\n"
      "v_mfma_f32_16x16x32_bf16 v[52:55], a[176:179], a[192:195], v[52:55]\n"
      "ds_read_b128 a[208:211], v2 offset:128\n"
      "v_fmac_f32_e64 v36, s46, v120\n"
      "v_mfma_f32_16x16x32_bf16 v[56:59], a[168:171], a[192:195], v[56:59]\n"
      "v_cvt_pk_bf16_f32 v21, v21, v25\n"
      "v_fmac_f32 v37, s46, v121\n"
      "v_mfma_f32_16x16x32_bf16 v[60:63], a[160:163], a[192:195], v[60:63]\n"
      "ds_write_addtid_b32 v21 offset:46848\n"
      "v_fmac_f32 v38, s46, v122\n"
      "v_cvt_pk_bf16_f32 v22, v22, v26\n"
      "v_mfma_f32_16x16x32_bf16 v[64:67], a[172:175], a[196:199], v[64:67]\n"
      "ds_read_b128 a[212:215], v2 offset:4224\n"
      "ds_write_addtid_b32 v22 offset:50944\n"
      "v_mfma_f32_16x16x32_bf16 v[68:71], a[164:167], a[196:199], v[68:71]\n"
      "v_fmac_f32 v39, s46, v123\n"
      "v_fmac_f32 v48, s45, v36\n"
      "v_mfma_f32_16x16x32_bf16 v[72:75], a[156:159], a[196:199], v[72:75]\n"
      "v_cvt_pk_bf16_f32 v23, v23, v27\n"
      "v_fmac_f32_e64 v49, s45, v37\n"
      "v_mfma_f32_16x16x32_bf16 v[52:55], a[152:155], a[200:203], v[52:55]\n"
      "ds_read_b128 a[192:195], v3 offset:0\n"
      "ds_write_addtid_b32 v23 offset:55040\n"
      "v_mfma_f32_16x16x32_bf16 v[56:59], a[144:147], a[200:203], v[56:59]\n"
      "v_fmac_f32 v50, s45, v38\n"
      "v_fmac_f32_e64 v51, s45, v39\n"
      "v_fma_f32 v36, s47, v36, v96\n"
      "v_mfma_f32_16x16x32_bf16 v[60:63], a[136:139], a[200:203], v[60:63]\n"
      "ds_read_b64_tr_b16 a[224:225], v7 offset:53248\n"
      "ds_read_b64_tr_b16 a[226:227], v7 offset:53256\n"
      "s_waitcnt lgkmcnt(4)\n"
      "v_mfma_f32_4x4x4_16b_bf16 v[12:15], a[216:217], a[220:221], 0\n"
      "v_fma_f32 v37, s47, v37, v97\n"
      "v_mfma_f32_4x4x4_16b_bf16 v[16:19], a[216:217], a[222:223], 0\n"
      "v_fma_f32 v38, s47, v38, v98\n"
      "v_fma_f32 v39, s47, v39, v99\n"
      "v_cvt_pk_bf16_f32 v48, v48, v49\n"
      "v_mfma_f32_16x16x32_bf16 v[64:67], a[148:151], a[204:207], v[64:67]\n"
      "ds_read_b128 a[196:199], v3 offset:64\n"
      "ds_write_addtid_b32 v48 offset:17408\n"
      "v_mfma_f32_16x16x32_bf16 v[68:71], a[140:143], a[204:207], v[68:71]\n"
      "v_cvt_pk_bf16_f32 v12, v12, v16\n"
      "v_cvt_pk_bf16_f32 v13, v13, v17\n"
      "v_mfma_f32_16x16x32_bf16 v[72:75], a[132:135], a[204:207], v[72:75]\n"
      "ds_write_addtid_b32 v12 offset:43264\n"
      "ds_write_addtid_b32 v13 offset:47360\n"
      "v_mfma_f32_16x16x32_bf16 v[52:55], a[128:131], a[208:211], v[52:55]\n"
      "ds_read_b128 a[200:203], v3 offset:128\n"
      "v_cvt_pk_bf16_f32 v14, v14, v18\n"
      "v_mfma_f32_16x16x32_bf16 v[56:59], a[120:123], a[208:211], v[56:59]\n"
      "ds_write_addtid_b32 v14 offset:51456\n"
      "v_cvt_pk_bf16_f32 v50, v50, v51\n"
      "v_mfma_f32_16x16x32_bf16 v[60:63], a[112:115], a[208:211], v[60:63]\n"
      "ds_write_addtid_b32 v50 offset:17920\n"
      "v_cvt_pk_bf16_f32 v36, v36, v37\n"
      "v_mfma_f32_16x16x32_bf16 v[64:67], a[124:127], a[212:215], v[64:67]\n"
      "ds_read_b128 a[204:207], v4 offset:0\n"
      "ds_write_addtid_b32 v36 offset:19456\n"
      "v_cvt_pk_bf16_f32 v15, v15, v19\n"
      "v_mfma_f32_16x16x32_bf16 v[68:71], a[116:119], a[212:215], v[68:71]\n"
      "ds_write_addtid_b32 v15 offset:55552\n"
      "v_cvt_pk_bf16_f32 v38, v38, v39\n"
      "v_mfma_f32_16x16x32_bf16 v[72:75], a[108:111], a[212:215], v[72:75]\n"
      "ds_write_addtid_b32 v38 offset:19968\n"
      "v_sub_f32 v52, v64, v52\n"
      "s_waitcnt lgkmcnt(5)\n"
      "v_mfma_f32_16x16x32_bf16 v[100:103], a[0:3], a[192:195], 0\n"
      "ds_read_b128 a[208:211], v4 offset:64\n"
      "ds_write_addtid_b32 v52 offset:0\n"
      "v_mfma_f32_16x16x32_bf16 v[104:107], a[4:7], a[192:195], 0\n"
      "v_sub_f32 v53, v65, v53\n"
      "v_sub_f32 v54, v66, v54\n"
      "v_mfma_f32_4x4x4_16b_bf16 v[20:23], a[216:217], a[224:225], 0\n"
      "v_sub_f32 v55, v67, v55\n"
      "v_sub_f32 v56, v68, v56\n"
      "v_mfma_f32_4x4x4_16b_bf16 v[24:27], a[216:217], a[226:227], 0\n"
      "v_sub_f32 v57, v69, v57\n"
      "v_sub_f32 v58, v70, v58\n"
      "v_sub_f32_e64 v59, v71, v59\n"
      "v_mfma_f32_16x16x32_bf16 v[108:111], a[8:11], a[192:195], 0\n"
      "ds_write_addtid_b32 v53 offset:512\n"
      "ds_write_addtid_b32 v54 offset:1024\n"
      "v_mfma_f32_16x16x32_bf16 v[100:103], a[12:15], a[196:199], v[100:103]\n"
      "ds_read_b128 a[212:215], v4 offset:128\n"
      "ds_write_addtid_b32 v55 offset:1536\n"
      "v_mfma_f32_16x16x32_bf16 v[104:107], a[16:19], a[196:199], v[104:107]\n"
      "ds_write_addtid_b32 v56 offset:2048\n"
      "ds_write_addtid_b32 v57 offset:2560\n"
      "v_mfma_f32_16x16x32_bf16 v[108:111], a[20:23], a[196:199], v[108:111]\n"
      "ds_write_addtid_b32 v58 offset:3072\n"
      "ds_write_addtid_b32 v59 offset:3584\n"
      "v_mfma_f32_16x16x32_bf16 v[100:103], a[24:27], a[200:203], v[100:103]\n"
      "ds_read_b128 a[192:195], v5 offset:0\n"
      "v_cvt_pk_bf16_f32 v20, v20, v24\n"
      "v_mfma_f32_16x16x32_bf16 v[104:107], a[28:31], a[200:203], v[104:107]\n"
      "ds_write_addtid_b32 v20 offset:43776\n"
      "v_cvt_pk_bf16_f32 v21, v21, v25\n"
      "v_mfma_f32_16x16x32_bf16 v[108:111], a[32:35], a[200:203], v[108:111]\n"
      "ds_write_addtid_b32 v21 offset:47872\n"
      "v_sub_f32 v60, v72, v60\n"
      "s_waitcnt lgkmcnt(6)\n"
      "v_mfma_f32_16x16x32_bf16 v[100:103], a[36:39], a[204:207], v[100:103]\n"
      "ds_read_b128 a[196:199], v5 offset:64\n"
      "ds_write_addtid_b32 v60 offset:4096\n"
      "v_mfma_f32_16x16x32_bf16 v[104:107], a[40:43], a[204:207], v[104:107]\n"
      "v_cvt_pk_bf16_f32 v22, v22, v26\n"
      "v_cvt_pk_bf16_f32 v23, v23, v27\n"
      "v_sub_f32_e64 v61, v73, v61\n"
      "v_mfma_f32_16x16x32_bf16 v[108:111], a[44:47], a[204:207], v[108:111]\n"
      "ds_write_addtid_b32 v22 offset:51968\n"
      "ds_write_addtid_b32 v23 offset:56064\n"
      "v_sub_f32 v62, v74, v62\n"
      "v_sub_f32 v63, v75, v63\n"
      "v_mfma_f32_16x16x32_bf16 v[100:103], a[48:51], a[208:211], v[100:103]\n"
      "ds_read_b128 a[200:203], v5 offset:128\n"
      "ds_write_addtid_b32 v61 offset:4608\n"
      "v_mfma_f32_16x16x32_bf16 v[104:107], a[52:55], a[208:211], v[104:107]\n"
      "ds_write_addtid_b32 v62 offset:5120\n"
      "ds_write_addtid_b32 v63 offset:5632\n"
      "v_mfma_f32_16x16x32_bf16 v[108:111], a[56:59], a[208:211], v[108:111]\n"
      "v_mfma_f32_16x16x32_bf16 v[100:103], a[60:63], a[212:215], v[100:103]\n"
      "v_fma_f32 v40, 0.5, v52, -v64\n"
      "v_fma_f32 v41, 0.5, v53, -v65\n"
      "v_fma_f32 v42, 0.5, v54, -v66\n"
      "v_fma_f32 v43, 0.5, v55, -v67\n"
      "v_mfma_f32_16x16x32_bf16 v[104:107], a[64:67], a[212:215], v[104:107]\n"
      "v_fma_f32 v44, 0.5, v56, -v68\n"
      "v_fma_f32 v45, 0.5, v57, -v69\n"
      "v_fma_f32 v46, 0.5, v58, -v70\n"
      "v_fma_f32 v47, 0.5, v59, -v71\n"
      "v_mfma_f32_16x16x32_bf16 v[108:111], a[68:71], a[212:215], v[108:111]\n"
      "v_fma_f32 v48, 0.5, v60, -v72\n"
      "v_fma_f32 v49, 0.5, v61, -v73\n"
      "v_fma_f32 v50, 0.5, v62, -v74\n"
      "v_fma_f32 v51, 0.5, v63, -v75\n"
      "s_waitcnt lgkmcnt(0)\n"
      "s_barrier\n"
      "v_mfma_f32_16x16x32_bf16 v[100:103], a[72:75], a[192:195], v[100:103]\n"
      "ds_read_b128 a[204:207], v0 offset:16384\n"
      "ds_read2st64_b32 v[28:29], v6 offset0:0 offset1:2\n"
      "ds_read2st64_b32 v[30:31], v6 offset0:4 offset1:6\n"
      "ds_read2st64_b32 v[32:33], v6 offset0:8 offset1:10\n"
      "v_mfma_f32_16x16x32_bf16 v[104:107], a[76:79], a[192:195], v[104:107]\n"
      "ds_read_b128 a[208:211], v0 offset:20480\n"
      "s_add_u32 s59, s62, 5704704\n"
      "ds_read2st64_b32 v[34:35], v6 offset0:12 offset1:14\n"
      "v_add_u32 v12, 128, v8\n"
      "v_add_u32 v13, 128, v9\n"
      "v_add_u32 v14, 128, v10\n"
      "v_mfma_f32_16x16x32_bf16 v[108:111], a[80:83], a[192:195], v[108:111]\n"
      "ds_read2st64_b32 v[36:37], v6 offset0:16 offset1:18\n"
      "v_mfma_f32_16x16x32_bf16 v[100:103], a[84:87], a[196:199], v[100:103]\n"
      "ds_read_b128 a[212:215], v0 offset:16448\n"
      "v_mfma_f32_16x16x32_bf16 v[104:107], a[88:91], a[196:199], v[104:107]\n"
      "ds_read2st64_b32 v[38:39], v6 offset0:20 offset1:22\n"
      "v_mfma_f32_16x16x32_bf16 v[108:111], a[92:95], a[196:199], v[108:111]\n"
      "s_waitcnt lgkmcnt(6)\n"
      "v_sub_f32 v28, v28, v40\n"
      "v_mfma_f32_16x16x32_bf16 v[100:103], a[96:99], a[200:203], v[100:103]\n"
      "ds_read_b128 a[192:195], v0 offset:20544\n"
      "v_sub_f32 v29, v29, v41\n"
      "v_mfma_f32_16x16x32_bf16 v[104:107], a[100:103], a[200:203], v[104:107]\n"
      "ds_read2st64_b32 a[232:233], v8 offset0:4 offset1:6\n"
      "v_sub_f32 v30, v30, v42\n"
      "v_mfma_f32_16x16x32_bf16 v[108:111], a[104:107], a[200:203], v[108:111]\n"
      "ds_read2st64_b32 a[234:235], v12 offset0:4 offset1:6\n"
      "v_sub_f32 v31, v31, v43\n"
      "v_mfma_f32_16x16x32_bf16 v[52:55], v[252:255], a[204:207], v[88:91]\n"
      "ds_read_b128 a[196:199], v0 offset:16512\n"
      "v_fmac_f32 v28, s46, v76\n"
      "v_mfma_f32_16x16x32_bf16 v[56:59], v[244:247], a[204:207], v[92:95]\n"
      "v_fmac_f32 v29, s46, v77\n"
      "v_fmac_f32 v30, s46, v78\n"
      "v_mfma_f32_16x16x32_bf16 v[60:63], v[236:239], a[204:207], v[96:99]\n"
      "v_fmac_f32 v31, s46, v79\n"
      "s_waitcnt lgkmcnt(4)\n"
      "v_mfma_f32_16x16x32_bf16 v[64:67], v[248:251], a[208:211], v[88:91]\n"
      "ds_read_b128 a[200:203], v0 offset:20608\n"
      "v_fmac_f32 v40, s45, v28\n"
      "v_mfma_f32_16x16x32_bf16 v[68:71], v[240:243], a[208:211], v[92:95]\n"
      "v_fmac_f32 v41, s45, v29\n"
      "v_fmac_f32 v42, s45, v30\n"
      "v_mfma_f32_16x16x32_bf16 v[72:75], v[232:235], a[208:211], v[96:99]\n"
      "v_fmac_f32 v43, s45, v31\n"
      "v_fma_f32 v28, s47, v28, v100\n"
      "v_mfma_f32_16x16x32_bf16 v[52:55], v[228:231], a[212:215], v[52:55]\n"
      "ds_read_b128 a[204:207], v1 offset:16384\n"
      "v_fma_f32 v29, s47, v29, v101\n"
      "v_mfma_f32_16x16x32_bf16 v[56:59], v[220:223], a[212:215], v[56:59]\n"
      "v_fma_f32 v30, s47, v30, v102\n"
      "v_fma_f32 v31, s47, v31, v103\n"
      "v_mfma_f32_16x16x32_bf16 v[60:63], v[212:215], a[212:215], v[60:63]\n"
      "v_cvt_pk_bf16_f32 v40, v40, v41\n"
      "s_nop 0\n"
      "s_waitcnt lgkmcnt(1)\n"
      "v_mfma_f32_16x16x32_bf16 v[64:67], v[224:227], a[192:195], v[64:67]\n"
      "ds_read_b128 a[208:211], v1 offset:20480\n"
      "ds_write_addtid_b32 v40 offset:12288\n"
      "v_mfma_f32_16x16x32_bf16 v[68:71], v[216:219], a[192:195], v[68:71]\n"
      "v_cvt_pk_bf16_f32 v42, v42, v43\n"
      "v_cvt_pk_bf16_f32 v28, v28, v29\n"
      "v_mfma_f32_16x16x32_bf16 v[72:75], v[208:211], a[192:195], v[72:75]\n"
      "buffer_store_dwordx4 a[232:235], v11, s[8:11], s59 offen offset:0 sc1\n"
      "v_mfma_f32_16x16x32_bf16 v[52:55], v[204:207], a[196:199], v[52:55]\n"
      "ds_read_b128 a[212:215], v1 offset:16448\n"
      "ds_write_addtid_b32 v42 offset:12800\n"
      "v_mfma_f32_16x16x32_bf16 v[56:59], v[196:199], a[196:199], v[56:59]\n"
      "ds_write_addtid_b32 v28 offset:14336\n"
      "ds_read2st64_b32 a[236:237], v9 offset0:4 offset1:6\n"
      "v_mfma_f32_16x16x32_bf16 v[60:63], v[188:191], a[196:199], v[60:63]\n"
      "ds_read2st64_b32 a[238:239], v13 offset0:4 offset1:6\n"
      "v_cvt_pk_bf16_f32 v30, v30, v31\n"
      "v_mfma_f32_16x16x32_bf16 v[64:67], v[200:203], a[200:203], v[64:67]\n"
      "ds_read_b128 a[192:195], v1 offset:20544\n"
      "ds_write_addtid_b32 v30 offset:14848\n"
      "v_mfma_f32_16x16x32_bf16 v[68:71], v[192:195], a[200:203], v[68:71]\n"
      "v_sub_f32 v32, v32, v44\n"
      "v_sub_f32 v33, v33, v45\n"
      "v_mfma_f32_16x16x32_bf16 v[72:75], v[184:187], a[200:203], v[72:75]\n"
      "v_sub_f32 v34, v34, v46\n"
      "s_waitcnt lgkmcnt(6)\n"
      "v_mfma_f32_16x16x32_bf16 v[52:55], v[180:183], a[204:207], v[52:55]\n"
      "ds_read_b128 a[196:199], v1 offset:16512\n"
      "v_sub_f32 v35, v35, v47\n"
      "v_mfma_f32_16x16x32_bf16 v[56:59], v[172:175], a[204:207], v[56:59]\n"
      "v_fmac_f32 v32, s46, v80\n"
      "v_fmac_f32 v33, s46, v81\n"
      "v_mfma_f32_16x16x32_bf16 v[60:63], v[164:167], a[204:207], v[60:63]\n"
      "v_fmac_f32 v34, s46, v82\n"
      "v_fmac_f32 v35, s46, v83\n"
      "v_mfma_f32_16x16x32_bf16 v[64:67], v[176:179], a[208:211], v[64:67]\n"
      "ds_read_b128 a[200:203], v1 offset:20608\n"
      "v_fmac_f32 v44, s45, v32\n"
      "v_mfma_f32_16x16x32_bf16 v[68:71], v[168:171], a[208:211], v[68:71]\n"
      "v_fmac_f32 v45, s45, v33\n"
      "v_fmac_f32 v46, s45, v34\n"
      "v_mfma_f32_16x16x32_bf16 v[72:75], v[160:163], a[208:211], v[72:75]\n"
      "v_fmac_f32_e64 v47, s45, v35\n"
      "v_fma_f32 v32, s47, v32, v104\n"
      "v_mfma_f32_16x16x32_bf16 v[52:55], v[156:159], a[212:215], v[52:55]\n"
      "ds_read_b128 a[204:207], v2 offset:16384\n"
      "v_fma_f32 v33, s47, v33, v105\n"
      "v_mfma_f32_16x16x32_bf16 v[56:59], v[148:151], a[212:215], v[56:59]\n"
      "v_fma_f32 v34, s47, v34, v106\n"
      "v_fma_f32 v35, s47, v35, v107\n"
      "v_mfma_f32_16x16x32_bf16 v[60:63], v[140:143], a[212:215], v[60:63]\n"
      "v_cvt_pk_bf16_f32 v44, v44, v45\n"
      "s_nop 0\n"
      "s_waitcnt lgkmcnt(1)\n"
      "v_mfma_f32_16x16x32_bf16 v[64:67], v[152:155], a[192:195], v[64:67]\n"
      "buffer_store_dwordx4 a[236:239], v11, s[8:11], s59 offen offset:128 sc1\n"
      "ds_read_b128 a[208:211], v2 offset:20480\n"
      "v_mfma_f32_16x16x32_bf16 v[68:71], v[144:147], a[192:195], v[68:71]\n"
      "ds_write_addtid_b32 v44 offset:20484\n"
      "v_cvt_pk_bf16_f32 v46, v46, v47\n"
      "v_mfma_f32_16x16x32_bf16 v[72:75], v[136:139], a[192:195], v[72:75]\n"
      "ds_write_addtid_b32 v46 offset:20996\n"
      "ds_read2st64_b32 a[240:241], v10 offset0:4 offset1:6\n"
      "v_mfma_f32_16x16x32_bf16 v[52:55], v[132:135], a[196:199], v[52:55]\n"
      "ds_read_b128 a[212:215], v2 offset:16448\n"
      "ds_read2st64_b32 a[242:243], v14 offset0:4 offset1:6\n"
      "v_mfma_f32_16x16x32_bf16 v[56:59], v[124:127], a[196:199], v[56:59]\n"
      "v_cvt_pk_bf16_f32 v32, v32, v33\n"
      "v_cvt_pk_bf16_f32 v34, v34, v35\n"
      "v_mfma_f32_16x16x32_bf16 v[60:63], a[184:187], a[196:199], v[60:63]\n"
      "ds_write_addtid_b32 v32 offset:22532\n"
      "ds_write_addtid_b32 v34 offset:23044\n"
      "v_mfma_f32_16x16x32_bf16 v[64:67], v[128:131], a[200:203], v[64:67]\n"
      "ds_read_b128 a[192:195], v2 offset:20544\n"
      "v_mfma_f32_16x16x32_bf16 v[68:71], a[188:191], a[200:203], v[68:71]\n"
      "v_sub_f32 v36, v36, v48\n"
      "v_sub_f32 v37, v37, v49\n"
      "v_mfma_f32_16x16x32_bf16 v[72:75], a[180:183], a[200:203], v[72:75]\n"
      "v_sub_f32 v38, v38, v50\n"
      "s_waitcnt lgkmcnt(5)\n"
      "v_mfma_f32_16x16x32_bf16 v[52:55], a[176:179], a[204:207], v[52:55]\n"
      "ds_read_b128 a[196:199], v2 offset:16512\n"
      "v_sub_f32 v39, v39, v51\n"
      "v_mfma_f32_16x16x32_bf16 v[56:59], a[168:171], a[204:207], v[56:59]\n"
      "v_fmac_f32 v36, s46, v84\n"
      "v_fmac_f32_e64 v37, s46, v85\n"
      "v_mfma_f32_16x16x32_bf16 v[60:63], a[160:163], a[204:207], v[60:63]\n"
      "v_fmac_f32 v38, s46, v86\n"
      "v_fmac_f32 v39, s46, v87\n"
      "v_mfma_f32_16x16x32_bf16 v[64:67], a[172:175], a[208:211], v[64:67]\n"
      "ds_read_b128 a[200:203], v2 offset:20608\n"
      "v_fmac_f32 v48, s45, v36\n"
      "v_mfma_f32_16x16x32_bf16 v[68:71], a[164:167], a[208:211], v[68:71]\n"
      "v_fmac_f32 v49, s45, v37\n"
      "v_fmac_f32_e64 v50, s45, v38\n"
      "v_mfma_f32_16x16x32_bf16 v[72:75], a[156:159], a[208:211], v[72:75]\n"
      "v_fmac_f32 v51, s45, v39\n"
      "s_waitcnt lgkmcnt(1)\n"
      "v_mfma_f32_16x16x32_bf16 v[52:55], a[152:155], a[212:215], v[52:55]\n"
      "ds_read_b128 a[204:207], v3 offset:16896\n"
      "v_fma_f32 v36, s47, v36, v108\n"
      "v_mfma_f32_16x16x32_bf16 v[56:59], a[144:147], a[212:215], v[56:59]\n"
      "buffer_store_dwordx4 a[240:243], v11, s[8:11], s59 offen offset:256 sc1\n"
      "v_mfma_f32_16x16x32_bf16 v[60:63], a[136:139], a[212:215], v[60:63]\n"
      "v_fma_f32 v37, s47, v37, v109\n"
      "v_fma_f32 v38, s47, v38, v110\n"
      "v_mfma_f32_16x16x32_bf16 v[64:67], a[148:151], a[192:195], v[64:67]\n"
      "ds_read_b128 a[208:211], v3 offset:16960\n"
      "v_fma_f32 v39, s47, v39, v111\n"
      "v_mfma_f32_16x16x32_bf16 v[68:71], a[140:143], a[192:195], v[68:71]\n"
      "v_cvt_pk_bf16_f32 v48, v48, v49\n"
      "v_cvt_pk_bf16_f32 v50, v50, v51\n"
      "v_mfma_f32_16x16x32_bf16 v[72:75], a[132:135], a[192:195], v[72:75]\n"
      "ds_write_addtid_b32 v48 offset:16384\n"
      "ds_write_addtid_b32 v50 offset:16896\n"
      "v_mfma_f32_16x16x32_bf16 v[52:55], a[128:131], a[196:199], v[52:55]\n"
      "ds_read_b128 a[212:215], v3 offset:17024\n"
      "v_cvt_pk_bf16_f32 v36, v36, v37\n"
      "v_mfma_f32_16x16x32_bf16 v[56:59], a[120:123], a[196:199], v[56:59]\n"
      "ds_write_addtid_b32 v36 offset:18432\n"
      "v_cvt_pk_bf16_f32 v38, v38, v39\n"
      "v_mfma_f32_16x16x32_bf16 v[60:63], a[112:115], a[196:199], v[60:63]\n"
      "ds_write_addtid_b32 v38 offset:18944\n"
      "s_nop 0\n"
      "s_waitcnt lgkmcnt(5)\n"
      "v_mfma_f32_16x16x32_bf16 v[64:67], a[124:127], a[200:203], v[64:67]\n"
      "ds_read_b128 a[192:195], v4 offset:16896\n"
      "v_mfma_f32_16x16x32_bf16 v[68:71], a[116:119], a[200:203], v[68:71]\n"
      "v_mfma_f32_16x16x32_bf16 v[72:75], a[108:111], a[200:203], v[72:75]\n"
      "v_mfma_f32_16x16x32_bf16 v[112:115], a[0:3], a[204:207], 0\n"
      "ds_read_b128 a[196:199], v4 offset:16960\n"
      "v_mfma_f32_16x16x32_bf16 v[116:119], a[4:7], a[204:207], 0\n"
      "v_mfma_f32_16x16x32_bf16 v[120:123], a[8:11], a[204:207], 0\n"
      "v_mfma_f32_16x16x32_bf16 v[112:115], a[12:15], a[208:211], v[112:115]\n"
      "ds_read_b128 a[200:203], v4 offset:17024\n"
      "v_sub_f32 v52, v64, v52\n"
      "v_sub_f32 v53, v65, v53\n"
      "v_sub_f32 v54, v66, v54\n"
      "v_sub_f32 v55, v67, v55\n"
      "v_sub_f32_e64 v56, v68, v56\n"
      "v_mfma_f32_16x16x32_bf16 v[116:119], a[16:19], a[208:211], v[116:119]\n"
      "ds_write_addtid_b32 v52 offset:6144\n"
      "ds_write_addtid_b32 v53 offset:6656\n"
      "v_sub_f32 v57, v69, v57\n"
      "v_sub_f32_e64 v58, v70, v58\n"
      "v_mfma_f32_16x16x32_bf16 v[120:123], a[20:23], a[208:211], v[120:123]\n"
      "ds_write_addtid_b32 v54 offset:7168\n"
      "ds_write_addtid_b32 v55 offset:7680\n"
      "s_waitcnt lgkmcnt(5)\n"
      "v_mfma_f32_16x16x32_bf16 v[112:115], a[24:27], a[212:215], v[112:115]\n"
      "ds_read_b128 a[204:207], v5 offset:16896\n"
      "ds_write_addtid_b32 v56 offset:8192\n"
      "v_mfma_f32_16x16x32_bf16 v[116:119], a[28:31], a[212:215], v[116:119]\n"
      "ds_write_addtid_b32 v57 offset:8704\n"
      "ds_write_addtid_b32 v58 offset:9216\n"
      "v_mfma_f32_16x16x32_bf16 v[120:123], a[32:35], a[212:215], v[120:123]\n"
      "v_sub_f32 v59, v71, v59\n"
      "v_sub_f32 v60, v72, v60\n"
      "v_mfma_f32_16x16x32_bf16 v[112:115], a[36:39], a[192:195], v[112:115]\n"
      "ds_read_b128 a[208:211], v5 offset:16960\n"
      "ds_write_addtid_b32 v59 offset:9728\n"
      "v_mfma_f32_16x16x32_bf16 v[116:119], a[40:43], a[192:195], v[116:119]\n"
      "ds_write_addtid_b32 v60 offset:10240\n"
      "v_sub_f32 v61, v73, v61\n"
      "v_mfma_f32_16x16x32_bf16 v[120:123], a[44:47], a[192:195], v[120:123]\n"
      "ds_write_addtid_b32 v61 offset:10752\n"
      "v_sub_f32 v62, v74, v62\n"
      "v_sub_f32 v63, v75, v63\n"
      "v_mfma_f32_16x16x32_bf16 v[112:115], a[48:51], a[196:199], v[112:115]\n"
      "ds_read_b128 a[212:215], v5 offset:17024\n"
      "ds_write_addtid_b32 v62 offset:11264\n"
      "v_mfma_f32_16x16x32_bf16 v[116:119], a[52:55], a[196:199], v[116:119]\n"
      "ds_write_addtid_b32 v63 offset:11776\n"
      "v_mfma_f32_16x16x32_bf16 v[120:123], a[56:59], a[196:199], v[120:123]\n"
      "s_waitcnt lgkmcnt(4)\n"
      "v_mfma_f32_16x16x32_bf16 v[112:115], a[60:63], a[200:203], v[112:115]\n"
      "v_fma_f32 v40, 0.5, v52, -v64\n"
      "v_fma_f32 v41, 0.5, v53, -v65\n"
      "v_fma_f32 v42, 0.5, v54, -v66\n"
      "v_fma_f32 v43, 0.5, v55, -v67\n"
      "v_mfma_f32_16x16x32_bf16 v[116:119], a[64:67], a[200:203], v[116:119]\n"
      "v_fma_f32 v44, 0.5, v56, -v68\n"
      "v_fma_f32 v45, 0.5, v57, -v69\n"
      "v_fma_f32 v46, 0.5, v58, -v70\n"
      "v_fma_f32 v47, 0.5, v59, -v71\n"
      "v_mfma_f32_16x16x32_bf16 v[120:123], a[68:71], a[200:203], v[120:123]\n"
      "v_fma_f32 v48, 0.5, v60, -v72\n"
      "v_fma_f32 v49, 0.5, v61, -v73\n"
      "v_fma_f32 v50, 0.5, v62, -v74\n"
      "v_fma_f32 v51, 0.5, v63, -v75\n"
      "s_waitcnt lgkmcnt(0)\n"
      "s_barrier\n"
      "v_mfma_f32_16x16x32_bf16 v[112:115], a[72:75], a[204:207], v[112:115]\n"
      "ds_read2st64_b32 v[28:29], v6 offset0:24 offset1:26\n"
      "ds_read2st64_b32 a[232:233], v8 offset0:0 offset1:2\n"
      "s_add_u32 s59, s62, 6488064\n"
      "v_mfma_f32_16x16x32_bf16 v[116:119], a[76:79], a[204:207], v[116:119]\n"
      "ds_read2st64_b32 v[30:31], v6 offset0:28 offset1:30\n"
      "ds_read2st64_b32 a[236:237], v9 offset0:0 offset1:2\n"
      "v_mfma_f32_16x16x32_bf16 v[120:123], a[80:83], a[204:207], v[120:123]\n"
      "ds_read2st64_b32 a[234:235], v12 offset0:0 offset1:2\n"
      "v_mfma_f32_16x16x32_bf16 v[112:115], a[84:87], a[208:211], v[112:115]\n"
      "ds_read2st64_b32 v[32:33], v6 offset0:32 offset1:34\n"
      "v_mfma_f32_16x16x32_bf16 v[116:119], a[88:91], a[208:211], v[116:119]\n"
      "ds_read2st64_b32 v[34:35], v6 offset0:36 offset1:38\n"
      "ds_read2st64_b32 v[36:37], v6 offset0:40 offset1:42\n"
      "ds_read2st64_b32 v[38:39], v6 offset0:44 offset1:46\n"
      "ds_read2st64_b32 a[238:239], v13 offset0:0 offset1:2\n"
      "v_mfma_f32_16x16x32_bf16 v[120:123], a[92:95], a[208:211], v[120:123]\n"
      "v_mfma_f32_16x16x32_bf16 v[112:115], a[96:99], a[212:215], v[112:115]\n"
      "s_waitcnt lgkmcnt(5)\n"
      "buffer_store_dwordx4 a[232:235], v11, s[8:11], s59 offen offset:0 sc1\n"
      "ds_read2st64_b32 a[240:241], v10 offset0:0 offset1:2\n"
      "ds_read2st64_b32 a[242:243], v14 offset0:0 offset1:2\n"
      "v_mfma_f32_16x16x32_bf16 v[116:119], a[100:103], a[212:215], v[116:119]\n"
      "v_sub_f32 v28, v28, v40\n"
      "v_sub_f32 v29, v29, v41\n"
      "v_mfma_f32_16x16x32_bf16 v[120:123], a[104:107], a[212:215], v[120:123]\n"
      "s_waitcnt lgkmcnt(2)\n"
      "buffer_store_dwordx4 a[236:239], v11, s[8:11], s59 offen offset:128 sc1\n"
      "v_sub_f32 v30, v30, v42\n"
      "v_sub_f32 v31, v31, v43\n"
      "v_fmac_f32 v28, s46, v88\n"
      "v_fmac_f32 v29, s46, v89\n"
      "v_fmac_f32 v30, s46, v90\n"
      "v_fmac_f32_e64 v31, s46, v91\n"
      "v_pk_fma_f32 v[40:41], v[28:29], s[44:45], v[40:41] op_sel:[0,1,0] op_sel_hi:[1,1,1]\n"
      "s_waitcnt lgkmcnt(0)\n"
      "buffer_store_dwordx4 a[240:243], v11, s[8:11], s59 offen offset:256 sc1\n"
      "v_pk_fma_f32 v[42:43], v[30:31], s[44:45], v[42:43] op_sel:[0,1,0] op_sel_hi:[1,1,1]\n"
      "v_pk_fma_f32 v[28:29], v[28:29], s[46:47], v[112:113] op_sel:[0,1,0] op_sel_hi:[1,1,1]\n"
      "v_pk_fma_f32 v[30:31], v[30:31], s[46:47], v[114:115] op_sel:[0,1,0] op_sel_hi:[1,1,1]\n"
      "v_cvt_pk_bf16_f32 v40, v40, v41\n"
      "v_cvt_pk_bf16_f32 v42, v42, v43\n"
      "ds_write_addtid_b32 v40 offset:13312\n"
      "ds_write_addtid_b32 v42 offset:13824\n"
      "v_cvt_pk_bf16_f32 v28, v28, v29\n"
      "v_cvt_pk_bf16_f32 v30, v30, v31\n"
      "v_pk_add_f32 v[32:33], v[32:33], v[44:45] neg_lo:[0,1] neg_hi:[0,1]\n"
      "ds_write_addtid_b32 v28 offset:15360\n"
      "ds_write_addtid_b32 v30 offset:15872\n"
      "v_pk_add_f32 v[34:35], v[34:35], v[46:47] neg_lo:[0,1] neg_hi:[0,1]\n"
      "v_pk_fma_f32 v[32:33], v[92:93], s[46:47], v[32:33] op_sel:[0,0,0] op_sel_hi:[1,0,1]\n"
      "v_pk_fma_f32 v[34:35], v[94:95], s[46:47], v[34:35] op_sel:[0,0,0] op_sel_hi:[1,0,1]\n"
      "v_pk_fma_f32 v[44:45], v[32:33], s[44:45], v[44:45] op_sel:[0,1,0] op_sel_hi:[1,1,1]\n"
      "v_pk_fma_f32 v[46:47], v[34:35], s[44:45], v[46:47] op_sel:[0,1,0] op_sel_hi:[1,1,1]\n"
      "v_pk_fma_f32 v[32:33], v[32:33], s[46:47], v[116:117] op_sel:[0,1,0] op_sel_hi:[1,1,1]\n"
      "v_pk_fma_f32 v[34:35], v[34:35], s[46:47], v[118:119] op_sel:[0,1,0] op_sel_hi:[1,1,1]\n"
      "v_cvt_pk_bf16_f32 v44, v44, v45\n"
      "v_cvt_pk_bf16_f32 v46, v46, v47\n"
      "v_cvt_pk_bf16_f32 v32, v32, v33\n"
      "v_cvt_pk_bf16_f32 v34, v34, v35\n"
      "ds_write_addtid_b32 v44 offset:21508\n"
      "ds_write_addtid_b32 v46 offset:22020\n"
      "ds_write_addtid_b32 v32 offset:23556\n"
      "v_pk_add_f32 v[36:37], v[36:37], v[48:49] neg_lo:[0,1] neg_hi:[0,1]\n"
      "v_sub_f32 v38, v38, v50\n"
      "ds_write_addtid_b32 v34 offset:24068\n"
      "v_sub_f32 v39, v39, v51\n"
      "v_pk_fma_f32 v[36:37], v[96:97], s[46:47], v[36:37] op_sel:[0,0,0] op_sel_hi:[1,0,1]\n"
      "v_pk_fma_f32 v[38:39], v[98:99], s[46:47], v[38:39] op_sel:[0,0,0] op_sel_hi:[1,0,1]\n"
      "v_pk_fma_f32 v[48:49], v[36:37], s[44:45], v[48:49] op_sel:[0,1,0] op_sel_hi:[1,1,1]\n"
      "v_pk_fma_f32 v[50:51], v[38:39], s[44:45], v[50:51] op_sel:[0,1,0] op_sel_hi:[1,1,1]\n"
      "v_pk_fma_f32 v[36:37], v[36:37], s[46:47], v[120:121] op_sel:[0,1,0] op_sel_hi:[1,1,1]\n"
      "v_pk_fma_f32 v[38:39], v[38:39], s[46:47], v[122:123] op_sel:[0,1,0] op_sel_hi:[1,1,1]\n"
      "v_cvt_pk_bf16_f32 v48, v48, v49\n"
      "v_cvt_pk_bf16_f32 v50, v50, v51\n"
      "v_cvt_pk_bf16_f32 v36, v36, v37\n"
      "v_cvt_pk_bf16_f32 v38, v38, v39\n"
      "ds_write_addtid_b32 v48 offset:17408\n"
      "ds_write_addtid_b32 v50 offset:17920\n"
      "ds_write_addtid_b32 v36 offset:19456\n"
      "ds_write_addtid_b32 v38 offset:19968\n"
      "s_waitcnt lgkmcnt(0)\n"
      "s_barrier\n"
      "ds_read2st64_b32 a[232:233], v8 offset0:4 offset1:6\n"
      "ds_read2st64_b32 a[234:235], v12 offset0:4 offset1:6\n"
      "ds_read2st64_b32 a[236:237], v9 offset0:4 offset1:6\n"
      "ds_read2st64_b32 a[238:239], v13 offset0:4 offset1:6\n"
      "ds_read2st64_b32 a[240:241], v10 offset0:4 offset1:6\n"
      "ds_read2st64_b32 a[242:243], v14 offset0:4 offset1:6\n"
      "s_add_u32 s59, s59, 3072\n"
      "s_waitcnt lgkmcnt(4)\n"
      "buffer_store_dwordx4 a[232:235], v11, s[8:11], s59 offen offset:0 sc1\n"
      "s_waitcnt lgkmcnt(2)\n"
      "buffer_store_dwordx4 a[236:239], v11, s[8:11], s59 offen offset:128 sc1\n"
      "s_waitcnt lgkmcnt(0)\n"
      "buffer_store_dwordx4 a[240:243], v11, s[8:11], s59 offen offset:256 sc1\n"
      "LSend_%=:\n"
      "Lfin_%=:\n"
      "s_waitcnt vmcnt(0) lgkmcnt(0)\n"
      "s_endpgm\n"
      ".rept 512\n"
      "s_nop 0\n"
      ".endr\n"
      ".Lwg_end:\n"
      : "+{v0}"(tid)
      : "{s[0:1]}"(__builtin_amdgcn_kernarg_segment_ptr()), "{s2}"(blockIdx.x)
      : "v1", "v2", "v3", "v4", "v5", "v6", "v7", "v8", "v9", "v10", "v11", "v12", "v13", "v14", "v15", "v16", "v17", "v18", "v19", "v20", "v21", "v22", "v23", "v24", "v25", "v26", "v27", "v28", "v29", "v30", "v31", "v32", "v33", "v34", "v35", "v36", "v37", "v38", "v39", "v40", "v41", "v42", "v43", "v44", "v45", "v46", "v47", "v48", "v49", "v50", "v51", "v52", "v53", "v54", "v55", "v56", "v57", "v58", "v59", "v60", "v61", "v62", "v63", "v64", "v65", "v66", "v67", "v68", "v69", "v70", "v71", "v72", "v73", "v74", "v75", "v76", "v77", "v78", "v79", "v80", "v81", "v82", "v83", "v84", "v85", "v86", "v87", "v88", "v89", "v90", "v91", "v92", "v93", "v94", "v95", "v96", "v97", "v98", "v99", "v100", "v101", "v102", "v103", "v104", "v105", "v106", "v107", "v108", "v109", "v110", "v111", "v112", "v113", "v114", "v115", "v116", "v117", "v118", "v119", "v120", "v121", "v122", "v123", "v124", "v125", "v126", "v127", "v128", "v129", "v130", "v131", "v132", "v133", "v134", "v135", "v136", "v137", "v138", "v139", "v140", "v141", "v142", "v143", "v144", "v145", "v146", "v147", "v148", "v149", "v150", "v151", "v152", "v153", "v154", "v155", "v156", "v157", "v158", "v159", "v160", "v161", "v162", "v163", "v164", "v165", "v166", "v167", "v168", "v169", "v170", "v171", "v172", "v173", "v174", "v175", "v176", "v177", "v178", "v179", "v180", "v181", "v182", "v183", "v184", "v185", "v186", "v187", "v188", "v189", "v190", "v191", "v192", "v193", "v194", "v195", "v196", "v197", "v198", "v199", "v200", "v201", "v202", "v203", "v204", "v205", "v206", "v207", "v208", "v209", "v210", "v211", "v212", "v213", "v214", "v215", "v216", "v217", "v218", "v219", "v220", "v221", "v222", "v223", "v224", "v225", "v226", "v227", "v228", "v229", "v230", "v231", "v232", "v233", "v234", "v235", "v236", "v237", "v238", "v239", "v240", "v241", "v242", "v243", "v244", "v245", "v246", "v247", "v248", "v249", "v250", "v251", "v252", "v253", "v254", "v255", "a0", "a1", "a2", "a3", "a4", "a5", "a6", "a7", "a8", "a9", "a10", "a11", "a12", "a13", "a14", "a15", "a16", "a17", "a18", "a19", "a20", "a21", "a22", "a23", "a24", "a25", "a26", "a27", "a28", "a29", "a30", "a31", "a32", "a33", "a34", "a35", "a36", "a37", "a38", "a39", "a40", "a41", "a42", "a43", "a44", "a45", "a46", "a47", "a48", "a49", "a50", "a51", "a52", "a53", "a54", "a55", "a56", "a57", "a58", "a59", "a60", "a61", "a62", "a63", "a64", "a65", "a66", "a67", "a68", "a69", "a70", "a71", "a72", "a73", "a74", "a75", "a76", "a77", "a78", "a79", "a80", "a81", "a82", "a83", "a84", "a85", "a86", "a87", "a88", "a89", "a90", "a91", "a92", "a93", "a94", "a95", "a96", "a97", "a98", "a99", "a100", "a101", "a102", "a103", "a104", "a105", "a106", "a107", "a108", "a109", "a110", "a111", "a112", "a113", "a114", "a115", "a116", "a117", "a118", "a119", "a120", "a121", "a122", "a123", "a124", "a125", "a126", "a127", "a128", "a129", "a130", "a131", "a132", "a133", "a134", "a135", "a136", "a137", "a138", "a139", "a140", "a141", "a142", "a143", "a144", "a145", "a146", "a147", "a148", "a149", "a150", "a151", "a152", "a153", "a154", "a155", "a156", "a157", "a158", "a159", "a160", "a161", "a162", "a163", "a164", "a165", "a166", "a167", "a168", "a169", "a170", "a171", "a172", "a173", "a174", "a175", "a176", "a177", "a178", "a179", "a180", "a181", "a182", "a183", "a184", "a185", "a186", "a187", "a188", "a189", "a190", "a191", "a192", "a193", "a194", "a195", "a196", "a197", "a198", "a199", "a200", "a201", "a202", "a203", "a204", "a205", "a206", "a207", "a208", "a209", "a210", "a211", "a212", "a213", "a214", "a215", "a216", "a217", "a218", "a219", "a220", "a221", "a222", "a223", "a224", "a225", "a226", "a227", "a228", "a229", "a230", "a231", "a232", "a233", "a234", "a235", "a236", "a237", "a238", "a239", "a240", "a241", "a242", "a243", "a244", "a245", "a246", "a247", "a248", "a249", "a250", "a251", "a252", "a253", "a254", "a255", "s3", "s4", "s5", "s6", "s7", "s8", "s9", "s10", "s11", "s12", "s13", "s14", "s15", "s16", "s17", "s18", "s19", "s20", "s21", "s22", "s23", "s24", "s25", "s26", "s27", "s28", "s29", "s30", "s31", "s33", "s34", "s35", "s36", "s37", "s38", "s39", "s40", "s41", "s42", "s43", "s44", "s45", "s46", "s47", "s48", "s49", "s50", "s51", "s52", "s53", "s54", "s55", "s56", "s57", "s58", "s59", "s60", "s61", "s62", "s63", "s64", "s65", "s66", "s67", "s68", "s69", "s70", "s71", "s72", "s73", "s74", "s75", "s76", "s77", "s78", "s79", "s80", "s81", "s82", "s83", "s84", "s85", "s86", "s87", "s88", "s89", "s90", "s91", "s92", "s93", "s94", "s95", "s96", "s97", "s98", "s99", "scc", "vcc", "memory");
  asm volatile("" :: "v"((unsigned)(uintptr_t)smem) : "memory");
}

extern "C" void kernel_launch(void* const* d_in, const int* in_sizes, int n_in, void* d_out,
                              int out_size, void* d_ws, size_t ws_size, hipStream_t stream) {
  conv_kernel<<<256, 256, 0, stream>>>((const char*)d_in[0], (const char*)d_in[1], (char*)d_out, (char*)d_ws);
}
```

```cpp
#include <hip/hip_runtime.h>
#include <hip/hip_bf16.h>
#include <stdint.h>

__global__ void __launch_bounds__(256, 1)
conv_kernel(const char* __restrict__ x, const char* __restrict__ w, char* __restrict__ out, char* __restrict__ dbg) {
  __shared__ __attribute__((aligned(1024))) char smem[163840];
  unsigned tid = threadIdx.x;
  asm volatile(
      "s_load_dwordx2 s[12:13], s[0:1], 8\n"
      "s_load_dwordx2 s[4:5], s[0:1], 0\n"
      "s_load_dwordx2 s[8:9], s[0:1], 16\n"
      "v_readfirstlane_b32 s16, v0\n"
      "s_getpc_b64 s[26:27]\n"
      ".Lwg_pc:\n"
      "v_and_b32 v37, 3, v0\n"
      "s_lshr_b32 s23, s2, 3\n"
      "s_lshl_b32 s28, s16, 3\n"
      "v_lshlrev_b32 v40, 7, v37\n"
      "s_lshl_b32 s29, s23, 11\n"
      "s_mov_b32 m0, 150016\n"
      "s_add_u32 s28, s28, s29\n"
      "v_and_b32 v36, 63, v0\n"
      "v_add_u32 v40, s28, v40\n"
      "s_lshr_b32 s16, s16, 6\n"
      "v_min_u32 v40, .Lwg_end-.Lwg_pc-4, v40\n"
      "s_and_b32 s18, s16, 1\n"
      "global_load_lds_dword v40, s[26:27]\n"
      "v_lshlrev_b32 v92, 4, v36\n"
      "s_lshr_b32 s17, s16, 1\n"
      "s_and_b32 s28, s2, 7\n"
      "s_mul_i32 s19, s28, 6291456\n"
      "s_and_b32 s28, s23, 1\n"
      "s_mul_i32 s20, s28, 98304\n"
      "s_lshr_b32 s28, s23, 1\n"
      "s_mul_i32 s29, s28, 6144\n"
      "s_add_u32 s21, s20, s29\n"
      "s_add_u32 s6, s19, 6492160\n"
      "s_min_u32 s6, s6, 50335744\n"
      "s_mov_b32 s7, 0x20000\n"
      "s_mov_b32 s10, 50331648\n"
      "s_mov_b32 s11, 0x20000\n"
      "s_mov_b32 s14, 165888\n"
      "s_mov_b32 s15, 0x20000\n"
      "s_mul_i32 s63, s18, 3072\n"
      "s_add_u32 s54, s19, s20\n"
      "s_sub_u32 s54, s54, 196608\n"
      "s_lshl_b32 s64, s18, 8\n"
      "s_mul_i32 s28, s17, 24832\n"
      "s_add_u32 s64, s64, s28\n"
      "s_mov_b32 s55, 0\n"
      "v_bitop3_b32 v93, v36, v92, 32 bitop3:0x6c\n"
      "v_xor_b32 v94, 32, v92\n"
      "v_bfe_u32 v38, v36, 2, 2\n"
      "v_lshrrev_b32 v39, 4, v36\n"
      "s_cmp_eq_u32 s17, 1\n"
      "s_cbranch_scc1 LBD_%=\n"
      "v_lshlrev_b32 v40, 3, v92\n"
      "s_mul_i32 s53, s18, 2688\n"
      "s_mul_i32 s34, s23, 5184\n"
      "v_min_u32 v40, 2560, v40\n"
      "s_add_u32 s53, s53, s34\n"
      "v_lshlrev_b32 v43, 4, v37\n"
      "s_mul_i32 s26, s17, 0x8080\n"
      "s_sub_u32 s26, 0xbf80, s26\n"
      "s_mov_b32 s27, 0\n"
      "s_lshl_b32 s34, s17, 7\n"
      "s_mov_b32 s29, 0\n"
      "s_sub_u32 s28, s26, s34\n"
      "s_mov_b32 s31, 0\n"
      "s_sub_u32 s30, s28, s34\n"
      "v_lshlrev_b64 v[44:45], v43, s[26:27]\n"
      "v_lshlrev_b64 v[46:47], v43, s[28:29]\n"
      "s_xor_b32 s28, s28, 0x8000\n"
      "v_lshlrev_b64 v[48:49], v43, s[30:31]\n"
      "v_lshlrev_b64 v[50:51], v43, s[28:29]\n"
      "v_accvgpr_write_b32 a244, v44\n"
      "v_accvgpr_write_b32 a245, v45\n"
      "v_accvgpr_write_b32 a246, v46\n"
      "v_accvgpr_write_b32 a247, v47\n"
      "v_accvgpr_write_b32 a248, v48\n"
      "v_accvgpr_write_b32 a249, v49\n"
      "v_accvgpr_write_b32 a250, v50\n"
      "v_accvgpr_write_b32 a251, v51\n"
      "s_waitcnt lgkmcnt(0)\n"
      "s_and_b32 s13, s13, 0xffff\n"
      "buffer_load_dword v40, s[12:15], s53 offen lds\n"
      "s_add_u32 m0, s63, 30976\n"
      "s_add_u32 s52, s63, 55296\n"
      "buffer_load_dwordx4 v92, s[12:15], s52 offen offset:0 lds\n"
      "buffer_load_dwordx4 v93, s[12:15], s52 offen offset:1024 lds\n"
      "buffer_load_dwordx4 v94, s[12:15], s52 offen offset:2048 lds\n"
      "s_add_u32 m0, s63, 0\n"
      "s_add_u32 s52, s63, 110592\n"
      "buffer_load_dwordx4 v92, s[12:15], s52 offen offset:0 lds\n"
      "buffer_load_dwordx4 v93, s[12:15], s52 offen offset:1024 lds\n"
      "buffer_load_dwordx4 v94, s[12:15], s52 offen offset:2048 lds\n"
      "s_branch LS_%=\n"
      "LBD_%=:\n"
      "s_sub_u32 s26, s21, s20\n"
      "s_lshl_b32 s37, s18, 11\n"
      "s_add_u32 s26, s26, s37\n"
      "s_add_u32 s27, s26, 3712\n"
      "v_add_u32 v44, s27, v92\n"
      "s_cmp_eq_u32 s18, 1\n"
      "s_cselect_b32 s28, 16, 48\n"
      "s_cselect_b32 s29, 0, 32\n"
      "s_cselect_b32 s27, 48, 64\n"
      "v_cmp_le_u32 vcc, s28, v36\n"
      "v_cmp_gt_u32 s[30:31], s29, v36\n"
      "v_xor_b32 v45, 32, v44\n"
      "s_cmp_eq_u32 s26, 0\n"
      "s_cselect_b32 s28, 24, 0\n"
      "v_cndmask_b32 v10, v44, v45, vcc\n"
      "v_cndmask_b32 v46, v44, v45, s[30:31]\n"
      "v_cmp_le_u32 vcc, s28, v36\n"
      "v_cmp_gt_u32 s[30:31], s27, v36\n"
      "s_cmp_eq_u32 s26, 94208\n"
      "s_cselect_b32 s27, 24, 64\n"
      "v_cmp_gt_u32 s[34:35], s27, v36\n"
      "v_subrev_u32 v45, 256, v44\n"
      "v_cndmask_b32 v8, 2.0, v10, vcc\n"
      "v_cndmask_b32 v9, v45, v46, s[30:31]\n"
      "v_cndmask_b32 v11, 2.0, v9, s[34:35]\n"
      "s_add_u32 s34, s19, s21\n"
      "s_cmp_eq_u32 s19, 0\n"
      "s_cselect_b32 s36, 0x80000000, s54\n"
      "s_waitcnt lgkmcnt(0)\n"
      "s_load_dword s44, s[4:5], s34\n"
      "s_and_b32 s13, s13, 0xffff\n"
      "s_add_u32 m0, s63, 24832\n"
      "s_add_u32 s52, s63, 0\n"
      "buffer_load_dwordx4 v92, s[12:15], s52 offen offset:0 lds\n"
      "buffer_load_dwordx4 v93, s[12:15], s52 offen offset:1024 lds\n"
      "buffer_load_dwordx4 v94, s[12:15], s52 offen offset:2048 lds\n"
      "s_add_u32 s4, s4, -4096\n"
      "s_addc_u32 s5, s5, -1\n"
      "s_and_b32 s5, s5, 0xffff\n"
      "s_add_u32 s53, s54, 196608\n"
      "s_add_u32 m0, s37, 82432\n"
      "s_nop 0\n"
      "buffer_load_dwordx4 v8, s[4:7], s53 offen offset:0 nt lds\n"
      "buffer_load_dwordx4 v9, s[4:7], s53 offen offset:1024 nt lds\n"
      "s_add_u32 s53, s54, 393216\n"
      "s_add_u32 m0, s37, 86536\n"
      "s_nop 0\n"
      "buffer_load_dwordx4 v8, s[4:7], s53 offen offset:0 nt lds\n"
      "buffer_load_dwordx4 v9, s[4:7], s53 offen offset:1024 nt lds\n"
      "s_add_u32 s53, s54, 589824\n"
      "s_add_u32 m0, s37, 90760\n"
      "s_nop 0\n"
      "buffer_load_dwordx4 v8, s[4:7], s53 offen offset:0 nt lds\n"
      "buffer_load_dwordx4 v9, s[4:7], s53 offen offset:1024 nt lds\n"
      "s_add_u32 s53, s54, 786432\n"
      "s_add_u32 m0, s37, 95104\n"
      "s_nop 0\n"
      "buffer_load_dwordx4 v8, s[4:7], s53 offen offset:0 nt lds\n"
      "buffer_load_dwordx4 v9, s[4:7], s53 offen offset:1024 nt lds\n"
      "s_add_u32 s53, s36, 0\n"
      "s_add_u32 m0, s37, 150272\n"
      "s_nop 0\n"
      "s_nop 0\n"
      "buffer_load_dwordx4 v8, s[4:7], s53 offen offset:0 nt lds\n"
      "buffer_load_dwordx4 v9, s[4:7], s53 offen offset:1024 nt lds\n"
      "v_lshlrev_b32 v43, 4, v37\n"
      "v_lshlrev_b32 v44, 2, v37\n"
      "v_and_b32 v43, 16, v43\n"
      "v_and_or_b32 v43, v44, 8, v43\n"
      "v_mul_u32_u24 v44, 192, v38\n"
      "v_mul_u32_u24 v45, 1536, v39\n"
      "s_lshl_b32 s27, s18, 5\n"
      "v_lshlrev_b32 v46, 1, v36\n"
      "v_add3_u32 v43, v43, v44, v45\n"
      "v_bitop3_b32 v46, v46, s27, 32 bitop3:0x6c\n"
      "v_add_u32 v95, v43, v46\n"
      "v_add_u32 v96, 57344, v95\n"
      "v_add_u32 v97, 114688, v95\n"
      "v_lshlrev_b32 v43, 4, v37\n"
      "s_mul_i32 s26, s17, 0x8080\n"
      "s_sub_u32 s26, 0xbf80, s26\n"
      "s_mov_b32 s27, 0\n"
      "s_lshl_b32 s34, s17, 7\n"
      "s_mov_b32 s29, 0\n"
      "s_sub_u32 s28, s26, s34\n"
      "s_mov_b32 s31, 0\n"
      "s_sub_u32 s30, s28, s34\n"
      "v_lshlrev_b64 v[44:45], v43, s[26:27]\n"
      "v_lshlrev_b64 v[46:47], v43, s[28:29]\n"
      "s_xor_b32 s28, s28, 0x8000\n"
      "v_lshlrev_b64 v[48:49], v43, s[30:31]\n"
      "v_lshlrev_b64 v[50:51], v43, s[28:29]\n"
      "v_accvgpr_write_b32 a244, v44\n"
      "v_accvgpr_write_b32 a245, v45\n"
      "v_accvgpr_write_b32 a246, v46\n"
      "v_accvgpr_write_b32 a247, v47\n"
      "v_accvgpr_write_b32 a248, v48\n"
      "v_accvgpr_write_b32 a249, v49\n"
      "v_accvgpr_write_b32 a250, v50\n"
      "v_accvgpr_write_b32 a251, v51\n"
      "s_lshl_b32 s26, s18, 3\n"
      "v_and_b32 v42, 15, v36\n"
      "v_xor_b32 v42, s26, v42\n"
      "s_lshl_b32 s26, s17, 13\n"
      "s_add_u32 s26, s26, 24832\n"
      "s_mul_i32 s27, s17, 12672\n"
      "v_mul_u32_u24 v44, 192, v42\n"
      "v_lshlrev_b32 v43, 3, v42\n"
      "v_and_b32 v45, 48, v36\n"
      "s_add_u32 s27, s27, 82432\n"
      "v_bitop3_b32 v46, v43, v45, 32 bitop3:0x6c\n"
      "v_add3_u32 v0, v46, v44, s26\n"
      "v_add3_u32 v3, v46, v44, s27\n"
      "v_add_u32 v43, 16, v43\n"
      "v_add_u32 v44, 384, v44\n"
      "v_bitop3_b32 v46, v43, v45, 32 bitop3:0x6c\n"
      "v_add3_u32 v1, v46, v44, s26\n"
      "v_add3_u32 v4, v46, v44, s27\n"
      "v_add_u32 v43, 16, v43\n"
      "v_add_u32 v44, 384, v44\n"
      "v_bitop3_b32 v46, v43, v45, 32 bitop3:0x6c\n"
      "v_add3_u32 v2, v46, v44, s26\n"
      "v_add3_u32 v5, v46, v44, s27\n"
      "v_lshlrev_b32 v43, 31, v36\n"
      "v_and_b32 v44, 2, v36\n"
      "v_or_b32 v43, 0x3f803f80, v43\n"
      "v_mul_u32_u24 v45, 0xc00080, v44\n"
      "v_lshl_add_u32 v44, v44, 22, v43\n"
      "v_add_u32 v45, v45, v43\n"
      "v_accvgpr_write_b32 a216, v44\n"
      "v_accvgpr_write_b32 a217, v45\n"
      "v_lshlrev_b32 v43, 3, v38\n"
      "s_mov_b32 s26, 806420736\n"
      "v_bfe_u32 v43, s26, v43, 8\n"
      "v_lshlrev_b32 v44, 12, v38\n"
      "s_lshl_b32 s28, s18, 8\n"
      "s_add_u32 s28, s28, 82432\n"
      "v_lshl_add_u32 v43, v43, 3, v44\n"
      "v_lshl_add_u32 v44, v39, 2, v37\n"
      "v_lshl_add_u32 v43, v44, 4, v43\n"
      "v_add_u32 v7, s28, v43\n"
      "s_mul_i32 s26, s17, 70144\n"
      "s_lshl_b32 s27, s18, 8\n"
      "s_sub_u32 s26, 70144, s26\n"
      "s_add_u32 s26, s26, s27\n"
      "v_lshl_add_u32 v6, v36, 2, s26\n"
      "v_lshrrev_b32 v43, 3, v36\n"
      "v_and_b32 v44, 7, v36\n"
      "v_mul_u32_u24 v45, 384, v43\n"
      "v_lshl_add_u32 v18, v44, 4, v45\n"
      "v_and_b32 v44, 2, v36\n"
      "v_lshlrev_b32 v43, 3, v43\n"
      "v_lshlrev_b32 v45, 4, v44\n"
      "v_xor_b32 v43, v43, v45\n"
      "v_lshl_add_u32 v43, v44, 7, v43\n"
      "v_and_b32 v44, 1, v36\n"
      "s_lshl_b32 s26, s18, 11\n"
      "v_lshl_add_u32 v43, v44, 6, v43\n"
      "s_add_u32 s26, s26, 12288\n"
      "v_bfe_u32 v44, v36, 2, 1\n"
      "v_add_u32 v43, s26, v43\n"
      "v_mul_i32_i24 v45, 8196, v44\n"
      "s_mov_b32 s27, 0\n"
      "v_add3_u32 v12, v43, v45, s27\n"
      "v_add_u32 v15, 128, v12\n"
      "v_mul_i32_i24 v45, -4092, v44\n"
      "s_mov_b32 s27, 4096\n"
      "v_add3_u32 v13, v43, v45, s27\n"
      "v_add_u32 v16, 128, v13\n"
      "v_mul_i32_i24 v45, -4100, v44\n"
      "s_mov_b32 s27, 8200\n"
      "v_add3_u32 v14, v43, v45, s27\n"
      "v_add_u32 v17, 128, v14\n"
      "s_mul_i32 s62, s18, 393216\n"
      "s_add_u32 s62, s62, s19\n"
      "s_add_u32 s62, s62, s21\n"
      "s_sub_u32 s62, s62, 786432\n"
      "s_mov_b32 s58, 0x80000000\n"
      "s_mov_b32 s59, 0x80000000\n"
      "s_and_b32 s9, s9, 0xffff\n"
      "s_mov_b32 s45, 0xbf2aaaab\n"
      "s_mov_b32 s46, 0xbe800000\n"
      "s_mov_b32 s47, 0x3f2aaaab\n"
      "s_mov_b32 s48, 0xc0c00000\n"
      "s_mov_b32 s49, 0x3f000000\n"
      "s_mov_b32 s38, 4.0\n"
      "s_mov_b32 s39, 0xc0a00000\n"
      "s_add_u32 m0, s63, 116224\n"
      "s_add_u32 s52, s63, 6144\n"
      "buffer_load_dwordx4 v92, s[12:15], s52 offen offset:0 lds\n"
      "buffer_load_dwordx4 v93, s[12:15], s52 offen offset:1024 lds\n"
      "buffer_load_dwordx4 v94, s[12:15], s52 offen offset:2048 lds\n"
      "LDpro_%=:\n"
      "s_waitcnt vmcnt(13)\n"
      "s_barrier\n"
      "ds_read_b64_tr_b16 a[0:1], v95 offset:24832\n"
      "ds_read_b64_tr_b16 a[2:3], v95 offset:25600\n"
      "ds_read_b64_tr_b16 a[224:225], v95 offset:0\n"
      "ds_read_b64_tr_b16 a[226:227], v95 offset:768\n"
      "ds_read_b64_tr_b16 a[220:221], v95 offset:30976\n"
      "ds_read_b64_tr_b16 a[222:223], v95 offset:31744\n"
      "ds_read_b64_tr_b16 a[4:5], v95 offset:24896\n"
      "ds_read_b64_tr_b16 a[6:7], v95 offset:25664\n"
      "ds_read_b64_tr_b16 a[232:233], v95 offset:64\n"
      "ds_read_b64_tr_b16 a[234:235], v95 offset:832\n"
      "ds_read_b64_tr_b16 a[228:229], v95 offset:31040\n"
      "ds_read_b64_tr_b16 a[230:231], v95 offset:31808\n"
      "s_add_u32 m0, s63, 18432\n"
      "s_add_u32 s52, s63, 122880\n"
      "s_waitcnt lgkmcnt(10)\n"
      "v_mfma_f32_4x4x4_16b_bf16 v[28:31], a[244:245], a[0:1], 0\n"
      "v_mfma_f32_4x4x4_16b_bf16 v[32:35], a[244:245], a[2:3], 0\n"
      "buffer_load_dwordx4 v92, s[12:15], s52 offen offset:0 lds\n"
      "s_waitcnt lgkmcnt(8)\n"
      "v_mfma_f32_4x4x4_16b_bf16 v[28:31], a[248:249], a[224:225], v[28:31]\n"
      "ds_read_b64_tr_b16 a[8:9], v95 offset:24960\n"
      "v_mfma_f32_4x4x4_16b_bf16 v[32:35], a[248:249], a[226:227], v[32:35]\n"
      "buffer_load_dwordx4 v93, s[12:15], s52 offen offset:1024 lds\n"
      "s_waitcnt lgkmcnt(7)\n"
      "v_mfma_f32_4x4x4_16b_bf16 v[36:39], a[246:247], a[220:221], v[28:31]\n"
      "ds_read_b64_tr_b16 a[10:11], v95 offset:25728\n"
      "v_mfma_f32_4x4x4_16b_bf16 v[40:43], a[246:247], a[222:223], v[32:35]\n"
      "buffer_load_dwordx4 v94, s[12:15], s52 offen offset:2048 lds\n"
      "v_mfma_f32_4x4x4_16b_bf16 v[28:31], a[250:251], a[220:221], v[28:31]\n"
      "ds_read_b64_tr_b16 a[240:241], v95 offset:128\n"
      "v_mfma_f32_4x4x4_16b_bf16 v[32:35], a[250:251], a[222:223], v[32:35]\n"
      "ds_read_b64_tr_b16 a[242:243], v95 offset:896\n"
      "ds_read_b64_tr_b16 a[236:237], v95 offset:31104\n"
      "ds_read_b64_tr_b16 a[238:239], v95 offset:31872\n"
      "s_add_u32 m0, s63, 53504\n"
      "s_add_u32 s52, s63, 129024\n"
      "s_waitcnt lgkmcnt(5)\n"
      "v_mfma_f32_4x4x4_16b_bf16 v[76:79], a[244:245], a[4:5], 0\n"
      "v_mfma_f32_4x4x4_16b_bf16 v[80:83], a[244:245], a[6:7], 0\n"
      "buffer_load_dwordx4 v92, s[12:15], s52 offen offset:0 lds\n"
      "v_mfma_f32_4x4x4_16b_bf16 v[76:79], a[248:249], a[232:233], v[76:79]\n"
      "buffer_load_dwordx4 v93, s[12:15], s52 offen offset:1024 lds\n"
      "v_mfma_f32_4x4x4_16b_bf16 v[80:83], a[248:249], a[234:235], v[80:83]\n"
      "buffer_load_dwordx4 v94, s[12:15], s52 offen offset:2048 lds\n"
      "v_mfma_f32_4x4x4_16b_bf16 v[84:87], a[246:247], a[228:229], v[76:79]\n"
      "v_mfma_f32_4x4x4_16b_bf16 v[88:91], a[246:247], a[230:231], v[80:83]\n"
      "v_mfma_f32_4x4x4_16b_bf16 v[76:79], a[250:251], a[228:229], v[76:79]\n"
      "v_mfma_f32_4x4x4_16b_bf16 v[80:83], a[250:251], a[230:231], v[80:83]\n"
      "v_cvt_pk_bf16_f32 v252, v36, v40\n"
      "v_cvt_pk_bf16_f32 v253, v37, v41\n"
      "v_cvt_pk_bf16_f32 v254, v38, v42\n"
      "v_cvt_pk_bf16_f32 v255, v39, v43\n"
      "v_cvt_pk_bf16_f32 v248, v28, v32\n"
      "v_cvt_pk_bf16_f32 v249, v29, v33\n"
      "v_cvt_pk_bf16_f32 v250, v30, v34\n"
      "v_cvt_pk_bf16_f32 v251, v31, v35\n"
      "s_waitcnt lgkmcnt(0)\n"
      "v_mfma_f32_4x4x4_16b_bf16 v[28:31], a[244:245], a[8:9], 0\n"
      "v_mfma_f32_4x4x4_16b_bf16 v[32:35], a[244:245], a[10:11], 0\n"
      "s_nop 0\n"
      "v_mfma_f32_4x4x4_16b_bf16 v[28:31], a[248:249], a[240:241], v[28:31]\n"
      "v_mfma_f32_4x4x4_16b_bf16 v[32:35], a[248:249], a[242:243], v[32:35]\n"
      "s_nop 0\n"
      "s_nop 0\n"
      "v_mfma_f32_4x4x4_16b_bf16 v[36:39], a[246:247], a[236:237], v[28:31]\n"
      "v_mfma_f32_4x4x4_16b_bf16 v[40:43], a[246:247], a[238:239], v[32:35]\n"
      "v_mfma_f32_4x4x4_16b_bf16 v[28:31], a[250:251], a[236:237], v[28:31]\n"
      "v_mfma_f32_4x4x4_16b_bf16 v[32:35], a[250:251], a[238:239], v[32:35]\n"
      "v_cvt_pk_bf16_f32 v244, v84, v88\n"
      "v_cvt_pk_bf16_f32 v245, v85, v89\n"
      "v_cvt_pk_bf16_f32 v246, v86, v90\n"
      "v_cvt_pk_bf16_f32 v247, v87, v91\n"
      "v_cvt_pk_bf16_f32 v240, v76, v80\n"
      "v_cvt_pk_bf16_f32 v241, v77, v81\n"
      "v_cvt_pk_bf16_f32 v242, v78, v82\n"
      "v_cvt_pk_bf16_f32 v243, v79, v83\n"
      "s_waitcnt vmcnt(6)\n"
      "s_barrier\n"
      "ds_read_b64_tr_b16 a[12:13], v97 offset:1536\n"
      "ds_read_b64_tr_b16 a[14:15], v97 offset:2304\n"
      "ds_read_b64_tr_b16 a[224:225], v97 offset:13824\n"
      "ds_read_b64_tr_b16 a[226:227], v97 offset:14592\n"
      "ds_read_b64_tr_b16 a[220:221], v97 offset:7680\n"
      "ds_read_b64_tr_b16 a[222:223], v97 offset:8448\n"
      "s_add_u32 m0, s63, 71936\n"
      "s_add_u32 s52, s63, 135168\n"
      "s_waitcnt lgkmcnt(4)\n"
      "v_mfma_f32_4x4x4_16b_bf16 v[76:79], a[244:245], a[12:13], 0\n"
      "v_mfma_f32_4x4x4_16b_bf16 v[80:83], a[244:245], a[14:15], 0\n"
      "buffer_load_dwordx4 v92, s[12:15], s52 offen offset:0 lds\n"
      "s_waitcnt lgkmcnt(2)\n"
      "v_mfma_f32_4x4x4_16b_bf16 v[76:79], a[248:249], a[224:225], v[76:79]\n"
      "ds_read_b64_tr_b16 a[16:17], v97 offset:1600\n"
      "v_mfma_f32_4x4x4_16b_bf16 v[80:83], a[248:249], a[226:227], v[80:83]\n"
      "buffer_load_dwordx4 v93, s[12:15], s52 offen offset:1024 lds\n"
      "s_nop 0\n"
      "s_waitcnt lgkmcnt(1)\n"
      "v_mfma_f32_4x4x4_16b_bf16 v[84:87], a[246:247], a[220:221], v[76:79]\n"
      "ds_read_b64_tr_b16 a[18:19], v97 offset:2368\n"
      "v_mfma_f32_4x4x4_16b_bf16 v[88:91], a[246:247], a[222:223], v[80:83]\n"
      "buffer_load_dwordx4 v94, s[12:15], s52 offen offset:2048 lds\n"
      "v_mfma_f32_4x4x4_16b_bf16 v[76:79], a[250:251], a[220:221], v[76:79]\n"
      "ds_read_b64_tr_b16 a[232:233], v97 offset:13888\n"
      "v_mfma_f32_4x4x4_16b_bf16 v[80:83], a[250:251], a[222:223], v[80:83]\n"
      "ds_read_b64_tr_b16 a[234:235], v97 offset:14656\n"
      "ds_read_b64_tr_b16 a[228:229], v97 offset:7744\n"
      "ds_read_b64_tr_b16 a[230:231], v97 offset:8512\n"
      "ds_read_b64_tr_b16 a[20:21], v97 offset:1664\n"
      "ds_read_b64_tr_b16 a[22:23], v97 offset:2432\n"
      "ds_read_b64_tr_b16 a[240:241], v97 offset:13952\n"
      "ds_read_b64_tr_b16 a[242:243], v97 offset:14720\n"
      "ds_read_b64_tr_b16 a[236:237], v97 offset:7808\n"
      "ds_read_b64_tr_b16 a[238:239], v97 offset:8576\n"
      "ds_read_b64_tr_b16 a[188:189], v7 offset:2048\n"
      "ds_read_b64_tr_b16 a[190:191], v7 offset:2056\n"
      "ds_read_b128 a[192:195], v3 offset:55168\n"
      "v_cvt_pk_bf16_f32 v236, v36, v40\n"
      "v_cvt_pk_bf16_f32 v237, v37, v41\n"
      "v_cvt_pk_bf16_f32 v238, v38, v42\n"
      "v_cvt_pk_bf16_f32 v239, v39, v43\n"
      "v_cvt_pk_bf16_f32 v232, v28, v32\n"
      "v_cvt_pk_bf16_f32 v233, v29, v33\n"
      "v_cvt_pk_bf16_f32 v234, v30, v34\n"
      "v_cvt_pk_bf16_f32 v235, v31, v35\n"
      "s_waitcnt vmcnt(6)\n"
      "s_barrier\n"
      "s_nop 0\n"
      "s_waitcnt lgkmcnt(8)\n"
      "v_mfma_f32_4x4x4_16b_bf16 v[28:31], a[244:245], a[16:17], 0\n"
      "v_mfma_f32_4x4x4_16b_bf16 v[32:35], a[244:245], a[18:19], 0\n"
      "ds_read_b64_tr_b16 a[24:25], v95 offset:6144\n"
      "v_mfma_f32_4x4x4_16b_bf16 v[28:31], a[248:249], a[232:233], v[28:31]\n"
      "ds_read_b64_tr_b16 a[26:27], v95 offset:6912\n"
      "v_mfma_f32_4x4x4_16b_bf16 v[32:35], a[248:249], a[234:235], v[32:35]\n"
      "ds_read_b64_tr_b16 a[224:225], v95 offset:18432\n"
      "v_mfma_f32_4x4x4_16b_bf16 v[36:39], a[246:247], a[228:229], v[28:31]\n"
      "ds_read_b64_tr_b16 a[226:227], v95 offset:19200\n"
      "v_mfma_f32_4x4x4_16b_bf16 v[40:43], a[246:247], a[230:231], v[32:35]\n"
      "ds_read_b64_tr_b16 a[220:221], v95 offset:12288\n"
      "v_mfma_f32_4x4x4_16b_bf16 v[28:31], a[250:251], a[228:229], v[28:31]\n"
      "ds_read_b64_tr_b16 a[222:223], v95 offset:13056\n"
      "v_mfma_f32_4x4x4_16b_bf16 v[32:35], a[250:251], a[230:231], v[32:35]\n"
      "ds_read_b64_tr_b16 a[184:185], v7 offset:2560\n"
      "s_waitcnt lgkmcnt(7)\n"
      "v_mfma_f32_4x4x4_16b_bf16 v[44:47], a[216:217], a[188:189], 0\n"
      "ds_read_b64_tr_b16 a[186:187], v7 offset:2568\n"
      "v_mfma_f32_4x4x4_16b_bf16 v[48:51], a[216:217], a[190:191], 0\n"
      "ds_read_b128 a[196:199], v3 offset:55232\n"
      "v_cvt_pk_bf16_f32 v228, v84, v88\n"
      "v_cvt_pk_bf16_f32 v229, v85, v89\n"
      "v_cvt_pk_bf16_f32 v230, v86, v90\n"
      "v_cvt_pk_bf16_f32 v231, v87, v91\n"
      "v_cvt_pk_bf16_f32 v224, v76, v80\n"
      "v_cvt_pk_bf16_f32 v225, v77, v81\n"
      "v_cvt_pk_bf16_f32 v226, v78, v82\n"
      "v_cvt_pk_bf16_f32 v227, v79, v83\n"
      "s_add_u32 m0, s63, 0\n"
      "s_add_u32 s52, s63, 141312\n"
      "v_mfma_f32_4x4x4_16b_bf16 v[76:79], a[244:245], a[20:21], 0\n"
      "v_mfma_f32_4x4x4_16b_bf16 v[80:83], a[244:245], a[22:23], 0\n"
      "buffer_load_dwordx4 v92, s[12:15], s52 offen offset:0 lds\n"
      "v_mfma_f32_4x4x4_16b_bf16 v[76:79], a[248:249], a[240:241], v[76:79]\n"
      "ds_read_b64_tr_b16 a[28:29], v95 offset:6208\n"
      "v_mfma_f32_4x4x4_16b_bf16 v[80:83], a[248:249], a[242:243], v[80:83]\n"
      "buffer_load_dwordx4 v93, s[12:15], s52 offen offset:1024 lds\n"
      "v_mfma_f32_4x4x4_16b_bf16 v[84:87], a[246:247], a[236:237], v[76:79]\n"
      "ds_read_b64_tr_b16 a[30:31], v95 offset:6976\n"
      "v_mfma_f32_4x4x4_16b_bf16 v[88:91], a[246:247], a[238:239], v[80:83]\n"
      "buffer_load_dwordx4 v94, s[12:15], s52 offen offset:2048 lds\n"
      "v_mfma_f32_4x4x4_16b_bf16 v[76:79], a[250:251], a[236:237], v[76:79]\n"
      "ds_read_b64_tr_b16 a[232:233], v95 offset:18496\n"
      "v_mfma_f32_4x4x4_16b_bf16 v[80:83], a[250:251], a[238:239], v[80:83]\n"
      "ds_read_b64_tr_b16 a[234:235], v95 offset:19264\n"
      "s_nop 0\n"
      "s_waitcnt lgkmcnt(4)\n"
      "v_mfma_f32_4x4x4_16b_bf16 v[112:115], a[216:217], a[184:185], 0\n"
      "ds_read_b64_tr_b16 a[228:229], v95 offset:12352\n"
      "v_mfma_f32_4x4x4_16b_bf16 v[116:119], a[216:217], a[186:187], 0\n"
      "ds_read_b64_tr_b16 a[230:231], v95 offset:13120\n"
      "ds_read_b64_tr_b16 a[180:181], v7 offset:3072\n"
      "ds_read_b64_tr_b16 a[182:183], v7 offset:3080\n"
      "v_cvt_pk_bf16_f32 v220, v36, v40\n"
      "v_cvt_pk_bf16_f32 v221, v37, v41\n"
      "v_cvt_pk_bf16_f32 v222, v38, v42\n"
      "v_cvt_pk_bf16_f32 v223, v39, v43\n"
      "v_cvt_pk_bf16_f32 v216, v28, v32\n"
      "v_cvt_pk_bf16_f32 v217, v29, v33\n"
      "v_mfma_f32_16x16x32_bf16 v[100:103], a[0:3], a[192:195], 0\n"
      "v_cvt_pk_bf16_f32 v218, v30, v34\n"
      "v_cvt_pk_bf16_f32 v219, v31, v35\n"
      "v_mfma_f32_16x16x32_bf16 v[104:107], a[4:7], a[192:195], 0\n"
      "s_add_u32 m0, s37, 99328\n"
      "v_cvt_pk_bf16_f32 v44, v44, v48\n"
      "v_cvt_pk_bf16_f32 v45, v45, v49\n"
      "v_mfma_f32_16x16x32_bf16 v[108:111], a[8:11], a[192:195], 0\n"
      "s_add_u32 s53, s54, 199680\n"
      "v_cvt_pk_bf16_f32 v46, v46, v50\n"
      "v_cvt_pk_bf16_f32 v47, v47, v51\n"
      "v_mfma_f32_4x4x4_16b_bf16 v[28:31], a[244:245], a[24:25], 0\n"
      "v_mfma_f32_4x4x4_16b_bf16 v[32:35], a[244:245], a[26:27], 0\n"
      "buffer_load_dwordx4 v10, s[4:7], s53 offen offset:0 nt lds\n"
      "buffer_load_dwordx4 v11, s[4:7], s53 offen offset:1024 nt lds\n"
      "v_mfma_f32_4x4x4_16b_bf16 v[28:31], a[248:249], a[224:225], v[28:31]\n"
      "ds_read_b64_tr_b16 a[32:33], v95 offset:6272\n"
      "v_mfma_f32_4x4x4_16b_bf16 v[32:35], a[248:249], a[226:227], v[32:35]\n"
      "s_add_u32 s53, s54, 396288\n"
      "v_mfma_f32_4x4x4_16b_bf16 v[36:39], a[246:247], a[220:221], v[28:31]\n"
      "ds_read_b64_tr_b16 a[34:35], v95 offset:7040\n"
      "v_mfma_f32_4x4x4_16b_bf16 v[40:43], a[246:247], a[222:223], v[32:35]\n"
      "s_add_u32 m0, s37, 103432\n"
      "v_mfma_f32_4x4x4_16b_bf16 v[28:31], a[250:251], a[220:221], v[28:31]\n"
      "ds_read_b64_tr_b16 a[240:241], v95 offset:18560\n"
      "v_mfma_f32_4x4x4_16b_bf16 v[32:35], a[250:251], a[222:223], v[32:35]\n"
      "buffer_load_dwordx4 v10, s[4:7], s53 offen offset:0 nt lds\n"
      "buffer_load_dwordx4 v11, s[4:7], s53 offen offset:1024 nt lds\n"
      "s_nop 0\n"
      "s_waitcnt lgkmcnt(3)\n"
      "v_mfma_f32_4x4x4_16b_bf16 v[20:23], a[216:217], a[180:181], 0\n"
      "ds_read_b64_tr_b16 a[242:243], v95 offset:19328\n"
      "v_mfma_f32_4x4x4_16b_bf16 v[24:27], a[216:217], a[182:183], 0\n"
      "ds_read_b64_tr_b16 a[236:237], v95 offset:12416\n"
      "ds_read_b64_tr_b16 a[238:239], v95 offset:13184\n"
      "ds_read_b64_tr_b16 a[176:177], v7 offset:3584\n"
      "ds_read_b64_tr_b16 a[178:179], v7 offset:3592\n"
      "s_mov_b32 m0, s64\n"
      "s_nop 0\n"
      "ds_write_addtid_b32 v44 offset:2048\n"
      "ds_write_addtid_b32 v45 offset:6144\n"
      "ds_write_addtid_b32 v46 offset:10240\n"
      "ds_write_addtid_b32 v47 offset:14336\n"
      "v_cvt_pk_bf16_f32 v212, v84, v88\n"
      "v_cvt_pk_bf16_f32 v213, v85, v89\n"
      "v_cvt_pk_bf16_f32 v214, v86, v90\n"
      "v_cvt_pk_bf16_f32 v215, v87, v91\n"
      "v_cvt_pk_bf16_f32 v208, v76, v80\n"
      "v_cvt_pk_bf16_f32 v209, v77, v81\n"
      "v_mfma_f32_16x16x32_bf16 v[100:103], a[12:15], a[196:199], v[100:103]\n"
      "s_nop 0\n"
      "v_cvt_pk_bf16_f32 v210, v78, v82\n"
      "v_cvt_pk_bf16_f32 v211, v79, v83\n"
      "v_mfma_f32_16x16x32_bf16 v[104:107], a[16:19], a[196:199], v[104:107]\n"
      "s_waitcnt vmcnt(10)\n"
      "v_cvt_pk_bf16_f32 v112, v112, v116\n"
      "v_cvt_pk_bf16_f32 v113, v113, v117\n"
      "v_mfma_f32_16x16x32_bf16 v[108:111], a[20:23], a[196:199], v[108:111]\n"
      "s_waitcnt lgkmcnt(0)\n"
      "v_cvt_pk_bf16_f32 v114, v114, v118\n"
      "v_cvt_pk_bf16_f32 v115, v115, v119\n"
      "s_barrier\n"
      "s_add_u32 s53, s54, 592896\n"
      "s_add_u32 m0, s37, 107656\n"
      "v_mfma_f32_4x4x4_16b_bf16 v[76:79], a[244:245], a[28:29], 0\n"
      "v_mfma_f32_4x4x4_16b_bf16 v[80:83], a[244:245], a[30:31], 0\n"
      "buffer_load_dwordx4 v10, s[4:7], s53 offen offset:0 nt lds\n"
      "buffer_load_dwordx4 v11, s[4:7], s53 offen offset:1024 nt lds\n"
      "v_mfma_f32_4x4x4_16b_bf16 v[76:79], a[248:249], a[232:233], v[76:79]\n"
      "ds_read_b64_tr_b16 a[36:37], v95 offset:41216\n"
      "v_mfma_f32_4x4x4_16b_bf16 v[80:83], a[248:249], a[234:235], v[80:83]\n"
      "s_add_u32 s53, s54, 789504\n"
      "v_mfma_f32_4x4x4_16b_bf16 v[84:87], a[246:247], a[228:229], v[76:79]\n"
      "ds_read_b64_tr_b16 a[38:39], v95 offset:41984\n"
      "v_mfma_f32_4x4x4_16b_bf16 v[88:91], a[246:247], a[230:231], v[80:83]\n"
      "s_add_u32 m0, s37, 112000\n"
      "v_mfma_f32_4x4x4_16b_bf16 v[76:79], a[250:251], a[228:229], v[76:79]\n"
      "ds_read_b64_tr_b16 a[224:225], v95 offset:53504\n"
      "v_mfma_f32_4x4x4_16b_bf16 v[80:83], a[250:251], a[230:231], v[80:83]\n"
      "buffer_load_dwordx4 v10, s[4:7], s53 offen offset:0 nt lds\n"
      "buffer_load_dwordx4 v11, s[4:7], s53 offen offset:1024 nt lds\n"
      "v_mfma_f32_4x4x4_16b_bf16 v[124:127], a[216:217], a[176:177], 0\n"
      "ds_read_b64_tr_b16 a[226:227], v95 offset:54272\n"
      "v_mfma_f32_4x4x4_16b_bf16 v[128:131], a[216:217], a[178:179], 0\n"
      "ds_read_b64_tr_b16 a[220:221], v95 offset:47360\n"
      "ds_read_b64_tr_b16 a[222:223], v95 offset:48128\n"
      "s_mov_b32 m0, s64\n"
      "s_nop 0\n"
      "ds_write_addtid_b32 v112 offset:2560\n"
      "ds_write_addtid_b32 v113 offset:6656\n"
      "ds_write_addtid_b32 v114 offset:10752\n"
      "ds_write_addtid_b32 v115 offset:14848\n"
      "ds_read_b128 a[200:203], v3 offset:55296\n"
      "v_cvt_pk_bf16_f32 v204, v36, v40\n"
      "v_cvt_pk_bf16_f32 v205, v37, v41\n"
      "v_cvt_pk_bf16_f32 v206, v38, v42\n"
      "v_cvt_pk_bf16_f32 v207, v39, v43\n"
      "v_cvt_pk_bf16_f32 v200, v28, v32\n"
      "v_cvt_pk_bf16_f32 v201, v29, v33\n"
      "v_cvt_pk_bf16_f32 v202, v30, v34\n"
      "v_cvt_pk_bf16_f32 v203, v31, v35\n"
      "v_cvt_pk_bf16_f32 v20, v20, v24\n"
      "v_cvt_pk_bf16_f32 v21, v21, v25\n"
      "v_cvt_pk_bf16_f32 v22, v22, v26\n"
      "v_cvt_pk_bf16_f32 v23, v23, v27\n"
      "s_add_u32 s53, s36, 3072\n"
      "s_add_u32 m0, s37, 154368\n"
      "v_mfma_f32_4x4x4_16b_bf16 v[28:31], a[244:245], a[32:33], 0\n"
      "v_mfma_f32_4x4x4_16b_bf16 v[32:35], a[244:245], a[34:35], 0\n"
      "buffer_load_dwordx4 v10, s[4:7], s53 offen offset:0 nt lds\n"
      "buffer_load_dwordx4 v11, s[4:7], s53 offen offset:1024 nt lds\n"
      "v_mfma_f32_4x4x4_16b_bf16 v[28:31], a[248:249], a[240:241], v[28:31]\n"
      "ds_read_b64_tr_b16 a[40:41], v95 offset:41280\n"
      "v_mfma_f32_4x4x4_16b_bf16 v[32:35], a[248:249], a[242:243], v[32:35]\n"
      "ds_read_b64_tr_b16 a[42:43], v95 offset:42048\n"
      "v_mfma_f32_4x4x4_16b_bf16 v[36:39], a[246:247], a[236:237], v[28:31]\n"
      "ds_read_b64_tr_b16 a[232:233], v95 offset:53568\n"
      "v_mfma_f32_4x4x4_16b_bf16 v[40:43], a[246:247], a[238:239], v[32:35]\n"
      "ds_read_b64_tr_b16 a[234:235], v95 offset:54336\n"
      "v_mfma_f32_4x4x4_16b_bf16 v[28:31], a[250:251], a[236:237], v[28:31]\n"
      "ds_read_b64_tr_b16 a[228:229], v95 offset:47424\n"
      "v_mfma_f32_4x4x4_16b_bf16 v[32:35], a[250:251], a[238:239], v[32:35]\n"
      "ds_read_b64_tr_b16 a[230:231], v95 offset:48192\n"
      "s_mov_b32 m0, s64\n"
      "s_nop 0\n"
      "ds_write_addtid_b32 v20 offset:3072\n"
      "ds_write_addtid_b32 v21 offset:7168\n"
      "ds_write_addtid_b32 v22 offset:11264\n"
      "ds_write_addtid_b32 v23 offset:15360\n"
      "v_cvt_pk_bf16_f32 v196, v84, v88\n"
      "v_cvt_pk_bf16_f32 v197, v85, v89\n"
      "v_cvt_pk_bf16_f32 v198, v86, v90\n"
      "v_cvt_pk_bf16_f32 v199, v87, v91\n"
      "v_cvt_pk_bf16_f32 v192, v76, v80\n"
      "v_cvt_pk_bf16_f32 v193, v77, v81\n"
      "v_cvt_pk_bf16_f32 v194, v78, v82\n"
      "v_cvt_pk_bf16_f32 v195, v79, v83\n"
      "v_cvt_pk_bf16_f32 v124, v124, v128\n"
      "v_cvt_pk_bf16_f32 v125, v125, v129\n"
      "v_cvt_pk_bf16_f32 v126, v126, v130\n"
      "v_cvt_pk_bf16_f32 v127, v127, v131\n"
      "s_nop 0\n"
      "s_waitcnt lgkmcnt(4)\n"
      "v_mfma_f32_4x4x4_16b_bf16 v[76:79], a[244:245], a[36:37], 0\n"
      "v_mfma_f32_4x4x4_16b_bf16 v[80:83], a[244:245], a[38:39], 0\n"
      "ds_read_b64_tr_b16 a[44:45], v95 offset:41344\n"
      "v_mfma_f32_4x4x4_16b_bf16 v[76:79], a[248:249], a[224:225], v[76:79]\n"
      "ds_read_b64_tr_b16 a[46:47], v95 offset:42112\n"
      "v_mfma_f32_4x4x4_16b_bf16 v[80:83], a[248:249], a[226:227], v[80:83]\n"
      "ds_read_b64_tr_b16 a[240:241], v95 offset:53632\n"
      "v_mfma_f32_4x4x4_16b_bf16 v[84:87], a[246:247], a[220:221], v[76:79]\n"
      "ds_read_b64_tr_b16 a[242:243], v95 offset:54400\n"
      "v_mfma_f32_4x4x4_16b_bf16 v[88:91], a[246:247], a[222:223], v[80:83]\n"
      "ds_read_b64_tr_b16 a[236:237], v95 offset:47488\n"
      "v_mfma_f32_4x4x4_16b_bf16 v[76:79], a[250:251], a[220:221], v[76:79]\n"
      "ds_read_b64_tr_b16 a[238:239], v95 offset:48256\n"
      "v_mfma_f32_4x4x4_16b_bf16 v[80:83], a[250:251], a[222:223], v[80:83]\n"
      "ds_write_addtid_b32 v124 offset:3584\n"
      "ds_write_addtid_b32 v125 offset:7680\n"
      "ds_write_addtid_b32 v126 offset:11776\n"
      "ds_write_addtid_b32 v127 offset:15872\n"
      "v_cvt_pk_bf16_f32 v188, v36, v40\n"
      "v_cvt_pk_bf16_f32 v189, v37, v41\n"
      "v_mfma_f32_16x16x32_bf16 v[100:103], a[24:27], a[200:203], v[100:103]\n"
      "v_cvt_pk_bf16_f32 v190, v38, v42\n"
      "v_cvt_pk_bf16_f32 v191, v39, v43\n"
      "v_mfma_f32_16x16x32_bf16 v[104:107], a[28:31], a[200:203], v[104:107]\n"
      "s_waitcnt vmcnt(13)\n"
      "v_cvt_pk_bf16_f32 v184, v28, v32\n"
      "v_cvt_pk_bf16_f32 v185, v29, v33\n"
      "v_mfma_f32_16x16x32_bf16 v[108:111], a[32:35], a[200:203], v[108:111]\n"
      "s_waitcnt lgkmcnt(0)\n"
      "v_cvt_pk_bf16_f32 v186, v30, v34\n"
      "v_cvt_pk_bf16_f32 v187, v31, v35\n"
      "s_nop 0\n"
      "s_barrier\n"
      "v_mfma_f32_4x4x4_16b_bf16 v[28:31], a[244:245], a[40:41], 0\n"
      "v_mfma_f32_4x4x4_16b_bf16 v[32:35], a[244:245], a[42:43], 0\n"
      "ds_read_b64_tr_b16 a[48:49], v96 offset:2304\n"
      "v_mfma_f32_4x4x4_16b_bf16 v[28:31], a[248:249], a[232:233], v[28:31]\n"
      "ds_read_b64_tr_b16 a[50:51], v96 offset:3072\n"
      "v_mfma_f32_4x4x4_16b_bf16 v[32:35], a[248:249], a[234:235], v[32:35]\n"
      "ds_read_b64_tr_b16 a[224:225], v96 offset:14592\n"
      "v_mfma_f32_4x4x4_16b_bf16 v[36:39], a[246:247], a[228:229], v[28:31]\n"
      "ds_read_b64_tr_b16 a[226:227], v96 offset:15360\n"
      "v_mfma_f32_4x4x4_16b_bf16 v[40:43], a[246:247], a[230:231], v[32:35]\n"
      "ds_read_b64_tr_b16 a[220:221], v96 offset:8448\n"
      "v_mfma_f32_4x4x4_16b_bf16 v[28:31], a[250:251], a[228:229], v[28:31]\n"
      "ds_read_b64_tr_b16 a[222:223], v96 offset:9216\n"
      "v_mfma_f32_4x4x4_16b_bf16 v[32:35], a[250:251], a[230:231], v[32:35]\n"
      "ds_read_b128 a[204:207], v4 offset:55168\n"
      "ds_read_b128 a[208:211], v0 offset:0\n"
      "ds_read_b128 a[212:215], v0 offset:4096\n"
      "v_cvt_pk_bf16_f32 v180, v84, v88\n"
      "v_cvt_pk_bf16_f32 v181, v85, v89\n"
      "v_cvt_pk_bf16_f32 v182, v86, v90\n"
      "v_cvt_pk_bf16_f32 v183, v87, v91\n"
      "v_cvt_pk_bf16_f32 v176, v76, v80\n"
      "v_cvt_pk_bf16_f32 v177, v77, v81\n"
      "v_cvt_pk_bf16_f32 v178, v78, v82\n"
      "v_cvt_pk_bf16_f32 v179, v79, v83\n"
      "v_mfma_f32_4x4x4_16b_bf16 v[76:79], a[244:245], a[44:45], 0\n"
      "v_mfma_f32_4x4x4_16b_bf16 v[80:83], a[244:245], a[46:47], 0\n"
      "ds_read_b64_tr_b16 a[52:53], v96 offset:2368\n"
      "v_mfma_f32_4x4x4_16b_bf16 v[76:79], a[248:249], a[240:241], v[76:79]\n"
      "ds_read_b64_tr_b16 a[54:55], v96 offset:3136\n"
      "v_mfma_f32_4x4x4_16b_bf16 v[80:83], a[248:249], a[242:243], v[80:83]\n"
      "ds_read_b64_tr_b16 a[232:233], v96 offset:14656\n"
      "v_mfma_f32_4x4x4_16b_bf16 v[84:87], a[246:247], a[236:237], v[76:79]\n"
      "ds_read_b64_tr_b16 a[234:235], v96 offset:15424\n"
      "v_mfma_f32_4x4x4_16b_bf16 v[88:91], a[246:247], a[238:239], v[80:83]\n"
      "ds_read_b64_tr_b16 a[228:229], v96 offset:8512\n"
      "v_mfma_f32_4x4x4_16b_bf16 v[76:79], a[250:251], a[236:237], v[76:79]\n"
      "ds_read_b64_tr_b16 a[230:231], v96 offset:9280\n"
      "v_mfma_f32_4x4x4_16b_bf16 v[80:83], a[250:251], a[238:239], v[80:83]\n"
      "ds_read_b128 a[192:195], v0 offset:64\n"
      "ds_read_b128 a[196:199], v0 offset:4160\n"
      "ds_read_b128 a[200:203], v0 offset:128\n"
      "v_cvt_pk_bf16_f32 v172, v36, v40\n"
      "v_cvt_pk_bf16_f32 v173, v37, v41\n"
      "v_cvt_pk_bf16_f32 v174, v38, v42\n"
      "v_cvt_pk_bf16_f32 v175, v39, v43\n"
      "v_cvt_pk_bf16_f32 v168, v28, v32\n"
      "v_cvt_pk_bf16_f32 v169, v29, v33\n"
      "v_cvt_pk_bf16_f32 v170, v30, v34\n"
      "v_cvt_pk_bf16_f32 v171, v31, v35\n"
      "s_add_u32 m0, s63, 18432\n"
      "s_add_u32 s52, s63, 147456\n"
      "s_nop 0\n"
      "s_waitcnt lgkmcnt(4)\n"
      "v_mfma_f32_4x4x4_16b_bf16 v[28:31], a[244:245], a[48:49], 0\n"
      "v_mfma_f32_4x4x4_16b_bf16 v[32:35], a[244:245], a[50:51], 0\n"
      "buffer_load_dwordx4 v92, s[12:15], s52 offen offset:0 lds\n"
      "v_mfma_f32_4x4x4_16b_bf16 v[28:31], a[248:249], a[224:225], v[28:31]\n"
      "ds_read_b64_tr_b16 a[56:57], v96 offset:2432\n"
      "v_mfma_f32_4x4x4_16b_bf16 v[32:35], a[248:249], a[226:227], v[32:35]\n"
      "buffer_load_dwordx4 v93, s[12:15], s52 offen offset:1024 lds\n"
      "v_mfma_f32_4x4x4_16b_bf16 v[36:39], a[246:247], a[220:221], v[28:31]\n"
      "ds_read_b64_tr_b16 a[58:59], v96 offset:3200\n"
      "v_mfma_f32_4x4x4_16b_bf16 v[40:43], a[246:247], a[222:223], v[32:35]\n"
      "buffer_load_dwordx4 v94, s[12:15], s52 offen offset:2048 lds\n"
      "v_mfma_f32_4x4x4_16b_bf16 v[28:31], a[250:251], a[220:221], v[28:31]\n"
      "ds_read_b64_tr_b16 a[240:241], v96 offset:14720\n"
      "v_mfma_f32_4x4x4_16b_bf16 v[32:35], a[250:251], a[222:223], v[32:35]\n"
      "ds_read_b64_tr_b16 a[242:243], v96 offset:15488\n"
      "ds_read_b64_tr_b16 a[236:237], v96 offset:8576\n"
      "ds_read_b64_tr_b16 a[238:239], v96 offset:9344\n"
      "v_mfma_f32_16x16x32_bf16 v[100:103], a[36:39], a[204:207], v[100:103]\n"
      "v_cvt_pk_bf16_f32 v164, v84, v88\n"
      "v_cvt_pk_bf16_f32 v165, v85, v89\n"
      "v_mfma_f32_16x16x32_bf16 v[104:107], a[40:43], a[204:207], v[104:107]\n"
      "v_cvt_pk_bf16_f32 v166, v86, v90\n"
      "v_cvt_pk_bf16_f32 v167, v87, v91\n"
      "v_mfma_f32_16x16x32_bf16 v[108:111], a[44:47], a[204:207], v[108:111]\n"
      "v_cvt_pk_bf16_f32 v160, v76, v80\n"
      "v_cvt_pk_bf16_f32 v161, v77, v81\n"
      "v_mfma_f32_16x16x32_bf16 v[52:55], v[252:255], a[208:211], 0\n"
      "s_waitcnt vmcnt(13)\n"
      "v_cvt_pk_bf16_f32 v162, v78, v82\n"
      "v_cvt_pk_bf16_f32 v163, v79, v83\n"
      "s_barrier\n"
      "v_mfma_f32_4x4x4_16b_bf16 v[76:79], a[244:245], a[52:53], 0\n"
      "v_mfma_f32_4x4x4_16b_bf16 v[80:83], a[244:245], a[54:55], 0\n"
      "ds_read_b64_tr_b16 a[60:61], v97 offset:19968\n"
      "v_mfma_f32_4x4x4_16b_bf16 v[76:79], a[248:249], a[232:233], v[76:79]\n"
      "ds_read_b64_tr_b16 a[62:63], v97 offset:20736\n"
      "v_mfma_f32_4x4x4_16b_bf16 v[80:83], a[248:249], a[234:235], v[80:83]\n"
      "ds_read_b64_tr_b16 a[224:225], v95 offset:0\n"
      "s_nop 0\n"
      "s_waitcnt lgkmcnt(3)\n"
      "v_mfma_f32_4x4x4_16b_bf16 v[84:87], a[246:247], a[228:229], v[76:79]\n"
      "ds_read_b64_tr_b16 a[226:227], v95 offset:768\n"
      "v_mfma_f32_4x4x4_16b_bf16 v[88:91], a[246:247], a[230:231], v[80:83]\n"
      "ds_read_b64_tr_b16 a[220:221], v97 offset:26112\n"
      "v_mfma_f32_4x4x4_16b_bf16 v[76:79], a[250:251], a[228:229], v[76:79]\n"
      "ds_read_b64_tr_b16 a[222:223], v97 offset:26880\n"
      "v_mfma_f32_4x4x4_16b_bf16 v[80:83], a[250:251], a[230:231], v[80:83]\n"
      "ds_read_b128 a[204:207], v0 offset:4224\n"
      "v_mfma_f32_16x16x32_bf16 v[56:59], v[244:247], a[208:211], 0\n"
      "v_cvt_pk_bf16_f32 v156, v36, v40\n"
      "v_cvt_pk_bf16_f32 v157, v37, v41\n"
      "v_mfma_f32_16x16x32_bf16 v[60:63], v[236:239], a[208:211], 0\n"
      "v_cvt_pk_bf16_f32 v158, v38, v42\n"
      "v_cvt_pk_bf16_f32 v159, v39, v43\n"
      "v_mfma_f32_16x16x32_bf16 v[64:67], v[248:251], a[212:215], 0\n"
      "s_nop 0\n"
      "v_cvt_pk_bf16_f32 v152, v28, v32\n"
      "v_cvt_pk_bf16_f32 v153, v29, v33\n"
      "v_mfma_f32_16x16x32_bf16 v[68:71], v[240:243], a[212:215], 0\n"
      "s_nop 0\n"
      "v_cvt_pk_bf16_f32 v154, v30, v34\n"
      "v_cvt_pk_bf16_f32 v155, v31, v35\n"
      "v_mfma_f32_4x4x4_16b_bf16 v[28:31], a[244:245], a[56:57], 0\n"
      "v_mfma_f32_4x4x4_16b_bf16 v[32:35], a[244:245], a[58:59], 0\n"
      "ds_read_b64_tr_b16 a[64:65], v97 offset:20032\n"
      "v_mfma_f32_4x4x4_16b_bf16 v[28:31], a[248:249], a[240:241], v[28:31]\n"
      "ds_read_b64_tr_b16 a[66:67], v97 offset:20800\n"
      "v_mfma_f32_4x4x4_16b_bf16 v[32:35], a[248:249], a[242:243], v[32:35]\n"
      "ds_read_b64_tr_b16 a[232:233], v95 offset:64\n"
      "v_mfma_f32_4x4x4_16b_bf16 v[36:39], a[246:247], a[236:237], v[28:31]\n"
      "ds_read_b64_tr_b16 a[234:235], v95 offset:832\n"
      "v_mfma_f32_4x4x4_16b_bf16 v[40:43], a[246:247], a[238:239], v[32:35]\n"
      "ds_read_b64_tr_b16 a[228:229], v97 offset:26176\n"
      "v_mfma_f32_4x4x4_16b_bf16 v[28:31], a[250:251], a[236:237], v[28:31]\n"
      "ds_read_b64_tr_b16 a[230:231], v97 offset:26944\n"
      "v_mfma_f32_4x4x4_16b_bf16 v[32:35], a[250:251], a[238:239], v[32:35]\n"
      "ds_read_b128 a[208:211], v1 offset:0\n"
      "v_mfma_f32_16x16x32_bf16 v[72:75], v[232:235], a[212:215], 0\n"
      "s_nop 0\n"
      "v_cvt_pk_bf16_f32 v148, v84, v88\n"
      "v_cvt_pk_bf16_f32 v149, v85, v89\n"
      "v_mfma_f32_16x16x32_bf16 v[52:55], v[228:231], a[192:195], v[52:55]\n"
      "s_nop 0\n"
      "v_cvt_pk_bf16_f32 v150, v86, v90\n"
      "v_cvt_pk_bf16_f32 v151, v87, v91\n"
      "v_mfma_f32_16x16x32_bf16 v[56:59], v[220:223], a[192:195], v[56:59]\n"
      "s_nop 0\n"
      "v_cvt_pk_bf16_f32 v144, v76, v80\n"
      "v_cvt_pk_bf16_f32 v145, v77, v81\n"
      "v_mfma_f32_16x16x32_bf16 v[60:63], v[212:215], a[192:195], v[60:63]\n"
      "s_add_u32 m0, s63, 53504\n"
      "v_cvt_pk_bf16_f32 v146, v78, v82\n"
      "v_cvt_pk_bf16_f32 v147, v79, v83\n"
      "s_add_u32 s52, s63, 153600\n"
      "s_waitcnt lgkmcnt(0)\n"
      "v_mfma_f32_4x4x4_16b_bf16 v[76:79], a[244:245], a[60:61], 0\n"
      "v_mfma_f32_4x4x4_16b_bf16 v[80:83], a[244:245], a[62:63], 0\n"
      "buffer_load_dwordx4 v92, s[12:15], s52 offen offset:0 lds\n"
      "v_mfma_f32_4x4x4_16b_bf16 v[76:79], a[248:249], a[224:225], v[76:79]\n"
      "ds_read_b64_tr_b16 a[68:69], v97 offset:20096\n"
      "v_mfma_f32_4x4x4_16b_bf16 v[80:83], a[248:249], a[226:227], v[80:83]\n"
      "buffer_load_dwordx4 v93, s[12:15], s52 offen offset:1024 lds\n"
      "v_mfma_f32_4x4x4_16b_bf16 v[84:87], a[246:247], a[220:221], v[76:79]\n"
      "ds_read_b64_tr_b16 a[70:71], v97 offset:20864\n"
      "v_mfma_f32_4x4x4_16b_bf16 v[88:91], a[246:247], a[222:223], v[80:83]\n"
      "buffer_load_dwordx4 v94, s[12:15], s52 offen offset:2048 lds\n"
      "v_mfma_f32_4x4x4_16b_bf16 v[76:79], a[250:251], a[220:221], v[76:79]\n"
      "ds_read_b64_tr_b16 a[240:241], v95 offset:128\n"
      "v_mfma_f32_4x4x4_16b_bf16 v[80:83], a[250:251], a[222:223], v[80:83]\n"
      "ds_read_b64_tr_b16 a[242:243], v95 offset:896\n"
      "ds_read_b64_tr_b16 a[236:237], v97 offset:26240\n"
      "ds_read_b64_tr_b16 a[238:239], v97 offset:27008\n"
      "ds_read_b128 a[212:215], v1 offset:4096\n"
      "ds_read_b128 a[192:195], v4 offset:55232\n"
      "v_mfma_f32_16x16x32_bf16 v[64:67], v[224:227], a[196:199], v[64:67]\n"
      "v_cvt_pk_bf16_f32 v140, v36, v40\n"
      "v_cvt_pk_bf16_f32 v141, v37, v41\n"
      "v_mfma_f32_16x16x32_bf16 v[68:71], v[216:219], a[196:199], v[68:71]\n"
      "v_cvt_pk_bf16_f32 v142, v38, v42\n"
      "v_cvt_pk_bf16_f32 v143, v39, v43\n"
      "v_mfma_f32_16x16x32_bf16 v[72:75], v[208:211], a[196:199], v[72:75]\n"
      "v_cvt_pk_bf16_f32 v136, v28, v32\n"
      "v_cvt_pk_bf16_f32 v137, v29, v33\n"
      "v_mfma_f32_16x16x32_bf16 v[52:55], v[204:207], a[200:203], v[52:55]\n"
      "s_waitcnt vmcnt(3)\n"
      "v_cvt_pk_bf16_f32 v138, v30, v34\n"
      "v_cvt_pk_bf16_f32 v139, v31, v35\n"
      "s_barrier\n"
      "s_add_u32 m0, s63, 71936\n"
      "s_add_u32 s52, s63, 159744\n"
      "v_mfma_f32_4x4x4_16b_bf16 v[28:31], a[244:245], a[64:65], 0\n"
      "v_mfma_f32_4x4x4_16b_bf16 v[32:35], a[244:245], a[66:67], 0\n"
      "buffer_load_dwordx4 v92, s[12:15], s52 offen offset:0 lds\n"
      "v_mfma_f32_4x4x4_16b_bf16 v[28:31], a[248:249], a[232:233], v[28:31]\n"
      "ds_read_b64_tr_b16 a[72:73], v95 offset:6144\n"
      "v_mfma_f32_4x4x4_16b_bf16 v[32:35], a[248:249], a[234:235], v[32:35]\n"
      "buffer_load_dwordx4 v93, s[12:15], s52 offen offset:1024 lds\n"
      "v_mfma_f32_4x4x4_16b_bf16 v[36:39], a[246:247], a[228:229], v[28:31]\n"
      "ds_read_b64_tr_b16 a[74:75], v95 offset:6912\n"
      "v_mfma_f32_4x4x4_16b_bf16 v[40:43], a[246:247], a[230:231], v[32:35]\n"
      "buffer_load_dwordx4 v94, s[12:15], s52 offen offset:2048 lds\n"
      "v_mfma_f32_4x4x4_16b_bf16 v[28:31], a[250:251], a[228:229], v[28:31]\n"
      "ds_read_b64_tr_b16 a[224:225], v95 offset:18432\n"
      "v_mfma_f32_4x4x4_16b_bf16 v[32:35], a[250:251], a[230:231], v[32:35]\n"
      "ds_read_b64_tr_b16 a[226:227], v95 offset:19200\n"
      "ds_read_b64_tr_b16 a[220:221], v95 offset:12288\n"
      "ds_read_b64_tr_b16 a[222:223], v95 offset:13056\n"
      "ds_read_b128 a[196:199], v1 offset:64\n"
      "v_mfma_f32_16x16x32_bf16 v[56:59], v[196:199], a[200:203], v[56:59]\n"
      "v_cvt_pk_bf16_f32 v132, v84, v88\n"
      "v_cvt_pk_bf16_f32 v133, v85, v89\n"
      "v_mfma_f32_16x16x32_bf16 v[60:63], v[188:191], a[200:203], v[60:63]\n"
      "v_cvt_pk_bf16_f32 v134, v86, v90\n"
      "v_cvt_pk_bf16_f32 v135, v87, v91\n"
      "v_mfma_f32_16x16x32_bf16 v[64:67], v[200:203], a[204:207], v[64:67]\n"
      "s_nop 0\n"
      "v_cvt_pk_bf16_f32 v128, v76, v80\n"
      "v_cvt_pk_bf16_f32 v129, v77, v81\n"
      "v_mfma_f32_16x16x32_bf16 v[68:71], v[192:195], a[204:207], v[68:71]\n"
      "s_waitcnt lgkmcnt(2)\n"
      "v_cvt_pk_bf16_f32 v130, v78, v82\n"
      "v_cvt_pk_bf16_f32 v131, v79, v83\n"
      "v_mfma_f32_4x4x4_16b_bf16 v[76:79], a[244:245], a[68:69], 0\n"
      "v_mfma_f32_4x4x4_16b_bf16 v[80:83], a[244:245], a[70:71], 0\n"
      "ds_read_b64_tr_b16 a[76:77], v95 offset:6208\n"
      "v_mfma_f32_4x4x4_16b_bf16 v[76:79], a[248:249], a[240:241], v[76:79]\n"
      "ds_read_b64_tr_b16 a[78:79], v95 offset:6976\n"
      "v_mfma_f32_4x4x4_16b_bf16 v[80:83], a[248:249], a[242:243], v[80:83]\n"
      "ds_read_b64_tr_b16 a[232:233], v95 offset:18496\n"
      "v_mfma_f32_4x4x4_16b_bf16 v[84:87], a[246:247], a[236:237], v[76:79]\n"
      "ds_read_b64_tr_b16 a[234:235], v95 offset:19264\n"
      "v_mfma_f32_4x4x4_16b_bf16 v[88:91], a[246:247], a[238:239], v[80:83]\n"
      "ds_read_b64_tr_b16 a[228:229], v95 offset:12352\n"
      "v_mfma_f32_4x4x4_16b_bf16 v[76:79], a[250:251], a[236:237], v[76:79]\n"
      "ds_read_b64_tr_b16 a[230:231], v95 offset:13120\n"
      "v_mfma_f32_4x4x4_16b_bf16 v[80:83], a[250:251], a[238:239], v[80:83]\n"
      "ds_read_b128 a[200:203], v1 offset:4160\n"
      "v_mfma_f32_16x16x32_bf16 v[72:75], v[184:187], a[204:207], v[72:75]\n"
      "v_cvt_pk_bf16_f32 v124, v36, v40\n"
      "v_cvt_pk_bf16_f32 v125, v37, v41\n"
      "v_mfma_f32_16x16x32_bf16 v[52:55], v[180:183], a[208:211], v[52:55]\n"
      "v_cvt_pk_bf16_f32 v126, v38, v42\n"
      "v_cvt_pk_bf16_f32 v127, v39, v43\n"
      "v_mfma_f32_16x16x32_bf16 v[56:59], v[172:175], a[208:211], v[56:59]\n"
      "v_cvt_pk_bf16_f32 v28, v28, v32\n"
      "v_cvt_pk_bf16_f32 v29, v29, v33\n"
      "v_mfma_f32_16x16x32_bf16 v[60:63], v[164:167], a[208:211], v[60:63]\n"
      "v_cvt_pk_bf16_f32 v30, v30, v34\n"
      "v_cvt_pk_bf16_f32 v31, v31, v35\n"
      "v_mfma_f32_16x16x32_bf16 v[64:67], v[176:179], a[212:215], v[64:67]\n"
      "s_add_u32 m0, s37, 116224\n"
      "v_accvgpr_write_b32 a188, v28\n"
      "v_accvgpr_write_b32 a189, v29\n"
      "v_mfma_f32_16x16x32_bf16 v[68:71], v[168:171], a[212:215], v[68:71]\n"
      "s_add_u32 s53, s54, 983040\n"
      "v_accvgpr_write_b32 a190, v30\n"
      "v_accvgpr_write_b32 a191, v31\n"
      "v_mfma_f32_4x4x4_16b_bf16 v[28:31], a[244:245], a[72:73], 0\n"
      "v_mfma_f32_4x4x4_16b_bf16 v[32:35], a[244:245], a[74:75], 0\n"
      "buffer_load_dwordx4 v8, s[4:7], s53 offen offset:0 nt lds\n"
      "buffer_load_dwordx4 v9, s[4:7], s53 offen offset:1024 nt lds\n"
      "v_mfma_f32_4x4x4_16b_bf16 v[28:31], a[248:249], a[224:225], v[28:31]\n"
      "ds_read_b64_tr_b16 a[80:81], v95 offset:6272\n"
      "v_mfma_f32_4x4x4_16b_bf16 v[32:35], a[248:249], a[226:227], v[32:35]\n"
      "ds_read_b64_tr_b16 a[82:83], v95 offset:7040\n"
      "s_nop 0\n"
      "s_waitcnt lgkmcnt(2)\n"
      "v_mfma_f32_4x4x4_16b_bf16 v[36:39], a[246:247], a[220:221], v[28:31]\n"
      "ds_read_b64_tr_b16 a[240:241], v95 offset:18560\n"
      "v_mfma_f32_4x4x4_16b_bf16 v[40:43], a[246:247], a[222:223], v[32:35]\n"
      "ds_read_b64_tr_b16 a[242:243], v95 offset:19328\n"
      "v_mfma_f32_4x4x4_16b_bf16 v[28:31], a[250:251], a[220:221], v[28:31]\n"
      "ds_read_b64_tr_b16 a[236:237], v95 offset:12416\n"
      "v_mfma_f32_4x4x4_16b_bf16 v[32:35], a[250:251], a[222:223], v[32:35]\n"
      "ds_read_b64_tr_b16 a[238:239], v95 offset:13184\n"
      "ds_read_b128 a[204:207], v4 offset:55296\n"
      "ds_read_b128 a[208:211], v1 offset:128\n"
      "v_cvt_pk_bf16_f32 v84, v84, v88\n"
      "v_cvt_pk_bf16_f32 v85, v85, v89\n"
      "v_cvt_pk_bf16_f32 v86, v86, v90\n"
      "v_cvt_pk_bf16_f32 v87, v87, v91\n"
      "v_mfma_f32_16x16x32_bf16 v[72:75], v[160:163], a[212:215], v[72:75]\n"
      "s_nop 0\n"
      "v_accvgpr_write_b32 a184, v84\n"
      "v_accvgpr_write_b32 a185, v85\n"
      "v_mfma_f32_16x16x32_bf16 v[100:103], a[48:51], a[192:195], v[100:103]\n"
      "s_nop 0\n"
      "v_accvgpr_write_b32 a186, v86\n"
      "v_accvgpr_write_b32 a187, v87\n"
      "v_mfma_f32_16x16x32_bf16 v[104:107], a[52:55], a[192:195], v[104:107]\n"
      "s_nop 0\n"
      "v_cvt_pk_bf16_f32 v76, v76, v80\n"
      "v_cvt_pk_bf16_f32 v77, v77, v81\n"
      "v_mfma_f32_16x16x32_bf16 v[108:111], a[56:59], a[192:195], v[108:111]\n"
      "s_nop 0\n"
      "v_cvt_pk_bf16_f32 v78, v78, v82\n"
      "v_cvt_pk_bf16_f32 v79, v79, v83\n"
      "v_mfma_f32_16x16x32_bf16 v[52:55], v[156:159], a[196:199], v[52:55]\n"
      "v_accvgpr_write_b32 a180, v76\n"
      "v_accvgpr_write_b32 a181, v77\n"
      "v_mfma_f32_16x16x32_bf16 v[56:59], v[148:151], a[196:199], v[56:59]\n"
      "s_waitcnt vmcnt(5)\n"
      "v_accvgpr_write_b32 a182, v78\n"
      "v_accvgpr_write_b32 a183, v79\n"
      "s_barrier\n"
      "s_add_u32 s53, s54, 1179648\n"
      "s_add_u32 m0, s37, 120328\n"
      "v_mfma_f32_4x4x4_16b_bf16 v[76:79], a[244:245], a[76:77], 0\n"
      "v_mfma_f32_4x4x4_16b_bf16 v[80:83], a[244:245], a[78:79], 0\n"
      "buffer_load_dwordx4 v8, s[4:7], s53 offen offset:0 nt lds\n"
      "buffer_load_dwordx4 v9, s[4:7], s53 offen offset:1024 nt lds\n"
      "v_mfma_f32_4x4x4_16b_bf16 v[76:79], a[248:249], a[232:233], v[76:79]\n"
      "ds_read_b64_tr_b16 a[84:85], v95 offset:41216\n"
      "v_mfma_f32_4x4x4_16b_bf16 v[80:83], a[248:249], a[234:235], v[80:83]\n"
      "ds_read_b64_tr_b16 a[86:87], v95 offset:41984\n"
      "v_mfma_f32_4x4x4_16b_bf16 v[84:87], a[246:247], a[228:229], v[76:79]\n"
      "ds_read_b64_tr_b16 a[224:225], v95 offset:53504\n"
      "v_mfma_f32_4x4x4_16b_bf16 v[88:91], a[246:247], a[230:231], v[80:83]\n"
      "ds_read_b64_tr_b16 a[226:227], v95 offset:54272\n"
      "v_mfma_f32_4x4x4_16b_bf16 v[76:79], a[250:251], a[228:229], v[76:79]\n"
      "ds_read_b64_tr_b16 a[220:221], v95 offset:47360\n"
      "v_mfma_f32_4x4x4_16b_bf16 v[80:83], a[250:251], a[230:231], v[80:83]\n"
      "ds_read_b64_tr_b16 a[222:223], v95 offset:48128\n"
      "ds_read_b128 a[212:215], v1 offset:4224\n"
      "ds_read_b128 a[192:195], v5 offset:55168\n"
      "v_cvt_pk_bf16_f32 v36, v36, v40\n"
      "v_cvt_pk_bf16_f32 v37, v37, v41\n"
      "v_cvt_pk_bf16_f32 v38, v38, v42\n"
      "v_cvt_pk_bf16_f32 v39, v39, v43\n"
      "v_mfma_f32_16x16x32_bf16 v[60:63], v[140:143], a[196:199], v[60:63]\n"
      "s_nop 0\n"
      "v_accvgpr_write_b32 a176, v36\n"
      "v_accvgpr_write_b32 a177, v37\n"
      "v_mfma_f32_16x16x32_bf16 v[64:67], v[152:155], a[200:203], v[64:67]\n"
      "s_nop 0\n"
      "v_accvgpr_write_b32 a178, v38\n"
      "v_accvgpr_write_b32 a179, v39\n"
      "v_mfma_f32_16x16x32_bf16 v[68:71], v[144:147], a[200:203], v[68:71]\n"
      "s_nop 0\n"
      "v_cvt_pk_bf16_f32 v28, v28, v32\n"
      "v_cvt_pk_bf16_f32 v29, v29, v33\n"
      "v_mfma_f32_16x16x32_bf16 v[72:75], v[136:139], a[200:203], v[72:75]\n"
      "s_waitcnt lgkmcnt(1)\n"
      "v_cvt_pk_bf16_f32 v30, v30, v34\n"
      "v_cvt_pk_bf16_f32 v31, v31, v35\n"
      "v_mfma_f32_16x16x32_bf16 v[100:103], a[60:63], a[204:207], v[100:103]\n"
      "s_add_u32 m0, s37, 124552\n"
      "v_accvgpr_write_b32 a172, v28\n"
      "v_accvgpr_write_b32 a173, v29\n"
      "v_mfma_f32_16x16x32_bf16 v[104:107], a[64:67], a[204:207], v[104:107]\n"
      "s_add_u32 s53, s54, 1376256\n"
      "v_accvgpr_write_b32 a174, v30\n"
      "v_accvgpr_write_b32 a175, v31\n"
      "v_mfma_f32_4x4x4_16b_bf16 v[28:31], a[244:245], a[80:81], 0\n"
      "v_mfma_f32_4x4x4_16b_bf16 v[32:35], a[244:245], a[82:83], 0\n"
      "buffer_load_dwordx4 v8, s[4:7], s53 offen offset:0 nt lds\n"
      "buffer_load_dwordx4 v9, s[4:7], s53 offen offset:1024 nt lds\n"
      "v_mfma_f32_4x4x4_16b_bf16 v[28:31], a[248:249], a[240:241], v[28:31]\n"
      "ds_read_b64_tr_b16 a[88:89], v95 offset:41280\n"
      "v_mfma_f32_4x4x4_16b_bf16 v[32:35], a[248:249], a[242:243], v[32:35]\n"
      "ds_read_b64_tr_b16 a[90:91], v95 offset:42048\n"
      "v_mfma_f32_4x4x4_16b_bf16 v[36:39], a[246:247], a[236:237], v[28:31]\n"
      "ds_read_b64_tr_b16 a[232:233], v95 offset:53568\n"
      "v_mfma_f32_4x4x4_16b_bf16 v[40:43], a[246:247], a[238:239], v[32:35]\n"
      "ds_read_b64_tr_b16 a[234:235], v95 offset:54336\n"
      "v_mfma_f32_4x4x4_16b_bf16 v[28:31], a[250:251], a[236:237], v[28:31]\n"
      "ds_read_b64_tr_b16 a[228:229], v95 offset:47424\n"
      "v_mfma_f32_4x4x4_16b_bf16 v[32:35], a[250:251], a[238:239], v[32:35]\n"
      "ds_read_b64_tr_b16 a[230:231], v95 offset:48192\n"
      "ds_read_b128 a[196:199], v2 offset:0\n"
      "ds_read_b128 a[200:203], v2 offset:4096\n"
      "v_cvt_pk_bf16_f32 v84, v84, v88\n"
      "v_cvt_pk_bf16_f32 v85, v85, v89\n"
      "v_cvt_pk_bf16_f32 v86, v86, v90\n"
      "v_cvt_pk_bf16_f32 v87, v87, v91\n"
      "v_mfma_f32_16x16x32_bf16 v[108:111], a[68:71], a[204:207], v[108:111]\n"
      "v_accvgpr_write_b32 a168, v84\n"
      "v_accvgpr_write_b32 a169, v85\n"
      "v_mfma_f32_16x16x32_bf16 v[52:55], v[132:135], a[208:211], v[52:55]\n"
      "v_accvgpr_write_b32 a170, v86\n"
      "v_accvgpr_write_b32 a171, v87\n"
      "v_mfma_f32_16x16x32_bf16 v[56:59], v[124:127], a[208:211], v[56:59]\n"
      "v_cvt_pk_bf16_f32 v76, v76, v80\n"
      "v_cvt_pk_bf16_f32 v77, v77, v81\n"
      "v_mfma_f32_16x16x32_bf16 v[60:63], a[184:187], a[208:211], v[60:63]\n"
      "v_cvt_pk_bf16_f32 v78, v78, v82\n"
      "v_cvt_pk_bf16_f32 v79, v79, v83\n"
      "v_mfma_f32_16x16x32_bf16 v[64:67], v[128:131], a[212:215], v[64:67]\n"
      "s_add_u32 m0, s37, 128896\n"
      "v_accvgpr_write_b32 a164, v76\n"
      "v_accvgpr_write_b32 a165, v77\n"
      "v_mfma_f32_16x16x32_bf16 v[68:71], a[188:191], a[212:215], v[68:71]\n"
      "s_add_u32 s53, s54, 1572864\n"
      "v_accvgpr_write_b32 a166, v78\n"
      "v_accvgpr_write_b32 a167, v79\n"
      "v_mfma_f32_4x4x4_16b_bf16 v[76:79], a[244:245], a[84:85], 0\n"
      "v_mfma_f32_4x4x4_16b_bf16 v[80:83], a[244:245], a[86:87], 0\n"
      "buffer_load_dwordx4 v8, s[4:7], s53 offen offset:0 nt lds\n"
      "buffer_load_dwordx4 v9, s[4:7], s53 offen offset:1024 nt lds\n"
      "v_mfma_f32_4x4x4_16b_bf16 v[76:79], a[248:249], a[224:225], v[76:79]\n"
      "ds_read_b64_tr_b16 a[92:93], v95 offset:41344\n"
      "v_mfma_f32_4x4x4_16b_bf16 v[80:83], a[248:249], a[226:227], v[80:83]\n"
      "ds_read_b64_tr_b16 a[94:95], v95 offset:42112\n"
      "v_mfma_f32_4x4x4_16b_bf16 v[84:87], a[246:247], a[220:221], v[76:79]\n"
      "ds_read_b64_tr_b16 a[240:241], v95 offset:53632\n"
      "v_mfma_f32_4x4x4_16b_bf16 v[88:91], a[246:247], a[222:223], v[80:83]\n"
      "ds_read_b64_tr_b16 a[242:243], v95 offset:54400\n"
      "v_mfma_f32_4x4x4_16b_bf16 v[76:79], a[250:251], a[220:221], v[76:79]\n"
      "ds_read_b64_tr_b16 a[236:237], v95 offset:47488\n"
      "v_mfma_f32_4x4x4_16b_bf16 v[80:83], a[250:251], a[222:223], v[80:83]\n"
      "ds_read_b64_tr_b16 a[238:239], v95 offset:48256\n"
      "v_cvt_pk_bf16_f32 v36, v36, v40\n"
      "v_cvt_pk_bf16_f32 v37, v37, v41\n"
      "v_cvt_pk_bf16_f32 v38, v38, v42\n"
      "v_cvt_pk_bf16_f32 v39, v39, v43\n"
      "v_accvgpr_write_b32 a160, v36\n"
      "v_accvgpr_write_b32 a161, v37\n"
      "v_accvgpr_write_b32 a162, v38\n"
      "v_accvgpr_write_b32 a163, v39\n"
      "v_mfma_f32_16x16x32_bf16 v[72:75], a[180:183], a[212:215], v[72:75]\n"
      "s_waitcnt lgkmcnt(2)\n"
      "v_cvt_pk_bf16_f32 v28, v28, v32\n"
      "v_cvt_pk_bf16_f32 v29, v29, v33\n"
      "v_mfma_f32_16x16x32_bf16 v[100:103], a[72:75], a[192:195], v[100:103]\n"
      "v_cvt_pk_bf16_f32 v30, v30, v34\n"
      "v_cvt_pk_bf16_f32 v31, v31, v35\n"
      "v_mfma_f32_16x16x32_bf16 v[104:107], a[76:79], a[192:195], v[104:107]\n"
      "s_nop 0\n"
      "v_accvgpr_write_b32 a156, v28\n"
      "v_accvgpr_write_b32 a157, v29\n"
      "v_mfma_f32_16x16x32_bf16 v[108:111], a[80:83], a[192:195], v[108:111]\n"
      "s_waitcnt vmcnt(8)\n"
      "v_accvgpr_write_b32 a158, v30\n"
      "v_accvgpr_write_b32 a159, v31\n"
      "s_barrier\n"
      "s_add_u32 s53, s54, 986112\n"
      "s_add_u32 m0, s37, 133120\n"
      "v_mfma_f32_4x4x4_16b_bf16 v[28:31], a[244:245], a[88:89], 0\n"
      "v_mfma_f32_4x4x4_16b_bf16 v[32:35], a[244:245], a[90:91], 0\n"
      "buffer_load_dwordx4 v10, s[4:7], s53 offen offset:0 nt lds\n"
      "buffer_load_dwordx4 v11, s[4:7], s53 offen offset:1024 nt lds\n"
      "v_mfma_f32_4x4x4_16b_bf16 v[28:31], a[248:249], a[232:233], v[28:31]\n"
      "ds_read_b64_tr_b16 a[96:97], v96 offset:2304\n"
      "v_mfma_f32_4x4x4_16b_bf16 v[32:35], a[248:249], a[234:235], v[32:35]\n"
      "ds_read_b64_tr_b16 a[98:99], v96 offset:3072\n"
      "v_mfma_f32_4x4x4_16b_bf16 v[36:39], a[246:247], a[228:229], v[28:31]\n"
      "ds_read_b64_tr_b16 a[224:225], v96 offset:14592\n"
      "v_mfma_f32_4x4x4_16b_bf16 v[40:43], a[246:247], a[230:231], v[32:35]\n"
      "ds_read_b64_tr_b16 a[226:227], v96 offset:15360\n"
      "v_mfma_f32_4x4x4_16b_bf16 v[28:31], a[250:251], a[228:229], v[28:31]\n"
      "ds_read_b64_tr_b16 a[220:221], v96 offset:8448\n"
      "v_mfma_f32_4x4x4_16b_bf16 v[32:35], a[250:251], a[230:231], v[32:35]\n"
      "ds_read_b64_tr_b16 a[222:223], v96 offset:9216\n"
      "ds_read_b128 a[204:207], v5 offset:55232\n"
      "v_cvt_pk_bf16_f32 v84, v84, v88\n"
      "v_cvt_pk_bf16_f32 v85, v85, v89\n"
      "v_cvt_pk_bf16_f32 v86, v86, v90\n"
      "v_cvt_pk_bf16_f32 v87, v87, v91\n"
      "v_mfma_f32_16x16x32_bf16 v[52:55], a[176:179], a[196:199], v[52:55]\n"
      "v_accvgpr_write_b32 a152, v84\n"
      "v_accvgpr_write_b32 a153, v85\n"
      "v_mfma_f32_16x16x32_bf16 v[56:59], a[168:171], a[196:199], v[56:59]\n"
      "v_accvgpr_write_b32 a154, v86\n"
      "v_accvgpr_write_b32 a155, v87\n"
      "v_mfma_f32_16x16x32_bf16 v[60:63], a[160:163], a[196:199], v[60:63]\n"
      "v_cvt_pk_bf16_f32 v76, v76, v80\n"
      "v_cvt_pk_bf16_f32 v77, v77, v81\n"
      "v_mfma_f32_16x16x32_bf16 v[64:67], a[172:175], a[200:203], v[64:67]\n"
      "v_cvt_pk_bf16_f32 v78, v78, v82\n"
      "v_cvt_pk_bf16_f32 v79, v79, v83\n"
      "v_mfma_f32_16x16x32_bf16 v[68:71], a[164:167], a[200:203], v[68:71]\n"
      "s_add_u32 m0, s37, 137224\n"
      "v_accvgpr_write_b32 a148, v76\n"
      "v_accvgpr_write_b32 a149, v77\n"
      "v_mfma_f32_16x16x32_bf16 v[72:75], a[156:159], a[200:203], v[72:75]\n"
      "s_add_u32 s53, s54, 1182720\n"
      "v_accvgpr_write_b32 a150, v78\n"
      "v_accvgpr_write_b32 a151, v79\n"
      "v_mfma_f32_4x4x4_16b_bf16 v[76:79], a[244:245], a[92:93], 0\n"
      "v_mfma_f32_4x4x4_16b_bf16 v[80:83], a[244:245], a[94:95], 0\n"
      "buffer_load_dwordx4 v10, s[4:7], s53 offen offset:0 nt lds\n"
      "buffer_load_dwordx4 v11, s[4:7], s53 offen offset:1024 nt lds\n"
      "v_mfma_f32_4x4x4_16b_bf16 v[76:79], a[248:249], a[240:241], v[76:79]\n"
      "ds_read_b64_tr_b16 a[100:101], v96 offset:2368\n"
      "v_mfma_f32_4x4x4_16b_bf16 v[80:83], a[248:249], a[242:243], v[80:83]\n"
      "ds_read_b64_tr_b16 a[102:103], v96 offset:3136\n"
      "s_nop 0\n"
      "s_waitcnt lgkmcnt(2)\n"
      "v_mfma_f32_4x4x4_16b_bf16 v[84:87], a[246:247], a[236:237], v[76:79]\n"
      "ds_read_b64_tr_b16 a[232:233], v96 offset:14656\n"
      "v_mfma_f32_4x4x4_16b_bf16 v[88:91], a[246:247], a[238:239], v[80:83]\n"
      "ds_read_b64_tr_b16 a[234:235], v96 offset:15424\n"
      "v_mfma_f32_4x4x4_16b_bf16 v[76:79], a[250:251], a[236:237], v[76:79]\n"
      "ds_read_b64_tr_b16 a[228:229], v96 offset:8512\n"
      "v_mfma_f32_4x4x4_16b_bf16 v[80:83], a[250:251], a[238:239], v[80:83]\n"
      "ds_read_b64_tr_b16 a[230:231], v96 offset:9280\n"
      "ds_read_b128 a[208:211], v2 offset:64\n"
      "ds_read_b128 a[212:215], v2 offset:4160\n"
      "v_cvt_pk_bf16_f32 v36, v36, v40\n"
      "v_cvt_pk_bf16_f32 v37, v37, v41\n"
      "v_cvt_pk_bf16_f32 v38, v38, v42\n"
      "v_cvt_pk_bf16_f32 v39, v39, v43\n"
      "v_accvgpr_write_b32 a144, v36\n"
      "v_accvgpr_write_b32 a145, v37\n"
      "v_accvgpr_write_b32 a146, v38\n"
      "v_accvgpr_write_b32 a147, v39\n"
      "v_cvt_pk_bf16_f32 v28, v28, v32\n"
      "v_cvt_pk_bf16_f32 v29, v29, v33\n"
      "v_cvt_pk_bf16_f32 v30, v30, v34\n"
      "v_cvt_pk_bf16_f32 v31, v31, v35\n"
      "v_accvgpr_write_b32 a140, v28\n"
      "v_accvgpr_write_b32 a141, v29\n"
      "v_accvgpr_write_b32 a142, v30\n"
      "v_accvgpr_write_b32 a143, v31\n"
      "s_add_u32 s53, s54, 1379328\n"
      "s_add_u32 m0, s37, 141448\n"
      "v_mfma_f32_4x4x4_16b_bf16 v[28:31], a[244:245], a[96:97], 0\n"
      "v_mfma_f32_4x4x4_16b_bf16 v[32:35], a[244:245], a[98:99], 0\n"
      "buffer_load_dwordx4 v10, s[4:7], s53 offen offset:0 nt lds\n"
      "buffer_load_dwordx4 v11, s[4:7], s53 offen offset:1024 nt lds\n"
      "v_mfma_f32_4x4x4_16b_bf16 v[28:31], a[248:249], a[224:225], v[28:31]\n"
      "ds_read_b64_tr_b16 a[104:105], v96 offset:2432\n"
      "v_mfma_f32_4x4x4_16b_bf16 v[32:35], a[248:249], a[226:227], v[32:35]\n"
      "ds_read_b64_tr_b16 a[106:107], v96 offset:3200\n"
      "v_mfma_f32_4x4x4_16b_bf16 v[36:39], a[246:247], a[220:221], v[28:31]\n"
      "ds_read_b64_tr_b16 a[240:241], v96 offset:14720\n"
      "v_mfma_f32_4x4x4_16b_bf16 v[40:43], a[246:247], a[222:223], v[32:35]\n"
      "ds_read_b64_tr_b16 a[242:243], v96 offset:15488\n"
      "v_mfma_f32_4x4x4_16b_bf16 v[28:31], a[250:251], a[220:221], v[28:31]\n"
      "ds_read_b64_tr_b16 a[236:237], v96 offset:8576\n"
      "v_mfma_f32_4x4x4_16b_bf16 v[32:35], a[250:251], a[222:223], v[32:35]\n"
      "ds_read_b64_tr_b16 a[238:239], v96 offset:9344\n"
      "v_cvt_pk_bf16_f32 v84, v84, v88\n"
      "v_cvt_pk_bf16_f32 v85, v85, v89\n"
      "v_cvt_pk_bf16_f32 v86, v86, v90\n"
      "v_cvt_pk_bf16_f32 v87, v87, v91\n"
      "v_accvgpr_write_b32 a136, v84\n"
      "v_accvgpr_write_b32 a137, v85\n"
      "v_accvgpr_write_b32 a138, v86\n"
      "v_accvgpr_write_b32 a139, v87\n"
      "v_cvt_pk_bf16_f32 v76, v76, v80\n"
      "v_cvt_pk_bf16_f32 v77, v77, v81\n"
      "v_mfma_f32_16x16x32_bf16 v[100:103], a[84:87], a[204:207], v[100:103]\n"
      "v_cvt_pk_bf16_f32 v78, v78, v82\n"
      "v_cvt_pk_bf16_f32 v79, v79, v83\n"
      "v_mfma_f32_16x16x32_bf16 v[104:107], a[88:91], a[204:207], v[104:107]\n"
      "s_add_u32 m0, s37, 145792\n"
      "v_accvgpr_write_b32 a132, v76\n"
      "v_accvgpr_write_b32 a133, v77\n"
      "v_mfma_f32_16x16x32_bf16 v[108:111], a[92:95], a[204:207], v[108:111]\n"
      "s_add_u32 s53, s54, 1575936\n"
      "v_accvgpr_write_b32 a134, v78\n"
      "v_accvgpr_write_b32 a135, v79\n"
      "s_waitcnt lgkmcnt(0)\n"
      "v_mfma_f32_4x4x4_16b_bf16 v[76:79], a[244:245], a[100:101], 0\n"
      "v_mfma_f32_4x4x4_16b_bf16 v[80:83], a[244:245], a[102:103], 0\n"
      "buffer_load_dwordx4 v10, s[4:7], s53 offen offset:0 nt lds\n"
      "buffer_load_dwordx4 v11, s[4:7], s53 offen offset:1024 nt lds\n"
      "v_mfma_f32_4x4x4_16b_bf16 v[76:79], a[248:249], a[232:233], v[76:79]\n"
      "ds_read_b128 a[192:195], v5 offset:55296\n"
      "v_mfma_f32_4x4x4_16b_bf16 v[80:83], a[248:249], a[234:235], v[80:83]\n"
      "v_mfma_f32_4x4x4_16b_bf16 v[84:87], a[246:247], a[228:229], v[76:79]\n"
      "s_nop 0\n"
      "v_mfma_f32_4x4x4_16b_bf16 v[88:91], a[246:247], a[230:231], v[80:83]\n"
      "v_mfma_f32_4x4x4_16b_bf16 v[76:79], a[250:251], a[228:229], v[76:79]\n"
      "v_mfma_f32_4x4x4_16b_bf16 v[80:83], a[250:251], a[230:231], v[80:83]\n"
      "v_cvt_pk_bf16_f32 v36, v36, v40\n"
      "v_cvt_pk_bf16_f32 v37, v37, v41\n"
      "v_cvt_pk_bf16_f32 v38, v38, v42\n"
      "v_cvt_pk_bf16_f32 v39, v39, v43\n"
      "v_mfma_f32_16x16x32_bf16 v[52:55], a[152:155], a[208:211], v[52:55]\n"
      "v_accvgpr_write_b32 a128, v36\n"
      "v_accvgpr_write_b32 a129, v37\n"
      "v_mfma_f32_16x16x32_bf16 v[56:59], a[144:147], a[208:211], v[56:59]\n"
      "v_accvgpr_write_b32 a130, v38\n"
      "v_accvgpr_write_b32 a131, v39\n"
      "v_mfma_f32_16x16x32_bf16 v[60:63], a[136:139], a[208:211], v[60:63]\n"
      "v_cvt_pk_bf16_f32 v28, v28, v32\n"
      "v_cvt_pk_bf16_f32 v29, v29, v33\n"
      "v_mfma_f32_16x16x32_bf16 v[64:67], a[148:151], a[212:215], v[64:67]\n"
      "v_cvt_pk_bf16_f32 v30, v30, v34\n"
      "v_cvt_pk_bf16_f32 v31, v31, v35\n"
      "v_mfma_f32_16x16x32_bf16 v[68:71], a[140:143], a[212:215], v[68:71]\n"
      "v_accvgpr_write_b32 a124, v28\n"
      "v_accvgpr_write_b32 a125, v29\n"
      "v_mfma_f32_16x16x32_bf16 v[72:75], a[132:135], a[212:215], v[72:75]\n"
      "v_accvgpr_write_b32 a126, v30\n"
      "v_accvgpr_write_b32 a127, v31\n"
      "v_mfma_f32_4x4x4_16b_bf16 v[28:31], a[244:245], a[104:105], 0\n"
      "v_mfma_f32_4x4x4_16b_bf16 v[32:35], a[244:245], a[106:107], 0\n"
      "ds_read_b128 a[196:199], v2 offset:128\n"
      "v_mfma_f32_4x4x4_16b_bf16 v[28:31], a[248:249], a[240:241], v[28:31]\n"
      "ds_read_b128 a[200:203], v2 offset:4224\n"
      "v_mfma_f32_4x4x4_16b_bf16 v[32:35], a[248:249], a[242:243], v[32:35]\n"
      "v_mfma_f32_4x4x4_16b_bf16 v[36:39], a[246:247], a[236:237], v[28:31]\n"
      "s_nop 0\n"
      "s_nop 0\n"
      "v_mfma_f32_4x4x4_16b_bf16 v[40:43], a[246:247], a[238:239], v[32:35]\n"
      "v_mfma_f32_4x4x4_16b_bf16 v[28:31], a[250:251], a[236:237], v[28:31]\n"
      "v_mfma_f32_4x4x4_16b_bf16 v[32:35], a[250:251], a[238:239], v[32:35]\n"
      "v_cvt_pk_bf16_f32 v84, v84, v88\n"
      "v_cvt_pk_bf16_f32 v85, v85, v89\n"
      "v_cvt_pk_bf16_f32 v86, v86, v90\n"
      "v_cvt_pk_bf16_f32 v87, v87, v91\n"
      "v_accvgpr_write_b32 a120, v84\n"
      "v_accvgpr_write_b32 a121, v85\n"
      "v_accvgpr_write_b32 a122, v86\n"
      "v_accvgpr_write_b32 a123, v87\n"
      "v_cvt_pk_bf16_f32 v76, v76, v80\n"
      "v_cvt_pk_bf16_f32 v77, v77, v81\n"
      "v_cvt_pk_bf16_f32 v78, v78, v82\n"
      "v_cvt_pk_bf16_f32 v79, v79, v83\n"
      "v_accvgpr_write_b32 a116, v76\n"
      "v_accvgpr_write_b32 a117, v77\n"
      "v_accvgpr_write_b32 a118, v78\n"
      "v_accvgpr_write_b32 a119, v79\n"
      "s_waitcnt lgkmcnt(0)\n"
      "s_waitcnt vmcnt(14)\n"
      "s_barrier\n"
      "s_mov_b32 m0, s64\n"
      "s_nop 0\n"
      "s_nop 0\n"
      "v_mfma_f32_16x16x32_bf16 v[100:103], a[96:99], a[192:195], v[100:103]\n"
      "s_add_u32 s53, s54, 1769472\n"
      "ds_read_b128 a[204:207], v3 offset:59264\n"
      "ds_read_b64_tr_b16 a[220:221], v7 offset:18944\n"
      "ds_read_b64_tr_b16 a[222:223], v7 offset:18952\n"
      "v_cvt_pk_bf16_f32 v36, v36, v40\n"
      "v_cvt_pk_bf16_f32 v37, v37, v41\n"
      "v_cvt_pk_bf16_f32 v38, v38, v42\n"
      "v_mfma_f32_16x16x32_bf16 v[104:107], a[100:103], a[192:195], v[104:107]\n"
      "s_add_u32 m0, s37, 82432\n"
      "ds_read_b128 a[208:211], v3 offset:59328\n"
      "v_cvt_pk_bf16_f32 v39, v39, v43\n"
      "v_accvgpr_write_b32 a112, v36\n"
      "v_accvgpr_write_b32 a113, v37\n"
      "v_accvgpr_write_b32 a114, v38\n"
      "v_accvgpr_write_b32 a115, v39\n"
      "v_mfma_f32_16x16x32_bf16 v[108:111], a[104:107], a[192:195], v[108:111]\n"
      "buffer_load_dwordx4 v8, s[4:7], s53 offen offset:0 nt lds\n"
      "buffer_load_dwordx4 v9, s[4:7], s53 offen offset:1024 nt lds\n"
      "v_cvt_pk_bf16_f32 v28, v28, v32\n"
      "v_cvt_pk_bf16_f32 v29, v29, v33\n"
      "v_mfma_f32_16x16x32_bf16 v[52:55], a[128:131], a[196:199], v[52:55]\n"
      "s_mov_b32 m0, s64\n"
      "ds_read_b128 a[212:215], v3 offset:59392\n"
      "v_cvt_pk_bf16_f32 v30, v30, v34\n"
      "v_cvt_pk_bf16_f32 v31, v31, v35\n"
      "v_accvgpr_write_b32 a108, v28\n"
      "v_accvgpr_write_b32 a109, v29\n"
      "v_accvgpr_write_b32 a110, v30\n"
      "v_accvgpr_write_b32 a111, v31\n"
      "v_mfma_f32_16x16x32_bf16 v[56:59], a[120:123], a[196:199], v[56:59]\n"
      "s_nop 0\n"
      "v_mfma_f32_16x16x32_bf16 v[60:63], a[112:115], a[196:199], v[60:63]\n"
      "s_add_u32 s53, s54, 1966080\n"
      "v_mfma_f32_16x16x32_bf16 v[64:67], a[124:127], a[200:203], v[64:67]\n"
      "s_add_u32 m0, s37, 86536\n"
      "ds_read_b128 a[192:195], v4 offset:59264\n"
      "ds_read_b64_tr_b16 a[224:225], v7 offset:19456\n"
      "v_mfma_f32_16x16x32_bf16 v[68:71], a[116:119], a[200:203], v[68:71]\n"
      "s_waitcnt lgkmcnt(2)\n"
      "buffer_load_dwordx4 v8, s[4:7], s53 offen offset:0 nt lds\n"
      "buffer_load_dwordx4 v9, s[4:7], s53 offen offset:1024 nt lds\n"
      "v_mfma_f32_16x16x32_bf16 v[72:75], a[108:111], a[200:203], v[72:75]\n"
      "s_mov_b32 m0, s64\n"
      "ds_read_b64_tr_b16 a[226:227], v7 offset:19464\n"
      "v_add_f32 v52, v52, v64\n"
      "v_mfma_f32_4x4x4_16b_bf16 v[20:23], a[216:217], a[220:221], 0\n"
      "v_add_f32 v53, v53, v65\n"
      "v_mfma_f32_4x4x4_16b_bf16 v[24:27], a[216:217], a[222:223], 0\n"
      "v_add_f32 v54, v54, v66\n"
      "v_add_f32 v55, v55, v67\n"
      "v_mfma_f32_16x16x32_bf16 v[112:115], a[0:3], a[204:207], 0\n"
      "ds_read_b128 a[196:199], v4 offset:59328\n"
      "ds_write_addtid_b32 v52 offset:45312\n"
      "v_mfma_f32_16x16x32_bf16 v[116:119], a[4:7], a[204:207], 0\n"
      "s_add_u32 s53, s54, 6488064\n"
      "ds_write_addtid_b32 v53 offset:45824\n"
      "ds_write_addtid_b32 v54 offset:46336\n"
      "v_mfma_f32_16x16x32_bf16 v[120:123], a[8:11], a[204:207], 0\n"
      "s_add_u32 m0, s37, 150272\n"
      "v_cvt_pk_bf16_f32 v20, v20, v24\n"
      "v_cvt_pk_bf16_f32 v21, v21, v25\n"
      "v_mfma_f32_16x16x32_bf16 v[112:115], a[12:15], a[208:211], v[112:115]\n"
      "buffer_load_dwordx4 v8, s[4:7], s53 offen offset:0 nt lds\n"
      "buffer_load_dwordx4 v9, s[4:7], s53 offen offset:1024 nt lds\n"
      "ds_read_b128 a[200:203], v4 offset:59392\n"
      "v_mfma_f32_16x16x32_bf16 v[116:119], a[16:19], a[208:211], v[116:119]\n"
      "s_mov_b32 m0, s64\n"
      "v_cvt_pk_bf16_f32 v22, v22, v26\n"
      "v_add_f32 v56, v56, v68\n"
      "v_mfma_f32_16x16x32_bf16 v[120:123], a[20:23], a[208:211], v[120:123]\n"
      "ds_write_addtid_b32 v20 offset:18432\n"
      "ds_write_addtid_b32 v21 offset:22528\n"
      "v_mfma_f32_16x16x32_bf16 v[112:115], a[24:27], a[212:215], v[112:115]\n"
      "s_add_u32 s53, s54, 6491136\n"
      "ds_read_b128 a[204:207], v5 offset:59264\n"
      "ds_read_b64_tr_b16 a[228:229], v7 offset:19968\n"
      "v_mfma_f32_16x16x32_bf16 v[116:119], a[28:31], a[212:215], v[116:119]\n"
      "s_add_u32 m0, s37, 158464\n"
      "ds_read_b64_tr_b16 a[230:231], v7 offset:19976\n"
      "v_cvt_pk_bf16_f32 v23, v23, v27\n"
      "v_mfma_f32_16x16x32_bf16 v[120:123], a[32:35], a[212:215], v[120:123]\n"
      "s_waitcnt lgkmcnt(5)\n"
      "buffer_load_dwordx4 v10, s[4:7], s53 offen offset:0 nt lds\n"
      "buffer_load_dwordx4 v11, s[4:7], s53 offen offset:1024 nt lds\n"
      "v_mfma_f32_4x4x4_16b_bf16 v[28:31], a[216:217], a[224:225], 0\n"
      "v_add_f32 v57, v57, v69\n"
      "v_mfma_f32_4x4x4_16b_bf16 v[32:35], a[216:217], a[226:227], 0\n"
      "v_add_f32 v58, v58, v70\n"
      "v_add_f32_e64 v59, v59, v71\n"
      "v_mfma_f32_16x16x32_bf16 v[112:115], a[36:39], a[192:195], v[112:115]\n"
      "s_mov_b32 m0, s64\n"
      "ds_read_b128 a[208:211], v5 offset:59328\n"
      "ds_write_addtid_b32 v22 offset:26624\n"
      "v_mfma_f32_16x16x32_bf16 v[116:119], a[40:43], a[192:195], v[116:119]\n"
      "ds_write_addtid_b32 v23 offset:30720\n"
      "ds_write_addtid_b32 v55 offset:46848\n"
      "v_mfma_f32_16x16x32_bf16 v[120:123], a[44:47], a[192:195], v[120:123]\n"
      "ds_write_addtid_b32 v56 offset:47360\n"
      "ds_write_addtid_b32 v57 offset:47872\n"
      "v_mfma_f32_16x16x32_bf16 v[112:115], a[48:51], a[196:199], v[112:115]\n"
      "ds_read_b128 a[212:215], v5 offset:59392\n"
      "ds_write_addtid_b32 v58 offset:48384\n"
      "v_mfma_f32_16x16x32_bf16 v[116:119], a[52:55], a[196:199], v[116:119]\n"
      "ds_write_addtid_b32 v59 offset:48896\n"
      "v_cvt_pk_bf16_f32 v28, v28, v32\n"
      "v_mfma_f32_16x16x32_bf16 v[120:123], a[56:59], a[196:199], v[120:123]\n"
      "ds_write_addtid_b32 v28 offset:18944\n"
      "v_cvt_pk_bf16_f32 v29, v29, v33\n"
      "v_mfma_f32_16x16x32_bf16 v[112:115], a[60:63], a[200:203], v[112:115]\n"
      "ds_read_b128 a[192:195], v3 offset:0\n"
      "ds_write_addtid_b32 v29 offset:23040\n"
      "v_mfma_f32_16x16x32_bf16 v[116:119], a[64:67], a[200:203], v[116:119]\n"
      "ds_read_b64_tr_b16 a[232:233], v7 offset:20480\n"
      "ds_read_b64_tr_b16 a[234:235], v7 offset:20488\n"
      "v_mfma_f32_16x16x32_bf16 v[120:123], a[68:71], a[200:203], v[120:123]\n"
      "s_waitcnt lgkmcnt(6)\n"
      "v_cvt_pk_bf16_f32 v30, v30, v34\n"
      "v_cvt_pk_bf16_f32 v31, v31, v35\n"
      "v_mfma_f32_4x4x4_16b_bf16 v[20:23], a[216:217], a[228:229], 0\n"
      "v_add_f32 v60, v60, v72\n"
      "v_mfma_f32_4x4x4_16b_bf16 v[24:27], a[216:217], a[230:231], 0\n"
      "ds_write_addtid_b32 v30 offset:27136\n"
      "ds_write_addtid_b32 v31 offset:31232\n"
      "v_mfma_f32_16x16x32_bf16 v[112:115], a[72:75], a[204:207], v[112:115]\n"
      "ds_read_b128 a[196:199], v3 offset:64\n"
      "ds_write_addtid_b32 v60 offset:49408\n"
      "v_mfma_f32_16x16x32_bf16 v[116:119], a[76:79], a[204:207], v[116:119]\n"
      "v_cvt_pk_bf16_f32 v20, v20, v24\n"
      "v_cvt_pk_bf16_f32 v21, v21, v25\n"
      "v_mfma_f32_16x16x32_bf16 v[120:123], a[80:83], a[204:207], v[120:123]\n"
      "ds_write_addtid_b32 v20 offset:19456\n"
      "ds_write_addtid_b32 v21 offset:23552\n"
      "v_mfma_f32_16x16x32_bf16 v[112:115], a[84:87], a[208:211], v[112:115]\n"
      "ds_read_b128 a[200:203], v3 offset:128\n"
      "v_cvt_pk_bf16_f32 v22, v22, v26\n"
      "v_mfma_f32_16x16x32_bf16 v[116:119], a[88:91], a[208:211], v[116:119]\n"
      "ds_write_addtid_b32 v22 offset:27648\n"
      "v_cvt_pk_bf16_f32 v23, v23, v27\n"
      "v_mfma_f32_16x16x32_bf16 v[120:123], a[92:95], a[208:211], v[120:123]\n"
      "ds_write_addtid_b32 v23 offset:31744\n"
      "v_add_f32 v61, v61, v73\n"
      "v_mfma_f32_16x16x32_bf16 v[112:115], a[96:99], a[212:215], v[112:115]\n"
      "ds_read_b128 a[204:207], v4 offset:0\n"
      "ds_write_addtid_b32 v61 offset:49920\n"
      "v_mfma_f32_16x16x32_bf16 v[116:119], a[100:103], a[212:215], v[116:119]\n"
      "v_add_f32 v62, v62, v74\n"
      "v_add_f32 v63, v63, v75\n"
      "v_mfma_f32_16x16x32_bf16 v[120:123], a[104:107], a[212:215], v[120:123]\n"
      "s_waitcnt lgkmcnt(4)\n"
      "ds_write_addtid_b32 v62 offset:50432\n"
      "ds_write_addtid_b32 v63 offset:50944\n"
      "v_mfma_f32_4x4x4_16b_bf16 v[28:31], a[216:217], a[232:233], 0\n"
      "v_mfma_f32_4x4x4_16b_bf16 v[32:35], a[216:217], a[234:235], 0\n"
      "v_mfma_f32_16x16x32_bf16 v[76:79], a[0:3], a[192:195], 0\n"
      "ds_read_b128 a[208:211], v4 offset:64\n"
      "v_mfma_f32_16x16x32_bf16 v[80:83], a[4:7], a[192:195], 0\n"
      "v_mfma_f32_16x16x32_bf16 v[84:87], a[8:11], a[192:195], 0\n"
      "v_mfma_f32_16x16x32_bf16 v[76:79], a[12:15], a[196:199], v[76:79]\n"
      "ds_read_b128 a[212:215], v4 offset:128\n"
      "v_cvt_pk_bf16_f32 v28, v28, v32\n"
      "v_mfma_f32_16x16x32_bf16 v[80:83], a[16:19], a[196:199], v[80:83]\n"
      "ds_write_addtid_b32 v28 offset:19968\n"
      "v_cvt_pk_bf16_f32 v29, v29, v33\n"
      "v_mfma_f32_16x16x32_bf16 v[84:87], a[20:23], a[196:199], v[84:87]\n"
      "ds_write_addtid_b32 v29 offset:24064\n"
      "v_cvt_pk_bf16_f32 v30, v30, v34\n"
      "v_mfma_f32_16x16x32_bf16 v[76:79], a[24:27], a[200:203], v[76:79]\n"
      "ds_read_b128 a[192:195], v5 offset:0\n"
      "ds_write_addtid_b32 v30 offset:28160\n"
      "v_mfma_f32_16x16x32_bf16 v[80:83], a[28:31], a[200:203], v[80:83]\n"
      "s_nop 0\n"
      "v_cvt_pk_bf16_f32 v31, v31, v35\n"
      "v_mfma_f32_16x16x32_bf16 v[84:87], a[32:35], a[200:203], v[84:87]\n"
      "s_waitcnt lgkmcnt(4)\n"
      "ds_write_addtid_b32 v31 offset:32256\n"
      "v_mfma_f32_16x16x32_bf16 v[76:79], a[36:39], a[204:207], v[76:79]\n"
      "ds_read_b128 a[196:199], v5 offset:64\n"
      "v_mfma_f32_16x16x32_bf16 v[80:83], a[40:43], a[204:207], v[80:83]\n"
      "v_fma_f32 v40, -2.0, v64, v52\n"
      "v_fma_f32 v41, -2.0, v65, v53\n"
      "v_mfma_f32_16x16x32_bf16 v[84:87], a[44:47], a[204:207], v[84:87]\n"
      "v_fma_f32 v42, -2.0, v66, v54\n"
      "v_fma_f32 v43, -2.0, v67, v55\n"
      "v_mfma_f32_16x16x32_bf16 v[76:79], a[48:51], a[208:211], v[76:79]\n"
      "ds_read_b128 a[200:203], v5 offset:128\n"
      "v_fma_f32 v44, -2.0, v68, v56\n"
      "v_mfma_f32_16x16x32_bf16 v[80:83], a[52:55], a[208:211], v[80:83]\n"
      "v_fma_f32 v45, -2.0, v69, v57\n"
      "v_mfma_f32_16x16x32_bf16 v[84:87], a[56:59], a[208:211], v[84:87]\n"
      "v_mfma_f32_16x16x32_bf16 v[76:79], a[60:63], a[212:215], v[76:79]\n"
      "v_mfma_f32_16x16x32_bf16 v[80:83], a[64:67], a[212:215], v[80:83]\n"
      "s_waitcnt vmcnt(14)\n"
      "v_mfma_f32_16x16x32_bf16 v[84:87], a[68:71], a[212:215], v[84:87]\n"
      "s_waitcnt lgkmcnt(0)\n"
      "s_barrier\n"
      "s_add_u32 s54, s54, 1966080\n"
      "s_add_u32 s6, s19, 6295552\n"
      ".p2align 6\n"
      "LDloop_%=:\n"
      "v_mfma_f32_16x16x32_bf16 v[76:79], a[72:75], a[192:195], v[76:79]\n"
      "s_add_u32 s58, s58, 3072\n"
      "ds_read_b128 a[204:207], v0 offset:16384\n"
      "ds_read2st64_b32 v[28:29], v6 offset0:0 offset1:2\n"
      "ds_read2st64_b32 v[30:31], v6 offset0:4 offset1:6\n"
      "v_mfma_f32_16x16x32_bf16 v[80:83], a[76:79], a[192:195], v[80:83]\n"
      "s_add_u32 s59, s58, 196608\n"
      "ds_read_b128 a[208:211], v0 offset:20480\n"
      "ds_read2st64_b32 v[32:33], v6 offset0:8 offset1:10\n"
      "v_mfma_f32_16x16x32_bf16 v[84:87], a[80:83], a[192:195], v[84:87]\n"
      "ds_read2st64_b32 v[34:35], v6 offset0:12 offset1:14\n"
      "v_fma_f32 v46, -2.0, v70, v58\n"
      "v_mfma_f32_16x16x32_bf16 v[76:79], a[84:87], a[196:199], v[76:79]\n"
      "ds_read_b128 a[212:215], v0 offset:16448\n"
      "v_fma_f32 v47, -2.0, v71, v59\n"
      "v_fma_f32 v48, -2.0, v72, v60\n"
      "v_mfma_f32_16x16x32_bf16 v[80:83], a[88:91], a[196:199], v[80:83]\n"
      "ds_read2st64_b32 a[220:221], v12 offset0:181 offset1:183\n"
      "v_fma_f32 v49, -2.0, v73, v61\n"
      "v_mfma_f32_16x16x32_bf16 v[84:87], a[92:95], a[196:199], v[84:87]\n"
      "ds_read2st64_b32 a[222:223], v15 offset0:181 offset1:183\n"
      "v_fma_f32 v50, -2.0, v74, v62\n"
      "v_fma_f32 v51, -2.0, v75, v63\n"
      "v_mfma_f32_16x16x32_bf16 v[76:79], a[96:99], a[200:203], v[76:79]\n"
      "ds_read_b128 a[192:195], v0 offset:20544\n"
      "ds_read_b64_tr_b16 a[244:245], v7 offset:36864\n"
      "v_mfma_f32_16x16x32_bf16 v[80:83], a[100:103], a[200:203], v[80:83]\n"
      "v_mfma_f32_16x16x32_bf16 v[84:87], a[104:107], a[200:203], v[84:87]\n"
      "s_waitcnt lgkmcnt(6)\n"
      "ds_read_b64_tr_b16 a[246:247], v7 offset:36872\n"
      "ds_read2st64_b32 v[36:37], v6 offset0:16 offset1:18\n"
      "ds_read2st64_b32 v[38:39], v6 offset0:20 offset1:22\n"
      "v_fmac_f32 v40, s46, v28\n"
      "v_fmac_f32 v41, s46, v29\n"
      "v_mfma_f32_16x16x32_bf16 v[52:55], v[252:255], a[204:207], 0\n"
      "ds_read_b128 a[196:199], v0 offset:16512\n"
      "v_fmac_f32 v42, s46, v30\n"
      "v_mfma_f32_16x16x32_bf16 v[56:59], v[244:247], a[204:207], 0\n"
      "s_add_u32 s54, s54, -193536\n"
      "v_fmac_f32_e64 v43, s46, v31\n"
      "v_fma_f32 v28, 0.5, v28, v100\n"
      "v_fma_f32 v29, 0.5, v29, v101\n"
      "v_mfma_f32_16x16x32_bf16 v[60:63], v[236:239], a[204:207], 0\n"
      "s_add_u32 m0, s37, 99328\n"
      "v_fma_f32 v30, 0.5, v30, v102\n"
      "v_fma_f32 v31, 0.5, v31, v103\n"
      "v_mfma_f32_16x16x32_bf16 v[64:67], v[248:251], a[208:211], 0\n"
      "buffer_load_dwordx4 v10, s[4:7], s54 offen offset:0 nt lds\n"
      "buffer_load_dwordx4 v11, s[4:7], s54 offen offset:1024 nt lds\n"
      "ds_read_b128 a[200:203], v0 offset:20608\n"
      "v_mfma_f32_16x16x32_bf16 v[68:71], v[240:243], a[208:211], 0\n"
      "s_mov_b32 m0, s64\n"
      "v_fmac_f32 v40, s48, v76\n"
      "v_fmac_f32 v41, s48, v77\n"
      "v_fmac_f32 v42, s48, v78\n"
      "v_mfma_f32_16x16x32_bf16 v[72:75], v[232:235], a[208:211], 0\n"
      "s_waitcnt lgkmcnt(2)\n"
      "v_fmac_f32 v43, s48, v79\n"
      "v_fmac_f32 v28, s45, v40\n"
      "v_mfma_f32_16x16x32_bf16 v[52:55], v[228:231], a[212:215], v[52:55]\n"
      "ds_read_b128 a[204:207], v1 offset:16384\n"
      "v_fmac_f32 v29, s45, v41\n"
      "v_fmac_f32 v30, s45, v42\n"
      "v_mfma_f32_16x16x32_bf16 v[56:59], v[220:223], a[212:215], v[56:59]\n"
      "v_fmac_f32 v31, s45, v43\n"
      "v_fma_f32 v40, s47, v40, -v76\n"
      "v_mfma_f32_16x16x32_bf16 v[60:63], v[212:215], a[212:215], v[60:63]\n"
      "ds_read2st64_b32 a[224:225], v13 offset0:181 offset1:183\n"
      "ds_read2st64_b32 a[226:227], v16 offset0:181 offset1:183\n"
      "v_fma_f32 v41, s47, v41, -v77\n"
      "v_mfma_f32_4x4x4_16b_bf16 v[20:23], a[216:217], a[244:245], 0\n"
      "v_fma_f32 v42, s47, v42, -v78\n"
      "v_mfma_f32_4x4x4_16b_bf16 v[24:27], a[216:217], a[246:247], 0\n"
      "v_fma_f32 v43, s47, v43, -v79\n"
      "v_cvt_pk_bf16_f32 v28, v28, v29\n"
      "v_mfma_f32_16x16x32_bf16 v[64:67], v[224:227], a[192:195], v[64:67]\n"
      "buffer_store_dwordx4 a[220:223], v18, s[8:11], s58 offen offset:0 sc1\n"
      "ds_read_b128 a[208:211], v1 offset:20480\n"
      "v_mfma_f32_16x16x32_bf16 v[68:71], v[216:219], a[192:195], v[68:71]\n"
      "s_nop 0\n"
      "ds_write_addtid_b32 v28 offset:32768\n"
      "v_cvt_pk_bf16_f32 v20, v20, v24\n"
      "v_cvt_pk_bf16_f32 v21, v21, v25\n"
      "v_mfma_f32_16x16x32_bf16 v[72:75], v[208:211], a[192:195], v[72:75]\n"
      "s_waitcnt lgkmcnt(2)\n"
      "ds_write_addtid_b32 v20 offset:3072\n"
      "ds_write_addtid_b32 v21 offset:7168\n"
      "v_cvt_pk_bf16_f32 v30, v30, v31\n"
      "v_mfma_f32_16x16x32_bf16 v[52:55], v[204:207], a[196:199], v[52:55]\n"
      "s_add_u32 s54, s54, 196608\n"
      "ds_read_b128 a[212:215], v1 offset:16448\n"
      "ds_write_addtid_b32 v30 offset:33280\n"
      "v_mfma_f32_16x16x32_bf16 v[56:59], v[196:199], a[196:199], v[56:59]\n"
      "s_add_u32 m0, s37, 103432\n"
      "v_cvt_pk_bf16_f32 v40, v40, v41\n"
      "v_cvt_pk_bf16_f32 v22, v22, v26\n"
      "v_mfma_f32_16x16x32_bf16 v[60:63], v[188:191], a[196:199], v[60:63]\n"
      "s_nop 0\n"
      "buffer_load_dwordx4 v10, s[4:7], s54 offen offset:0 nt lds\n"
      "buffer_load_dwordx4 v11, s[4:7], s54 offen offset:1024 nt lds\n"
      "v_cvt_pk_bf16_f32 v42, v42, v43\n"
      "v_mfma_f32_16x16x32_bf16 v[64:67], v[200:203], a[200:203], v[64:67]\n"
      "s_mov_b32 m0, s64\n"
      "ds_read_b128 a[192:195], v1 offset:20544\n"
      "ds_write_addtid_b32 v40 offset:34816\n"
      "v_mfma_f32_16x16x32_bf16 v[68:71], v[192:195], a[200:203], v[68:71]\n"
      "ds_write_addtid_b32 v42 offset:35328\n"
      "ds_write_addtid_b32 v22 offset:11264\n"
      "v_mfma_f32_16x16x32_bf16 v[72:75], v[184:187], a[200:203], v[72:75]\n"
      "v_cvt_pk_bf16_f32 v23, v23, v27\n"
      "v_fmac_f32 v44, s46, v32\n"
      "v_fmac_f32 v45, s46, v33\n"
      "v_mfma_f32_16x16x32_bf16 v[52:55], v[180:183], a[204:207], v[52:55]\n"
      "ds_read_b128 a[196:199], v1 offset:16512\n"
      "ds_write_addtid_b32 v23 offset:15360\n"
      "v_mfma_f32_16x16x32_bf16 v[56:59], v[172:175], a[204:207], v[56:59]\n"
      "ds_read2st64_b32 a[228:229], v14 offset0:181 offset1:183\n"
      "ds_read2st64_b32 a[230:231], v17 offset0:181 offset1:183\n"
      "v_fmac_f32 v46, s46, v34\n"
      "v_mfma_f32_16x16x32_bf16 v[60:63], v[164:167], a[204:207], v[60:63]\n"
      "s_waitcnt lgkmcnt(5)\n"
      "v_fmac_f32_e64 v47, s46, v35\n"
      "v_fma_f32 v32, 0.5, v32, v104\n"
      "v_mfma_f32_16x16x32_bf16 v[64:67], v[176:179], a[208:211], v[64:67]\n"
      "ds_read_b128 a[200:203], v1 offset:20608\n"
      "v_fma_f32 v33, 0.5, v33, v105\n"
      "v_mfma_f32_16x16x32_bf16 v[68:71], v[168:171], a[208:211], v[68:71]\n"
      "buffer_store_dwordx4 a[224:227], v18, s[8:11], s58 offen offset:128 sc1\n"
      "v_fma_f32 v34, 0.5, v34, v106\n"
      "v_mfma_f32_16x16x32_bf16 v[72:75], v[160:163], a[208:211], v[72:75]\n"
      "v_fma_f32 v35, 0.5, v35, v107\n"
      "v_fmac_f32 v44, s48, v80\n"
      "v_mfma_f32_16x16x32_bf16 v[52:55], v[156:159], a[212:215], v[52:55]\n"
      "ds_read_b128 a[204:207], v2 offset:16384\n"
      "v_fmac_f32 v45, s48, v81\n"
      "v_mfma_f32_16x16x32_bf16 v[56:59], v[148:151], a[212:215], v[56:59]\n"
      "s_add_u32 s54, s54, 193536\n"
      "ds_read_b64_tr_b16 a[248:249], v7 offset:37376\n"
      "ds_read_b64_tr_b16 a[250:251], v7 offset:37384\n"
      "v_fmac_f32_e64 v46, s48, v82\n"
      "v_mfma_f32_16x16x32_bf16 v[60:63], v[140:143], a[212:215], v[60:63]\n"
      "s_add_u32 m0, s37, 90760\n"
      "v_fmac_f32 v47, s48, v83\n"
      "v_fmac_f32 v32, s45, v44\n"
      "v_mfma_f32_16x16x32_bf16 v[64:67], v[152:155], a[192:195], v[64:67]\n"
      "buffer_load_dwordx4 v8, s[4:7], s54 offen offset:0 nt lds\n"
      "buffer_load_dwordx4 v9, s[4:7], s54 offen offset:1024 nt lds\n"
      "ds_read_b128 a[208:211], v2 offset:20480\n"
      "v_fmac_f32_e64 v33, s45, v45\n"
      "v_mfma_f32_16x16x32_bf16 v[68:71], v[144:147], a[192:195], v[68:71]\n"
      "s_mov_b32 m0, s64\n"
      "v_fmac_f32 v34, s45, v46\n"
      "v_fmac_f32 v35, s45, v47\n"
      "v_mfma_f32_16x16x32_bf16 v[72:75], v[136:139], a[192:195], v[72:75]\n"
      "s_waitcnt lgkmcnt(1)\n"
      "v_fma_f32 v44, s47, v44, -v80\n"
      "v_fma_f32 v45, s47, v45, -v81\n"
      "v_mfma_f32_16x16x32_bf16 v[52:55], v[132:135], a[196:199], v[52:55]\n"
      "ds_read_b128 a[212:215], v2 offset:16448\n"
      "v_fma_f32 v46, s47, v46, -v82\n"
      "v_mfma_f32_16x16x32_bf16 v[56:59], v[124:127], a[196:199], v[56:59]\n"
      "v_fma_f32 v47, s47, v47, -v83\n"
      "v_cvt_pk_bf16_f32 v32, v32, v33\n"
      "v_mfma_f32_16x16x32_bf16 v[60:63], a[184:187], a[196:199], v[60:63]\n"
      "ds_write_addtid_b32 v32 offset:40964\n"
      "v_cvt_pk_bf16_f32 v34, v34, v35\n"
      "v_mfma_f32_16x16x32_bf16 v[64:67], v[128:131], a[200:203], v[64:67]\n"
      "buffer_store_dwordx4 a[228:231], v18, s[8:11], s58 offen offset:256 sc1\n"
      "ds_read_b128 a[192:195], v2 offset:20544\n"
      "v_mfma_f32_16x16x32_bf16 v[68:71], a[188:191], a[200:203], v[68:71]\n"
      "ds_write_addtid_b32 v34 offset:41476\n"
      "ds_read2st64_b32 a[232:233], v12 offset0:4 offset1:6\n"
      "ds_read2st64_b32 a[234:235], v15 offset0:4 offset1:6\n"
      "v_cvt_pk_bf16_f32 v44, v44, v45\n"
      "v_cvt_pk_bf16_f32 v46, v46, v47\n"
      "v_mfma_f32_16x16x32_bf16 v[72:75], a[180:183], a[200:203], v[72:75]\n"
      "ds_write_addtid_b32 v44 offset:43012\n"
      "ds_write_addtid_b32 v46 offset:43524\n"
      "v_mfma_f32_16x16x32_bf16 v[52:55], a[176:179], a[204:207], v[52:55]\n"
      "ds_read_b128 a[196:199], v2 offset:16512\n"
      "v_fmac_f32 v48, s46, v36\n"
      "v_mfma_f32_4x4x4_16b_bf16 v[20:23], a[216:217], a[248:249], 0\n"
      "v_fmac_f32 v49, s46, v37\n"
      "v_fmac_f32_e64 v50, s46, v38\n"
      "v_mfma_f32_4x4x4_16b_bf16 v[24:27], a[216:217], a[250:251], 0\n"
      "v_fmac_f32 v51, s46, v39\n"
      "v_fma_f32 v36, 0.5, v36, v108\n"
      "v_mfma_f32_16x16x32_bf16 v[56:59], a[168:171], a[204:207], v[56:59]\n"
      "v_fma_f32 v37, 0.5, v37, v109\n"
      "v_fma_f32 v38, 0.5, v38, v110\n"
      "v_cvt_pk_bf16_f32 v20, v20, v24\n"
      "v_mfma_f32_16x16x32_bf16 v[60:63], a[160:163], a[204:207], v[60:63]\n"
      "s_waitcnt lgkmcnt(3)\n"
      "ds_write_addtid_b32 v20 offset:3584\n"
      "v_cvt_pk_bf16_f32 v21, v21, v25\n"
      "v_mfma_f32_16x16x32_bf16 v[64:67], a[172:175], a[208:211], v[64:67]\n"
      "s_add_u32 s54, s54, 196608\n"
      "ds_read_b128 a[200:203], v2 offset:20608\n"
      "ds_write_addtid_b32 v21 offset:7680\n"
      "v_mfma_f32_16x16x32_bf16 v[68:71], a[164:167], a[208:211], v[68:71]\n"
      "s_add_u32 m0, s37, 95104\n"
      "v_fma_f32 v39, 0.5, v39, v111\n"
      "v_fmac_f32 v48, s48, v84\n"
      "v_cvt_pk_bf16_f32 v22, v22, v26\n"
      "v_mfma_f32_16x16x32_bf16 v[72:75], a[156:159], a[208:211], v[72:75]\n"
      "buffer_load_dwordx4 v8, s[4:7], s54 offen offset:0 nt lds\n"
      "buffer_load_dwordx4 v9, s[4:7], s54 offen offset:1024 nt lds\n"
      "v_mfma_f32_16x16x32_bf16 v[52:55], a[152:155], a[212:215], v[52:55]\n"
      "s_mov_b32 m0, s64\n"
      "ds_read_b128 a[204:207], v3 offset:16896\n"
      "ds_read2st64_b32 a[236:237], v13 offset0:4 offset1:6\n"
      "v_mfma_f32_16x16x32_bf16 v[56:59], a[144:147], a[212:215], v[56:59]\n"
      "ds_write_addtid_b32 v22 offset:11776\n"
      "ds_read2st64_b32 a[238:239], v16 offset0:4 offset1:6\n"
      "v_fmac_f32 v49, s48, v85\n"
      "v_mfma_f32_16x16x32_bf16 v[60:63], a[136:139], a[212:215], v[60:63]\n"
      "v_fmac_f32 v50, s48, v86\n"
      "v_cvt_pk_bf16_f32 v23, v23, v27\n"
      "v_mfma_f32_16x16x32_bf16 v[64:67], a[148:151], a[192:195], v[64:67]\n"
      "ds_read_b128 a[208:211], v3 offset:16960\n"
      "ds_write_addtid_b32 v23 offset:15872\n"
      "v_fmac_f32 v51, s48, v87\n"
      "v_mfma_f32_16x16x32_bf16 v[68:71], a[140:143], a[192:195], v[68:71]\n"
      "v_fmac_f32 v36, s45, v48\n"
      "v_fmac_f32 v37, s45, v49\n"
      "v_mfma_f32_16x16x32_bf16 v[72:75], a[132:135], a[192:195], v[72:75]\n"
      "s_waitcnt lgkmcnt(3)\n"
      "buffer_store_dwordx4 a[232:235], v18, s[8:11], s59 offen offset:0 sc1\n"
      "v_mfma_f32_16x16x32_bf16 v[52:55], a[128:131], a[196:199], v[52:55]\n"
      "ds_read_b128 a[212:215], v3 offset:17024\n"
      "v_fmac_f32 v38, s45, v50\n"
      "v_mfma_f32_16x16x32_bf16 v[56:59], a[120:123], a[196:199], v[56:59]\n"
      "ds_read2st64_b32 a[240:241], v14 offset0:4 offset1:6\n"
      "ds_read2st64_b32 a[242:243], v17 offset0:4 offset1:6\n"
      "v_fmac_f32 v39, s45, v51\n"
      "v_mfma_f32_16x16x32_bf16 v[60:63], a[112:115], a[196:199], v[60:63]\n"
      "v_fma_f32 v48, s47, v48, -v84\n"
      "v_fma_f32 v49, s47, v49, -v85\n"
      "v_mfma_f32_16x16x32_bf16 v[64:67], a[124:127], a[200:203], v[64:67]\n"
      "ds_read_b128 a[192:195], v4 offset:16896\n"
      "v_fma_f32 v50, s47, v50, -v86\n"
      "v_mfma_f32_16x16x32_bf16 v[68:71], a[116:119], a[200:203], v[68:71]\n"
      "v_fma_f32 v51, s47, v51, -v87\n"
      "v_cvt_pk_bf16_f32 v36, v36, v37\n"
      "v_mfma_f32_16x16x32_bf16 v[72:75], a[108:111], a[200:203], v[72:75]\n"
      "ds_write_addtid_b32 v36 offset:36864\n"
      "v_cvt_pk_bf16_f32 v38, v38, v39\n"
      "v_add_f32 v52, v52, v64\n"
      "v_add_f32 v53, v53, v65\n"
      "v_mfma_f32_16x16x32_bf16 v[88:91], a[0:3], a[204:207], 0\n"
      "s_waitcnt lgkmcnt(2)\n"
      "ds_read_b128 a[196:199], v4 offset:16960\n"
      "ds_write_addtid_b32 v38 offset:37376\n"
      "v_mfma_f32_16x16x32_bf16 v[92:95], a[4:7], a[204:207], 0\n"
      "s_nop 0\n"
      "buffer_store_dwordx4 a[236:239], v18, s[8:11], s59 offen offset:128 sc1\n"
      "v_mfma_f32_16x16x32_bf16 v[96:99], a[8:11], a[204:207], 0\n"
      "ds_write_addtid_b32 v52 offset:51456\n"
      "ds_write_addtid_b32 v53 offset:51968\n"
      "v_add_f32 v54, v54, v66\n"
      "v_mfma_f32_16x16x32_bf16 v[88:91], a[12:15], a[208:211], v[88:91]\n"
      "ds_read_b128 a[200:203], v4 offset:17024\n"
      "ds_write_addtid_b32 v54 offset:52480\n"
      "v_mfma_f32_16x16x32_bf16 v[92:95], a[16:19], a[208:211], v[92:95]\n"
      "v_add_f32 v55, v55, v67\n"
      "v_add_f32 v56, v56, v68\n"
      "v_add_f32 v57, v57, v69\n"
      "v_mfma_f32_16x16x32_bf16 v[96:99], a[20:23], a[208:211], v[96:99]\n"
      "ds_write_addtid_b32 v55 offset:52992\n"
      "ds_write_addtid_b32 v56 offset:53504\n"
      "v_mfma_f32_16x16x32_bf16 v[88:91], a[24:27], a[212:215], v[88:91]\n"
      "ds_read_b128 a[204:207], v5 offset:16896\n"
      "ds_write_addtid_b32 v57 offset:54016\n"
      "v_mfma_f32_16x16x32_bf16 v[92:95], a[28:31], a[212:215], v[92:95]\n"
      "s_nop 0\n"
      "v_add_f32_e64 v58, v58, v70\n"
      "v_cvt_pk_bf16_f32 v48, v48, v49\n"
      "v_add_f32_e64 v59, v59, v71\n"
      "v_mfma_f32_16x16x32_bf16 v[96:99], a[32:35], a[212:215], v[96:99]\n"
      "s_waitcnt lgkmcnt(4)\n"
      "ds_write_addtid_b32 v48 offset:38912\n"
      "ds_write_addtid_b32 v58 offset:54528\n"
      "v_mfma_f32_16x16x32_bf16 v[88:91], a[36:39], a[192:195], v[88:91]\n"
      "s_nop 0\n"
      "buffer_store_dwordx4 a[240:243], v18, s[8:11], s59 offen offset:256 sc1\n"
      "ds_read_b128 a[208:211], v5 offset:16960\n"
      "v_cvt_pk_bf16_f32 v50, v50, v51\n"
      "v_mfma_f32_16x16x32_bf16 v[92:95], a[40:43], a[192:195], v[92:95]\n"
      "s_nop 0\n"
      "ds_write_addtid_b32 v50 offset:39424\n"
      "ds_write_addtid_b32 v59 offset:55040\n"
      "v_mfma_f32_16x16x32_bf16 v[96:99], a[44:47], a[192:195], v[96:99]\n"
      "s_nop 0\n"
      "v_add_f32_e64 v60, v60, v72\n"
      "v_add_f32_e64 v61, v61, v73\n"
      "v_add_f32_e64 v62, v62, v74\n"
      "v_add_f32_e64 v63, v63, v75\n"
      "v_mfma_f32_16x16x32_bf16 v[88:91], a[48:51], a[196:199], v[88:91]\n"
      "s_nop 0\n"
      "ds_read_b128 a[212:215], v5 offset:17024\n"
      "ds_write_addtid_b32 v60 offset:55552\n"
      "v_mfma_f32_16x16x32_bf16 v[92:95], a[52:55], a[196:199], v[92:95]\n"
      "ds_write_addtid_b32 v61 offset:56064\n"
      "ds_write_addtid_b32 v62 offset:56576\n"
      "v_mfma_f32_16x16x32_bf16 v[96:99], a[56:59], a[196:199], v[96:99]\n"
      "s_nop 0\n"
      "ds_write_addtid_b32 v63 offset:57088\n"
      "v_fma_f32 v40, -2.0, v64, v52\n"
      "v_mfma_f32_16x16x32_bf16 v[88:91], a[60:63], a[200:203], v[88:91]\n"
      "s_nop 0\n"
      "v_fma_f32 v41, -2.0, v65, v53\n"
      "v_fma_f32 v42, -2.0, v66, v54\n"
      "v_mfma_f32_16x16x32_bf16 v[92:95], a[64:67], a[200:203], v[92:95]\n"
      "s_waitcnt vmcnt(17)\n"
      "v_fma_f32 v43, -2.0, v67, v55\n"
      "v_fma_f32 v44, -2.0, v68, v56\n"
      "v_mfma_f32_16x16x32_bf16 v[96:99], a[68:71], a[200:203], v[96:99]\n"
      "s_waitcnt lgkmcnt(0)\n"
      "v_fma_f32 v45, -2.0, v69, v57\n"
      "s_barrier\n"
      "LDmid_%=:\n"
      "v_mfma_f32_16x16x32_bf16 v[88:91], a[72:75], a[204:207], v[88:91]\n"
      "s_add_u32 s62, s62, 786432\n"
      "ds_read_b128 a[192:195], v0 offset:0\n"
      "ds_read2st64_b32 v[28:29], v6 offset0:24 offset1:26\n"
      "ds_read2st64_b32 v[30:31], v6 offset0:28 offset1:30\n"
      "v_mfma_f32_16x16x32_bf16 v[92:95], a[76:79], a[204:207], v[92:95]\n"
      "s_add_u32 s58, s62, 0\n"
      "ds_read_b128 a[196:199], v0 offset:4096\n"
      "ds_read2st64_b32 v[32:33], v6 offset0:32 offset1:34\n"
      "v_mfma_f32_16x16x32_bf16 v[96:99], a[80:83], a[204:207], v[96:99]\n"
      "s_add_u32 s59, s58, 196608\n"
      "ds_read2st64_b32 v[34:35], v6 offset0:36 offset1:38\n"
      "v_fma_f32 v46, -2.0, v70, v58\n"
      "v_mfma_f32_16x16x32_bf16 v[88:91], a[84:87], a[208:211], v[88:91]\n"
      "ds_read_b128 a[200:203], v0 offset:64\n"
      "v_fma_f32 v47, -2.0, v71, v59\n"
      "v_fma_f32 v48, -2.0, v72, v60\n"
      "v_mfma_f32_16x16x32_bf16 v[92:95], a[88:91], a[208:211], v[92:95]\n"
      "ds_read2st64_b32 a[220:221], v12 offset0:177 offset1:179\n"
      "v_fma_f32 v49, -2.0, v73, v61\n"
      "v_mfma_f32_16x16x32_bf16 v[96:99], a[92:95], a[208:211], v[96:99]\n"
      "ds_read2st64_b32 a[222:223], v15 offset0:177 offset1:179\n"
      "v_fma_f32 v50, -2.0, v74, v62\n"
      "v_fma_f32 v51, -2.0, v75, v63\n"
      "v_mfma_f32_16x16x32_bf16 v[88:91], a[96:99], a[212:215], v[88:91]\n"
      "ds_read_b128 a[204:207], v0 offset:4160\n"
      "ds_read_b64_tr_b16 a[244:245], v7 offset:53760\n"
      "v_mfma_f32_16x16x32_bf16 v[92:95], a[100:103], a[212:215], v[92:95]\n"
      "s_nop 0\n"
      "v_mfma_f32_16x16x32_bf16 v[96:99], a[104:107], a[212:215], v[96:99]\n"
      "s_waitcnt lgkmcnt(6)\n"
      "ds_read_b64_tr_b16 a[246:247], v7 offset:53768\n"
      "ds_read2st64_b32 v[36:37], v6 offset0:40 offset1:42\n"
      "ds_read2st64_b32 v[38:39], v6 offset0:44 offset1:46\n"
      "v_fmac_f32 v40, s46, v28\n"
      "v_fmac_f32 v41, s46, v29\n"
      "v_mfma_f32_16x16x32_bf16 v[52:55], v[252:255], a[192:195], 0\n"
      "ds_read_b128 a[208:211], v0 offset:128\n"
      "v_fmac_f32 v42, s46, v30\n"
      "v_mfma_f32_16x16x32_bf16 v[56:59], v[244:247], a[192:195], 0\n"
      "s_add_u32 s54, s54, -193536\n"
      "v_fmac_f32 v43, s46, v31\n"
      "v_fma_f32 v28, 0.5, v28, v112\n"
      "v_fma_f32 v29, 0.5, v29, v113\n"
      "v_mfma_f32_16x16x32_bf16 v[60:63], v[236:239], a[192:195], 0\n"
      "s_add_u32 m0, s37, 107656\n"
      "v_fma_f32 v30, 0.5, v30, v114\n"
      "v_fma_f32 v31, 0.5, v31, v115\n"
      "v_mfma_f32_16x16x32_bf16 v[64:67], v[248:251], a[196:199], 0\n"
      "buffer_load_dwordx4 v10, s[4:7], s54 offen offset:0 nt lds\n"
      "buffer_load_dwordx4 v11, s[4:7], s54 offen offset:1024 nt lds\n"
      "ds_read_b128 a[212:215], v0 offset:4224\n"
      "v_mfma_f32_16x16x32_bf16 v[68:71], v[240:243], a[196:199], 0\n"
      "s_mov_b32 m0, s64\n"
      "v_fmac_f32 v40, s48, v88\n"
      "v_fmac_f32 v41, s48, v89\n"
      "v_fmac_f32 v42, s48, v90\n"
      "v_mfma_f32_16x16x32_bf16 v[72:75], v[232:235], a[196:199], 0\n"
      "s_waitcnt lgkmcnt(2)\n"
      "v_fmac_f32 v43, s48, v91\n"
      "v_fmac_f32 v28, s45, v40\n"
      "v_mfma_f32_16x16x32_bf16 v[52:55], v[228:231], a[200:203], v[52:55]\n"
      "s_nop 0\n"
      "ds_read_b128 a[192:195], v1 offset:0\n"
      "v_fmac_f32 v29, s45, v41\n"
      "v_fmac_f32 v30, s45, v42\n"
      "v_mfma_f32_16x16x32_bf16 v[56:59], v[220:223], a[200:203], v[56:59]\n"
      "v_fmac_f32 v31, s45, v43\n"
      "v_fma_f32 v40, s47, v40, -v88\n"
      "v_mfma_f32_16x16x32_bf16 v[60:63], v[212:215], a[200:203], v[60:63]\n"
      "s_nop 0\n"
      "ds_read2st64_b32 a[224:225], v13 offset0:177 offset1:179\n"
      "ds_read2st64_b32 a[226:227], v16 offset0:177 offset1:179\n"
      "v_fma_f32 v41, s47, v41, -v89\n"
      "v_mfma_f32_4x4x4_16b_bf16 v[20:23], a[216:217], a[244:245], 0\n"
      "v_fma_f32 v42, s47, v42, -v90\n"
      "v_mfma_f32_4x4x4_16b_bf16 v[24:27], a[216:217], a[246:247], 0\n"
      "v_fma_f32 v43, s47, v43, -v91\n"
      "v_cvt_pk_bf16_f32 v28, v28, v29\n"
      "v_mfma_f32_16x16x32_bf16 v[64:67], v[224:227], a[204:207], v[64:67]\n"
      "buffer_store_dwordx4 a[220:223], v18, s[8:11], s58 offen offset:0 sc1\n"
      "ds_read_b128 a[196:199], v1 offset:4096\n"
      "v_mfma_f32_16x16x32_bf16 v[68:71], v[216:219], a[204:207], v[68:71]\n"
      "s_nop 0\n"
      "ds_write_addtid_b32 v28 offset:33792\n"
      "v_cvt_pk_bf16_f32 v20, v20, v24\n"
      "v_cvt_pk_bf16_f32 v21, v21, v25\n"
      "v_mfma_f32_16x16x32_bf16 v[72:75], v[208:211], a[204:207], v[72:75]\n"
      "s_waitcnt lgkmcnt(2)\n"
      "ds_write_addtid_b32 v20 offset:19456\n"
      "ds_write_addtid_b32 v21 offset:23552\n"
      "v_cvt_pk_bf16_f32 v30, v30, v31\n"
      "v_mfma_f32_16x16x32_bf16 v[52:55], v[204:207], a[208:211], v[52:55]\n"
      "s_add_u32 s54, s54, 196608\n"
      "ds_read_b128 a[200:203], v1 offset:64\n"
      "ds_write_addtid_b32 v30 offset:34304\n"
      "v_mfma_f32_16x16x32_bf16 v[56:59], v[196:199], a[208:211], v[56:59]\n"
      "s_add_u32 m0, s37, 112000\n"
      "v_cvt_pk_bf16_f32 v40, v40, v41\n"
      "v_cvt_pk_bf16_f32 v22, v22, v26\n"
      "v_mfma_f32_16x16x32_bf16 v[60:63], v[188:191], a[208:211], v[60:63]\n"
      "s_nop 0\n"
      "buffer_load_dwordx4 v10, s[4:7], s54 offen offset:0 nt lds\n"
      "buffer_load_dwordx4 v11, s[4:7], s54 offen offset:1024 nt lds\n"
      "v_cvt_pk_bf16_f32 v42, v42, v43\n"
      "v_mfma_f32_16x16x32_bf16 v[64:67], v[200:203], a[212:215], v[64:67]\n"
      "s_mov_b32 m0, s64\n"
      "ds_read_b128 a[204:207], v1 offset:4160\n"
      "ds_write_addtid_b32 v40 offset:35840\n"
      "v_mfma_f32_16x16x32_bf16 v[68:71], v[192:195], a[212:215], v[68:71]\n"
      "ds_write_addtid_b32 v42 offset:36352\n"
      "ds_write_addtid_b32 v22 offset:27648\n"
      "v_mfma_f32_16x16x32_bf16 v[72:75], v[184:187], a[212:215], v[72:75]\n"
      "v_cvt_pk_bf16_f32 v23, v23, v27\n"
      "v_fmac_f32 v44, s46, v32\n"
      "v_fmac_f32 v45, s46, v33\n"
      "v_mfma_f32_16x16x32_bf16 v[52:55], v[180:183], a[192:195], v[52:55]\n"
      "ds_read_b128 a[208:211], v1 offset:128\n"
      "ds_write_addtid_b32 v23 offset:31744\n"
      "v_mfma_f32_16x16x32_bf16 v[56:59], v[172:175], a[192:195], v[56:59]\n"
      "ds_read2st64_b32 a[228:229], v14 offset0:177 offset1:179\n"
      "ds_read2st64_b32 a[230:231], v17 offset0:177 offset1:179\n"
      "v_fmac_f32 v46, s46, v34\n"
      "v_mfma_f32_16x16x32_bf16 v[60:63], v[164:167], a[192:195], v[60:63]\n"
      "s_waitcnt lgkmcnt(5)\n"
      "v_fmac_f32 v47, s46, v35\n"
      "v_fma_f32 v32, 0.5, v32, v116\n"
      "v_mfma_f32_16x16x32_bf16 v[64:67], v[176:179], a[196:199], v[64:67]\n"
      "ds_read_b128 a[212:215], v1 offset:4224\n"
      "v_fma_f32 v33, 0.5, v33, v117\n"
      "v_mfma_f32_16x16x32_bf16 v[68:71], v[168:171], a[196:199], v[68:71]\n"
      "s_nop 0\n"
      "buffer_store_dwordx4 a[224:227], v18, s[8:11], s58 offen offset:128 sc1\n"
      "v_fma_f32 v34, 0.5, v34, v118\n"
      "v_mfma_f32_16x16x32_bf16 v[72:75], v[160:163], a[196:199], v[72:75]\n"
      "v_fma_f32 v35, 0.5, v35, v119\n"
      "v_fmac_f32 v44, s48, v92\n"
      "v_mfma_f32_16x16x32_bf16 v[52:55], v[156:159], a[200:203], v[52:55]\n"
      "ds_read_b128 a[192:195], v2 offset:0\n"
      "v_fmac_f32 v45, s48, v93\n"
      "v_mfma_f32_16x16x32_bf16 v[56:59], v[148:151], a[200:203], v[56:59]\n"
      "s_add_u32 s54, s54, 193536\n"
      "ds_read_b64_tr_b16 a[248:249], v7 offset:54272\n"
      "ds_read_b64_tr_b16 a[250:251], v7 offset:54280\n"
      "v_fmac_f32 v46, s48, v94\n"
      "v_mfma_f32_16x16x32_bf16 v[60:63], v[140:143], a[200:203], v[60:63]\n"
      "s_add_u32 m0, s37, 116224\n"
      "v_fmac_f32 v47, s48, v95\n"
      "v_fmac_f32 v32, s45, v44\n"
      "v_mfma_f32_16x16x32_bf16 v[64:67], v[152:155], a[204:207], v[64:67]\n"
      "s_nop 0\n"
      "buffer_load_dwordx4 v8, s[4:7], s54 offen offset:0 nt lds\n"
      "buffer_load_dwordx4 v9, s[4:7], s54 offen offset:1024 nt lds\n"
      "ds_read_b128 a[196:199], v2 offset:4096\n"
      "v_fmac_f32 v33, s45, v45\n"
      "v_mfma_f32_16x16x32_bf16 v[68:71], v[144:147], a[204:207], v[68:71]\n"
      "s_mov_b32 m0, s64\n"
      "v_fmac_f32 v34, s45, v46\n"
      "v_fmac_f32_e64 v35, s45, v47\n"
      "v_mfma_f32_16x16x32_bf16 v[72:75], v[136:139], a[204:207], v[72:75]\n"
      "s_waitcnt lgkmcnt(1)\n"
      "v_fma_f32 v44, s47, v44, -v92\n"
      "v_fma_f32 v45, s47, v45, -v93\n"
      "v_mfma_f32_16x16x32_bf16 v[52:55], v[132:135], a[208:211], v[52:55]\n"
      "ds_read_b128 a[200:203], v2 offset:64\n"
      "v_fma_f32 v46, s47, v46, -v94\n"
      "v_mfma_f32_16x16x32_bf16 v[56:59], v[124:127], a[208:211], v[56:59]\n"
      "v_fma_f32 v47, s47, v47, -v95\n"
      "v_cvt_pk_bf16_f32 v32, v32, v33\n"
      "v_mfma_f32_16x16x32_bf16 v[60:63], a[184:187], a[208:211], v[60:63]\n"
      "ds_write_addtid_b32 v32 offset:41988\n"
      "v_cvt_pk_bf16_f32 v34, v34, v35\n"
      "v_mfma_f32_16x16x32_bf16 v[64:67], v[128:131], a[212:215], v[64:67]\n"
      "buffer_store_dwordx4 a[228:231], v18, s[8:11], s58 offen offset:256 sc1\n"
      "ds_read_b128 a[204:207], v2 offset:4160\n"
      "v_mfma_f32_16x16x32_bf16 v[68:71], a[188:191], a[212:215], v[68:71]\n"
      "ds_write_addtid_b32 v34 offset:42500\n"
      "ds_read2st64_b32 a[232:233], v12 offset0:0 offset1:2\n"
      "ds_read2st64_b32 a[234:235], v15 offset0:0 offset1:2\n"
      "v_cvt_pk_bf16_f32 v44, v44, v45\n"
      "v_cvt_pk_bf16_f32 v46, v46, v47\n"
      "v_mfma_f32_16x16x32_bf16 v[72:75], a[180:183], a[212:215], v[72:75]\n"
      "s_nop 0\n"
      "ds_write_addtid_b32 v44 offset:44036\n"
      "ds_write_addtid_b32 v46 offset:44548\n"
      "v_mfma_f32_16x16x32_bf16 v[52:55], a[176:179], a[192:195], v[52:55]\n"
      "s_nop 0\n"
      "ds_read_b128 a[208:211], v2 offset:128\n"
      "v_fmac_f32_e64 v48, s46, v36\n"
      "v_mfma_f32_4x4x4_16b_bf16 v[20:23], a[216:217], a[248:249], 0\n"
      "v_fmac_f32_e64 v49, s46, v37\n"
      "v_fmac_f32_e64 v50, s46, v38\n"
      "v_mfma_f32_4x4x4_16b_bf16 v[24:27], a[216:217], a[250:251], 0\n"
      "v_fmac_f32_e64 v51, s46, v39\n"
      "v_fma_f32 v36, 0.5, v36, v120\n"
      "v_mfma_f32_16x16x32_bf16 v[56:59], a[168:171], a[192:195], v[56:59]\n"
      "s_nop 0\n"
      "v_fma_f32 v37, 0.5, v37, v121\n"
      "v_fma_f32 v38, 0.5, v38, v122\n"
      "v_cvt_pk_bf16_f32 v20, v20, v24\n"
      "v_mfma_f32_16x16x32_bf16 v[60:63], a[160:163], a[192:195], v[60:63]\n"
      "s_waitcnt lgkmcnt(3)\n"
      "ds_write_addtid_b32 v20 offset:19968\n"
      "v_cvt_pk_bf16_f32 v21, v21, v25\n"
      "v_mfma_f32_16x16x32_bf16 v[64:67], a[172:175], a[196:199], v[64:67]\n"
      "s_add_u32 s54, s54, 196608\n"
      "ds_read_b128 a[212:215], v2 offset:4224\n"
      "ds_write_addtid_b32 v21 offset:24064\n"
      "v_mfma_f32_16x16x32_bf16 v[68:71], a[164:167], a[196:199], v[68:71]\n"
      "s_add_u32 m0, s37, 120328\n"
      "v_fma_f32 v39, 0.5, v39, v123\n"
      "v_fmac_f32_e64 v48, s48, v96\n"
      "v_cvt_pk_bf16_f32 v22, v22, v26\n"
      "v_mfma_f32_16x16x32_bf16 v[72:75], a[156:159], a[196:199], v[72:75]\n"
      "buffer_load_dwordx4 v8, s[4:7], s54 offen offset:0 nt lds\n"
      "buffer_load_dwordx4 v9, s[4:7], s54 offen offset:1024 nt lds\n"
      "v_mfma_f32_16x16x32_bf16 v[52:55], a[152:155], a[200:203], v[52:55]\n"
      "s_mov_b32 m0, s64\n"
      "ds_read_b128 a[192:195], v3 offset:33792\n"
      "ds_read2st64_b32 a[236:237], v13 offset0:0 offset1:2\n"
      "v_mfma_f32_16x16x32_bf16 v[56:59], a[144:147], a[200:203], v[56:59]\n"
      "s_nop 0\n"
      "ds_write_addtid_b32 v22 offset:28160\n"
      "ds_read2st64_b32 a[238:239], v16 offset0:0 offset1:2\n"
      "v_fmac_f32 v49, s48, v97\n"
      "v_mfma_f32_16x16x32_bf16 v[60:63], a[136:139], a[200:203], v[60:63]\n"
      "v_fmac_f32 v50, s48, v98\n"
      "v_cvt_pk_bf16_f32 v23, v23, v27\n"
      "v_mfma_f32_16x16x32_bf16 v[64:67], a[148:151], a[204:207], v[64:67]\n"
      "ds_read_b128 a[196:199], v3 offset:33856\n"
      "ds_write_addtid_b32 v23 offset:32256\n"
      "v_fmac_f32 v51, s48, v99\n"
      "v_mfma_f32_16x16x32_bf16 v[68:71], a[140:143], a[204:207], v[68:71]\n"
      "v_fmac_f32 v36, s45, v48\n"
      "v_fmac_f32 v37, s45, v49\n"
      "v_mfma_f32_16x16x32_bf16 v[72:75], a[132:135], a[204:207], v[72:75]\n"
      "s_waitcnt lgkmcnt(3)\n"
      "buffer_store_dwordx4 a[232:235], v18, s[8:11], s59 offen offset:0 sc1\n"
      "v_mfma_f32_16x16x32_bf16 v[52:55], a[128:131], a[208:211], v[52:55]\n"
      "ds_read_b128 a[200:203], v3 offset:33920\n"
      "v_fmac_f32_e64 v38, s45, v50\n"
      "v_mfma_f32_16x16x32_bf16 v[56:59], a[120:123], a[208:211], v[56:59]\n"
      "ds_read2st64_b32 a[240:241], v14 offset0:0 offset1:2\n"
      "ds_read2st64_b32 a[242:243], v17 offset0:0 offset1:2\n"
      "v_fmac_f32 v39, s45, v51\n"
      "v_mfma_f32_16x16x32_bf16 v[60:63], a[112:115], a[208:211], v[60:63]\n"
      "v_fma_f32 v48, s47, v48, -v96\n"
      "v_fma_f32 v49, s47, v49, -v97\n"
      "v_mfma_f32_16x16x32_bf16 v[64:67], a[124:127], a[212:215], v[64:67]\n"
      "s_nop 0\n"
      "ds_read_b128 a[204:207], v4 offset:33792\n"
      "v_fma_f32 v50, s47, v50, -v98\n"
      "v_mfma_f32_16x16x32_bf16 v[68:71], a[116:119], a[212:215], v[68:71]\n"
      "v_fma_f32 v51, s47, v51, -v99\n"
      "v_cvt_pk_bf16_f32 v36, v36, v37\n"
      "v_mfma_f32_16x16x32_bf16 v[72:75], a[108:111], a[212:215], v[72:75]\n"
      "ds_write_addtid_b32 v36 offset:37888\n"
      "v_cvt_pk_bf16_f32 v38, v38, v39\n"
      "v_add_f32 v52, v52, v64\n"
      "v_add_f32 v53, v53, v65\n"
      "v_mfma_f32_16x16x32_bf16 v[100:103], a[0:3], a[192:195], 0\n"
      "s_waitcnt lgkmcnt(2)\n"
      "ds_read_b128 a[208:211], v4 offset:33856\n"
      "ds_write_addtid_b32 v38 offset:38400\n"
      "v_mfma_f32_16x16x32_bf16 v[104:107], a[4:7], a[192:195], 0\n"
      "buffer_store_dwordx4 a[236:239], v18, s[8:11], s59 offen offset:128 sc1\n"
      "v_mfma_f32_16x16x32_bf16 v[108:111], a[8:11], a[192:195], 0\n"
      "s_nop 0\n"
      "ds_write_addtid_b32 v52 offset:45312\n"
      "ds_write_addtid_b32 v53 offset:45824\n"
      "v_add_f32 v54, v54, v66\n"
      "v_mfma_f32_16x16x32_bf16 v[100:103], a[12:15], a[196:199], v[100:103]\n"
      "ds_read_b128 a[212:215], v4 offset:33920\n"
      "ds_write_addtid_b32 v54 offset:46336\n"
      "v_mfma_f32_16x16x32_bf16 v[104:107], a[16:19], a[196:199], v[104:107]\n"
      "v_add_f32 v55, v55, v67\n"
      "v_add_f32 v56, v56, v68\n"
      "v_add_f32 v57, v57, v69\n"
      "v_mfma_f32_16x16x32_bf16 v[108:111], a[20:23], a[196:199], v[108:111]\n"
      "ds_write_addtid_b32 v55 offset:46848\n"
      "ds_write_addtid_b32 v56 offset:47360\n"
      "v_mfma_f32_16x16x32_bf16 v[100:103], a[24:27], a[200:203], v[100:103]\n"
      "ds_read_b128 a[192:195], v5 offset:33792\n"
      "ds_write_addtid_b32 v57 offset:47872\n"
      "v_mfma_f32_16x16x32_bf16 v[104:107], a[28:31], a[200:203], v[104:107]\n"
      "v_add_f32_e64 v58, v58, v70\n"
      "v_cvt_pk_bf16_f32 v48, v48, v49\n"
      "v_add_f32 v59, v59, v71\n"
      "v_mfma_f32_16x16x32_bf16 v[108:111], a[32:35], a[200:203], v[108:111]\n"
      "s_waitcnt lgkmcnt(4)\n"
      "ds_write_addtid_b32 v48 offset:39936\n"
      "ds_write_addtid_b32 v58 offset:48384\n"
      "v_mfma_f32_16x16x32_bf16 v[100:103], a[36:39], a[204:207], v[100:103]\n"
      "buffer_store_dwordx4 a[240:243], v18, s[8:11], s59 offen offset:256 sc1\n"
      "ds_read_b128 a[196:199], v5 offset:33856\n"
      "v_cvt_pk_bf16_f32 v50, v50, v51\n"
      "v_mfma_f32_16x16x32_bf16 v[104:107], a[40:43], a[204:207], v[104:107]\n"
      "ds_write_addtid_b32 v50 offset:40448\n"
      "ds_write_addtid_b32 v59 offset:48896\n"
      "v_mfma_f32_16x16x32_bf16 v[108:111], a[44:47], a[204:207], v[108:111]\n"
      "v_add_f32 v60, v60, v72\n"
      "v_add_f32 v61, v61, v73\n"
      "v_add_f32 v62, v62, v74\n"
      "v_add_f32 v63, v63, v75\n"
      "v_mfma_f32_16x16x32_bf16 v[100:103], a[48:51], a[208:211], v[100:103]\n"
      "ds_read_b128 a[200:203], v5 offset:33920\n"
      "ds_write_addtid_b32 v60 offset:49408\n"
      "v_mfma_f32_16x16x32_bf16 v[104:107], a[52:55], a[208:211], v[104:107]\n"
      "ds_write_addtid_b32 v61 offset:49920\n"
      "ds_write_addtid_b32 v62 offset:50432\n"
      "v_mfma_f32_16x16x32_bf16 v[108:111], a[56:59], a[208:211], v[108:111]\n"
      "ds_write_addtid_b32 v63 offset:50944\n"
      "v_fma_f32 v40, -2.0, v64, v52\n"
      "v_mfma_f32_16x16x32_bf16 v[100:103], a[60:63], a[212:215], v[100:103]\n"
      "s_nop 0\n"
      "v_fma_f32 v41, -2.0, v65, v53\n"
      "v_fma_f32 v42, -2.0, v66, v54\n"
      "v_mfma_f32_16x16x32_bf16 v[104:107], a[64:67], a[212:215], v[104:107]\n"
      "s_waitcnt vmcnt(17)\n"
      "v_fma_f32 v43, -2.0, v67, v55\n"
      "v_fma_f32 v44, -2.0, v68, v56\n"
      "v_mfma_f32_16x16x32_bf16 v[108:111], a[68:71], a[212:215], v[108:111]\n"
      "s_waitcnt lgkmcnt(0)\n"
      "v_fma_f32 v45, -2.0, v69, v57\n"
      "s_barrier\n"
      "v_mfma_f32_16x16x32_bf16 v[100:103], a[72:75], a[192:195], v[100:103]\n"
      "s_add_u32 s58, s58, 3072\n"
      "ds_read_b128 a[204:207], v0 offset:16384\n"
      "ds_read2st64_b32 v[28:29], v6 offset0:0 offset1:2\n"
      "ds_read2st64_b32 v[30:31], v6 offset0:4 offset1:6\n"
      "v_mfma_f32_16x16x32_bf16 v[104:107], a[76:79], a[192:195], v[104:107]\n"
      "s_add_u32 s59, s58, 196608\n"
      "ds_read_b128 a[208:211], v0 offset:20480\n"
      "ds_read2st64_b32 v[32:33], v6 offset0:8 offset1:10\n"
      "v_mfma_f32_16x16x32_bf16 v[108:111], a[80:83], a[192:195], v[108:111]\n"
      "ds_read2st64_b32 v[34:35], v6 offset0:12 offset1:14\n"
      "v_fma_f32 v46, -2.0, v70, v58\n"
      "v_mfma_f32_16x16x32_bf16 v[100:103], a[84:87], a[196:199], v[100:103]\n"
      "ds_read_b128 a[212:215], v0 offset:16448\n"
      "v_fma_f32 v47, -2.0, v71, v59\n"
      "v_fma_f32 v48, -2.0, v72, v60\n"
      "v_mfma_f32_16x16x32_bf16 v[104:107], a[88:91], a[196:199], v[104:107]\n"
      "ds_read2st64_b32 a[220:221], v12 offset0:181 offset1:183\n"
      "v_fma_f32 v49, -2.0, v73, v61\n"
      "v_mfma_f32_16x16x32_bf16 v[108:111], a[92:95], a[196:199], v[108:111]\n"
      "ds_read2st64_b32 a[222:223], v15 offset0:181 offset1:183\n"
      "v_fma_f32 v50, -2.0, v74, v62\n"
      "v_fma_f32 v51, -2.0, v75, v63\n"
      "v_mfma_f32_16x16x32_bf16 v[100:103], a[96:99], a[200:203], v[100:103]\n"
      "ds_read_b128 a[192:195], v0 offset:20544\n"
      "ds_read_b64_tr_b16 a[244:245], v7 offset:3072\n"
      "v_mfma_f32_16x16x32_bf16 v[104:107], a[100:103], a[200:203], v[104:107]\n"
      "s_nop 0\n"
      "v_mfma_f32_16x16x32_bf16 v[108:111], a[104:107], a[200:203], v[108:111]\n"
      "s_waitcnt lgkmcnt(6)\n"
      "ds_read_b64_tr_b16 a[246:247], v7 offset:3080\n"
      "ds_read2st64_b32 v[36:37], v6 offset0:16 offset1:18\n"
      "ds_read2st64_b32 v[38:39], v6 offset0:20 offset1:22\n"
      "v_fmac_f32 v40, s46, v28\n"
      "v_fmac_f32 v41, s46, v29\n"
      "v_mfma_f32_16x16x32_bf16 v[52:55], v[252:255], a[204:207], 0\n"
      "ds_read_b128 a[196:199], v0 offset:16512\n"
      "v_fmac_f32 v42, s46, v30\n"
      "v_mfma_f32_16x16x32_bf16 v[56:59], v[244:247], a[204:207], 0\n"
      "s_add_u32 s54, s54, -193536\n"
      "v_fmac_f32 v43, s46, v31\n"
      "v_fma_f32 v28, 0.5, v28, v76\n"
      "v_fma_f32 v29, 0.5, v29, v77\n"
      "v_mfma_f32_16x16x32_bf16 v[60:63], v[236:239], a[204:207], 0\n"
      "s_add_u32 m0, s37, 133120\n"
      "v_fma_f32 v30, 0.5, v30, v78\n"
      "v_fma_f32 v31, 0.5, v31, v79\n"
      "v_mfma_f32_16x16x32_bf16 v[64:67], v[248:251], a[208:211], 0\n"
      "buffer_load_dwordx4 v10, s[4:7], s54 offen offset:0 nt lds\n"
      "buffer_load_dwordx4 v11, s[4:7], s54 offen offset:1024 nt lds\n"
      "ds_read_b128 a[200:203], v0 offset:20608\n"
      "v_mfma_f32_16x16x32_bf16 v[68:71], v[240:243], a[208:211], 0\n"
      "s_mov_b32 m0, s64\n"
      "v_fmac_f32 v40, s48, v100\n"
      "v_fmac_f32 v41, s48, v101\n"
      "v_fmac_f32 v42, s48, v102\n"
      "v_mfma_f32_16x16x32_bf16 v[72:75], v[232:235], a[208:211], 0\n"
      "s_waitcnt lgkmcnt(2)\n"
      "v_fmac_f32 v43, s48, v103\n"
      "v_fmac_f32_e64 v28, s45, v40\n"
      "v_mfma_f32_16x16x32_bf16 v[52:55], v[228:231], a[212:215], v[52:55]\n"
      "ds_read_b128 a[204:207], v1 offset:16384\n"
      "v_fmac_f32 v29, s45, v41\n"
      "v_fmac_f32 v30, s45, v42\n"
      "v_mfma_f32_16x16x32_bf16 v[56:59], v[220:223], a[212:215], v[56:59]\n"
      "v_fmac_f32 v31, s45, v43\n"
      "v_fma_f32 v40, s47, v40, -v100\n"
      "v_mfma_f32_16x16x32_bf16 v[60:63], v[212:215], a[212:215], v[60:63]\n"
      "s_nop 0\n"
      "ds_read2st64_b32 a[224:225], v13 offset0:181 offset1:183\n"
      "ds_read2st64_b32 a[226:227], v16 offset0:181 offset1:183\n"
      "v_fma_f32 v41, s47, v41, -v101\n"
      "v_mfma_f32_4x4x4_16b_bf16 v[20:23], a[216:217], a[244:245], 0\n"
      "v_fma_f32 v42, s47, v42, -v102\n"
      "v_mfma_f32_4x4x4_16b_bf16 v[24:27], a[216:217], a[246:247], 0\n"
      "v_fma_f32 v43, s47, v43, -v103\n"
      "v_cvt_pk_bf16_f32 v28, v28, v29\n"
      "v_mfma_f32_16x16x32_bf16 v[64:67], v[224:227], a[192:195], v[64:67]\n"
      "buffer_store_dwordx4 a[220:223], v18, s[8:11], s58 offen offset:0 sc1\n"
      "ds_read_b128 a[208:211], v1 offset:20480\n"
      "v_mfma_f32_16x16x32_bf16 v[68:71], v[216:219], a[192:195], v[68:71]\n"
      "s_nop 0\n"
      "ds_write_addtid_b32 v28 offset:32768\n"
      "v_cvt_pk_bf16_f32 v20, v20, v24\n"
      "v_cvt_pk_bf16_f32 v21, v21, v25\n"
      "v_mfma_f32_16x16x32_bf16 v[72:75], v[208:211], a[192:195], v[72:75]\n"
      "s_waitcnt lgkmcnt(2)\n"
      "ds_write_addtid_b32 v20 offset:3072\n"
      "ds_write_addtid_b32 v21 offset:7168\n"
      "v_cvt_pk_bf16_f32 v30, v30, v31\n"
      "v_mfma_f32_16x16x32_bf16 v[52:55], v[204:207], a[196:199], v[52:55]\n"
      "s_add_u32 s54, s54, 196608\n"
      "ds_read_b128 a[212:215], v1 offset:16448\n"
      "ds_write_addtid_b32 v30 offset:33280\n"
      "v_mfma_f32_16x16x32_bf16 v[56:59], v[196:199], a[196:199], v[56:59]\n"
      "s_add_u32 m0, s37, 137224\n"
      "v_cvt_pk_bf16_f32 v40, v40, v41\n"
      "v_cvt_pk_bf16_f32 v22, v22, v26\n"
      "v_mfma_f32_16x16x32_bf16 v[60:63], v[188:191], a[196:199], v[60:63]\n"
      "buffer_load_dwordx4 v10, s[4:7], s54 offen offset:0 nt lds\n"
      "buffer_load_dwordx4 v11, s[4:7], s54 offen offset:1024 nt lds\n"
      "v_cvt_pk_bf16_f32 v42, v42, v43\n"
      "v_mfma_f32_16x16x32_bf16 v[64:67], v[200:203], a[200:203], v[64:67]\n"
      "s_mov_b32 m0, s64\n"
      "ds_read_b128 a[192:195], v1 offset:20544\n"
      "ds_write_addtid_b32 v40 offset:34816\n"
      "v_mfma_f32_16x16x32_bf16 v[68:71], v[192:195], a[200:203], v[68:71]\n"
      "ds_write_addtid_b32 v42 offset:35328\n"
      "ds_write_addtid_b32 v22 offset:11264\n"
      "v_mfma_f32_16x16x32_bf16 v[72:75], v[184:187], a[200:203], v[72:75]\n"
      "v_cvt_pk_bf16_f32 v23, v23, v27\n"
      "v_fmac_f32 v44, s46, v32\n"
      "v_fmac_f32 v45, s46, v33\n"
      "v_mfma_f32_16x16x32_bf16 v[52:55], v[180:183], a[204:207], v[52:55]\n"
      "ds_read_b128 a[196:199], v1 offset:16512\n"
      "ds_write_addtid_b32 v23 offset:15360\n"
      "v_mfma_f32_16x16x32_bf16 v[56:59], v[172:175], a[204:207], v[56:59]\n"
      "ds_read2st64_b32 a[228:229], v14 offset0:181 offset1:183\n"
      "ds_read2st64_b32 a[230:231], v17 offset0:181 offset1:183\n"
      "v_fmac_f32 v46, s46, v34\n"
      "v_mfma_f32_16x16x32_bf16 v[60:63], v[164:167], a[204:207], v[60:63]\n"
      "s_waitcnt lgkmcnt(5)\n"
      "v_fmac_f32 v47, s46, v35\n"
      "v_fma_f32 v32, 0.5, v32, v80\n"
      "v_mfma_f32_16x16x32_bf16 v[64:67], v[176:179], a[208:211], v[64:67]\n"
      "ds_read_b128 a[200:203], v1 offset:20608\n"
      "v_fma_f32 v33, 0.5, v33, v81\n"
      "v_mfma_f32_16x16x32_bf16 v[68:71], v[168:171], a[208:211], v[68:71]\n"
      "buffer_store_dwordx4 a[224:227], v18, s[8:11], s58 offen offset:128 sc1\n"
      "v_fma_f32 v34, 0.5, v34, v82\n"
      "v_mfma_f32_16x16x32_bf16 v[72:75], v[160:163], a[208:211], v[72:75]\n"
      "v_fma_f32 v35, 0.5, v35, v83\n"
      "v_fmac_f32 v44, s48, v104\n"
      "v_mfma_f32_16x16x32_bf16 v[52:55], v[156:159], a[212:215], v[52:55]\n"
      "ds_read_b128 a[204:207], v2 offset:16384\n"
      "v_fmac_f32 v45, s48, v105\n"
      "v_mfma_f32_16x16x32_bf16 v[56:59], v[148:151], a[212:215], v[56:59]\n"
      "s_add_u32 s54, s54, 193536\n"
      "ds_read_b64_tr_b16 a[248:249], v7 offset:3584\n"
      "ds_read_b64_tr_b16 a[250:251], v7 offset:3592\n"
      "v_fmac_f32 v46, s48, v106\n"
      "v_mfma_f32_16x16x32_bf16 v[60:63], v[140:143], a[212:215], v[60:63]\n"
      "s_add_u32 m0, s37, 124552\n"
      "v_fmac_f32 v47, s48, v107\n"
      "v_fmac_f32 v32, s45, v44\n"
      "v_mfma_f32_16x16x32_bf16 v[64:67], v[152:155], a[192:195], v[64:67]\n"
      "s_nop 0\n"
      "buffer_load_dwordx4 v8, s[4:7], s54 offen offset:0 nt lds\n"
      "buffer_load_dwordx4 v9, s[4:7], s54 offen offset:1024 nt lds\n"
      "ds_read_b128 a[208:211], v2 offset:20480\n"
      "v_fmac_f32 v33, s45, v45\n"
      "v_mfma_f32_16x16x32_bf16 v[68:71], v[144:147], a[192:195], v[68:71]\n"
      "s_mov_b32 m0, s64\n"
      "v_fmac_f32 v34, s45, v46\n"
      "v_fmac_f32_e64 v35, s45, v47\n"
      "v_mfma_f32_16x16x32_bf16 v[72:75], v[136:139], a[192:195], v[72:75]\n"
      "s_waitcnt lgkmcnt(1)\n"
      "v_fma_f32 v44, s47, v44, -v104\n"
      "v_fma_f32 v45, s47, v45, -v105\n"
      "v_mfma_f32_16x16x32_bf16 v[52:55], v[132:135], a[196:199], v[52:55]\n"
      "ds_read_b128 a[212:215], v2 offset:16448\n"
      "v_fma_f32 v46, s47, v46, -v106\n"
      "v_mfma_f32_16x16x32_bf16 v[56:59], v[124:127], a[196:199], v[56:59]\n"
      "v_fma_f32 v47, s47, v47, -v107\n"
      "v_cvt_pk_bf16_f32 v32, v32, v33\n"
      "v_mfma_f32_16x16x32_bf16 v[60:63], a[184:187], a[196:199], v[60:63]\n"
      "ds_write_addtid_b32 v32 offset:40964\n"
      "v_cvt_pk_bf16_f32 v34, v34, v35\n"
      "v_mfma_f32_16x16x32_bf16 v[64:67], v[128:131], a[200:203], v[64:67]\n"
      "buffer_store_dwordx4 a[228:231], v18, s[8:11], s58 offen offset:256 sc1\n"
      "ds_read_b128 a[192:195], v2 offset:20544\n"
      "v_mfma_f32_16x16x32_bf16 v[68:71], a[188:191], a[200:203], v[68:71]\n"
      "ds_write_addtid_b32 v34 offset:41476\n"
      "ds_read2st64_b32 a[232:233], v12 offset0:4 offset1:6\n"
      "ds_read2st64_b32 a[234:235], v15 offset0:4 offset1:6\n"
      "v_cvt_pk_bf16_f32 v44, v44, v45\n"
      "v_cvt_pk_bf16_f32 v46, v46, v47\n"
      "v_mfma_f32_16x16x32_bf16 v[72:75], a[180:183], a[200:203], v[72:75]\n"
      "s_nop 0\n"
      "ds_write_addtid_b32 v44 offset:43012\n"
      "ds_write_addtid_b32 v46 offset:43524\n"
      "v_mfma_f32_16x16x32_bf16 v[52:55], a[176:179], a[204:207], v[52:55]\n"
      "s_nop 0\n"
      "ds_read_b128 a[196:199], v2 offset:16512\n"
      "v_fmac_f32_e64 v48, s46, v36\n"
      "v_mfma_f32_4x4x4_16b_bf16 v[20:23], a[216:217], a[248:249], 0\n"
      "v_fmac_f32_e64 v49, s46, v37\n"
      "v_fmac_f32_e64 v50, s46, v38\n"
      "v_mfma_f32_4x4x4_16b_bf16 v[24:27], a[216:217], a[250:251], 0\n"
      "v_fmac_f32_e64 v51, s46, v39\n"
      "v_fma_f32 v36, 0.5, v36, v84\n"
      "v_mfma_f32_16x16x32_bf16 v[56:59], a[168:171], a[204:207], v[56:59]\n"
      "s_nop 0\n"
      "v_fma_f32 v37, 0.5, v37, v85\n"
      "v_fma_f32 v38, 0.5, v38, v86\n"
      "v_cvt_pk_bf16_f32 v20, v20, v24\n"
      "v_mfma_f32_16x16x32_bf16 v[60:63], a[160:163], a[204:207], v[60:63]\n"
      "s_waitcnt lgkmcnt(3)\n"
      "ds_write_addtid_b32 v20 offset:3584\n"
      "v_cvt_pk_bf16_f32 v21, v21, v25\n"
      "v_mfma_f32_16x16x32_bf16 v[64:67], a[172:175], a[208:211], v[64:67]\n"
      "s_add_u32 s54, s54, 196608\n"
      "ds_read_b128 a[200:203], v2 offset:20608\n"
      "ds_write_addtid_b32 v21 offset:7680\n"
      "v_mfma_f32_16x16x32_bf16 v[68:71], a[164:167], a[208:211], v[68:71]\n"
      "s_add_u32 m0, s37, 128896\n"
      "v_fma_f32 v39, 0.5, v39, v87\n"
      "v_fmac_f32_e64 v48, s48, v108\n"
      "v_cvt_pk_bf16_f32 v22, v22, v26\n"
      "v_mfma_f32_16x16x32_bf16 v[72:75], a[156:159], a[208:211], v[72:75]\n"
      "buffer_load_dwordx4 v8, s[4:7], s54 offen offset:0 nt lds\n"
      "buffer_load_dwordx4 v9, s[4:7], s54 offen offset:1024 nt lds\n"
      "v_mfma_f32_16x16x32_bf16 v[52:55], a[152:155], a[212:215], v[52:55]\n"
      "s_mov_b32 m0, s64\n"
      "ds_read_b128 a[204:207], v3 offset:50688\n"
      "ds_read2st64_b32 a[236:237], v13 offset0:4 offset1:6\n"
      "v_mfma_f32_16x16x32_bf16 v[56:59], a[144:147], a[212:215], v[56:59]\n"
      "s_nop 0\n"
      "ds_write_addtid_b32 v22 offset:11776\n"
      "ds_read2st64_b32 a[238:239], v16 offset0:4 offset1:6\n"
      "v_fmac_f32 v49, s48, v109\n"
      "v_mfma_f32_16x16x32_bf16 v[60:63], a[136:139], a[212:215], v[60:63]\n"
      "v_fmac_f32 v50, s48, v110\n"
      "v_cvt_pk_bf16_f32 v23, v23, v27\n"
      "v_mfma_f32_16x16x32_bf16 v[64:67], a[148:151], a[192:195], v[64:67]\n"
      "ds_read_b128 a[208:211], v3 offset:50752\n"
      "ds_write_addtid_b32 v23 offset:15872\n"
      "v_fmac_f32 v51, s48, v111\n"
      "v_mfma_f32_16x16x32_bf16 v[68:71], a[140:143], a[192:195], v[68:71]\n"
      "v_fmac_f32 v36, s45, v48\n"
      "v_fmac_f32 v37, s45, v49\n"
      "v_mfma_f32_16x16x32_bf16 v[72:75], a[132:135], a[192:195], v[72:75]\n"
      "s_waitcnt lgkmcnt(3)\n"
      "buffer_store_dwordx4 a[232:235], v18, s[8:11], s59 offen offset:0 sc1\n"
      "v_mfma_f32_16x16x32_bf16 v[52:55], a[128:131], a[196:199], v[52:55]\n"
      "ds_read_b128 a[212:215], v3 offset:50816\n"
      "v_fmac_f32_e64 v38, s45, v50\n"
      "v_mfma_f32_16x16x32_bf16 v[56:59], a[120:123], a[196:199], v[56:59]\n"
      "ds_read2st64_b32 a[240:241], v14 offset0:4 offset1:6\n"
      "ds_read2st64_b32 a[242:243], v17 offset0:4 offset1:6\n"
      "v_fmac_f32 v39, s45, v51\n"
      "v_mfma_f32_16x16x32_bf16 v[60:63], a[112:115], a[196:199], v[60:63]\n"
      "v_fma_f32 v48, s47, v48, -v108\n"
      "v_fma_f32 v49, s47, v49, -v109\n"
      "v_mfma_f32_16x16x32_bf16 v[64:67], a[124:127], a[200:203], v[64:67]\n"
      "s_nop 0\n"
      "ds_read_b128 a[192:195], v4 offset:50688\n"
      "v_fma_f32 v50, s47, v50, -v110\n"
      "v_mfma_f32_16x16x32_bf16 v[68:71], a[116:119], a[200:203], v[68:71]\n"
      "v_fma_f32 v51, s47, v51, -v111\n"
      "v_cvt_pk_bf16_f32 v36, v36, v37\n"
      "v_mfma_f32_16x16x32_bf16 v[72:75], a[108:111], a[200:203], v[72:75]\n"
      "ds_write_addtid_b32 v36 offset:36864\n"
      "v_cvt_pk_bf16_f32 v38, v38, v39\n"
      "v_add_f32 v52, v52, v64\n"
      "v_add_f32 v53, v53, v65\n"
      "v_mfma_f32_16x16x32_bf16 v[112:115], a[0:3], a[204:207], 0\n"
      "s_waitcnt lgkmcnt(2)\n"
      "ds_read_b128 a[196:199], v4 offset:50752\n"
      "ds_write_addtid_b32 v38 offset:37376\n"
      "v_mfma_f32_16x16x32_bf16 v[116:119], a[4:7], a[204:207], 0\n"
      "buffer_store_dwordx4 a[236:239], v18, s[8:11], s59 offen offset:128 sc1\n"
      "v_mfma_f32_16x16x32_bf16 v[120:123], a[8:11], a[204:207], 0\n"
      "s_nop 0\n"
      "ds_write_addtid_b32 v52 offset:51456\n"
      "ds_write_addtid_b32 v53 offset:51968\n"
      "v_add_f32 v54, v54, v66\n"
      "v_mfma_f32_16x16x32_bf16 v[112:115], a[12:15], a[208:211], v[112:115]\n"
      "ds_read_b128 a[200:203], v4 offset:50816\n"
      "ds_write_addtid_b32 v54 offset:52480\n"
      "v_mfma_f32_16x16x32_bf16 v[116:119], a[16:19], a[208:211], v[116:119]\n"
      "v_add_f32 v55, v55, v67\n"
      "v_add_f32 v56, v56, v68\n"
      "v_add_f32 v57, v57, v69\n"
      "v_mfma_f32_16x16x32_bf16 v[120:123], a[20:23], a[208:211], v[120:123]\n"
      "ds_write_addtid_b32 v55 offset:52992\n"
      "ds_write_addtid_b32 v56 offset:53504\n"
      "v_mfma_f32_16x16x32_bf16 v[112:115], a[24:27], a[212:215], v[112:115]\n"
      "ds_read_b128 a[204:207], v5 offset:50688\n"
      "ds_write_addtid_b32 v57 offset:54016\n"
      "v_mfma_f32_16x16x32_bf16 v[116:119], a[28:31], a[212:215], v[116:119]\n"
      "v_add_f32_e64 v58, v58, v70\n"
      "v_cvt_pk_bf16_f32 v48, v48, v49\n"
      "v_add_f32 v59, v59, v71\n"
      "v_mfma_f32_16x16x32_bf16 v[120:123], a[32:35], a[212:215], v[120:123]\n"
      "s_waitcnt lgkmcnt(4)\n"
      "ds_write_addtid_b32 v48 offset:38912\n"
      "ds_write_addtid_b32 v58 offset:54528\n"
      "v_mfma_f32_16x16x32_bf16 v[112:115], a[36:39], a[192:195], v[112:115]\n"
      "buffer_store_dwordx4 a[240:243], v18, s[8:11], s59 offen offset:256 sc1\n"
      "ds_read_b128 a[208:211], v5 offset:50752\n"
      "v_cvt_pk_bf16_f32 v50, v50, v51\n"
      "v_mfma_f32_16x16x32_bf16 v[116:119], a[40:43], a[192:195], v[116:119]\n"
      "ds_write_addtid_b32 v50 offset:39424\n"
      "ds_write_addtid_b32 v59 offset:55040\n"
      "v_mfma_f32_16x16x32_bf16 v[120:123], a[44:47], a[192:195], v[120:123]\n"
      "v_add_f32 v60, v60, v72\n"
      "v_add_f32 v61, v61, v73\n"
      "v_add_f32 v62, v62, v74\n"
      "v_add_f32 v63, v63, v75\n"
      "v_mfma_f32_16x16x32_bf16 v[112:115], a[48:51], a[196:199], v[112:115]\n"
      "ds_read_b128 a[212:215], v5 offset:50816\n"
      "ds_write_addtid_b32 v60 offset:55552\n"
      "v_mfma_f32_16x16x32_bf16 v[116:119], a[52:55], a[196:199], v[116:119]\n"
      "ds_write_addtid_b32 v61 offset:56064\n"
      "ds_write_addtid_b32 v62 offset:56576\n"
      "v_mfma_f32_16x16x32_bf16 v[120:123], a[56:59], a[196:199], v[120:123]\n"
      "ds_write_addtid_b32 v63 offset:57088\n"
      "v_fma_f32 v40, -2.0, v64, v52\n"
      "v_mfma_f32_16x16x32_bf16 v[112:115], a[60:63], a[200:203], v[112:115]\n"
      "v_fma_f32 v41, -2.0, v65, v53\n"
      "v_fma_f32 v42, -2.0, v66, v54\n"
      "v_mfma_f32_16x16x32_bf16 v[116:119], a[64:67], a[200:203], v[116:119]\n"
      "s_waitcnt vmcnt(17)\n"
      "v_fma_f32 v43, -2.0, v67, v55\n"
      "v_fma_f32 v44, -2.0, v68, v56\n"
      "v_mfma_f32_16x16x32_bf16 v[120:123], a[68:71], a[200:203], v[120:123]\n"
      "s_waitcnt lgkmcnt(0)\n"
      "v_fma_f32 v45, -2.0, v69, v57\n"
      "s_barrier\n"
      "v_mfma_f32_16x16x32_bf16 v[112:115], a[72:75], a[204:207], v[112:115]\n"
      "s_add_u32 s62, s62, 786432\n"
      "ds_read_b128 a[192:195], v0 offset:0\n"
      "ds_read2st64_b32 v[28:29], v6 offset0:24 offset1:26\n"
      "ds_read2st64_b32 v[30:31], v6 offset0:28 offset1:30\n"
      "v_mfma_f32_16x16x32_bf16 v[116:119], a[76:79], a[204:207], v[116:119]\n"
      "s_add_u32 s58, s62, 0\n"
      "ds_read_b128 a[196:199], v0 offset:4096\n"
      "ds_read2st64_b32 v[32:33], v6 offset0:32 offset1:34\n"
      "v_mfma_f32_16x16x32_bf16 v[120:123], a[80:83], a[204:207], v[120:123]\n"
      "s_add_u32 s59, s58, 196608\n"
      "ds_read2st64_b32 v[34:35], v6 offset0:36 offset1:38\n"
      "v_fma_f32 v46, -2.0, v70, v58\n"
      "v_mfma_f32_16x16x32_bf16 v[112:115], a[84:87], a[208:211], v[112:115]\n"
      "ds_read_b128 a[200:203], v0 offset:64\n"
      "v_fma_f32 v47, -2.0, v71, v59\n"
      "v_fma_f32 v48, -2.0, v72, v60\n"
      "v_mfma_f32_16x16x32_bf16 v[116:119], a[88:91], a[208:211], v[116:119]\n"
      "ds_read2st64_b32 a[220:221], v12 offset0:177 offset1:179\n"
      "v_fma_f32 v49, -2.0, v73, v61\n"
      "v_mfma_f32_16x16x32_bf16 v[120:123], a[92:95], a[208:211], v[120:123]\n"
      "ds_read2st64_b32 a[222:223], v15 offset0:177 offset1:179\n"
      "v_fma_f32 v50, -2.0, v74, v62\n"
      "v_fma_f32 v51, -2.0, v75, v63\n"
      "v_mfma_f32_16x16x32_bf16 v[112:115], a[96:99], a[212:215], v[112:115]\n"
      "ds_read_b128 a[204:207], v0 offset:4160\n"
      "ds_read_b64_tr_b16 a[244:245], v7 offset:19968\n"
      "v_mfma_f32_16x16x32_bf16 v[116:119], a[100:103], a[212:215], v[116:119]\n"
      "s_nop 0\n"
      "v_mfma_f32_16x16x32_bf16 v[120:123], a[104:107], a[212:215], v[120:123]\n"
      "s_waitcnt lgkmcnt(6)\n"
      "ds_read_b64_tr_b16 a[246:247], v7 offset:19976\n"
      "ds_read2st64_b32 v[36:37], v6 offset0:40 offset1:42\n"
      "ds_read2st64_b32 v[38:39], v6 offset0:44 offset1:46\n"
      "v_fmac_f32 v40, s46, v28\n"
      "v_fmac_f32 v41, s46, v29\n"
      "v_mfma_f32_16x16x32_bf16 v[52:55], v[252:255], a[192:195], 0\n"
      "ds_read_b128 a[208:211], v0 offset:128\n"
      "v_fmac_f32 v42, s46, v30\n"
      "v_mfma_f32_16x16x32_bf16 v[56:59], v[244:247], a[192:195], 0\n"
      "s_add_u32 s54, s54, -193536\n"
      "v_fmac_f32 v43, s46, v31\n"
      "v_fma_f32 v28, 0.5, v28, v88\n"
      "v_fma_f32 v29, 0.5, v29, v89\n"
      "v_mfma_f32_16x16x32_bf16 v[60:63], v[236:239], a[192:195], 0\n"
      "s_add_u32 m0, s37, 141448\n"
      "v_fma_f32 v30, 0.5, v30, v90\n"
      "v_fma_f32 v31, 0.5, v31, v91\n"
      "v_mfma_f32_16x16x32_bf16 v[64:67], v[248:251], a[196:199], 0\n"
      "buffer_load_dwordx4 v10, s[4:7], s54 offen offset:0 nt lds\n"
      "buffer_load_dwordx4 v11, s[4:7], s54 offen offset:1024 nt lds\n"
      "ds_read_b128 a[212:215], v0 offset:4224\n"
      "v_mfma_f32_16x16x32_bf16 v[68:71], v[240:243], a[196:199], 0\n"
      "s_mov_b32 m0, s64\n"
      "v_fmac_f32 v40, s48, v112\n"
      "v_fmac_f32 v41, s48, v113\n"
      "v_fmac_f32 v42, s48, v114\n"
      "v_mfma_f32_16x16x32_bf16 v[72:75], v[232:235], a[196:199], 0\n"
      "s_waitcnt lgkmcnt(2)\n"
      "v_fmac_f32 v43, s48, v115\n"
      "v_fmac_f32_e64 v28, s45, v40\n"
      "v_mfma_f32_16x16x32_bf16 v[52:55], v[228:231], a[200:203], v[52:55]\n"
      "ds_read_b128 a[192:195], v1 offset:0\n"
      "v_fmac_f32 v29, s45, v41\n"
      "v_fmac_f32 v30, s45, v42\n"
      "v_mfma_f32_16x16x32_bf16 v[56:59], v[220:223], a[200:203], v[56:59]\n"
      "v_fmac_f32 v31, s45, v43\n"
      "v_fma_f32 v40, s47, v40, -v112\n"
      "v_mfma_f32_16x16x32_bf16 v[60:63], v[212:215], a[200:203], v[60:63]\n"
      "s_nop 0\n"
      "ds_read2st64_b32 a[224:225], v13 offset0:177 offset1:179\n"
      "ds_read2st64_b32 a[226:227], v16 offset0:177 offset1:179\n"
      "v_fma_f32 v41, s47, v41, -v113\n"
      "v_mfma_f32_4x4x4_16b_bf16 v[20:23], a[216:217], a[244:245], 0\n"
      "v_fma_f32 v42, s47, v42, -v114\n"
      "v_mfma_f32_4x4x4_16b_bf16 v[24:27], a[216:217], a[246:247], 0\n"
      "v_fma_f32 v43, s47, v43, -v115\n"
      "v_cvt_pk_bf16_f32 v28, v28, v29\n"
      "v_mfma_f32_16x16x32_bf16 v[64:67], v[224:227], a[204:207], v[64:67]\n"
      "buffer_store_dwordx4 a[220:223], v18, s[8:11], s58 offen offset:0 sc1\n"
      "ds_read_b128 a[196:199], v1 offset:4096\n"
      "v_mfma_f32_16x16x32_bf16 v[68:71], v[216:219], a[204:207], v[68:71]\n"
      "s_nop 0\n"
      "ds_write_addtid_b32 v28 offset:33792\n"
      "v_cvt_pk_bf16_f32 v20, v20, v24\n"
      "v_cvt_pk_bf16_f32 v21, v21, v25\n"
      "v_mfma_f32_16x16x32_bf16 v[72:75], v[208:211], a[204:207], v[72:75]\n"
      "s_waitcnt lgkmcnt(2)\n"
      "ds_write_addtid_b32 v20 offset:19456\n"
      "ds_write_addtid_b32 v21 offset:23552\n"
      "v_cvt_pk_bf16_f32 v30, v30, v31\n"
      "v_mfma_f32_16x16x32_bf16 v[52:55], v[204:207], a[208:211], v[52:55]\n"
      "s_add_u32 s54, s54, 196608\n"
      "ds_read_b128 a[200:203], v1 offset:64\n"
      "ds_write_addtid_b32 v30 offset:34304\n"
      "v_mfma_f32_16x16x32_bf16 v[56:59], v[196:199], a[208:211], v[56:59]\n"
      "s_add_u32 m0, s37, 145792\n"
      "v_cvt_pk_bf16_f32 v40, v40, v41\n"
      "v_cvt_pk_bf16_f32 v22, v22, v26\n"
      "v_mfma_f32_16x16x32_bf16 v[60:63], v[188:191], a[208:211], v[60:63]\n"
      "buffer_load_dwordx4 v10, s[4:7], s54 offen offset:0 nt lds\n"
      "buffer_load_dwordx4 v11, s[4:7], s54 offen offset:1024 nt lds\n"
      "v_cvt_pk_bf16_f32 v42, v42, v43\n"
      "v_mfma_f32_16x16x32_bf16 v[64:67], v[200:203], a[212:215], v[64:67]\n"
      "s_mov_b32 m0, s64\n"
      "ds_read_b128 a[204:207], v1 offset:4160\n"
      "ds_write_addtid_b32 v40 offset:35840\n"
      "v_mfma_f32_16x16x32_bf16 v[68:71], v[192:195], a[212:215], v[68:71]\n"
      "ds_write_addtid_b32 v42 offset:36352\n"
      "ds_write_addtid_b32 v22 offset:27648\n"
      "v_mfma_f32_16x16x32_bf16 v[72:75], v[184:187], a[212:215], v[72:75]\n"
      "v_cvt_pk_bf16_f32 v23, v23, v27\n"
      "v_fmac_f32 v44, s46, v32\n"
      "v_fmac_f32 v45, s46, v33\n"
      "v_mfma_f32_16x16x32_bf16 v[52:55], v[180:183], a[192:195], v[52:55]\n"
      "ds_read_b128 a[208:211], v1 offset:128\n"
      "ds_write_addtid_b32 v23 offset:31744\n"
      "v_mfma_f32_16x16x32_bf16 v[56:59], v[172:175], a[192:195], v[56:59]\n"
      "ds_read2st64_b32 a[228:229], v14 offset0:177 offset1:179\n"
      "ds_read2st64_b32 a[230:231], v17 offset0:177 offset1:179\n"
      "v_fmac_f32 v46, s46, v34\n"
      "v_mfma_f32_16x16x32_bf16 v[60:63], v[164:167], a[192:195], v[60:63]\n"
      "s_waitcnt lgkmcnt(5)\n"
      "v_fmac_f32 v47, s46, v35\n"
      "v_fma_f32 v32, 0.5, v32, v92\n"
      "v_mfma_f32_16x16x32_bf16 v[64:67], v[176:179], a[196:199], v[64:67]\n"
      "ds_read_b128 a[212:215], v1 offset:4224\n"
      "v_fma_f32 v33, 0.5, v33, v93\n"
      "v_mfma_f32_16x16x32_bf16 v[68:71], v[168:171], a[196:199], v[68:71]\n"
      "buffer_store_dwordx4 a[224:227], v18, s[8:11], s58 offen offset:128 sc1\n"
      "v_fma_f32 v34, 0.5, v34, v94\n"
      "v_mfma_f32_16x16x32_bf16 v[72:75], v[160:163], a[196:199], v[72:75]\n"
      "v_fma_f32 v35, 0.5, v35, v95\n"
      "v_fmac_f32 v44, s48, v116\n"
      "v_mfma_f32_16x16x32_bf16 v[52:55], v[156:159], a[200:203], v[52:55]\n"
      "ds_read_b128 a[192:195], v2 offset:0\n"
      "v_fmac_f32 v45, s48, v117\n"
      "v_mfma_f32_16x16x32_bf16 v[56:59], v[148:151], a[200:203], v[56:59]\n"
      "s_add_u32 s54, s54, 193536\n"
      "ds_read_b64_tr_b16 a[248:249], v7 offset:20480\n"
      "ds_read_b64_tr_b16 a[250:251], v7 offset:20488\n"
      "v_fmac_f32 v46, s48, v118\n"
      "v_mfma_f32_16x16x32_bf16 v[60:63], v[140:143], a[200:203], v[60:63]\n"
      "s_add_u32 m0, s37, 82432\n"
      "v_fmac_f32 v47, s48, v119\n"
      "v_fmac_f32 v32, s45, v44\n"
      "v_mfma_f32_16x16x32_bf16 v[64:67], v[152:155], a[204:207], v[64:67]\n"
      "s_nop 0\n"
      "buffer_load_dwordx4 v8, s[4:7], s54 offen offset:0 nt lds\n"
      "buffer_load_dwordx4 v9, s[4:7], s54 offen offset:1024 nt lds\n"
      "ds_read_b128 a[196:199], v2 offset:4096\n"
      "v_fmac_f32 v33, s45, v45\n"
      "v_mfma_f32_16x16x32_bf16 v[68:71], v[144:147], a[204:207], v[68:71]\n"
      "s_mov_b32 m0, s64\n"
      "v_fmac_f32 v34, s45, v46\n"
      "v_fmac_f32_e64 v35, s45, v47\n"
      "v_mfma_f32_16x16x32_bf16 v[72:75], v[136:139], a[204:207], v[72:75]\n"
      "s_waitcnt lgkmcnt(1)\n"
      "v_fma_f32 v44, s47, v44, -v116\n"
      "v_fma_f32 v45, s47, v45, -v117\n"
      "v_mfma_f32_16x16x32_bf16 v[52:55], v[132:135], a[208:211], v[52:55]\n"
      "ds_read_b128 a[200:203], v2 offset:64\n"
      "v_fma_f32 v46, s47, v46, -v118\n"
      "v_mfma_f32_16x16x32_bf16 v[56:59], v[124:127], a[208:211], v[56:59]\n"
      "v_fma_f32 v47, s47, v47, -v119\n"
      "v_cvt_pk_bf16_f32 v32, v32, v33\n"
      "v_mfma_f32_16x16x32_bf16 v[60:63], a[184:187], a[208:211], v[60:63]\n"
      "ds_write_addtid_b32 v32 offset:41988\n"
      "v_cvt_pk_bf16_f32 v34, v34, v35\n"
      "v_mfma_f32_16x16x32_bf16 v[64:67], v[128:131], a[212:215], v[64:67]\n"
      "buffer_store_dwordx4 a[228:231], v18, s[8:11], s58 offen offset:256 sc1\n"
      "ds_read_b128 a[204:207], v2 offset:4160\n"
      "v_mfma_f32_16x16x32_bf16 v[68:71], a[188:191], a[212:215], v[68:71]\n"
      "ds_write_addtid_b32 v34 offset:42500\n"
      "ds_read2st64_b32 a[232:233], v12 offset0:0 offset1:2\n"
      "ds_read2st64_b32 a[234:235], v15 offset0:0 offset1:2\n"
      "v_cvt_pk_bf16_f32 v44, v44, v45\n"
      "v_cvt_pk_bf16_f32 v46, v46, v47\n"
      "v_mfma_f32_16x16x32_bf16 v[72:75], a[180:183], a[212:215], v[72:75]\n"
      "s_nop 0\n"
      "ds_write_addtid_b32 v44 offset:44036\n"
      "ds_write_addtid_b32 v46 offset:44548\n"
      "v_mfma_f32_16x16x32_bf16 v[52:55], a[176:179], a[192:195], v[52:55]\n"
      "s_nop 0\n"
      "ds_read_b128 a[208:211], v2 offset:128\n"
      "v_fmac_f32_e64 v48, s46, v36\n"
      "v_mfma_f32_4x4x4_16b_bf16 v[20:23], a[216:217], a[248:249], 0\n"
      "v_fmac_f32_e64 v49, s46, v37\n"
      "v_fmac_f32_e64 v50, s46, v38\n"
      "v_mfma_f32_4x4x4_16b_bf16 v[24:27], a[216:217], a[250:251], 0\n"
      "v_fmac_f32_e64 v51, s46, v39\n"
      "v_fma_f32 v36, 0.5, v36, v96\n"
      "v_mfma_f32_16x16x32_bf16 v[56:59], a[168:171], a[192:195], v[56:59]\n"
      "s_nop 0\n"
      "v_fma_f32 v37, 0.5, v37, v97\n"
      "v_fma_f32 v38, 0.5, v38, v98\n"
      "v_cvt_pk_bf16_f32 v20, v20, v24\n"
      "v_mfma_f32_16x16x32_bf16 v[60:63], a[160:163], a[192:195], v[60:63]\n"
      "s_waitcnt lgkmcnt(3)\n"
      "ds_write_addtid_b32 v20 offset:19968\n"
      "v_cvt_pk_bf16_f32 v21, v21, v25\n"
      "v_mfma_f32_16x16x32_bf16 v[64:67], a[172:175], a[196:199], v[64:67]\n"
      "s_add_u32 s54, s54, 196608\n"
      "ds_read_b128 a[212:215], v2 offset:4224\n"
      "ds_write_addtid_b32 v21 offset:24064\n"
      "v_mfma_f32_16x16x32_bf16 v[68:71], a[164:167], a[196:199], v[68:71]\n"
      "s_add_u32 m0, s37, 86536\n"
      "v_fma_f32 v39, 0.5, v39, v99\n"
      "v_fmac_f32_e64 v48, s48, v120\n"
      "v_cvt_pk_bf16_f32 v22, v22, v26\n"
      "v_mfma_f32_16x16x32_bf16 v[72:75], a[156:159], a[196:199], v[72:75]\n"
      "buffer_load_dwordx4 v8, s[4:7], s54 offen offset:0 nt lds\n"
      "buffer_load_dwordx4 v9, s[4:7], s54 offen offset:1024 nt lds\n"
      "v_mfma_f32_16x16x32_bf16 v[52:55], a[152:155], a[200:203], v[52:55]\n"
      "s_mov_b32 m0, s64\n"
      "ds_read_b128 a[192:195], v3 offset:0\n"
      "ds_read2st64_b32 a[236:237], v13 offset0:0 offset1:2\n"
      "v_mfma_f32_16x16x32_bf16 v[56:59], a[144:147], a[200:203], v[56:59]\n"
      "s_nop 0\n"
      "ds_write_addtid_b32 v22 offset:28160\n"
      "ds_read2st64_b32 a[238:239], v16 offset0:0 offset1:2\n"
      "v_fmac_f32 v49, s48, v121\n"
      "v_mfma_f32_16x16x32_bf16 v[60:63], a[136:139], a[200:203], v[60:63]\n"
      "v_fmac_f32 v50, s48, v122\n"
      "v_cvt_pk_bf16_f32 v23, v23, v27\n"
      "v_mfma_f32_16x16x32_bf16 v[64:67], a[148:151], a[204:207], v[64:67]\n"
      "ds_read_b128 a[196:199], v3 offset:64\n"
      "ds_write_addtid_b32 v23 offset:32256\n"
      "v_fmac_f32 v51, s48, v123\n"
      "v_mfma_f32_16x16x32_bf16 v[68:71], a[140:143], a[204:207], v[68:71]\n"
      "v_fmac_f32 v36, s45, v48\n"
      "v_fmac_f32 v37, s45, v49\n"
      "v_mfma_f32_16x16x32_bf16 v[72:75], a[132:135], a[204:207], v[72:75]\n"
      "s_waitcnt lgkmcnt(3)\n"
      "buffer_store_dwordx4 a[232:235], v18, s[8:11], s59 offen offset:0 sc1\n"
      "v_mfma_f32_16x16x32_bf16 v[52:55], a[128:131], a[208:211], v[52:55]\n"
      "ds_read_b128 a[200:203], v3 offset:128\n"
      "v_fmac_f32_e64 v38, s45, v50\n"
      "v_mfma_f32_16x16x32_bf16 v[56:59], a[120:123], a[208:211], v[56:59]\n"
      "ds_read2st64_b32 a[240:241], v14 offset0:0 offset1:2\n"
      "ds_read2st64_b32 a[242:243], v17 offset0:0 offset1:2\n"
      "v_fmac_f32 v39, s45, v51\n"
      "v_mfma_f32_16x16x32_bf16 v[60:63], a[112:115], a[208:211], v[60:63]\n"
      "v_fma_f32 v48, s47, v48, -v120\n"
      "v_fma_f32 v49, s47, v49, -v121\n"
      "v_mfma_f32_16x16x32_bf16 v[64:67], a[124:127], a[212:215], v[64:67]\n"
      "s_nop 0\n"
      "ds_read_b128 a[204:207], v4 offset:0\n"
      "v_fma_f32 v50, s47, v50, -v122\n"
      "v_mfma_f32_16x16x32_bf16 v[68:71], a[116:119], a[212:215], v[68:71]\n"
      "v_fma_f32 v51, s47, v51, -v123\n"
      "v_cvt_pk_bf16_f32 v36, v36, v37\n"
      "v_mfma_f32_16x16x32_bf16 v[72:75], a[108:111], a[212:215], v[72:75]\n"
      "ds_write_addtid_b32 v36 offset:37888\n"
      "v_cvt_pk_bf16_f32 v38, v38, v39\n"
      "v_add_f32 v52, v52, v64\n"
      "v_add_f32 v53, v53, v65\n"
      "v_mfma_f32_16x16x32_bf16 v[76:79], a[0:3], a[192:195], 0\n"
      "s_waitcnt lgkmcnt(2)\n"
      "ds_read_b128 a[208:211], v4 offset:64\n"
      "ds_write_addtid_b32 v38 offset:38400\n"
      "v_mfma_f32_16x16x32_bf16 v[80:83], a[4:7], a[192:195], 0\n"
      "buffer_store_dwordx4 a[236:239], v18, s[8:11], s59 offen offset:128 sc1\n"
      "v_mfma_f32_16x16x32_bf16 v[84:87], a[8:11], a[192:195], 0\n"
      "s_nop 0\n"
      "ds_write_addtid_b32 v52 offset:45312\n"
      "ds_write_addtid_b32 v53 offset:45824\n"
      "v_add_f32 v54, v54, v66\n"
      "v_mfma_f32_16x16x32_bf16 v[76:79], a[12:15], a[196:199], v[76:79]\n"
      "ds_read_b128 a[212:215], v4 offset:128\n"
      "ds_write_addtid_b32 v54 offset:46336\n"
      "v_mfma_f32_16x16x32_bf16 v[80:83], a[16:19], a[196:199], v[80:83]\n"
      "v_add_f32 v55, v55, v67\n"
      "v_add_f32 v56, v56, v68\n"
      "v_add_f32 v57, v57, v69\n"
      "v_mfma_f32_16x16x32_bf16 v[84:87], a[20:23], a[196:199], v[84:87]\n"
      "ds_write_addtid_b32 v55 offset:46848\n"
      "ds_write_addtid_b32 v56 offset:47360\n"
      "v_mfma_f32_16x16x32_bf16 v[76:79], a[24:27], a[200:203], v[76:79]\n"
      "ds_read_b128 a[192:195], v5 offset:0\n"
      "ds_write_addtid_b32 v57 offset:47872\n"
      "v_mfma_f32_16x16x32_bf16 v[80:83], a[28:31], a[200:203], v[80:83]\n"
      "v_add_f32_e64 v58, v58, v70\n"
      "v_cvt_pk_bf16_f32 v48, v48, v49\n"
      "v_add_f32 v59, v59, v71\n"
      "v_mfma_f32_16x16x32_bf16 v[84:87], a[32:35], a[200:203], v[84:87]\n"
      "s_waitcnt lgkmcnt(4)\n"
      "ds_write_addtid_b32 v48 offset:39936\n"
      "ds_write_addtid_b32 v58 offset:48384\n"
      "v_mfma_f32_16x16x32_bf16 v[76:79], a[36:39], a[204:207], v[76:79]\n"
      "buffer_store_dwordx4 a[240:243], v18, s[8:11], s59 offen offset:256 sc1\n"
      "ds_read_b128 a[196:199], v5 offset:64\n"
      "v_cvt_pk_bf16_f32 v50, v50, v51\n"
      "v_mfma_f32_16x16x32_bf16 v[80:83], a[40:43], a[204:207], v[80:83]\n"
      "ds_write_addtid_b32 v50 offset:40448\n"
      "ds_write_addtid_b32 v59 offset:48896\n"
      "v_mfma_f32_16x16x32_bf16 v[84:87], a[44:47], a[204:207], v[84:87]\n"
      "v_add_f32 v60, v60, v72\n"
      "v_add_f32 v61, v61, v73\n"
      "v_add_f32 v62, v62, v74\n"
      "v_add_f32 v63, v63, v75\n"
      "v_mfma_f32_16x16x32_bf16 v[76:79], a[48:51], a[208:211], v[76:79]\n"
      "ds_read_b128 a[200:203], v5 offset:128\n"
      "ds_write_addtid_b32 v60 offset:49408\n"
      "v_mfma_f32_16x16x32_bf16 v[80:83], a[52:55], a[208:211], v[80:83]\n"
      "ds_write_addtid_b32 v61 offset:49920\n"
      "ds_write_addtid_b32 v62 offset:50432\n"
      "v_mfma_f32_16x16x32_bf16 v[84:87], a[56:59], a[208:211], v[84:87]\n"
      "ds_write_addtid_b32 v63 offset:50944\n"
      "v_fma_f32 v40, -2.0, v64, v52\n"
      "v_mfma_f32_16x16x32_bf16 v[76:79], a[60:63], a[212:215], v[76:79]\n"
      "v_fma_f32 v41, -2.0, v65, v53\n"
      "v_fma_f32 v42, -2.0, v66, v54\n"
      "v_mfma_f32_16x16x32_bf16 v[80:83], a[64:67], a[212:215], v[80:83]\n"
      "s_waitcnt vmcnt(17)\n"
      "v_fma_f32 v43, -2.0, v67, v55\n"
      "v_fma_f32 v44, -2.0, v68, v56\n"
      "v_mfma_f32_16x16x32_bf16 v[84:87], a[68:71], a[212:215], v[84:87]\n"
      "s_waitcnt lgkmcnt(0)\n"
      "v_fma_f32 v45, -2.0, v69, v57\n"
      "s_barrier\n"
      "s_add_u32 s55, s55, 2\n"
      "s_cmp_lt_u32 s55, 6\n"
      "s_cbranch_scc1 LDloop_%=\n"
      "LDlast_%=:\n"
      "v_mfma_f32_16x16x32_bf16 v[76:79], a[72:75], a[192:195], v[76:79]\n"
      "s_add_u32 s58, s58, 3072\n"
      "ds_read_b128 a[204:207], v0 offset:16384\n"
      "ds_read2st64_b32 v[28:29], v6 offset0:0 offset1:2\n"
      "v_fma_f32 v46, -2.0, v70, v58\n"
      "v_mfma_f32_16x16x32_bf16 v[80:83], a[76:79], a[192:195], v[80:83]\n"
      "s_add_u32 s59, s58, 196608\n"
      "ds_read_b128 a[208:211], v0 offset:20480\n"
      "ds_read2st64_b32 v[30:31], v6 offset0:4 offset1:6\n"
      "v_mfma_f32_16x16x32_bf16 v[84:87], a[80:83], a[192:195], v[84:87]\n"
      "ds_read2st64_b32 v[32:33], v6 offset0:8 offset1:10\n"
      "ds_read2st64_b32 v[34:35], v6 offset0:12 offset1:14\n"
      "v_mfma_f32_16x16x32_bf16 v[76:79], a[84:87], a[196:199], v[76:79]\n"
      "ds_read_b128 a[212:215], v0 offset:16448\n"
      "v_fma_f32 v47, -2.0, v71, v59\n"
      "v_mfma_f32_16x16x32_bf16 v[80:83], a[88:91], a[196:199], v[80:83]\n"
      "v_fma_f32 v48, -2.0, v72, v60\n"
      "v_fma_f32 v49, -2.0, v73, v61\n"
      "v_fma_f32 v50, -2.0, v74, v62\n"
      "v_mfma_f32_16x16x32_bf16 v[84:87], a[92:95], a[196:199], v[84:87]\n"
      "ds_read_b64_tr_b16 a[244:245], v7 offset:36864\n"
      "ds_read_b64_tr_b16 a[246:247], v7 offset:36872\n"
      "v_mfma_f32_16x16x32_bf16 v[76:79], a[96:99], a[200:203], v[76:79]\n"
      "ds_read_b128 a[192:195], v0 offset:20544\n"
      "v_fma_f32 v51, -2.0, v75, v63\n"
      "v_mfma_f32_16x16x32_bf16 v[80:83], a[100:103], a[200:203], v[80:83]\n"
      "ds_read2st64_b32 v[36:37], v6 offset0:16 offset1:18\n"
      "ds_read2st64_b32 v[38:39], v6 offset0:20 offset1:22\n"
      "v_mfma_f32_16x16x32_bf16 v[84:87], a[104:107], a[200:203], v[84:87]\n"
      "s_waitcnt lgkmcnt(7)\n"
      "v_fmac_f32 v40, s46, v28\n"
      "v_fmac_f32 v41, s46, v29\n"
      "v_fmac_f32 v42, s46, v30\n"
      "v_mfma_f32_16x16x32_bf16 v[52:55], v[252:255], a[204:207], 0\n"
      "ds_read_b128 a[196:199], v0 offset:16512\n"
      "v_fmac_f32 v43, s46, v31\n"
      "v_mfma_f32_16x16x32_bf16 v[56:59], v[244:247], a[204:207], 0\n"
      "s_nop 0\n"
      "v_fma_f32 v28, 0.5, v28, v100\n"
      "v_fma_f32 v29, 0.5, v29, v101\n"
      "v_mfma_f32_16x16x32_bf16 v[60:63], v[236:239], a[204:207], 0\n"
      "v_fma_f32 v30, 0.5, v30, v102\n"
      "v_fma_f32 v31, 0.5, v31, v103\n"
      "v_fmac_f32 v40, s48, v76\n"
      "v_mfma_f32_16x16x32_bf16 v[64:67], v[248:251], a[208:211], 0\n"
      "ds_read_b128 a[200:203], v0 offset:20608\n"
      "v_fmac_f32 v41, s48, v77\n"
      "v_mfma_f32_16x16x32_bf16 v[68:71], v[240:243], a[208:211], 0\n"
      "v_fmac_f32 v42, s48, v78\n"
      "v_fmac_f32 v43, s48, v79\n"
      "v_mfma_f32_16x16x32_bf16 v[72:75], v[232:235], a[208:211], 0\n"
      "s_waitcnt lgkmcnt(2)\n"
      "v_fmac_f32 v28, s45, v40\n"
      "v_fmac_f32 v29, s45, v41\n"
      "v_fmac_f32 v30, s45, v42\n"
      "v_mfma_f32_16x16x32_bf16 v[52:55], v[228:231], a[212:215], v[52:55]\n"
      "ds_read_b128 a[204:207], v1 offset:16384\n"
      "v_fmac_f32_e64 v31, s45, v43\n"
      "v_mfma_f32_4x4x4_16b_bf16 v[20:23], a[216:217], a[244:245], 0\n"
      "v_fma_f32 v40, s47, v40, -v76\n"
      "v_mfma_f32_4x4x4_16b_bf16 v[24:27], a[216:217], a[246:247], 0\n"
      "v_fma_f32 v41, s47, v41, -v77\n"
      "v_fma_f32 v42, s47, v42, -v78\n"
      "v_mfma_f32_16x16x32_bf16 v[56:59], v[220:223], a[212:215], v[56:59]\n"
      "v_fma_f32 v43, s47, v43, -v79\n"
      "v_cvt_pk_bf16_f32 v28, v28, v29\n"
      "v_cvt_pk_bf16_f32 v20, v20, v24\n"
      "v_mfma_f32_16x16x32_bf16 v[60:63], v[212:215], a[212:215], v[60:63]\n"
      "ds_write_addtid_b32 v28 offset:32768\n"
      "ds_write_addtid_b32 v20 offset:3072\n"
      "v_mfma_f32_16x16x32_bf16 v[64:67], v[224:227], a[192:195], v[64:67]\n"
      "ds_read_b128 a[208:211], v1 offset:20480\n"
      "ds_read2st64_b32 a[220:221], v12 offset0:181 offset1:183\n"
      "v_mfma_f32_16x16x32_bf16 v[68:71], v[216:219], a[192:195], v[68:71]\n"
      "ds_read2st64_b32 a[222:223], v15 offset0:181 offset1:183\n"
      "v_cvt_pk_bf16_f32 v21, v21, v25\n"
      "v_cvt_pk_bf16_f32 v22, v22, v26\n"
      "v_mfma_f32_16x16x32_bf16 v[72:75], v[208:211], a[192:195], v[72:75]\n"
      "s_waitcnt lgkmcnt(5)\n"
      "ds_write_addtid_b32 v21 offset:7168\n"
      "ds_write_addtid_b32 v22 offset:11264\n"
      "v_mfma_f32_16x16x32_bf16 v[52:55], v[204:207], a[196:199], v[52:55]\n"
      "s_nop 0\n"
      "ds_read_b128 a[212:215], v1 offset:16448\n"
      "v_cvt_pk_bf16_f32 v30, v30, v31\n"
      "v_mfma_f32_16x16x32_bf16 v[56:59], v[196:199], a[196:199], v[56:59]\n"
      "ds_write_addtid_b32 v30 offset:33280\n"
      "v_cvt_pk_bf16_f32 v40, v40, v41\n"
      "v_mfma_f32_16x16x32_bf16 v[60:63], v[188:191], a[196:199], v[60:63]\n"
      "ds_write_addtid_b32 v40 offset:34816\n"
      "v_cvt_pk_bf16_f32 v42, v42, v43\n"
      "v_cvt_pk_bf16_f32 v23, v23, v27\n"
      "v_mfma_f32_16x16x32_bf16 v[64:67], v[200:203], a[200:203], v[64:67]\n"
      "ds_read_b128 a[192:195], v1 offset:20544\n"
      "ds_write_addtid_b32 v42 offset:35328\n"
      "v_mfma_f32_16x16x32_bf16 v[68:71], v[192:195], a[200:203], v[68:71]\n"
      "ds_write_addtid_b32 v23 offset:15360\n"
      "v_fmac_f32 v44, s46, v32\n"
      "v_mfma_f32_16x16x32_bf16 v[72:75], v[184:187], a[200:203], v[72:75]\n"
      "v_fmac_f32 v45, s46, v33\n"
      "v_fmac_f32 v46, s46, v34\n"
      "v_fmac_f32 v47, s46, v35\n"
      "v_mfma_f32_16x16x32_bf16 v[52:55], v[180:183], a[204:207], v[52:55]\n"
      "ds_read_b128 a[196:199], v1 offset:16512\n"
      "v_fma_f32 v32, 0.5, v32, v104\n"
      "v_mfma_f32_16x16x32_bf16 v[56:59], v[172:175], a[204:207], v[56:59]\n"
      "s_nop 0\n"
      "ds_read2st64_b32 a[224:225], v13 offset0:181 offset1:183\n"
      "ds_read2st64_b32 a[226:227], v16 offset0:181 offset1:183\n"
      "v_mfma_f32_16x16x32_bf16 v[60:63], v[164:167], a[204:207], v[60:63]\n"
      "s_waitcnt lgkmcnt(4)\n"
      "v_fma_f32 v33, 0.5, v33, v105\n"
      "v_fma_f32 v34, 0.5, v34, v106\n"
      "v_mfma_f32_16x16x32_bf16 v[64:67], v[176:179], a[208:211], v[64:67]\n"
      "ds_read_b128 a[200:203], v1 offset:20608\n"
      "v_fma_f32 v35, 0.5, v35, v107\n"
      "v_fmac_f32 v44, s48, v80\n"
      "v_mfma_f32_16x16x32_bf16 v[68:71], v[168:171], a[208:211], v[68:71]\n"
      "buffer_store_dwordx4 a[220:223], v18, s[8:11], s58 offen offset:0 sc1\n"
      "v_mfma_f32_16x16x32_bf16 v[72:75], v[160:163], a[208:211], v[72:75]\n"
      "s_nop 0\n"
      "ds_read_b64_tr_b16 a[248:249], v7 offset:37376\n"
      "ds_read_b64_tr_b16 a[250:251], v7 offset:37384\n"
      "v_mfma_f32_16x16x32_bf16 v[52:55], v[156:159], a[212:215], v[52:55]\n"
      "ds_read_b128 a[204:207], v2 offset:16384\n"
      "v_fmac_f32 v45, s48, v81\n"
      "v_mfma_f32_16x16x32_bf16 v[56:59], v[148:151], a[212:215], v[56:59]\n"
      "v_fmac_f32 v46, s48, v82\n"
      "v_fmac_f32 v47, s48, v83\n"
      "v_fmac_f32 v32, s45, v44\n"
      "v_mfma_f32_16x16x32_bf16 v[60:63], v[140:143], a[212:215], v[60:63]\n"
      "v_fmac_f32 v33, s45, v45\n"
      "v_fmac_f32 v34, s45, v46\n"
      "v_mfma_f32_16x16x32_bf16 v[64:67], v[152:155], a[192:195], v[64:67]\n"
      "ds_read_b128 a[208:211], v2 offset:20480\n"
      "v_fmac_f32 v35, s45, v47\n"
      "v_mfma_f32_16x16x32_bf16 v[68:71], v[144:147], a[192:195], v[68:71]\n"
      "v_fma_f32 v44, s47, v44, -v80\n"
      "v_fma_f32 v45, s47, v45, -v81\n"
      "v_mfma_f32_16x16x32_bf16 v[72:75], v[136:139], a[192:195], v[72:75]\n"
      "s_waitcnt lgkmcnt(1)\n"
      "v_fma_f32 v46, s47, v46, -v82\n"
      "v_fma_f32 v47, s47, v47, -v83\n"
      "v_mfma_f32_16x16x32_bf16 v[52:55], v[132:135], a[196:199], v[52:55]\n"
      "ds_read_b128 a[212:215], v2 offset:16448\n"
      "v_cvt_pk_bf16_f32 v32, v32, v33\n"
      "v_mfma_f32_16x16x32_bf16 v[56:59], v[124:127], a[196:199], v[56:59]\n"
      "ds_write_addtid_b32 v32 offset:40964\n"
      "v_cvt_pk_bf16_f32 v34, v34, v35\n"
      "v_mfma_f32_16x16x32_bf16 v[60:63], a[184:187], a[196:199], v[60:63]\n"
      "ds_write_addtid_b32 v34 offset:41476\n"
      "v_cvt_pk_bf16_f32 v44, v44, v45\n"
      "v_cvt_pk_bf16_f32 v46, v46, v47\n"
      "v_mfma_f32_16x16x32_bf16 v[64:67], v[128:131], a[200:203], v[64:67]\n"
      "buffer_store_dwordx4 a[224:227], v18, s[8:11], s58 offen offset:128 sc1\n"
      "ds_read_b128 a[192:195], v2 offset:20544\n"
      "v_mfma_f32_16x16x32_bf16 v[68:71], a[188:191], a[200:203], v[68:71]\n"
      "ds_write_addtid_b32 v44 offset:43012\n"
      "ds_write_addtid_b32 v46 offset:43524\n"
      "v_fmac_f32 v48, s46, v36\n"
      "v_mfma_f32_4x4x4_16b_bf16 v[20:23], a[216:217], a[248:249], 0\n"
      "v_fmac_f32 v49, s46, v37\n"
      "v_fmac_f32_e64 v50, s46, v38\n"
      "v_mfma_f32_4x4x4_16b_bf16 v[24:27], a[216:217], a[250:251], 0\n"
      "ds_read2st64_b32 a[228:229], v14 offset0:181 offset1:183\n"
      "ds_read2st64_b32 a[230:231], v17 offset0:181 offset1:183\n"
      "v_mfma_f32_16x16x32_bf16 v[72:75], a[180:183], a[200:203], v[72:75]\n"
      "v_fmac_f32 v51, s46, v39\n"
      "v_fma_f32 v36, 0.5, v36, v108\n"
      "v_mfma_f32_16x16x32_bf16 v[52:55], a[176:179], a[204:207], v[52:55]\n"
      "ds_read_b128 a[196:199], v2 offset:16512\n"
      "v_cvt_pk_bf16_f32 v20, v20, v24\n"
      "v_mfma_f32_16x16x32_bf16 v[56:59], a[168:171], a[204:207], v[56:59]\n"
      "s_nop 0\n"
      "ds_write_addtid_b32 v20 offset:3584\n"
      "v_fma_f32 v37, 0.5, v37, v109\n"
      "v_cvt_pk_bf16_f32 v21, v21, v25\n"
      "v_mfma_f32_16x16x32_bf16 v[60:63], a[160:163], a[204:207], v[60:63]\n"
      "s_waitcnt lgkmcnt(4)\n"
      "ds_write_addtid_b32 v21 offset:7680\n"
      "v_fma_f32 v38, 0.5, v38, v110\n"
      "v_mfma_f32_16x16x32_bf16 v[64:67], a[172:175], a[208:211], v[64:67]\n"
      "s_nop 0\n"
      "ds_read_b128 a[200:203], v2 offset:20608\n"
      "v_fma_f32 v39, 0.5, v39, v111\n"
      "v_mfma_f32_16x16x32_bf16 v[68:71], a[164:167], a[208:211], v[68:71]\n"
      "v_cvt_pk_bf16_f32 v22, v22, v26\n"
      "v_fmac_f32 v48, s48, v84\n"
      "v_cvt_pk_bf16_f32 v23, v23, v27\n"
      "v_mfma_f32_16x16x32_bf16 v[72:75], a[156:159], a[208:211], v[72:75]\n"
      "s_nop 0\n"
      "ds_read2st64_b32 a[232:233], v12 offset0:4 offset1:6\n"
      "ds_write_addtid_b32 v22 offset:11776\n"
      "v_mfma_f32_16x16x32_bf16 v[52:55], a[152:155], a[212:215], v[52:55]\n"
      "ds_read_b128 a[204:207], v3 offset:16896\n"
      "ds_read2st64_b32 a[234:235], v15 offset0:4 offset1:6\n"
      "v_mfma_f32_16x16x32_bf16 v[56:59], a[144:147], a[212:215], v[56:59]\n"
      "ds_write_addtid_b32 v23 offset:15872\n"
      "v_fmac_f32 v49, s48, v85\n"
      "v_fmac_f32 v50, s48, v86\n"
      "v_mfma_f32_16x16x32_bf16 v[60:63], a[136:139], a[212:215], v[60:63]\n"
      "v_fmac_f32 v51, s48, v87\n"
      "v_fmac_f32 v36, s45, v48\n"
      "v_mfma_f32_16x16x32_bf16 v[64:67], a[148:151], a[192:195], v[64:67]\n"
      "ds_read_b128 a[208:211], v3 offset:16960\n"
      "v_fmac_f32 v37, s45, v49\n"
      "v_mfma_f32_16x16x32_bf16 v[68:71], a[140:143], a[192:195], v[68:71]\n"
      "s_waitcnt lgkmcnt(4)\n"
      "v_fmac_f32 v38, s45, v50\n"
      "v_fmac_f32 v39, s45, v51\n"
      "v_mfma_f32_16x16x32_bf16 v[72:75], a[132:135], a[192:195], v[72:75]\n"
      "buffer_store_dwordx4 a[228:231], v18, s[8:11], s58 offen offset:256 sc1\n"
      "v_mfma_f32_16x16x32_bf16 v[52:55], a[128:131], a[196:199], v[52:55]\n"
      "ds_read_b128 a[212:215], v3 offset:17024\n"
      "v_fma_f32 v48, s47, v48, -v84\n"
      "v_mfma_f32_16x16x32_bf16 v[56:59], a[120:123], a[196:199], v[56:59]\n"
      "ds_read2st64_b32 a[236:237], v13 offset0:4 offset1:6\n"
      "ds_read2st64_b32 a[238:239], v16 offset0:4 offset1:6\n"
      "v_fma_f32 v49, s47, v49, -v85\n"
      "v_mfma_f32_16x16x32_bf16 v[60:63], a[112:115], a[196:199], v[60:63]\n"
      "v_fma_f32 v50, s47, v50, -v86\n"
      "v_fma_f32 v51, s47, v51, -v87\n"
      "v_mfma_f32_16x16x32_bf16 v[64:67], a[124:127], a[200:203], v[64:67]\n"
      "ds_read_b128 a[192:195], v4 offset:16896\n"
      "v_cvt_pk_bf16_f32 v36, v36, v37\n"
      "v_mfma_f32_16x16x32_bf16 v[68:71], a[116:119], a[200:203], v[68:71]\n"
      "ds_write_addtid_b32 v36 offset:36864\n"
      "v_cvt_pk_bf16_f32 v38, v38, v39\n"
      "v_mfma_f32_16x16x32_bf16 v[72:75], a[108:111], a[200:203], v[72:75]\n"
      "s_waitcnt lgkmcnt(4)\n"
      "ds_write_addtid_b32 v38 offset:37376\n"
      "v_add_f32 v52, v52, v64\n"
      "v_add_f32 v53, v53, v65\n"
      "v_mfma_f32_16x16x32_bf16 v[88:91], a[0:3], a[204:207], 0\n"
      "ds_read_b128 a[196:199], v4 offset:16960\n"
      "ds_write_addtid_b32 v52 offset:51456\n"
      "v_add_f32 v54, v54, v66\n"
      "v_mfma_f32_16x16x32_bf16 v[92:95], a[4:7], a[204:207], 0\n"
      "buffer_store_dwordx4 a[232:235], v18, s[8:11], s59 offen offset:0 sc1\n"
      "v_mfma_f32_16x16x32_bf16 v[96:99], a[8:11], a[204:207], 0\n"
      "ds_write_addtid_b32 v53 offset:51968\n"
      "ds_write_addtid_b32 v54 offset:52480\n"
      "v_mfma_f32_16x16x32_bf16 v[88:91], a[12:15], a[208:211], v[88:91]\n"
      "ds_read_b128 a[200:203], v4 offset:17024\n"
      "v_add_f32 v55, v55, v67\n"
      "v_mfma_f32_16x16x32_bf16 v[92:95], a[16:19], a[208:211], v[92:95]\n"
      "ds_write_addtid_b32 v55 offset:52992\n"
      "v_add_f32 v56, v56, v68\n"
      "v_cvt_pk_bf16_f32 v48, v48, v49\n"
      "v_mfma_f32_16x16x32_bf16 v[96:99], a[20:23], a[208:211], v[96:99]\n"
      "ds_write_addtid_b32 v48 offset:38912\n"
      "ds_write_addtid_b32 v56 offset:53504\n"
      "v_mfma_f32_16x16x32_bf16 v[88:91], a[24:27], a[212:215], v[88:91]\n"
      "ds_read_b128 a[204:207], v5 offset:16896\n"
      "v_add_f32 v57, v57, v69\n"
      "v_mfma_f32_16x16x32_bf16 v[92:95], a[28:31], a[212:215], v[92:95]\n"
      "s_nop 0\n"
      "ds_write_addtid_b32 v57 offset:54016\n"
      "v_add_f32 v58, v58, v70\n"
      "v_add_f32 v59, v59, v71\n"
      "v_mfma_f32_16x16x32_bf16 v[96:99], a[32:35], a[212:215], v[96:99]\n"
      "s_waitcnt lgkmcnt(4)\n"
      "ds_write_addtid_b32 v58 offset:54528\n"
      "ds_write_addtid_b32 v59 offset:55040\n"
      "v_mfma_f32_16x16x32_bf16 v[88:91], a[36:39], a[192:195], v[88:91]\n"
      "s_nop 0\n"
      "buffer_store_dwordx4 a[236:239], v18, s[8:11], s59 offen offset:128 sc1\n"
      "ds_read_b128 a[208:211], v5 offset:16960\n"
      "v_cvt_pk_bf16_f32 v50, v50, v51\n"
      "v_mfma_f32_16x16x32_bf16 v[92:95], a[40:43], a[192:195], v[92:95]\n"
      "ds_write_addtid_b32 v50 offset:39424\n"
      "v_add_f32 v60, v60, v72\n"
      "v_mfma_f32_16x16x32_bf16 v[96:99], a[44:47], a[192:195], v[96:99]\n"
      "ds_write_addtid_b32 v60 offset:55552\n"
      "v_add_f32 v61, v61, v73\n"
      "v_add_f32 v62, v62, v74\n"
      "v_add_f32 v63, v63, v75\n"
      "v_mfma_f32_16x16x32_bf16 v[88:91], a[48:51], a[196:199], v[88:91]\n"
      "ds_read_b128 a[212:215], v5 offset:17024\n"
      "ds_write_addtid_b32 v61 offset:56064\n"
      "v_mfma_f32_16x16x32_bf16 v[92:95], a[52:55], a[196:199], v[92:95]\n"
      "ds_write_addtid_b32 v62 offset:56576\n"
      "ds_write_addtid_b32 v63 offset:57088\n"
      "v_mfma_f32_16x16x32_bf16 v[96:99], a[56:59], a[196:199], v[96:99]\n"
      "v_fma_f32 v40, -2.0, v64, v52\n"
      "v_fma_f32 v41, -2.0, v65, v53\n"
      "v_mfma_f32_16x16x32_bf16 v[88:91], a[60:63], a[200:203], v[88:91]\n"
      "s_nop 0\n"
      "v_fma_f32 v42, -2.0, v66, v54\n"
      "v_fma_f32 v43, -2.0, v67, v55\n"
      "v_mfma_f32_16x16x32_bf16 v[92:95], a[64:67], a[200:203], v[92:95]\n"
      "s_waitcnt vmcnt(14)\n"
      "v_fma_f32 v44, -2.0, v68, v56\n"
      "v_fma_f32 v45, -2.0, v69, v57\n"
      "v_mfma_f32_16x16x32_bf16 v[96:99], a[68:71], a[200:203], v[96:99]\n"
      "s_waitcnt lgkmcnt(0)\n"
      "s_barrier\n"
      "v_mfma_f32_16x16x32_bf16 v[88:91], a[72:75], a[204:207], v[88:91]\n"
      "s_add_u32 s62, s62, 786432\n"
      "ds_read_b128 a[192:195], v0 offset:0\n"
      "ds_read2st64_b32 v[28:29], v6 offset0:24 offset1:26\n"
      "v_fma_f32 v46, -2.0, v70, v58\n"
      "v_mfma_f32_16x16x32_bf16 v[92:95], a[76:79], a[204:207], v[92:95]\n"
      "s_add_u32 s58, s62, 0\n"
      "ds_read_b128 a[196:199], v0 offset:4096\n"
      "ds_read2st64_b32 v[30:31], v6 offset0:28 offset1:30\n"
      "v_mfma_f32_16x16x32_bf16 v[96:99], a[80:83], a[204:207], v[96:99]\n"
      "s_add_u32 s59, s58, 196608\n"
      "ds_read2st64_b32 v[32:33], v6 offset0:32 offset1:34\n"
      "ds_read2st64_b32 v[34:35], v6 offset0:36 offset1:38\n"
      "v_mfma_f32_16x16x32_bf16 v[88:91], a[84:87], a[208:211], v[88:91]\n"
      "s_nop 0\n"
      "ds_read_b128 a[200:203], v0 offset:64\n"
      "v_fma_f32 v47, -2.0, v71, v59\n"
      "v_mfma_f32_16x16x32_bf16 v[92:95], a[88:91], a[208:211], v[92:95]\n"
      "v_fma_f32 v48, -2.0, v72, v60\n"
      "v_fma_f32 v49, -2.0, v73, v61\n"
      "v_fma_f32 v50, -2.0, v74, v62\n"
      "v_mfma_f32_16x16x32_bf16 v[96:99], a[92:95], a[208:211], v[96:99]\n"
      "ds_read_b64_tr_b16 a[244:245], v7 offset:53760\n"
      "ds_read_b64_tr_b16 a[246:247], v7 offset:53768\n"
      "v_mfma_f32_16x16x32_bf16 v[88:91], a[96:99], a[212:215], v[88:91]\n"
      "ds_read_b128 a[204:207], v0 offset:4160\n"
      "v_fma_f32 v51, -2.0, v75, v63\n"
      "v_mfma_f32_16x16x32_bf16 v[92:95], a[100:103], a[212:215], v[92:95]\n"
      "ds_read2st64_b32 v[36:37], v6 offset0:40 offset1:42\n"
      "ds_read2st64_b32 v[38:39], v6 offset0:44 offset1:46\n"
      "v_mfma_f32_16x16x32_bf16 v[96:99], a[104:107], a[212:215], v[96:99]\n"
      "s_waitcnt lgkmcnt(7)\n"
      "v_fmac_f32 v40, s46, v28\n"
      "v_fmac_f32 v41, s46, v29\n"
      "v_fmac_f32 v42, s46, v30\n"
      "v_mfma_f32_16x16x32_bf16 v[52:55], v[252:255], a[192:195], 0\n"
      "ds_read_b128 a[208:211], v0 offset:128\n"
      "v_fmac_f32 v43, s46, v31\n"
      "v_mfma_f32_16x16x32_bf16 v[56:59], v[244:247], a[192:195], 0\n"
      "s_nop 0\n"
      "v_fma_f32 v28, 0.5, v28, v112\n"
      "v_fma_f32 v29, 0.5, v29, v113\n"
      "v_mfma_f32_16x16x32_bf16 v[60:63], v[236:239], a[192:195], 0\n"
      "v_fma_f32 v30, 0.5, v30, v114\n"
      "v_fma_f32 v31, 0.5, v31, v115\n"
      "v_fmac_f32 v40, s48, v88\n"
      "v_mfma_f32_16x16x32_bf16 v[64:67], v[248:251], a[196:199], 0\n"
      "ds_read_b128 a[212:215], v0 offset:4224\n"
      "v_fmac_f32 v41, s48, v89\n"
      "v_mfma_f32_16x16x32_bf16 v[68:71], v[240:243], a[196:199], 0\n"
      "v_fmac_f32 v42, s48, v90\n"
      "v_fmac_f32 v43, s48, v91\n"
      "v_mfma_f32_16x16x32_bf16 v[72:75], v[232:235], a[196:199], 0\n"
      "s_waitcnt lgkmcnt(2)\n"
      "v_fmac_f32 v28, s45, v40\n"
      "v_fmac_f32 v29, s45, v41\n"
      "v_fmac_f32 v30, s45, v42\n"
      "v_mfma_f32_16x16x32_bf16 v[52:55], v[228:231], a[200:203], v[52:55]\n"
      "ds_read_b128 a[192:195], v1 offset:0\n"
      "v_fmac_f32_e64 v31, s45, v43\n"
      "v_mfma_f32_4x4x4_16b_bf16 v[20:23], a[216:217], a[244:245], 0\n"
      "v_fma_f32 v40, s47, v40, -v88\n"
      "v_mfma_f32_4x4x4_16b_bf16 v[24:27], a[216:217], a[246:247], 0\n"
      "v_fma_f32 v41, s47, v41, -v89\n"
      "v_fma_f32 v42, s47, v42, -v90\n"
      "v_mfma_f32_16x16x32_bf16 v[56:59], v[220:223], a[200:203], v[56:59]\n"
      "v_fma_f32 v43, s47, v43, -v91\n"
      "v_cvt_pk_bf16_f32 v28, v28, v29\n"
      "v_cvt_pk_bf16_f32 v20, v20, v24\n"
      "v_mfma_f32_16x16x32_bf16 v[60:63], v[212:215], a[200:203], v[60:63]\n"
      "ds_write_addtid_b32 v28 offset:33792\n"
      "ds_write_addtid_b32 v20 offset:19456\n"
      "v_mfma_f32_16x16x32_bf16 v[64:67], v[224:227], a[204:207], v[64:67]\n"
      "ds_read_b128 a[196:199], v1 offset:4096\n"
      "ds_read2st64_b32 a[220:221], v12 offset0:177 offset1:179\n"
      "v_mfma_f32_16x16x32_bf16 v[68:71], v[216:219], a[204:207], v[68:71]\n"
      "ds_read2st64_b32 a[222:223], v15 offset0:177 offset1:179\n"
      "v_cvt_pk_bf16_f32 v21, v21, v25\n"
      "v_cvt_pk_bf16_f32 v22, v22, v26\n"
      "v_mfma_f32_16x16x32_bf16 v[72:75], v[208:211], a[204:207], v[72:75]\n"
      "s_waitcnt lgkmcnt(5)\n"
      "ds_write_addtid_b32 v21 offset:23552\n"
      "ds_write_addtid_b32 v22 offset:27648\n"
      "v_mfma_f32_16x16x32_bf16 v[52:55], v[204:207], a[208:211], v[52:55]\n"
      "s_nop 0\n"
      "ds_read_b128 a[200:203], v1 offset:64\n"
      "v_cvt_pk_bf16_f32 v30, v30, v31\n"
      "v_mfma_f32_16x16x32_bf16 v[56:59], v[196:199], a[208:211], v[56:59]\n"
      "ds_write_addtid_b32 v30 offset:34304\n"
      "v_cvt_pk_bf16_f32 v40, v40, v41\n"
      "v_mfma_f32_16x16x32_bf16 v[60:63], v[188:191], a[208:211], v[60:63]\n"
      "ds_write_addtid_b32 v40 offset:35840\n"
      "v_cvt_pk_bf16_f32 v42, v42, v43\n"
      "v_cvt_pk_bf16_f32 v23, v23, v27\n"
      "v_mfma_f32_16x16x32_bf16 v[64:67], v[200:203], a[212:215], v[64:67]\n"
      "ds_read_b128 a[204:207], v1 offset:4160\n"
      "ds_write_addtid_b32 v42 offset:36352\n"
      "v_mfma_f32_16x16x32_bf16 v[68:71], v[192:195], a[212:215], v[68:71]\n"
      "ds_write_addtid_b32 v23 offset:31744\n"
      "v_fmac_f32 v44, s46, v32\n"
      "v_mfma_f32_16x16x32_bf16 v[72:75], v[184:187], a[212:215], v[72:75]\n"
      "v_fmac_f32 v45, s46, v33\n"
      "v_fmac_f32 v46, s46, v34\n"
      "v_fmac_f32 v47, s46, v35\n"
      "v_mfma_f32_16x16x32_bf16 v[52:55], v[180:183], a[192:195], v[52:55]\n"
      "ds_read_b128 a[208:211], v1 offset:128\n"
      "v_fma_f32 v32, 0.5, v32, v116\n"
      "v_mfma_f32_16x16x32_bf16 v[56:59], v[172:175], a[192:195], v[56:59]\n"
      "s_nop 0\n"
      "ds_read2st64_b32 a[224:225], v13 offset0:177 offset1:179\n"
      "ds_read2st64_b32 a[226:227], v16 offset0:177 offset1:179\n"
      "v_mfma_f32_16x16x32_bf16 v[60:63], v[164:167], a[192:195], v[60:63]\n"
      "s_waitcnt lgkmcnt(4)\n"
      "v_fma_f32 v33, 0.5, v33, v117\n"
      "v_fma_f32 v34, 0.5, v34, v118\n"
      "v_mfma_f32_16x16x32_bf16 v[64:67], v[176:179], a[196:199], v[64:67]\n"
      "ds_read_b128 a[212:215], v1 offset:4224\n"
      "v_fma_f32 v35, 0.5, v35, v119\n"
      "v_fmac_f32 v44, s48, v92\n"
      "v_mfma_f32_16x16x32_bf16 v[68:71], v[168:171], a[196:199], v[68:71]\n"
      "buffer_store_dwordx4 a[220:223], v18, s[8:11], s58 offen offset:0 sc1\n"
      "v_mfma_f32_16x16x32_bf16 v[72:75], v[160:163], a[196:199], v[72:75]\n"
      "s_nop 0\n"
      "ds_read_b64_tr_b16 a[248:249], v7 offset:54272\n"
      "ds_read_b64_tr_b16 a[250:251], v7 offset:54280\n"
      "v_mfma_f32_16x16x32_bf16 v[52:55], v[156:159], a[200:203], v[52:55]\n"
      "ds_read_b128 a[192:195], v2 offset:0\n"
      "v_fmac_f32 v45, s48, v93\n"
      "v_mfma_f32_16x16x32_bf16 v[56:59], v[148:151], a[200:203], v[56:59]\n"
      "v_fmac_f32 v46, s48, v94\n"
      "v_fmac_f32 v47, s48, v95\n"
      "v_fmac_f32 v32, s45, v44\n"
      "v_mfma_f32_16x16x32_bf16 v[60:63], v[140:143], a[200:203], v[60:63]\n"
      "v_fmac_f32 v33, s45, v45\n"
      "v_fmac_f32 v34, s45, v46\n"
      "v_mfma_f32_16x16x32_bf16 v[64:67], v[152:155], a[204:207], v[64:67]\n"
      "ds_read_b128 a[196:199], v2 offset:4096\n"
      "v_fmac_f32 v35, s45, v47\n"
      "v_mfma_f32_16x16x32_bf16 v[68:71], v[144:147], a[204:207], v[68:71]\n"
      "v_fma_f32 v44, s47, v44, -v92\n"
      "v_fma_f32 v45, s47, v45, -v93\n"
      "v_mfma_f32_16x16x32_bf16 v[72:75], v[136:139], a[204:207], v[72:75]\n"
      "s_waitcnt lgkmcnt(1)\n"
      "v_fma_f32 v46, s47, v46, -v94\n"
      "v_fma_f32 v47, s47, v47, -v95\n"
      "v_mfma_f32_16x16x32_bf16 v[52:55], v[132:135], a[208:211], v[52:55]\n"
      "ds_read_b128 a[200:203], v2 offset:64\n"
      "v_cvt_pk_bf16_f32 v32, v32, v33\n"
      "v_mfma_f32_16x16x32_bf16 v[56:59], v[124:127], a[208:211], v[56:59]\n"
      "ds_write_addtid_b32 v32 offset:41988\n"
      "v_cvt_pk_bf16_f32 v34, v34, v35\n"
      "v_mfma_f32_16x16x32_bf16 v[60:63], a[184:187], a[208:211], v[60:63]\n"
      "ds_write_addtid_b32 v34 offset:42500\n"
      "v_cvt_pk_bf16_f32 v44, v44, v45\n"
      "v_cvt_pk_bf16_f32 v46, v46, v47\n"
      "v_mfma_f32_16x16x32_bf16 v[64:67], v[128:131], a[212:215], v[64:67]\n"
      "buffer_store_dwordx4 a[224:227], v18, s[8:11], s58 offen offset:128 sc1\n"
      "ds_read_b128 a[204:207], v2 offset:4160\n"
      "v_mfma_f32_16x16x32_bf16 v[68:71], a[188:191], a[212:215], v[68:71]\n"
      "ds_write_addtid_b32 v44 offset:44036\n"
      "ds_write_addtid_b32 v46 offset:44548\n"
      "v_fmac_f32 v48, s46, v36\n"
      "v_mfma_f32_4x4x4_16b_bf16 v[20:23], a[216:217], a[248:249], 0\n"
      "v_fmac_f32 v49, s46, v37\n"
      "v_fmac_f32_e64 v50, s46, v38\n"
      "v_mfma_f32_4x4x4_16b_bf16 v[24:27], a[216:217], a[250:251], 0\n"
      "ds_read2st64_b32 a[228:229], v14 offset0:177 offset1:179\n"
      "ds_read2st64_b32 a[230:231], v17 offset0:177 offset1:179\n"
      "v_mfma_f32_16x16x32_bf16 v[72:75], a[180:183], a[212:215], v[72:75]\n"
      "v_fmac_f32 v51, s46, v39\n"
      "v_fma_f32 v36, 0.5, v36, v120\n"
      "v_mfma_f32_16x16x32_bf16 v[52:55], a[176:179], a[192:195], v[52:55]\n"
      "ds_read_b128 a[208:211], v2 offset:128\n"
      "v_cvt_pk_bf16_f32 v20, v20, v24\n"
      "v_mfma_f32_16x16x32_bf16 v[56:59], a[168:171], a[192:195], v[56:59]\n"
      "s_nop 0\n"
      "ds_write_addtid_b32 v20 offset:19968\n"
      "v_fma_f32 v37, 0.5, v37, v121\n"
      "v_cvt_pk_bf16_f32 v21, v21, v25\n"
      "v_mfma_f32_16x16x32_bf16 v[60:63], a[160:163], a[192:195], v[60:63]\n"
      "s_waitcnt lgkmcnt(4)\n"
      "ds_write_addtid_b32 v21 offset:24064\n"
      "v_fma_f32 v38, 0.5, v38, v122\n"
      "v_mfma_f32_16x16x32_bf16 v[64:67], a[172:175], a[196:199], v[64:67]\n"
      "s_nop 0\n"
      "ds_read_b128 a[212:215], v2 offset:4224\n"
      "v_fma_f32 v39, 0.5, v39, v123\n"
      "v_mfma_f32_16x16x32_bf16 v[68:71], a[164:167], a[196:199], v[68:71]\n"
      "v_cvt_pk_bf16_f32 v22, v22, v26\n"
      "v_fmac_f32 v48, s48, v96\n"
      "v_cvt_pk_bf16_f32 v23, v23, v27\n"
      "v_mfma_f32_16x16x32_bf16 v[72:75], a[156:159], a[196:199], v[72:75]\n"
      "s_nop 0\n"
      "ds_read2st64_b32 a[232:233], v12 offset0:0 offset1:2\n"
      "ds_write_addtid_b32 v22 offset:28160\n"
      "v_mfma_f32_16x16x32_bf16 v[52:55], a[152:155], a[200:203], v[52:55]\n"
      "ds_read_b128 a[192:195], v3 offset:33792\n"
      "ds_read2st64_b32 a[234:235], v15 offset0:0 offset1:2\n"
      "v_mfma_f32_16x16x32_bf16 v[56:59], a[144:147], a[200:203], v[56:59]\n"
      "ds_write_addtid_b32 v23 offset:32256\n"
      "v_fmac_f32 v49, s48, v97\n"
      "v_fmac_f32 v50, s48, v98\n"
      "v_mfma_f32_16x16x32_bf16 v[60:63], a[136:139], a[200:203], v[60:63]\n"
      "v_fmac_f32 v51, s48, v99\n"
      "v_fmac_f32 v36, s45, v48\n"
      "v_mfma_f32_16x16x32_bf16 v[64:67], a[148:151], a[204:207], v[64:67]\n"
      "ds_read_b128 a[196:199], v3 offset:33856\n"
      "v_fmac_f32 v37, s45, v49\n"
      "v_mfma_f32_16x16x32_bf16 v[68:71], a[140:143], a[204:207], v[68:71]\n"
      "s_waitcnt lgkmcnt(4)\n"
      "v_fmac_f32 v38, s45, v50\n"
      "v_fmac_f32 v39, s45, v51\n"
      "v_mfma_f32_16x16x32_bf16 v[72:75], a[132:135], a[204:207], v[72:75]\n"
      "buffer_store_dwordx4 a[228:231], v18, s[8:11], s58 offen offset:256 sc1\n"
      "v_mfma_f32_16x16x32_bf16 v[52:55], a[128:131], a[208:211], v[52:55]\n"
      "ds_read_b128 a[200:203], v3 offset:33920\n"
      "v_fma_f32 v48, s47, v48, -v96\n"
      "v_mfma_f32_16x16x32_bf16 v[56:59], a[120:123], a[208:211], v[56:59]\n"
      "ds_read2st64_b32 a[236:237], v13 offset0:0 offset1:2\n"
      "ds_read2st64_b32 a[238:239], v16 offset0:0 offset1:2\n"
      "v_fma_f32 v49, s47, v49, -v97\n"
      "v_mfma_f32_16x16x32_bf16 v[60:63], a[112:115], a[208:211], v[60:63]\n"
      "v_fma_f32 v50, s47, v50, -v98\n"
      "v_fma_f32 v51, s47, v51, -v99\n"
      "v_mfma_f32_16x16x32_bf16 v[64:67], a[124:127], a[212:215], v[64:67]\n"
      "ds_read_b128 a[204:207], v4 offset:33792\n"
      "v_cvt_pk_bf16_f32 v36, v36, v37\n"
      "v_mfma_f32_16x16x32_bf16 v[68:71], a[116:119], a[212:215], v[68:71]\n"
      "ds_write_addtid_b32 v36 offset:37888\n"
      "v_cvt_pk_bf16_f32 v38, v38, v39\n"
      "v_mfma_f32_16x16x32_bf16 v[72:75], a[108:111], a[212:215], v[72:75]\n"
      "s_waitcnt lgkmcnt(4)\n"
      "ds_write_addtid_b32 v38 offset:38400\n"
      "v_add_f32 v52, v52, v64\n"
      "v_add_f32 v53, v53, v65\n"
      "v_mfma_f32_16x16x32_bf16 v[100:103], a[0:3], a[192:195], 0\n"
      "ds_read_b128 a[208:211], v4 offset:33856\n"
      "ds_write_addtid_b32 v52 offset:45312\n"
      "v_add_f32 v54, v54, v66\n"
      "v_mfma_f32_16x16x32_bf16 v[104:107], a[4:7], a[192:195], 0\n"
      "buffer_store_dwordx4 a[232:235], v18, s[8:11], s59 offen offset:0 sc1\n"
      "v_mfma_f32_16x16x32_bf16 v[108:111], a[8:11], a[192:195], 0\n"
      "ds_write_addtid_b32 v53 offset:45824\n"
      "ds_write_addtid_b32 v54 offset:46336\n"
      "v_mfma_f32_16x16x32_bf16 v[100:103], a[12:15], a[196:199], v[100:103]\n"
      "ds_read_b128 a[212:215], v4 offset:33920\n"
      "v_add_f32 v55, v55, v67\n"
      "v_mfma_f32_16x16x32_bf16 v[104:107], a[16:19], a[196:199], v[104:107]\n"
      "ds_write_addtid_b32 v55 offset:46848\n"
      "v_add_f32 v56, v56, v68\n"
      "v_cvt_pk_bf16_f32 v48, v48, v49\n"
      "v_mfma_f32_16x16x32_bf16 v[108:111], a[20:23], a[196:199], v[108:111]\n"
      "ds_write_addtid_b32 v48 offset:39936\n"
      "ds_write_addtid_b32 v56 offset:47360\n"
      "v_mfma_f32_16x16x32_bf16 v[100:103], a[24:27], a[200:203], v[100:103]\n"
      "ds_read_b128 a[192:195], v5 offset:33792\n"
      "v_add_f32 v57, v57, v69\n"
      "v_mfma_f32_16x16x32_bf16 v[104:107], a[28:31], a[200:203], v[104:107]\n"
      "s_nop 0\n"
      "ds_write_addtid_b32 v57 offset:47872\n"
      "v_add_f32 v58, v58, v70\n"
      "v_add_f32 v59, v59, v71\n"
      "v_mfma_f32_16x16x32_bf16 v[108:111], a[32:35], a[200:203], v[108:111]\n"
      "s_waitcnt lgkmcnt(4)\n"
      "ds_write_addtid_b32 v58 offset:48384\n"
      "ds_write_addtid_b32 v59 offset:48896\n"
      "v_mfma_f32_16x16x32_bf16 v[100:103], a[36:39], a[204:207], v[100:103]\n"
      "s_nop 0\n"
      "buffer_store_dwordx4 a[236:239], v18, s[8:11], s59 offen offset:128 sc1\n"
      "ds_read_b128 a[196:199], v5 offset:33856\n"
      "v_cvt_pk_bf16_f32 v50, v50, v51\n"
      "v_mfma_f32_16x16x32_bf16 v[104:107], a[40:43], a[204:207], v[104:107]\n"
      "ds_write_addtid_b32 v50 offset:40448\n"
      "v_add_f32 v60, v60, v72\n"
      "v_mfma_f32_16x16x32_bf16 v[108:111], a[44:47], a[204:207], v[108:111]\n"
      "ds_write_addtid_b32 v60 offset:49408\n"
      "v_add_f32 v61, v61, v73\n"
      "v_add_f32 v62, v62, v74\n"
      "v_add_f32 v63, v63, v75\n"
      "v_mfma_f32_16x16x32_bf16 v[100:103], a[48:51], a[208:211], v[100:103]\n"
      "ds_read_b128 a[200:203], v5 offset:33920\n"
      "ds_write_addtid_b32 v61 offset:49920\n"
      "v_mfma_f32_16x16x32_bf16 v[104:107], a[52:55], a[208:211], v[104:107]\n"
      "ds_write_addtid_b32 v62 offset:50432\n"
      "ds_write_addtid_b32 v63 offset:50944\n"
      "v_mfma_f32_16x16x32_bf16 v[108:111], a[56:59], a[208:211], v[108:111]\n"
      "v_fma_f32 v40, -2.0, v64, v52\n"
      "v_fma_f32 v41, -2.0, v65, v53\n"
      "v_mfma_f32_16x16x32_bf16 v[100:103], a[60:63], a[212:215], v[100:103]\n"
      "v_fma_f32 v42, -2.0, v66, v54\n"
      "v_fma_f32 v43, -2.0, v67, v55\n"
      "v_mfma_f32_16x16x32_bf16 v[104:107], a[64:67], a[212:215], v[104:107]\n"
      "v_fma_f32 v44, -2.0, v68, v56\n"
      "v_fma_f32 v45, -2.0, v69, v57\n"
      "v_mfma_f32_16x16x32_bf16 v[108:111], a[68:71], a[212:215], v[108:111]\n"
      "s_waitcnt lgkmcnt(0)\n"
      "s_barrier\n"
      "v_mfma_f32_16x16x32_bf16 v[100:103], a[72:75], a[192:195], v[100:103]\n"
      "s_add_u32 s58, s58, 3072\n"
      "ds_read_b128 a[204:207], v0 offset:16384\n"
      "ds_read2st64_b32 v[28:29], v6 offset0:0 offset1:2\n"
      "v_fma_f32 v46, -2.0, v70, v58\n"
      "v_mfma_f32_16x16x32_bf16 v[104:107], a[76:79], a[192:195], v[104:107]\n"
      "ds_read_b128 a[208:211], v0 offset:20480\n"
      "ds_read2st64_b32 v[30:31], v6 offset0:4 offset1:6\n"
      "v_mfma_f32_16x16x32_bf16 v[108:111], a[80:83], a[192:195], v[108:111]\n"
      "ds_read2st64_b32 v[32:33], v6 offset0:8 offset1:10\n"
      "ds_read2st64_b32 v[34:35], v6 offset0:12 offset1:14\n"
      "v_mfma_f32_16x16x32_bf16 v[100:103], a[84:87], a[196:199], v[100:103]\n"
      "ds_read_b128 a[212:215], v0 offset:16448\n"
      "v_fma_f32 v47, -2.0, v71, v59\n"
      "v_mfma_f32_16x16x32_bf16 v[104:107], a[88:91], a[196:199], v[104:107]\n"
      "v_fma_f32 v48, -2.0, v72, v60\n"
      "v_fma_f32 v49, -2.0, v73, v61\n"
      "v_mfma_f32_16x16x32_bf16 v[108:111], a[92:95], a[196:199], v[108:111]\n"
      "ds_read2st64_b32 v[36:37], v6 offset0:16 offset1:18\n"
      "ds_read2st64_b32 v[38:39], v6 offset0:20 offset1:22\n"
      "v_mfma_f32_16x16x32_bf16 v[100:103], a[96:99], a[200:203], v[100:103]\n"
      "ds_read_b128 a[192:195], v0 offset:20544\n"
      "ds_read2st64_b32 a[220:221], v12 offset0:181 offset1:183\n"
      "v_mfma_f32_16x16x32_bf16 v[104:107], a[100:103], a[200:203], v[104:107]\n"
      "ds_read2st64_b32 a[222:223], v15 offset0:181 offset1:183\n"
      "v_fma_f32 v50, -2.0, v74, v62\n"
      "v_fma_f32 v51, -2.0, v75, v63\n"
      "v_mfma_f32_16x16x32_bf16 v[108:111], a[104:107], a[200:203], v[108:111]\n"
      "s_waitcnt lgkmcnt(8)\n"
      "v_fmac_f32 v40, s46, v28\n"
      "v_fmac_f32 v41, s46, v29\n"
      "v_mfma_f32_16x16x32_bf16 v[52:55], v[252:255], a[204:207], 0\n"
      "s_nop 0\n"
      "ds_read_b128 a[196:199], v0 offset:16512\n"
      "v_fmac_f32 v42, s46, v30\n"
      "v_mfma_f32_16x16x32_bf16 v[56:59], v[244:247], a[204:207], 0\n"
      "v_fmac_f32 v43, s46, v31\n"
      "v_fma_f32 v28, 0.5, v28, v76\n"
      "v_mfma_f32_16x16x32_bf16 v[60:63], v[236:239], a[204:207], 0\n"
      "v_fma_f32 v29, 0.5, v29, v77\n"
      "v_fma_f32 v30, 0.5, v30, v78\n"
      "v_mfma_f32_16x16x32_bf16 v[64:67], v[248:251], a[208:211], 0\n"
      "ds_read_b128 a[200:203], v0 offset:20608\n"
      "v_fma_f32 v31, 0.5, v31, v79\n"
      "v_mfma_f32_16x16x32_bf16 v[68:71], v[240:243], a[208:211], 0\n"
      "v_fmac_f32 v40, s48, v100\n"
      "v_fmac_f32 v41, s48, v101\n"
      "v_mfma_f32_16x16x32_bf16 v[72:75], v[232:235], a[208:211], 0\n"
      "s_waitcnt lgkmcnt(2)\n"
      "v_fmac_f32 v42, s48, v102\n"
      "v_fmac_f32 v43, s48, v103\n"
      "v_mfma_f32_16x16x32_bf16 v[52:55], v[228:231], a[212:215], v[52:55]\n"
      "s_nop 0\n"
      "ds_read_b128 a[204:207], v1 offset:16384\n"
      "v_fmac_f32 v28, s45, v40\n"
      "v_mfma_f32_16x16x32_bf16 v[56:59], v[220:223], a[212:215], v[56:59]\n"
      "v_fmac_f32 v29, s45, v41\n"
      "v_fmac_f32 v30, s45, v42\n"
      "v_fmac_f32 v31, s45, v43\n"
      "v_mfma_f32_16x16x32_bf16 v[60:63], v[212:215], a[212:215], v[60:63]\n"
      "v_fma_f32 v40, s47, v40, -v100\n"
      "v_fma_f32 v41, s47, v41, -v101\n"
      "v_mfma_f32_16x16x32_bf16 v[64:67], v[224:227], a[192:195], v[64:67]\n"
      "ds_read_b128 a[208:211], v1 offset:20480\n"
      "v_fma_f32 v42, s47, v42, -v102\n"
      "v_mfma_f32_16x16x32_bf16 v[68:71], v[216:219], a[192:195], v[68:71]\n"
      "s_nop 0\n"
      "v_fma_f32 v43, s47, v43, -v103\n"
      "v_cvt_pk_bf16_f32 v28, v28, v29\n"
      "v_mfma_f32_16x16x32_bf16 v[72:75], v[208:211], a[192:195], v[72:75]\n"
      "s_waitcnt lgkmcnt(1)\n"
      "buffer_store_dwordx4 a[220:223], v18, s[8:11], s58 offen offset:0 sc1\n"
      "v_mfma_f32_16x16x32_bf16 v[52:55], v[204:207], a[196:199], v[52:55]\n"
      "ds_read_b128 a[212:215], v1 offset:16448\n"
      "ds_write_addtid_b32 v28 offset:32768\n"
      "v_mfma_f32_16x16x32_bf16 v[56:59], v[196:199], a[196:199], v[56:59]\n"
      "ds_read2st64_b32 a[224:225], v13 offset0:181 offset1:183\n"
      "ds_read2st64_b32 a[226:227], v16 offset0:181 offset1:183\n"
      "v_cvt_pk_bf16_f32 v30, v30, v31\n"
      "v_mfma_f32_16x16x32_bf16 v[60:63], v[188:191], a[196:199], v[60:63]\n"
      "ds_write_addtid_b32 v30 offset:33280\n"
      "v_cvt_pk_bf16_f32 v40, v40, v41\n"
      "v_mfma_f32_16x16x32_bf16 v[64:67], v[200:203], a[200:203], v[64:67]\n"
      "ds_read_b128 a[192:195], v1 offset:20544\n"
      "ds_write_addtid_b32 v40 offset:34816\n"
      "v_mfma_f32_16x16x32_bf16 v[68:71], v[192:195], a[200:203], v[68:71]\n"
      "v_cvt_pk_bf16_f32 v42, v42, v43\n"
      "v_fmac_f32_e64 v44, s46, v32\n"
      "v_mfma_f32_16x16x32_bf16 v[72:75], v[184:187], a[200:203], v[72:75]\n"
      "ds_write_addtid_b32 v42 offset:35328\n"
      "v_fmac_f32 v45, s46, v33\n"
      "v_mfma_f32_16x16x32_bf16 v[52:55], v[180:183], a[204:207], v[52:55]\n"
      "ds_read_b128 a[196:199], v1 offset:16512\n"
      "v_fmac_f32 v46, s46, v34\n"
      "v_mfma_f32_16x16x32_bf16 v[56:59], v[172:175], a[204:207], v[56:59]\n"
      "v_fmac_f32_e64 v47, s46, v35\n"
      "v_fma_f32 v32, 0.5, v32, v80\n"
      "v_mfma_f32_16x16x32_bf16 v[60:63], v[164:167], a[204:207], v[60:63]\n"
      "s_waitcnt lgkmcnt(4)\n"
      "v_fma_f32 v33, 0.5, v33, v81\n"
      "v_fma_f32 v34, 0.5, v34, v82\n"
      "v_mfma_f32_16x16x32_bf16 v[64:67], v[176:179], a[208:211], v[64:67]\n"
      "ds_read_b128 a[200:203], v1 offset:20608\n"
      "v_fma_f32 v35, 0.5, v35, v83\n"
      "v_fmac_f32 v44, s48, v104\n"
      "v_mfma_f32_16x16x32_bf16 v[68:71], v[168:171], a[208:211], v[68:71]\n"
      "v_fmac_f32 v45, s48, v105\n"
      "v_fmac_f32 v46, s48, v106\n"
      "v_mfma_f32_16x16x32_bf16 v[72:75], v[160:163], a[208:211], v[72:75]\n"
      "v_fmac_f32 v47, s48, v107\n"
      "v_fmac_f32 v32, s45, v44\n"
      "v_mfma_f32_16x16x32_bf16 v[52:55], v[156:159], a[212:215], v[52:55]\n"
      "ds_read_b128 a[204:207], v2 offset:16384\n"
      "v_fmac_f32 v33, s45, v45\n"
      "v_mfma_f32_16x16x32_bf16 v[56:59], v[148:151], a[212:215], v[56:59]\n"
      "v_fmac_f32 v34, s45, v46\n"
      "v_fmac_f32_e64 v35, s45, v47\n"
      "v_mfma_f32_16x16x32_bf16 v[60:63], v[140:143], a[212:215], v[60:63]\n"
      "s_waitcnt lgkmcnt(1)\n"
      "v_fma_f32 v44, s47, v44, -v104\n"
      "v_fma_f32 v45, s47, v45, -v105\n"
      "v_mfma_f32_16x16x32_bf16 v[64:67], v[152:155], a[192:195], v[64:67]\n"
      "buffer_store_dwordx4 a[224:227], v18, s[8:11], s58 offen offset:128 sc1\n"
      "ds_read_b128 a[208:211], v2 offset:20480\n"
      "v_mfma_f32_16x16x32_bf16 v[68:71], v[144:147], a[192:195], v[68:71]\n"
      "s_nop 0\n"
      "v_fma_f32 v46, s47, v46, -v106\n"
      "v_fma_f32 v47, s47, v47, -v107\n"
      "v_cvt_pk_bf16_f32 v32, v32, v33\n"
      "v_mfma_f32_16x16x32_bf16 v[72:75], v[136:139], a[192:195], v[72:75]\n"
      "ds_write_addtid_b32 v32 offset:40964\n"
      "ds_read2st64_b32 a[228:229], v14 offset0:181 offset1:183\n"
      "v_mfma_f32_16x16x32_bf16 v[52:55], v[132:135], a[196:199], v[52:55]\n"
      "ds_read_b128 a[212:215], v2 offset:16448\n"
      "ds_read2st64_b32 a[230:231], v17 offset0:181 offset1:183\n"
      "v_mfma_f32_16x16x32_bf16 v[56:59], v[124:127], a[196:199], v[56:59]\n"
      "v_cvt_pk_bf16_f32 v34, v34, v35\n"
      "v_cvt_pk_bf16_f32 v44, v44, v45\n"
      "v_mfma_f32_16x16x32_bf16 v[60:63], a[184:187], a[196:199], v[60:63]\n"
      "ds_write_addtid_b32 v34 offset:41476\n"
      "ds_write_addtid_b32 v44 offset:43012\n"
      "v_mfma_f32_16x16x32_bf16 v[64:67], v[128:131], a[200:203], v[64:67]\n"
      "ds_read_b128 a[192:195], v2 offset:20544\n"
      "v_cvt_pk_bf16_f32 v46, v46, v47\n"
      "v_mfma_f32_16x16x32_bf16 v[68:71], a[188:191], a[200:203], v[68:71]\n"
      "ds_write_addtid_b32 v46 offset:43524\n"
      "v_fmac_f32 v48, s46, v36\n"
      "v_fmac_f32 v49, s46, v37\n"
      "v_mfma_f32_16x16x32_bf16 v[72:75], a[180:183], a[200:203], v[72:75]\n"
      "s_waitcnt lgkmcnt(4)\n"
      "v_fmac_f32 v50, s46, v38\n"
      "v_fmac_f32_e64 v51, s46, v39\n"
      "v_mfma_f32_16x16x32_bf16 v[52:55], a[176:179], a[204:207], v[52:55]\n"
      "ds_read_b128 a[196:199], v2 offset:16512\n"
      "v_fma_f32 v36, 0.5, v36, v84\n"
      "v_mfma_f32_16x16x32_bf16 v[56:59], a[168:171], a[204:207], v[56:59]\n"
      "v_fma_f32 v37, 0.5, v37, v85\n"
      "v_fma_f32 v38, 0.5, v38, v86\n"
      "v_mfma_f32_16x16x32_bf16 v[60:63], a[160:163], a[204:207], v[60:63]\n"
      "v_fma_f32 v39, 0.5, v39, v87\n"
      "v_fmac_f32 v48, s48, v108\n"
      "v_mfma_f32_16x16x32_bf16 v[64:67], a[172:175], a[208:211], v[64:67]\n"
      "ds_read_b128 a[200:203], v2 offset:20608\n"
      "v_fmac_f32 v49, s48, v109\n"
      "v_mfma_f32_16x16x32_bf16 v[68:71], a[164:167], a[208:211], v[68:71]\n"
      "v_fmac_f32 v50, s48, v110\n"
      "v_fmac_f32 v51, s48, v111\n"
      "v_mfma_f32_16x16x32_bf16 v[72:75], a[156:159], a[208:211], v[72:75]\n"
      "v_fmac_f32 v36, s45, v48\n"
      "v_fmac_f32 v37, s45, v49\n"
      "v_mfma_f32_16x16x32_bf16 v[52:55], a[152:155], a[212:215], v[52:55]\n"
      "ds_read_b128 a[204:207], v3 offset:50688\n"
      "v_fmac_f32 v38, s45, v50\n"
      "v_fmac_f32 v39, s45, v51\n"
      "v_mfma_f32_16x16x32_bf16 v[56:59], a[144:147], a[212:215], v[56:59]\n"
      "s_nop 0\n"
      "buffer_store_dwordx4 a[228:231], v18, s[8:11], s58 offen offset:256 sc1\n"
      "v_mfma_f32_16x16x32_bf16 v[60:63], a[136:139], a[212:215], v[60:63]\n"
      "s_waitcnt lgkmcnt(1)\n"
      "v_fma_f32 v48, s47, v48, -v108\n"
      "v_fma_f32 v49, s47, v49, -v109\n"
      "v_mfma_f32_16x16x32_bf16 v[64:67], a[148:151], a[192:195], v[64:67]\n"
      "ds_read_b128 a[208:211], v3 offset:50752\n"
      "v_fma_f32 v50, s47, v50, -v110\n"
      "v_mfma_f32_16x16x32_bf16 v[68:71], a[140:143], a[192:195], v[68:71]\n"
      "v_fma_f32 v51, s47, v51, -v111\n"
      "v_cvt_pk_bf16_f32 v36, v36, v37\n"
      "v_mfma_f32_16x16x32_bf16 v[72:75], a[132:135], a[192:195], v[72:75]\n"
      "ds_write_addtid_b32 v36 offset:36864\n"
      "v_cvt_pk_bf16_f32 v38, v38, v39\n"
      "v_mfma_f32_16x16x32_bf16 v[52:55], a[128:131], a[196:199], v[52:55]\n"
      "ds_read_b128 a[212:215], v3 offset:50816\n"
      "ds_write_addtid_b32 v38 offset:37376\n"
      "v_cvt_pk_bf16_f32 v48, v48, v49\n"
      "v_cvt_pk_bf16_f32 v50, v50, v51\n"
      "v_mfma_f32_16x16x32_bf16 v[56:59], a[120:123], a[196:199], v[56:59]\n"
      "ds_write_addtid_b32 v48 offset:38912\n"
      "ds_write_addtid_b32 v50 offset:39424\n"
      "v_mfma_f32_16x16x32_bf16 v[60:63], a[112:115], a[196:199], v[60:63]\n"
      "v_mfma_f32_16x16x32_bf16 v[64:67], a[124:127], a[200:203], v[64:67]\n"
      "ds_read_b128 a[192:195], v4 offset:50688\n"
      "v_mfma_f32_16x16x32_bf16 v[68:71], a[116:119], a[200:203], v[68:71]\n"
      "v_mfma_f32_16x16x32_bf16 v[72:75], a[108:111], a[200:203], v[72:75]\n"
      "s_waitcnt lgkmcnt(3)\n"
      "v_mfma_f32_16x16x32_bf16 v[112:115], a[0:3], a[204:207], 0\n"
      "s_nop 0\n"
      "ds_read_b128 a[196:199], v4 offset:50752\n"
      "v_mfma_f32_16x16x32_bf16 v[116:119], a[4:7], a[204:207], 0\n"
      "v_mfma_f32_16x16x32_bf16 v[120:123], a[8:11], a[204:207], 0\n"
      "v_add_f32 v52, v52, v64\n"
      "v_add_f32 v53, v53, v65\n"
      "v_add_f32 v54, v54, v66\n"
      "v_add_f32 v55, v55, v67\n"
      "v_add_f32 v56, v56, v68\n"
      "v_add_f32 v57, v57, v69\n"
      "v_add_f32 v58, v58, v70\n"
      "v_mfma_f32_16x16x32_bf16 v[112:115], a[12:15], a[208:211], v[112:115]\n"
      "s_nop 0\n"
      "ds_read_b128 a[200:203], v4 offset:50816\n"
      "ds_write_addtid_b32 v52 offset:51456\n"
      "ds_write_addtid_b32 v53 offset:51968\n"
      "v_add_f32 v59, v59, v71\n"
      "v_add_f32 v60, v60, v72\n"
      "v_add_f32 v61, v61, v73\n"
      "v_add_f32_e64 v62, v62, v74\n"
      "v_mfma_f32_16x16x32_bf16 v[116:119], a[16:19], a[208:211], v[116:119]\n"
      "s_nop 0\n"
      "ds_write_addtid_b32 v54 offset:52480\n"
      "ds_write_addtid_b32 v55 offset:52992\n"
      "ds_write_addtid_b32 v56 offset:53504\n"
      "v_mfma_f32_16x16x32_bf16 v[120:123], a[20:23], a[208:211], v[120:123]\n"
      "ds_write_addtid_b32 v57 offset:54016\n"
      "ds_write_addtid_b32 v58 offset:54528\n"
      "ds_write_addtid_b32 v59 offset:55040\n"
      "v_add_f32_e64 v63, v63, v75\n"
      "v_mfma_f32_16x16x32_bf16 v[112:115], a[24:27], a[212:215], v[112:115]\n"
      "s_nop 0\n"
      "ds_write_addtid_b32 v60 offset:55552\n"
      "ds_write_addtid_b32 v61 offset:56064\n"
      "v_mfma_f32_16x16x32_bf16 v[116:119], a[28:31], a[212:215], v[116:119]\n"
      "s_waitcnt vmcnt(16)\n"
      "ds_write_addtid_b32 v62 offset:56576\n"
      "ds_write_addtid_b32 v63 offset:57088\n"
      "v_mfma_f32_16x16x32_bf16 v[120:123], a[32:35], a[212:215], v[120:123]\n"
      "s_waitcnt lgkmcnt(0)\n"
      "s_barrier\n"
      "v_mfma_f32_16x16x32_bf16 v[112:115], a[36:39], a[192:195], v[112:115]\n"
      "s_add_u32 s58, s58, 783360\n"
      "ds_read_b128 a[204:207], v5 offset:50688\n"
      "ds_read2st64_b32 v[28:29], v6 offset0:24 offset1:26\n"
      "v_mfma_f32_16x16x32_bf16 v[116:119], a[40:43], a[192:195], v[116:119]\n"
      "ds_read2st64_b32 v[30:31], v6 offset0:28 offset1:30\n"
      "ds_read2st64_b32 v[32:33], v6 offset0:32 offset1:34\n"
      "v_mfma_f32_16x16x32_bf16 v[120:123], a[44:47], a[192:195], v[120:123]\n"
      "ds_read2st64_b32 v[34:35], v6 offset0:36 offset1:38\n"
      "ds_read2st64_b32 v[36:37], v6 offset0:40 offset1:42\n"
      "v_mfma_f32_16x16x32_bf16 v[112:115], a[48:51], a[196:199], v[112:115]\n"
      "ds_read_b128 a[208:211], v5 offset:50752\n"
      "ds_read2st64_b32 v[38:39], v6 offset0:44 offset1:46\n"
      "v_mfma_f32_16x16x32_bf16 v[116:119], a[52:55], a[196:199], v[116:119]\n"
      "v_fma_f32 v40, -2.0, v64, v52\n"
      "v_fma_f32 v41, -2.0, v65, v53\n"
      "v_mfma_f32_16x16x32_bf16 v[120:123], a[56:59], a[196:199], v[120:123]\n"
      "ds_read2st64_b32 a[220:221], v12 offset0:177 offset1:179\n"
      "ds_read2st64_b32 a[222:223], v15 offset0:177 offset1:179\n"
      "v_mfma_f32_16x16x32_bf16 v[112:115], a[60:63], a[200:203], v[112:115]\n"
      "ds_read_b128 a[212:215], v5 offset:50816\n"
      "v_fma_f32 v42, -2.0, v66, v54\n"
      "v_mfma_f32_16x16x32_bf16 v[116:119], a[64:67], a[200:203], v[116:119]\n"
      "ds_read2st64_b32 a[224:225], v13 offset0:177 offset1:179\n"
      "ds_read2st64_b32 a[226:227], v16 offset0:177 offset1:179\n"
      "v_mfma_f32_16x16x32_bf16 v[120:123], a[68:71], a[200:203], v[120:123]\n"
      "s_waitcnt lgkmcnt(9)\n"
      "v_fma_f32 v43, -2.0, v67, v55\n"
      "v_fma_f32 v44, -2.0, v68, v56\n"
      "v_mfma_f32_16x16x32_bf16 v[112:115], a[72:75], a[204:207], v[112:115]\n"
      "s_waitcnt lgkmcnt(3)\n"
      "buffer_store_dwordx4 a[220:223], v18, s[8:11], s58 offen offset:0 sc1\n"
      "v_mfma_f32_16x16x32_bf16 v[116:119], a[76:79], a[204:207], v[116:119]\n"
      "v_fma_f32 v45, -2.0, v69, v57\n"
      "v_fma_f32 v46, -2.0, v70, v58\n"
      "v_mfma_f32_16x16x32_bf16 v[120:123], a[80:83], a[204:207], v[120:123]\n"
      "s_nop 0\n"
      "ds_read2st64_b32 a[228:229], v14 offset0:177 offset1:179\n"
      "ds_read2st64_b32 a[230:231], v17 offset0:177 offset1:179\n"
      "v_mfma_f32_16x16x32_bf16 v[112:115], a[84:87], a[208:211], v[112:115]\n"
      "s_waitcnt lgkmcnt(2)\n"
      "buffer_store_dwordx4 a[224:227], v18, s[8:11], s58 offen offset:128 sc1\n"
      "v_mfma_f32_16x16x32_bf16 v[116:119], a[88:91], a[208:211], v[116:119]\n"
      "v_fma_f32 v47, -2.0, v71, v59\n"
      "v_fma_f32 v48, -2.0, v72, v60\n"
      "v_mfma_f32_16x16x32_bf16 v[120:123], a[92:95], a[208:211], v[120:123]\n"
      "s_nop 0\n"
      "v_fma_f32 v49, -2.0, v73, v61\n"
      "v_fma_f32 v50, -2.0, v74, v62\n"
      "v_mfma_f32_16x16x32_bf16 v[112:115], a[96:99], a[212:215], v[112:115]\n"
      "s_waitcnt lgkmcnt(0)\n"
      "buffer_store_dwordx4 a[228:231], v18, s[8:11], s58 offen offset:256 sc1\n"
      "v_mfma_f32_16x16x32_bf16 v[116:119], a[100:103], a[212:215], v[116:119]\n"
      "v_fma_f32 v51, -2.0, v75, v63\n"
      "v_fmac_f32 v40, s46, v28\n"
      "v_mfma_f32_16x16x32_bf16 v[120:123], a[104:107], a[212:215], v[120:123]\n"
      "v_fmac_f32 v41, s46, v29\n"
      "v_fmac_f32 v42, s46, v30\n"
      "v_fmac_f32 v43, s46, v31\n"
      "v_fma_f32 v28, 0.5, v28, v88\n"
      "v_fma_f32 v29, 0.5, v29, v89\n"
      "v_fma_f32 v30, 0.5, v30, v90\n"
      "v_fma_f32 v31, 0.5, v31, v91\n"
      "v_fmac_f32_e64 v44, s46, v32\n"
      "v_fmac_f32_e64 v45, s46, v33\n"
      "v_pk_fma_f32 v[46:47], v[34:35], s[46:47], v[46:47] op_sel:[0,0,0] op_sel_hi:[1,0,1]\n"
      "v_pk_fma_f32 v[32:33], v[32:33], s[48:49], v[92:93] op_sel:[0,1,0] op_sel_hi:[1,1,1]\n"
      "v_pk_fma_f32 v[34:35], v[34:35], s[48:49], v[94:95] op_sel:[0,1,0] op_sel_hi:[1,1,1]\n"
      "v_pk_fma_f32 v[48:49], v[36:37], s[46:47], v[48:49] op_sel:[0,0,0] op_sel_hi:[1,0,1]\n"
      "v_pk_fma_f32 v[50:51], v[38:39], s[46:47], v[50:51] op_sel:[0,0,0] op_sel_hi:[1,0,1]\n"
      "v_pk_fma_f32 v[36:37], v[36:37], s[48:49], v[96:97] op_sel:[0,1,0] op_sel_hi:[1,1,1]\n"
      "v_pk_fma_f32 v[38:39], v[38:39], s[48:49], v[98:99] op_sel:[0,1,0] op_sel_hi:[1,1,1]\n"
      "v_pk_fma_f32 v[40:41], v[112:113], s[48:49], v[40:41] op_sel:[0,0,0] op_sel_hi:[1,0,1]\n"
      "v_pk_fma_f32 v[42:43], v[114:115], s[48:49], v[42:43] op_sel:[0,0,0] op_sel_hi:[1,0,1]\n"
      "v_pk_fma_f32 v[28:29], v[40:41], s[44:45], v[28:29] op_sel:[0,1,0] op_sel_hi:[1,1,1]\n"
      "v_pk_fma_f32 v[30:31], v[42:43], s[44:45], v[30:31] op_sel:[0,1,0] op_sel_hi:[1,1,1]\n"
      "v_pk_fma_f32 v[40:41], v[40:41], s[46:47], v[112:113] op_sel:[0,1,0] op_sel_hi:[1,1,1] neg_lo:[0,0,1] neg_hi:[0,0,1]\n"
      "v_pk_fma_f32 v[42:43], v[42:43], s[46:47], v[114:115] op_sel:[0,1,0] op_sel_hi:[1,1,1] neg_lo:[0,0,1] neg_hi:[0,0,1]\n"
      "v_cvt_pk_bf16_f32 v28, v28, v29\n"
      "v_cvt_pk_bf16_f32 v30, v30, v31\n"
      "v_cvt_pk_bf16_f32 v40, v40, v41\n"
      "v_cvt_pk_bf16_f32 v42, v42, v43\n"
      "v_pk_fma_f32 v[44:45], v[116:117], s[48:49], v[44:45] op_sel:[0,0,0] op_sel_hi:[1,0,1]\n"
      "v_pk_fma_f32 v[46:47], v[118:119], s[48:49], v[46:47] op_sel:[0,0,0] op_sel_hi:[1,0,1]\n"
      "v_pk_fma_f32 v[32:33], v[44:45], s[44:45], v[32:33] op_sel:[0,1,0] op_sel_hi:[1,1,1]\n"
      "v_pk_fma_f32 v[34:35], v[46:47], s[44:45], v[34:35] op_sel:[0,1,0] op_sel_hi:[1,1,1]\n"
      "v_pk_fma_f32 v[44:45], v[44:45], s[46:47], v[116:117] op_sel:[0,1,0] op_sel_hi:[1,1,1] neg_lo:[0,0,1] neg_hi:[0,0,1]\n"
      "v_pk_fma_f32 v[46:47], v[46:47], s[46:47], v[118:119] op_sel:[0,1,0] op_sel_hi:[1,1,1] neg_lo:[0,0,1] neg_hi:[0,0,1]\n"
      "v_cvt_pk_bf16_f32 v32, v32, v33\n"
      "v_cvt_pk_bf16_f32 v34, v34, v35\n"
      "ds_write_addtid_b32 v28 offset:33792\n"
      "ds_write_addtid_b32 v30 offset:34304\n"
      "ds_write_addtid_b32 v40 offset:35840\n"
      "ds_write_addtid_b32 v42 offset:36352\n"
      "ds_write_addtid_b32 v32 offset:41988\n"
      "ds_write_addtid_b32 v34 offset:42500\n"
      "v_cvt_pk_bf16_f32 v44, v44, v45\n"
      "v_cvt_pk_bf16_f32 v46, v46, v47\n"
      "v_pk_fma_f32 v[48:49], v[120:121], s[48:49], v[48:49] op_sel:[0,0,0] op_sel_hi:[1,0,1]\n"
      "v_pk_fma_f32 v[50:51], v[122:123], s[48:49], v[50:51] op_sel:[0,0,0] op_sel_hi:[1,0,1]\n"
      "v_pk_fma_f32 v[36:37], v[48:49], s[44:45], v[36:37] op_sel:[0,1,0] op_sel_hi:[1,1,1]\n"
      "v_pk_fma_f32 v[38:39], v[50:51], s[44:45], v[38:39] op_sel:[0,1,0] op_sel_hi:[1,1,1]\n"
      "v_pk_fma_f32 v[48:49], v[48:49], s[46:47], v[120:121] op_sel:[0,1,0] op_sel_hi:[1,1,1] neg_lo:[0,0,1] neg_hi:[0,0,1]\n"
      "v_pk_fma_f32 v[50:51], v[50:51], s[46:47], v[122:123] op_sel:[0,1,0] op_sel_hi:[1,1,1] neg_lo:[0,0,1] neg_hi:[0,0,1]\n"
      "v_cvt_pk_bf16_f32 v36, v36, v37\n"
      "v_cvt_pk_bf16_f32 v38, v38, v39\n"
      "v_cvt_pk_bf16_f32 v48, v48, v49\n"
      "v_cvt_pk_bf16_f32 v50, v50, v51\n"
      "ds_write_addtid_b32 v44 offset:44036\n"
      "ds_write_addtid_b32 v46 offset:44548\n"
      "ds_write_addtid_b32 v36 offset:37888\n"
      "ds_write_addtid_b32 v38 offset:38400\n"
      "ds_write_addtid_b32 v48 offset:39936\n"
      "ds_write_addtid_b32 v50 offset:40448\n"
      "s_waitcnt lgkmcnt(0)\n"
      "s_barrier\n"
      "ds_read2st64_b32 a[220:221], v12 offset0:181 offset1:183\n"
      "ds_read2st64_b32 a[222:223], v15 offset0:181 offset1:183\n"
      "ds_read2st64_b32 a[224:225], v13 offset0:181 offset1:183\n"
      "ds_read2st64_b32 a[226:227], v16 offset0:181 offset1:183\n"
      "ds_read2st64_b32 a[228:229], v14 offset0:181 offset1:183\n"
      "ds_read2st64_b32 a[230:231], v17 offset0:181 offset1:183\n"
      "s_add_u32 s58, s58, 3072\n"
      "s_waitcnt lgkmcnt(4)\n"
      "buffer_store_dwordx4 a[220:223], v18, s[8:11], s58 offen offset:0 sc1\n"
      "s_waitcnt lgkmcnt(2)\n"
      "buffer_store_dwordx4 a[224:227], v18, s[8:11], s58 offen offset:128 sc1\n"
      "s_waitcnt lgkmcnt(0)\n"
      "buffer_store_dwordx4 a[228:231], v18, s[8:11], s58 offen offset:256 sc1\n"
      "LDend_%=:\n"
      "s_branch Lfin_%=\n"
      ".p2align 6\n"
      "LS_%=:\n"
      "v_lshlrev_b32 v43, 4, v37\n"
      "v_lshlrev_b32 v44, 2, v37\n"
      "v_and_b32 v43, 16, v43\n"
      "v_and_or_b32 v43, v44, 8, v43\n"
      "v_mul_u32_u24 v44, 192, v38\n"
      "v_mul_u32_u24 v45, 1536, v39\n"
      "s_lshl_b32 s27, s18, 5\n"
      "v_lshlrev_b32 v46, 1, v36\n"
      "v_add3_u32 v43, v43, v44, v45\n"
      "v_bitop3_b32 v46, v46, s27, 32 bitop3:0x6c\n"
      "v_add_u32 v95, v43, v46\n"
      "v_add_u32 v96, 57344, v95\n"
      "v_add_u32 v97, 114688, v95\n"
      "s_lshl_b32 s26, s18, 3\n"
      "v_and_b32 v42, 15, v36\n"
      "v_xor_b32 v42, s26, v42\n"
      "s_lshl_b32 s26, s17, 13\n"
      "s_add_u32 s26, s26, 24832\n"
      "s_mul_i32 s27, s17, 12672\n"
      "v_mul_u32_u24 v44, 192, v42\n"
      "v_lshlrev_b32 v43, 3, v42\n"
      "v_and_b32 v45, 48, v36\n"
      "s_add_u32 s27, s27, 82432\n"
      "v_bitop3_b32 v46, v43, v45, 32 bitop3:0x6c\n"
      "v_add3_u32 v0, v46, v44, s26\n"
      "v_add3_u32 v3, v46, v44, s27\n"
      "v_add_u32 v43, 16, v43\n"
      "v_add_u32 v44, 384, v44\n"
      "v_bitop3_b32 v46, v43, v45, 32 bitop3:0x6c\n"
      "v_add3_u32 v1, v46, v44, s26\n"
      "v_add3_u32 v4, v46, v44, s27\n"
      "v_add_u32 v43, 16, v43\n"
      "v_add_u32 v44, 384, v44\n"
      "v_bitop3_b32 v46, v43, v45, 32 bitop3:0x6c\n"
      "v_add3_u32 v2, v46, v44, s26\n"
      "v_add3_u32 v5, v46, v44, s27\n"
      "v_lshlrev_b32 v43, 31, v36\n"
      "v_and_b32 v44, 2, v36\n"
      "v_or_b32 v43, 0x3f803f80, v43\n"
      "v_mul_u32_u24 v45, 0xc00080, v44\n"
      "v_lshl_add_u32 v44, v44, 22, v43\n"
      "v_add_u32 v45, v45, v43\n"
      "v_accvgpr_write_b32 a216, v44\n"
      "v_accvgpr_write_b32 a217, v45\n"
      "v_lshlrev_b32 v43, 3, v38\n"
      "s_mov_b32 s26, 806420736\n"
      "v_bfe_u32 v43, s26, v43, 8\n"
      "v_lshlrev_b32 v44, 12, v38\n"
      "s_lshl_b32 s28, s18, 8\n"
      "s_add_u32 s28, s28, 82432\n"
      "v_lshl_add_u32 v43, v43, 3, v44\n"
      "v_lshl_add_u32 v44, v39, 2, v37\n"
      "v_lshl_add_u32 v43, v44, 4, v43\n"
      "v_add_u32 v7, s28, v43\n"
      "s_mul_i32 s26, s17, 70144\n"
      "s_lshl_b32 s27, s18, 8\n"
      "s_sub_u32 s26, 70144, s26\n"
      "s_add_u32 s26, s26, s27\n"
      "v_lshl_add_u32 v6, v36, 2, s26\n"
      "v_lshrrev_b32 v43, 3, v36\n"
      "v_and_b32 v44, 7, v36\n"
      "v_mul_u32_u24 v45, 384, v43\n"
      "v_lshl_add_u32 v11, v44, 4, v45\n"
      "v_and_b32 v44, 2, v36\n"
      "v_lshlrev_b32 v43, 3, v43\n"
      "v_lshlrev_b32 v45, 4, v44\n"
      "v_xor_b32 v43, v43, v45\n"
      "v_lshl_add_u32 v43, v44, 7, v43\n"
      "v_and_b32 v44, 1, v36\n"
      "s_lshl_b32 s26, s18, 11\n"
      "v_lshl_add_u32 v43, v44, 6, v43\n"
      "s_add_u32 s26, s26, 12288\n"
      "v_bfe_u32 v44, v36, 2, 1\n"
      "v_add_u32 v43, s26, v43\n"
      "v_mul_i32_i24 v45, -4100, v44\n"
      "s_mov_b32 s27, 8200\n"
      "v_add3_u32 v8, v43, v45, s27\n"
      "v_add_u32 v9, 128, v8\n"
      "s_mul_i32 s62, s18, 393216\n"
      "s_add_u32 s62, s62, s19\n"
      "s_add_u32 s62, s62, s21\n"
      "s_sub_u32 s62, s62, 786432\n"
      "s_mov_b32 s58, 0x80000000\n"
      "s_mov_b32 s59, 0x80000000\n"
      "s_and_b32 s9, s9, 0xffff\n"
      "s_mov_b32 s45, 0xbeaaaaab\n"
      "s_mov_b32 s46, 0xbe800000\n"
      "s_mov_b32 s47, 0x3eaaaaab\n"
      "s_add_u32 m0, s63, 122368\n"
      "s_add_u32 s52, s63, 61440\n"
      "buffer_load_dwordx4 v92, s[12:15], s52 offen offset:0 lds\n"
      "buffer_load_dwordx4 v93, s[12:15], s52 offen offset:1024 lds\n"
      "buffer_load_dwordx4 v94, s[12:15], s52 offen offset:2048 lds\n"
      "s_add_u32 m0, s63, 128512\n"
      "s_add_u32 s52, s63, 116736\n"
      "buffer_load_dwordx4 v92, s[12:15], s52 offen offset:0 lds\n"
      "buffer_load_dwordx4 v93, s[12:15], s52 offen offset:1024 lds\n"
      "buffer_load_dwordx4 v94, s[12:15], s52 offen offset:2048 lds\n"
      "LSpro_%=:\n"
      "s_waitcnt vmcnt(6)\n"
      "s_barrier\n"
      "ds_read_b64_tr_b16 a[224:225], v95 offset:24832\n"
      "ds_read_b64_tr_b16 a[226:227], v95 offset:25600\n"
      "ds_read_b64_tr_b16 a[0:1], v95 offset:0\n"
      "ds_read_b64_tr_b16 a[2:3], v95 offset:768\n"
      "ds_read_b64_tr_b16 a[220:221], v95 offset:30976\n"
      "ds_read_b64_tr_b16 a[222:223], v95 offset:31744\n"
      "ds_read_b64_tr_b16 a[232:233], v95 offset:24896\n"
      "ds_read_b64_tr_b16 a[234:235], v95 offset:25664\n"
      "ds_read_b64_tr_b16 a[4:5], v95 offset:64\n"
      "ds_read_b64_tr_b16 a[6:7], v95 offset:832\n"
      "ds_read_b64_tr_b16 a[228:229], v95 offset:31040\n"
      "ds_read_b64_tr_b16 a[230:231], v95 offset:31808\n"
      "s_add_u32 m0, s63, 6144\n"
      "s_add_u32 s52, s63, 12288\n"
      "s_waitcnt lgkmcnt(10)\n"
      "v_mfma_f32_4x4x4_16b_bf16 v[28:31], a[244:245], a[224:225], 0\n"
      "v_mfma_f32_4x4x4_16b_bf16 v[32:35], a[244:245], a[226:227], 0\n"
      "buffer_load_dwordx4 v92, s[12:15], s52 offen offset:0 lds\n"
      "s_waitcnt lgkmcnt(8)\n"
      "v_mfma_f32_4x4x4_16b_bf16 v[28:31], a[248:249], a[0:1], v[28:31]\n"
      "ds_read_b64_tr_b16 a[240:241], v95 offset:24960\n"
      "v_mfma_f32_4x4x4_16b_bf16 v[32:35], a[248:249], a[2:3], v[32:35]\n"
      "buffer_load_dwordx4 v93, s[12:15], s52 offen offset:1024 lds\n"
      "s_waitcnt lgkmcnt(7)\n"
      "v_mfma_f32_4x4x4_16b_bf16 v[36:39], a[246:247], a[220:221], v[28:31]\n"
      "ds_read_b64_tr_b16 a[242:243], v95 offset:25728\n"
      "v_mfma_f32_4x4x4_16b_bf16 v[40:43], a[246:247], a[222:223], v[32:35]\n"
      "buffer_load_dwordx4 v94, s[12:15], s52 offen offset:2048 lds\n"
      "v_mfma_f32_4x4x4_16b_bf16 v[28:31], a[250:251], a[220:221], v[28:31]\n"
      "ds_read_b64_tr_b16 a[8:9], v95 offset:128\n"
      "v_mfma_f32_4x4x4_16b_bf16 v[32:35], a[250:251], a[222:223], v[32:35]\n"
      "ds_read_b64_tr_b16 a[10:11], v95 offset:896\n"
      "ds_read_b64_tr_b16 a[236:237], v95 offset:31104\n"
      "ds_read_b64_tr_b16 a[238:239], v95 offset:31872\n"
      "s_add_u32 m0, s63, 12288\n"
      "s_add_u32 s52, s63, 67584\n"
      "s_waitcnt lgkmcnt(5)\n"
      "v_mfma_f32_4x4x4_16b_bf16 v[76:79], a[244:245], a[232:233], 0\n"
      "v_mfma_f32_4x4x4_16b_bf16 v[80:83], a[244:245], a[234:235], 0\n"
      "buffer_load_dwordx4 v92, s[12:15], s52 offen offset:0 lds\n"
      "v_mfma_f32_4x4x4_16b_bf16 v[76:79], a[248:249], a[4:5], v[76:79]\n"
      "buffer_load_dwordx4 v93, s[12:15], s52 offen offset:1024 lds\n"
      "v_mfma_f32_4x4x4_16b_bf16 v[80:83], a[248:249], a[6:7], v[80:83]\n"
      "buffer_load_dwordx4 v94, s[12:15], s52 offen offset:2048 lds\n"
      "v_mfma_f32_4x4x4_16b_bf16 v[84:87], a[246:247], a[228:229], v[76:79]\n"
      "v_mfma_f32_4x4x4_16b_bf16 v[88:91], a[246:247], a[230:231], v[80:83]\n"
      "v_mfma_f32_4x4x4_16b_bf16 v[76:79], a[250:251], a[228:229], v[76:79]\n"
      "v_mfma_f32_4x4x4_16b_bf16 v[80:83], a[250:251], a[230:231], v[80:83]\n"
      "v_cvt_pk_bf16_f32 v252, v36, v40\n"
      "v_cvt_pk_bf16_f32 v253, v37, v41\n"
      "v_cvt_pk_bf16_f32 v254, v38, v42\n"
      "v_cvt_pk_bf16_f32 v255, v39, v43\n"
      "v_cvt_pk_bf16_f32 v248, v28, v32\n"
      "v_cvt_pk_bf16_f32 v249, v29, v33\n"
      "v_cvt_pk_bf16_f32 v250, v30, v34\n"
      "v_cvt_pk_bf16_f32 v251, v31, v35\n"
      "s_add_u32 m0, s63, 41216\n"
      "s_add_u32 s52, s63, 18432\n"
      "s_nop 0\n"
      "s_waitcnt lgkmcnt(0)\n"
      "v_mfma_f32_4x4x4_16b_bf16 v[28:31], a[244:245], a[240:241], 0\n"
      "v_mfma_f32_4x4x4_16b_bf16 v[32:35], a[244:245], a[242:243], 0\n"
      "buffer_load_dwordx4 v92, s[12:15], s52 offen offset:0 lds\n"
      "v_mfma_f32_4x4x4_16b_bf16 v[28:31], a[248:249], a[8:9], v[28:31]\n"
      "buffer_load_dwordx4 v93, s[12:15], s52 offen offset:1024 lds\n"
      "v_mfma_f32_4x4x4_16b_bf16 v[32:35], a[248:249], a[10:11], v[32:35]\n"
      "buffer_load_dwordx4 v94, s[12:15], s52 offen offset:2048 lds\n"
      "v_mfma_f32_4x4x4_16b_bf16 v[36:39], a[246:247], a[236:237], v[28:31]\n"
      "v_mfma_f32_4x4x4_16b_bf16 v[40:43], a[246:247], a[238:239], v[32:35]\n"
      "v_mfma_f32_4x4x4_16b_bf16 v[28:31], a[250:251], a[236:237], v[28:31]\n"
      "v_mfma_f32_4x4x4_16b_bf16 v[32:35], a[250:251], a[238:239], v[32:35]\n"
      "v_cvt_pk_bf16_f32 v244, v84, v88\n"
      "v_cvt_pk_bf16_f32 v245, v85, v89\n"
      "v_cvt_pk_bf16_f32 v246, v86, v90\n"
      "v_cvt_pk_bf16_f32 v247, v87, v91\n"
      "v_cvt_pk_bf16_f32 v240, v76, v80\n"
      "v_cvt_pk_bf16_f32 v241, v77, v81\n"
      "v_cvt_pk_bf16_f32 v242, v78, v82\n"
      "v_cvt_pk_bf16_f32 v243, v79, v83\n"
      "s_waitcnt vmcnt(9)\n"
      "s_barrier\n"
      "ds_read_b64_tr_b16 a[224:225], v97 offset:1536\n"
      "ds_read_b64_tr_b16 a[226:227], v97 offset:2304\n"
      "ds_read_b64_tr_b16 a[12:13], v97 offset:13824\n"
      "ds_read_b64_tr_b16 a[14:15], v97 offset:14592\n"
      "ds_read_b64_tr_b16 a[220:221], v97 offset:7680\n"
      "ds_read_b64_tr_b16 a[222:223], v97 offset:8448\n"
      "s_add_u32 m0, s63, 47360\n"
      "s_add_u32 s52, s63, 73728\n"
      "s_nop 0\n"
      "s_waitcnt lgkmcnt(4)\n"
      "v_mfma_f32_4x4x4_16b_bf16 v[76:79], a[244:245], a[224:225], 0\n"
      "v_mfma_f32_4x4x4_16b_bf16 v[80:83], a[244:245], a[226:227], 0\n"
      "buffer_load_dwordx4 v92, s[12:15], s52 offen offset:0 lds\n"
      "s_waitcnt lgkmcnt(2)\n"
      "v_mfma_f32_4x4x4_16b_bf16 v[76:79], a[248:249], a[12:13], v[76:79]\n"
      "ds_read_b64_tr_b16 a[232:233], v97 offset:1600\n"
      "v_mfma_f32_4x4x4_16b_bf16 v[80:83], a[248:249], a[14:15], v[80:83]\n"
      "buffer_load_dwordx4 v93, s[12:15], s52 offen offset:1024 lds\n"
      "s_waitcnt lgkmcnt(1)\n"
      "v_mfma_f32_4x4x4_16b_bf16 v[84:87], a[246:247], a[220:221], v[76:79]\n"
      "ds_read_b64_tr_b16 a[234:235], v97 offset:2368\n"
      "v_mfma_f32_4x4x4_16b_bf16 v[88:91], a[246:247], a[222:223], v[80:83]\n"
      "buffer_load_dwordx4 v94, s[12:15], s52 offen offset:2048 lds\n"
      "v_mfma_f32_4x4x4_16b_bf16 v[76:79], a[250:251], a[220:221], v[76:79]\n"
      "ds_read_b64_tr_b16 a[16:17], v97 offset:13888\n"
      "v_mfma_f32_4x4x4_16b_bf16 v[80:83], a[250:251], a[222:223], v[80:83]\n"
      "ds_read_b64_tr_b16 a[18:19], v97 offset:14656\n"
      "ds_read_b64_tr_b16 a[228:229], v97 offset:7744\n"
      "ds_read_b64_tr_b16 a[230:231], v97 offset:8512\n"
      "ds_read_b64_tr_b16 a[240:241], v97 offset:1664\n"
      "ds_read_b64_tr_b16 a[242:243], v97 offset:2432\n"
      "ds_read_b64_tr_b16 a[20:21], v97 offset:13952\n"
      "ds_read_b64_tr_b16 a[22:23], v97 offset:14720\n"
      "ds_read_b64_tr_b16 a[236:237], v97 offset:7808\n"
      "ds_read_b64_tr_b16 a[238:239], v97 offset:8576\n"
      "ds_read_b64_tr_b16 a[188:189], v7 offset:0\n"
      "ds_read_b64_tr_b16 a[190:191], v7 offset:8\n"
      "ds_read_b128 a[192:195], v3 offset:0\n"
      "v_cvt_pk_bf16_f32 v236, v36, v40\n"
      "v_cvt_pk_bf16_f32 v237, v37, v41\n"
      "v_cvt_pk_bf16_f32 v238, v38, v42\n"
      "v_cvt_pk_bf16_f32 v239, v39, v43\n"
      "v_cvt_pk_bf16_f32 v232, v28, v32\n"
      "v_cvt_pk_bf16_f32 v233, v29, v33\n"
      "v_cvt_pk_bf16_f32 v234, v30, v34\n"
      "v_cvt_pk_bf16_f32 v235, v31, v35\n"
      "s_waitcnt vmcnt(6)\n"
      "s_barrier\n"
      "s_add_u32 m0, s63, 59648\n"
      "s_add_u32 s52, s63, 24576\n"
      "s_nop 0\n"
      "s_waitcnt lgkmcnt(7)\n"
      "v_mfma_f32_4x4x4_16b_bf16 v[28:31], a[244:245], a[232:233], 0\n"
      "v_mfma_f32_4x4x4_16b_bf16 v[32:35], a[244:245], a[234:235], 0\n"
      "buffer_load_dwordx4 v92, s[12:15], s52 offen offset:0 lds\n"
      "v_mfma_f32_4x4x4_16b_bf16 v[28:31], a[248:249], a[16:17], v[28:31]\n"
      "ds_read_b64_tr_b16 a[224:225], v95 offset:6144\n"
      "v_mfma_f32_4x4x4_16b_bf16 v[32:35], a[248:249], a[18:19], v[32:35]\n"
      "buffer_load_dwordx4 v93, s[12:15], s52 offen offset:1024 lds\n"
      "v_mfma_f32_4x4x4_16b_bf16 v[36:39], a[246:247], a[228:229], v[28:31]\n"
      "ds_read_b64_tr_b16 a[226:227], v95 offset:6912\n"
      "v_mfma_f32_4x4x4_16b_bf16 v[40:43], a[246:247], a[230:231], v[32:35]\n"
      "buffer_load_dwordx4 v94, s[12:15], s52 offen offset:2048 lds\n"
      "v_mfma_f32_4x4x4_16b_bf16 v[28:31], a[250:251], a[228:229], v[28:31]\n"
      "ds_read_b64_tr_b16 a[24:25], v95 offset:18432\n"
      "v_mfma_f32_4x4x4_16b_bf16 v[32:35], a[250:251], a[230:231], v[32:35]\n"
      "ds_read_b64_tr_b16 a[26:27], v95 offset:19200\n"
      "s_nop 0\n"
      "s_waitcnt lgkmcnt(4)\n"
      "v_mfma_f32_4x4x4_16b_bf16 v[44:47], a[216:217], a[188:189], 0\n"
      "ds_read_b64_tr_b16 a[220:221], v95 offset:12288\n"
      "v_mfma_f32_4x4x4_16b_bf16 v[48:51], a[216:217], a[190:191], 0\n"
      "ds_read_b64_tr_b16 a[222:223], v95 offset:13056\n"
      "ds_read_b64_tr_b16 a[184:185], v7 offset:512\n"
      "ds_read_b64_tr_b16 a[186:187], v7 offset:520\n"
      "ds_read_b128 a[196:199], v3 offset:64\n"
      "v_cvt_pk_bf16_f32 v228, v84, v88\n"
      "v_cvt_pk_bf16_f32 v229, v85, v89\n"
      "v_cvt_pk_bf16_f32 v230, v86, v90\n"
      "v_cvt_pk_bf16_f32 v231, v87, v91\n"
      "v_cvt_pk_bf16_f32 v224, v76, v80\n"
      "v_cvt_pk_bf16_f32 v225, v77, v81\n"
      "v_cvt_pk_bf16_f32 v226, v78, v82\n"
      "v_cvt_pk_bf16_f32 v227, v79, v83\n"
      "s_add_u32 m0, s63, 65792\n"
      "s_add_u32 s52, s63, 79872\n"
      "v_mfma_f32_4x4x4_16b_bf16 v[76:79], a[244:245], a[240:241], 0\n"
      "v_mfma_f32_4x4x4_16b_bf16 v[80:83], a[244:245], a[242:243], 0\n"
      "buffer_load_dwordx4 v92, s[12:15], s52 offen offset:0 lds\n"
      "v_mfma_f32_4x4x4_16b_bf16 v[76:79], a[248:249], a[20:21], v[76:79]\n"
      "ds_read_b64_tr_b16 a[232:233], v95 offset:6208\n"
      "v_mfma_f32_4x4x4_16b_bf16 v[80:83], a[248:249], a[22:23], v[80:83]\n"
      "buffer_load_dwordx4 v93, s[12:15], s52 offen offset:1024 lds\n"
      "v_mfma_f32_4x4x4_16b_bf16 v[84:87], a[246:247], a[236:237], v[76:79]\n"
      "ds_read_b64_tr_b16 a[234:235], v95 offset:6976\n"
      "v_mfma_f32_4x4x4_16b_bf16 v[88:91], a[246:247], a[238:239], v[80:83]\n"
      "buffer_load_dwordx4 v94, s[12:15], s52 offen offset:2048 lds\n"
      "v_mfma_f32_4x4x4_16b_bf16 v[76:79], a[250:251], a[236:237], v[76:79]\n"
      "ds_read_b64_tr_b16 a[28:29], v95 offset:18496\n"
      "v_mfma_f32_4x4x4_16b_bf16 v[80:83], a[250:251], a[238:239], v[80:83]\n"
      "ds_read_b64_tr_b16 a[30:31], v95 offset:19264\n"
      "s_nop 0\n"
      "s_waitcnt lgkmcnt(4)\n"
      "v_mfma_f32_4x4x4_16b_bf16 v[112:115], a[216:217], a[184:185], 0\n"
      "ds_read_b64_tr_b16 a[228:229], v95 offset:12352\n"
      "v_mfma_f32_4x4x4_16b_bf16 v[116:119], a[216:217], a[186:187], 0\n"
      "ds_read_b64_tr_b16 a[230:231], v95 offset:13120\n"
      "ds_read_b64_tr_b16 a[180:181], v7 offset:1024\n"
      "ds_read_b64_tr_b16 a[182:183], v7 offset:1032\n"
      "v_cvt_pk_bf16_f32 v220, v36, v40\n"
      "v_cvt_pk_bf16_f32 v221, v37, v41\n"
      "v_cvt_pk_bf16_f32 v222, v38, v42\n"
      "v_cvt_pk_bf16_f32 v223, v39, v43\n"
      "v_cvt_pk_bf16_f32 v216, v28, v32\n"
      "v_cvt_pk_bf16_f32 v217, v29, v33\n"
      "v_mfma_f32_16x16x32_bf16 v[100:103], a[0:3], a[192:195], 0\n"
      "v_cvt_pk_bf16_f32 v218, v30, v34\n"
      "v_cvt_pk_bf16_f32 v219, v31, v35\n"
      "v_mfma_f32_16x16x32_bf16 v[104:107], a[4:7], a[192:195], 0\n"
      "v_cvt_pk_bf16_f32 v44, v44, v48\n"
      "v_cvt_pk_bf16_f32 v45, v45, v49\n"
      "v_mfma_f32_16x16x32_bf16 v[108:111], a[8:11], a[192:195], 0\n"
      "s_add_u32 m0, s63, 134656\n"
      "v_cvt_pk_bf16_f32 v46, v46, v50\n"
      "v_cvt_pk_bf16_f32 v47, v47, v51\n"
      "s_add_u32 s52, s63, 30720\n"
      "v_mfma_f32_4x4x4_16b_bf16 v[28:31], a[244:245], a[224:225], 0\n"
      "v_mfma_f32_4x4x4_16b_bf16 v[32:35], a[244:245], a[226:227], 0\n"
      "buffer_load_dwordx4 v92, s[12:15], s52 offen offset:0 lds\n"
      "v_mfma_f32_4x4x4_16b_bf16 v[28:31], a[248:249], a[24:25], v[28:31]\n"
      "ds_read_b64_tr_b16 a[240:241], v95 offset:6272\n"
      "v_mfma_f32_4x4x4_16b_bf16 v[32:35], a[248:249], a[26:27], v[32:35]\n"
      "buffer_load_dwordx4 v93, s[12:15], s52 offen offset:1024 lds\n"
      "v_mfma_f32_4x4x4_16b_bf16 v[36:39], a[246:247], a[220:221], v[28:31]\n"
      "ds_read_b64_tr_b16 a[242:243], v95 offset:7040\n"
      "v_mfma_f32_4x4x4_16b_bf16 v[40:43], a[246:247], a[222:223], v[32:35]\n"
      "buffer_load_dwordx4 v94, s[12:15], s52 offen offset:2048 lds\n"
      "v_mfma_f32_4x4x4_16b_bf16 v[28:31], a[250:251], a[220:221], v[28:31]\n"
      "ds_read_b64_tr_b16 a[32:33], v95 offset:18560\n"
      "v_mfma_f32_4x4x4_16b_bf16 v[32:35], a[250:251], a[222:223], v[32:35]\n"
      "ds_read_b64_tr_b16 a[34:35], v95 offset:19328\n"
      "s_nop 0\n"
      "s_waitcnt lgkmcnt(4)\n"
      "v_mfma_f32_4x4x4_16b_bf16 v[20:23], a[216:217], a[180:181], 0\n"
      "ds_read_b64_tr_b16 a[236:237], v95 offset:12416\n"
      "v_mfma_f32_4x4x4_16b_bf16 v[24:27], a[216:217], a[182:183], 0\n"
      "ds_read_b64_tr_b16 a[238:239], v95 offset:13184\n"
      "ds_read_b64_tr_b16 a[176:177], v7 offset:1536\n"
      "ds_read_b64_tr_b16 a[178:179], v7 offset:1544\n"
      "s_mov_b32 m0, s64\n"
      "s_nop 0\n"
      "ds_write_addtid_b32 v44 offset:24832\n"
      "ds_write_addtid_b32 v45 offset:28928\n"
      "ds_write_addtid_b32 v46 offset:33024\n"
      "ds_write_addtid_b32 v47 offset:37120\n"
      "v_cvt_pk_bf16_f32 v212, v84, v88\n"
      "v_cvt_pk_bf16_f32 v213, v85, v89\n"
      "v_cvt_pk_bf16_f32 v214, v86, v90\n"
      "v_cvt_pk_bf16_f32 v215, v87, v91\n"
      "v_cvt_pk_bf16_f32 v208, v76, v80\n"
      "v_cvt_pk_bf16_f32 v209, v77, v81\n"
      "v_mfma_f32_16x16x32_bf16 v[100:103], a[12:15], a[196:199], v[100:103]\n"
      "s_nop 0\n"
      "v_cvt_pk_bf16_f32 v210, v78, v82\n"
      "v_cvt_pk_bf16_f32 v211, v79, v83\n"
      "v_mfma_f32_16x16x32_bf16 v[104:107], a[16:19], a[196:199], v[104:107]\n"
      "s_waitcnt vmcnt(9)\n"
      "v_cvt_pk_bf16_f32 v112, v112, v116\n"
      "v_cvt_pk_bf16_f32 v113, v113, v117\n"
      "v_mfma_f32_16x16x32_bf16 v[108:111], a[20:23], a[196:199], v[108:111]\n"
      "s_waitcnt lgkmcnt(0)\n"
      "v_cvt_pk_bf16_f32 v114, v114, v118\n"
      "v_cvt_pk_bf16_f32 v115, v115, v119\n"
      "s_barrier\n"
      "s_add_u32 m0, s63, 140800\n"
      "s_add_u32 s52, s63, 86016\n"
      "v_mfma_f32_4x4x4_16b_bf16 v[76:79], a[244:245], a[232:233], 0\n"
      "v_mfma_f32_4x4x4_16b_bf16 v[80:83], a[244:245], a[234:235], 0\n"
      "buffer_load_dwordx4 v92, s[12:15], s52 offen offset:0 lds\n"
      "v_mfma_f32_4x4x4_16b_bf16 v[76:79], a[248:249], a[28:29], v[76:79]\n"
      "ds_read_b64_tr_b16 a[224:225], v95 offset:41216\n"
      "v_mfma_f32_4x4x4_16b_bf16 v[80:83], a[248:249], a[30:31], v[80:83]\n"
      "buffer_load_dwordx4 v93, s[12:15], s52 offen offset:1024 lds\n"
      "v_mfma_f32_4x4x4_16b_bf16 v[84:87], a[246:247], a[228:229], v[76:79]\n"
      "ds_read_b64_tr_b16 a[226:227], v95 offset:41984\n"
      "v_mfma_f32_4x4x4_16b_bf16 v[88:91], a[246:247], a[230:231], v[80:83]\n"
      "buffer_load_dwordx4 v94, s[12:15], s52 offen offset:2048 lds\n"
      "v_mfma_f32_4x4x4_16b_bf16 v[76:79], a[250:251], a[228:229], v[76:79]\n"
      "ds_read_b64_tr_b16 a[36:37], v95 offset:53504\n"
      "v_mfma_f32_4x4x4_16b_bf16 v[80:83], a[250:251], a[230:231], v[80:83]\n"
      "ds_read_b64_tr_b16 a[38:39], v95 offset:54272\n"
      "v_mfma_f32_4x4x4_16b_bf16 v[12:15], a[216:217], a[176:177], 0\n"
      "ds_read_b64_tr_b16 a[220:221], v95 offset:47360\n"
      "v_mfma_f32_4x4x4_16b_bf16 v[16:19], a[216:217], a[178:179], 0\n"
      "ds_read_b64_tr_b16 a[222:223], v95 offset:48128\n"
      "s_mov_b32 m0, s64\n"
      "s_nop 0\n"
      "ds_write_addtid_b32 v112 offset:25344\n"
      "ds_write_addtid_b32 v113 offset:29440\n"
      "ds_write_addtid_b32 v114 offset:33536\n"
      "ds_write_addtid_b32 v115 offset:37632\n"
      "ds_read_b128 a[200:203], v3 offset:128\n"
      "v_cvt_pk_bf16_f32 v204, v36, v40\n"
      "v_cvt_pk_bf16_f32 v205, v37, v41\n"
      "v_cvt_pk_bf16_f32 v206, v38, v42\n"
      "v_cvt_pk_bf16_f32 v207, v39, v43\n"
      "v_cvt_pk_bf16_f32 v200, v28, v32\n"
      "v_cvt_pk_bf16_f32 v201, v29, v33\n"
      "v_cvt_pk_bf16_f32 v202, v30, v34\n"
      "v_cvt_pk_bf16_f32 v203, v31, v35\n"
      "v_cvt_pk_bf16_f32 v20, v20, v24\n"
      "v_cvt_pk_bf16_f32 v21, v21, v25\n"
      "v_cvt_pk_bf16_f32 v22, v22, v26\n"
      "v_cvt_pk_bf16_f32 v23, v23, v27\n"
      "v_mfma_f32_4x4x4_16b_bf16 v[28:31], a[244:245], a[240:241], 0\n"
      "v_mfma_f32_4x4x4_16b_bf16 v[32:35], a[244:245], a[242:243], 0\n"
      "ds_read_b64_tr_b16 a[232:233], v95 offset:41280\n"
      "v_mfma_f32_4x4x4_16b_bf16 v[28:31], a[248:249], a[32:33], v[28:31]\n"
      "ds_read_b64_tr_b16 a[234:235], v95 offset:42048\n"
      "v_mfma_f32_4x4x4_16b_bf16 v[32:35], a[248:249], a[34:35], v[32:35]\n"
      "ds_read_b64_tr_b16 a[40:41], v95 offset:53568\n"
      "v_mfma_f32_4x4x4_16b_bf16 v[36:39], a[246:247], a[236:237], v[28:31]\n"
      "ds_read_b64_tr_b16 a[42:43], v95 offset:54336\n"
      "v_mfma_f32_4x4x4_16b_bf16 v[40:43], a[246:247], a[238:239], v[32:35]\n"
      "ds_read_b64_tr_b16 a[228:229], v95 offset:47424\n"
      "v_mfma_f32_4x4x4_16b_bf16 v[28:31], a[250:251], a[236:237], v[28:31]\n"
      "ds_read_b64_tr_b16 a[230:231], v95 offset:48192\n"
      "v_mfma_f32_4x4x4_16b_bf16 v[32:35], a[250:251], a[238:239], v[32:35]\n"
      "ds_write_addtid_b32 v20 offset:25856\n"
      "ds_write_addtid_b32 v21 offset:29952\n"
      "ds_write_addtid_b32 v22 offset:34048\n"
      "ds_write_addtid_b32 v23 offset:38144\n"
      "v_cvt_pk_bf16_f32 v196, v84, v88\n"
      "v_cvt_pk_bf16_f32 v197, v85, v89\n"
      "v_cvt_pk_bf16_f32 v198, v86, v90\n"
      "v_cvt_pk_bf16_f32 v199, v87, v91\n"
      "v_cvt_pk_bf16_f32 v192, v76, v80\n"
      "v_cvt_pk_bf16_f32 v193, v77, v81\n"
      "v_cvt_pk_bf16_f32 v194, v78, v82\n"
      "v_cvt_pk_bf16_f32 v195, v79, v83\n"
      "v_cvt_pk_bf16_f32 v12, v12, v16\n"
      "v_cvt_pk_bf16_f32 v13, v13, v17\n"
      "v_cvt_pk_bf16_f32 v14, v14, v18\n"
      "v_cvt_pk_bf16_f32 v15, v15, v19\n"
      "s_nop 0\n"
      "s_waitcnt lgkmcnt(4)\n"
      "v_mfma_f32_4x4x4_16b_bf16 v[76:79], a[244:245], a[224:225], 0\n"
      "v_mfma_f32_4x4x4_16b_bf16 v[80:83], a[244:245], a[226:227], 0\n"
      "ds_read_b64_tr_b16 a[240:241], v95 offset:41344\n"
      "v_mfma_f32_4x4x4_16b_bf16 v[76:79], a[248:249], a[36:37], v[76:79]\n"
      "ds_read_b64_tr_b16 a[242:243], v95 offset:42112\n"
      "v_mfma_f32_4x4x4_16b_bf16 v[80:83], a[248:249], a[38:39], v[80:83]\n"
      "ds_read_b64_tr_b16 a[44:45], v95 offset:53632\n"
      "v_mfma_f32_4x4x4_16b_bf16 v[84:87], a[246:247], a[220:221], v[76:79]\n"
      "ds_read_b64_tr_b16 a[46:47], v95 offset:54400\n"
      "v_mfma_f32_4x4x4_16b_bf16 v[88:91], a[246:247], a[222:223], v[80:83]\n"
      "ds_read_b64_tr_b16 a[236:237], v95 offset:47488\n"
      "v_mfma_f32_4x4x4_16b_bf16 v[76:79], a[250:251], a[220:221], v[76:79]\n"
      "ds_read_b64_tr_b16 a[238:239], v95 offset:48256\n"
      "v_mfma_f32_4x4x4_16b_bf16 v[80:83], a[250:251], a[222:223], v[80:83]\n"
      "ds_write_addtid_b32 v12 offset:26368\n"
      "ds_write_addtid_b32 v13 offset:30464\n"
      "ds_write_addtid_b32 v14 offset:34560\n"
      "ds_write_addtid_b32 v15 offset:38656\n"
      "v_cvt_pk_bf16_f32 v188, v36, v40\n"
      "v_cvt_pk_bf16_f32 v189, v37, v41\n"
      "v_mfma_f32_16x16x32_bf16 v[100:103], a[24:27], a[200:203], v[100:103]\n"
      "v_cvt_pk_bf16_f32 v190, v38, v42\n"
      "v_cvt_pk_bf16_f32 v191, v39, v43\n"
      "v_mfma_f32_16x16x32_bf16 v[104:107], a[28:31], a[200:203], v[104:107]\n"
      "s_waitcnt vmcnt(6)\n"
      "v_cvt_pk_bf16_f32 v184, v28, v32\n"
      "v_cvt_pk_bf16_f32 v185, v29, v33\n"
      "v_mfma_f32_16x16x32_bf16 v[108:111], a[32:35], a[200:203], v[108:111]\n"
      "s_waitcnt lgkmcnt(0)\n"
      "v_cvt_pk_bf16_f32 v186, v30, v34\n"
      "v_cvt_pk_bf16_f32 v187, v31, v35\n"
      "s_nop 0\n"
      "s_barrier\n"
      "s_add_u32 m0, s63, 6144\n"
      "s_add_u32 s52, s63, 36864\n"
      "v_mfma_f32_4x4x4_16b_bf16 v[28:31], a[244:245], a[232:233], 0\n"
      "v_mfma_f32_4x4x4_16b_bf16 v[32:35], a[244:245], a[234:235], 0\n"
      "buffer_load_dwordx4 v92, s[12:15], s52 offen offset:0 lds\n"
      "v_mfma_f32_4x4x4_16b_bf16 v[28:31], a[248:249], a[40:41], v[28:31]\n"
      "ds_read_b64_tr_b16 a[224:225], v96 offset:2304\n"
      "v_mfma_f32_4x4x4_16b_bf16 v[32:35], a[248:249], a[42:43], v[32:35]\n"
      "buffer_load_dwordx4 v93, s[12:15], s52 offen offset:1024 lds\n"
      "v_mfma_f32_4x4x4_16b_bf16 v[36:39], a[246:247], a[228:229], v[28:31]\n"
      "ds_read_b64_tr_b16 a[226:227], v96 offset:3072\n"
      "v_mfma_f32_4x4x4_16b_bf16 v[40:43], a[246:247], a[230:231], v[32:35]\n"
      "buffer_load_dwordx4 v94, s[12:15], s52 offen offset:2048 lds\n"
      "v_mfma_f32_4x4x4_16b_bf16 v[28:31], a[250:251], a[228:229], v[28:31]\n"
      "ds_read_b64_tr_b16 a[48:49], v96 offset:14592\n"
      "v_mfma_f32_4x4x4_16b_bf16 v[32:35], a[250:251], a[230:231], v[32:35]\n"
      "ds_read_b64_tr_b16 a[50:51], v96 offset:15360\n"
      "ds_read_b64_tr_b16 a[220:221], v96 offset:8448\n"
      "ds_read_b64_tr_b16 a[222:223], v96 offset:9216\n"
      "ds_read_b128 a[204:207], v4 offset:0\n"
      "ds_read_b128 a[208:211], v0 offset:0\n"
      "ds_read_b128 a[212:215], v0 offset:4096\n"
      "v_cvt_pk_bf16_f32 v180, v84, v88\n"
      "v_cvt_pk_bf16_f32 v181, v85, v89\n"
      "v_cvt_pk_bf16_f32 v182, v86, v90\n"
      "v_cvt_pk_bf16_f32 v183, v87, v91\n"
      "v_cvt_pk_bf16_f32 v176, v76, v80\n"
      "v_cvt_pk_bf16_f32 v177, v77, v81\n"
      "v_cvt_pk_bf16_f32 v178, v78, v82\n"
      "v_cvt_pk_bf16_f32 v179, v79, v83\n"
      "s_add_u32 m0, s63, 12288\n"
      "s_add_u32 s52, s63, 92160\n"
      "v_mfma_f32_4x4x4_16b_bf16 v[76:79], a[244:245], a[240:241], 0\n"
      "v_mfma_f32_4x4x4_16b_bf16 v[80:83], a[244:245], a[242:243], 0\n"
      "buffer_load_dwordx4 v92, s[12:15], s52 offen offset:0 lds\n"
      "v_mfma_f32_4x4x4_16b_bf16 v[76:79], a[248:249], a[44:45], v[76:79]\n"
      "ds_read_b64_tr_b16 a[232:233], v96 offset:2368\n"
      "v_mfma_f32_4x4x4_16b_bf16 v[80:83], a[248:249], a[46:47], v[80:83]\n"
      "buffer_load_dwordx4 v93, s[12:15], s52 offen offset:1024 lds\n"
      "v_mfma_f32_4x4x4_16b_bf16 v[84:87], a[246:247], a[236:237], v[76:79]\n"
      "ds_read_b64_tr_b16 a[234:235], v96 offset:3136\n"
      "v_mfma_f32_4x4x4_16b_bf16 v[88:91], a[246:247], a[238:239], v[80:83]\n"
      "buffer_load_dwordx4 v94, s[12:15], s52 offen offset:2048 lds\n"
      "v_mfma_f32_4x4x4_16b_bf16 v[76:79], a[250:251], a[236:237], v[76:79]\n"
      "ds_read_b64_tr_b16 a[52:53], v96 offset:14656\n"
      "v_mfma_f32_4x4x4_16b_bf16 v[80:83], a[250:251], a[238:239], v[80:83]\n"
      "ds_read_b64_tr_b16 a[54:55], v96 offset:15424\n"
      "ds_read_b64_tr_b16 a[228:229], v96 offset:8512\n"
      "ds_read_b64_tr_b16 a[230:231], v96 offset:9280\n"
      "ds_read_b128 a[192:195], v0 offset:64\n"
      "ds_read_b128 a[196:199], v0 offset:4160\n"
      "ds_read_b128 a[200:203], v0 offset:128\n"
      "v_cvt_pk_bf16_f32 v172, v36, v40\n"
      "v_cvt_pk_bf16_f32 v173, v37, v41\n"
      "v_cvt_pk_bf16_f32 v174, v38, v42\n"
      "v_cvt_pk_bf16_f32 v175, v39, v43\n"
      "v_cvt_pk_bf16_f32 v168, v28, v32\n"
      "v_cvt_pk_bf16_f32 v169, v29, v33\n"
      "v_cvt_pk_bf16_f32 v170, v30, v34\n"
      "v_cvt_pk_bf16_f32 v171, v31, v35\n"
      "s_nop 0\n"
      "s_waitcnt lgkmcnt(7)\n"
      "v_mfma_f32_4x4x4_16b_bf16 v[28:31], a[244:245], a[224:225], 0\n"
      "v_mfma_f32_4x4x4_16b_bf16 v[32:35], a[244:245], a[226:227], 0\n"
      "ds_read_b64_tr_b16 a[240:241], v96 offset:2432\n"
      "v_mfma_f32_4x4x4_16b_bf16 v[28:31], a[248:249], a[48:49], v[28:31]\n"
      "ds_read_b64_tr_b16 a[242:243], v96 offset:3200\n"
      "v_mfma_f32_4x4x4_16b_bf16 v[32:35], a[248:249], a[50:51], v[32:35]\n"
      "ds_read_b64_tr_b16 a[56:57], v96 offset:14720\n"
      "v_mfma_f32_4x4x4_16b_bf16 v[36:39], a[246:247], a[220:221], v[28:31]\n"
      "ds_read_b64_tr_b16 a[58:59], v96 offset:15488\n"
      "v_mfma_f32_4x4x4_16b_bf16 v[40:43], a[246:247], a[222:223], v[32:35]\n"
      "ds_read_b64_tr_b16 a[236:237], v96 offset:8576\n"
      "v_mfma_f32_4x4x4_16b_bf16 v[28:31], a[250:251], a[220:221], v[28:31]\n"
      "ds_read_b64_tr_b16 a[238:239], v96 offset:9344\n"
      "v_mfma_f32_4x4x4_16b_bf16 v[32:35], a[250:251], a[222:223], v[32:35]\n"
      "v_mfma_f32_16x16x32_bf16 v[100:103], a[36:39], a[204:207], v[100:103]\n"
      "v_cvt_pk_bf16_f32 v164, v84, v88\n"
      "v_cvt_pk_bf16_f32 v165, v85, v89\n"
      "v_mfma_f32_16x16x32_bf16 v[104:107], a[40:43], a[204:207], v[104:107]\n"
      "v_cvt_pk_bf16_f32 v166, v86, v90\n"
      "v_cvt_pk_bf16_f32 v167, v87, v91\n"
      "v_mfma_f32_16x16x32_bf16 v[108:111], a[44:47], a[204:207], v[108:111]\n"
      "s_nop 0\n"
      "v_cvt_pk_bf16_f32 v160, v76, v80\n"
      "v_cvt_pk_bf16_f32 v161, v77, v81\n"
      "v_mfma_f32_16x16x32_bf16 v[52:55], v[252:255], a[208:211], 0\n"
      "s_waitcnt vmcnt(6)\n"
      "v_cvt_pk_bf16_f32 v162, v78, v82\n"
      "v_cvt_pk_bf16_f32 v163, v79, v83\n"
      "s_barrier\n"
      "s_add_u32 m0, s63, 41216\n"
      "s_add_u32 s52, s63, 43008\n"
      "v_mfma_f32_4x4x4_16b_bf16 v[76:79], a[244:245], a[232:233], 0\n"
      "v_mfma_f32_4x4x4_16b_bf16 v[80:83], a[244:245], a[234:235], 0\n"
      "buffer_load_dwordx4 v92, s[12:15], s52 offen offset:0 lds\n"
      "s_waitcnt lgkmcnt(0)\n"
      "v_mfma_f32_4x4x4_16b_bf16 v[76:79], a[248:249], a[52:53], v[76:79]\n"
      "ds_read_b64_tr_b16 a[224:225], v97 offset:19968\n"
      "v_mfma_f32_4x4x4_16b_bf16 v[80:83], a[248:249], a[54:55], v[80:83]\n"
      "buffer_load_dwordx4 v93, s[12:15], s52 offen offset:1024 lds\n"
      "v_mfma_f32_4x4x4_16b_bf16 v[84:87], a[246:247], a[228:229], v[76:79]\n"
      "ds_read_b64_tr_b16 a[226:227], v97 offset:20736\n"
      "v_mfma_f32_4x4x4_16b_bf16 v[88:91], a[246:247], a[230:231], v[80:83]\n"
      "buffer_load_dwordx4 v94, s[12:15], s52 offen offset:2048 lds\n"
      "v_mfma_f32_4x4x4_16b_bf16 v[76:79], a[250:251], a[228:229], v[76:79]\n"
      "ds_read_b64_tr_b16 a[60:61], v95 offset:0\n"
      "v_mfma_f32_4x4x4_16b_bf16 v[80:83], a[250:251], a[230:231], v[80:83]\n"
      "ds_read_b64_tr_b16 a[62:63], v95 offset:768\n"
      "ds_read_b64_tr_b16 a[220:221], v97 offset:26112\n"
      "ds_read_b64_tr_b16 a[222:223], v97 offset:26880\n"
      "ds_read_b128 a[204:207], v0 offset:4224\n"
      "v_mfma_f32_16x16x32_bf16 v[56:59], v[244:247], a[208:211], 0\n"
      "v_cvt_pk_bf16_f32 v156, v36, v40\n"
      "v_cvt_pk_bf16_f32 v157, v37, v41\n"
      "v_mfma_f32_16x16x32_bf16 v[60:63], v[236:239], a[208:211], 0\n"
      "v_cvt_pk_bf16_f32 v158, v38, v42\n"
      "v_cvt_pk_bf16_f32 v159, v39, v43\n"
      "v_mfma_f32_16x16x32_bf16 v[64:67], v[248:251], a[212:215], 0\n"
      "v_cvt_pk_bf16_f32 v152, v28, v32\n"
      "v_cvt_pk_bf16_f32 v153, v29, v33\n"
      "v_mfma_f32_16x16x32_bf16 v[68:71], v[240:243], a[212:215], 0\n"
      "s_add_u32 m0, s63, 47360\n"
      "v_cvt_pk_bf16_f32 v154, v30, v34\n"
      "v_cvt_pk_bf16_f32 v155, v31, v35\n"
      "s_add_u32 s52, s63, 98304\n"
      "v_mfma_f32_4x4x4_16b_bf16 v[28:31], a[244:245], a[240:241], 0\n"
      "v_mfma_f32_4x4x4_16b_bf16 v[32:35], a[244:245], a[242:243], 0\n"
      "buffer_load_dwordx4 v92, s[12:15], s52 offen offset:0 lds\n"
      "v_mfma_f32_4x4x4_16b_bf16 v[28:31], a[248:249], a[56:57], v[28:31]\n"
      "ds_read_b64_tr_b16 a[232:233], v97 offset:20032\n"
      "v_mfma_f32_4x4x4_16b_bf16 v[32:35], a[248:249], a[58:59], v[32:35]\n"
      "buffer_load_dwordx4 v93, s[12:15], s52 offen offset:1024 lds\n"
      "v_mfma_f32_4x4x4_16b_bf16 v[36:39], a[246:247], a[236:237], v[28:31]\n"
      "ds_read_b64_tr_b16 a[234:235], v97 offset:20800\n"
      "v_mfma_f32_4x4x4_16b_bf16 v[40:43], a[246:247], a[238:239], v[32:35]\n"
      "buffer_load_dwordx4 v94, s[12:15], s52 offen offset:2048 lds\n"
      "v_mfma_f32_4x4x4_16b_bf16 v[28:31], a[250:251], a[236:237], v[28:31]\n"
      "ds_read_b64_tr_b16 a[64:65], v95 offset:64\n"
      "v_mfma_f32_4x4x4_16b_bf16 v[32:35], a[250:251], a[238:239], v[32:35]\n"
      "ds_read_b64_tr_b16 a[66:67], v95 offset:832\n"
      "ds_read_b64_tr_b16 a[228:229], v97 offset:26176\n"
      "ds_read_b64_tr_b16 a[230:231], v97 offset:26944\n"
      "ds_read_b128 a[208:211], v1 offset:0\n"
      "v_mfma_f32_16x16x32_bf16 v[72:75], v[232:235], a[212:215], 0\n"
      "v_cvt_pk_bf16_f32 v148, v84, v88\n"
      "v_cvt_pk_bf16_f32 v149, v85, v89\n"
      "v_mfma_f32_16x16x32_bf16 v[52:55], v[228:231], a[192:195], v[52:55]\n"
      "v_cvt_pk_bf16_f32 v150, v86, v90\n"
      "v_cvt_pk_bf16_f32 v151, v87, v91\n"
      "v_mfma_f32_16x16x32_bf16 v[56:59], v[220:223], a[192:195], v[56:59]\n"
      "s_nop 0\n"
      "v_cvt_pk_bf16_f32 v144, v76, v80\n"
      "v_cvt_pk_bf16_f32 v145, v77, v81\n"
      "v_mfma_f32_16x16x32_bf16 v[60:63], v[212:215], a[192:195], v[60:63]\n"
      "s_waitcnt lgkmcnt(2)\n"
      "v_cvt_pk_bf16_f32 v146, v78, v82\n"
      "v_cvt_pk_bf16_f32 v147, v79, v83\n"
      "v_mfma_f32_4x4x4_16b_bf16 v[76:79], a[244:245], a[224:225], 0\n"
      "v_mfma_f32_4x4x4_16b_bf16 v[80:83], a[244:245], a[226:227], 0\n"
      "ds_read_b64_tr_b16 a[240:241], v97 offset:20096\n"
      "v_mfma_f32_4x4x4_16b_bf16 v[76:79], a[248:249], a[60:61], v[76:79]\n"
      "ds_read_b64_tr_b16 a[242:243], v97 offset:20864\n"
      "v_mfma_f32_4x4x4_16b_bf16 v[80:83], a[248:249], a[62:63], v[80:83]\n"
      "ds_read_b64_tr_b16 a[68:69], v95 offset:128\n"
      "v_mfma_f32_4x4x4_16b_bf16 v[84:87], a[246:247], a[220:221], v[76:79]\n"
      "ds_read_b64_tr_b16 a[70:71], v95 offset:896\n"
      "v_mfma_f32_4x4x4_16b_bf16 v[88:91], a[246:247], a[222:223], v[80:83]\n"
      "ds_read_b64_tr_b16 a[236:237], v97 offset:26240\n"
      "v_mfma_f32_4x4x4_16b_bf16 v[76:79], a[250:251], a[220:221], v[76:79]\n"
      "ds_read_b64_tr_b16 a[238:239], v97 offset:27008\n"
      "v_mfma_f32_4x4x4_16b_bf16 v[80:83], a[250:251], a[222:223], v[80:83]\n"
      "ds_read_b128 a[212:215], v1 offset:4096\n"
      "ds_read_b128 a[192:195], v4 offset:64\n"
      "v_mfma_f32_16x16x32_bf16 v[64:67], v[224:227], a[196:199], v[64:67]\n"
      "v_cvt_pk_bf16_f32 v140, v36, v40\n"
      "v_cvt_pk_bf16_f32 v141, v37, v41\n"
      "v_mfma_f32_16x16x32_bf16 v[68:71], v[216:219], a[196:199], v[68:71]\n"
      "v_cvt_pk_bf16_f32 v142, v38, v42\n"
      "v_cvt_pk_bf16_f32 v143, v39, v43\n"
      "v_mfma_f32_16x16x32_bf16 v[72:75], v[208:211], a[196:199], v[72:75]\n"
      "v_cvt_pk_bf16_f32 v136, v28, v32\n"
      "v_cvt_pk_bf16_f32 v137, v29, v33\n"
      "v_mfma_f32_16x16x32_bf16 v[52:55], v[204:207], a[200:203], v[52:55]\n"
      "s_waitcnt vmcnt(6)\n"
      "v_cvt_pk_bf16_f32 v138, v30, v34\n"
      "v_cvt_pk_bf16_f32 v139, v31, v35\n"
      "s_barrier\n"
      "s_add_u32 m0, s63, 59648\n"
      "s_add_u32 s52, s63, 49152\n"
      "v_mfma_f32_4x4x4_16b_bf16 v[28:31], a[244:245], a[232:233], 0\n"
      "v_mfma_f32_4x4x4_16b_bf16 v[32:35], a[244:245], a[234:235], 0\n"
      "buffer_load_dwordx4 v92, s[12:15], s52 offen offset:0 lds\n"
      "v_mfma_f32_4x4x4_16b_bf16 v[28:31], a[248:249], a[64:65], v[28:31]\n"
      "ds_read_b64_tr_b16 a[224:225], v95 offset:6144\n"
      "v_mfma_f32_4x4x4_16b_bf16 v[32:35], a[248:249], a[66:67], v[32:35]\n"
      "buffer_load_dwordx4 v93, s[12:15], s52 offen offset:1024 lds\n"
      "s_nop 0\n"
      "s_waitcnt lgkmcnt(1)\n"
      "v_mfma_f32_4x4x4_16b_bf16 v[36:39], a[246:247], a[228:229], v[28:31]\n"
      "ds_read_b64_tr_b16 a[226:227], v95 offset:6912\n"
      "v_mfma_f32_4x4x4_16b_bf16 v[40:43], a[246:247], a[230:231], v[32:35]\n"
      "buffer_load_dwordx4 v94, s[12:15], s52 offen offset:2048 lds\n"
      "v_mfma_f32_4x4x4_16b_bf16 v[28:31], a[250:251], a[228:229], v[28:31]\n"
      "ds_read_b64_tr_b16 a[72:73], v95 offset:18432\n"
      "v_mfma_f32_4x4x4_16b_bf16 v[32:35], a[250:251], a[230:231], v[32:35]\n"
      "ds_read_b64_tr_b16 a[74:75], v95 offset:19200\n"
      "ds_read_b64_tr_b16 a[220:221], v95 offset:12288\n"
      "ds_read_b64_tr_b16 a[222:223], v95 offset:13056\n"
      "ds_read_b128 a[196:199], v1 offset:64\n"
      "v_mfma_f32_16x16x32_bf16 v[56:59], v[196:199], a[200:203], v[56:59]\n"
      "v_cvt_pk_bf16_f32 v132, v84, v88\n"
      "v_cvt_pk_bf16_f32 v133, v85, v89\n"
      "v_mfma_f32_16x16x32_bf16 v[60:63], v[188:191], a[200:203], v[60:63]\n"
      "v_cvt_pk_bf16_f32 v134, v86, v90\n"
      "v_cvt_pk_bf16_f32 v135, v87, v91\n"
      "v_mfma_f32_16x16x32_bf16 v[64:67], v[200:203], a[204:207], v[64:67]\n"
      "v_cvt_pk_bf16_f32 v128, v76, v80\n"
      "v_cvt_pk_bf16_f32 v129, v77, v81\n"
      "v_mfma_f32_16x16x32_bf16 v[68:71], v[192:195], a[204:207], v[68:71]\n"
      "s_add_u32 m0, s63, 65792\n"
      "v_cvt_pk_bf16_f32 v130, v78, v82\n"
      "v_cvt_pk_bf16_f32 v131, v79, v83\n"
      "s_add_u32 s52, s63, 104448\n"
      "v_mfma_f32_4x4x4_16b_bf16 v[76:79], a[244:245], a[240:241], 0\n"
      "v_mfma_f32_4x4x4_16b_bf16 v[80:83], a[244:245], a[242:243], 0\n"
      "buffer_load_dwordx4 v92, s[12:15], s52 offen offset:0 lds\n"
      "v_mfma_f32_4x4x4_16b_bf16 v[76:79], a[248:249], a[68:69], v[76:79]\n"
      "ds_read_b64_tr_b16 a[232:233], v95 offset:6208\n"
      "v_mfma_f32_4x4x4_16b_bf16 v[80:83], a[248:249], a[70:71], v[80:83]\n"
      "buffer_load_dwordx4 v93, s[12:15], s52 offen offset:1024 lds\n"
      "v_mfma_f32_4x4x4_16b_bf16 v[84:87], a[246:247], a[236:237], v[76:79]\n"
      "ds_read_b64_tr_b16 a[234:235], v95 offset:6976\n"
      "v_mfma_f32_4x4x4_16b_bf16 v[88:91], a[246:247], a[238:239], v[80:83]\n"
      "buffer_load_dwordx4 v94, s[12:15], s52 offen offset:2048 lds\n"
      "v_mfma_f32_4x4x4_16b_bf16 v[76:79], a[250:251], a[236:237], v[76:79]\n"
      "ds_read_b64_tr_b16 a[76:77], v95 offset:18496\n"
      "v_mfma_f32_4x4x4_16b_bf16 v[80:83], a[250:251], a[238:239], v[80:83]\n"
      "ds_read_b64_tr_b16 a[78:79], v95 offset:19264\n"
      "ds_read_b64_tr_b16 a[228:229], v95 offset:12352\n"
      "ds_read_b64_tr_b16 a[230:231], v95 offset:13120\n"
      "ds_read_b128 a[200:203], v1 offset:4160\n"
      "v_mfma_f32_16x16x32_bf16 v[72:75], v[184:187], a[204:207], v[72:75]\n"
      "v_cvt_pk_bf16_f32 v124, v36, v40\n"
      "v_cvt_pk_bf16_f32 v125, v37, v41\n"
      "v_mfma_f32_16x16x32_bf16 v[52:55], v[180:183], a[208:211], v[52:55]\n"
      "v_cvt_pk_bf16_f32 v126, v38, v42\n"
      "v_cvt_pk_bf16_f32 v127, v39, v43\n"
      "v_mfma_f32_16x16x32_bf16 v[56:59], v[172:175], a[208:211], v[56:59]\n"
      "v_cvt_pk_bf16_f32 v28, v28, v32\n"
      "v_cvt_pk_bf16_f32 v29, v29, v33\n"
      "v_mfma_f32_16x16x32_bf16 v[60:63], v[164:167], a[208:211], v[60:63]\n"
      "v_cvt_pk_bf16_f32 v30, v30, v34\n"
      "v_cvt_pk_bf16_f32 v31, v31, v35\n"
      "v_mfma_f32_16x16x32_bf16 v[64:67], v[176:179], a[212:215], v[64:67]\n"
      "s_nop 0\n"
      "v_accvgpr_write_b32 a188, v28\n"
      "v_accvgpr_write_b32 a189, v29\n"
      "v_mfma_f32_16x16x32_bf16 v[68:71], v[168:171], a[212:215], v[68:71]\n"
      "s_waitcnt lgkmcnt(0)\n"
      "v_accvgpr_write_b32 a190, v30\n"
      "v_accvgpr_write_b32 a191, v31\n"
      "v_mfma_f32_4x4x4_16b_bf16 v[28:31], a[244:245], a[224:225], 0\n"
      "v_mfma_f32_4x4x4_16b_bf16 v[32:35], a[244:245], a[226:227], 0\n"
      "ds_read_b64_tr_b16 a[240:241], v95 offset:6272\n"
      "v_mfma_f32_4x4x4_16b_bf16 v[28:31], a[248:249], a[72:73], v[28:31]\n"
      "ds_read_b64_tr_b16 a[242:243], v95 offset:7040\n"
      "v_mfma_f32_4x4x4_16b_bf16 v[32:35], a[248:249], a[74:75], v[32:35]\n"
      "ds_read_b64_tr_b16 a[80:81], v95 offset:18560\n"
      "v_mfma_f32_4x4x4_16b_bf16 v[36:39], a[246:247], a[220:221], v[28:31]\n"
      "ds_read_b64_tr_b16 a[82:83], v95 offset:19328\n"
      "v_mfma_f32_4x4x4_16b_bf16 v[40:43], a[246:247], a[222:223], v[32:35]\n"
      "ds_read_b64_tr_b16 a[236:237], v95 offset:12416\n"
      "v_mfma_f32_4x4x4_16b_bf16 v[28:31], a[250:251], a[220:221], v[28:31]\n"
      "ds_read_b64_tr_b16 a[238:239], v95 offset:13184\n"
      "v_mfma_f32_4x4x4_16b_bf16 v[32:35], a[250:251], a[222:223], v[32:35]\n"
      "ds_read_b128 a[204:207], v4 offset:128\n"
      "ds_read_b128 a[208:211], v1 offset:128\n"
      "v_cvt_pk_bf16_f32 v84, v84, v88\n"
      "v_cvt_pk_bf16_f32 v85, v85, v89\n"
      "v_cvt_pk_bf16_f32 v86, v86, v90\n"
      "v_cvt_pk_bf16_f32 v87, v87, v91\n"
      "v_mfma_f32_16x16x32_bf16 v[72:75], v[160:163], a[212:215], v[72:75]\n"
      "v_accvgpr_write_b32 a184, v84\n"
      "v_accvgpr_write_b32 a185, v85\n"
      "v_mfma_f32_16x16x32_bf16 v[100:103], a[48:51], a[192:195], v[100:103]\n"
      "v_accvgpr_write_b32 a186, v86\n"
      "v_accvgpr_write_b32 a187, v87\n"
      "v_mfma_f32_16x16x32_bf16 v[104:107], a[52:55], a[192:195], v[104:107]\n"
      "v_cvt_pk_bf16_f32 v76, v76, v80\n"
      "v_cvt_pk_bf16_f32 v77, v77, v81\n"
      "v_mfma_f32_16x16x32_bf16 v[108:111], a[56:59], a[192:195], v[108:111]\n"
      "v_cvt_pk_bf16_f32 v78, v78, v82\n"
      "v_cvt_pk_bf16_f32 v79, v79, v83\n"
      "v_mfma_f32_16x16x32_bf16 v[52:55], v[156:159], a[196:199], v[52:55]\n"
      "v_accvgpr_write_b32 a180, v76\n"
      "v_accvgpr_write_b32 a181, v77\n"
      "v_mfma_f32_16x16x32_bf16 v[56:59], v[148:151], a[196:199], v[56:59]\n"
      "s_waitcnt vmcnt(6)\n"
      "v_accvgpr_write_b32 a182, v78\n"
      "v_accvgpr_write_b32 a183, v79\n"
      "s_barrier\n"
      "v_mfma_f32_4x4x4_16b_bf16 v[76:79], a[244:245], a[232:233], 0\n"
      "v_mfma_f32_4x4x4_16b_bf16 v[80:83], a[244:245], a[234:235], 0\n"
      "ds_read_b64_tr_b16 a[224:225], v95 offset:41216\n"
      "v_mfma_f32_4x4x4_16b_bf16 v[76:79], a[248:249], a[76:77], v[76:79]\n"
      "ds_read_b64_tr_b16 a[226:227], v95 offset:41984\n"
      "v_mfma_f32_4x4x4_16b_bf16 v[80:83], a[248:249], a[78:79], v[80:83]\n"
      "ds_read_b64_tr_b16 a[84:85], v95 offset:53504\n"
      "v_mfma_f32_4x4x4_16b_bf16 v[84:87], a[246:247], a[228:229], v[76:79]\n"
      "ds_read_b64_tr_b16 a[86:87], v95 offset:54272\n"
      "v_mfma_f32_4x4x4_16b_bf16 v[88:91], a[246:247], a[230:231], v[80:83]\n"
      "ds_read_b64_tr_b16 a[220:221], v95 offset:47360\n"
      "v_mfma_f32_4x4x4_16b_bf16 v[76:79], a[250:251], a[228:229], v[76:79]\n"
      "ds_read_b64_tr_b16 a[222:223], v95 offset:48128\n"
      "v_mfma_f32_4x4x4_16b_bf16 v[80:83], a[250:251], a[230:231], v[80:83]\n"
      "ds_read_b128 a[212:215], v1 offset:4224\n"
      "ds_read_b128 a[192:195], v5 offset:0\n"
      "v_cvt_pk_bf16_f32 v36, v36, v40\n"
      "v_cvt_pk_bf16_f32 v37, v37, v41\n"
      "v_cvt_pk_bf16_f32 v38, v38, v42\n"
      "v_cvt_pk_bf16_f32 v39, v39, v43\n"
      "v_mfma_f32_16x16x32_bf16 v[60:63], v[140:143], a[196:199], v[60:63]\n"
      "v_accvgpr_write_b32 a176, v36\n"
      "v_accvgpr_write_b32 a177, v37\n"
      "v_mfma_f32_16x16x32_bf16 v[64:67], v[152:155], a[200:203], v[64:67]\n"
      "v_accvgpr_write_b32 a178, v38\n"
      "v_accvgpr_write_b32 a179, v39\n"
      "v_mfma_f32_16x16x32_bf16 v[68:71], v[144:147], a[200:203], v[68:71]\n"
      "v_cvt_pk_bf16_f32 v28, v28, v32\n"
      "v_cvt_pk_bf16_f32 v29, v29, v33\n"
      "v_mfma_f32_16x16x32_bf16 v[72:75], v[136:139], a[200:203], v[72:75]\n"
      "s_waitcnt lgkmcnt(1)\n"
      "v_cvt_pk_bf16_f32 v30, v30, v34\n"
      "v_cvt_pk_bf16_f32 v31, v31, v35\n"
      "v_mfma_f32_16x16x32_bf16 v[100:103], a[60:63], a[204:207], v[100:103]\n"
      "s_nop 0\n"
      "v_accvgpr_write_b32 a172, v28\n"
      "v_accvgpr_write_b32 a173, v29\n"
      "v_mfma_f32_16x16x32_bf16 v[104:107], a[64:67], a[204:207], v[104:107]\n"
      "v_accvgpr_write_b32 a174, v30\n"
      "v_accvgpr_write_b32 a175, v31\n"
      "v_mfma_f32_4x4x4_16b_bf16 v[28:31], a[244:245], a[240:241], 0\n"
      "v_mfma_f32_4x4x4_16b_bf16 v[32:35], a[244:245], a[242:243], 0\n"
      "ds_read_b64_tr_b16 a[232:233], v95 offset:41280\n"
      "v_mfma_f32_4x4x4_16b_bf16 v[28:31], a[248:249], a[80:81], v[28:31]\n"
      "ds_read_b64_tr_b16 a[234:235], v95 offset:42048\n"
      "v_mfma_f32_4x4x4_16b_bf16 v[32:35], a[248:249], a[82:83], v[32:35]\n"
      "ds_read_b64_tr_b16 a[88:89], v95 offset:53568\n"
      "v_mfma_f32_4x4x4_16b_bf16 v[36:39], a[246:247], a[236:237], v[28:31]\n"
      "ds_read_b64_tr_b16 a[90:91], v95 offset:54336\n"
      "v_mfma_f32_4x4x4_16b_bf16 v[40:43], a[246:247], a[238:239], v[32:35]\n"
      "ds_read_b64_tr_b16 a[228:229], v95 offset:47424\n"
      "v_mfma_f32_4x4x4_16b_bf16 v[28:31], a[250:251], a[236:237], v[28:31]\n"
      "ds_read_b64_tr_b16 a[230:231], v95 offset:48192\n"
      "v_mfma_f32_4x4x4_16b_bf16 v[32:35], a[250:251], a[238:239], v[32:35]\n"
      "ds_read_b128 a[196:199], v2 offset:0\n"
      "ds_read_b128 a[200:203], v2 offset:4096\n"
      "v_cvt_pk_bf16_f32 v84, v84, v88\n"
      "v_cvt_pk_bf16_f32 v85, v85, v89\n"
      "v_cvt_pk_bf16_f32 v86, v86, v90\n"
      "v_cvt_pk_bf16_f32 v87, v87, v91\n"
      "v_mfma_f32_16x16x32_bf16 v[108:111], a[68:71], a[204:207], v[108:111]\n"
      "v_accvgpr_write_b32 a168, v84\n"
      "v_accvgpr_write_b32 a169, v85\n"
      "v_mfma_f32_16x16x32_bf16 v[52:55], v[132:135], a[208:211], v[52:55]\n"
      "v_accvgpr_write_b32 a170, v86\n"
      "v_accvgpr_write_b32 a171, v87\n"
      "v_mfma_f32_16x16x32_bf16 v[56:59], v[124:127], a[208:211], v[56:59]\n"
      "v_cvt_pk_bf16_f32 v76, v76, v80\n"
      "v_cvt_pk_bf16_f32 v77, v77, v81\n"
      "v_mfma_f32_16x16x32_bf16 v[60:63], a[184:187], a[208:211], v[60:63]\n"
      "v_cvt_pk_bf16_f32 v78, v78, v82\n"
      "v_cvt_pk_bf16_f32 v79, v79, v83\n"
      "v_mfma_f32_16x16x32_bf16 v[64:67], v[128:131], a[212:215], v[64:67]\n"
      "v_accvgpr_write_b32 a164, v76\n"
      "v_accvgpr_write_b32 a165, v77\n"
      "v_mfma_f32_16x16x32_bf16 v[68:71], a[188:191], a[212:215], v[68:71]\n"
      "v_accvgpr_write_b32 a166, v78\n"
      "v_accvgpr_write_b32 a167, v79\n"
      "v_mfma_f32_4x4x4_16b_bf16 v[76:79], a[244:245], a[224:225], 0\n"
      "v_mfma_f32_4x4x4_16b_bf16 v[80:83], a[244:245], a[226:227], 0\n"
      "ds_read_b64_tr_b16 a[240:241], v95 offset:41344\n"
      "v_mfma_f32_4x4x4_16b_bf16 v[76:79], a[248:249], a[84:85], v[76:79]\n"
      "ds_read_b64_tr_b16 a[242:243], v95 offset:42112\n"
      "v_mfma_f32_4x4x4_16b_bf16 v[80:83], a[248:249], a[86:87], v[80:83]\n"
      "ds_read_b64_tr_b16 a[92:93], v95 offset:53632\n"
      "v_mfma_f32_4x4x4_16b_bf16 v[84:87], a[246:247], a[220:221], v[76:79]\n"
      "ds_read_b64_tr_b16 a[94:95], v95 offset:54400\n"
      "v_mfma_f32_4x4x4_16b_bf16 v[88:91], a[246:247], a[222:223], v[80:83]\n"
      "ds_read_b64_tr_b16 a[236:237], v95 offset:47488\n"
      "v_mfma_f32_4x4x4_16b_bf16 v[76:79], a[250:251], a[220:221], v[76:79]\n"
      "ds_read_b64_tr_b16 a[238:239], v95 offset:48256\n"
      "v_mfma_f32_4x4x4_16b_bf16 v[80:83], a[250:251], a[222:223], v[80:83]\n"
      "v_cvt_pk_bf16_f32 v36, v36, v40\n"
      "v_cvt_pk_bf16_f32 v37, v37, v41\n"
      "v_cvt_pk_bf16_f32 v38, v38, v42\n"
      "v_cvt_pk_bf16_f32 v39, v39, v43\n"
      "v_accvgpr_write_b32 a160, v36\n"
      "v_accvgpr_write_b32 a161, v37\n"
      "v_accvgpr_write_b32 a162, v38\n"
      "v_accvgpr_write_b32 a163, v39\n"
      "v_mfma_f32_16x16x32_bf16 v[72:75], a[180:183], a[212:215], v[72:75]\n"
      "s_waitcnt lgkmcnt(2)\n"
      "v_cvt_pk_bf16_f32 v28, v28, v32\n"
      "v_cvt_pk_bf16_f32 v29, v29, v33\n"
      "v_mfma_f32_16x16x32_bf16 v[100:103], a[72:75], a[192:195], v[100:103]\n"
      "s_nop 0\n"
      "v_cvt_pk_bf16_f32 v30, v30, v34\n"
      "v_cvt_pk_bf16_f32 v31, v31, v35\n"
      "v_mfma_f32_16x16x32_bf16 v[104:107], a[76:79], a[192:195], v[104:107]\n"
      "v_accvgpr_write_b32 a156, v28\n"
      "v_accvgpr_write_b32 a157, v29\n"
      "v_mfma_f32_16x16x32_bf16 v[108:111], a[80:83], a[192:195], v[108:111]\n"
      "s_waitcnt vmcnt(0)\n"
      "v_accvgpr_write_b32 a158, v30\n"
      "v_accvgpr_write_b32 a159, v31\n"
      "s_barrier\n"
      "v_mfma_f32_4x4x4_16b_bf16 v[28:31], a[244:245], a[232:233], 0\n"
      "v_mfma_f32_4x4x4_16b_bf16 v[32:35], a[244:245], a[234:235], 0\n"
      "ds_read_b64_tr_b16 a[224:225], v96 offset:2304\n"
      "v_mfma_f32_4x4x4_16b_bf16 v[28:31], a[248:249], a[88:89], v[28:31]\n"
      "ds_read_b64_tr_b16 a[226:227], v96 offset:3072\n"
      "v_mfma_f32_4x4x4_16b_bf16 v[32:35], a[248:249], a[90:91], v[32:35]\n"
      "ds_read_b64_tr_b16 a[96:97], v96 offset:14592\n"
      "v_mfma_f32_4x4x4_16b_bf16 v[36:39], a[246:247], a[228:229], v[28:31]\n"
      "ds_read_b64_tr_b16 a[98:99], v96 offset:15360\n"
      "v_mfma_f32_4x4x4_16b_bf16 v[40:43], a[246:247], a[230:231], v[32:35]\n"
      "ds_read_b64_tr_b16 a[220:221], v96 offset:8448\n"
      "v_mfma_f32_4x4x4_16b_bf16 v[28:31], a[250:251], a[228:229], v[28:31]\n"
      "ds_read_b64_tr_b16 a[222:223], v96 offset:9216\n"
      "v_mfma_f32_4x4x4_16b_bf16 v[32:35], a[250:251], a[230:231], v[32:35]\n"
      "ds_read_b128 a[204:207], v5 offset:64\n"
      "v_cvt_pk_bf16_f32 v84, v84, v88\n"
      "v_cvt_pk_bf16_f32 v85, v85, v89\n"
      "v_cvt_pk_bf16_f32 v86, v86, v90\n"
      "v_cvt_pk_bf16_f32 v87, v87, v91\n"
      "v_mfma_f32_16x16x32_bf16 v[52:55], a[176:179], a[196:199], v[52:55]\n"
      "s_nop 0\n"
      "v_accvgpr_write_b32 a152, v84\n"
      "v_accvgpr_write_b32 a153, v85\n"
      "v_mfma_f32_16x16x32_bf16 v[56:59], a[168:171], a[196:199], v[56:59]\n"
      "s_nop 0\n"
      "v_accvgpr_write_b32 a154, v86\n"
      "v_accvgpr_write_b32 a155, v87\n"
      "v_mfma_f32_16x16x32_bf16 v[60:63], a[160:163], a[196:199], v[60:63]\n"
      "s_nop 0\n"
      "v_cvt_pk_bf16_f32 v76, v76, v80\n"
      "v_cvt_pk_bf16_f32 v77, v77, v81\n"
      "v_mfma_f32_16x16x32_bf16 v[64:67], a[172:175], a[200:203], v[64:67]\n"
      "s_nop 0\n"
      "v_cvt_pk_bf16_f32 v78, v78, v82\n"
      "v_cvt_pk_bf16_f32 v79, v79, v83\n"
      "v_mfma_f32_16x16x32_bf16 v[68:71], a[164:167], a[200:203], v[68:71]\n"
      "s_nop 0\n"
      "v_accvgpr_write_b32 a148, v76\n"
      "v_accvgpr_write_b32 a149, v77\n"
      "v_mfma_f32_16x16x32_bf16 v[72:75], a[156:159], a[200:203], v[72:75]\n"
      "s_nop 0\n"
      "v_accvgpr_write_b32 a150, v78\n"
      "v_accvgpr_write_b32 a151, v79\n"
      "v_mfma_f32_4x4x4_16b_bf16 v[76:79], a[244:245], a[240:241], 0\n"
      "v_mfma_f32_4x4x4_16b_bf16 v[80:83], a[244:245], a[242:243], 0\n"
      "ds_read_b64_tr_b16 a[232:233], v96 offset:2368\n"
      "v_mfma_f32_4x4x4_16b_bf16 v[76:79], a[248:249], a[92:93], v[76:79]\n"
      "ds_read_b64_tr_b16 a[234:235], v96 offset:3136\n"
      "v_mfma_f32_4x4x4_16b_bf16 v[80:83], a[248:249], a[94:95], v[80:83]\n"
      "ds_read_b64_tr_b16 a[100:101], v96 offset:14656\n"
      "s_nop 0\n"
      "s_waitcnt lgkmcnt(3)\n"
      "v_mfma_f32_4x4x4_16b_bf16 v[84:87], a[246:247], a[236:237], v[76:79]\n"
      "ds_read_b64_tr_b16 a[102:103], v96 offset:15424\n"
      "v_mfma_f32_4x4x4_16b_bf16 v[88:91], a[246:247], a[238:239], v[80:83]\n"
      "ds_read_b64_tr_b16 a[228:229], v96 offset:8512\n"
      "v_mfma_f32_4x4x4_16b_bf16 v[76:79], a[250:251], a[236:237], v[76:79]\n"
      "ds_read_b64_tr_b16 a[230:231], v96 offset:9280\n"
      "v_mfma_f32_4x4x4_16b_bf16 v[80:83], a[250:251], a[238:239], v[80:83]\n"
      "ds_read_b128 a[208:211], v2 offset:64\n"
      "ds_read_b128 a[212:215], v2 offset:4160\n"
      "v_cvt_pk_bf16_f32 v36, v36, v40\n"
      "v_cvt_pk_bf16_f32 v37, v37, v41\n"
      "v_cvt_pk_bf16_f32 v38, v38, v42\n"
      "v_cvt_pk_bf16_f32 v39, v39, v43\n"
      "v_accvgpr_write_b32 a144, v36\n"
      "v_accvgpr_write_b32 a145, v37\n"
      "v_accvgpr_write_b32 a146, v38\n"
      "v_accvgpr_write_b32 a147, v39\n"
      "v_cvt_pk_bf16_f32 v28, v28, v32\n"
      "v_cvt_pk_bf16_f32 v29, v29, v33\n"
      "v_cvt_pk_bf16_f32 v30, v30, v34\n"
      "v_cvt_pk_bf16_f32 v31, v31, v35\n"
      "v_accvgpr_write_b32 a140, v28\n"
      "v_accvgpr_write_b32 a141, v29\n"
      "v_accvgpr_write_b32 a142, v30\n"
      "v_accvgpr_write_b32 a143, v31\n"
      "v_mfma_f32_4x4x4_16b_bf16 v[28:31], a[244:245], a[224:225], 0\n"
      "v_mfma_f32_4x4x4_16b_bf16 v[32:35], a[244:245], a[226:227], 0\n"
      "ds_read_b64_tr_b16 a[240:241], v96 offset:2432\n"
      "v_mfma_f32_4x4x4_16b_bf16 v[28:31], a[248:249], a[96:97], v[28:31]\n"
      "ds_read_b64_tr_b16 a[242:243], v96 offset:3200\n"
      "v_mfma_f32_4x4x4_16b_bf16 v[32:35], a[248:249], a[98:99], v[32:35]\n"
      "ds_read_b64_tr_b16 a[104:105], v96 offset:14720\n"
      "v_mfma_f32_4x4x4_16b_bf16 v[36:39], a[246:247], a[220:221], v[28:31]\n"
      "ds_read_b64_tr_b16 a[106:107], v96 offset:15488\n"
      "v_mfma_f32_4x4x4_16b_bf16 v[40:43], a[246:247], a[222:223], v[32:35]\n"
      "ds_read_b64_tr_b16 a[236:237], v96 offset:8576\n"
      "v_mfma_f32_4x4x4_16b_bf16 v[28:31], a[250:251], a[220:221], v[28:31]\n"
      "ds_read_b64_tr_b16 a[238:239], v96 offset:9344\n"
      "v_mfma_f32_4x4x4_16b_bf16 v[32:35], a[250:251], a[222:223], v[32:35]\n"
      "v_cvt_pk_bf16_f32 v84, v84, v88\n"
      "v_cvt_pk_bf16_f32 v85, v85, v89\n"
      "v_cvt_pk_bf16_f32 v86, v86, v90\n"
      "v_cvt_pk_bf16_f32 v87, v87, v91\n"
      "v_accvgpr_write_b32 a136, v84\n"
      "v_accvgpr_write_b32 a137, v85\n"
      "v_accvgpr_write_b32 a138, v86\n"
      "v_accvgpr_write_b32 a139, v87\n"
      "v_cvt_pk_bf16_f32 v76, v76, v80\n"
      "v_cvt_pk_bf16_f32 v77, v77, v81\n"
      "v_mfma_f32_16x16x32_bf16 v[100:103], a[84:87], a[204:207], v[100:103]\n"
      "s_nop 0\n"
      "v_cvt_pk_bf16_f32 v78, v78, v82\n"
      "v_cvt_pk_bf16_f32 v79, v79, v83\n"
      "v_mfma_f32_16x16x32_bf16 v[104:107], a[88:91], a[204:207], v[104:107]\n"
      "s_nop 0\n"
      "v_accvgpr_write_b32 a132, v76\n"
      "v_accvgpr_write_b32 a133, v77\n"
      "v_mfma_f32_16x16x32_bf16 v[108:111], a[92:95], a[204:207], v[108:111]\n"
      "s_waitcnt lgkmcnt(0)\n"
      "v_accvgpr_write_b32 a134, v78\n"
      "v_accvgpr_write_b32 a135, v79\n"
      "v_mfma_f32_4x4x4_16b_bf16 v[76:79], a[244:245], a[232:233], 0\n"
      "v_mfma_f32_4x4x4_16b_bf16 v[80:83], a[244:245], a[234:235], 0\n"
      "ds_read_b128 a[192:195], v5 offset:128\n"
      "v_mfma_f32_4x4x4_16b_bf16 v[76:79], a[248:249], a[100:101], v[76:79]\n"
      "v_mfma_f32_4x4x4_16b_bf16 v[80:83], a[248:249], a[102:103], v[80:83]\n"
      "s_nop 0\n"
      "v_mfma_f32_4x4x4_16b_bf16 v[84:87], a[246:247], a[228:229], v[76:79]\n"
      "v_mfma_f32_4x4x4_16b_bf16 v[88:91], a[246:247], a[230:231], v[80:83]\n"
      "v_mfma_f32_4x4x4_16b_bf16 v[76:79], a[250:251], a[228:229], v[76:79]\n"
      "v_mfma_f32_4x4x4_16b_bf16 v[80:83], a[250:251], a[230:231], v[80:83]\n"
      "v_cvt_pk_bf16_f32 v36, v36, v40\n"
      "v_cvt_pk_bf16_f32 v37, v37, v41\n"
      "v_cvt_pk_bf16_f32 v38, v38, v42\n"
      "v_cvt_pk_bf16_f32 v39, v39, v43\n"
      "v_mfma_f32_16x16x32_bf16 v[52:55], a[152:155], a[208:211], v[52:55]\n"
      "v_accvgpr_write_b32 a128, v36\n"
      "v_accvgpr_write_b32 a129, v37\n"
      "v_mfma_f32_16x16x32_bf16 v[56:59], a[144:147], a[208:211], v[56:59]\n"
      "v_accvgpr_write_b32 a130, v38\n"
      "v_accvgpr_write_b32 a131, v39\n"
      "v_mfma_f32_16x16x32_bf16 v[60:63], a[136:139], a[208:211], v[60:63]\n"
      "v_cvt_pk_bf16_f32 v28, v28, v32\n"
      "v_cvt_pk_bf16_f32 v29, v29, v33\n"
      "v_mfma_f32_16x16x32_bf16 v[64:67], a[148:151], a[212:215], v[64:67]\n"
      "v_cvt_pk_bf16_f32 v30, v30, v34\n"
      "v_cvt_pk_bf16_f32 v31, v31, v35\n"
      "v_mfma_f32_16x16x32_bf16 v[68:71], a[140:143], a[212:215], v[68:71]\n"
      "v_accvgpr_write_b32 a124, v28\n"
      "v_accvgpr_write_b32 a125, v29\n"
      "v_mfma_f32_16x16x32_bf16 v[72:75], a[132:135], a[212:215], v[72:75]\n"
      "v_accvgpr_write_b32 a126, v30\n"
      "v_accvgpr_write_b32 a127, v31\n"
      "v_mfma_f32_4x4x4_16b_bf16 v[28:31], a[244:245], a[240:241], 0\n"
      "v_mfma_f32_4x4x4_16b_bf16 v[32:35], a[244:245], a[242:243], 0\n"
      "ds_read_b128 a[196:199], v2 offset:128\n"
      "v_mfma_f32_4x4x4_16b_bf16 v[28:31], a[248:249], a[104:105], v[28:31]\n"
      "ds_read_b128 a[200:203], v2 offset:4224\n"
      "v_mfma_f32_4x4x4_16b_bf16 v[32:35], a[248:249], a[106:107], v[32:35]\n"
      "v_mfma_f32_4x4x4_16b_bf16 v[36:39], a[246:247], a[236:237], v[28:31]\n"
      "s_nop 0\n"
      "s_nop 0\n"
      "v_mfma_f32_4x4x4_16b_bf16 v[40:43], a[246:247], a[238:239], v[32:35]\n"
      "v_mfma_f32_4x4x4_16b_bf16 v[28:31], a[250:251], a[236:237], v[28:31]\n"
      "v_mfma_f32_4x4x4_16b_bf16 v[32:35], a[250:251], a[238:239], v[32:35]\n"
      "v_cvt_pk_bf16_f32 v84, v84, v88\n"
      "v_cvt_pk_bf16_f32 v85, v85, v89\n"
      "v_cvt_pk_bf16_f32 v86, v86, v90\n"
      "v_cvt_pk_bf16_f32 v87, v87, v91\n"
      "v_accvgpr_write_b32 a120, v84\n"
      "v_accvgpr_write_b32 a121, v85\n"
      "v_accvgpr_write_b32 a122, v86\n"
      "v_accvgpr_write_b32 a123, v87\n"
      "v_cvt_pk_bf16_f32 v76, v76, v80\n"
      "v_cvt_pk_bf16_f32 v77, v77, v81\n"
      "v_cvt_pk_bf16_f32 v78, v78, v82\n"
      "v_cvt_pk_bf16_f32 v79, v79, v83\n"
      "v_accvgpr_write_b32 a116, v76\n"
      "v_accvgpr_write_b32 a117, v77\n"
      "v_accvgpr_write_b32 a118, v78\n"
      "v_accvgpr_write_b32 a119, v79\n"
      "s_waitcnt lgkmcnt(0)\n"
      "s_barrier\n"
      "s_mov_b32 m0, s64\n"
      "s_nop 0\n"
      "v_mfma_f32_16x16x32_bf16 v[100:103], a[96:99], a[192:195], v[100:103]\n"
      "ds_read_b128 a[204:207], v3 offset:16896\n"
      "ds_read_b64_tr_b16 a[220:221], v7 offset:16896\n"
      "ds_read_b64_tr_b16 a[222:223], v7 offset:16904\n"
      "v_cvt_pk_bf16_f32 v36, v36, v40\n"
      "v_cvt_pk_bf16_f32 v37, v37, v41\n"
      "v_mfma_f32_16x16x32_bf16 v[104:107], a[100:103], a[192:195], v[104:107]\n"
      "ds_read_b128 a[208:211], v3 offset:16960\n"
      "v_cvt_pk_bf16_f32 v38, v38, v42\n"
      "v_cvt_pk_bf16_f32 v39, v39, v43\n"
      "v_accvgpr_write_b32 a112, v36\n"
      "v_mfma_f32_16x16x32_bf16 v[108:111], a[104:107], a[192:195], v[108:111]\n"
      "v_accvgpr_write_b32 a113, v37\n"
      "v_accvgpr_write_b32 a114, v38\n"
      "v_accvgpr_write_b32 a115, v39\n"
      "v_cvt_pk_bf16_f32 v28, v28, v32\n"
      "v_cvt_pk_bf16_f32 v29, v29, v33\n"
      "v_cvt_pk_bf16_f32 v30, v30, v34\n"
      "v_mfma_f32_16x16x32_bf16 v[52:55], a[128:131], a[196:199], v[52:55]\n"
      "ds_read_b128 a[212:215], v3 offset:17024\n"
      "v_cvt_pk_bf16_f32 v31, v31, v35\n"
      "v_accvgpr_write_b32 a108, v28\n"
      "v_accvgpr_write_b32 a109, v29\n"
      "v_accvgpr_write_b32 a110, v30\n"
      "v_accvgpr_write_b32 a111, v31\n"
      "v_mfma_f32_16x16x32_bf16 v[56:59], a[120:123], a[196:199], v[56:59]\n"
      "v_mfma_f32_16x16x32_bf16 v[60:63], a[112:115], a[196:199], v[60:63]\n"
      "v_mfma_f32_16x16x32_bf16 v[64:67], a[124:127], a[200:203], v[64:67]\n"
      "ds_read_b128 a[192:195], v4 offset:16896\n"
      "ds_read_b64_tr_b16 a[224:225], v7 offset:17408\n"
      "v_mfma_f32_16x16x32_bf16 v[68:71], a[116:119], a[200:203], v[68:71]\n"
      "ds_read_b64_tr_b16 a[226:227], v7 offset:17416\n"
      "v_mfma_f32_16x16x32_bf16 v[72:75], a[108:111], a[200:203], v[72:75]\n"
      "s_waitcnt lgkmcnt(3)\n"
      "v_mfma_f32_4x4x4_16b_bf16 v[20:23], a[216:217], a[220:221], 0\n"
      "v_mfma_f32_4x4x4_16b_bf16 v[24:27], a[216:217], a[222:223], 0\n"
      "v_sub_f32 v52, v64, v52\n"
      "v_sub_f32 v53, v65, v53\n"
      "v_sub_f32 v54, v66, v54\n"
      "v_sub_f32 v55, v67, v55\n"
      "v_sub_f32 v56, v68, v56\n"
      "v_mfma_f32_16x16x32_bf16 v[112:115], a[0:3], a[204:207], 0\n"
      "ds_read_b128 a[196:199], v4 offset:16960\n"
      "ds_write_addtid_b32 v52 offset:0\n"
      "v_mfma_f32_16x16x32_bf16 v[116:119], a[4:7], a[204:207], 0\n"
      "ds_write_addtid_b32 v53 offset:512\n"
      "ds_write_addtid_b32 v54 offset:1024\n"
      "v_mfma_f32_16x16x32_bf16 v[120:123], a[8:11], a[204:207], 0\n"
      "ds_write_addtid_b32 v55 offset:1536\n"
      "ds_write_addtid_b32 v56 offset:2048\n"
      "v_mfma_f32_16x16x32_bf16 v[112:115], a[12:15], a[208:211], v[112:115]\n"
      "ds_read_b128 a[200:203], v4 offset:17024\n"
      "v_cvt_pk_bf16_f32 v20, v20, v24\n"
      "v_mfma_f32_16x16x32_bf16 v[116:119], a[16:19], a[208:211], v[116:119]\n"
      "ds_write_addtid_b32 v20 offset:41216\n"
      "v_add_f32 v64, v64, v100\n"
      "v_mfma_f32_16x16x32_bf16 v[120:123], a[20:23], a[208:211], v[120:123]\n"
      "v_cvt_pk_bf16_f32 v21, v21, v25\n"
      "v_add_f32 v65, v65, v101\n"
      "v_mfma_f32_16x16x32_bf16 v[112:115], a[24:27], a[212:215], v[112:115]\n"
      "ds_read_b128 a[204:207], v5 offset:16896\n"
      "ds_write_addtid_b32 v21 offset:45312\n"
      "v_mfma_f32_16x16x32_bf16 v[116:119], a[28:31], a[212:215], v[116:119]\n"
      "s_nop 0\n"
      "ds_read_b64_tr_b16 a[228:229], v7 offset:17920\n"
      "ds_read_b64_tr_b16 a[230:231], v7 offset:17928\n"
      "v_mfma_f32_16x16x32_bf16 v[120:123], a[32:35], a[212:215], v[120:123]\n"
      "s_waitcnt lgkmcnt(5)\n"
      "v_cvt_pk_bf16_f32 v22, v22, v26\n"
      "v_cvt_pk_bf16_f32 v23, v23, v27\n"
      "v_mfma_f32_4x4x4_16b_bf16 v[28:31], a[216:217], a[224:225], 0\n"
      "v_sub_f32 v57, v69, v57\n"
      "v_mfma_f32_4x4x4_16b_bf16 v[32:35], a[216:217], a[226:227], 0\n"
      "ds_write_addtid_b32 v22 offset:49408\n"
      "ds_write_addtid_b32 v23 offset:53504\n"
      "v_mfma_f32_16x16x32_bf16 v[112:115], a[36:39], a[192:195], v[112:115]\n"
      "s_nop 0\n"
      "ds_read_b128 a[208:211], v5 offset:16960\n"
      "ds_write_addtid_b32 v57 offset:2560\n"
      "v_mfma_f32_16x16x32_bf16 v[116:119], a[40:43], a[192:195], v[116:119]\n"
      "v_add_f32 v66, v66, v102\n"
      "v_cvt_pk_bf16_f32 v28, v28, v32\n"
      "v_mfma_f32_16x16x32_bf16 v[120:123], a[44:47], a[192:195], v[120:123]\n"
      "ds_write_addtid_b32 v28 offset:41728\n"
      "v_add_f32 v67, v67, v103\n"
      "v_mfma_f32_16x16x32_bf16 v[112:115], a[48:51], a[196:199], v[112:115]\n"
      "ds_read_b128 a[212:215], v5 offset:17024\n"
      "v_sub_f32_e64 v58, v70, v58\n"
      "v_mfma_f32_16x16x32_bf16 v[116:119], a[52:55], a[196:199], v[116:119]\n"
      "ds_write_addtid_b32 v58 offset:3072\n"
      "v_cvt_pk_bf16_f32 v29, v29, v33\n"
      "v_mfma_f32_16x16x32_bf16 v[120:123], a[56:59], a[196:199], v[120:123]\n"
      "ds_write_addtid_b32 v29 offset:45824\n"
      "v_cvt_pk_bf16_f32 v30, v30, v34\n"
      "v_mfma_f32_16x16x32_bf16 v[112:115], a[60:63], a[200:203], v[112:115]\n"
      "ds_read_b128 a[192:195], v3 offset:33792\n"
      "ds_read_b64_tr_b16 a[232:233], v7 offset:18432\n"
      "v_mfma_f32_16x16x32_bf16 v[116:119], a[64:67], a[200:203], v[116:119]\n"
      "ds_write_addtid_b32 v30 offset:49920\n"
      "ds_read_b64_tr_b16 a[234:235], v7 offset:18440\n"
      "v_mfma_f32_16x16x32_bf16 v[120:123], a[68:71], a[200:203], v[120:123]\n"
      "s_waitcnt lgkmcnt(6)\n"
      "v_cvt_pk_bf16_f32 v31, v31, v35\n"
      "v_add_f32 v68, v68, v104\n"
      "v_mfma_f32_4x4x4_16b_bf16 v[20:23], a[216:217], a[228:229], 0\n"
      "v_add_f32 v69, v69, v105\n"
      "v_mfma_f32_4x4x4_16b_bf16 v[24:27], a[216:217], a[230:231], 0\n"
      "ds_write_addtid_b32 v31 offset:54016\n"
      "v_sub_f32 v59, v71, v59\n"
      "v_mfma_f32_16x16x32_bf16 v[112:115], a[72:75], a[204:207], v[112:115]\n"
      "ds_read_b128 a[196:199], v3 offset:33856\n"
      "ds_write_addtid_b32 v59 offset:3584\n"
      "v_mfma_f32_16x16x32_bf16 v[116:119], a[76:79], a[204:207], v[116:119]\n"
      "v_cvt_pk_bf16_f32 v20, v20, v24\n"
      "v_add_f32 v70, v70, v106\n"
      "v_mfma_f32_16x16x32_bf16 v[120:123], a[80:83], a[204:207], v[120:123]\n"
      "ds_write_addtid_b32 v20 offset:42240\n"
      "v_cvt_pk_bf16_f32 v21, v21, v25\n"
      "v_mfma_f32_16x16x32_bf16 v[112:115], a[84:87], a[208:211], v[112:115]\n"
      "ds_read_b128 a[200:203], v3 offset:33920\n"
      "ds_write_addtid_b32 v21 offset:46336\n"
      "v_mfma_f32_16x16x32_bf16 v[116:119], a[88:91], a[208:211], v[116:119]\n"
      "v_sub_f32 v60, v72, v60\n"
      "v_add_f32 v71, v71, v107\n"
      "v_mfma_f32_16x16x32_bf16 v[120:123], a[92:95], a[208:211], v[120:123]\n"
      "ds_write_addtid_b32 v60 offset:4096\n"
      "v_cvt_pk_bf16_f32 v22, v22, v26\n"
      "v_mfma_f32_16x16x32_bf16 v[112:115], a[96:99], a[212:215], v[112:115]\n"
      "ds_read_b128 a[204:207], v4 offset:33792\n"
      "ds_write_addtid_b32 v22 offset:50432\n"
      "v_mfma_f32_16x16x32_bf16 v[116:119], a[100:103], a[212:215], v[116:119]\n"
      "s_nop 0\n"
      "v_cvt_pk_bf16_f32 v23, v23, v27\n"
      "v_add_f32 v72, v72, v108\n"
      "v_mfma_f32_16x16x32_bf16 v[120:123], a[104:107], a[212:215], v[120:123]\n"
      "s_waitcnt lgkmcnt(3)\n"
      "ds_write_addtid_b32 v23 offset:54528\n"
      "v_sub_f32 v61, v73, v61\n"
      "v_mfma_f32_4x4x4_16b_bf16 v[28:31], a[216:217], a[232:233], 0\n"
      "v_add_f32 v73, v73, v109\n"
      "v_mfma_f32_4x4x4_16b_bf16 v[32:35], a[216:217], a[234:235], 0\n"
      "ds_write_addtid_b32 v61 offset:4608\n"
      "v_sub_f32 v62, v74, v62\n"
      "v_mfma_f32_16x16x32_bf16 v[76:79], a[0:3], a[192:195], 0\n"
      "ds_read_b128 a[208:211], v4 offset:33856\n"
      "ds_write_addtid_b32 v62 offset:5120\n"
      "v_mfma_f32_16x16x32_bf16 v[80:83], a[4:7], a[192:195], 0\n"
      "v_cvt_pk_bf16_f32 v28, v28, v32\n"
      "v_cvt_pk_bf16_f32 v29, v29, v33\n"
      "v_mfma_f32_16x16x32_bf16 v[84:87], a[8:11], a[192:195], 0\n"
      "s_nop 0\n"
      "ds_write_addtid_b32 v28 offset:42752\n"
      "ds_write_addtid_b32 v29 offset:46848\n"
      "v_mfma_f32_16x16x32_bf16 v[76:79], a[12:15], a[196:199], v[76:79]\n"
      "ds_read_b128 a[212:215], v4 offset:33920\n"
      "v_add_f32 v74, v74, v110\n"
      "v_mfma_f32_16x16x32_bf16 v[80:83], a[16:19], a[196:199], v[80:83]\n"
      "v_cvt_pk_bf16_f32 v30, v30, v34\n"
      "v_sub_f32 v63, v75, v63\n"
      "v_mfma_f32_16x16x32_bf16 v[84:87], a[20:23], a[196:199], v[84:87]\n"
      "ds_write_addtid_b32 v30 offset:50944\n"
      "ds_write_addtid_b32 v63 offset:5632\n"
      "v_mfma_f32_16x16x32_bf16 v[76:79], a[24:27], a[200:203], v[76:79]\n"
      "ds_read_b128 a[192:195], v5 offset:33792\n"
      "v_add_f32 v75, v75, v111\n"
      "v_mfma_f32_16x16x32_bf16 v[80:83], a[28:31], a[200:203], v[80:83]\n"
      "v_cvt_pk_bf16_f32 v31, v31, v35\n"
      "v_mfma_f32_16x16x32_bf16 v[84:87], a[32:35], a[200:203], v[84:87]\n"
      "s_waitcnt lgkmcnt(3)\n"
      "ds_write_addtid_b32 v31 offset:55040\n"
      "v_mfma_f32_16x16x32_bf16 v[76:79], a[36:39], a[204:207], v[76:79]\n"
      "ds_read_b128 a[196:199], v5 offset:33856\n"
      "v_mfma_f32_16x16x32_bf16 v[80:83], a[40:43], a[204:207], v[80:83]\n"
      "v_fma_f32 v40, 0.5, v52, -v64\n"
      "v_fma_f32 v41, 0.5, v53, -v65\n"
      "v_mfma_f32_16x16x32_bf16 v[84:87], a[44:47], a[204:207], v[84:87]\n"
      "v_fma_f32 v42, 0.5, v54, -v66\n"
      "v_fma_f32 v43, 0.5, v55, -v67\n"
      "v_mfma_f32_16x16x32_bf16 v[76:79], a[48:51], a[208:211], v[76:79]\n"
      "ds_read_b128 a[200:203], v5 offset:33920\n"
      "v_fma_f32 v44, 0.5, v56, -v68\n"
      "v_mfma_f32_16x16x32_bf16 v[80:83], a[52:55], a[208:211], v[80:83]\n"
      "v_fma_f32 v45, 0.5, v57, -v69\n"
      "v_mfma_f32_16x16x32_bf16 v[84:87], a[56:59], a[208:211], v[84:87]\n"
      "v_mfma_f32_16x16x32_bf16 v[76:79], a[60:63], a[212:215], v[76:79]\n"
      "v_mfma_f32_16x16x32_bf16 v[80:83], a[64:67], a[212:215], v[80:83]\n"
      "v_mfma_f32_16x16x32_bf16 v[84:87], a[68:71], a[212:215], v[84:87]\n"
      "s_waitcnt lgkmcnt(0)\n"
      "s_barrier\n"
      ".p2align 6\n"
      "LSloop_%=:\n"
      "v_mfma_f32_16x16x32_bf16 v[76:79], a[72:75], a[192:195], v[76:79]\n"
      "ds_read_b128 a[204:207], v0 offset:16384\n"
      "ds_read_b64_tr_b16 a[220:221], v7 offset:33792\n"
      "v_fma_f32 v46, 0.5, v58, -v70\n"
      "v_mfma_f32_16x16x32_bf16 v[80:83], a[76:79], a[192:195], v[80:83]\n"
      "ds_read_b128 a[208:211], v0 offset:20480\n"
      "ds_read2st64_b32 v[28:29], v6 offset0:0 offset1:2\n"
      "v_mfma_f32_16x16x32_bf16 v[84:87], a[80:83], a[192:195], v[84:87]\n"
      "ds_read_b64_tr_b16 a[222:223], v7 offset:33800\n"
      "ds_read2st64_b32 v[30:31], v6 offset0:4 offset1:6\n"
      "v_mfma_f32_16x16x32_bf16 v[76:79], a[84:87], a[196:199], v[76:79]\n"
      "ds_read_b128 a[212:215], v0 offset:16448\n"
      "v_fma_f32 v47, 0.5, v59, -v71\n"
      "v_mfma_f32_16x16x32_bf16 v[80:83], a[88:91], a[196:199], v[80:83]\n"
      "ds_read2st64_b32 v[32:33], v6 offset0:8 offset1:10\n"
      "v_fma_f32 v48, 0.5, v60, -v72\n"
      "v_fma_f32 v49, 0.5, v61, -v73\n"
      "v_mfma_f32_16x16x32_bf16 v[84:87], a[92:95], a[196:199], v[84:87]\n"
      "ds_read2st64_b32 v[34:35], v6 offset0:12 offset1:14\n"
      "v_fma_f32 v50, 0.5, v62, -v74\n"
      "v_mfma_f32_16x16x32_bf16 v[76:79], a[96:99], a[200:203], v[76:79]\n"
      "ds_read_b128 a[192:195], v0 offset:20544\n"
      "v_fma_f32 v51, 0.5, v63, -v75\n"
      "v_mfma_f32_16x16x32_bf16 v[80:83], a[100:103], a[200:203], v[80:83]\n"
      "ds_read2st64_b32 v[36:37], v6 offset0:16 offset1:18\n"
      "ds_read2st64_b32 v[38:39], v6 offset0:20 offset1:22\n"
      "v_mfma_f32_16x16x32_bf16 v[84:87], a[104:107], a[200:203], v[84:87]\n"
      "s_waitcnt lgkmcnt(8)\n"
      "v_mfma_f32_16x16x32_bf16 v[52:55], v[252:255], a[204:207], v[112:115]\n"
      "s_waitcnt lgkmcnt(6)\n"
      "ds_read_b128 a[196:199], v0 offset:16512\n"
      "v_sub_f32 v28, v28, v40\n"
      "v_sub_f32 v29, v29, v41\n"
      "v_sub_f32 v30, v30, v42\n"
      "v_sub_f32 v31, v31, v43\n"
      "v_mfma_f32_4x4x4_16b_bf16 v[12:15], a[216:217], a[220:221], 0\n"
      "v_fmac_f32 v28, s46, v100\n"
      "v_mfma_f32_4x4x4_16b_bf16 v[16:19], a[216:217], a[222:223], 0\n"
      "ds_read_b64_tr_b16 a[224:225], v7 offset:34304\n"
      "ds_read_b64_tr_b16 a[226:227], v7 offset:34312\n"
      "v_mfma_f32_16x16x32_bf16 v[56:59], v[244:247], a[204:207], v[116:119]\n"
      "v_fmac_f32 v29, s46, v101\n"
      "v_fmac_f32 v30, s46, v102\n"
      "v_mfma_f32_16x16x32_bf16 v[60:63], v[236:239], a[204:207], v[120:123]\n"
      "v_fmac_f32 v31, s46, v103\n"
      "v_fmac_f32 v40, s45, v28\n"
      "v_cvt_pk_bf16_f32 v12, v12, v16\n"
      "v_mfma_f32_16x16x32_bf16 v[64:67], v[248:251], a[208:211], v[112:115]\n"
      "ds_read_b128 a[200:203], v0 offset:20608\n"
      "ds_write_addtid_b32 v12 offset:24832\n"
      "v_mfma_f32_16x16x32_bf16 v[68:71], v[240:243], a[208:211], v[116:119]\n"
      "v_fmac_f32 v41, s45, v29\n"
      "v_cvt_pk_bf16_f32 v13, v13, v17\n"
      "v_mfma_f32_16x16x32_bf16 v[72:75], v[232:235], a[208:211], v[120:123]\n"
      "s_waitcnt lgkmcnt(4)\n"
      "ds_write_addtid_b32 v13 offset:28928\n"
      "v_fmac_f32 v42, s45, v30\n"
      "v_mfma_f32_16x16x32_bf16 v[52:55], v[228:231], a[212:215], v[52:55]\n"
      "ds_read_b128 a[204:207], v1 offset:16384\n"
      "v_fmac_f32 v43, s45, v31\n"
      "v_cvt_pk_bf16_f32 v14, v14, v18\n"
      "v_mfma_f32_16x16x32_bf16 v[56:59], v[220:223], a[212:215], v[56:59]\n"
      "s_nop 0\n"
      "ds_write_addtid_b32 v14 offset:33024\n"
      "v_fma_f32 v28, s47, v28, v76\n"
      "v_mfma_f32_16x16x32_bf16 v[60:63], v[212:215], a[212:215], v[60:63]\n"
      "v_cvt_pk_bf16_f32 v15, v15, v19\n"
      "v_fma_f32 v29, s47, v29, v77\n"
      "v_mfma_f32_16x16x32_bf16 v[64:67], v[224:227], a[192:195], v[64:67]\n"
      "ds_read_b128 a[208:211], v1 offset:20480\n"
      "ds_write_addtid_b32 v15 offset:37120\n"
      "v_mfma_f32_16x16x32_bf16 v[68:71], v[216:219], a[192:195], v[68:71]\n"
      "s_nop 0\n"
      "v_fma_f32 v30, s47, v30, v78\n"
      "v_fma_f32 v31, s47, v31, v79\n"
      "v_cvt_pk_bf16_f32 v40, v40, v41\n"
      "v_mfma_f32_16x16x32_bf16 v[72:75], v[208:211], a[192:195], v[72:75]\n"
      "s_waitcnt lgkmcnt(2)\n"
      "ds_write_addtid_b32 v40 offset:12288\n"
      "ds_read_b64_tr_b16 a[228:229], v7 offset:34816\n"
      "v_mfma_f32_4x4x4_16b_bf16 v[20:23], a[216:217], a[224:225], 0\n"
      "ds_read_b64_tr_b16 a[230:231], v7 offset:34824\n"
      "v_mfma_f32_4x4x4_16b_bf16 v[24:27], a[216:217], a[226:227], 0\n"
      "v_cvt_pk_bf16_f32 v42, v42, v43\n"
      "v_cvt_pk_bf16_f32 v28, v28, v29\n"
      "v_mfma_f32_16x16x32_bf16 v[52:55], v[204:207], a[196:199], v[52:55]\n"
      "ds_read_b128 a[212:215], v1 offset:16448\n"
      "ds_write_addtid_b32 v42 offset:12800\n"
      "v_mfma_f32_16x16x32_bf16 v[56:59], v[196:199], a[196:199], v[56:59]\n"
      "ds_write_addtid_b32 v28 offset:14336\n"
      "v_cvt_pk_bf16_f32 v20, v20, v24\n"
      "v_cvt_pk_bf16_f32 v30, v30, v31\n"
      "v_mfma_f32_16x16x32_bf16 v[60:63], v[188:191], a[196:199], v[60:63]\n"
      "ds_write_addtid_b32 v30 offset:14848\n"
      "ds_write_addtid_b32 v20 offset:25344\n"
      "v_mfma_f32_16x16x32_bf16 v[64:67], v[200:203], a[200:203], v[64:67]\n"
      "ds_read_b128 a[192:195], v1 offset:20544\n"
      "v_sub_f32 v32, v32, v44\n"
      "v_mfma_f32_16x16x32_bf16 v[68:71], v[192:195], a[200:203], v[68:71]\n"
      "v_cvt_pk_bf16_f32 v21, v21, v25\n"
      "v_sub_f32 v33, v33, v45\n"
      "v_mfma_f32_16x16x32_bf16 v[72:75], v[184:187], a[200:203], v[72:75]\n"
      "ds_write_addtid_b32 v21 offset:29440\n"
      "v_sub_f32 v34, v34, v46\n"
      "v_cvt_pk_bf16_f32 v22, v22, v26\n"
      "v_mfma_f32_16x16x32_bf16 v[52:55], v[180:183], a[204:207], v[52:55]\n"
      "s_nop 0\n"
      "ds_read_b128 a[196:199], v1 offset:16512\n"
      "ds_write_addtid_b32 v22 offset:33536\n"
      "v_mfma_f32_16x16x32_bf16 v[56:59], v[172:175], a[204:207], v[56:59]\n"
      "v_sub_f32 v35, v35, v47\n"
      "v_fmac_f32 v32, s46, v104\n"
      "v_mfma_f32_16x16x32_bf16 v[60:63], v[164:167], a[204:207], v[60:63]\n"
      "s_waitcnt lgkmcnt(3)\n"
      "v_cvt_pk_bf16_f32 v23, v23, v27\n"
      "v_fmac_f32 v33, s46, v105\n"
      "v_mfma_f32_16x16x32_bf16 v[64:67], v[176:179], a[208:211], v[64:67]\n"
      "ds_read_b128 a[200:203], v1 offset:20608\n"
      "ds_write_addtid_b32 v23 offset:37632\n"
      "v_fmac_f32 v34, s46, v106\n"
      "v_mfma_f32_16x16x32_bf16 v[68:71], v[168:171], a[208:211], v[68:71]\n"
      "ds_read_b64_tr_b16 a[232:233], v7 offset:35328\n"
      "ds_read_b64_tr_b16 a[234:235], v7 offset:35336\n"
      "v_mfma_f32_4x4x4_16b_bf16 v[12:15], a[216:217], a[228:229], 0\n"
      "v_fmac_f32 v35, s46, v107\n"
      "v_mfma_f32_4x4x4_16b_bf16 v[16:19], a[216:217], a[230:231], 0\n"
      "v_fmac_f32 v44, s45, v32\n"
      "v_fmac_f32 v45, s45, v33\n"
      "v_mfma_f32_16x16x32_bf16 v[72:75], v[160:163], a[208:211], v[72:75]\n"
      "v_fmac_f32 v46, s45, v34\n"
      "v_fmac_f32 v47, s45, v35\n"
      "v_cvt_pk_bf16_f32 v12, v12, v16\n"
      "v_mfma_f32_16x16x32_bf16 v[52:55], v[156:159], a[212:215], v[52:55]\n"
      "ds_read_b128 a[204:207], v2 offset:16384\n"
      "ds_write_addtid_b32 v12 offset:25856\n"
      "v_mfma_f32_16x16x32_bf16 v[56:59], v[148:151], a[212:215], v[56:59]\n"
      "v_cvt_pk_bf16_f32 v13, v13, v17\n"
      "v_fma_f32 v32, s47, v32, v80\n"
      "v_mfma_f32_16x16x32_bf16 v[60:63], v[140:143], a[212:215], v[60:63]\n"
      "ds_write_addtid_b32 v13 offset:29952\n"
      "v_fma_f32 v33, s47, v33, v81\n"
      "v_mfma_f32_16x16x32_bf16 v[64:67], v[152:155], a[192:195], v[64:67]\n"
      "ds_read_b128 a[208:211], v2 offset:20480\n"
      "v_cvt_pk_bf16_f32 v14, v14, v18\n"
      "v_mfma_f32_16x16x32_bf16 v[68:71], v[144:147], a[192:195], v[68:71]\n"
      "s_nop 0\n"
      "ds_write_addtid_b32 v14 offset:34048\n"
      "v_fma_f32 v34, s47, v34, v82\n"
      "v_fma_f32 v35, s47, v35, v83\n"
      "v_mfma_f32_16x16x32_bf16 v[72:75], v[136:139], a[192:195], v[72:75]\n"
      "s_waitcnt lgkmcnt(3)\n"
      "v_cvt_pk_bf16_f32 v15, v15, v19\n"
      "v_cvt_pk_bf16_f32 v44, v44, v45\n"
      "v_mfma_f32_16x16x32_bf16 v[52:55], v[132:135], a[196:199], v[52:55]\n"
      "ds_read_b128 a[212:215], v2 offset:16448\n"
      "ds_write_addtid_b32 v44 offset:20484\n"
      "v_mfma_f32_16x16x32_bf16 v[56:59], v[124:127], a[196:199], v[56:59]\n"
      "ds_write_addtid_b32 v15 offset:38144\n"
      "v_cvt_pk_bf16_f32 v46, v46, v47\n"
      "v_mfma_f32_16x16x32_bf16 v[60:63], a[184:187], a[196:199], v[60:63]\n"
      "ds_write_addtid_b32 v46 offset:20996\n"
      "v_cvt_pk_bf16_f32 v32, v32, v33\n"
      "v_cvt_pk_bf16_f32 v34, v34, v35\n"
      "v_mfma_f32_16x16x32_bf16 v[64:67], v[128:131], a[200:203], v[64:67]\n"
      "ds_read_b128 a[192:195], v2 offset:20544\n"
      "ds_write_addtid_b32 v32 offset:22532\n"
      "v_mfma_f32_4x4x4_16b_bf16 v[20:23], a[216:217], a[232:233], 0\n"
      "ds_write_addtid_b32 v34 offset:23044\n"
      "v_mfma_f32_4x4x4_16b_bf16 v[24:27], a[216:217], a[234:235], 0\n"
      "ds_read_b64_tr_b16 a[220:221], v7 offset:35840\n"
      "ds_read_b64_tr_b16 a[222:223], v7 offset:35848\n"
      "v_mfma_f32_16x16x32_bf16 v[68:71], a[188:191], a[200:203], v[68:71]\n"
      "v_sub_f32 v36, v36, v48\n"
      "v_sub_f32 v37, v37, v49\n"
      "v_cvt_pk_bf16_f32 v20, v20, v24\n"
      "v_mfma_f32_16x16x32_bf16 v[72:75], a[180:183], a[200:203], v[72:75]\n"
      "ds_write_addtid_b32 v20 offset:26368\n"
      "v_sub_f32_e64 v38, v38, v50\n"
      "v_mfma_f32_16x16x32_bf16 v[52:55], a[176:179], a[204:207], v[52:55]\n"
      "ds_read_b128 a[196:199], v2 offset:16512\n"
      "v_sub_f32 v39, v39, v51\n"
      "v_mfma_f32_16x16x32_bf16 v[56:59], a[168:171], a[204:207], v[56:59]\n"
      "v_fmac_f32 v36, s46, v108\n"
      "v_cvt_pk_bf16_f32 v21, v21, v25\n"
      "v_mfma_f32_16x16x32_bf16 v[60:63], a[160:163], a[204:207], v[60:63]\n"
      "s_waitcnt lgkmcnt(3)\n"
      "ds_write_addtid_b32 v21 offset:30464\n"
      "v_fmac_f32 v37, s46, v109\n"
      "v_cvt_pk_bf16_f32 v22, v22, v26\n"
      "v_mfma_f32_16x16x32_bf16 v[64:67], a[172:175], a[208:211], v[64:67]\n"
      "ds_read_b128 a[200:203], v2 offset:20608\n"
      "ds_write_addtid_b32 v22 offset:34560\n"
      "v_mfma_f32_16x16x32_bf16 v[68:71], a[164:167], a[208:211], v[68:71]\n"
      "v_fmac_f32 v38, s46, v110\n"
      "v_fmac_f32 v39, s46, v111\n"
      "v_mfma_f32_16x16x32_bf16 v[72:75], a[156:159], a[208:211], v[72:75]\n"
      "v_cvt_pk_bf16_f32 v23, v23, v27\n"
      "v_fmac_f32 v48, s45, v36\n"
      "v_mfma_f32_16x16x32_bf16 v[52:55], a[152:155], a[212:215], v[52:55]\n"
      "ds_read_b128 a[204:207], v3 offset:50688\n"
      "ds_write_addtid_b32 v23 offset:38656\n"
      "v_mfma_f32_16x16x32_bf16 v[56:59], a[144:147], a[212:215], v[56:59]\n"
      "v_fmac_f32 v49, s45, v37\n"
      "v_fmac_f32 v50, s45, v38\n"
      "v_fmac_f32_e64 v51, s45, v39\n"
      "v_mfma_f32_16x16x32_bf16 v[60:63], a[136:139], a[212:215], v[60:63]\n"
      "s_waitcnt lgkmcnt(2)\n"
      "ds_read_b64_tr_b16 a[224:225], v7 offset:36352\n"
      "ds_read_b64_tr_b16 a[226:227], v7 offset:36360\n"
      "v_mfma_f32_4x4x4_16b_bf16 v[12:15], a[216:217], a[220:221], 0\n"
      "v_fma_f32 v36, s47, v36, v84\n"
      "v_mfma_f32_4x4x4_16b_bf16 v[16:19], a[216:217], a[222:223], 0\n"
      "v_fma_f32 v37, s47, v37, v85\n"
      "v_fma_f32 v38, s47, v38, v86\n"
      "v_mfma_f32_16x16x32_bf16 v[64:67], a[148:151], a[192:195], v[64:67]\n"
      "ds_read_b128 a[208:211], v3 offset:50752\n"
      "v_fma_f32 v39, s47, v39, v87\n"
      "v_cvt_pk_bf16_f32 v12, v12, v16\n"
      "v_mfma_f32_16x16x32_bf16 v[68:71], a[140:143], a[192:195], v[68:71]\n"
      "ds_write_addtid_b32 v12 offset:26880\n"
      "v_cvt_pk_bf16_f32 v13, v13, v17\n"
      "v_mfma_f32_16x16x32_bf16 v[72:75], a[132:135], a[192:195], v[72:75]\n"
      "ds_write_addtid_b32 v13 offset:30976\n"
      "v_cvt_pk_bf16_f32 v48, v48, v49\n"
      "v_mfma_f32_16x16x32_bf16 v[52:55], a[128:131], a[196:199], v[52:55]\n"
      "ds_read_b128 a[212:215], v3 offset:50816\n"
      "ds_write_addtid_b32 v48 offset:16384\n"
      "v_mfma_f32_16x16x32_bf16 v[56:59], a[120:123], a[196:199], v[56:59]\n"
      "v_cvt_pk_bf16_f32 v14, v14, v18\n"
      "v_cvt_pk_bf16_f32 v50, v50, v51\n"
      "v_cvt_pk_bf16_f32 v15, v15, v19\n"
      "v_mfma_f32_16x16x32_bf16 v[60:63], a[112:115], a[196:199], v[60:63]\n"
      "ds_write_addtid_b32 v50 offset:16896\n"
      "ds_write_addtid_b32 v14 offset:35072\n"
      "v_mfma_f32_16x16x32_bf16 v[64:67], a[124:127], a[200:203], v[64:67]\n"
      "ds_read_b128 a[192:195], v4 offset:50688\n"
      "ds_write_addtid_b32 v15 offset:39168\n"
      "v_mfma_f32_16x16x32_bf16 v[68:71], a[116:119], a[200:203], v[68:71]\n"
      "v_cvt_pk_bf16_f32 v36, v36, v37\n"
      "v_cvt_pk_bf16_f32 v38, v38, v39\n"
      "v_mfma_f32_16x16x32_bf16 v[72:75], a[108:111], a[200:203], v[72:75]\n"
      "s_waitcnt lgkmcnt(4)\n"
      "ds_write_addtid_b32 v36 offset:18432\n"
      "ds_write_addtid_b32 v38 offset:18944\n"
      "v_sub_f32 v52, v64, v52\n"
      "v_mfma_f32_16x16x32_bf16 v[88:91], a[0:3], a[204:207], 0\n"
      "ds_read_b128 a[196:199], v4 offset:50752\n"
      "ds_write_addtid_b32 v52 offset:6144\n"
      "v_mfma_f32_16x16x32_bf16 v[92:95], a[4:7], a[204:207], 0\n"
      "v_sub_f32 v53, v65, v53\n"
      "v_sub_f32 v54, v66, v54\n"
      "v_mfma_f32_4x4x4_16b_bf16 v[20:23], a[216:217], a[224:225], 0\n"
      "v_sub_f32 v55, v67, v55\n"
      "v_mfma_f32_4x4x4_16b_bf16 v[24:27], a[216:217], a[226:227], 0\n"
      "ds_write_addtid_b32 v53 offset:6656\n"
      "ds_write_addtid_b32 v54 offset:7168\n"
      "v_mfma_f32_16x16x32_bf16 v[96:99], a[8:11], a[204:207], 0\n"
      "s_nop 0\n"
      "ds_write_addtid_b32 v55 offset:7680\n"
      "v_sub_f32 v56, v68, v56\n"
      "v_sub_f32 v57, v69, v57\n"
      "v_mfma_f32_16x16x32_bf16 v[88:91], a[12:15], a[208:211], v[88:91]\n"
      "ds_read_b128 a[200:203], v4 offset:50816\n"
      "ds_write_addtid_b32 v56 offset:8192\n"
      "v_mfma_f32_16x16x32_bf16 v[92:95], a[16:19], a[208:211], v[92:95]\n"
      "ds_write_addtid_b32 v57 offset:8704\n"
      "v_cvt_pk_bf16_f32 v20, v20, v24\n"
      "v_mfma_f32_16x16x32_bf16 v[96:99], a[20:23], a[208:211], v[96:99]\n"
      "ds_write_addtid_b32 v20 offset:27392\n"
      "v_sub_f32 v58, v70, v58\n"
      "v_mfma_f32_16x16x32_bf16 v[88:91], a[24:27], a[212:215], v[88:91]\n"
      "ds_read_b128 a[204:207], v5 offset:50688\n"
      "ds_write_addtid_b32 v58 offset:9216\n"
      "v_sub_f32 v59, v71, v59\n"
      "v_mfma_f32_16x16x32_bf16 v[92:95], a[28:31], a[212:215], v[92:95]\n"
      "ds_write_addtid_b32 v59 offset:9728\n"
      "v_cvt_pk_bf16_f32 v21, v21, v25\n"
      "v_mfma_f32_16x16x32_bf16 v[96:99], a[32:35], a[212:215], v[96:99]\n"
      "s_waitcnt lgkmcnt(7)\n"
      "ds_write_addtid_b32 v21 offset:31488\n"
      "v_sub_f32 v60, v72, v60\n"
      "v_mfma_f32_16x16x32_bf16 v[88:91], a[36:39], a[192:195], v[88:91]\n"
      "ds_read_b128 a[208:211], v5 offset:50752\n"
      "ds_write_addtid_b32 v60 offset:10240\n"
      "v_mfma_f32_16x16x32_bf16 v[92:95], a[40:43], a[192:195], v[92:95]\n"
      "v_cvt_pk_bf16_f32 v22, v22, v26\n"
      "v_cvt_pk_bf16_f32 v23, v23, v27\n"
      "v_sub_f32 v61, v73, v61\n"
      "v_mfma_f32_16x16x32_bf16 v[96:99], a[44:47], a[192:195], v[96:99]\n"
      "ds_write_addtid_b32 v22 offset:35584\n"
      "ds_write_addtid_b32 v23 offset:39680\n"
      "v_sub_f32 v62, v74, v62\n"
      "v_sub_f32 v63, v75, v63\n"
      "v_mfma_f32_16x16x32_bf16 v[88:91], a[48:51], a[196:199], v[88:91]\n"
      "s_nop 0\n"
      "ds_read_b128 a[212:215], v5 offset:50816\n"
      "ds_write_addtid_b32 v61 offset:10752\n"
      "v_mfma_f32_16x16x32_bf16 v[92:95], a[52:55], a[196:199], v[92:95]\n"
      "s_nop 0\n"
      "ds_write_addtid_b32 v62 offset:11264\n"
      "ds_write_addtid_b32 v63 offset:11776\n"
      "v_mfma_f32_16x16x32_bf16 v[96:99], a[56:59], a[196:199], v[96:99]\n"
      "s_waitcnt lgkmcnt(6)\n"
      "v_fma_f32 v40, 0.5, v52, -v64\n"
      "v_fma_f32 v41, 0.5, v53, -v65\n"
      "v_mfma_f32_16x16x32_bf16 v[88:91], a[60:63], a[200:203], v[88:91]\n"
      "v_fma_f32 v42, 0.5, v54, -v66\n"
      "v_fma_f32 v43, 0.5, v55, -v67\n"
      "v_mfma_f32_16x16x32_bf16 v[92:95], a[64:67], a[200:203], v[92:95]\n"
      "v_fma_f32 v44, 0.5, v56, -v68\n"
      "v_fma_f32 v45, 0.5, v57, -v69\n"
      "v_mfma_f32_16x16x32_bf16 v[96:99], a[68:71], a[200:203], v[96:99]\n"
      "s_waitcnt lgkmcnt(0)\n"
      "s_barrier\n"
      "LSmid_%=:\n"
      "v_mfma_f32_16x16x32_bf16 v[88:91], a[72:75], a[204:207], v[88:91]\n"
      "ds_read_b128 a[192:195], v0 offset:0\n"
      "ds_read_b64_tr_b16 a[220:221], v7 offset:50688\n"
      "v_fma_f32 v46, 0.5, v58, -v70\n"
      "v_mfma_f32_16x16x32_bf16 v[92:95], a[76:79], a[204:207], v[92:95]\n"
      "ds_read_b128 a[196:199], v0 offset:4096\n"
      "ds_read2st64_b32 v[28:29], v6 offset0:24 offset1:26\n"
      "v_mfma_f32_16x16x32_bf16 v[96:99], a[80:83], a[204:207], v[96:99]\n"
      "ds_read_b64_tr_b16 a[222:223], v7 offset:50696\n"
      "ds_read2st64_b32 v[30:31], v6 offset0:28 offset1:30\n"
      "v_mfma_f32_16x16x32_bf16 v[88:91], a[84:87], a[208:211], v[88:91]\n"
      "ds_read_b128 a[200:203], v0 offset:64\n"
      "v_fma_f32 v47, 0.5, v59, -v71\n"
      "v_mfma_f32_16x16x32_bf16 v[92:95], a[88:91], a[208:211], v[92:95]\n"
      "ds_read2st64_b32 v[32:33], v6 offset0:32 offset1:34\n"
      "v_fma_f32 v48, 0.5, v60, -v72\n"
      "v_fma_f32 v49, 0.5, v61, -v73\n"
      "v_mfma_f32_16x16x32_bf16 v[96:99], a[92:95], a[208:211], v[96:99]\n"
      "ds_read2st64_b32 v[34:35], v6 offset0:36 offset1:38\n"
      "v_fma_f32 v50, 0.5, v62, -v74\n"
      "v_mfma_f32_16x16x32_bf16 v[88:91], a[96:99], a[212:215], v[88:91]\n"
      "ds_read_b128 a[204:207], v0 offset:4160\n"
      "v_fma_f32 v51, 0.5, v63, -v75\n"
      "v_mfma_f32_16x16x32_bf16 v[92:95], a[100:103], a[212:215], v[92:95]\n"
      "ds_read2st64_b32 v[36:37], v6 offset0:40 offset1:42\n"
      "ds_read2st64_b32 v[38:39], v6 offset0:44 offset1:46\n"
      "v_mfma_f32_16x16x32_bf16 v[96:99], a[104:107], a[212:215], v[96:99]\n"
      "s_waitcnt lgkmcnt(8)\n"
      "v_mfma_f32_16x16x32_bf16 v[52:55], v[252:255], a[192:195], v[76:79]\n"
      "s_waitcnt lgkmcnt(6)\n"
      "ds_read_b128 a[208:211], v0 offset:128\n"
      "v_sub_f32 v28, v28, v40\n"
      "v_sub_f32 v29, v29, v41\n"
      "v_sub_f32 v30, v30, v42\n"
      "v_sub_f32 v31, v31, v43\n"
      "v_mfma_f32_4x4x4_16b_bf16 v[12:15], a[216:217], a[220:221], 0\n"
      "v_fmac_f32 v28, s46, v112\n"
      "v_mfma_f32_4x4x4_16b_bf16 v[16:19], a[216:217], a[222:223], 0\n"
      "ds_read_b64_tr_b16 a[224:225], v7 offset:51200\n"
      "ds_read_b64_tr_b16 a[226:227], v7 offset:51208\n"
      "v_mfma_f32_16x16x32_bf16 v[56:59], v[244:247], a[192:195], v[80:83]\n"
      "v_fmac_f32 v29, s46, v113\n"
      "v_fmac_f32_e64 v30, s46, v114\n"
      "v_mfma_f32_16x16x32_bf16 v[60:63], v[236:239], a[192:195], v[84:87]\n"
      "v_fmac_f32 v31, s46, v115\n"
      "v_fmac_f32 v40, s45, v28\n"
      "v_cvt_pk_bf16_f32 v12, v12, v16\n"
      "v_mfma_f32_16x16x32_bf16 v[64:67], v[248:251], a[196:199], v[76:79]\n"
      "ds_read_b128 a[212:215], v0 offset:4224\n"
      "ds_write_addtid_b32 v12 offset:41216\n"
      "v_mfma_f32_16x16x32_bf16 v[68:71], v[240:243], a[196:199], v[80:83]\n"
      "v_fmac_f32_e64 v41, s45, v29\n"
      "v_cvt_pk_bf16_f32 v13, v13, v17\n"
      "v_mfma_f32_16x16x32_bf16 v[72:75], v[232:235], a[196:199], v[84:87]\n"
      "s_waitcnt lgkmcnt(4)\n"
      "ds_write_addtid_b32 v13 offset:45312\n"
      "v_fmac_f32 v42, s45, v30\n"
      "v_mfma_f32_16x16x32_bf16 v[52:55], v[228:231], a[200:203], v[52:55]\n"
      "ds_read_b128 a[192:195], v1 offset:0\n"
      "v_fmac_f32_e64 v43, s45, v31\n"
      "v_cvt_pk_bf16_f32 v14, v14, v18\n"
      "v_mfma_f32_16x16x32_bf16 v[56:59], v[220:223], a[200:203], v[56:59]\n"
      "ds_write_addtid_b32 v14 offset:49408\n"
      "v_fma_f32 v28, s47, v28, v88\n"
      "v_mfma_f32_16x16x32_bf16 v[60:63], v[212:215], a[200:203], v[60:63]\n"
      "v_cvt_pk_bf16_f32 v15, v15, v19\n"
      "v_fma_f32 v29, s47, v29, v89\n"
      "v_mfma_f32_16x16x32_bf16 v[64:67], v[224:227], a[204:207], v[64:67]\n"
      "ds_read_b128 a[196:199], v1 offset:4096\n"
      "ds_write_addtid_b32 v15 offset:53504\n"
      "v_mfma_f32_16x16x32_bf16 v[68:71], v[216:219], a[204:207], v[68:71]\n"
      "s_nop 0\n"
      "v_fma_f32 v30, s47, v30, v90\n"
      "v_fma_f32 v31, s47, v31, v91\n"
      "v_cvt_pk_bf16_f32 v40, v40, v41\n"
      "v_mfma_f32_16x16x32_bf16 v[72:75], v[208:211], a[204:207], v[72:75]\n"
      "s_waitcnt lgkmcnt(2)\n"
      "ds_write_addtid_b32 v40 offset:13312\n"
      "ds_read_b64_tr_b16 a[228:229], v7 offset:51712\n"
      "v_mfma_f32_4x4x4_16b_bf16 v[20:23], a[216:217], a[224:225], 0\n"
      "ds_read_b64_tr_b16 a[230:231], v7 offset:51720\n"
      "v_mfma_f32_4x4x4_16b_bf16 v[24:27], a[216:217], a[226:227], 0\n"
      "v_cvt_pk_bf16_f32 v42, v42, v43\n"
      "v_cvt_pk_bf16_f32 v28, v28, v29\n"
      "v_mfma_f32_16x16x32_bf16 v[52:55], v[204:207], a[208:211], v[52:55]\n"
      "ds_read_b128 a[200:203], v1 offset:64\n"
      "ds_write_addtid_b32 v42 offset:13824\n"
      "v_mfma_f32_16x16x32_bf16 v[56:59], v[196:199], a[208:211], v[56:59]\n"
      "ds_write_addtid_b32 v28 offset:15360\n"
      "v_cvt_pk_bf16_f32 v20, v20, v24\n"
      "v_cvt_pk_bf16_f32 v30, v30, v31\n"
      "v_mfma_f32_16x16x32_bf16 v[60:63], v[188:191], a[208:211], v[60:63]\n"
      "ds_write_addtid_b32 v30 offset:15872\n"
      "ds_write_addtid_b32 v20 offset:41728\n"
      "v_mfma_f32_16x16x32_bf16 v[64:67], v[200:203], a[212:215], v[64:67]\n"
      "ds_read_b128 a[204:207], v1 offset:4160\n"
      "v_sub_f32 v32, v32, v44\n"
      "v_mfma_f32_16x16x32_bf16 v[68:71], v[192:195], a[212:215], v[68:71]\n"
      "v_cvt_pk_bf16_f32 v21, v21, v25\n"
      "v_sub_f32 v33, v33, v45\n"
      "v_mfma_f32_16x16x32_bf16 v[72:75], v[184:187], a[212:215], v[72:75]\n"
      "ds_write_addtid_b32 v21 offset:45824\n"
      "v_sub_f32_e64 v34, v34, v46\n"
      "v_cvt_pk_bf16_f32 v22, v22, v26\n"
      "v_mfma_f32_16x16x32_bf16 v[52:55], v[180:183], a[192:195], v[52:55]\n"
      "ds_read_b128 a[208:211], v1 offset:128\n"
      "ds_write_addtid_b32 v22 offset:49920\n"
      "v_mfma_f32_16x16x32_bf16 v[56:59], v[172:175], a[192:195], v[56:59]\n"
      "v_sub_f32 v35, v35, v47\n"
      "v_fmac_f32_e64 v32, s46, v116\n"
      "v_mfma_f32_16x16x32_bf16 v[60:63], v[164:167], a[192:195], v[60:63]\n"
      "s_waitcnt lgkmcnt(3)\n"
      "v_cvt_pk_bf16_f32 v23, v23, v27\n"
      "v_fmac_f32 v33, s46, v117\n"
      "v_mfma_f32_16x16x32_bf16 v[64:67], v[176:179], a[196:199], v[64:67]\n"
      "ds_read_b128 a[212:215], v1 offset:4224\n"
      "ds_write_addtid_b32 v23 offset:54016\n"
      "v_fmac_f32 v34, s46, v118\n"
      "v_mfma_f32_16x16x32_bf16 v[68:71], v[168:171], a[196:199], v[68:71]\n"
      "ds_read_b64_tr_b16 a[232:233], v7 offset:52224\n"
      "ds_read_b64_tr_b16 a[234:235], v7 offset:52232\n"
      "v_mfma_f32_4x4x4_16b_bf16 v[12:15], a[216:217], a[228:229], 0\n"
      "v_fmac_f32 v35, s46, v119\n"
      "v_mfma_f32_4x4x4_16b_bf16 v[16:19], a[216:217], a[230:231], 0\n"
      "v_fmac_f32 v44, s45, v32\n"
      "v_fmac_f32 v45, s45, v33\n"
      "v_mfma_f32_16x16x32_bf16 v[72:75], v[160:163], a[196:199], v[72:75]\n"
      "v_fmac_f32 v46, s45, v34\n"
      "v_fmac_f32 v47, s45, v35\n"
      "v_cvt_pk_bf16_f32 v12, v12, v16\n"
      "v_mfma_f32_16x16x32_bf16 v[52:55], v[156:159], a[200:203], v[52:55]\n"
      "s_nop 0\n"
      "ds_read_b128 a[192:195], v2 offset:0\n"
      "ds_write_addtid_b32 v12 offset:42240\n"
      "v_mfma_f32_16x16x32_bf16 v[56:59], v[148:151], a[200:203], v[56:59]\n"
      "v_cvt_pk_bf16_f32 v13, v13, v17\n"
      "v_fma_f32 v32, s47, v32, v92\n"
      "v_mfma_f32_16x16x32_bf16 v[60:63], v[140:143], a[200:203], v[60:63]\n"
      "ds_write_addtid_b32 v13 offset:46336\n"
      "v_fma_f32 v33, s47, v33, v93\n"
      "v_mfma_f32_16x16x32_bf16 v[64:67], v[152:155], a[204:207], v[64:67]\n"
      "ds_read_b128 a[196:199], v2 offset:4096\n"
      "v_cvt_pk_bf16_f32 v14, v14, v18\n"
      "v_mfma_f32_16x16x32_bf16 v[68:71], v[144:147], a[204:207], v[68:71]\n"
      "ds_write_addtid_b32 v14 offset:50432\n"
      "v_fma_f32 v34, s47, v34, v94\n"
      "v_fma_f32 v35, s47, v35, v95\n"
      "v_mfma_f32_16x16x32_bf16 v[72:75], v[136:139], a[204:207], v[72:75]\n"
      "s_waitcnt lgkmcnt(3)\n"
      "v_cvt_pk_bf16_f32 v15, v15, v19\n"
      "v_cvt_pk_bf16_f32 v44, v44, v45\n"
      "v_mfma_f32_16x16x32_bf16 v[52:55], v[132:135], a[208:211], v[52:55]\n"
      "ds_read_b128 a[200:203], v2 offset:64\n"
      "ds_write_addtid_b32 v44 offset:21508\n"
      "v_mfma_f32_16x16x32_bf16 v[56:59], v[124:127], a[208:211], v[56:59]\n"
      "s_nop 0\n"
      "ds_write_addtid_b32 v15 offset:54528\n"
      "v_cvt_pk_bf16_f32 v46, v46, v47\n"
      "v_mfma_f32_16x16x32_bf16 v[60:63], a[184:187], a[208:211], v[60:63]\n"
      "ds_write_addtid_b32 v46 offset:22020\n"
      "v_cvt_pk_bf16_f32 v32, v32, v33\n"
      "v_cvt_pk_bf16_f32 v34, v34, v35\n"
      "v_mfma_f32_16x16x32_bf16 v[64:67], v[128:131], a[212:215], v[64:67]\n"
      "ds_read_b128 a[204:207], v2 offset:4160\n"
      "ds_write_addtid_b32 v32 offset:23556\n"
      "v_mfma_f32_4x4x4_16b_bf16 v[20:23], a[216:217], a[232:233], 0\n"
      "ds_write_addtid_b32 v34 offset:24068\n"
      "v_mfma_f32_4x4x4_16b_bf16 v[24:27], a[216:217], a[234:235], 0\n"
      "ds_read_b64_tr_b16 a[220:221], v7 offset:52736\n"
      "ds_read_b64_tr_b16 a[222:223], v7 offset:52744\n"
      "v_mfma_f32_16x16x32_bf16 v[68:71], a[188:191], a[212:215], v[68:71]\n"
      "v_sub_f32 v36, v36, v48\n"
      "v_sub_f32 v37, v37, v49\n"
      "v_cvt_pk_bf16_f32 v20, v20, v24\n"
      "v_mfma_f32_16x16x32_bf16 v[72:75], a[180:183], a[212:215], v[72:75]\n"
      "ds_write_addtid_b32 v20 offset:42752\n"
      "v_sub_f32 v38, v38, v50\n"
      "v_mfma_f32_16x16x32_bf16 v[52:55], a[176:179], a[192:195], v[52:55]\n"
      "ds_read_b128 a[208:211], v2 offset:128\n"
      "v_sub_f32 v39, v39, v51\n"
      "v_mfma_f32_16x16x32_bf16 v[56:59], a[168:171], a[192:195], v[56:59]\n"
      "v_fmac_f32_e64 v36, s46, v120\n"
      "v_cvt_pk_bf16_f32 v21, v21, v25\n"
      "v_mfma_f32_16x16x32_bf16 v[60:63], a[160:163], a[192:195], v[60:63]\n"
      "s_waitcnt lgkmcnt(3)\n"
      "ds_write_addtid_b32 v21 offset:46848\n"
      "v_fmac_f32 v37, s46, v121\n"
      "v_cvt_pk_bf16_f32 v22, v22, v26\n"
      "v_mfma_f32_16x16x32_bf16 v[64:67], a[172:175], a[196:199], v[64:67]\n"
      "ds_read_b128 a[212:215], v2 offset:4224\n"
      "ds_write_addtid_b32 v22 offset:50944\n"
      "v_mfma_f32_16x16x32_bf16 v[68:71], a[164:167], a[196:199], v[68:71]\n"
      "v_fmac_f32 v38, s46, v122\n"
      "v_fmac_f32 v39, s46, v123\n"
      "v_mfma_f32_16x16x32_bf16 v[72:75], a[156:159], a[196:199], v[72:75]\n"
      "v_cvt_pk_bf16_f32 v23, v23, v27\n"
      "v_fmac_f32 v48, s45, v36\n"
      "v_mfma_f32_16x16x32_bf16 v[52:55], a[152:155], a[200:203], v[52:55]\n"
      "ds_read_b128 a[192:195], v3 offset:0\n"
      "ds_write_addtid_b32 v23 offset:55040\n"
      "v_mfma_f32_16x16x32_bf16 v[56:59], a[144:147], a[200:203], v[56:59]\n"
      "v_fmac_f32 v49, s45, v37\n"
      "v_fmac_f32 v50, s45, v38\n"
      "v_fmac_f32_e64 v51, s45, v39\n"
      "v_mfma_f32_16x16x32_bf16 v[60:63], a[136:139], a[200:203], v[60:63]\n"
      "s_waitcnt lgkmcnt(2)\n"
      "ds_read_b64_tr_b16 a[224:225], v7 offset:53248\n"
      "ds_read_b64_tr_b16 a[226:227], v7 offset:53256\n"
      "v_mfma_f32_4x4x4_16b_bf16 v[12:15], a[216:217], a[220:221], 0\n"
      "v_fma_f32 v36, s47, v36, v96\n"
      "v_mfma_f32_4x4x4_16b_bf16 v[16:19], a[216:217], a[222:223], 0\n"
      "v_fma_f32 v37, s47, v37, v97\n"
      "v_fma_f32 v38, s47, v38, v98\n"
      "v_mfma_f32_16x16x32_bf16 v[64:67], a[148:151], a[204:207], v[64:67]\n"
      "ds_read_b128 a[196:199], v3 offset:64\n"
      "v_fma_f32 v39, s47, v39, v99\n"
      "v_cvt_pk_bf16_f32 v12, v12, v16\n"
      "v_mfma_f32_16x16x32_bf16 v[68:71], a[140:143], a[204:207], v[68:71]\n"
      "ds_write_addtid_b32 v12 offset:43264\n"
      "v_cvt_pk_bf16_f32 v13, v13, v17\n"
      "v_mfma_f32_16x16x32_bf16 v[72:75], a[132:135], a[204:207], v[72:75]\n"
      "ds_write_addtid_b32 v13 offset:47360\n"
      "v_cvt_pk_bf16_f32 v48, v48, v49\n"
      "v_mfma_f32_16x16x32_bf16 v[52:55], a[128:131], a[208:211], v[52:55]\n"
      "ds_read_b128 a[200:203], v3 offset:128\n"
      "ds_write_addtid_b32 v48 offset:17408\n"
      "v_mfma_f32_16x16x32_bf16 v[56:59], a[120:123], a[208:211], v[56:59]\n"
      "v_cvt_pk_bf16_f32 v14, v14, v18\n"
      "v_cvt_pk_bf16_f32 v50, v50, v51\n"
      "v_cvt_pk_bf16_f32 v15, v15, v19\n"
      "v_mfma_f32_16x16x32_bf16 v[60:63], a[112:115], a[208:211], v[60:63]\n"
      "ds_write_addtid_b32 v50 offset:17920\n"
      "ds_write_addtid_b32 v14 offset:51456\n"
      "v_mfma_f32_16x16x32_bf16 v[64:67], a[124:127], a[212:215], v[64:67]\n"
      "ds_read_b128 a[204:207], v4 offset:0\n"
      "ds_write_addtid_b32 v15 offset:55552\n"
      "v_mfma_f32_16x16x32_bf16 v[68:71], a[116:119], a[212:215], v[68:71]\n"
      "v_cvt_pk_bf16_f32 v36, v36, v37\n"
      "v_cvt_pk_bf16_f32 v38, v38, v39\n"
      "v_mfma_f32_16x16x32_bf16 v[72:75], a[108:111], a[212:215], v[72:75]\n"
      "s_waitcnt lgkmcnt(4)\n"
      "ds_write_addtid_b32 v36 offset:19456\n"
      "ds_write_addtid_b32 v38 offset:19968\n"
      "v_sub_f32 v52, v64, v52\n"
      "v_mfma_f32_16x16x32_bf16 v[100:103], a[0:3], a[192:195], 0\n"
      "ds_read_b128 a[208:211], v4 offset:64\n"
      "ds_write_addtid_b32 v52 offset:0\n"
      "v_mfma_f32_16x16x32_bf16 v[104:107], a[4:7], a[192:195], 0\n"
      "v_sub_f32 v53, v65, v53\n"
      "v_sub_f32 v54, v66, v54\n"
      "v_mfma_f32_4x4x4_16b_bf16 v[20:23], a[216:217], a[224:225], 0\n"
      "v_sub_f32_e64 v55, v67, v55\n"
      "v_mfma_f32_4x4x4_16b_bf16 v[24:27], a[216:217], a[226:227], 0\n"
      "ds_write_addtid_b32 v53 offset:512\n"
      "ds_write_addtid_b32 v54 offset:1024\n"
      "v_mfma_f32_16x16x32_bf16 v[108:111], a[8:11], a[192:195], 0\n"
      "ds_write_addtid_b32 v55 offset:1536\n"
      "v_sub_f32 v56, v68, v56\n"
      "v_sub_f32 v57, v69, v57\n"
      "v_mfma_f32_16x16x32_bf16 v[100:103], a[12:15], a[196:199], v[100:103]\n"
      "ds_read_b128 a[212:215], v4 offset:128\n"
      "ds_write_addtid_b32 v56 offset:2048\n"
      "v_mfma_f32_16x16x32_bf16 v[104:107], a[16:19], a[196:199], v[104:107]\n"
      "ds_write_addtid_b32 v57 offset:2560\n"
      "v_cvt_pk_bf16_f32 v20, v20, v24\n"
      "v_mfma_f32_16x16x32_bf16 v[108:111], a[20:23], a[196:199], v[108:111]\n"
      "ds_write_addtid_b32 v20 offset:43776\n"
      "v_sub_f32 v58, v70, v58\n"
      "v_mfma_f32_16x16x32_bf16 v[100:103], a[24:27], a[200:203], v[100:103]\n"
      "s_nop 0\n"
      "ds_read_b128 a[192:195], v5 offset:0\n"
      "ds_write_addtid_b32 v58 offset:3072\n"
      "v_sub_f32 v59, v71, v59\n"
      "v_mfma_f32_16x16x32_bf16 v[104:107], a[28:31], a[200:203], v[104:107]\n"
      "ds_write_addtid_b32 v59 offset:3584\n"
      "v_cvt_pk_bf16_f32 v21, v21, v25\n"
      "v_mfma_f32_16x16x32_bf16 v[108:111], a[32:35], a[200:203], v[108:111]\n"
      "s_waitcnt lgkmcnt(7)\n"
      "ds_write_addtid_b32 v21 offset:47872\n"
      "v_sub_f32_e64 v60, v72, v60\n"
      "v_mfma_f32_16x16x32_bf16 v[100:103], a[36:39], a[204:207], v[100:103]\n"
      "ds_read_b128 a[196:199], v5 offset:64\n"
      "ds_write_addtid_b32 v60 offset:4096\n"
      "v_mfma_f32_16x16x32_bf16 v[104:107], a[40:43], a[204:207], v[104:107]\n"
      "v_cvt_pk_bf16_f32 v22, v22, v26\n"
      "v_cvt_pk_bf16_f32 v23, v23, v27\n"
      "v_sub_f32 v61, v73, v61\n"
      "v_mfma_f32_16x16x32_bf16 v[108:111], a[44:47], a[204:207], v[108:111]\n"
      "s_nop 0\n"
      "ds_write_addtid_b32 v22 offset:51968\n"
      "ds_write_addtid_b32 v23 offset:56064\n"
      "v_sub_f32 v62, v74, v62\n"
      "v_sub_f32 v63, v75, v63\n"
      "v_mfma_f32_16x16x32_bf16 v[100:103], a[48:51], a[208:211], v[100:103]\n"
      "ds_read_b128 a[200:203], v5 offset:128\n"
      "ds_write_addtid_b32 v61 offset:4608\n"
      "v_mfma_f32_16x16x32_bf16 v[104:107], a[52:55], a[208:211], v[104:107]\n"
      "ds_write_addtid_b32 v62 offset:5120\n"
      "ds_write_addtid_b32 v63 offset:5632\n"
      "v_mfma_f32_16x16x32_bf16 v[108:111], a[56:59], a[208:211], v[108:111]\n"
      "s_waitcnt lgkmcnt(6)\n"
      "v_fma_f32 v40, 0.5, v52, -v64\n"
      "v_fma_f32 v41, 0.5, v53, -v65\n"
      "v_mfma_f32_16x16x32_bf16 v[100:103], a[60:63], a[212:215], v[100:103]\n"
      "s_nop 0\n"
      "v_fma_f32 v42, 0.5, v54, -v66\n"
      "v_fma_f32 v43, 0.5, v55, -v67\n"
      "v_mfma_f32_16x16x32_bf16 v[104:107], a[64:67], a[212:215], v[104:107]\n"
      "v_fma_f32 v44, 0.5, v56, -v68\n"
      "v_fma_f32 v45, 0.5, v57, -v69\n"
      "v_mfma_f32_16x16x32_bf16 v[108:111], a[68:71], a[212:215], v[108:111]\n"
      "s_waitcnt lgkmcnt(0)\n"
      "s_barrier\n"
      "v_mfma_f32_16x16x32_bf16 v[100:103], a[72:75], a[192:195], v[100:103]\n"
      "ds_read_b128 a[204:207], v0 offset:16384\n"
      "ds_read_b64_tr_b16 a[220:221], v7 offset:0\n"
      "v_fma_f32 v46, 0.5, v58, -v70\n"
      "v_mfma_f32_16x16x32_bf16 v[104:107], a[76:79], a[192:195], v[104:107]\n"
      "ds_read_b128 a[208:211], v0 offset:20480\n"
      "ds_read2st64_b32 v[28:29], v6 offset0:0 offset1:2\n"
      "v_mfma_f32_16x16x32_bf16 v[108:111], a[80:83], a[192:195], v[108:111]\n"
      "ds_read_b64_tr_b16 a[222:223], v7 offset:8\n"
      "ds_read2st64_b32 v[30:31], v6 offset0:4 offset1:6\n"
      "v_mfma_f32_16x16x32_bf16 v[100:103], a[84:87], a[196:199], v[100:103]\n"
      "ds_read_b128 a[212:215], v0 offset:16448\n"
      "v_fma_f32 v47, 0.5, v59, -v71\n"
      "v_mfma_f32_16x16x32_bf16 v[104:107], a[88:91], a[196:199], v[104:107]\n"
      "ds_read2st64_b32 v[32:33], v6 offset0:8 offset1:10\n"
      "v_fma_f32 v48, 0.5, v60, -v72\n"
      "v_fma_f32 v49, 0.5, v61, -v73\n"
      "v_mfma_f32_16x16x32_bf16 v[108:111], a[92:95], a[196:199], v[108:111]\n"
      "ds_read2st64_b32 v[34:35], v6 offset0:12 offset1:14\n"
      "v_fma_f32 v50, 0.5, v62, -v74\n"
      "v_mfma_f32_16x16x32_bf16 v[100:103], a[96:99], a[200:203], v[100:103]\n"
      "ds_read_b128 a[192:195], v0 offset:20544\n"
      "v_fma_f32 v51, 0.5, v63, -v75\n"
      "v_mfma_f32_16x16x32_bf16 v[104:107], a[100:103], a[200:203], v[104:107]\n"
      "ds_read2st64_b32 v[36:37], v6 offset0:16 offset1:18\n"
      "ds_read2st64_b32 v[38:39], v6 offset0:20 offset1:22\n"
      "v_mfma_f32_16x16x32_bf16 v[108:111], a[104:107], a[200:203], v[108:111]\n"
      "s_waitcnt lgkmcnt(8)\n"
      "v_mfma_f32_16x16x32_bf16 v[52:55], v[252:255], a[204:207], v[88:91]\n"
      "s_waitcnt lgkmcnt(6)\n"
      "ds_read_b128 a[196:199], v0 offset:16512\n"
      "v_sub_f32 v28, v28, v40\n"
      "v_sub_f32 v29, v29, v41\n"
      "v_sub_f32 v30, v30, v42\n"
      "v_sub_f32 v31, v31, v43\n"
      "v_mfma_f32_4x4x4_16b_bf16 v[12:15], a[216:217], a[220:221], 0\n"
      "v_fmac_f32_e64 v28, s46, v76\n"
      "v_mfma_f32_4x4x4_16b_bf16 v[16:19], a[216:217], a[222:223], 0\n"
      "ds_read_b64_tr_b16 a[224:225], v7 offset:512\n"
      "ds_read_b64_tr_b16 a[226:227], v7 offset:520\n"
      "v_mfma_f32_16x16x32_bf16 v[56:59], v[244:247], a[204:207], v[92:95]\n"
      "v_fmac_f32 v29, s46, v77\n"
      "v_fmac_f32 v30, s46, v78\n"
      "v_mfma_f32_16x16x32_bf16 v[60:63], v[236:239], a[204:207], v[96:99]\n"
      "v_fmac_f32 v31, s46, v79\n"
      "v_fmac_f32 v40, s45, v28\n"
      "v_cvt_pk_bf16_f32 v12, v12, v16\n"
      "v_mfma_f32_16x16x32_bf16 v[64:67], v[248:251], a[208:211], v[88:91]\n"
      "ds_read_b128 a[200:203], v0 offset:20608\n"
      "ds_write_addtid_b32 v12 offset:24832\n"
      "v_mfma_f32_16x16x32_bf16 v[68:71], v[240:243], a[208:211], v[92:95]\n"
      "v_fmac_f32 v41, s45, v29\n"
      "v_cvt_pk_bf16_f32 v13, v13, v17\n"
      "v_mfma_f32_16x16x32_bf16 v[72:75], v[232:235], a[208:211], v[96:99]\n"
      "s_waitcnt lgkmcnt(4)\n"
      "ds_write_addtid_b32 v13 offset:28928\n"
      "v_fmac_f32_e64 v42, s45, v30\n"
      "v_mfma_f32_16x16x32_bf16 v[52:55], v[228:231], a[212:215], v[52:55]\n"
      "s_nop 0\n"
      "ds_read_b128 a[204:207], v1 offset:16384\n"
      "v_fmac_f32_e64 v43, s45, v31\n"
      "v_cvt_pk_bf16_f32 v14, v14, v18\n"
      "v_mfma_f32_16x16x32_bf16 v[56:59], v[220:223], a[212:215], v[56:59]\n"
      "s_nop 0\n"
      "ds_write_addtid_b32 v14 offset:33024\n"
      "v_fma_f32 v28, s47, v28, v100\n"
      "v_mfma_f32_16x16x32_bf16 v[60:63], v[212:215], a[212:215], v[60:63]\n"
      "s_nop 0\n"
      "v_cvt_pk_bf16_f32 v15, v15, v19\n"
      "v_fma_f32 v29, s47, v29, v101\n"
      "v_mfma_f32_16x16x32_bf16 v[64:67], v[224:227], a[192:195], v[64:67]\n"
      "s_nop 0\n"
      "ds_read_b128 a[208:211], v1 offset:20480\n"
      "ds_write_addtid_b32 v15 offset:37120\n"
      "v_mfma_f32_16x16x32_bf16 v[68:71], v[216:219], a[192:195], v[68:71]\n"
      "s_nop 0\n"
      "v_fma_f32 v30, s47, v30, v102\n"
      "v_fma_f32 v31, s47, v31, v103\n"
      "v_cvt_pk_bf16_f32 v40, v40, v41\n"
      "v_mfma_f32_16x16x32_bf16 v[72:75], v[208:211], a[192:195], v[72:75]\n"
      "s_waitcnt lgkmcnt(2)\n"
      "ds_write_addtid_b32 v40 offset:12288\n"
      "ds_read_b64_tr_b16 a[228:229], v7 offset:1024\n"
      "v_mfma_f32_4x4x4_16b_bf16 v[20:23], a[216:217], a[224:225], 0\n"
      "ds_read_b64_tr_b16 a[230:231], v7 offset:1032\n"
      "v_mfma_f32_4x4x4_16b_bf16 v[24:27], a[216:217], a[226:227], 0\n"
      "v_cvt_pk_bf16_f32 v42, v42, v43\n"
      "v_cvt_pk_bf16_f32 v28, v28, v29\n"
      "v_mfma_f32_16x16x32_bf16 v[52:55], v[204:207], a[196:199], v[52:55]\n"
      "ds_read_b128 a[212:215], v1 offset:16448\n"
      "ds_write_addtid_b32 v42 offset:12800\n"
      "v_mfma_f32_16x16x32_bf16 v[56:59], v[196:199], a[196:199], v[56:59]\n"
      "ds_write_addtid_b32 v28 offset:14336\n"
      "v_cvt_pk_bf16_f32 v20, v20, v24\n"
      "v_cvt_pk_bf16_f32 v30, v30, v31\n"
      "v_mfma_f32_16x16x32_bf16 v[60:63], v[188:191], a[196:199], v[60:63]\n"
      "ds_write_addtid_b32 v30 offset:14848\n"
      "ds_write_addtid_b32 v20 offset:25344\n"
      "v_mfma_f32_16x16x32_bf16 v[64:67], v[200:203], a[200:203], v[64:67]\n"
      "ds_read_b128 a[192:195], v1 offset:20544\n"
      "v_sub_f32 v32, v32, v44\n"
      "v_mfma_f32_16x16x32_bf16 v[68:71], v[192:195], a[200:203], v[68:71]\n"
      "v_cvt_pk_bf16_f32 v21, v21, v25\n"
      "v_sub_f32 v33, v33, v45\n"
      "v_mfma_f32_16x16x32_bf16 v[72:75], v[184:187], a[200:203], v[72:75]\n"
      "ds_write_addtid_b32 v21 offset:29440\n"
      "v_sub_f32 v34, v34, v46\n"
      "v_cvt_pk_bf16_f32 v22, v22, v26\n"
      "v_mfma_f32_16x16x32_bf16 v[52:55], v[180:183], a[204:207], v[52:55]\n"
      "s_nop 0\n"
      "ds_read_b128 a[196:199], v1 offset:16512\n"
      "ds_write_addtid_b32 v22 offset:33536\n"
      "v_mfma_f32_16x16x32_bf16 v[56:59], v[172:175], a[204:207], v[56:59]\n"
      "v_sub_f32 v35, v35, v47\n"
      "v_fmac_f32 v32, s46, v80\n"
      "v_mfma_f32_16x16x32_bf16 v[60:63], v[164:167], a[204:207], v[60:63]\n"
      "s_waitcnt lgkmcnt(3)\n"
      "v_cvt_pk_bf16_f32 v23, v23, v27\n"
      "v_fmac_f32 v33, s46, v81\n"
      "v_mfma_f32_16x16x32_bf16 v[64:67], v[176:179], a[208:211], v[64:67]\n"
      "ds_read_b128 a[200:203], v1 offset:20608\n"
      "ds_write_addtid_b32 v23 offset:37632\n"
      "v_fmac_f32 v34, s46, v82\n"
      "v_mfma_f32_16x16x32_bf16 v[68:71], v[168:171], a[208:211], v[68:71]\n"
      "ds_read_b64_tr_b16 a[232:233], v7 offset:1536\n"
      "ds_read_b64_tr_b16 a[234:235], v7 offset:1544\n"
      "v_mfma_f32_4x4x4_16b_bf16 v[12:15], a[216:217], a[228:229], 0\n"
      "v_fmac_f32 v35, s46, v83\n"
      "v_mfma_f32_4x4x4_16b_bf16 v[16:19], a[216:217], a[230:231], 0\n"
      "v_fmac_f32 v44, s45, v32\n"
      "v_fmac_f32 v45, s45, v33\n"
      "v_mfma_f32_16x16x32_bf16 v[72:75], v[160:163], a[208:211], v[72:75]\n"
      "v_fmac_f32 v46, s45, v34\n"
      "v_fmac_f32 v47, s45, v35\n"
      "v_cvt_pk_bf16_f32 v12, v12, v16\n"
      "v_mfma_f32_16x16x32_bf16 v[52:55], v[156:159], a[212:215], v[52:55]\n"
      "ds_read_b128 a[204:207], v2 offset:16384\n"
      "ds_write_addtid_b32 v12 offset:25856\n"
      "v_mfma_f32_16x16x32_bf16 v[56:59], v[148:151], a[212:215], v[56:59]\n"
      "v_cvt_pk_bf16_f32 v13, v13, v17\n"
      "v_fma_f32 v32, s47, v32, v104\n"
      "v_mfma_f32_16x16x32_bf16 v[60:63], v[140:143], a[212:215], v[60:63]\n"
      "ds_write_addtid_b32 v13 offset:29952\n"
      "v_fma_f32 v33, s47, v33, v105\n"
      "v_mfma_f32_16x16x32_bf16 v[64:67], v[152:155], a[192:195], v[64:67]\n"
      "ds_read_b128 a[208:211], v2 offset:20480\n"
      "v_cvt_pk_bf16_f32 v14, v14, v18\n"
      "v_mfma_f32_16x16x32_bf16 v[68:71], v[144:147], a[192:195], v[68:71]\n"
      "s_nop 0\n"
      "ds_write_addtid_b32 v14 offset:34048\n"
      "v_fma_f32 v34, s47, v34, v106\n"
      "v_fma_f32 v35, s47, v35, v107\n"
      "v_mfma_f32_16x16x32_bf16 v[72:75], v[136:139], a[192:195], v[72:75]\n"
      "s_waitcnt lgkmcnt(3)\n"
      "v_cvt_pk_bf16_f32 v15, v15, v19\n"
      "v_cvt_pk_bf16_f32 v44, v44, v45\n"
      "v_mfma_f32_16x16x32_bf16 v[52:55], v[132:135], a[196:199], v[52:55]\n"
      "ds_read_b128 a[212:215], v2 offset:16448\n"
      "ds_write_addtid_b32 v44 offset:20484\n"
      "v_mfma_f32_16x16x32_bf16 v[56:59], v[124:127], a[196:199], v[56:59]\n"
      "ds_write_addtid_b32 v15 offset:38144\n"
      "v_cvt_pk_bf16_f32 v46, v46, v47\n"
      "v_mfma_f32_16x16x32_bf16 v[60:63], a[184:187], a[196:199], v[60:63]\n"
      "ds_write_addtid_b32 v46 offset:20996\n"
      "v_cvt_pk_bf16_f32 v32, v32, v33\n"
      "v_cvt_pk_bf16_f32 v34, v34, v35\n"
      "v_mfma_f32_16x16x32_bf16 v[64:67], v[128:131], a[200:203], v[64:67]\n"
      "ds_read_b128 a[192:195], v2 offset:20544\n"
      "ds_write_addtid_b32 v32 offset:22532\n"
      "v_mfma_f32_4x4x4_16b_bf16 v[20:23], a[216:217], a[232:233], 0\n"
      "ds_write_addtid_b32 v34 offset:23044\n"
      "v_mfma_f32_4x4x4_16b_bf16 v[24:27], a[216:217], a[234:235], 0\n"
      "ds_read_b64_tr_b16 a[220:221], v7 offset:2048\n"
      "ds_read_b64_tr_b16 a[222:223], v7 offset:2056\n"
      "v_mfma_f32_16x16x32_bf16 v[68:71], a[188:191], a[200:203], v[68:71]\n"
      "v_sub_f32 v36, v36, v48\n"
      "v_sub_f32 v37, v37, v49\n"
      "v_cvt_pk_bf16_f32 v20, v20, v24\n"
      "v_mfma_f32_16x16x32_bf16 v[72:75], a[180:183], a[200:203], v[72:75]\n"
      "ds_write_addtid_b32 v20 offset:26368\n"
      "v_sub_f32_e64 v38, v38, v50\n"
      "v_mfma_f32_16x16x32_bf16 v[52:55], a[176:179], a[204:207], v[52:55]\n"
      "ds_read_b128 a[196:199], v2 offset:16512\n"
      "v_sub_f32 v39, v39, v51\n"
      "v_mfma_f32_16x16x32_bf16 v[56:59], a[168:171], a[204:207], v[56:59]\n"
      "v_fmac_f32 v36, s46, v84\n"
      "v_cvt_pk_bf16_f32 v21, v21, v25\n"
      "v_mfma_f32_16x16x32_bf16 v[60:63], a[160:163], a[204:207], v[60:63]\n"
      "s_waitcnt lgkmcnt(3)\n"
      "ds_write_addtid_b32 v21 offset:30464\n"
      "v_fmac_f32 v37, s46, v85\n"
      "v_cvt_pk_bf16_f32 v22, v22, v26\n"
      "v_mfma_f32_16x16x32_bf16 v[64:67], a[172:175], a[208:211], v[64:67]\n"
      "ds_read_b128 a[200:203], v2 offset:20608\n"
      "ds_write_addtid_b32 v22 offset:34560\n"
      "v_mfma_f32_16x16x32_bf16 v[68:71], a[164:167], a[208:211], v[68:71]\n"
      "v_fmac_f32 v38, s46, v86\n"
      "v_fmac_f32 v39, s46, v87\n"
      "v_mfma_f32_16x16x32_bf16 v[72:75], a[156:159], a[208:211], v[72:75]\n"
      "v_cvt_pk_bf16_f32 v23, v23, v27\n"
      "v_fmac_f32 v48, s45, v36\n"
      "v_mfma_f32_16x16x32_bf16 v[52:55], a[152:155], a[212:215], v[52:55]\n"
      "ds_read_b128 a[204:207], v3 offset:16896\n"
      "ds_write_addtid_b32 v23 offset:38656\n"
      "v_mfma_f32_16x16x32_bf16 v[56:59], a[144:147], a[212:215], v[56:59]\n"
      "v_fmac_f32 v49, s45, v37\n"
      "v_fmac_f32 v50, s45, v38\n"
      "v_fmac_f32_e64 v51, s45, v39\n"
      "v_mfma_f32_16x16x32_bf16 v[60:63], a[136:139], a[212:215], v[60:63]\n"
      "s_waitcnt lgkmcnt(2)\n"
      "ds_read_b64_tr_b16 a[224:225], v7 offset:2560\n"
      "ds_read_b64_tr_b16 a[226:227], v7 offset:2568\n"
      "v_mfma_f32_4x4x4_16b_bf16 v[12:15], a[216:217], a[220:221], 0\n"
      "v_fma_f32 v36, s47, v36, v108\n"
      "v_mfma_f32_4x4x4_16b_bf16 v[16:19], a[216:217], a[222:223], 0\n"
      "v_fma_f32 v37, s47, v37, v109\n"
      "v_fma_f32 v38, s47, v38, v110\n"
      "v_mfma_f32_16x16x32_bf16 v[64:67], a[148:151], a[192:195], v[64:67]\n"
      "ds_read_b128 a[208:211], v3 offset:16960\n"
      "v_fma_f32 v39, s47, v39, v111\n"
      "v_cvt_pk_bf16_f32 v12, v12, v16\n"
      "v_mfma_f32_16x16x32_bf16 v[68:71], a[140:143], a[192:195], v[68:71]\n"
      "ds_write_addtid_b32 v12 offset:26880\n"
      "v_cvt_pk_bf16_f32 v13, v13, v17\n"
      "v_mfma_f32_16x16x32_bf16 v[72:75], a[132:135], a[192:195], v[72:75]\n"
      "ds_write_addtid_b32 v13 offset:30976\n"
      "v_cvt_pk_bf16_f32 v48, v48, v49\n"
      "v_mfma_f32_16x16x32_bf16 v[52:55], a[128:131], a[196:199], v[52:55]\n"
      "ds_read_b128 a[212:215], v3 offset:17024\n"
      "ds_write_addtid_b32 v48 offset:16384\n"
      "v_mfma_f32_16x16x32_bf16 v[56:59], a[120:123], a[196:199], v[56:59]\n"
      "v_cvt_pk_bf16_f32 v14, v14, v18\n"
      "v_cvt_pk_bf16_f32 v50, v50, v51\n"
      "v_cvt_pk_bf16_f32 v15, v15, v19\n"
      "v_mfma_f32_16x16x32_bf16 v[60:63], a[112:115], a[196:199], v[60:63]\n"
      "ds_write_addtid_b32 v50 offset:16896\n"
      "ds_write_addtid_b32 v14 offset:35072\n"
      "v_mfma_f32_16x16x32_bf16 v[64:67], a[124:127], a[200:203], v[64:67]\n"
      "ds_read_b128 a[192:195], v4 offset:16896\n"
      "ds_write_addtid_b32 v15 offset:39168\n"
      "v_mfma_f32_16x16x32_bf16 v[68:71], a[116:119], a[200:203], v[68:71]\n"
      "v_cvt_pk_bf16_f32 v36, v36, v37\n"
      "v_cvt_pk_bf16_f32 v38, v38, v39\n"
      "v_mfma_f32_16x16x32_bf16 v[72:75], a[108:111], a[200:203], v[72:75]\n"
      "s_waitcnt lgkmcnt(4)\n"
      "ds_write_addtid_b32 v36 offset:18432\n"
      "ds_write_addtid_b32 v38 offset:18944\n"
      "v_sub_f32 v52, v64, v52\n"
      "v_mfma_f32_16x16x32_bf16 v[112:115], a[0:3], a[204:207], 0\n"
      "ds_read_b128 a[196:199], v4 offset:16960\n"
      "ds_write_addtid_b32 v52 offset:6144\n"
      "v_mfma_f32_16x16x32_bf16 v[116:119], a[4:7], a[204:207], 0\n"
      "v_sub_f32 v53, v65, v53\n"
      "v_sub_f32 v54, v66, v54\n"
      "v_mfma_f32_4x4x4_16b_bf16 v[20:23], a[216:217], a[224:225], 0\n"
      "v_sub_f32 v55, v67, v55\n"
      "v_mfma_f32_4x4x4_16b_bf16 v[24:27], a[216:217], a[226:227], 0\n"
      "ds_write_addtid_b32 v53 offset:6656\n"
      "ds_write_addtid_b32 v54 offset:7168\n"
      "v_mfma_f32_16x16x32_bf16 v[120:123], a[8:11], a[204:207], 0\n"
      "s_nop 0\n"
      "ds_write_addtid_b32 v55 offset:7680\n"
      "v_sub_f32 v56, v68, v56\n"
      "v_sub_f32 v57, v69, v57\n"
      "v_mfma_f32_16x16x32_bf16 v[112:115], a[12:15], a[208:211], v[112:115]\n"
      "ds_read_b128 a[200:203], v4 offset:17024\n"
      "ds_write_addtid_b32 v56 offset:8192\n"
      "v_mfma_f32_16x16x32_bf16 v[116:119], a[16:19], a[208:211], v[116:119]\n"
      "ds_write_addtid_b32 v57 offset:8704\n"
      "v_cvt_pk_bf16_f32 v20, v20, v24\n"
      "v_mfma_f32_16x16x32_bf16 v[120:123], a[20:23], a[208:211], v[120:123]\n"
      "ds_write_addtid_b32 v20 offset:27392\n"
      "v_sub_f32 v58, v70, v58\n"
      "v_mfma_f32_16x16x32_bf16 v[112:115], a[24:27], a[212:215], v[112:115]\n"
      "ds_read_b128 a[204:207], v5 offset:16896\n"
      "ds_write_addtid_b32 v58 offset:9216\n"
      "v_sub_f32 v59, v71, v59\n"
      "v_mfma_f32_16x16x32_bf16 v[116:119], a[28:31], a[212:215], v[116:119]\n"
      "ds_write_addtid_b32 v59 offset:9728\n"
      "v_cvt_pk_bf16_f32 v21, v21, v25\n"
      "v_mfma_f32_16x16x32_bf16 v[120:123], a[32:35], a[212:215], v[120:123]\n"
      "s_waitcnt lgkmcnt(7)\n"
      "ds_write_addtid_b32 v21 offset:31488\n"
      "v_sub_f32 v60, v72, v60\n"
      "v_mfma_f32_16x16x32_bf16 v[112:115], a[36:39], a[192:195], v[112:115]\n"
      "ds_read_b128 a[208:211], v5 offset:16960\n"
      "ds_write_addtid_b32 v60 offset:10240\n"
      "v_mfma_f32_16x16x32_bf16 v[116:119], a[40:43], a[192:195], v[116:119]\n"
      "v_cvt_pk_bf16_f32 v22, v22, v26\n"
      "v_cvt_pk_bf16_f32 v23, v23, v27\n"
      "v_sub_f32 v61, v73, v61\n"
      "v_mfma_f32_16x16x32_bf16 v[120:123], a[44:47], a[192:195], v[120:123]\n"
      "ds_write_addtid_b32 v22 offset:35584\n"
      "ds_write_addtid_b32 v23 offset:39680\n"
      "v_sub_f32 v62, v74, v62\n"
      "v_sub_f32 v63, v75, v63\n"
      "v_mfma_f32_16x16x32_bf16 v[112:115], a[48:51], a[196:199], v[112:115]\n"
      "s_nop 0\n"
      "ds_read_b128 a[212:215], v5 offset:17024\n"
      "ds_write_addtid_b32 v61 offset:10752\n"
      "v_mfma_f32_16x16x32_bf16 v[116:119], a[52:55], a[196:199], v[116:119]\n"
      "s_nop 0\n"
      "ds_write_addtid_b32 v62 offset:11264\n"
      "ds_write_addtid_b32 v63 offset:11776\n"
      "v_mfma_f32_16x16x32_bf16 v[120:123], a[56:59], a[196:199], v[120:123]\n"
      "s_waitcnt lgkmcnt(6)\n"
      "v_fma_f32 v40, 0.5, v52, -v64\n"
      "v_fma_f32 v41, 0.5, v53, -v65\n"
      "v_mfma_f32_16x16x32_bf16 v[112:115], a[60:63], a[200:203], v[112:115]\n"
      "v_fma_f32 v42, 0.5, v54, -v66\n"
      "v_fma_f32 v43, 0.5, v55, -v67\n"
      "v_mfma_f32_16x16x32_bf16 v[116:119], a[64:67], a[200:203], v[116:119]\n"
      "v_fma_f32 v44, 0.5, v56, -v68\n"
      "v_fma_f32 v45, 0.5, v57, -v69\n"
      "v_mfma_f32_16x16x32_bf16 v[120:123], a[68:71], a[200:203], v[120:123]\n"
      "s_waitcnt lgkmcnt(0)\n"
      "s_barrier\n"
      "v_mfma_f32_16x16x32_bf16 v[112:115], a[72:75], a[204:207], v[112:115]\n"
      "ds_read_b128 a[192:195], v0 offset:0\n"
      "ds_read_b64_tr_b16 a[220:221], v7 offset:16896\n"
      "v_fma_f32 v46, 0.5, v58, -v70\n"
      "v_mfma_f32_16x16x32_bf16 v[116:119], a[76:79], a[204:207], v[116:119]\n"
      "ds_read_b128 a[196:199], v0 offset:4096\n"
      "ds_read2st64_b32 v[28:29], v6 offset0:24 offset1:26\n"
      "v_mfma_f32_16x16x32_bf16 v[120:123], a[80:83], a[204:207], v[120:123]\n"
      "ds_read_b64_tr_b16 a[222:223], v7 offset:16904\n"
      "ds_read2st64_b32 v[30:31], v6 offset0:28 offset1:30\n"
      "v_mfma_f32_16x16x32_bf16 v[112:115], a[84:87], a[208:211], v[112:115]\n"
      "ds_read_b128 a[200:203], v0 offset:64\n"
      "v_fma_f32 v47, 0.5, v59, -v71\n"
      "v_mfma_f32_16x16x32_bf16 v[116:119], a[88:91], a[208:211], v[116:119]\n"
      "ds_read2st64_b32 v[32:33], v6 offset0:32 offset1:34\n"
      "v_fma_f32 v48, 0.5, v60, -v72\n"
      "v_fma_f32 v49, 0.5, v61, -v73\n"
      "v_mfma_f32_16x16x32_bf16 v[120:123], a[92:95], a[208:211], v[120:123]\n"
      "ds_read2st64_b32 v[34:35], v6 offset0:36 offset1:38\n"
      "v_fma_f32 v50, 0.5, v62, -v74\n"
      "v_mfma_f32_16x16x32_bf16 v[112:115], a[96:99], a[212:215], v[112:115]\n"
      "ds_read_b128 a[204:207], v0 offset:4160\n"
      "v_fma_f32 v51, 0.5, v63, -v75\n"
      "v_mfma_f32_16x16x32_bf16 v[116:119], a[100:103], a[212:215], v[116:119]\n"
      "ds_read2st64_b32 v[36:37], v6 offset0:40 offset1:42\n"
      "ds_read2st64_b32 v[38:39], v6 offset0:44 offset1:46\n"
      "v_mfma_f32_16x16x32_bf16 v[120:123], a[104:107], a[212:215], v[120:123]\n"
      "s_waitcnt lgkmcnt(8)\n"
      "v_mfma_f32_16x16x32_bf16 v[52:55], v[252:255], a[192:195], v[100:103]\n"
      "s_waitcnt lgkmcnt(6)\n"
      "ds_read_b128 a[208:211], v0 offset:128\n"
      "v_sub_f32 v28, v28, v40\n"
      "v_sub_f32 v29, v29, v41\n"
      "v_sub_f32 v30, v30, v42\n"
      "v_sub_f32 v31, v31, v43\n"
      "v_mfma_f32_4x4x4_16b_bf16 v[12:15], a[216:217], a[220:221], 0\n"
      "v_fmac_f32 v28, s46, v88\n"
      "v_mfma_f32_4x4x4_16b_bf16 v[16:19], a[216:217], a[222:223], 0\n"
      "ds_read_b64_tr_b16 a[224:225], v7 offset:17408\n"
      "ds_read_b64_tr_b16 a[226:227], v7 offset:17416\n"
      "v_mfma_f32_16x16x32_bf16 v[56:59], v[244:247], a[192:195], v[104:107]\n"
      "v_fmac_f32 v29, s46, v89\n"
      "v_fmac_f32_e64 v30, s46, v90\n"
      "v_mfma_f32_16x16x32_bf16 v[60:63], v[236:239], a[192:195], v[108:111]\n"
      "v_fmac_f32 v31, s46, v91\n"
      "v_fmac_f32 v40, s45, v28\n"
      "v_cvt_pk_bf16_f32 v12, v12, v16\n"
      "v_mfma_f32_16x16x32_bf16 v[64:67], v[248:251], a[196:199], v[100:103]\n"
      "ds_read_b128 a[212:215], v0 offset:4224\n"
      "ds_write_addtid_b32 v12 offset:41216\n"
      "v_mfma_f32_16x16x32_bf16 v[68:71], v[240:243], a[196:199], v[104:107]\n"
      "v_fmac_f32_e64 v41, s45, v29\n"
      "v_cvt_pk_bf16_f32 v13, v13, v17\n"
      "v_mfma_f32_16x16x32_bf16 v[72:75], v[232:235], a[196:199], v[108:111]\n"
      "s_waitcnt lgkmcnt(4)\n"
      "ds_write_addtid_b32 v13 offset:45312\n"
      "v_fmac_f32 v42, s45, v30\n"
      "v_mfma_f32_16x16x32_bf16 v[52:55], v[228:231], a[200:203], v[52:55]\n"
      "ds_read_b128 a[192:195], v1 offset:0\n"
      "v_fmac_f32_e64 v43, s45, v31\n"
      "v_cvt_pk_bf16_f32 v14, v14, v18\n"
      "v_mfma_f32_16x16x32_bf16 v[56:59], v[220:223], a[200:203], v[56:59]\n"
      "ds_write_addtid_b32 v14 offset:49408\n"
      "v_fma_f32 v28, s47, v28, v112\n"
      "v_mfma_f32_16x16x32_bf16 v[60:63], v[212:215], a[200:203], v[60:63]\n"
      "v_cvt_pk_bf16_f32 v15, v15, v19\n"
      "v_fma_f32 v29, s47, v29, v113\n"
      "v_mfma_f32_16x16x32_bf16 v[64:67], v[224:227], a[204:207], v[64:67]\n"
      "ds_read_b128 a[196:199], v1 offset:4096\n"
      "ds_write_addtid_b32 v15 offset:53504\n"
      "v_mfma_f32_16x16x32_bf16 v[68:71], v[216:219], a[204:207], v[68:71]\n"
      "s_nop 0\n"
      "v_fma_f32 v30, s47, v30, v114\n"
      "v_fma_f32 v31, s47, v31, v115\n"
      "v_cvt_pk_bf16_f32 v40, v40, v41\n"
      "v_mfma_f32_16x16x32_bf16 v[72:75], v[208:211], a[204:207], v[72:75]\n"
      "s_waitcnt lgkmcnt(2)\n"
      "ds_write_addtid_b32 v40 offset:13312\n"
      "ds_read_b64_tr_b16 a[228:229], v7 offset:17920\n"
      "v_mfma_f32_4x4x4_16b_bf16 v[20:23], a[216:217], a[224:225], 0\n"
      "ds_read_b64_tr_b16 a[230:231], v7 offset:17928\n"
      "v_mfma_f32_4x4x4_16b_bf16 v[24:27], a[216:217], a[226:227], 0\n"
      "v_cvt_pk_bf16_f32 v42, v42, v43\n"
      "v_cvt_pk_bf16_f32 v28, v28, v29\n"
      "v_mfma_f32_16x16x32_bf16 v[52:55], v[204:207], a[208:211], v[52:55]\n"
      "ds_read_b128 a[200:203], v1 offset:64\n"
      "ds_write_addtid_b32 v42 offset:13824\n"
      "v_mfma_f32_16x16x32_bf16 v[56:59], v[196:199], a[208:211], v[56:59]\n"
      "ds_write_addtid_b32 v28 offset:15360\n"
      "v_cvt_pk_bf16_f32 v20, v20, v24\n"
      "v_cvt_pk_bf16_f32 v30, v30, v31\n"
      "v_mfma_f32_16x16x32_bf16 v[60:63], v[188:191], a[208:211], v[60:63]\n"
      "ds_write_addtid_b32 v30 offset:15872\n"
      "ds_write_addtid_b32 v20 offset:41728\n"
      "v_mfma_f32_16x16x32_bf16 v[64:67], v[200:203], a[212:215], v[64:67]\n"
      "ds_read_b128 a[204:207], v1 offset:4160\n"
      "v_sub_f32 v32, v32, v44\n"
      "v_mfma_f32_16x16x32_bf16 v[68:71], v[192:195], a[212:215], v[68:71]\n"
      "v_cvt_pk_bf16_f32 v21, v21, v25\n"
      "v_sub_f32 v33, v33, v45\n"
      "v_mfma_f32_16x16x32_bf16 v[72:75], v[184:187], a[212:215], v[72:75]\n"
      "ds_write_addtid_b32 v21 offset:45824\n"
      "v_sub_f32_e64 v34, v34, v46\n"
      "v_cvt_pk_bf16_f32 v22, v22, v26\n"
      "v_mfma_f32_16x16x32_bf16 v[52:55], v[180:183], a[192:195], v[52:55]\n"
      "ds_read_b128 a[208:211], v1 offset:128\n"
      "ds_write_addtid_b32 v22 offset:49920\n"
      "v_mfma_f32_16x16x32_bf16 v[56:59], v[172:175], a[192:195], v[56:59]\n"
      "v_sub_f32 v35, v35, v47\n"
      "v_fmac_f32_e64 v32, s46, v92\n"
      "v_mfma_f32_16x16x32_bf16 v[60:63], v[164:167], a[192:195], v[60:63]\n"
      "s_waitcnt lgkmcnt(3)\n"
      "v_cvt_pk_bf16_f32 v23, v23, v27\n"
      "v_fmac_f32 v33, s46, v93\n"
      "v_mfma_f32_16x16x32_bf16 v[64:67], v[176:179], a[196:199], v[64:67]\n"
      "ds_read_b128 a[212:215], v1 offset:4224\n"
      "ds_write_addtid_b32 v23 offset:54016\n"
      "v_fmac_f32 v34, s46, v94\n"
      "v_mfma_f32_16x16x32_bf16 v[68:71], v[168:171], a[196:199], v[68:71]\n"
      "ds_read_b64_tr_b16 a[232:233], v7 offset:18432\n"
      "ds_read_b64_tr_b16 a[234:235], v7 offset:18440\n"
      "v_mfma_f32_4x4x4_16b_bf16 v[12:15], a[216:217], a[228:229], 0\n"
      "v_fmac_f32 v35, s46, v95\n"
      "v_mfma_f32_4x4x4_16b_bf16 v[16:19], a[216:217], a[230:231], 0\n"
      "v_fmac_f32 v44, s45, v32\n"
      "v_fmac_f32 v45, s45, v33\n"
      "v_mfma_f32_16x16x32_bf16 v[72:75], v[160:163], a[196:199], v[72:75]\n"
      "v_fmac_f32 v46, s45, v34\n"
      "v_fmac_f32 v47, s45, v35\n"
      "v_cvt_pk_bf16_f32 v12, v12, v16\n"
      "v_mfma_f32_16x16x32_bf16 v[52:55], v[156:159], a[200:203], v[52:55]\n"
      "s_nop 0\n"
      "ds_read_b128 a[192:195], v2 offset:0\n"
      "ds_write_addtid_b32 v12 offset:42240\n"
      "v_mfma_f32_16x16x32_bf16 v[56:59], v[148:151], a[200:203], v[56:59]\n"
      "v_cvt_pk_bf16_f32 v13, v13, v17\n"
      "v_fma_f32 v32, s47, v32, v116\n"
      "v_mfma_f32_16x16x32_bf16 v[60:63], v[140:143], a[200:203], v[60:63]\n"
      "ds_write_addtid_b32 v13 offset:46336\n"
      "v_fma_f32 v33, s47, v33, v117\n"
      "v_mfma_f32_16x16x32_bf16 v[64:67], v[152:155], a[204:207], v[64:67]\n"
      "ds_read_b128 a[196:199], v2 offset:4096\n"
      "v_cvt_pk_bf16_f32 v14, v14, v18\n"
      "v_mfma_f32_16x16x32_bf16 v[68:71], v[144:147], a[204:207], v[68:71]\n"
      "ds_write_addtid_b32 v14 offset:50432\n"
      "v_fma_f32 v34, s47, v34, v118\n"
      "v_fma_f32 v35, s47, v35, v119\n"
      "v_mfma_f32_16x16x32_bf16 v[72:75], v[136:139], a[204:207], v[72:75]\n"
      "s_waitcnt lgkmcnt(3)\n"
      "v_cvt_pk_bf16_f32 v15, v15, v19\n"
      "v_cvt_pk_bf16_f32 v44, v44, v45\n"
      "v_mfma_f32_16x16x32_bf16 v[52:55], v[132:135], a[208:211], v[52:55]\n"
      "ds_read_b128 a[200:203], v2 offset:64\n"
      "ds_write_addtid_b32 v44 offset:21508\n"
      "v_mfma_f32_16x16x32_bf16 v[56:59], v[124:127], a[208:211], v[56:59]\n"
      "s_nop 0\n"
      "ds_write_addtid_b32 v15 offset:54528\n"
      "v_cvt_pk_bf16_f32 v46, v46, v47\n"
      "v_mfma_f32_16x16x32_bf16 v[60:63], a[184:187], a[208:211], v[60:63]\n"
      "ds_write_addtid_b32 v46 offset:22020\n"
      "v_cvt_pk_bf16_f32 v32, v32, v33\n"
      "v_cvt_pk_bf16_f32 v34, v34, v35\n"
      "v_mfma_f32_16x16x32_bf16 v[64:67], v[128:131], a[212:215], v[64:67]\n"
      "ds_read_b128 a[204:207], v2 offset:4160\n"
      "ds_write_addtid_b32 v32 offset:23556\n"
      "v_mfma_f32_4x4x4_16b_bf16 v[20:23], a[216:217], a[232:233], 0\n"
      "ds_write_addtid_b32 v34 offset:24068\n"
      "v_mfma_f32_4x4x4_16b_bf16 v[24:27], a[216:217], a[234:235], 0\n"
      "ds_read_b64_tr_b16 a[220:221], v7 offset:18944\n"
      "ds_read_b64_tr_b16 a[222:223], v7 offset:18952\n"
      "v_mfma_f32_16x16x32_bf16 v[68:71], a[188:191], a[212:215], v[68:71]\n"
      "v_sub_f32 v36, v36, v48\n"
      "v_sub_f32 v37, v37, v49\n"
      "v_cvt_pk_bf16_f32 v20, v20, v24\n"
      "v_mfma_f32_16x16x32_bf16 v[72:75], a[180:183], a[212:215], v[72:75]\n"
      "ds_write_addtid_b32 v20 offset:42752\n"
      "v_sub_f32 v38, v38, v50\n"
      "v_mfma_f32_16x16x32_bf16 v[52:55], a[176:179], a[192:195], v[52:55]\n"
      "ds_read_b128 a[208:211], v2 offset:128\n"
      "v_sub_f32 v39, v39, v51\n"
      "v_mfma_f32_16x16x32_bf16 v[56:59], a[168:171], a[192:195], v[56:59]\n"
      "v_fmac_f32_e64 v36, s46, v96\n"
      "v_cvt_pk_bf16_f32 v21, v21, v25\n"
      "v_mfma_f32_16x16x32_bf16 v[60:63], a[160:163], a[192:195], v[60:63]\n"
      "s_waitcnt lgkmcnt(3)\n"
      "ds_write_addtid_b32 v21 offset:46848\n"
      "v_fmac_f32 v37, s46, v97\n"
      "v_cvt_pk_bf16_f32 v22, v22, v26\n"
      "v_mfma_f32_16x16x32_bf16 v[64:67], a[172:175], a[196:199], v[64:67]\n"
      "ds_read_b128 a[212:215], v2 offset:4224\n"
      "ds_write_addtid_b32 v22 offset:50944\n"
      "v_mfma_f32_16x16x32_bf16 v[68:71], a[164:167], a[196:199], v[68:71]\n"
      "v_fmac_f32 v38, s46, v98\n"
      "v_fmac_f32 v39, s46, v99\n"
      "v_mfma_f32_16x16x32_bf16 v[72:75], a[156:159], a[196:199], v[72:75]\n"
      "v_cvt_pk_bf16_f32 v23, v23, v27\n"
      "v_fmac_f32 v48, s45, v36\n"
      "v_mfma_f32_16x16x32_bf16 v[52:55], a[152:155], a[200:203], v[52:55]\n"
      "ds_read_b128 a[192:195], v3 offset:33792\n"
      "ds_write_addtid_b32 v23 offset:55040\n"
      "v_mfma_f32_16x16x32_bf16 v[56:59], a[144:147], a[200:203], v[56:59]\n"
      "v_fmac_f32 v49, s45, v37\n"
      "v_fmac_f32 v50, s45, v38\n"
      "v_fmac_f32_e64 v51, s45, v39\n"
      "v_mfma_f32_16x16x32_bf16 v[60:63], a[136:139], a[200:203], v[60:63]\n"
      "s_waitcnt lgkmcnt(2)\n"
      "ds_read_b64_tr_b16 a[224:225], v7 offset:19456\n"
      "ds_read_b64_tr_b16 a[226:227], v7 offset:19464\n"
      "v_mfma_f32_4x4x4_16b_bf16 v[12:15], a[216:217], a[220:221], 0\n"
      "v_fma_f32 v36, s47, v36, v120\n"
      "v_mfma_f32_4x4x4_16b_bf16 v[16:19], a[216:217], a[222:223], 0\n"
      "v_fma_f32 v37, s47, v37, v121\n"
      "v_fma_f32 v38, s47, v38, v122\n"
      "v_mfma_f32_16x16x32_bf16 v[64:67], a[148:151], a[204:207], v[64:67]\n"
      "ds_read_b128 a[196:199], v3 offset:33856\n"
      "v_fma_f32 v39, s47, v39, v123\n"
      "v_cvt_pk_bf16_f32 v12, v12, v16\n"
      "v_mfma_f32_16x16x32_bf16 v[68:71], a[140:143], a[204:207], v[68:71]\n"
      "ds_write_addtid_b32 v12 offset:43264\n"
      "v_cvt_pk_bf16_f32 v13, v13, v17\n"
      "v_mfma_f32_16x16x32_bf16 v[72:75], a[132:135], a[204:207], v[72:75]\n"
      "ds_write_addtid_b32 v13 offset:47360\n"
      "v_cvt_pk_bf16_f32 v48, v48, v49\n"
      "v_mfma_f32_16x16x32_bf16 v[52:55], a[128:131], a[208:211], v[52:55]\n"
      "ds_read_b128 a[200:203], v3 offset:33920\n"
      "ds_write_addtid_b32 v48 offset:17408\n"
      "v_mfma_f32_16x16x32_bf16 v[56:59], a[120:123], a[208:211], v[56:59]\n"
      "v_cvt_pk_bf16_f32 v14, v14, v18\n"
      "v_cvt_pk_bf16_f32 v50, v50, v51\n"
      "v_cvt_pk_bf16_f32 v15, v15, v19\n"
      "v_mfma_f32_16x16x32_bf16 v[60:63], a[112:115], a[208:211], v[60:63]\n"
      "ds_write_addtid_b32 v50 offset:17920\n"
      "ds_write_addtid_b32 v14 offset:51456\n"
      "v_mfma_f32_16x16x32_bf16 v[64:67], a[124:127], a[212:215], v[64:67]\n"
      "ds_read_b128 a[204:207], v4 offset:33792\n"
      "ds_write_addtid_b32 v15 offset:55552\n"
      "v_mfma_f32_16x16x32_bf16 v[68:71], a[116:119], a[212:215], v[68:71]\n"
      "v_cvt_pk_bf16_f32 v36, v36, v37\n"
      "v_cvt_pk_bf16_f32 v38, v38, v39\n"
      "v_mfma_f32_16x16x32_bf16 v[72:75], a[108:111], a[212:215], v[72:75]\n"
      "s_waitcnt lgkmcnt(4)\n"
      "ds_write_addtid_b32 v36 offset:19456\n"
      "ds_write_addtid_b32 v38 offset:19968\n"
      "v_sub_f32 v52, v64, v52\n"
      "v_mfma_f32_16x16x32_bf16 v[76:79], a[0:3], a[192:195], 0\n"
      "ds_read_b128 a[208:211], v4 offset:33856\n"
      "ds_write_addtid_b32 v52 offset:0\n"
      "v_mfma_f32_16x16x32_bf16 v[80:83], a[4:7], a[192:195], 0\n"
      "v_sub_f32 v53, v65, v53\n"
      "v_sub_f32 v54, v66, v54\n"
      "v_mfma_f32_4x4x4_16b_bf16 v[20:23], a[216:217], a[224:225], 0\n"
      "v_sub_f32_e64 v55, v67, v55\n"
      "v_mfma_f32_4x4x4_16b_bf16 v[24:27], a[216:217], a[226:227], 0\n"
      "ds_write_addtid_b32 v53 offset:512\n"
      "ds_write_addtid_b32 v54 offset:1024\n"
      "v_mfma_f32_16x16x32_bf16 v[84:87], a[8:11], a[192:195], 0\n"
      "ds_write_addtid_b32 v55 offset:1536\n"
      "v_sub_f32 v56, v68, v56\n"
      "v_sub_f32 v57, v69, v57\n"
      "v_mfma_f32_16x16x32_bf16 v[76:79], a[12:15], a[196:199], v[76:79]\n"
      "ds_read_b128 a[212:215], v4 offset:33920\n"
      "ds_write_addtid_b32 v56 offset:2048\n"
      "v_mfma_f32_16x16x32_bf16 v[80:83], a[16:19], a[196:199], v[80:83]\n"
      "ds_write_addtid_b32 v57 offset:2560\n"
      "v_cvt_pk_bf16_f32 v20, v20, v24\n"
      "v_mfma_f32_16x16x32_bf16 v[84:87], a[20:23], a[196:199], v[84:87]\n"
      "ds_write_addtid_b32 v20 offset:43776\n"
      "v_sub_f32 v58, v70, v58\n"
      "v_mfma_f32_16x16x32_bf16 v[76:79], a[24:27], a[200:203], v[76:79]\n"
      "s_nop 0\n"
      "ds_read_b128 a[192:195], v5 offset:33792\n"
      "ds_write_addtid_b32 v58 offset:3072\n"
      "v_sub_f32 v59, v71, v59\n"
      "v_mfma_f32_16x16x32_bf16 v[80:83], a[28:31], a[200:203], v[80:83]\n"
      "ds_write_addtid_b32 v59 offset:3584\n"
      "v_cvt_pk_bf16_f32 v21, v21, v25\n"
      "v_mfma_f32_16x16x32_bf16 v[84:87], a[32:35], a[200:203], v[84:87]\n"
      "s_waitcnt lgkmcnt(7)\n"
      "ds_write_addtid_b32 v21 offset:47872\n"
      "v_sub_f32_e64 v60, v72, v60\n"
      "v_mfma_f32_16x16x32_bf16 v[76:79], a[36:39], a[204:207], v[76:79]\n"
      "ds_read_b128 a[196:199], v5 offset:33856\n"
      "ds_write_addtid_b32 v60 offset:4096\n"
      "v_mfma_f32_16x16x32_bf16 v[80:83], a[40:43], a[204:207], v[80:83]\n"
      "v_cvt_pk_bf16_f32 v22, v22, v26\n"
      "v_cvt_pk_bf16_f32 v23, v23, v27\n"
      "v_sub_f32 v61, v73, v61\n"
      "v_mfma_f32_16x16x32_bf16 v[84:87], a[44:47], a[204:207], v[84:87]\n"
      "s_nop 0\n"
      "ds_write_addtid_b32 v22 offset:51968\n"
      "ds_write_addtid_b32 v23 offset:56064\n"
      "v_sub_f32 v62, v74, v62\n"
      "v_sub_f32 v63, v75, v63\n"
      "v_mfma_f32_16x16x32_bf16 v[76:79], a[48:51], a[208:211], v[76:79]\n"
      "ds_read_b128 a[200:203], v5 offset:33920\n"
      "ds_write_addtid_b32 v61 offset:4608\n"
      "v_mfma_f32_16x16x32_bf16 v[80:83], a[52:55], a[208:211], v[80:83]\n"
      "ds_write_addtid_b32 v62 offset:5120\n"
      "ds_write_addtid_b32 v63 offset:5632\n"
      "v_mfma_f32_16x16x32_bf16 v[84:87], a[56:59], a[208:211], v[84:87]\n"
      "s_waitcnt lgkmcnt(6)\n"
      "v_fma_f32 v40, 0.5, v52, -v64\n"
      "v_fma_f32 v41, 0.5, v53, -v65\n"
      "v_mfma_f32_16x16x32_bf16 v[76:79], a[60:63], a[212:215], v[76:79]\n"
      "s_nop 0\n"
      "v_fma_f32 v42, 0.5, v54, -v66\n"
      "v_fma_f32 v43, 0.5, v55, -v67\n"
      "v_mfma_f32_16x16x32_bf16 v[80:83], a[64:67], a[212:215], v[80:83]\n"
      "s_nop 0\n"
      "v_fma_f32 v44, 0.5, v56, -v68\n"
      "v_fma_f32 v45, 0.5, v57, -v69\n"
      "v_mfma_f32_16x16x32_bf16 v[84:87], a[68:71], a[212:215], v[84:87]\n"
      "s_waitcnt lgkmcnt(0)\n"
      "s_barrier\n"
      "s_add_u32 s55, s55, 2\n"
      "s_cmp_lt_u32 s55, 6\n"
      "s_cbranch_scc1 LSloop_%=\n"
      "LSlast_%=:\n"
      "v_mfma_f32_16x16x32_bf16 v[76:79], a[72:75], a[192:195], v[76:79]\n"
      "s_add_u32 s59, s62, 4918272\n"
      "ds_read_b128 a[204:207], v0 offset:16384\n"
      "ds_read_b64_tr_b16 a[220:221], v7 offset:33792\n"
      "v_fma_f32 v46, 0.5, v58, -v70\n"
      "v_mfma_f32_16x16x32_bf16 v[80:83], a[76:79], a[192:195], v[80:83]\n"
      "ds_read_b128 a[208:211], v0 offset:20480\n"
      "ds_read2st64_b32 v[28:29], v6 offset0:0 offset1:2\n"
      "v_mfma_f32_16x16x32_bf16 v[84:87], a[80:83], a[192:195], v[84:87]\n"
      "ds_read_b64_tr_b16 a[222:223], v7 offset:33800\n"
      "ds_read2st64_b32 v[30:31], v6 offset0:4 offset1:6\n"
      "v_mfma_f32_16x16x32_bf16 v[76:79], a[84:87], a[196:199], v[76:79]\n"
      "ds_read_b128 a[212:215], v0 offset:16448\n"
      "v_fma_f32 v47, 0.5, v59, -v71\n"
      "v_mfma_f32_16x16x32_bf16 v[80:83], a[88:91], a[196:199], v[80:83]\n"
      "ds_read2st64_b32 v[32:33], v6 offset0:8 offset1:10\n"
      "ds_read2st64_b32 v[34:35], v6 offset0:12 offset1:14\n"
      "v_fma_f32 v48, 0.5, v60, -v72\n"
      "v_mfma_f32_16x16x32_bf16 v[84:87], a[92:95], a[196:199], v[84:87]\n"
      "ds_read2st64_b32 v[36:37], v6 offset0:16 offset1:18\n"
      "v_fma_f32 v49, 0.5, v61, -v73\n"
      "v_mfma_f32_16x16x32_bf16 v[76:79], a[96:99], a[200:203], v[76:79]\n"
      "ds_read_b128 a[192:195], v0 offset:20544\n"
      "v_fma_f32 v50, 0.5, v62, -v74\n"
      "v_mfma_f32_16x16x32_bf16 v[80:83], a[100:103], a[200:203], v[80:83]\n"
      "ds_read2st64_b32 v[38:39], v6 offset0:20 offset1:22\n"
      "v_fma_f32 v51, 0.5, v63, -v75\n"
      "v_mfma_f32_16x16x32_bf16 v[84:87], a[104:107], a[200:203], v[84:87]\n"
      "s_waitcnt lgkmcnt(7)\n"
      "v_mfma_f32_16x16x32_bf16 v[52:55], v[252:255], a[204:207], v[112:115]\n"
      "s_nop 0\n"
      "ds_read_b128 a[196:199], v0 offset:16512\n"
      "ds_read_b64_tr_b16 a[224:225], v7 offset:34304\n"
      "v_mfma_f32_4x4x4_16b_bf16 v[12:15], a[216:217], a[220:221], 0\n"
      "ds_read_b64_tr_b16 a[226:227], v7 offset:34312\n"
      "v_mfma_f32_4x4x4_16b_bf16 v[16:19], a[216:217], a[222:223], 0\n"
      "v_mfma_f32_16x16x32_bf16 v[56:59], v[244:247], a[204:207], v[116:119]\n"
      "v_mfma_f32_16x16x32_bf16 v[60:63], v[236:239], a[204:207], v[120:123]\n"
      "v_mfma_f32_16x16x32_bf16 v[64:67], v[248:251], a[208:211], v[112:115]\n"
      "ds_read_b128 a[200:203], v0 offset:20608\n"
      "v_mfma_f32_16x16x32_bf16 v[68:71], v[240:243], a[208:211], v[116:119]\n"
      "v_cvt_pk_bf16_f32 v12, v12, v16\n"
      "v_cvt_pk_bf16_f32 v13, v13, v17\n"
      "v_cvt_pk_bf16_f32 v14, v14, v18\n"
      "v_mfma_f32_16x16x32_bf16 v[72:75], v[232:235], a[208:211], v[120:123]\n"
      "s_waitcnt lgkmcnt(2)\n"
      "ds_write_addtid_b32 v12 offset:24832\n"
      "ds_write_addtid_b32 v13 offset:28928\n"
      "v_sub_f32 v28, v28, v40\n"
      "v_sub_f32 v29, v29, v41\n"
      "v_sub_f32 v30, v30, v42\n"
      "v_sub_f32_e64 v31, v31, v43\n"
      "v_mfma_f32_16x16x32_bf16 v[52:55], v[228:231], a[212:215], v[52:55]\n"
      "ds_read_b128 a[204:207], v1 offset:16384\n"
      "ds_write_addtid_b32 v14 offset:33024\n"
      "v_fmac_f32 v28, s46, v100\n"
      "v_fmac_f32 v29, s46, v101\n"
      "v_fmac_f32 v30, s46, v102\n"
      "v_fmac_f32 v31, s46, v103\n"
      "v_mfma_f32_16x16x32_bf16 v[56:59], v[220:223], a[212:215], v[56:59]\n"
      "v_fmac_f32 v40, s45, v28\n"
      "v_fmac_f32 v41, s45, v29\n"
      "v_fmac_f32 v42, s45, v30\n"
      "v_fmac_f32 v43, s45, v31\n"
      "v_fma_f32 v28, s47, v28, v76\n"
      "v_fma_f32 v29, s47, v29, v77\n"
      "v_mfma_f32_16x16x32_bf16 v[60:63], v[212:215], a[212:215], v[60:63]\n"
      "v_cvt_pk_bf16_f32 v15, v15, v19\n"
      "v_fma_f32 v30, s47, v30, v78\n"
      "v_mfma_f32_16x16x32_bf16 v[64:67], v[224:227], a[192:195], v[64:67]\n"
      "ds_read_b128 a[208:211], v1 offset:20480\n"
      "ds_write_addtid_b32 v15 offset:37120\n"
      "v_mfma_f32_16x16x32_bf16 v[68:71], v[216:219], a[192:195], v[68:71]\n"
      "s_nop 0\n"
      "v_fma_f32 v31, s47, v31, v79\n"
      "v_cvt_pk_bf16_f32 v40, v40, v41\n"
      "v_mfma_f32_16x16x32_bf16 v[72:75], v[208:211], a[192:195], v[72:75]\n"
      "s_waitcnt lgkmcnt(2)\n"
      "ds_write_addtid_b32 v40 offset:12288\n"
      "ds_read_b64_tr_b16 a[228:229], v7 offset:34816\n"
      "v_cvt_pk_bf16_f32 v42, v42, v43\n"
      "v_mfma_f32_4x4x4_16b_bf16 v[20:23], a[216:217], a[224:225], 0\n"
      "v_cvt_pk_bf16_f32 v28, v28, v29\n"
      "v_mfma_f32_4x4x4_16b_bf16 v[24:27], a[216:217], a[226:227], 0\n"
      "ds_write_addtid_b32 v42 offset:12800\n"
      "ds_write_addtid_b32 v28 offset:14336\n"
      "v_mfma_f32_16x16x32_bf16 v[52:55], v[204:207], a[196:199], v[52:55]\n"
      "ds_read_b128 a[212:215], v1 offset:16448\n"
      "ds_read_b64_tr_b16 a[230:231], v7 offset:34824\n"
      "v_cvt_pk_bf16_f32 v30, v30, v31\n"
      "v_mfma_f32_16x16x32_bf16 v[56:59], v[196:199], a[196:199], v[56:59]\n"
      "ds_write_addtid_b32 v30 offset:14848\n"
      "v_cvt_pk_bf16_f32 v20, v20, v24\n"
      "v_mfma_f32_16x16x32_bf16 v[60:63], v[188:191], a[196:199], v[60:63]\n"
      "ds_write_addtid_b32 v20 offset:25344\n"
      "v_sub_f32 v32, v32, v44\n"
      "v_mfma_f32_16x16x32_bf16 v[64:67], v[200:203], a[200:203], v[64:67]\n"
      "s_nop 0\n"
      "ds_read_b128 a[192:195], v1 offset:20544\n"
      "v_sub_f32 v33, v33, v45\n"
      "v_mfma_f32_16x16x32_bf16 v[68:71], v[192:195], a[200:203], v[68:71]\n"
      "v_cvt_pk_bf16_f32 v21, v21, v25\n"
      "v_sub_f32 v34, v34, v46\n"
      "v_cvt_pk_bf16_f32 v22, v22, v26\n"
      "v_mfma_f32_16x16x32_bf16 v[72:75], v[184:187], a[200:203], v[72:75]\n"
      "ds_write_addtid_b32 v21 offset:29440\n"
      "ds_write_addtid_b32 v22 offset:33536\n"
      "v_mfma_f32_16x16x32_bf16 v[52:55], v[180:183], a[204:207], v[52:55]\n"
      "ds_read_b128 a[196:199], v1 offset:16512\n"
      "v_sub_f32 v35, v35, v47\n"
      "v_mfma_f32_16x16x32_bf16 v[56:59], v[172:175], a[204:207], v[56:59]\n"
      "v_fmac_f32 v32, s46, v104\n"
      "v_fmac_f32 v33, s46, v105\n"
      "v_cvt_pk_bf16_f32 v23, v23, v27\n"
      "v_mfma_f32_16x16x32_bf16 v[60:63], v[164:167], a[204:207], v[60:63]\n"
      "s_waitcnt lgkmcnt(3)\n"
      "ds_write_addtid_b32 v23 offset:37632\n"
      "v_fmac_f32 v34, s46, v106\n"
      "v_mfma_f32_16x16x32_bf16 v[64:67], v[176:179], a[208:211], v[64:67]\n"
      "ds_read_b128 a[200:203], v1 offset:20608\n"
      "ds_read2st64_b32 a[240:241], v8 offset0:4 offset1:6\n"
      "v_mfma_f32_16x16x32_bf16 v[68:71], v[168:171], a[208:211], v[68:71]\n"
      "s_nop 0\n"
      "ds_read_b64_tr_b16 a[232:233], v7 offset:35328\n"
      "ds_read2st64_b32 a[242:243], v9 offset0:4 offset1:6\n"
      "v_mfma_f32_4x4x4_16b_bf16 v[12:15], a[216:217], a[228:229], 0\n"
      "ds_read_b64_tr_b16 a[234:235], v7 offset:35336\n"
      "v_fmac_f32 v35, s46, v107\n"
      "v_mfma_f32_4x4x4_16b_bf16 v[16:19], a[216:217], a[230:231], 0\n"
      "v_fmac_f32 v44, s45, v32\n"
      "v_fmac_f32 v45, s45, v33\n"
      "v_mfma_f32_16x16x32_bf16 v[72:75], v[160:163], a[208:211], v[72:75]\n"
      "v_fmac_f32 v46, s45, v34\n"
      "v_fmac_f32 v47, s45, v35\n"
      "v_mfma_f32_16x16x32_bf16 v[52:55], v[156:159], a[212:215], v[52:55]\n"
      "s_nop 0\n"
      "ds_read_b128 a[204:207], v2 offset:16384\n"
      "v_cvt_pk_bf16_f32 v12, v12, v16\n"
      "v_cvt_pk_bf16_f32 v13, v13, v17\n"
      "v_mfma_f32_16x16x32_bf16 v[56:59], v[148:151], a[212:215], v[56:59]\n"
      "ds_write_addtid_b32 v12 offset:25856\n"
      "ds_write_addtid_b32 v13 offset:29952\n"
      "v_mfma_f32_16x16x32_bf16 v[60:63], v[140:143], a[212:215], v[60:63]\n"
      "v_fma_f32 v32, s47, v32, v80\n"
      "v_fma_f32 v33, s47, v33, v81\n"
      "v_mfma_f32_16x16x32_bf16 v[64:67], v[152:155], a[192:195], v[64:67]\n"
      "ds_read_b128 a[208:211], v2 offset:20480\n"
      "v_cvt_pk_bf16_f32 v14, v14, v18\n"
      "v_mfma_f32_16x16x32_bf16 v[68:71], v[144:147], a[192:195], v[68:71]\n"
      "ds_write_addtid_b32 v14 offset:34048\n"
      "v_fma_f32 v34, s47, v34, v82\n"
      "v_fma_f32 v35, s47, v35, v83\n"
      "v_mfma_f32_16x16x32_bf16 v[72:75], v[136:139], a[192:195], v[72:75]\n"
      "s_waitcnt lgkmcnt(3)\n"
      "v_cvt_pk_bf16_f32 v15, v15, v19\n"
      "v_cvt_pk_bf16_f32 v44, v44, v45\n"
      "v_mfma_f32_16x16x32_bf16 v[52:55], v[132:135], a[196:199], v[52:55]\n"
      "ds_read_b128 a[212:215], v2 offset:16448\n"
      "ds_write_addtid_b32 v44 offset:20484\n"
      "v_mfma_f32_16x16x32_bf16 v[56:59], v[124:127], a[196:199], v[56:59]\n"
      "s_nop 0\n"
      "ds_write_addtid_b32 v15 offset:38144\n"
      "v_cvt_pk_bf16_f32 v46, v46, v47\n"
      "v_cvt_pk_bf16_f32 v32, v32, v33\n"
      "v_mfma_f32_16x16x32_bf16 v[60:63], a[184:187], a[196:199], v[60:63]\n"
      "ds_write_addtid_b32 v46 offset:20996\n"
      "ds_write_addtid_b32 v32 offset:22532\n"
      "v_mfma_f32_16x16x32_bf16 v[64:67], v[128:131], a[200:203], v[64:67]\n"
      "ds_read_b128 a[192:195], v2 offset:20544\n"
      "ds_read_b64_tr_b16 a[220:221], v7 offset:35840\n"
      "v_mfma_f32_4x4x4_16b_bf16 v[20:23], a[216:217], a[232:233], 0\n"
      "ds_read_b64_tr_b16 a[222:223], v7 offset:35848\n"
      "v_mfma_f32_4x4x4_16b_bf16 v[24:27], a[216:217], a[234:235], 0\n"
      "v_cvt_pk_bf16_f32 v34, v34, v35\n"
      "v_sub_f32 v36, v36, v48\n"
      "v_sub_f32 v37, v37, v49\n"
      "v_mfma_f32_16x16x32_bf16 v[68:71], a[188:191], a[200:203], v[68:71]\n"
      "ds_write_addtid_b32 v34 offset:23044\n"
      "v_cvt_pk_bf16_f32 v20, v20, v24\n"
      "v_mfma_f32_16x16x32_bf16 v[72:75], a[180:183], a[200:203], v[72:75]\n"
      "buffer_store_dwordx4 a[240:243], v11, s[8:11], s59 offen offset:256 sc1\n"
      "v_mfma_f32_16x16x32_bf16 v[52:55], a[176:179], a[204:207], v[52:55]\n"
      "ds_read_b128 a[196:199], v2 offset:16512\n"
      "ds_write_addtid_b32 v20 offset:26368\n"
      "v_mfma_f32_16x16x32_bf16 v[56:59], a[168:171], a[204:207], v[56:59]\n"
      "v_sub_f32 v38, v38, v50\n"
      "v_sub_f32 v39, v39, v51\n"
      "v_cvt_pk_bf16_f32 v21, v21, v25\n"
      "v_mfma_f32_16x16x32_bf16 v[60:63], a[160:163], a[204:207], v[60:63]\n"
      "s_waitcnt lgkmcnt(2)\n"
      "ds_write_addtid_b32 v21 offset:30464\n"
      "v_fmac_f32 v36, s46, v108\n"
      "v_mfma_f32_16x16x32_bf16 v[64:67], a[172:175], a[208:211], v[64:67]\n"
      "ds_read_b128 a[200:203], v2 offset:20608\n"
      "v_cvt_pk_bf16_f32 v22, v22, v26\n"
      "v_mfma_f32_16x16x32_bf16 v[68:71], a[164:167], a[208:211], v[68:71]\n"
      "ds_write_addtid_b32 v22 offset:34560\n"
      "v_fmac_f32 v37, s46, v109\n"
      "v_fmac_f32 v38, s46, v110\n"
      "v_mfma_f32_16x16x32_bf16 v[72:75], a[156:159], a[208:211], v[72:75]\n"
      "v_cvt_pk_bf16_f32 v23, v23, v27\n"
      "v_fmac_f32 v39, s46, v111\n"
      "v_mfma_f32_16x16x32_bf16 v[52:55], a[152:155], a[212:215], v[52:55]\n"
      "ds_read_b128 a[204:207], v3 offset:50688\n"
      "ds_write_addtid_b32 v23 offset:38656\n"
      "v_mfma_f32_16x16x32_bf16 v[56:59], a[144:147], a[212:215], v[56:59]\n"
      "v_fmac_f32 v48, s45, v36\n"
      "v_fmac_f32 v49, s45, v37\n"
      "v_fmac_f32 v50, s45, v38\n"
      "v_mfma_f32_16x16x32_bf16 v[60:63], a[136:139], a[212:215], v[60:63]\n"
      "ds_read_b64_tr_b16 a[224:225], v7 offset:36352\n"
      "ds_read_b64_tr_b16 a[226:227], v7 offset:36360\n"
      "v_mfma_f32_4x4x4_16b_bf16 v[12:15], a[216:217], a[220:221], 0\n"
      "v_fmac_f32_e64 v51, s45, v39\n"
      "v_mfma_f32_4x4x4_16b_bf16 v[16:19], a[216:217], a[222:223], 0\n"
      "v_fma_f32 v36, s47, v36, v84\n"
      "v_fma_f32 v37, s47, v37, v85\n"
      "v_mfma_f32_16x16x32_bf16 v[64:67], a[148:151], a[192:195], v[64:67]\n"
      "ds_read_b128 a[208:211], v3 offset:50752\n"
      "v_fma_f32 v38, s47, v38, v86\n"
      "v_cvt_pk_bf16_f32 v12, v12, v16\n"
      "v_mfma_f32_16x16x32_bf16 v[68:71], a[140:143], a[192:195], v[68:71]\n"
      "ds_write_addtid_b32 v12 offset:26880\n"
      "v_cvt_pk_bf16_f32 v13, v13, v17\n"
      "v_mfma_f32_16x16x32_bf16 v[72:75], a[132:135], a[192:195], v[72:75]\n"
      "s_waitcnt lgkmcnt(3)\n"
      "ds_write_addtid_b32 v13 offset:30976\n"
      "v_fma_f32 v39, s47, v39, v87\n"
      "v_mfma_f32_16x16x32_bf16 v[52:55], a[128:131], a[196:199], v[52:55]\n"
      "ds_read_b128 a[212:215], v3 offset:50816\n"
      "v_cvt_pk_bf16_f32 v14, v14, v18\n"
      "v_mfma_f32_16x16x32_bf16 v[56:59], a[120:123], a[196:199], v[56:59]\n"
      "s_nop 0\n"
      "ds_write_addtid_b32 v14 offset:35072\n"
      "v_cvt_pk_bf16_f32 v48, v48, v49\n"
      "v_cvt_pk_bf16_f32 v15, v15, v19\n"
      "v_mfma_f32_16x16x32_bf16 v[60:63], a[112:115], a[196:199], v[60:63]\n"
      "ds_write_addtid_b32 v48 offset:16384\n"
      "ds_write_addtid_b32 v15 offset:39168\n"
      "v_mfma_f32_16x16x32_bf16 v[64:67], a[124:127], a[200:203], v[64:67]\n"
      "ds_read_b128 a[192:195], v4 offset:50688\n"
      "v_cvt_pk_bf16_f32 v50, v50, v51\n"
      "v_mfma_f32_16x16x32_bf16 v[68:71], a[116:119], a[200:203], v[68:71]\n"
      "ds_write_addtid_b32 v50 offset:16896\n"
      "v_cvt_pk_bf16_f32 v36, v36, v37\n"
      "v_cvt_pk_bf16_f32 v38, v38, v39\n"
      "v_mfma_f32_16x16x32_bf16 v[72:75], a[108:111], a[200:203], v[72:75]\n"
      "ds_write_addtid_b32 v36 offset:18432\n"
      "ds_write_addtid_b32 v38 offset:18944\n"
      "v_mfma_f32_16x16x32_bf16 v[88:91], a[0:3], a[204:207], 0\n"
      "ds_read_b128 a[196:199], v4 offset:50752\n"
      "v_sub_f32 v52, v64, v52\n"
      "v_mfma_f32_16x16x32_bf16 v[92:95], a[4:7], a[204:207], 0\n"
      "s_waitcnt lgkmcnt(7)\n"
      "ds_write_addtid_b32 v52 offset:6144\n"
      "v_sub_f32 v53, v65, v53\n"
      "v_mfma_f32_4x4x4_16b_bf16 v[20:23], a[216:217], a[224:225], 0\n"
      "v_sub_f32 v54, v66, v54\n"
      "v_sub_f32_e64 v55, v67, v55\n"
      "v_mfma_f32_4x4x4_16b_bf16 v[24:27], a[216:217], a[226:227], 0\n"
      "ds_write_addtid_b32 v53 offset:6656\n"
      "ds_write_addtid_b32 v54 offset:7168\n"
      "v_mfma_f32_16x16x32_bf16 v[96:99], a[8:11], a[204:207], 0\n"
      "ds_write_addtid_b32 v55 offset:7680\n"
      "v_sub_f32 v56, v68, v56\n"
      "v_mfma_f32_16x16x32_bf16 v[88:91], a[12:15], a[208:211], v[88:91]\n"
      "s_nop 0\n"
      "ds_read_b128 a[200:203], v4 offset:50816\n"
      "ds_write_addtid_b32 v56 offset:8192\n"
      "v_sub_f32 v57, v69, v57\n"
      "v_mfma_f32_16x16x32_bf16 v[92:95], a[16:19], a[208:211], v[92:95]\n"
      "ds_write_addtid_b32 v57 offset:8704\n"
      "v_cvt_pk_bf16_f32 v20, v20, v24\n"
      "v_mfma_f32_16x16x32_bf16 v[96:99], a[20:23], a[208:211], v[96:99]\n"
      "ds_write_addtid_b32 v20 offset:27392\n"
      "v_sub_f32 v58, v70, v58\n"
      "v_mfma_f32_16x16x32_bf16 v[88:91], a[24:27], a[212:215], v[88:91]\n"
      "ds_read_b128 a[204:207], v5 offset:50688\n"
      "ds_write_addtid_b32 v58 offset:9216\n"
      "v_mfma_f32_16x16x32_bf16 v[92:95], a[28:31], a[212:215], v[92:95]\n"
      "v_sub_f32 v59, v71, v59\n"
      "v_cvt_pk_bf16_f32 v21, v21, v25\n"
      "v_sub_f32_e64 v60, v72, v60\n"
      "v_mfma_f32_16x16x32_bf16 v[96:99], a[32:35], a[212:215], v[96:99]\n"
      "s_waitcnt lgkmcnt(5)\n"
      "ds_write_addtid_b32 v21 offset:31488\n"
      "ds_write_addtid_b32 v59 offset:9728\n"
      "v_mfma_f32_16x16x32_bf16 v[88:91], a[36:39], a[192:195], v[88:91]\n"
      "ds_read_b128 a[208:211], v5 offset:50752\n"
      "ds_write_addtid_b32 v60 offset:10240\n"
      "v_mfma_f32_16x16x32_bf16 v[92:95], a[40:43], a[192:195], v[92:95]\n"
      "v_cvt_pk_bf16_f32 v22, v22, v26\n"
      "v_cvt_pk_bf16_f32 v23, v23, v27\n"
      "v_sub_f32_e64 v61, v73, v61\n"
      "v_mfma_f32_16x16x32_bf16 v[96:99], a[44:47], a[192:195], v[96:99]\n"
      "ds_write_addtid_b32 v22 offset:35584\n"
      "ds_write_addtid_b32 v23 offset:39680\n"
      "v_sub_f32 v62, v74, v62\n"
      "v_sub_f32 v63, v75, v63\n"
      "v_mfma_f32_16x16x32_bf16 v[88:91], a[48:51], a[196:199], v[88:91]\n"
      "ds_read_b128 a[212:215], v5 offset:50816\n"
      "ds_write_addtid_b32 v61 offset:10752\n"
      "v_mfma_f32_16x16x32_bf16 v[92:95], a[52:55], a[196:199], v[92:95]\n"
      "ds_write_addtid_b32 v62 offset:11264\n"
      "ds_write_addtid_b32 v63 offset:11776\n"
      "v_mfma_f32_16x16x32_bf16 v[96:99], a[56:59], a[196:199], v[96:99]\n"
      "v_fma_f32 v40, 0.5, v52, -v64\n"
      "v_fma_f32 v41, 0.5, v53, -v65\n"
      "v_mfma_f32_16x16x32_bf16 v[88:91], a[60:63], a[200:203], v[88:91]\n"
      "v_fma_f32 v42, 0.5, v54, -v66\n"
      "v_fma_f32 v43, 0.5, v55, -v67\n"
      "v_mfma_f32_16x16x32_bf16 v[92:95], a[64:67], a[200:203], v[92:95]\n"
      "v_fma_f32 v44, 0.5, v56, -v68\n"
      "v_fma_f32 v45, 0.5, v57, -v69\n"
      "v_mfma_f32_16x16x32_bf16 v[96:99], a[68:71], a[200:203], v[96:99]\n"
      "s_waitcnt lgkmcnt(0)\n"
      "s_barrier\n"
      "v_mfma_f32_16x16x32_bf16 v[88:91], a[72:75], a[204:207], v[88:91]\n"
      "s_add_u32 s59, s62, 5701632\n"
      "ds_read_b128 a[192:195], v0 offset:0\n"
      "ds_read_b64_tr_b16 a[220:221], v7 offset:50688\n"
      "v_fma_f32 v46, 0.5, v58, -v70\n"
      "v_mfma_f32_16x16x32_bf16 v[92:95], a[76:79], a[204:207], v[92:95]\n"
      "ds_read_b128 a[196:199], v0 offset:4096\n"
      "ds_read2st64_b32 v[28:29], v6 offset0:24 offset1:26\n"
      "v_mfma_f32_16x16x32_bf16 v[96:99], a[80:83], a[204:207], v[96:99]\n"
      "ds_read_b64_tr_b16 a[222:223], v7 offset:50696\n"
      "ds_read2st64_b32 v[30:31], v6 offset0:28 offset1:30\n"
      "v_mfma_f32_16x16x32_bf16 v[88:91], a[84:87], a[208:211], v[88:91]\n"
      "ds_read_b128 a[200:203], v0 offset:64\n"
      "v_fma_f32 v47, 0.5, v59, -v71\n"
      "v_mfma_f32_16x16x32_bf16 v[92:95], a[88:91], a[208:211], v[92:95]\n"
      "ds_read2st64_b32 v[32:33], v6 offset0:32 offset1:34\n"
      "ds_read2st64_b32 v[34:35], v6 offset0:36 offset1:38\n"
      "v_fma_f32 v48, 0.5, v60, -v72\n"
      "v_mfma_f32_16x16x32_bf16 v[96:99], a[92:95], a[208:211], v[96:99]\n"
      "ds_read2st64_b32 v[36:37], v6 offset0:40 offset1:42\n"
      "v_fma_f32 v49, 0.5, v61, -v73\n"
      "v_mfma_f32_16x16x32_bf16 v[88:91], a[96:99], a[212:215], v[88:91]\n"
      "ds_read_b128 a[204:207], v0 offset:4160\n"
      "v_fma_f32 v50, 0.5, v62, -v74\n"
      "v_mfma_f32_16x16x32_bf16 v[92:95], a[100:103], a[212:215], v[92:95]\n"
      "ds_read2st64_b32 v[38:39], v6 offset0:44 offset1:46\n"
      "v_fma_f32 v51, 0.5, v63, -v75\n"
      "v_add_u32 v3, 67840, v3\n"
      "v_mfma_f32_16x16x32_bf16 v[96:99], a[104:107], a[212:215], v[96:99]\n"
      "s_waitcnt lgkmcnt(7)\n"
      "v_add_u32 v4, 67840, v4\n"
      "v_add_u32 v5, 67840, v5\n"
      "v_mfma_f32_16x16x32_bf16 v[52:55], v[252:255], a[192:195], v[76:79]\n"
      "s_nop 0\n"
      "ds_read_b128 a[208:211], v0 offset:128\n"
      "ds_read_b64_tr_b16 a[224:225], v7 offset:51200\n"
      "v_mfma_f32_4x4x4_16b_bf16 v[12:15], a[216:217], a[220:221], 0\n"
      "ds_read_b64_tr_b16 a[226:227], v7 offset:51208\n"
      "v_mfma_f32_4x4x4_16b_bf16 v[16:19], a[216:217], a[222:223], 0\n"
      "v_mfma_f32_16x16x32_bf16 v[56:59], v[244:247], a[192:195], v[80:83]\n"
      "v_mfma_f32_16x16x32_bf16 v[60:63], v[236:239], a[192:195], v[84:87]\n"
      "v_mfma_f32_16x16x32_bf16 v[64:67], v[248:251], a[196:199], v[76:79]\n"
      "ds_read_b128 a[212:215], v0 offset:4224\n"
      "v_mfma_f32_16x16x32_bf16 v[68:71], v[240:243], a[196:199], v[80:83]\n"
      "v_cvt_pk_bf16_f32 v12, v12, v16\n"
      "v_cvt_pk_bf16_f32 v13, v13, v17\n"
      "v_cvt_pk_bf16_f32 v14, v14, v18\n"
      "v_mfma_f32_16x16x32_bf16 v[72:75], v[232:235], a[196:199], v[84:87]\n"
      "s_waitcnt lgkmcnt(1)\n"
      "ds_write_addtid_b32 v12 offset:41216\n"
      "ds_write_addtid_b32 v13 offset:45312\n"
      "v_sub_f32 v28, v28, v40\n"
      "v_sub_f32 v29, v29, v41\n"
      "v_sub_f32 v30, v30, v42\n"
      "v_sub_f32 v31, v31, v43\n"
      "v_mfma_f32_16x16x32_bf16 v[52:55], v[228:231], a[200:203], v[52:55]\n"
      "ds_read_b128 a[192:195], v1 offset:0\n"
      "ds_write_addtid_b32 v14 offset:49408\n"
      "v_fmac_f32 v28, s46, v112\n"
      "v_fmac_f32 v29, s46, v113\n"
      "v_fmac_f32 v30, s46, v114\n"
      "v_fmac_f32 v31, s46, v115\n"
      "v_mfma_f32_16x16x32_bf16 v[56:59], v[220:223], a[200:203], v[56:59]\n"
      "v_fmac_f32 v40, s45, v28\n"
      "v_fmac_f32 v41, s45, v29\n"
      "v_fmac_f32 v42, s45, v30\n"
      "v_mfma_f32_16x16x32_bf16 v[60:63], v[212:215], a[200:203], v[60:63]\n"
      "v_fmac_f32 v43, s45, v31\n"
      "v_cvt_pk_bf16_f32 v15, v15, v19\n"
      "v_fma_f32 v28, s47, v28, v88\n"
      "v_mfma_f32_16x16x32_bf16 v[64:67], v[224:227], a[204:207], v[64:67]\n"
      "ds_read_b128 a[196:199], v1 offset:4096\n"
      "ds_write_addtid_b32 v15 offset:53504\n"
      "v_mfma_f32_16x16x32_bf16 v[68:71], v[216:219], a[204:207], v[68:71]\n"
      "s_nop 0\n"
      "v_fma_f32 v29, s47, v29, v89\n"
      "v_fma_f32 v30, s47, v30, v90\n"
      "v_mfma_f32_16x16x32_bf16 v[72:75], v[208:211], a[204:207], v[72:75]\n"
      "ds_read_b64_tr_b16 a[228:229], v7 offset:51712\n"
      "ds_read_b64_tr_b16 a[230:231], v7 offset:51720\n"
      "v_fma_f32 v31, s47, v31, v91\n"
      "v_mfma_f32_4x4x4_16b_bf16 v[20:23], a[216:217], a[224:225], 0\n"
      "v_cvt_pk_bf16_f32 v40, v40, v41\n"
      "v_mfma_f32_4x4x4_16b_bf16 v[24:27], a[216:217], a[226:227], 0\n"
      "ds_write_addtid_b32 v40 offset:13312\n"
      "v_cvt_pk_bf16_f32 v42, v42, v43\n"
      "v_mfma_f32_16x16x32_bf16 v[52:55], v[204:207], a[208:211], v[52:55]\n"
      "ds_read_b128 a[200:203], v1 offset:64\n"
      "ds_write_addtid_b32 v42 offset:13824\n"
      "v_mfma_f32_16x16x32_bf16 v[56:59], v[196:199], a[208:211], v[56:59]\n"
      "v_cvt_pk_bf16_f32 v20, v20, v24\n"
      "v_cvt_pk_bf16_f32 v28, v28, v29\n"
      "v_cvt_pk_bf16_f32 v21, v21, v25\n"
      "v_mfma_f32_16x16x32_bf16 v[60:63], v[188:191], a[208:211], v[60:63]\n"
      "s_waitcnt lgkmcnt(3)\n"
      "ds_write_addtid_b32 v28 offset:15360\n"
      "ds_write_addtid_b32 v20 offset:41728\n"
      "v_mfma_f32_16x16x32_bf16 v[64:67], v[200:203], a[212:215], v[64:67]\n"
      "s_nop 0\n"
      "ds_read_b128 a[204:207], v1 offset:4160\n"
      "ds_write_addtid_b32 v21 offset:45824\n"
      "v_mfma_f32_16x16x32_bf16 v[68:71], v[192:195], a[212:215], v[68:71]\n"
      "v_cvt_pk_bf16_f32 v30, v30, v31\n"
      "v_sub_f32 v32, v32, v44\n"
      "v_sub_f32 v33, v33, v45\n"
      "v_mfma_f32_16x16x32_bf16 v[72:75], v[184:187], a[212:215], v[72:75]\n"
      "ds_write_addtid_b32 v30 offset:15872\n"
      "v_cvt_pk_bf16_f32 v22, v22, v26\n"
      "v_mfma_f32_16x16x32_bf16 v[52:55], v[180:183], a[192:195], v[52:55]\n"
      "ds_read_b128 a[208:211], v1 offset:128\n"
      "ds_write_addtid_b32 v22 offset:49920\n"
      "v_mfma_f32_16x16x32_bf16 v[56:59], v[172:175], a[192:195], v[56:59]\n"
      "v_sub_f32 v34, v34, v46\n"
      "v_sub_f32 v35, v35, v47\n"
      "v_cvt_pk_bf16_f32 v23, v23, v27\n"
      "v_mfma_f32_16x16x32_bf16 v[60:63], v[164:167], a[192:195], v[60:63]\n"
      "ds_write_addtid_b32 v23 offset:54016\n"
      "v_fmac_f32_e64 v32, s46, v116\n"
      "v_mfma_f32_16x16x32_bf16 v[64:67], v[176:179], a[196:199], v[64:67]\n"
      "ds_read_b128 a[212:215], v1 offset:4224\n"
      "ds_read2st64_b32 a[240:241], v8 offset0:0 offset1:2\n"
      "v_mfma_f32_16x16x32_bf16 v[68:71], v[168:171], a[196:199], v[68:71]\n"
      "ds_read_b64_tr_b16 a[232:233], v7 offset:52224\n"
      "ds_read2st64_b32 a[242:243], v9 offset0:0 offset1:2\n"
      "v_fmac_f32 v33, s46, v117\n"
      "v_mfma_f32_4x4x4_16b_bf16 v[12:15], a[216:217], a[228:229], 0\n"
      "v_fmac_f32 v34, s46, v118\n"
      "v_mfma_f32_4x4x4_16b_bf16 v[16:19], a[216:217], a[230:231], 0\n"
      "ds_read_b64_tr_b16 a[234:235], v7 offset:52232\n"
      "v_fmac_f32 v35, s46, v119\n"
      "v_mfma_f32_16x16x32_bf16 v[72:75], v[160:163], a[196:199], v[72:75]\n"
      "s_waitcnt lgkmcnt(5)\n"
      "v_fmac_f32 v44, s45, v32\n"
      "v_fmac_f32 v45, s45, v33\n"
      "v_cvt_pk_bf16_f32 v12, v12, v16\n"
      "v_mfma_f32_16x16x32_bf16 v[52:55], v[156:159], a[200:203], v[52:55]\n"
      "ds_read_b128 a[192:195], v2 offset:0\n"
      "ds_write_addtid_b32 v12 offset:42240\n"
      "v_mfma_f32_16x16x32_bf16 v[56:59], v[148:151], a[200:203], v[56:59]\n"
      "v_fmac_f32 v46, s45, v34\n"
      "v_cvt_pk_bf16_f32 v13, v13, v17\n"
      "v_mfma_f32_16x16x32_bf16 v[60:63], v[140:143], a[200:203], v[60:63]\n"
      "ds_write_addtid_b32 v13 offset:46336\n"
      "v_fmac_f32 v47, s45, v35\n"
      "v_fma_f32 v32, s47, v32, v92\n"
      "v_mfma_f32_16x16x32_bf16 v[64:67], v[152:155], a[204:207], v[64:67]\n"
      "ds_read_b128 a[196:199], v2 offset:4096\n"
      "v_cvt_pk_bf16_f32 v14, v14, v18\n"
      "v_mfma_f32_16x16x32_bf16 v[68:71], v[144:147], a[204:207], v[68:71]\n"
      "ds_write_addtid_b32 v14 offset:50432\n"
      "v_fma_f32 v33, s47, v33, v93\n"
      "v_mfma_f32_16x16x32_bf16 v[72:75], v[136:139], a[204:207], v[72:75]\n"
      "v_fma_f32 v34, s47, v34, v94\n"
      "v_cvt_pk_bf16_f32 v15, v15, v19\n"
      "v_fma_f32 v35, s47, v35, v95\n"
      "v_mfma_f32_16x16x32_bf16 v[52:55], v[132:135], a[208:211], v[52:55]\n"
      "ds_read_b128 a[200:203], v2 offset:64\n"
      "ds_write_addtid_b32 v15 offset:54528\n"
      "v_mfma_f32_16x16x32_bf16 v[56:59], v[124:127], a[208:211], v[56:59]\n"
      "v_cvt_pk_bf16_f32 v44, v44, v45\n"
      "v_cvt_pk_bf16_f32 v46, v46, v47\n"
      "v_mfma_f32_16x16x32_bf16 v[60:63], a[184:187], a[208:211], v[60:63]\n"
      "s_waitcnt lgkmcnt(2)\n"
      "ds_write_addtid_b32 v44 offset:21508\n"
      "ds_write_addtid_b32 v46 offset:22020\n"
      "v_cvt_pk_bf16_f32 v32, v32, v33\n"
      "v_mfma_f32_16x16x32_bf16 v[64:67], v[128:131], a[212:215], v[64:67]\n"
      "s_nop 0\n"
      "ds_read_b128 a[204:207], v2 offset:4160\n"
      "ds_write_addtid_b32 v32 offset:23556\n"
      "v_mfma_f32_4x4x4_16b_bf16 v[20:23], a[216:217], a[232:233], 0\n"
      "ds_read_b64_tr_b16 a[220:221], v7 offset:52736\n"
      "v_mfma_f32_4x4x4_16b_bf16 v[24:27], a[216:217], a[234:235], 0\n"
      "ds_read_b64_tr_b16 a[222:223], v7 offset:52744\n"
      "v_cvt_pk_bf16_f32 v34, v34, v35\n"
      "v_mfma_f32_16x16x32_bf16 v[68:71], a[188:191], a[212:215], v[68:71]\n"
      "ds_write_addtid_b32 v34 offset:24068\n"
      "v_sub_f32_e64 v36, v36, v48\n"
      "v_cvt_pk_bf16_f32 v20, v20, v24\n"
      "v_mfma_f32_16x16x32_bf16 v[72:75], a[180:183], a[212:215], v[72:75]\n"
      "buffer_store_dwordx4 a[240:243], v11, s[8:11], s59 offen offset:256 sc1\n"
      "v_mfma_f32_16x16x32_bf16 v[52:55], a[176:179], a[192:195], v[52:55]\n"
      "ds_read_b128 a[208:211], v2 offset:128\n"
      "ds_write_addtid_b32 v20 offset:42752\n"
      "v_mfma_f32_16x16x32_bf16 v[56:59], a[168:171], a[192:195], v[56:59]\n"
      "v_sub_f32 v37, v37, v49\n"
      "v_sub_f32 v38, v38, v50\n"
      "v_cvt_pk_bf16_f32 v21, v21, v25\n"
      "v_mfma_f32_16x16x32_bf16 v[60:63], a[160:163], a[192:195], v[60:63]\n"
      "ds_write_addtid_b32 v21 offset:46848\n"
      "v_sub_f32 v39, v39, v51\n"
      "v_mfma_f32_16x16x32_bf16 v[64:67], a[172:175], a[196:199], v[64:67]\n"
      "ds_read_b128 a[212:215], v2 offset:4224\n"
      "v_cvt_pk_bf16_f32 v22, v22, v26\n"
      "v_mfma_f32_16x16x32_bf16 v[68:71], a[164:167], a[196:199], v[68:71]\n"
      "ds_write_addtid_b32 v22 offset:50944\n"
      "v_fmac_f32 v36, s46, v120\n"
      "v_fmac_f32 v37, s46, v121\n"
      "v_mfma_f32_16x16x32_bf16 v[72:75], a[156:159], a[196:199], v[72:75]\n"
      "s_waitcnt lgkmcnt(3)\n"
      "v_cvt_pk_bf16_f32 v23, v23, v27\n"
      "v_fmac_f32 v38, s46, v122\n"
      "v_mfma_f32_16x16x32_bf16 v[52:55], a[152:155], a[200:203], v[52:55]\n"
      "ds_read_b128 a[192:195], v3 offset:0\n"
      "ds_write_addtid_b32 v23 offset:55040\n"
      "v_mfma_f32_16x16x32_bf16 v[56:59], a[144:147], a[200:203], v[56:59]\n"
      "v_fmac_f32 v39, s46, v123\n"
      "v_fmac_f32 v48, s45, v36\n"
      "v_fmac_f32 v49, s45, v37\n"
      "v_mfma_f32_16x16x32_bf16 v[60:63], a[136:139], a[200:203], v[60:63]\n"
      "ds_read_b64_tr_b16 a[224:225], v7 offset:53248\n"
      "ds_read_b64_tr_b16 a[226:227], v7 offset:53256\n"
      "v_mfma_f32_4x4x4_16b_bf16 v[12:15], a[216:217], a[220:221], 0\n"
      "v_fmac_f32 v50, s45, v38\n"
      "v_mfma_f32_4x4x4_16b_bf16 v[16:19], a[216:217], a[222:223], 0\n"
      "v_fmac_f32 v51, s45, v39\n"
      "v_fma_f32 v36, s47, v36, v96\n"
      "v_mfma_f32_16x16x32_bf16 v[64:67], a[148:151], a[204:207], v[64:67]\n"
      "ds_read_b128 a[196:199], v3 offset:64\n"
      "v_fma_f32 v37, s47, v37, v97\n"
      "v_cvt_pk_bf16_f32 v12, v12, v16\n"
      "v_mfma_f32_16x16x32_bf16 v[68:71], a[140:143], a[204:207], v[68:71]\n"
      "ds_write_addtid_b32 v12 offset:43264\n"
      "v_cvt_pk_bf16_f32 v13, v13, v17\n"
      "v_mfma_f32_16x16x32_bf16 v[72:75], a[132:135], a[204:207], v[72:75]\n"
      "ds_write_addtid_b32 v13 offset:47360\n"
      "v_fma_f32 v38, s47, v38, v98\n"
      "v_mfma_f32_16x16x32_bf16 v[52:55], a[128:131], a[208:211], v[52:55]\n"
      "ds_read_b128 a[200:203], v3 offset:128\n"
      "v_cvt_pk_bf16_f32 v14, v14, v18\n"
      "v_fma_f32 v39, s47, v39, v99\n"
      "v_mfma_f32_16x16x32_bf16 v[56:59], a[120:123], a[208:211], v[56:59]\n"
      "ds_write_addtid_b32 v14 offset:51456\n"
      "v_cvt_pk_bf16_f32 v48, v48, v49\n"
      "v_mfma_f32_16x16x32_bf16 v[60:63], a[112:115], a[208:211], v[60:63]\n"
      "s_waitcnt lgkmcnt(4)\n"
      "ds_write_addtid_b32 v48 offset:17408\n"
      "v_cvt_pk_bf16_f32 v15, v15, v19\n"
      "v_mfma_f32_16x16x32_bf16 v[64:67], a[124:127], a[212:215], v[64:67]\n"
      "s_nop 0\n"
      "ds_read_b128 a[204:207], v4 offset:0\n"
      "ds_write_addtid_b32 v15 offset:55552\n"
      "v_cvt_pk_bf16_f32 v50, v50, v51\n"
      "v_mfma_f32_16x16x32_bf16 v[68:71], a[116:119], a[212:215], v[68:71]\n"
      "ds_write_addtid_b32 v50 offset:17920\n"
      "v_cvt_pk_bf16_f32 v36, v36, v37\n"
      "v_mfma_f32_16x16x32_bf16 v[72:75], a[108:111], a[212:215], v[72:75]\n"
      "ds_write_addtid_b32 v36 offset:19456\n"
      "v_sub_f32 v52, v64, v52\n"
      "v_mfma_f32_16x16x32_bf16 v[100:103], a[0:3], a[192:195], 0\n"
      "s_nop 0\n"
      "ds_read_b128 a[208:211], v4 offset:64\n"
      "ds_write_addtid_b32 v52 offset:0\n"
      "v_sub_f32 v53, v65, v53\n"
      "v_mfma_f32_16x16x32_bf16 v[104:107], a[4:7], a[192:195], 0\n"
      "ds_write_addtid_b32 v53 offset:512\n"
      "v_sub_f32 v54, v66, v54\n"
      "v_mfma_f32_4x4x4_16b_bf16 v[20:23], a[216:217], a[224:225], 0\n"
      "v_sub_f32_e64 v55, v67, v55\n"
      "v_mfma_f32_4x4x4_16b_bf16 v[24:27], a[216:217], a[226:227], 0\n"
      "ds_write_addtid_b32 v54 offset:1024\n"
      "ds_write_addtid_b32 v55 offset:1536\n"
      "v_sub_f32 v56, v68, v56\n"
      "v_mfma_f32_16x16x32_bf16 v[108:111], a[8:11], a[192:195], 0\n"
      "ds_write_addtid_b32 v56 offset:2048\n"
      "v_sub_f32 v57, v69, v57\n"
      "v_mfma_f32_16x16x32_bf16 v[100:103], a[12:15], a[196:199], v[100:103]\n"
      "ds_read_b128 a[212:215], v4 offset:128\n"
      "ds_write_addtid_b32 v57 offset:2560\n"
      "v_mfma_f32_16x16x32_bf16 v[104:107], a[16:19], a[196:199], v[104:107]\n"
      "v_cvt_pk_bf16_f32 v20, v20, v24\n"
      "v_sub_f32 v58, v70, v58\n"
      "v_mfma_f32_16x16x32_bf16 v[108:111], a[20:23], a[196:199], v[108:111]\n"
      "s_waitcnt lgkmcnt(5)\n"
      "ds_write_addtid_b32 v20 offset:43776\n"
      "ds_write_addtid_b32 v58 offset:3072\n"
      "v_cvt_pk_bf16_f32 v21, v21, v25\n"
      "v_mfma_f32_16x16x32_bf16 v[100:103], a[24:27], a[200:203], v[100:103]\n"
      "ds_read_b128 a[192:195], v5 offset:0\n"
      "ds_write_addtid_b32 v21 offset:47872\n"
      "v_mfma_f32_16x16x32_bf16 v[104:107], a[28:31], a[200:203], v[104:107]\n"
      "v_sub_f32 v59, v71, v59\n"
      "v_sub_f32 v60, v72, v60\n"
      "v_mfma_f32_16x16x32_bf16 v[108:111], a[32:35], a[200:203], v[108:111]\n"
      "ds_write_addtid_b32 v59 offset:3584\n"
      "ds_write_addtid_b32 v60 offset:4096\n"
      "v_cvt_pk_bf16_f32 v22, v22, v26\n"
      "v_mfma_f32_16x16x32_bf16 v[100:103], a[36:39], a[204:207], v[100:103]\n"
      "ds_read_b128 a[196:199], v5 offset:64\n"
      "ds_write_addtid_b32 v22 offset:51968\n"
      "v_cvt_pk_bf16_f32 v38, v38, v39\n"
      "v_mfma_f32_16x16x32_bf16 v[104:107], a[40:43], a[204:207], v[104:107]\n"
      "ds_write_addtid_b32 v38 offset:19968\n"
      "v_cvt_pk_bf16_f32 v23, v23, v27\n"
      "v_mfma_f32_16x16x32_bf16 v[108:111], a[44:47], a[204:207], v[108:111]\n"
      "ds_write_addtid_b32 v23 offset:56064\n"
      "v_sub_f32 v61, v73, v61\n"
      "v_sub_f32 v62, v74, v62\n"
      "v_sub_f32 v63, v75, v63\n"
      "v_mfma_f32_16x16x32_bf16 v[100:103], a[48:51], a[208:211], v[100:103]\n"
      "ds_read_b128 a[200:203], v5 offset:128\n"
      "ds_write_addtid_b32 v61 offset:4608\n"
      "v_mfma_f32_16x16x32_bf16 v[104:107], a[52:55], a[208:211], v[104:107]\n"
      "s_nop 0\n"
      "ds_write_addtid_b32 v62 offset:5120\n"
      "ds_write_addtid_b32 v63 offset:5632\n"
      "v_mfma_f32_16x16x32_bf16 v[108:111], a[56:59], a[208:211], v[108:111]\n"
      "s_waitcnt lgkmcnt(5)\n"
      "v_fma_f32 v40, 0.5, v52, -v64\n"
      "v_fma_f32 v41, 0.5, v53, -v65\n"
      "v_mfma_f32_16x16x32_bf16 v[100:103], a[60:63], a[212:215], v[100:103]\n"
      "s_nop 0\n"
      "v_fma_f32 v42, 0.5, v54, -v66\n"
      "v_fma_f32 v43, 0.5, v55, -v67\n"
      "v_mfma_f32_16x16x32_bf16 v[104:107], a[64:67], a[212:215], v[104:107]\n"
      "v_fma_f32 v44, 0.5, v56, -v68\n"
      "v_fma_f32 v45, 0.5, v57, -v69\n"
      "v_mfma_f32_16x16x32_bf16 v[108:111], a[68:71], a[212:215], v[108:111]\n"
      "s_waitcnt lgkmcnt(0)\n"
      "s_barrier\n"
      "v_mfma_f32_16x16x32_bf16 v[100:103], a[72:75], a[192:195], v[100:103]\n"
      "s_add_u32 s59, s62, 5704704\n"
      "ds_read_b128 a[204:207], v0 offset:16384\n"
      "ds_read2st64_b32 v[28:29], v6 offset0:0 offset1:2\n"
      "v_fma_f32 v46, 0.5, v58, -v70\n"
      "v_mfma_f32_16x16x32_bf16 v[104:107], a[76:79], a[192:195], v[104:107]\n"
      "ds_read_b128 a[208:211], v0 offset:20480\n"
      "ds_read2st64_b32 v[30:31], v6 offset0:4 offset1:6\n"
      "v_mfma_f32_16x16x32_bf16 v[108:111], a[80:83], a[192:195], v[108:111]\n"
      "ds_read2st64_b32 v[32:33], v6 offset0:8 offset1:10\n"
      "v_fma_f32 v47, 0.5, v59, -v71\n"
      "v_mfma_f32_16x16x32_bf16 v[100:103], a[84:87], a[196:199], v[100:103]\n"
      "s_nop 0\n"
      "ds_read_b128 a[212:215], v0 offset:16448\n"
      "v_fma_f32 v48, 0.5, v60, -v72\n"
      "v_mfma_f32_16x16x32_bf16 v[104:107], a[88:91], a[196:199], v[104:107]\n"
      "s_nop 0\n"
      "ds_read2st64_b32 v[34:35], v6 offset0:12 offset1:14\n"
      "ds_read2st64_b32 v[36:37], v6 offset0:16 offset1:18\n"
      "v_mfma_f32_16x16x32_bf16 v[108:111], a[92:95], a[196:199], v[108:111]\n"
      "s_nop 0\n"
      "v_bfe_u32 v16, v11, 6, 1\n"
      "v_mul_i32_i24 v17, 12296, v16\n"
      "s_mov_b32 s50, -8200\n"
      "v_add3_u32 v12, v8, v17, s50\n"
      "v_add_u32 v14, 128, v12\n"
      "v_mul_i32_i24 v17, 8, v16\n"
      "s_mov_b32 s50, -4104\n"
      "v_add3_u32 v13, v8, v17, s50\n"
      "v_add_u32 v15, 128, v13\n"
      "v_mfma_f32_16x16x32_bf16 v[100:103], a[96:99], a[200:203], v[100:103]\n"
      "ds_read_b128 a[192:195], v0 offset:20544\n"
      "ds_read2st64_b32 a[232:233], v12 offset0:4 offset1:6\n"
      "v_mfma_f32_16x16x32_bf16 v[104:107], a[100:103], a[200:203], v[104:107]\n"
      "ds_read2st64_b32 a[234:235], v14 offset0:4 offset1:6\n"
      "ds_read2st64_b32 v[38:39], v6 offset0:20 offset1:22\n"
      "v_fma_f32 v49, 0.5, v61, -v73\n"
      "v_fma_f32 v50, 0.5, v62, -v74\n"
      "v_fma_f32 v51, 0.5, v63, -v75\n"
      "v_mfma_f32_16x16x32_bf16 v[108:111], a[104:107], a[200:203], v[108:111]\n"
      "s_waitcnt lgkmcnt(6)\n"
      "v_sub_f32 v28, v28, v40\n"
      "v_sub_f32 v29, v29, v41\n"
      "v_mfma_f32_16x16x32_bf16 v[52:55], v[252:255], a[204:207], v[88:91]\n"
      "ds_read_b128 a[196:199], v0 offset:16512\n"
      "v_sub_f32 v30, v30, v42\n"
      "v_mfma_f32_16x16x32_bf16 v[56:59], v[244:247], a[204:207], v[92:95]\n"
      "v_sub_f32 v31, v31, v43\n"
      "v_fmac_f32 v28, s46, v76\n"
      "v_mfma_f32_16x16x32_bf16 v[60:63], v[236:239], a[204:207], v[96:99]\n"
      "v_fmac_f32 v29, s46, v77\n"
      "v_fmac_f32 v30, s46, v78\n"
      "v_mfma_f32_16x16x32_bf16 v[64:67], v[248:251], a[208:211], v[88:91]\n"
      "ds_read_b128 a[200:203], v0 offset:20608\n"
      "v_fmac_f32 v31, s46, v79\n"
      "v_mfma_f32_16x16x32_bf16 v[68:71], v[240:243], a[208:211], v[92:95]\n"
      "v_fmac_f32 v40, s45, v28\n"
      "v_fmac_f32_e64 v41, s45, v29\n"
      "v_mfma_f32_16x16x32_bf16 v[72:75], v[232:235], a[208:211], v[96:99]\n"
      "v_fmac_f32 v42, s45, v30\n"
      "v_fmac_f32 v43, s45, v31\n"
      "v_mfma_f32_16x16x32_bf16 v[52:55], v[228:231], a[212:215], v[52:55]\n"
      "ds_read_b128 a[204:207], v1 offset:16384\n"
      "v_fma_f32 v28, s47, v28, v100\n"
      "v_mfma_f32_16x16x32_bf16 v[56:59], v[220:223], a[212:215], v[56:59]\n"
      "v_fma_f32 v29, s47, v29, v101\n"
      "v_mfma_f32_16x16x32_bf16 v[60:63], v[212:215], a[212:215], v[60:63]\n"
      "s_waitcnt lgkmcnt(1)\n"
      "v_fma_f32 v30, s47, v30, v102\n"
      "v_mfma_f32_16x16x32_bf16 v[64:67], v[224:227], a[192:195], v[64:67]\n"
      "ds_read_b128 a[208:211], v1 offset:20480\n"
      "v_fma_f32 v31, s47, v31, v103\n"
      "v_mfma_f32_16x16x32_bf16 v[68:71], v[216:219], a[192:195], v[68:71]\n"
      "v_cvt_pk_bf16_f32 v40, v40, v41\n"
      "v_mfma_f32_16x16x32_bf16 v[72:75], v[208:211], a[192:195], v[72:75]\n"
      "s_nop 0\n"
      "buffer_store_dwordx4 a[232:235], v11, s[8:11], s59 offen offset:0 sc1\n"
      "v_mfma_f32_16x16x32_bf16 v[52:55], v[204:207], a[196:199], v[52:55]\n"
      "ds_read_b128 a[212:215], v1 offset:16448\n"
      "ds_write_addtid_b32 v40 offset:12288\n"
      "v_mfma_f32_16x16x32_bf16 v[56:59], v[196:199], a[196:199], v[56:59]\n"
      "ds_read2st64_b32 a[236:237], v13 offset0:4 offset1:6\n"
      "ds_read2st64_b32 a[238:239], v15 offset0:4 offset1:6\n"
      "v_mfma_f32_16x16x32_bf16 v[60:63], v[188:191], a[196:199], v[60:63]\n"
      "v_cvt_pk_bf16_f32 v42, v42, v43\n"
      "v_cvt_pk_bf16_f32 v28, v28, v29\n"
      "v_mfma_f32_16x16x32_bf16 v[64:67], v[200:203], a[200:203], v[64:67]\n"
      "ds_read_b128 a[192:195], v1 offset:20544\n"
      "ds_write_addtid_b32 v42 offset:12800\n"
      "v_mfma_f32_16x16x32_bf16 v[68:71], v[192:195], a[200:203], v[68:71]\n"
      "ds_write_addtid_b32 v28 offset:14336\n"
      "v_cvt_pk_bf16_f32 v30, v30, v31\n"
      "v_mfma_f32_16x16x32_bf16 v[72:75], v[184:187], a[200:203], v[72:75]\n"
      "s_waitcnt lgkmcnt(7)\n"
      "ds_write_addtid_b32 v30 offset:14848\n"
      "v_sub_f32 v32, v32, v44\n"
      "v_mfma_f32_16x16x32_bf16 v[52:55], v[180:183], a[204:207], v[52:55]\n"
      "ds_read_b128 a[196:199], v1 offset:16512\n"
      "v_sub_f32 v33, v33, v45\n"
      "v_mfma_f32_16x16x32_bf16 v[56:59], v[172:175], a[204:207], v[56:59]\n"
      "v_sub_f32 v34, v34, v46\n"
      "v_sub_f32_e64 v35, v35, v47\n"
      "v_mfma_f32_16x16x32_bf16 v[60:63], v[164:167], a[204:207], v[60:63]\n"
      "v_fmac_f32 v32, s46, v80\n"
      "v_fmac_f32 v33, s46, v81\n"
      "v_mfma_f32_16x16x32_bf16 v[64:67], v[176:179], a[208:211], v[64:67]\n"
      "ds_read_b128 a[200:203], v1 offset:20608\n"
      "v_fmac_f32 v34, s46, v82\n"
      "v_mfma_f32_16x16x32_bf16 v[68:71], v[168:171], a[208:211], v[68:71]\n"
      "v_fmac_f32 v35, s46, v83\n"
      "v_fmac_f32 v44, s45, v32\n"
      "v_mfma_f32_16x16x32_bf16 v[72:75], v[160:163], a[208:211], v[72:75]\n"
      "s_waitcnt lgkmcnt(1)\n"
      "v_fmac_f32 v45, s45, v33\n"
      "v_fmac_f32 v46, s45, v34\n"
      "v_mfma_f32_16x16x32_bf16 v[52:55], v[156:159], a[212:215], v[52:55]\n"
      "ds_read_b128 a[204:207], v2 offset:16384\n"
      "v_fmac_f32 v47, s45, v35\n"
      "v_mfma_f32_16x16x32_bf16 v[56:59], v[148:151], a[212:215], v[56:59]\n"
      "v_fma_f32 v32, s47, v32, v104\n"
      "v_fma_f32 v33, s47, v33, v105\n"
      "v_mfma_f32_16x16x32_bf16 v[60:63], v[140:143], a[212:215], v[60:63]\n"
      "s_nop 0\n"
      "v_fma_f32 v34, s47, v34, v106\n"
      "v_fma_f32 v35, s47, v35, v107\n"
      "v_mfma_f32_16x16x32_bf16 v[64:67], v[152:155], a[192:195], v[64:67]\n"
      "buffer_store_dwordx4 a[236:239], v11, s[8:11], s59 offen offset:128 sc1\n"
      "ds_read_b128 a[208:211], v2 offset:20480\n"
      "v_mfma_f32_16x16x32_bf16 v[68:71], v[144:147], a[192:195], v[68:71]\n"
      "v_cvt_pk_bf16_f32 v44, v44, v45\n"
      "v_cvt_pk_bf16_f32 v46, v46, v47\n"
      "v_mfma_f32_16x16x32_bf16 v[72:75], v[136:139], a[192:195], v[72:75]\n"
      "ds_write_addtid_b32 v44 offset:20484\n"
      "ds_read2st64_b32 a[240:241], v8 offset0:4 offset1:6\n"
      "v_mfma_f32_16x16x32_bf16 v[52:55], v[132:135], a[196:199], v[52:55]\n"
      "ds_read_b128 a[212:215], v2 offset:16448\n"
      "ds_write_addtid_b32 v46 offset:20996\n"
      "v_mfma_f32_16x16x32_bf16 v[56:59], v[124:127], a[196:199], v[56:59]\n"
      "ds_read2st64_b32 a[242:243], v9 offset0:4 offset1:6\n"
      "v_cvt_pk_bf16_f32 v32, v32, v33\n"
      "v_mfma_f32_16x16x32_bf16 v[60:63], a[184:187], a[196:199], v[60:63]\n"
      "s_waitcnt lgkmcnt(5)\n"
      "ds_write_addtid_b32 v32 offset:22532\n"
      "v_cvt_pk_bf16_f32 v34, v34, v35\n"
      "v_mfma_f32_16x16x32_bf16 v[64:67], v[128:131], a[200:203], v[64:67]\n"
      "s_nop 0\n"
      "ds_read_b128 a[192:195], v2 offset:20544\n"
      "ds_write_addtid_b32 v34 offset:23044\n"
      "v_mfma_f32_16x16x32_bf16 v[68:71], a[188:191], a[200:203], v[68:71]\n"
      "v_sub_f32 v36, v36, v48\n"
      "v_sub_f32 v37, v37, v49\n"
      "v_mfma_f32_16x16x32_bf16 v[72:75], a[180:183], a[200:203], v[72:75]\n"
      "v_sub_f32 v38, v38, v50\n"
      "v_sub_f32 v39, v39, v51\n"
      "v_mfma_f32_16x16x32_bf16 v[52:55], a[176:179], a[204:207], v[52:55]\n"
      "ds_read_b128 a[196:199], v2 offset:16512\n"
      "v_fmac_f32 v36, s46, v84\n"
      "v_mfma_f32_16x16x32_bf16 v[56:59], a[168:171], a[204:207], v[56:59]\n"
      "v_fmac_f32 v37, s46, v85\n"
      "v_fmac_f32 v38, s46, v86\n"
      "v_mfma_f32_16x16x32_bf16 v[60:63], a[160:163], a[204:207], v[60:63]\n"
      "v_fmac_f32 v39, s46, v87\n"
      "v_fmac_f32 v48, s45, v36\n"
      "v_mfma_f32_16x16x32_bf16 v[64:67], a[172:175], a[208:211], v[64:67]\n"
      "ds_read_b128 a[200:203], v2 offset:20608\n"
      "v_fmac_f32 v49, s45, v37\n"
      "v_mfma_f32_16x16x32_bf16 v[68:71], a[164:167], a[208:211], v[68:71]\n"
      "v_fmac_f32 v50, s45, v38\n"
      "v_fmac_f32 v51, s45, v39\n"
      "v_mfma_f32_16x16x32_bf16 v[72:75], a[156:159], a[208:211], v[72:75]\n"
      "s_waitcnt lgkmcnt(1)\n"
      "v_fma_f32 v36, s47, v36, v108\n"
      "v_fma_f32 v37, s47, v37, v109\n"
      "v_mfma_f32_16x16x32_bf16 v[52:55], a[152:155], a[212:215], v[52:55]\n"
      "ds_read_b128 a[204:207], v3 offset:8192\n"
      "v_fma_f32 v38, s47, v38, v110\n"
      "v_mfma_f32_16x16x32_bf16 v[56:59], a[144:147], a[212:215], v[56:59]\n"
      "s_nop 0\n"
      "buffer_store_dwordx4 a[240:243], v11, s[8:11], s59 offen offset:256 sc1\n"
      "v_mfma_f32_16x16x32_bf16 v[60:63], a[136:139], a[212:215], v[60:63]\n"
      "v_fma_f32 v39, s47, v39, v111\n"
      "v_cvt_pk_bf16_f32 v48, v48, v49\n"
      "v_mfma_f32_16x16x32_bf16 v[64:67], a[148:151], a[192:195], v[64:67]\n"
      "ds_read_b128 a[208:211], v3 offset:8256\n"
      "ds_write_addtid_b32 v48 offset:16384\n"
      "v_mfma_f32_16x16x32_bf16 v[68:71], a[140:143], a[192:195], v[68:71]\n"
      "v_cvt_pk_bf16_f32 v50, v50, v51\n"
      "v_cvt_pk_bf16_f32 v36, v36, v37\n"
      "v_mfma_f32_16x16x32_bf16 v[72:75], a[132:135], a[192:195], v[72:75]\n"
      "ds_write_addtid_b32 v50 offset:16896\n"
      "ds_write_addtid_b32 v36 offset:18432\n"
      "v_mfma_f32_16x16x32_bf16 v[52:55], a[128:131], a[196:199], v[52:55]\n"
      "ds_read_b128 a[212:215], v3 offset:8320\n"
      "v_cvt_pk_bf16_f32 v38, v38, v39\n"
      "v_mfma_f32_16x16x32_bf16 v[56:59], a[120:123], a[196:199], v[56:59]\n"
      "ds_write_addtid_b32 v38 offset:18944\n"
      "v_mfma_f32_16x16x32_bf16 v[60:63], a[112:115], a[196:199], v[60:63]\n"
      "s_waitcnt lgkmcnt(4)\n"
      "v_mfma_f32_16x16x32_bf16 v[64:67], a[124:127], a[200:203], v[64:67]\n"
      "ds_read_b128 a[192:195], v4 offset:8192\n"
      "v_mfma_f32_16x16x32_bf16 v[68:71], a[116:119], a[200:203], v[68:71]\n"
      "v_mfma_f32_16x16x32_bf16 v[72:75], a[108:111], a[200:203], v[72:75]\n"
      "v_mfma_f32_16x16x32_bf16 v[112:115], a[0:3], a[204:207], 0\n"
      "s_nop 0\n"
      "ds_read_b128 a[196:199], v4 offset:8256\n"
      "v_mfma_f32_16x16x32_bf16 v[116:119], a[4:7], a[204:207], 0\n"
      "v_mfma_f32_16x16x32_bf16 v[120:123], a[8:11], a[204:207], 0\n"
      "v_mfma_f32_16x16x32_bf16 v[112:115], a[12:15], a[208:211], v[112:115]\n"
      "ds_read_b128 a[200:203], v4 offset:8320\n"
      "v_sub_f32 v52, v64, v52\n"
      "v_sub_f32 v53, v65, v53\n"
      "v_sub_f32 v54, v66, v54\n"
      "v_sub_f32 v55, v67, v55\n"
      "v_sub_f32 v56, v68, v56\n"
      "v_sub_f32 v57, v69, v57\n"
      "v_mfma_f32_16x16x32_bf16 v[116:119], a[16:19], a[208:211], v[116:119]\n"
      "ds_write_addtid_b32 v52 offset:6144\n"
      "ds_write_addtid_b32 v53 offset:6656\n"
      "ds_write_addtid_b32 v54 offset:7168\n"
      "v_sub_f32 v58, v70, v58\n"
      "v_sub_f32 v59, v71, v59\n"
      "v_sub_f32 v60, v72, v60\n"
      "v_sub_f32_e64 v61, v73, v61\n"
      "v_mfma_f32_16x16x32_bf16 v[120:123], a[20:23], a[208:211], v[120:123]\n"
      "s_waitcnt lgkmcnt(3)\n"
      "ds_write_addtid_b32 v55 offset:7680\n"
      "ds_write_addtid_b32 v56 offset:8192\n"
      "ds_write_addtid_b32 v57 offset:8704\n"
      "v_sub_f32 v62, v74, v62\n"
      "v_sub_f32 v63, v75, v63\n"
      "v_mfma_f32_16x16x32_bf16 v[112:115], a[24:27], a[212:215], v[112:115]\n"
      "ds_write_addtid_b32 v58 offset:9216\n"
      "ds_write_addtid_b32 v59 offset:9728\n"
      "ds_write_addtid_b32 v60 offset:10240\n"
      "v_mfma_f32_16x16x32_bf16 v[116:119], a[28:31], a[212:215], v[116:119]\n"
      "ds_write_addtid_b32 v61 offset:10752\n"
      "ds_write_addtid_b32 v62 offset:11264\n"
      "ds_write_addtid_b32 v63 offset:11776\n"
      "v_mfma_f32_16x16x32_bf16 v[120:123], a[32:35], a[212:215], v[120:123]\n"
      "s_waitcnt lgkmcnt(0)\n"
      "s_barrier\n"
      "v_mfma_f32_16x16x32_bf16 v[112:115], a[36:39], a[192:195], v[112:115]\n"
      "s_add_u32 s59, s59, 783360\n"
      "ds_read_b128 a[204:207], v5 offset:8192\n"
      "ds_read2st64_b32 v[28:29], v6 offset0:24 offset1:26\n"
      "v_mfma_f32_16x16x32_bf16 v[116:119], a[40:43], a[192:195], v[116:119]\n"
      "ds_read2st64_b32 v[30:31], v6 offset0:28 offset1:30\n"
      "ds_read2st64_b32 v[32:33], v6 offset0:32 offset1:34\n"
      "v_mfma_f32_16x16x32_bf16 v[120:123], a[44:47], a[192:195], v[120:123]\n"
      "ds_read2st64_b32 v[34:35], v6 offset0:36 offset1:38\n"
      "ds_read2st64_b32 v[36:37], v6 offset0:40 offset1:42\n"
      "v_mfma_f32_16x16x32_bf16 v[112:115], a[48:51], a[196:199], v[112:115]\n"
      "ds_read_b128 a[208:211], v5 offset:8256\n"
      "ds_read2st64_b32 v[38:39], v6 offset0:44 offset1:46\n"
      "v_mfma_f32_16x16x32_bf16 v[116:119], a[52:55], a[196:199], v[116:119]\n"
      "v_fma_f32 v40, 0.5, v52, -v64\n"
      "v_fma_f32 v41, 0.5, v53, -v65\n"
      "v_mfma_f32_16x16x32_bf16 v[120:123], a[56:59], a[196:199], v[120:123]\n"
      "ds_read2st64_b32 a[232:233], v12 offset0:0 offset1:2\n"
      "ds_read2st64_b32 a[234:235], v14 offset0:0 offset1:2\n"
      "v_mfma_f32_16x16x32_bf16 v[112:115], a[60:63], a[200:203], v[112:115]\n"
      "ds_read_b128 a[212:215], v5 offset:8320\n"
      "v_fma_f32 v42, 0.5, v54, -v66\n"
      "v_mfma_f32_16x16x32_bf16 v[116:119], a[64:67], a[200:203], v[116:119]\n"
      "ds_read2st64_b32 a[236:237], v13 offset0:0 offset1:2\n"
      "ds_read2st64_b32 a[238:239], v15 offset0:0 offset1:2\n"
      "v_mfma_f32_16x16x32_bf16 v[120:123], a[68:71], a[200:203], v[120:123]\n"
      "s_waitcnt lgkmcnt(9)\n"
      "v_fma_f32 v43, 0.5, v55, -v67\n"
      "v_fma_f32 v44, 0.5, v56, -v68\n"
      "v_mfma_f32_16x16x32_bf16 v[112:115], a[72:75], a[204:207], v[112:115]\n"
      "s_waitcnt lgkmcnt(3)\n"
      "buffer_store_dwordx4 a[232:235], v11, s[8:11], s59 offen offset:0 sc1\n"
      "v_mfma_f32_16x16x32_bf16 v[116:119], a[76:79], a[204:207], v[116:119]\n"
      "v_fma_f32 v45, 0.5, v57, -v69\n"
      "v_fma_f32 v46, 0.5, v58, -v70\n"
      "v_mfma_f32_16x16x32_bf16 v[120:123], a[80:83], a[204:207], v[120:123]\n"
      "ds_read2st64_b32 a[240:241], v8 offset0:0 offset1:2\n"
      "ds_read2st64_b32 a[242:243], v9 offset0:0 offset1:2\n"
      "v_mfma_f32_16x16x32_bf16 v[112:115], a[84:87], a[208:211], v[112:115]\n"
      "s_waitcnt lgkmcnt(2)\n"
      "buffer_store_dwordx4 a[236:239], v11, s[8:11], s59 offen offset:128 sc1\n"
      "v_mfma_f32_16x16x32_bf16 v[116:119], a[88:91], a[208:211], v[116:119]\n"
      "s_nop 0\n"
      "v_fma_f32 v47, 0.5, v59, -v71\n"
      "v_fma_f32 v48, 0.5, v60, -v72\n"
      "v_mfma_f32_16x16x32_bf16 v[120:123], a[92:95], a[208:211], v[120:123]\n"
      "v_fma_f32 v49, 0.5, v61, -v73\n"
      "v_fma_f32 v50, 0.5, v62, -v74\n"
      "v_mfma_f32_16x16x32_bf16 v[112:115], a[96:99], a[212:215], v[112:115]\n"
      "s_waitcnt lgkmcnt(0)\n"
      "buffer_store_dwordx4 a[240:243], v11, s[8:11], s59 offen offset:256 sc1\n"
      "v_mfma_f32_16x16x32_bf16 v[116:119], a[100:103], a[212:215], v[116:119]\n"
      "s_nop 0\n"
      "v_fma_f32 v51, 0.5, v63, -v75\n"
      "v_sub_f32 v28, v28, v40\n"
      "v_mfma_f32_16x16x32_bf16 v[120:123], a[104:107], a[212:215], v[120:123]\n"
      "v_sub_f32 v29, v29, v41\n"
      "v_sub_f32 v30, v30, v42\n"
      "v_sub_f32 v31, v31, v43\n"
      "v_fmac_f32 v28, s46, v88\n"
      "v_fmac_f32 v29, s46, v89\n"
      "v_fmac_f32 v30, s46, v90\n"
      "v_fmac_f32 v31, s46, v91\n"
      "v_fmac_f32 v40, s45, v28\n"
      "v_fmac_f32 v41, s45, v29\n"
      "v_pk_fma_f32 v[42:43], v[30:31], s[44:45], v[42:43] op_sel:[0,1,0] op_sel_hi:[1,1,1]\n"
      "v_cvt_pk_bf16_f32 v40, v40, v41\n"
      "v_cvt_pk_bf16_f32 v42, v42, v43\n"
      "v_pk_add_f32 v[32:33], v[32:33], v[44:45] neg_lo:[0,1] neg_hi:[0,1]\n"
      "v_pk_add_f32 v[34:35], v[34:35], v[46:47] neg_lo:[0,1] neg_hi:[0,1]\n"
      "v_pk_fma_f32 v[32:33], v[92:93], s[46:47], v[32:33] op_sel:[0,0,0] op_sel_hi:[1,0,1]\n"
      "v_pk_fma_f32 v[34:35], v[94:95], s[46:47], v[34:35] op_sel:[0,0,0] op_sel_hi:[1,0,1]\n"
      "v_pk_fma_f32 v[44:45], v[32:33], s[44:45], v[44:45] op_sel:[0,1,0] op_sel_hi:[1,1,1]\n"
      "v_pk_fma_f32 v[46:47], v[34:35], s[44:45], v[46:47] op_sel:[0,1,0] op_sel_hi:[1,1,1]\n"
      "v_cvt_pk_bf16_f32 v44, v44, v45\n"
      "v_cvt_pk_bf16_f32 v46, v46, v47\n"
      "v_pk_add_f32 v[36:37], v[36:37], v[48:49] neg_lo:[0,1] neg_hi:[0,1]\n"
      "v_pk_add_f32 v[38:39], v[38:39], v[50:51] neg_lo:[0,1] neg_hi:[0,1]\n"
      "v_pk_fma_f32 v[36:37], v[96:97], s[46:47], v[36:37] op_sel:[0,0,0] op_sel_hi:[1,0,1]\n"
      "v_pk_fma_f32 v[38:39], v[98:99], s[46:47], v[38:39] op_sel:[0,0,0] op_sel_hi:[1,0,1]\n"
      "v_pk_fma_f32 v[48:49], v[36:37], s[44:45], v[48:49] op_sel:[0,1,0] op_sel_hi:[1,1,1]\n"
      "v_pk_fma_f32 v[50:51], v[38:39], s[44:45], v[50:51] op_sel:[0,1,0] op_sel_hi:[1,1,1]\n"
      "v_cvt_pk_bf16_f32 v48, v48, v49\n"
      "v_cvt_pk_bf16_f32 v50, v50, v51\n"
      "ds_write_addtid_b32 v40 offset:13312\n"
      "ds_write_addtid_b32 v42 offset:13824\n"
      "ds_write_addtid_b32 v44 offset:21508\n"
      "ds_write_addtid_b32 v46 offset:22020\n"
      "ds_write_addtid_b32 v48 offset:17408\n"
      "ds_write_addtid_b32 v50 offset:17920\n"
      "v_pk_fma_f32 v[28:29], v[28:29], s[46:47], v[112:113] op_sel:[0,1,0] op_sel_hi:[1,1,1]\n"
      "v_pk_fma_f32 v[30:31], v[30:31], s[46:47], v[114:115] op_sel:[0,1,0] op_sel_hi:[1,1,1]\n"
      "v_cvt_pk_bf16_f32 v28, v28, v29\n"
      "v_cvt_pk_bf16_f32 v30, v30, v31\n"
      "v_pk_fma_f32 v[32:33], v[32:33], s[46:47], v[116:117] op_sel:[0,1,0] op_sel_hi:[1,1,1]\n"
      "v_pk_fma_f32 v[34:35], v[34:35], s[46:47], v[118:119] op_sel:[0,1,0] op_sel_hi:[1,1,1]\n"
      "v_cvt_pk_bf16_f32 v32, v32, v33\n"
      "v_cvt_pk_bf16_f32 v34, v34, v35\n"
      "v_pk_fma_f32 v[36:37], v[36:37], s[46:47], v[120:121] op_sel:[0,1,0] op_sel_hi:[1,1,1]\n"
      "v_pk_fma_f32 v[38:39], v[38:39], s[46:47], v[122:123] op_sel:[0,1,0] op_sel_hi:[1,1,1]\n"
      "v_cvt_pk_bf16_f32 v36, v36, v37\n"
      "v_cvt_pk_bf16_f32 v38, v38, v39\n"
      "ds_write_addtid_b32 v28 offset:15360\n"
      "ds_write_addtid_b32 v30 offset:15872\n"
      "ds_write_addtid_b32 v32 offset:23556\n"
      "ds_write_addtid_b32 v34 offset:24068\n"
      "ds_write_addtid_b32 v36 offset:19456\n"
      "ds_write_addtid_b32 v38 offset:19968\n"
      "s_waitcnt lgkmcnt(0)\n"
      "s_barrier\n"
      "ds_read2st64_b32 a[232:233], v12 offset0:4 offset1:6\n"
      "ds_read2st64_b32 a[234:235], v14 offset0:4 offset1:6\n"
      "ds_read2st64_b32 a[236:237], v13 offset0:4 offset1:6\n"
      "ds_read2st64_b32 a[238:239], v15 offset0:4 offset1:6\n"
      "ds_read2st64_b32 a[240:241], v8 offset0:4 offset1:6\n"
      "ds_read2st64_b32 a[242:243], v9 offset0:4 offset1:6\n"
      "s_add_u32 s59, s59, 3072\n"
      "s_waitcnt lgkmcnt(4)\n"
      "buffer_store_dwordx4 a[232:235], v11, s[8:11], s59 offen offset:0 sc1\n"
      "s_waitcnt lgkmcnt(2)\n"
      "buffer_store_dwordx4 a[236:239], v11, s[8:11], s59 offen offset:128 sc1\n"
      "s_waitcnt lgkmcnt(0)\n"
      "buffer_store_dwordx4 a[240:243], v11, s[8:11], s59 offen offset:256 sc1\n"
      "LSend_%=:\n"
      "Lfin_%=:\n"
      "s_waitcnt vmcnt(0) lgkmcnt(0)\n"
      "s_endpgm\n"
      ".rept 512\n"
      "s_nop 0\n"
      ".endr\n"
      ".Lwg_end:\n"
      : "+{v0}"(tid)
      : "{s[0:1]}"(__builtin_amdgcn_kernarg_segment_ptr()), "{s2}"(blockIdx.x)
      : "v1", "v2", "v3", "v4", "v5", "v6", "v7", "v8", "v9", "v10", "v11", "v12", "v13", "v14", "v15", "v16", "v17", "v18", "v19", "v20", "v21", "v22", "v23", "v24", "v25", "v26", "v27", "v28", "v29", "v30", "v31", "v32", "v33", "v34", "v35", "v36", "v37", "v38", "v39", "v40", "v41", "v42", "v43", "v44", "v45", "v46", "v47", "v48", "v49", "v50", "v51", "v52", "v53", "v54", "v55", "v56", "v57", "v58", "v59", "v60", "v61", "v62", "v63", "v64", "v65", "v66", "v67", "v68", "v69", "v70", "v71", "v72", "v73", "v74", "v75", "v76", "v77", "v78", "v79", "v80", "v81", "v82", "v83", "v84", "v85", "v86", "v87", "v88", "v89", "v90", "v91", "v92", "v93", "v94", "v95", "v96", "v97", "v98", "v99", "v100", "v101", "v102", "v103", "v104", "v105", "v106", "v107", "v108", "v109", "v110", "v111", "v112", "v113", "v114", "v115", "v116", "v117", "v118", "v119", "v120", "v121", "v122", "v123", "v124", "v125", "v126", "v127", "v128", "v129", "v130", "v131", "v132", "v133", "v134", "v135", "v136", "v137", "v138", "v139", "v140", "v141", "v142", "v143", "v144", "v145", "v146", "v147", "v148", "v149", "v150", "v151", "v152", "v153", "v154", "v155", "v156", "v157", "v158", "v159", "v160", "v161", "v162", "v163", "v164", "v165", "v166", "v167", "v168", "v169", "v170", "v171", "v172", "v173", "v174", "v175", "v176", "v177", "v178", "v179", "v180", "v181", "v182", "v183", "v184", "v185", "v186", "v187", "v188", "v189", "v190", "v191", "v192", "v193", "v194", "v195", "v196", "v197", "v198", "v199", "v200", "v201", "v202", "v203", "v204", "v205", "v206", "v207", "v208", "v209", "v210", "v211", "v212", "v213", "v214", "v215", "v216", "v217", "v218", "v219", "v220", "v221", "v222", "v223", "v224", "v225", "v226", "v227", "v228", "v229", "v230", "v231", "v232", "v233", "v234", "v235", "v236", "v237", "v238", "v239", "v240", "v241", "v242", "v243", "v244", "v245", "v246", "v247", "v248", "v249", "v250", "v251", "v252", "v253", "v254", "v255", "a0", "a1", "a2", "a3", "a4", "a5", "a6", "a7", "a8", "a9", "a10", "a11", "a12", "a13", "a14", "a15", "a16", "a17", "a18", "a19", "a20", "a21", "a22", "a23", "a24", "a25", "a26", "a27", "a28", "a29", "a30", "a31", "a32", "a33", "a34", "a35", "a36", "a37", "a38", "a39", "a40", "a41", "a42", "a43", "a44", "a45", "a46", "a47", "a48", "a49", "a50", "a51", "a52", "a53", "a54", "a55", "a56", "a57", "a58", "a59", "a60", "a61", "a62", "a63", "a64", "a65", "a66", "a67", "a68", "a69", "a70", "a71", "a72", "a73", "a74", "a75", "a76", "a77", "a78", "a79", "a80", "a81", "a82", "a83", "a84", "a85", "a86", "a87", "a88", "a89", "a90", "a91", "a92", "a93", "a94", "a95", "a96", "a97", "a98", "a99", "a100", "a101", "a102", "a103", "a104", "a105", "a106", "a107", "a108", "a109", "a110", "a111", "a112", "a113", "a114", "a115", "a116", "a117", "a118", "a119", "a120", "a121", "a122", "a123", "a124", "a125", "a126", "a127", "a128", "a129", "a130", "a131", "a132", "a133", "a134", "a135", "a136", "a137", "a138", "a139", "a140", "a141", "a142", "a143", "a144", "a145", "a146", "a147", "a148", "a149", "a150", "a151", "a152", "a153", "a154", "a155", "a156", "a157", "a158", "a159", "a160", "a161", "a162", "a163", "a164", "a165", "a166", "a167", "a168", "a169", "a170", "a171", "a172", "a173", "a174", "a175", "a176", "a177", "a178", "a179", "a180", "a181", "a182", "a183", "a184", "a185", "a186", "a187", "a188", "a189", "a190", "a191", "a192", "a193", "a194", "a195", "a196", "a197", "a198", "a199", "a200", "a201", "a202", "a203", "a204", "a205", "a206", "a207", "a208", "a209", "a210", "a211", "a212", "a213", "a214", "a215", "a216", "a217", "a218", "a219", "a220", "a221", "a222", "a223", "a224", "a225", "a226", "a227", "a228", "a229", "a230", "a231", "a232", "a233", "a234", "a235", "a236", "a237", "a238", "a239", "a240", "a241", "a242", "a243", "a244", "a245", "a246", "a247", "a248", "a249", "a250", "a251", "a252", "a253", "a254", "a255", "s3", "s4", "s5", "s6", "s7", "s8", "s9", "s10", "s11", "s12", "s13", "s14", "s15", "s16", "s17", "s18", "s19", "s20", "s21", "s22", "s23", "s24", "s25", "s26", "s27", "s28", "s29", "s30", "s31", "s33", "s34", "s35", "s36", "s37", "s38", "s39", "s40", "s41", "s42", "s43", "s44", "s45", "s46", "s47", "s48", "s49", "s50", "s51", "s52", "s53", "s54", "s55", "s56", "s57", "s58", "s59", "s60", "s61", "s62", "s63", "s64", "s65", "s66", "s67", "s68", "s69", "s70", "s71", "s72", "s73", "s74", "s75", "s76", "s77", "s78", "s79", "s80", "s81", "s82", "s83", "s84", "s85", "s86", "s87", "s88", "s89", "s90", "s91", "s92", "s93", "s94", "s95", "s96", "s97", "s98", "s99", "scc", "vcc", "memory");
  asm volatile("" :: "v"((unsigned)(uintptr_t)smem) : "memory");
}

extern "C" void kernel_launch(void* const* d_in, const int* in_sizes, int n_in, void* d_out,
                              int out_size, void* d_ws, size_t ws_size, hipStream_t stream) {
  conv_kernel<<<256, 256, 0, stream>>>((const char*)d_in[0], (const char*)d_in[1], (char*)d_out, (char*)d_ws);
}
```

```cpp
#include <hip/hip_runtime.h>
#include <hip/hip_bf16.h>
#include <stdint.h>

__global__ void __launch_bounds__(256, 1)
conv_kernel(const char* __restrict__ x, const char* __restrict__ w, char* __restrict__ out, char* __restrict__ dbg) {
  __shared__ __attribute__((aligned(1024))) char smem[163840];
  unsigned tid = threadIdx.x;
  asm volatile(
      "s_load_dwordx2 s[12:13], s[0:1], 8\n"
      "s_load_dwordx2 s[4:5], s[0:1], 0\n"
      "s_load_dwordx2 s[8:9], s[0:1], 16\n"
      "v_readfirstlane_b32 s16, v0\n"
      "s_getpc_b64 s[26:27]\n"
      ".Lwg_pc:\n"
      "v_and_b32 v37, 3, v0\n"
      "s_lshr_b32 s23, s2, 3\n"
      "s_lshl_b32 s28, s16, 3\n"
      "v_lshlrev_b32 v40, 7, v37\n"
      "s_lshl_b32 s29, s23, 11\n"
      "s_mov_b32 m0, 150016\n"
      "s_add_u32 s28, s28, s29\n"
      "v_and_b32 v36, 63, v0\n"
      "v_add_u32 v40, s28, v40\n"
      "s_lshr_b32 s16, s16, 6\n"
      "v_min_u32 v40, .Lwg_end-.Lwg_pc-4, v40\n"
      "s_and_b32 s18, s16, 1\n"
      "global_load_lds_dword v40, s[26:27]\n"
      "v_lshlrev_b32 v92, 4, v36\n"
      "s_lshr_b32 s17, s16, 1\n"
      "s_and_b32 s28, s2, 7\n"
      "s_mul_i32 s19, s28, 6291456\n"
      "s_and_b32 s28, s23, 1\n"
      "s_mul_i32 s20, s28, 98304\n"
      "s_lshr_b32 s28, s23, 1\n"
      "s_mul_i32 s29, s28, 6144\n"
      "s_add_u32 s21, s20, s29\n"
      "s_add_u32 s6, s19, 6492160\n"
      "s_min_u32 s6, s6, 50335744\n"
      "s_mov_b32 s7, 0x20000\n"
      "s_mov_b32 s10, 50331648\n"
      "s_mov_b32 s11, 0x20000\n"
      "s_mov_b32 s14, 165888\n"
      "s_mov_b32 s15, 0x20000\n"
      "s_mul_i32 s63, s18, 3072\n"
      "s_add_u32 s54, s19, s20\n"
      "s_sub_u32 s54, s54, 196608\n"
      "s_lshl_b32 s64, s18, 8\n"
      "s_mul_i32 s28, s17, 24832\n"
      "s_add_u32 s64, s64, s28\n"
      "s_mov_b32 s55, 0\n"
      "v_bitop3_b32 v93, v36, v92, 32 bitop3:0x6c\n"
      "v_xor_b32 v94, 32, v92\n"
      "v_bfe_u32 v38, v36, 2, 2\n"
      "v_lshrrev_b32 v39, 4, v36\n"
      "s_cmp_eq_u32 s17, 1\n"
      "s_cbranch_scc1 LBD_%=\n"
      "v_lshlrev_b32 v40, 3, v92\n"
      "s_mul_i32 s53, s18, 2688\n"
      "s_mul_i32 s34, s23, 5184\n"
      "v_min_u32 v40, 2560, v40\n"
      "s_add_u32 s53, s53, s34\n"
      "v_lshlrev_b32 v43, 4, v37\n"
      "s_mul_i32 s26, s17, 0x8080\n"
      "s_sub_u32 s26, 0xbf80, s26\n"
      "s_mov_b32 s27, 0\n"
      "s_lshl_b32 s34, s17, 7\n"
      "s_mov_b32 s29, 0\n"
      "s_sub_u32 s28, s26, s34\n"
      "s_mov_b32 s31, 0\n"
      "s_sub_u32 s30, s28, s34\n"
      "v_lshlrev_b64 v[44:45], v43, s[26:27]\n"
      "v_lshlrev_b64 v[46:47], v43, s[28:29]\n"
      "s_xor_b32 s28, s28, 0x8000\n"
      "v_lshlrev_b64 v[48:49], v43, s[30:31]\n"
      "v_lshlrev_b64 v[50:51], v43, s[28:29]\n"
      "v_accvgpr_write_b32 a244, v44\n"
      "v_accvgpr_write_b32 a245, v45\n"
      "v_accvgpr_write_b32 a246, v46\n"
      "v_accvgpr_write_b32 a247, v47\n"
      "v_accvgpr_write_b32 a248, v48\n"
      "v_accvgpr_write_b32 a249, v49\n"
      "v_accvgpr_write_b32 a250, v50\n"
      "v_accvgpr_write_b32 a251, v51\n"
      "s_waitcnt lgkmcnt(0)\n"
      "s_and_b32 s13, s13, 0xffff\n"
      "buffer_load_dword v40, s[12:15], s53 offen lds\n"
      "s_add_u32 m0, s63, 30976\n"
      "s_add_u32 s52, s63, 55296\n"
      "buffer_load_dwordx4 v92, s[12:15], s52 offen offset:0 lds\n"
      "buffer_load_dwordx4 v93, s[12:15], s52 offen offset:1024 lds\n"
      "buffer_load_dwordx4 v94, s[12:15], s52 offen offset:2048 lds\n"
      "s_add_u32 m0, s63, 0\n"
      "s_add_u32 s52, s63, 110592\n"
      "buffer_load_dwordx4 v92, s[12:15], s52 offen offset:0 lds\n"
      "buffer_load_dwordx4 v93, s[12:15], s52 offen offset:1024 lds\n"
      "buffer_load_dwordx4 v94, s[12:15], s52 offen offset:2048 lds\n"
      "s_branch LS_%=\n"
      "LBD_%=:\n"
      "s_sub_u32 s26, s21, s20\n"
      "s_lshl_b32 s37, s18, 11\n"
      "s_add_u32 s26, s26, s37\n"
      "s_add_u32 s27, s26, 3712\n"
      "v_add_u32 v44, s27, v92\n"
      "s_cmp_eq_u32 s18, 1\n"
      "s_cselect_b32 s28, 16, 48\n"
      "s_cselect_b32 s29, 0, 32\n"
      "s_cselect_b32 s27, 48, 64\n"
      "v_cmp_le_u32 vcc, s28, v36\n"
      "v_cmp_gt_u32 s[30:31], s29, v36\n"
      "v_xor_b32 v45, 32, v44\n"
      "s_cmp_eq_u32 s26, 0\n"
      "s_cselect_b32 s28, 24, 0\n"
      "v_cndmask_b32 v10, v44, v45, vcc\n"
      "v_cndmask_b32 v46, v44, v45, s[30:31]\n"
      "v_cmp_le_u32 vcc, s28, v36\n"
      "v_cmp_gt_u32 s[30:31], s27, v36\n"
      "s_cmp_eq_u32 s26, 94208\n"
      "s_cselect_b32 s27, 24, 64\n"
      "v_cmp_gt_u32 s[34:35], s27, v36\n"
      "v_subrev_u32 v45, 256, v44\n"
      "v_cndmask_b32 v8, 2.0, v10, vcc\n"
      "v_cndmask_b32 v9, v45, v46, s[30:31]\n"
      "v_cndmask_b32 v11, 2.0, v9, s[34:35]\n"
      "s_add_u32 s34, s19, s21\n"
      "s_cmp_eq_u32 s19, 0\n"
      "s_cselect_b32 s36, 0x80000000, s54\n"
      "s_waitcnt lgkmcnt(0)\n"
      "s_load_dword s44, s[4:5], s34\n"
      "s_and_b32 s13, s13, 0xffff\n"
      "s_add_u32 m0, s63, 24832\n"
      "s_add_u32 s52, s63, 0\n"
      "buffer_load_dwordx4 v92, s[12:15], s52 offen offset:0 lds\n"
      "buffer_load_dwordx4 v93, s[12:15], s52 offen offset:1024 lds\n"
      "buffer_load_dwordx4 v94, s[12:15], s52 offen offset:2048 lds\n"
      "s_add_u32 s4, s4, -4096\n"
      "s_addc_u32 s5, s5, -1\n"
      "s_and_b32 s5, s5, 0xffff\n"
      "s_add_u32 s53, s54, 196608\n"
      "s_add_u32 m0, s37, 82432\n"
      "s_nop 0\n"
      "buffer_load_dwordx4 v8, s[4:7], s53 offen offset:0 nt lds\n"
      "buffer_load_dwordx4 v9, s[4:7], s53 offen offset:1024 nt lds\n"
      "s_add_u32 s53, s54, 393216\n"
      "s_add_u32 m0, s37, 86536\n"
      "s_nop 0\n"
      "buffer_load_dwordx4 v8, s[4:7], s53 offen offset:0 nt lds\n"
      "buffer_load_dwordx4 v9, s[4:7], s53 offen offset:1024 nt lds\n"
      "s_add_u32 s53, s54, 589824\n"
      "s_add_u32 m0, s37, 90760\n"
      "s_nop 0\n"
      "buffer_load_dwordx4 v8, s[4:7], s53 offen offset:0 nt lds\n"
      "buffer_load_dwordx4 v9, s[4:7], s53 offen offset:1024 nt lds\n"
      "s_add_u32 s53, s54, 786432\n"
      "s_add_u32 m0, s37, 95104\n"
      "s_nop 0\n"
      "buffer_load_dwordx4 v8, s[4:7], s53 offen offset:0 nt lds\n"
      "buffer_load_dwordx4 v9, s[4:7], s53 offen offset:1024 nt lds\n"
      "s_add_u32 s53, s36, 0\n"
      "s_add_u32 m0, s37, 150272\n"
      "s_nop 0\n"
      "s_nop 0\n"
      "buffer_load_dwordx4 v8, s[4:7], s53 offen offset:0 nt lds\n"
      "buffer_load_dwordx4 v9, s[4:7], s53 offen offset:1024 nt lds\n"
      "v_lshlrev_b32 v43, 4, v37\n"
      "v_lshlrev_b32 v44, 2, v37\n"
      "v_and_b32 v43, 16, v43\n"
      "v_and_or_b32 v43, v44, 8, v43\n"
      "v_mul_u32_u24 v44, 192, v38\n"
      "v_mul_u32_u24 v45, 1536, v39\n"
      "s_lshl_b32 s27, s18, 5\n"
      "v_lshlrev_b32 v46, 1, v36\n"
      "v_add3_u32 v43, v43, v44, v45\n"
      "v_bitop3_b32 v46, v46, s27, 32 bitop3:0x6c\n"
      "v_add_u32 v95, v43, v46\n"
      "v_add_u32 v96, 57344, v95\n"
      "v_add_u32 v97, 114688, v95\n"
      "v_lshlrev_b32 v43, 4, v37\n"
      "s_mul_i32 s26, s17, 0x8080\n"
      "s_sub_u32 s26, 0xbf80, s26\n"
      "s_mov_b32 s27, 0\n"
      "s_lshl_b32 s34, s17, 7\n"
      "s_mov_b32 s29, 0\n"
      "s_sub_u32 s28, s26, s34\n"
      "s_mov_b32 s31, 0\n"
      "s_sub_u32 s30, s28, s34\n"
      "v_lshlrev_b64 v[44:45], v43, s[26:27]\n"
      "v_lshlrev_b64 v[46:47], v43, s[28:29]\n"
      "s_xor_b32 s28, s28, 0x8000\n"
      "v_lshlrev_b64 v[48:49], v43, s[30:31]\n"
      "v_lshlrev_b64 v[50:51], v43, s[28:29]\n"
      "v_accvgpr_write_b32 a244, v44\n"
      "v_accvgpr_write_b32 a245, v45\n"
      "v_accvgpr_write_b32 a246, v46\n"
      "v_accvgpr_write_b32 a247, v47\n"
      "v_accvgpr_write_b32 a248, v48\n"
      "v_accvgpr_write_b32 a249, v49\n"
      "v_accvgpr_write_b32 a250, v50\n"
      "v_accvgpr_write_b32 a251, v51\n"
      "s_lshl_b32 s26, s18, 3\n"
      "v_and_b32 v42, 15, v36\n"
      "v_xor_b32 v42, s26, v42\n"
      "s_lshl_b32 s26, s17, 13\n"
      "s_add_u32 s26, s26, 24832\n"
      "s_mul_i32 s27, s17, 12672\n"
      "v_mul_u32_u24 v44, 192, v42\n"
      "v_lshlrev_b32 v43, 3, v42\n"
      "v_and_b32 v45, 48, v36\n"
      "s_add_u32 s27, s27, 82432\n"
      "v_bitop3_b32 v46, v43, v45, 32 bitop3:0x6c\n"
      "v_add3_u32 v0, v46, v44, s26\n"
      "v_add3_u32 v3, v46, v44, s27\n"
      "v_add_u32 v43, 16, v43\n"
      "v_add_u32 v44, 384, v44\n"
      "v_bitop3_b32 v46, v43, v45, 32 bitop3:0x6c\n"
      "v_add3_u32 v1, v46, v44, s26\n"
      "v_add3_u32 v4, v46, v44, s27\n"
      "v_add_u32 v43, 16, v43\n"
      "v_add_u32 v44, 384, v44\n"
      "v_bitop3_b32 v46, v43, v45, 32 bitop3:0x6c\n"
      "v_add3_u32 v2, v46, v44, s26\n"
      "v_add3_u32 v5, v46, v44, s27\n"
      "v_lshlrev_b32 v43, 31, v36\n"
      "v_and_b32 v44, 2, v36\n"
      "v_or_b32 v43, 0x3f803f80, v43\n"
      "v_mul_u32_u24 v45, 0xc00080, v44\n"
      "v_lshl_add_u32 v44, v44, 22, v43\n"
      "v_add_u32 v45, v45, v43\n"
      "v_accvgpr_write_b32 a216, v44\n"
      "v_accvgpr_write_b32 a217, v45\n"
      "v_lshlrev_b32 v43, 3, v38\n"
      "s_mov_b32 s26, 806420736\n"
      "v_bfe_u32 v43, s26, v43, 8\n"
      "v_lshlrev_b32 v44, 12, v38\n"
      "s_lshl_b32 s28, s18, 8\n"
      "s_add_u32 s28, s28, 82432\n"
      "v_lshl_add_u32 v43, v43, 3, v44\n"
      "v_lshl_add_u32 v44, v39, 2, v37\n"
      "v_lshl_add_u32 v43, v44, 4, v43\n"
      "v_add_u32 v7, s28, v43\n"
      "s_mul_i32 s26, s17, 70144\n"
      "s_lshl_b32 s27, s18, 8\n"
      "s_sub_u32 s26, 70144, s26\n"
      "s_add_u32 s26, s26, s27\n"
      "v_lshl_add_u32 v6, v36, 2, s26\n"
      "v_lshrrev_b32 v43, 3, v36\n"
      "v_and_b32 v44, 7, v36\n"
      "v_mul_u32_u24 v45, 384, v43\n"
      "v_lshl_add_u32 v18, v44, 4, v45\n"
      "v_and_b32 v44, 2, v36\n"
      "v_lshlrev_b32 v43, 3, v43\n"
      "v_lshlrev_b32 v45, 4, v44\n"
      "v_xor_b32 v43, v43, v45\n"
      "v_lshl_add_u32 v43, v44, 7, v43\n"
      "v_and_b32 v44, 1, v36\n"
      "s_lshl_b32 s26, s18, 11\n"
      "v_lshl_add_u32 v43, v44, 6, v43\n"
      "s_add_u32 s26, s26, 12288\n"
      "v_bfe_u32 v44, v36, 2, 1\n"
      "v_add_u32 v43, s26, v43\n"
      "v_mul_i32_i24 v45, 8196, v44\n"
      "s_mov_b32 s27, 0\n"
      "v_add3_u32 v12, v43, v45, s27\n"
      "v_add_u32 v15, 128, v12\n"
      "v_mul_i32_i24 v45, -4092, v44\n"
      "s_mov_b32 s27, 4096\n"
      "v_add3_u32 v13, v43, v45, s27\n"
      "v_add_u32 v16, 128, v13\n"
      "v_mul_i32_i24 v45, -4100, v44\n"
      "s_mov_b32 s27, 8200\n"
      "v_add3_u32 v14, v43, v45, s27\n"
      "v_add_u32 v17, 128, v14\n"
      "s_mul_i32 s62, s18, 393216\n"
      "s_add_u32 s62, s62, s19\n"
      "s_add_u32 s62, s62, s21\n"
      "s_sub_u32 s62, s62, 786432\n"
      "s_mov_b32 s58, 0x80000000\n"
      "s_mov_b32 s59, 0x80000000\n"
      "s_and_b32 s9, s9, 0xffff\n"
      "s_mov_b32 s45, 0xbf2aaaab\n"
      "s_mov_b32 s46, 0xbe800000\n"
      "s_mov_b32 s47, 0x3f2aaaab\n"
      "s_mov_b32 s48, 0xc0c00000\n"
      "s_mov_b32 s49, 0x3f000000\n"
      "s_mov_b32 s38, 4.0\n"
      "s_mov_b32 s39, 0xc0a00000\n"
      "s_add_u32 m0, s63, 6144\n"
      "s_add_u32 s52, s63, 6144\n"
      "buffer_load_dwordx4 v92, s[12:15], s52 offen offset:0 lds\n"
      "buffer_load_dwordx4 v93, s[12:15], s52 offen offset:1024 lds\n"
      "buffer_load_dwordx4 v94, s[12:15], s52 offen offset:2048 lds\n"
      "LDpro_%=:\n"
      "s_waitcnt vmcnt(13)\n"
      "s_barrier\n"
      "ds_read_b64_tr_b16 a[0:1], v95 offset:24832\n"
      "ds_read_b64_tr_b16 a[2:3], v95 offset:25600\n"
      "ds_read_b64_tr_b16 a[224:225], v95 offset:0\n"
      "ds_read_b64_tr_b16 a[226:227], v95 offset:768\n"
      "ds_read_b64_tr_b16 a[220:221], v95 offset:30976\n"
      "ds_read_b64_tr_b16 a[222:223], v95 offset:31744\n"
      "ds_read_b64_tr_b16 a[4:5], v95 offset:24896\n"
      "ds_read_b64_tr_b16 a[6:7], v95 offset:25664\n"
      "ds_read_b64_tr_b16 a[232:233], v95 offset:64\n"
      "ds_read_b64_tr_b16 a[234:235], v95 offset:832\n"
      "ds_read_b64_tr_b16 a[228:229], v95 offset:31040\n"
      "ds_read_b64_tr_b16 a[230:231], v95 offset:31808\n"
      "s_add_u32 m0, s63, 53504\n"
      "s_add_u32 s52, s63, 122880\n"
      "s_waitcnt lgkmcnt(10)\n"
      "v_mfma_f32_4x4x4_16b_bf16 v[28:31], a[244:245], a[0:1], 0\n"
      "v_mfma_f32_4x4x4_16b_bf16 v[32:35], a[244:245], a[2:3], 0\n"
      "buffer_load_dwordx4 v92, s[12:15], s52 offen offset:0 lds\n"
      "s_waitcnt lgkmcnt(8)\n"
      "v_mfma_f32_4x4x4_16b_bf16 v[28:31], a[248:249], a[224:225], v[28:31]\n"
      "ds_read_b64_tr_b16 a[8:9], v95 offset:24960\n"
      "v_mfma_f32_4x4x4_16b_bf16 v[32:35], a[248:249], a[226:227], v[32:35]\n"
      "buffer_load_dwordx4 v93, s[12:15], s52 offen offset:1024 lds\n"
      "s_waitcnt lgkmcnt(7)\n"
      "v_mfma_f32_4x4x4_16b_bf16 v[36:39], a[246:247], a[220:221], v[28:31]\n"
      "ds_read_b64_tr_b16 a[10:11], v95 offset:25728\n"
      "v_mfma_f32_4x4x4_16b_bf16 v[40:43], a[246:247], a[222:223], v[32:35]\n"
      "buffer_load_dwordx4 v94, s[12:15], s52 offen offset:2048 lds\n"
      "v_mfma_f32_4x4x4_16b_bf16 v[28:31], a[250:251], a[220:221], v[28:31]\n"
      "ds_read_b64_tr_b16 a[240:241], v95 offset:128\n"
      "v_mfma_f32_4x4x4_16b_bf16 v[32:35], a[250:251], a[222:223], v[32:35]\n"
      "ds_read_b64_tr_b16 a[242:243], v95 offset:896\n"
      "ds_read_b64_tr_b16 a[236:237], v95 offset:31104\n"
      "ds_read_b64_tr_b16 a[238:239], v95 offset:31872\n"
      "s_add_u32 m0, s63, 71936\n"
      "s_add_u32 s52, s63, 129024\n"
      "s_waitcnt lgkmcnt(5)\n"
      "v_mfma_f32_4x4x4_16b_bf16 v[76:79], a[244:245], a[4:5], 0\n"
      "v_mfma_f32_4x4x4_16b_bf16 v[80:83], a[244:245], a[6:7], 0\n"
      "buffer_load_dwordx4 v92, s[12:15], s52 offen offset:0 lds\n"
      "v_mfma_f32_4x4x4_16b_bf16 v[76:79], a[248:249], a[232:233], v[76:79]\n"
      "buffer_load_dwordx4 v93, s[12:15], s52 offen offset:1024 lds\n"
      "v_mfma_f32_4x4x4_16b_bf16 v[80:83], a[248:249], a[234:235], v[80:83]\n"
      "buffer_load_dwordx4 v94, s[12:15], s52 offen offset:2048 lds\n"
      "v_mfma_f32_4x4x4_16b_bf16 v[84:87], a[246:247], a[228:229], v[76:79]\n"
      "v_mfma_f32_4x4x4_16b_bf16 v[88:91], a[246:247], a[230:231], v[80:83]\n"
      "v_mfma_f32_4x4x4_16b_bf16 v[76:79], a[250:251], a[228:229], v[76:79]\n"
      "v_mfma_f32_4x4x4_16b_bf16 v[80:83], a[250:251], a[230:231], v[80:83]\n"
      "v_cvt_pk_bf16_f32 v252, v36, v40\n"
      "v_cvt_pk_bf16_f32 v253, v37, v41\n"
      "v_cvt_pk_bf16_f32 v254, v38, v42\n"
      "v_cvt_pk_bf16_f32 v255, v39, v43\n"
      "v_cvt_pk_bf16_f32 v248, v28, v32\n"
      "v_cvt_pk_bf16_f32 v249, v29, v33\n"
      "v_cvt_pk_bf16_f32 v250, v30, v34\n"
      "v_cvt_pk_bf16_f32 v251, v31, v35\n"
      "s_waitcnt lgkmcnt(0)\n"
      "v_mfma_f32_4x4x4_16b_bf16 v[28:31], a[244:245], a[8:9], 0\n"
      "v_mfma_f32_4x4x4_16b_bf16 v[32:35], a[244:245], a[10:11], 0\n"
      "s_nop 0\n"
      "v_mfma_f32_4x4x4_16b_bf16 v[28:31], a[248:249], a[240:241], v[28:31]\n"
      "v_mfma_f32_4x4x4_16b_bf16 v[32:35], a[248:249], a[242:243], v[32:35]\n"
      "s_nop 0\n"
      "s_nop 0\n"
      "v_mfma_f32_4x4x4_16b_bf16 v[36:39], a[246:247], a[236:237], v[28:31]\n"
      "v_mfma_f32_4x4x4_16b_bf16 v[40:43], a[246:247], a[238:239], v[32:35]\n"
      "v_mfma_f32_4x4x4_16b_bf16 v[28:31], a[250:251], a[236:237], v[28:31]\n"
      "v_mfma_f32_4x4x4_16b_bf16 v[32:35], a[250:251], a[238:239], v[32:35]\n"
      "v_cvt_pk_bf16_f32 v244, v84, v88\n"
      "v_cvt_pk_bf16_f32 v245, v85, v89\n"
      "v_cvt_pk_bf16_f32 v246, v86, v90\n"
      "v_cvt_pk_bf16_f32 v247, v87, v91\n"
      "v_cvt_pk_bf16_f32 v240, v76, v80\n"
      "v_cvt_pk_bf16_f32 v241, v77, v81\n"
      "v_cvt_pk_bf16_f32 v242, v78, v82\n"
      "v_cvt_pk_bf16_f32 v243, v79, v83\n"
      "s_waitcnt vmcnt(6)\n"
      "s_barrier\n"
      "ds_read_b64_tr_b16 a[12:13], v95 offset:6144\n"
      "ds_read_b64_tr_b16 a[14:15], v95 offset:6912\n"
      "ds_read_b64_tr_b16 a[224:225], v95 offset:18432\n"
      "ds_read_b64_tr_b16 a[226:227], v95 offset:19200\n"
      "ds_read_b64_tr_b16 a[220:221], v95 offset:12288\n"
      "ds_read_b64_tr_b16 a[222:223], v95 offset:13056\n"
      "s_add_u32 m0, s63, 128512\n"
      "s_add_u32 s52, s63, 135168\n"
      "s_waitcnt lgkmcnt(4)\n"
      "v_mfma_f32_4x4x4_16b_bf16 v[76:79], a[244:245], a[12:13], 0\n"
      "v_mfma_f32_4x4x4_16b_bf16 v[80:83], a[244:245], a[14:15], 0\n"
      "buffer_load_dwordx4 v92, s[12:15], s52 offen offset:0 lds\n"
      "s_waitcnt lgkmcnt(2)\n"
      "v_mfma_f32_4x4x4_16b_bf16 v[76:79], a[248:249], a[224:225], v[76:79]\n"
      "ds_read_b64_tr_b16 a[16:17], v95 offset:6208\n"
      "v_mfma_f32_4x4x4_16b_bf16 v[80:83], a[248:249], a[226:227], v[80:83]\n"
      "buffer_load_dwordx4 v93, s[12:15], s52 offen offset:1024 lds\n"
      "s_nop 0\n"
      "s_waitcnt lgkmcnt(1)\n"
      "v_mfma_f32_4x4x4_16b_bf16 v[84:87], a[246:247], a[220:221], v[76:79]\n"
      "ds_read_b64_tr_b16 a[18:19], v95 offset:6976\n"
      "v_mfma_f32_4x4x4_16b_bf16 v[88:91], a[246:247], a[222:223], v[80:83]\n"
      "buffer_load_dwordx4 v94, s[12:15], s52 offen offset:2048 lds\n"
      "v_mfma_f32_4x4x4_16b_bf16 v[76:79], a[250:251], a[220:221], v[76:79]\n"
      "ds_read_b64_tr_b16 a[232:233], v95 offset:18496\n"
      "v_mfma_f32_4x4x4_16b_bf16 v[80:83], a[250:251], a[222:223], v[80:83]\n"
      "ds_read_b64_tr_b16 a[234:235], v95 offset:19264\n"
      "ds_read_b64_tr_b16 a[228:229], v95 offset:12352\n"
      "ds_read_b64_tr_b16 a[230:231], v95 offset:13120\n"
      "ds_read_b64_tr_b16 a[20:21], v95 offset:6272\n"
      "ds_read_b64_tr_b16 a[22:23], v95 offset:7040\n"
      "ds_read_b64_tr_b16 a[240:241], v95 offset:18560\n"
      "ds_read_b64_tr_b16 a[242:243], v95 offset:19328\n"
      "ds_read_b64_tr_b16 a[236:237], v95 offset:12416\n"
      "ds_read_b64_tr_b16 a[238:239], v95 offset:13184\n"
      "ds_read_b64_tr_b16 a[188:189], v7 offset:2048\n"
      "ds_read_b64_tr_b16 a[190:191], v7 offset:2056\n"
      "ds_read_b128 a[192:195], v3 offset:55168\n"
      "v_cvt_pk_bf16_f32 v236, v36, v40\n"
      "v_cvt_pk_bf16_f32 v237, v37, v41\n"
      "v_cvt_pk_bf16_f32 v238, v38, v42\n"
      "v_cvt_pk_bf16_f32 v239, v39, v43\n"
      "v_cvt_pk_bf16_f32 v232, v28, v32\n"
      "v_cvt_pk_bf16_f32 v233, v29, v33\n"
      "v_cvt_pk_bf16_f32 v234, v30, v34\n"
      "v_cvt_pk_bf16_f32 v235, v31, v35\n"
      "s_waitcnt vmcnt(6)\n"
      "s_barrier\n"
      "s_nop 0\n"
      "s_waitcnt lgkmcnt(8)\n"
      "v_mfma_f32_4x4x4_16b_bf16 v[28:31], a[244:245], a[16:17], 0\n"
      "v_mfma_f32_4x4x4_16b_bf16 v[32:35], a[244:245], a[18:19], 0\n"
      "ds_read_b64_tr_b16 a[24:25], v95 offset:41216\n"
      "v_mfma_f32_4x4x4_16b_bf16 v[28:31], a[248:249], a[232:233], v[28:31]\n"
      "ds_read_b64_tr_b16 a[26:27], v95 offset:41984\n"
      "v_mfma_f32_4x4x4_16b_bf16 v[32:35], a[248:249], a[234:235], v[32:35]\n"
      "ds_read_b64_tr_b16 a[224:225], v95 offset:53504\n"
      "v_mfma_f32_4x4x4_16b_bf16 v[36:39], a[246:247], a[228:229], v[28:31]\n"
      "ds_read_b64_tr_b16 a[226:227], v95 offset:54272\n"
      "v_mfma_f32_4x4x4_16b_bf16 v[40:43], a[246:247], a[230:231], v[32:35]\n"
      "ds_read_b64_tr_b16 a[220:221], v95 offset:47360\n"
      "v_mfma_f32_4x4x4_16b_bf16 v[28:31], a[250:251], a[228:229], v[28:31]\n"
      "ds_read_b64_tr_b16 a[222:223], v95 offset:48128\n"
      "v_mfma_f32_4x4x4_16b_bf16 v[32:35], a[250:251], a[230:231], v[32:35]\n"
      "ds_read_b64_tr_b16 a[184:185], v7 offset:2560\n"
      "s_waitcnt lgkmcnt(7)\n"
      "v_mfma_f32_4x4x4_16b_bf16 v[44:47], a[216:217], a[188:189], 0\n"
      "ds_read_b64_tr_b16 a[186:187], v7 offset:2568\n"
      "v_mfma_f32_4x4x4_16b_bf16 v[48:51], a[216:217], a[190:191], 0\n"
      "ds_read_b128 a[196:199], v3 offset:55232\n"
      "v_cvt_pk_bf16_f32 v228, v84, v88\n"
      "v_cvt_pk_bf16_f32 v229, v85, v89\n"
      "v_cvt_pk_bf16_f32 v230, v86, v90\n"
      "v_cvt_pk_bf16_f32 v231, v87, v91\n"
      "v_cvt_pk_bf16_f32 v224, v76, v80\n"
      "v_cvt_pk_bf16_f32 v225, v77, v81\n"
      "v_cvt_pk_bf16_f32 v226, v78, v82\n"
      "v_cvt_pk_bf16_f32 v227, v79, v83\n"
      "s_add_u32 m0, s63, 0\n"
      "s_add_u32 s52, s63, 141312\n"
      "v_mfma_f32_4x4x4_16b_bf16 v[76:79], a[244:245], a[20:21], 0\n"
      "v_mfma_f32_4x4x4_16b_bf16 v[80:83], a[244:245], a[22:23], 0\n"
      "buffer_load_dwordx4 v92, s[12:15], s52 offen offset:0 lds\n"
      "v_mfma_f32_4x4x4_16b_bf16 v[76:79], a[248:249], a[240:241], v[76:79]\n"
      "ds_read_b64_tr_b16 a[28:29], v95 offset:41280\n"
      "v_mfma_f32_4x4x4_16b_bf16 v[80:83], a[248:249], a[242:243], v[80:83]\n"
      "buffer_load_dwordx4 v93, s[12:15], s52 offen offset:1024 lds\n"
      "v_mfma_f32_4x4x4_16b_bf16 v[84:87], a[246:247], a[236:237], v[76:79]\n"
      "ds_read_b64_tr_b16 a[30:31], v95 offset:42048\n"
      "v_mfma_f32_4x4x4_16b_bf16 v[88:91], a[246:247], a[238:239], v[80:83]\n"
      "buffer_load_dwordx4 v94, s[12:15], s52 offen offset:2048 lds\n"
      "v_mfma_f32_4x4x4_16b_bf16 v[76:79], a[250:251], a[236:237], v[76:79]\n"
      "ds_read_b64_tr_b16 a[232:233], v95 offset:53568\n"
      "v_mfma_f32_4x4x4_16b_bf16 v[80:83], a[250:251], a[238:239], v[80:83]\n"
      "s_add_u32 s53, s54, 199680\n"
      "s_nop 0\n"
      "s_waitcnt lgkmcnt(3)\n"
      "v_mfma_f32_4x4x4_16b_bf16 v[112:115], a[216:217], a[184:185], 0\n"
      "ds_read_b64_tr_b16 a[234:235], v95 offset:54336\n"
      "v_mfma_f32_4x4x4_16b_bf16 v[116:119], a[216:217], a[186:187], 0\n"
      "s_add_u32 m0, s37, 99328\n"
      "ds_read_b64_tr_b16 a[228:229], v95 offset:47424\n"
      "buffer_load_dwordx4 v10, s[4:7], s53 offen offset:0 nt lds\n"
      "buffer_load_dwordx4 v11, s[4:7], s53 offen offset:1024 nt lds\n"
      "ds_read_b64_tr_b16 a[230:231], v95 offset:48192\n"
      "s_add_u32 s53, s54, 396288\n"
      "ds_read_b64_tr_b16 a[180:181], v7 offset:3072\n"
      "s_add_u32 m0, s37, 103432\n"
      "ds_read_b64_tr_b16 a[182:183], v7 offset:3080\n"
      "buffer_load_dwordx4 v10, s[4:7], s53 offen offset:0 nt lds\n"
      "buffer_load_dwordx4 v11, s[4:7], s53 offen offset:1024 nt lds\n"
      "v_cvt_pk_bf16_f32 v220, v36, v40\n"
      "v_cvt_pk_bf16_f32 v221, v37, v41\n"
      "v_cvt_pk_bf16_f32 v222, v38, v42\n"
      "v_cvt_pk_bf16_f32 v223, v39, v43\n"
      "v_cvt_pk_bf16_f32 v216, v28, v32\n"
      "v_cvt_pk_bf16_f32 v217, v29, v33\n"
      "v_mfma_f32_16x16x32_bf16 v[100:103], a[0:3], a[192:195], 0\n"
      "v_cvt_pk_bf16_f32 v218, v30, v34\n"
      "v_cvt_pk_bf16_f32 v219, v31, v35\n"
      "v_mfma_f32_16x16x32_bf16 v[104:107], a[4:7], a[192:195], 0\n"
      "s_add_u32 m0, s37, 107656\n"
      "v_cvt_pk_bf16_f32 v44, v44, v48\n"
      "v_cvt_pk_bf16_f32 v45, v45, v49\n"
      "v_mfma_f32_16x16x32_bf16 v[108:111], a[8:11], a[192:195], 0\n"
      "s_add_u32 s53, s54, 592896\n"
      "v_cvt_pk_bf16_f32 v46, v46, v50\n"
      "v_cvt_pk_bf16_f32 v47, v47, v51\n"
      "v_mfma_f32_4x4x4_16b_bf16 v[28:31], a[244:245], a[24:25], 0\n"
      "v_mfma_f32_4x4x4_16b_bf16 v[32:35], a[244:245], a[26:27], 0\n"
      "buffer_load_dwordx4 v10, s[4:7], s53 offen offset:0 nt lds\n"
      "buffer_load_dwordx4 v11, s[4:7], s53 offen offset:1024 nt lds\n"
      "v_mfma_f32_4x4x4_16b_bf16 v[28:31], a[248:249], a[224:225], v[28:31]\n"
      "ds_read_b64_tr_b16 a[32:33], v95 offset:41344\n"
      "v_mfma_f32_4x4x4_16b_bf16 v[32:35], a[248:249], a[226:227], v[32:35]\n"
      "s_add_u32 s53, s54, 789504\n"
      "v_mfma_f32_4x4x4_16b_bf16 v[36:39], a[246:247], a[220:221], v[28:31]\n"
      "ds_read_b64_tr_b16 a[34:35], v95 offset:42112\n"
      "v_mfma_f32_4x4x4_16b_bf16 v[40:43], a[246:247], a[222:223], v[32:35]\n"
      "s_add_u32 m0, s37, 112000\n"
      "v_mfma_f32_4x4x4_16b_bf16 v[28:31], a[250:251], a[220:221], v[28:31]\n"
      "ds_read_b64_tr_b16 a[240:241], v95 offset:53632\n"
      "v_mfma_f32_4x4x4_16b_bf16 v[32:35], a[250:251], a[222:223], v[32:35]\n"
      "buffer_load_dwordx4 v10, s[4:7], s53 offen offset:0 nt lds\n"
      "buffer_load_dwordx4 v11, s[4:7], s53 offen offset:1024 nt lds\n"
      "s_nop 0\n"
      "s_waitcnt lgkmcnt(3)\n"
      "v_mfma_f32_4x4x4_16b_bf16 v[20:23], a[216:217], a[180:181], 0\n"
      "ds_read_b64_tr_b16 a[242:243], v95 offset:54400\n"
      "v_mfma_f32_4x4x4_16b_bf16 v[24:27], a[216:217], a[182:183], 0\n"
      "s_add_u32 s53, s36, 3072\n"
      "ds_read_b64_tr_b16 a[236:237], v95 offset:47488\n"
      "s_add_u32 m0, s37, 154368\n"
      "ds_read_b64_tr_b16 a[238:239], v95 offset:48256\n"
      "buffer_load_dwordx4 v10, s[4:7], s53 offen offset:0 nt lds\n"
      "buffer_load_dwordx4 v11, s[4:7], s53 offen offset:1024 nt lds\n"
      "ds_read_b64_tr_b16 a[176:177], v7 offset:3584\n"
      "ds_read_b64_tr_b16 a[178:179], v7 offset:3592\n"
      "s_mov_b32 m0, s64\n"
      "s_nop 0\n"
      "ds_write_addtid_b32 v44 offset:2048\n"
      "ds_write_addtid_b32 v45 offset:6144\n"
      "ds_write_addtid_b32 v46 offset:10240\n"
      "ds_write_addtid_b32 v47 offset:14336\n"
      "v_cvt_pk_bf16_f32 v212, v84, v88\n"
      "v_cvt_pk_bf16_f32 v213, v85, v89\n"
      "v_cvt_pk_bf16_f32 v214, v86, v90\n"
      "v_cvt_pk_bf16_f32 v215, v87, v91\n"
      "v_cvt_pk_bf16_f32 v208, v76, v80\n"
      "v_cvt_pk_bf16_f32 v209, v77, v81\n"
      "v_mfma_f32_16x16x32_bf16 v[100:103], a[12:15], a[196:199], v[100:103]\n"
      "v_cvt_pk_bf16_f32 v210, v78, v82\n"
      "v_cvt_pk_bf16_f32 v211, v79, v83\n"
      "v_mfma_f32_16x16x32_bf16 v[104:107], a[16:19], a[196:199], v[104:107]\n"
      "v_cvt_pk_bf16_f32 v112, v112, v116\n"
      "v_cvt_pk_bf16_f32 v113, v113, v117\n"
      "v_mfma_f32_16x16x32_bf16 v[108:111], a[20:23], a[196:199], v[108:111]\n"
      "s_waitcnt vmcnt(16)\n"
      "v_cvt_pk_bf16_f32 v114, v114, v118\n"
      "v_cvt_pk_bf16_f32 v115, v115, v119\n"
      "s_barrier\n"
      "v_mfma_f32_4x4x4_16b_bf16 v[76:79], a[244:245], a[28:29], 0\n"
      "v_mfma_f32_4x4x4_16b_bf16 v[80:83], a[244:245], a[30:31], 0\n"
      "ds_read_b64_tr_b16 a[36:37], v96 offset:2304\n"
      "v_mfma_f32_4x4x4_16b_bf16 v[76:79], a[248:249], a[232:233], v[76:79]\n"
      "ds_read_b64_tr_b16 a[38:39], v96 offset:3072\n"
      "v_mfma_f32_4x4x4_16b_bf16 v[80:83], a[248:249], a[234:235], v[80:83]\n"
      "ds_read_b64_tr_b16 a[224:225], v96 offset:14592\n"
      "v_mfma_f32_4x4x4_16b_bf16 v[84:87], a[246:247], a[228:229], v[76:79]\n"
      "ds_read_b64_tr_b16 a[226:227], v96 offset:15360\n"
      "v_mfma_f32_4x4x4_16b_bf16 v[88:91], a[246:247], a[230:231], v[80:83]\n"
      "ds_read_b64_tr_b16 a[220:221], v96 offset:8448\n"
      "v_mfma_f32_4x4x4_16b_bf16 v[76:79], a[250:251], a[228:229], v[76:79]\n"
      "ds_read_b64_tr_b16 a[222:223], v96 offset:9216\n"
      "v_mfma_f32_4x4x4_16b_bf16 v[80:83], a[250:251], a[230:231], v[80:83]\n"
      "ds_write_addtid_b32 v112 offset:2560\n"
      "s_nop 0\n"
      "s_waitcnt lgkmcnt(7)\n"
      "v_mfma_f32_4x4x4_16b_bf16 v[124:127], a[216:217], a[176:177], 0\n"
      "ds_write_addtid_b32 v113 offset:6656\n"
      "v_mfma_f32_4x4x4_16b_bf16 v[128:131], a[216:217], a[178:179], 0\n"
      "ds_write_addtid_b32 v114 offset:10752\n"
      "ds_write_addtid_b32 v115 offset:14848\n"
      "ds_read_b128 a[200:203], v3 offset:55296\n"
      "v_cvt_pk_bf16_f32 v204, v36, v40\n"
      "v_cvt_pk_bf16_f32 v205, v37, v41\n"
      "v_cvt_pk_bf16_f32 v206, v38, v42\n"
      "v_cvt_pk_bf16_f32 v207, v39, v43\n"
      "v_cvt_pk_bf16_f32 v200, v28, v32\n"
      "v_cvt_pk_bf16_f32 v201, v29, v33\n"
      "v_cvt_pk_bf16_f32 v202, v30, v34\n"
      "v_cvt_pk_bf16_f32 v203, v31, v35\n"
      "v_cvt_pk_bf16_f32 v20, v20, v24\n"
      "v_cvt_pk_bf16_f32 v21, v21, v25\n"
      "v_cvt_pk_bf16_f32 v22, v22, v26\n"
      "v_cvt_pk_bf16_f32 v23, v23, v27\n"
      "v_mfma_f32_4x4x4_16b_bf16 v[28:31], a[244:245], a[32:33], 0\n"
      "v_mfma_f32_4x4x4_16b_bf16 v[32:35], a[244:245], a[34:35], 0\n"
      "ds_read_b64_tr_b16 a[40:41], v96 offset:2368\n"
      "v_mfma_f32_4x4x4_16b_bf16 v[28:31], a[248:249], a[240:241], v[28:31]\n"
      "ds_read_b64_tr_b16 a[42:43], v96 offset:3136\n"
      "v_mfma_f32_4x4x4_16b_bf16 v[32:35], a[248:249], a[242:243], v[32:35]\n"
      "ds_read_b64_tr_b16 a[232:233], v96 offset:14656\n"
      "v_mfma_f32_4x4x4_16b_bf16 v[36:39], a[246:247], a[236:237], v[28:31]\n"
      "ds_read_b64_tr_b16 a[234:235], v96 offset:15424\n"
      "v_mfma_f32_4x4x4_16b_bf16 v[40:43], a[246:247], a[238:239], v[32:35]\n"
      "ds_read_b64_tr_b16 a[228:229], v96 offset:8512\n"
      "v_mfma_f32_4x4x4_16b_bf16 v[28:31], a[250:251], a[236:237], v[28:31]\n"
      "ds_read_b64_tr_b16 a[230:231], v96 offset:9280\n"
      "v_mfma_f32_4x4x4_16b_bf16 v[32:35], a[250:251], a[238:239], v[32:35]\n"
      "ds_write_addtid_b32 v20 offset:3072\n"
      "ds_write_addtid_b32 v21 offset:7168\n"
      "ds_write_addtid_b32 v22 offset:11264\n"
      "ds_write_addtid_b32 v23 offset:15360\n"
      "v_cvt_pk_bf16_f32 v196, v84, v88\n"
      "v_cvt_pk_bf16_f32 v197, v85, v89\n"
      "v_cvt_pk_bf16_f32 v198, v86, v90\n"
      "v_cvt_pk_bf16_f32 v199, v87, v91\n"
      "v_cvt_pk_bf16_f32 v192, v76, v80\n"
      "v_cvt_pk_bf16_f32 v193, v77, v81\n"
      "v_cvt_pk_bf16_f32 v194, v78, v82\n"
      "v_cvt_pk_bf16_f32 v195, v79, v83\n"
      "v_cvt_pk_bf16_f32 v124, v124, v128\n"
      "v_cvt_pk_bf16_f32 v125, v125, v129\n"
      "v_cvt_pk_bf16_f32 v126, v126, v130\n"
      "v_cvt_pk_bf16_f32 v127, v127, v131\n"
      "s_add_u32 m0, s63, 18432\n"
      "s_add_u32 s52, s63, 147456\n"
      "s_nop 0\n"
      "s_waitcnt lgkmcnt(3)\n"
      "v_mfma_f32_4x4x4_16b_bf16 v[76:79], a[244:245], a[36:37], 0\n"
      "v_mfma_f32_4x4x4_16b_bf16 v[80:83], a[244:245], a[38:39], 0\n"
      "buffer_load_dwordx4 v92, s[12:15], s52 offen offset:0 lds\n"
      "v_mfma_f32_4x4x4_16b_bf16 v[76:79], a[248:249], a[224:225], v[76:79]\n"
      "ds_read_b64_tr_b16 a[44:45], v96 offset:2432\n"
      "v_mfma_f32_4x4x4_16b_bf16 v[80:83], a[248:249], a[226:227], v[80:83]\n"
      "buffer_load_dwordx4 v93, s[12:15], s52 offen offset:1024 lds\n"
      "v_mfma_f32_4x4x4_16b_bf16 v[84:87], a[246:247], a[220:221], v[76:79]\n"
      "ds_read_b64_tr_b16 a[46:47], v96 offset:3200\n"
      "v_mfma_f32_4x4x4_16b_bf16 v[88:91], a[246:247], a[222:223], v[80:83]\n"
      "buffer_load_dwordx4 v94, s[12:15], s52 offen offset:2048 lds\n"
      "v_mfma_f32_4x4x4_16b_bf16 v[76:79], a[250:251], a[220:221], v[76:79]\n"
      "ds_read_b64_tr_b16 a[240:241], v96 offset:14720\n"
      "v_mfma_f32_4x4x4_16b_bf16 v[80:83], a[250:251], a[222:223], v[80:83]\n"
      "ds_read_b64_tr_b16 a[242:243], v96 offset:15488\n"
      "ds_read_b64_tr_b16 a[236:237], v96 offset:8576\n"
      "ds_read_b64_tr_b16 a[238:239], v96 offset:9344\n"
      "s_mov_b32 m0, s64\n"
      "s_nop 0\n"
      "ds_write_addtid_b32 v124 offset:3584\n"
      "ds_write_addtid_b32 v125 offset:7680\n"
      "ds_write_addtid_b32 v126 offset:11776\n"
      "ds_write_addtid_b32 v127 offset:15872\n"
      "v_cvt_pk_bf16_f32 v188, v36, v40\n"
      "v_cvt_pk_bf16_f32 v189, v37, v41\n"
      "v_mfma_f32_16x16x32_bf16 v[100:103], a[24:27], a[200:203], v[100:103]\n"
      "s_nop 0\n"
      "v_cvt_pk_bf16_f32 v190, v38, v42\n"
      "v_cvt_pk_bf16_f32 v191, v39, v43\n"
      "v_mfma_f32_16x16x32_bf16 v[104:107], a[28:31], a[200:203], v[104:107]\n"
      "s_waitcnt vmcnt(16)\n"
      "v_cvt_pk_bf16_f32 v184, v28, v32\n"
      "v_cvt_pk_bf16_f32 v185, v29, v33\n"
      "v_mfma_f32_16x16x32_bf16 v[108:111], a[32:35], a[200:203], v[108:111]\n"
      "s_waitcnt lgkmcnt(0)\n"
      "v_cvt_pk_bf16_f32 v186, v30, v34\n"
      "v_cvt_pk_bf16_f32 v187, v31, v35\n"
      "s_barrier\n"
      "v_mfma_f32_4x4x4_16b_bf16 v[28:31], a[244:245], a[40:41], 0\n"
      "v_mfma_f32_4x4x4_16b_bf16 v[32:35], a[244:245], a[42:43], 0\n"
      "ds_read_b64_tr_b16 a[48:49], v97 offset:1536\n"
      "v_mfma_f32_4x4x4_16b_bf16 v[28:31], a[248:249], a[232:233], v[28:31]\n"
      "ds_read_b64_tr_b16 a[50:51], v97 offset:2304\n"
      "v_mfma_f32_4x4x4_16b_bf16 v[32:35], a[248:249], a[234:235], v[32:35]\n"
      "ds_read_b64_tr_b16 a[224:225], v97 offset:13824\n"
      "v_mfma_f32_4x4x4_16b_bf16 v[36:39], a[246:247], a[228:229], v[28:31]\n"
      "ds_read_b64_tr_b16 a[226:227], v97 offset:14592\n"
      "v_mfma_f32_4x4x4_16b_bf16 v[40:43], a[246:247], a[230:231], v[32:35]\n"
      "ds_read_b64_tr_b16 a[220:221], v97 offset:7680\n"
      "v_mfma_f32_4x4x4_16b_bf16 v[28:31], a[250:251], a[228:229], v[28:31]\n"
      "ds_read_b64_tr_b16 a[222:223], v97 offset:8448\n"
      "v_mfma_f32_4x4x4_16b_bf16 v[32:35], a[250:251], a[230:231], v[32:35]\n"
      "ds_read_b128 a[204:207], v4 offset:55168\n"
      "ds_read_b128 a[208:211], v0 offset:0\n"
      "ds_read_b128 a[212:215], v0 offset:4096\n"
      "v_cvt_pk_bf16_f32 v180, v84, v88\n"
      "v_cvt_pk_bf16_f32 v181, v85, v89\n"
      "v_cvt_pk_bf16_f32 v182, v86, v90\n"
      "v_cvt_pk_bf16_f32 v183, v87, v91\n"
      "v_cvt_pk_bf16_f32 v176, v76, v80\n"
      "v_cvt_pk_bf16_f32 v177, v77, v81\n"
      "v_cvt_pk_bf16_f32 v178, v78, v82\n"
      "v_cvt_pk_bf16_f32 v179, v79, v83\n"
      "v_mfma_f32_4x4x4_16b_bf16 v[76:79], a[244:245], a[44:45], 0\n"
      "v_mfma_f32_4x4x4_16b_bf16 v[80:83], a[244:245], a[46:47], 0\n"
      "ds_read_b64_tr_b16 a[52:53], v97 offset:1600\n"
      "v_mfma_f32_4x4x4_16b_bf16 v[76:79], a[248:249], a[240:241], v[76:79]\n"
      "ds_read_b64_tr_b16 a[54:55], v97 offset:2368\n"
      "v_mfma_f32_4x4x4_16b_bf16 v[80:83], a[248:249], a[242:243], v[80:83]\n"
      "ds_read_b64_tr_b16 a[232:233], v97 offset:13888\n"
      "v_mfma_f32_4x4x4_16b_bf16 v[84:87], a[246:247], a[236:237], v[76:79]\n"
      "ds_read_b64_tr_b16 a[234:235], v97 offset:14656\n"
      "v_mfma_f32_4x4x4_16b_bf16 v[88:91], a[246:247], a[238:239], v[80:83]\n"
      "ds_read_b64_tr_b16 a[228:229], v97 offset:7744\n"
      "v_mfma_f32_4x4x4_16b_bf16 v[76:79], a[250:251], a[236:237], v[76:79]\n"
      "ds_read_b64_tr_b16 a[230:231], v97 offset:8512\n"
      "v_mfma_f32_4x4x4_16b_bf16 v[80:83], a[250:251], a[238:239], v[80:83]\n"
      "ds_read_b128 a[192:195], v0 offset:64\n"
      "ds_read_b128 a[196:199], v0 offset:4160\n"
      "ds_read_b128 a[200:203], v0 offset:128\n"
      "v_cvt_pk_bf16_f32 v172, v36, v40\n"
      "v_cvt_pk_bf16_f32 v173, v37, v41\n"
      "v_cvt_pk_bf16_f32 v174, v38, v42\n"
      "v_cvt_pk_bf16_f32 v175, v39, v43\n"
      "v_cvt_pk_bf16_f32 v168, v28, v32\n"
      "v_cvt_pk_bf16_f32 v169, v29, v33\n"
      "v_cvt_pk_bf16_f32 v170, v30, v34\n"
      "v_cvt_pk_bf16_f32 v171, v31, v35\n"
      "s_add_u32 m0, s63, 53504\n"
      "s_add_u32 s52, s63, 153600\n"
      "s_nop 0\n"
      "s_waitcnt lgkmcnt(4)\n"
      "v_mfma_f32_4x4x4_16b_bf16 v[28:31], a[244:245], a[48:49], 0\n"
      "v_mfma_f32_4x4x4_16b_bf16 v[32:35], a[244:245], a[50:51], 0\n"
      "buffer_load_dwordx4 v92, s[12:15], s52 offen offset:0 lds\n"
      "v_mfma_f32_4x4x4_16b_bf16 v[28:31], a[248:249], a[224:225], v[28:31]\n"
      "ds_read_b64_tr_b16 a[56:57], v97 offset:1664\n"
      "v_mfma_f32_4x4x4_16b_bf16 v[32:35], a[248:249], a[226:227], v[32:35]\n"
      "buffer_load_dwordx4 v93, s[12:15], s52 offen offset:1024 lds\n"
      "v_mfma_f32_4x4x4_16b_bf16 v[36:39], a[246:247], a[220:221], v[28:31]\n"
      "ds_read_b64_tr_b16 a[58:59], v97 offset:2432\n"
      "v_mfma_f32_4x4x4_16b_bf16 v[40:43], a[246:247], a[222:223], v[32:35]\n"
      "buffer_load_dwordx4 v94, s[12:15], s52 offen offset:2048 lds\n"
      "v_mfma_f32_4x4x4_16b_bf16 v[28:31], a[250:251], a[220:221], v[28:31]\n"
      "ds_read_b64_tr_b16 a[240:241], v97 offset:13952\n"
      "v_mfma_f32_4x4x4_16b_bf16 v[32:35], a[250:251], a[222:223], v[32:35]\n"
      "ds_read_b64_tr_b16 a[242:243], v97 offset:14720\n"
      "ds_read_b64_tr_b16 a[236:237], v97 offset:7808\n"
      "ds_read_b64_tr_b16 a[238:239], v97 offset:8576\n"
      "v_mfma_f32_16x16x32_bf16 v[100:103], a[36:39], a[204:207], v[100:103]\n"
      "v_cvt_pk_bf16_f32 v164, v84, v88\n"
      "v_cvt_pk_bf16_f32 v165, v85, v89\n"
      "v_mfma_f32_16x16x32_bf16 v[104:107], a[40:43], a[204:207], v[104:107]\n"
      "v_cvt_pk_bf16_f32 v166, v86, v90\n"
      "v_cvt_pk_bf16_f32 v167, v87, v91\n"
      "v_mfma_f32_16x16x32_bf16 v[108:111], a[44:47], a[204:207], v[108:111]\n"
      "v_cvt_pk_bf16_f32 v160, v76, v80\n"
      "v_cvt_pk_bf16_f32 v161, v77, v81\n"
      "v_mfma_f32_16x16x32_bf16 v[52:55], v[252:255], a[208:211], 0\n"
      "s_waitcnt vmcnt(16)\n"
      "v_cvt_pk_bf16_f32 v162, v78, v82\n"
      "v_cvt_pk_bf16_f32 v163, v79, v83\n"
      "s_barrier\n"
      "s_add_u32 m0, s63, 71936\n"
      "s_add_u32 s52, s63, 159744\n"
      "v_mfma_f32_4x4x4_16b_bf16 v[76:79], a[244:245], a[52:53], 0\n"
      "v_mfma_f32_4x4x4_16b_bf16 v[80:83], a[244:245], a[54:55], 0\n"
      "buffer_load_dwordx4 v92, s[12:15], s52 offen offset:0 lds\n"
      "v_mfma_f32_4x4x4_16b_bf16 v[76:79], a[248:249], a[232:233], v[76:79]\n"
      "ds_read_b64_tr_b16 a[60:61], v97 offset:19968\n"
      "v_mfma_f32_4x4x4_16b_bf16 v[80:83], a[248:249], a[234:235], v[80:83]\n"
      "buffer_load_dwordx4 v93, s[12:15], s52 offen offset:1024 lds\n"
      "s_nop 0\n"
      "s_waitcnt lgkmcnt(1)\n"
      "v_mfma_f32_4x4x4_16b_bf16 v[84:87], a[246:247], a[228:229], v[76:79]\n"
      "ds_read_b64_tr_b16 a[62:63], v97 offset:20736\n"
      "v_mfma_f32_4x4x4_16b_bf16 v[88:91], a[246:247], a[230:231], v[80:83]\n"
      "buffer_load_dwordx4 v94, s[12:15], s52 offen offset:2048 lds\n"
      "v_mfma_f32_4x4x4_16b_bf16 v[76:79], a[250:251], a[228:229], v[76:79]\n"
      "ds_read_b64_tr_b16 a[224:225], v95 offset:0\n"
      "v_mfma_f32_4x4x4_16b_bf16 v[80:83], a[250:251], a[230:231], v[80:83]\n"
      "ds_read_b64_tr_b16 a[226:227], v95 offset:768\n"
      "ds_read_b64_tr_b16 a[220:221], v97 offset:26112\n"
      "ds_read_b64_tr_b16 a[222:223], v97 offset:26880\n"
      "ds_read_b128 a[204:207], v0 offset:4224\n"
      "v_mfma_f32_16x16x32_bf16 v[56:59], v[244:247], a[208:211], 0\n"
      "v_cvt_pk_bf16_f32 v156, v36, v40\n"
      "v_cvt_pk_bf16_f32 v157, v37, v41\n"
      "v_mfma_f32_16x16x32_bf16 v[60:63], v[236:239], a[208:211], 0\n"
      "v_cvt_pk_bf16_f32 v158, v38, v42\n"
      "v_cvt_pk_bf16_f32 v159, v39, v43\n"
      "v_mfma_f32_16x16x32_bf16 v[64:67], v[248:251], a[212:215], 0\n"
      "v_cvt_pk_bf16_f32 v152, v28, v32\n"
      "v_cvt_pk_bf16_f32 v153, v29, v33\n"
      "v_mfma_f32_16x16x32_bf16 v[68:71], v[240:243], a[212:215], 0\n"
      "v_cvt_pk_bf16_f32 v154, v30, v34\n"
      "v_cvt_pk_bf16_f32 v155, v31, v35\n"
      "v_mfma_f32_4x4x4_16b_bf16 v[28:31], a[244:245], a[56:57], 0\n"
      "v_mfma_f32_4x4x4_16b_bf16 v[32:35], a[244:245], a[58:59], 0\n"
      "ds_read_b64_tr_b16 a[64:65], v97 offset:20032\n"
      "v_mfma_f32_4x4x4_16b_bf16 v[28:31], a[248:249], a[240:241], v[28:31]\n"
      "ds_read_b64_tr_b16 a[66:67], v97 offset:20800\n"
      "v_mfma_f32_4x4x4_16b_bf16 v[32:35], a[248:249], a[242:243], v[32:35]\n"
      "ds_read_b64_tr_b16 a[232:233], v95 offset:64\n"
      "v_mfma_f32_4x4x4_16b_bf16 v[36:39], a[246:247], a[236:237], v[28:31]\n"
      "ds_read_b64_tr_b16 a[234:235], v95 offset:832\n"
      "v_mfma_f32_4x4x4_16b_bf16 v[40:43], a[246:247], a[238:239], v[32:35]\n"
      "ds_read_b64_tr_b16 a[228:229], v97 offset:26176\n"
      "v_mfma_f32_4x4x4_16b_bf16 v[28:31], a[250:251], a[236:237], v[28:31]\n"
      "ds_read_b64_tr_b16 a[230:231], v97 offset:26944\n"
      "v_mfma_f32_4x4x4_16b_bf16 v[32:35], a[250:251], a[238:239], v[32:35]\n"
      "ds_read_b128 a[208:211], v1 offset:0\n"
      "v_mfma_f32_16x16x32_bf16 v[72:75], v[232:235], a[212:215], 0\n"
      "s_nop 0\n"
      "v_cvt_pk_bf16_f32 v148, v84, v88\n"
      "v_cvt_pk_bf16_f32 v149, v85, v89\n"
      "v_mfma_f32_16x16x32_bf16 v[52:55], v[228:231], a[192:195], v[52:55]\n"
      "s_nop 0\n"
      "v_cvt_pk_bf16_f32 v150, v86, v90\n"
      "v_cvt_pk_bf16_f32 v151, v87, v91\n"
      "v_mfma_f32_16x16x32_bf16 v[56:59], v[220:223], a[192:195], v[56:59]\n"
      "s_nop 0\n"
      "v_cvt_pk_bf16_f32 v144, v76, v80\n"
      "v_cvt_pk_bf16_f32 v145, v77, v81\n"
      "v_mfma_f32_16x16x32_bf16 v[60:63], v[212:215], a[192:195], v[60:63]\n"
      "s_waitcnt lgkmcnt(1)\n"
      "v_cvt_pk_bf16_f32 v146, v78, v82\n"
      "v_cvt_pk_bf16_f32 v147, v79, v83\n"
      "v_mfma_f32_4x4x4_16b_bf16 v[76:79], a[244:245], a[60:61], 0\n"
      "v_mfma_f32_4x4x4_16b_bf16 v[80:83], a[244:245], a[62:63], 0\n"
      "ds_read_b64_tr_b16 a[68:69], v97 offset:20096\n"
      "v_mfma_f32_4x4x4_16b_bf16 v[76:79], a[248:249], a[224:225], v[76:79]\n"
      "ds_read_b64_tr_b16 a[70:71], v97 offset:20864\n"
      "v_mfma_f32_4x4x4_16b_bf16 v[80:83], a[248:249], a[226:227], v[80:83]\n"
      "ds_read_b64_tr_b16 a[240:241], v95 offset:128\n"
      "v_mfma_f32_4x4x4_16b_bf16 v[84:87], a[246:247], a[220:221], v[76:79]\n"
      "ds_read_b64_tr_b16 a[242:243], v95 offset:896\n"
      "v_mfma_f32_4x4x4_16b_bf16 v[88:91], a[246:247], a[222:223], v[80:83]\n"
      "ds_read_b64_tr_b16 a[236:237], v97 offset:26240\n"
      "v_mfma_f32_4x4x4_16b_bf16 v[76:79], a[250:251], a[220:221], v[76:79]\n"
      "ds_read_b64_tr_b16 a[238:239], v97 offset:27008\n"
      "v_mfma_f32_4x4x4_16b_bf16 v[80:83], a[250:251], a[222:223], v[80:83]\n"
      "ds_read_b128 a[212:215], v1 offset:4096\n"
      "ds_read_b128 a[192:195], v4 offset:55232\n"
      "v_mfma_f32_16x16x32_bf16 v[64:67], v[224:227], a[196:199], v[64:67]\n"
      "v_cvt_pk_bf16_f32 v140, v36, v40\n"
      "v_cvt_pk_bf16_f32 v141, v37, v41\n"
      "v_mfma_f32_16x16x32_bf16 v[68:71], v[216:219], a[196:199], v[68:71]\n"
      "v_cvt_pk_bf16_f32 v142, v38, v42\n"
      "v_cvt_pk_bf16_f32 v143, v39, v43\n"
      "v_mfma_f32_16x16x32_bf16 v[72:75], v[208:211], a[196:199], v[72:75]\n"
      "v_cvt_pk_bf16_f32 v136, v28, v32\n"
      "v_cvt_pk_bf16_f32 v137, v29, v33\n"
      "v_mfma_f32_16x16x32_bf16 v[52:55], v[204:207], a[200:203], v[52:55]\n"
      "s_waitcnt vmcnt(6)\n"
      "v_cvt_pk_bf16_f32 v138, v30, v34\n"
      "v_cvt_pk_bf16_f32 v139, v31, v35\n"
      "s_barrier\n"
      "s_add_u32 s53, s54, 983040\n"
      "s_add_u32 m0, s37, 116224\n"
      "v_mfma_f32_4x4x4_16b_bf16 v[28:31], a[244:245], a[64:65], 0\n"
      "v_mfma_f32_4x4x4_16b_bf16 v[32:35], a[244:245], a[66:67], 0\n"
      "buffer_load_dwordx4 v8, s[4:7], s53 offen offset:0 nt lds\n"
      "buffer_load_dwordx4 v9, s[4:7], s53 offen offset:1024 nt lds\n"
      "v_mfma_f32_4x4x4_16b_bf16 v[28:31], a[248:249], a[232:233], v[28:31]\n"
      "ds_read_b64_tr_b16 a[72:73], v95 offset:6144\n"
      "v_mfma_f32_4x4x4_16b_bf16 v[32:35], a[248:249], a[234:235], v[32:35]\n"
      "ds_read_b64_tr_b16 a[74:75], v95 offset:6912\n"
      "v_mfma_f32_4x4x4_16b_bf16 v[36:39], a[246:247], a[228:229], v[28:31]\n"
      "ds_read_b64_tr_b16 a[224:225], v95 offset:18432\n"
      "v_mfma_f32_4x4x4_16b_bf16 v[40:43], a[246:247], a[230:231], v[32:35]\n"
      "ds_read_b64_tr_b16 a[226:227], v95 offset:19200\n"
      "v_mfma_f32_4x4x4_16b_bf16 v[28:31], a[250:251], a[228:229], v[28:31]\n"
      "ds_read_b64_tr_b16 a[220:221], v95 offset:12288\n"
      "v_mfma_f32_4x4x4_16b_bf16 v[32:35], a[250:251], a[230:231], v[32:35]\n"
      "ds_read_b64_tr_b16 a[222:223], v95 offset:13056\n"
      "ds_read_b128 a[196:199], v1 offset:64\n"
      "v_mfma_f32_16x16x32_bf16 v[56:59], v[196:199], a[200:203], v[56:59]\n"
      "v_cvt_pk_bf16_f32 v132, v84, v88\n"
      "v_cvt_pk_bf16_f32 v133, v85, v89\n"
      "v_mfma_f32_16x16x32_bf16 v[60:63], v[188:191], a[200:203], v[60:63]\n"
      "v_cvt_pk_bf16_f32 v134, v86, v90\n"
      "v_cvt_pk_bf16_f32 v135, v87, v91\n"
      "v_mfma_f32_16x16x32_bf16 v[64:67], v[200:203], a[204:207], v[64:67]\n"
      "s_add_u32 m0, s37, 120328\n"
      "v_cvt_pk_bf16_f32 v128, v76, v80\n"
      "v_cvt_pk_bf16_f32 v129, v77, v81\n"
      "v_mfma_f32_16x16x32_bf16 v[68:71], v[192:195], a[204:207], v[68:71]\n"
      "s_add_u32 s53, s54, 1179648\n"
      "v_cvt_pk_bf16_f32 v130, v78, v82\n"
      "v_cvt_pk_bf16_f32 v131, v79, v83\n"
      "s_nop 0\n"
      "s_waitcnt lgkmcnt(1)\n"
      "v_mfma_f32_4x4x4_16b_bf16 v[76:79], a[244:245], a[68:69], 0\n"
      "v_mfma_f32_4x4x4_16b_bf16 v[80:83], a[244:245], a[70:71], 0\n"
      "buffer_load_dwordx4 v8, s[4:7], s53 offen offset:0 nt lds\n"
      "buffer_load_dwordx4 v9, s[4:7], s53 offen offset:1024 nt lds\n"
      "v_mfma_f32_4x4x4_16b_bf16 v[76:79], a[248:249], a[240:241], v[76:79]\n"
      "ds_read_b64_tr_b16 a[76:77], v95 offset:6208\n"
      "v_mfma_f32_4x4x4_16b_bf16 v[80:83], a[248:249], a[242:243], v[80:83]\n"
      "ds_read_b64_tr_b16 a[78:79], v95 offset:6976\n"
      "v_mfma_f32_4x4x4_16b_bf16 v[84:87], a[246:247], a[236:237], v[76:79]\n"
      "ds_read_b64_tr_b16 a[232:233], v95 offset:18496\n"
      "v_mfma_f32_4x4x4_16b_bf16 v[88:91], a[246:247], a[238:239], v[80:83]\n"
      "ds_read_b64_tr_b16 a[234:235], v95 offset:19264\n"
      "v_mfma_f32_4x4x4_16b_bf16 v[76:79], a[250:251], a[236:237], v[76:79]\n"
      "ds_read_b64_tr_b16 a[228:229], v95 offset:12352\n"
      "v_mfma_f32_4x4x4_16b_bf16 v[80:83], a[250:251], a[238:239], v[80:83]\n"
      "ds_read_b64_tr_b16 a[230:231], v95 offset:13120\n"
      "ds_read_b128 a[200:203], v1 offset:4160\n"
      "v_mfma_f32_16x16x32_bf16 v[72:75], v[184:187], a[204:207], v[72:75]\n"
      "v_cvt_pk_bf16_f32 v124, v36, v40\n"
      "v_cvt_pk_bf16_f32 v125, v37, v41\n"
      "v_mfma_f32_16x16x32_bf16 v[52:55], v[180:183], a[208:211], v[52:55]\n"
      "v_cvt_pk_bf16_f32 v126, v38, v42\n"
      "v_cvt_pk_bf16_f32 v127, v39, v43\n"
      "v_mfma_f32_16x16x32_bf16 v[56:59], v[172:175], a[208:211], v[56:59]\n"
      "v_cvt_pk_bf16_f32 v28, v28, v32\n"
      "v_cvt_pk_bf16_f32 v29, v29, v33\n"
      "v_mfma_f32_16x16x32_bf16 v[60:63], v[164:167], a[208:211], v[60:63]\n"
      "v_cvt_pk_bf16_f32 v30, v30, v34\n"
      "v_cvt_pk_bf16_f32 v31, v31, v35\n"
      "v_mfma_f32_16x16x32_bf16 v[64:67], v[176:179], a[212:215], v[64:67]\n"
      "s_add_u32 m0, s37, 124552\n"
      "v_accvgpr_write_b32 a188, v28\n"
      "v_accvgpr_write_b32 a189, v29\n"
      "v_mfma_f32_16x16x32_bf16 v[68:71], v[168:171], a[212:215], v[68:71]\n"
      "s_add_u32 s53, s54, 1376256\n"
      "v_accvgpr_write_b32 a190, v30\n"
      "v_accvgpr_write_b32 a191, v31\n"
      "v_mfma_f32_4x4x4_16b_bf16 v[28:31], a[244:245], a[72:73], 0\n"
      "v_mfma_f32_4x4x4_16b_bf16 v[32:35], a[244:245], a[74:75], 0\n"
      "buffer_load_dwordx4 v8, s[4:7], s53 offen offset:0 nt lds\n"
      "buffer_load_dwordx4 v9, s[4:7], s53 offen offset:1024 nt lds\n"
      "v_mfma_f32_4x4x4_16b_bf16 v[28:31], a[248:249], a[224:225], v[28:31]\n"
      "ds_read_b64_tr_b16 a[80:81], v95 offset:6272\n"
      "v_mfma_f32_4x4x4_16b_bf16 v[32:35], a[248:249], a[226:227], v[32:35]\n"
      "ds_read_b64_tr_b16 a[82:83], v95 offset:7040\n"
      "v_mfma_f32_4x4x4_16b_bf16 v[36:39], a[246:247], a[220:221], v[28:31]\n"
      "ds_read_b64_tr_b16 a[240:241], v95 offset:18560\n"
      "v_mfma_f32_4x4x4_16b_bf16 v[40:43], a[246:247], a[222:223], v[32:35]\n"
      "ds_read_b64_tr_b16 a[242:243], v95 offset:19328\n"
      "v_mfma_f32_4x4x4_16b_bf16 v[28:31], a[250:251], a[220:221], v[28:31]\n"
      "ds_read_b64_tr_b16 a[236:237], v95 offset:12416\n"
      "v_mfma_f32_4x4x4_16b_bf16 v[32:35], a[250:251], a[222:223], v[32:35]\n"
      "ds_read_b64_tr_b16 a[238:239], v95 offset:13184\n"
      "ds_read_b128 a[204:207], v4 offset:55296\n"
      "ds_read_b128 a[208:211], v1 offset:128\n"
      "v_cvt_pk_bf16_f32 v84, v84, v88\n"
      "v_cvt_pk_bf16_f32 v85, v85, v89\n"
      "v_cvt_pk_bf16_f32 v86, v86, v90\n"
      "v_cvt_pk_bf16_f32 v87, v87, v91\n"
      "v_mfma_f32_16x16x32_bf16 v[72:75], v[160:163], a[212:215], v[72:75]\n"
      "v_accvgpr_write_b32 a184, v84\n"
      "v_accvgpr_write_b32 a185, v85\n"
      "v_mfma_f32_16x16x32_bf16 v[100:103], a[48:51], a[192:195], v[100:103]\n"
      "v_accvgpr_write_b32 a186, v86\n"
      "v_accvgpr_write_b32 a187, v87\n"
      "v_mfma_f32_16x16x32_bf16 v[104:107], a[52:55], a[192:195], v[104:107]\n"
      "v_cvt_pk_bf16_f32 v76, v76, v80\n"
      "v_cvt_pk_bf16_f32 v77, v77, v81\n"
      "v_mfma_f32_16x16x32_bf16 v[108:111], a[56:59], a[192:195], v[108:111]\n"
      "s_waitcnt lgkmcnt(1)\n"
      "v_cvt_pk_bf16_f32 v78, v78, v82\n"
      "v_cvt_pk_bf16_f32 v79, v79, v83\n"
      "v_mfma_f32_16x16x32_bf16 v[52:55], v[156:159], a[196:199], v[52:55]\n"
      "s_nop 0\n"
      "v_accvgpr_write_b32 a180, v76\n"
      "v_accvgpr_write_b32 a181, v77\n"
      "v_mfma_f32_16x16x32_bf16 v[56:59], v[148:151], a[196:199], v[56:59]\n"
      "s_waitcnt vmcnt(9)\n"
      "v_accvgpr_write_b32 a182, v78\n"
      "v_accvgpr_write_b32 a183, v79\n"
      "s_barrier\n"
      "s_add_u32 s53, s54, 1572864\n"
      "s_add_u32 m0, s37, 128896\n"
      "v_mfma_f32_4x4x4_16b_bf16 v[76:79], a[244:245], a[76:77], 0\n"
      "v_mfma_f32_4x4x4_16b_bf16 v[80:83], a[244:245], a[78:79], 0\n"
      "buffer_load_dwordx4 v8, s[4:7], s53 offen offset:0 nt lds\n"
      "buffer_load_dwordx4 v9, s[4:7], s53 offen offset:1024 nt lds\n"
      "v_mfma_f32_4x4x4_16b_bf16 v[76:79], a[248:249], a[232:233], v[76:79]\n"
      "ds_read_b64_tr_b16 a[84:85], v95 offset:41216\n"
      "v_mfma_f32_4x4x4_16b_bf16 v[80:83], a[248:249], a[234:235], v[80:83]\n"
      "ds_read_b64_tr_b16 a[86:87], v95 offset:41984\n"
      "v_mfma_f32_4x4x4_16b_bf16 v[84:87], a[246:247], a[228:229], v[76:79]\n"
      "ds_read_b64_tr_b16 a[224:225], v95 offset:53504\n"
      "v_mfma_f32_4x4x4_16b_bf16 v[88:91], a[246:247], a[230:231], v[80:83]\n"
      "ds_read_b64_tr_b16 a[226:227], v95 offset:54272\n"
      "v_mfma_f32_4x4x4_16b_bf16 v[76:79], a[250:251], a[228:229], v[76:79]\n"
      "ds_read_b64_tr_b16 a[220:221], v95 offset:47360\n"
      "v_mfma_f32_4x4x4_16b_bf16 v[80:83], a[250:251], a[230:231], v[80:83]\n"
      "ds_read_b64_tr_b16 a[222:223], v95 offset:48128\n"
      "ds_read_b128 a[212:215], v1 offset:4224\n"
      "ds_read_b128 a[192:195], v5 offset:55168\n"
      "v_cvt_pk_bf16_f32 v36, v36, v40\n"
      "v_cvt_pk_bf16_f32 v37, v37, v41\n"
      "v_cvt_pk_bf16_f32 v38, v38, v42\n"
      "v_cvt_pk_bf16_f32 v39, v39, v43\n"
      "v_mfma_f32_16x16x32_bf16 v[60:63], v[140:143], a[196:199], v[60:63]\n"
      "v_accvgpr_write_b32 a176, v36\n"
      "v_accvgpr_write_b32 a177, v37\n"
      "v_mfma_f32_16x16x32_bf16 v[64:67], v[152:155], a[200:203], v[64:67]\n"
      "v_accvgpr_write_b32 a178, v38\n"
      "v_accvgpr_write_b32 a179, v39\n"
      "v_mfma_f32_16x16x32_bf16 v[68:71], v[144:147], a[200:203], v[68:71]\n"
      "s_nop 0\n"
      "v_cvt_pk_bf16_f32 v28, v28, v32\n"
      "v_cvt_pk_bf16_f32 v29, v29, v33\n"
      "v_mfma_f32_16x16x32_bf16 v[72:75], v[136:139], a[200:203], v[72:75]\n"
      "s_nop 0\n"
      "v_cvt_pk_bf16_f32 v30, v30, v34\n"
      "v_cvt_pk_bf16_f32 v31, v31, v35\n"
      "v_mfma_f32_16x16x32_bf16 v[100:103], a[60:63], a[204:207], v[100:103]\n"
      "s_add_u32 m0, s37, 133120\n"
      "v_accvgpr_write_b32 a172, v28\n"
      "v_accvgpr_write_b32 a173, v29\n"
      "v_mfma_f32_16x16x32_bf16 v[104:107], a[64:67], a[204:207], v[104:107]\n"
      "s_add_u32 s53, s54, 986112\n"
      "v_accvgpr_write_b32 a174, v30\n"
      "v_accvgpr_write_b32 a175, v31\n"
      "v_mfma_f32_4x4x4_16b_bf16 v[28:31], a[244:245], a[80:81], 0\n"
      "v_mfma_f32_4x4x4_16b_bf16 v[32:35], a[244:245], a[82:83], 0\n"
      "buffer_load_dwordx4 v10, s[4:7], s53 offen offset:0 nt lds\n"
      "buffer_load_dwordx4 v11, s[4:7], s53 offen offset:1024 nt lds\n"
      "v_mfma_f32_4x4x4_16b_bf16 v[28:31], a[248:249], a[240:241], v[28:31]\n"
      "ds_read_b64_tr_b16 a[88:89], v95 offset:41280\n"
      "v_mfma_f32_4x4x4_16b_bf16 v[32:35], a[248:249], a[242:243], v[32:35]\n"
      "ds_read_b64_tr_b16 a[90:91], v95 offset:42048\n"
      "v_mfma_f32_4x4x4_16b_bf16 v[36:39], a[246:247], a[236:237], v[28:31]\n"
      "ds_read_b64_tr_b16 a[232:233], v95 offset:53568\n"
      "v_mfma_f32_4x4x4_16b_bf16 v[40:43], a[246:247], a[238:239], v[32:35]\n"
      "ds_read_b64_tr_b16 a[234:235], v95 offset:54336\n"
      "v_mfma_f32_4x4x4_16b_bf16 v[28:31], a[250:251], a[236:237], v[28:31]\n"
      "ds_read_b64_tr_b16 a[228:229], v95 offset:47424\n"
      "v_mfma_f32_4x4x4_16b_bf16 v[32:35], a[250:251], a[238:239], v[32:35]\n"
      "ds_read_b64_tr_b16 a[230:231], v95 offset:48192\n"
      "ds_read_b128 a[196:199], v2 offset:0\n"
      "ds_read_b128 a[200:203], v2 offset:4096\n"
      "v_cvt_pk_bf16_f32 v84, v84, v88\n"
      "v_cvt_pk_bf16_f32 v85, v85, v89\n"
      "v_cvt_pk_bf16_f32 v86, v86, v90\n"
      "v_cvt_pk_bf16_f32 v87, v87, v91\n"
      "v_mfma_f32_16x16x32_bf16 v[108:111], a[68:71], a[204:207], v[108:111]\n"
      "s_waitcnt lgkmcnt(5)\n"
      "v_accvgpr_write_b32 a168, v84\n"
      "v_accvgpr_write_b32 a169, v85\n"
      "v_mfma_f32_16x16x32_bf16 v[52:55], v[132:135], a[208:211], v[52:55]\n"
      "v_accvgpr_write_b32 a170, v86\n"
      "v_accvgpr_write_b32 a171, v87\n"
      "v_mfma_f32_16x16x32_bf16 v[56:59], v[124:127], a[208:211], v[56:59]\n"
      "s_nop 0\n"
      "v_cvt_pk_bf16_f32 v76, v76, v80\n"
      "v_cvt_pk_bf16_f32 v77, v77, v81\n"
      "v_mfma_f32_16x16x32_bf16 v[60:63], a[184:187], a[208:211], v[60:63]\n"
      "v_cvt_pk_bf16_f32 v78, v78, v82\n"
      "v_cvt_pk_bf16_f32 v79, v79, v83\n"
      "v_mfma_f32_16x16x32_bf16 v[64:67], v[128:131], a[212:215], v[64:67]\n"
      "s_add_u32 m0, s37, 137224\n"
      "v_accvgpr_write_b32 a164, v76\n"
      "v_accvgpr_write_b32 a165, v77\n"
      "v_mfma_f32_16x16x32_bf16 v[68:71], a[188:191], a[212:215], v[68:71]\n"
      "s_add_u32 s53, s54, 1182720\n"
      "v_accvgpr_write_b32 a166, v78\n"
      "v_accvgpr_write_b32 a167, v79\n"
      "v_mfma_f32_4x4x4_16b_bf16 v[76:79], a[244:245], a[84:85], 0\n"
      "v_mfma_f32_4x4x4_16b_bf16 v[80:83], a[244:245], a[86:87], 0\n"
      "buffer_load_dwordx4 v10, s[4:7], s53 offen offset:0 nt lds\n"
      "buffer_load_dwordx4 v11, s[4:7], s53 offen offset:1024 nt lds\n"
      "v_mfma_f32_4x4x4_16b_bf16 v[76:79], a[248:249], a[224:225], v[76:79]\n"
      "ds_read_b64_tr_b16 a[92:93], v95 offset:41344\n"
      "v_mfma_f32_4x4x4_16b_bf16 v[80:83], a[248:249], a[226:227], v[80:83]\n"
      "ds_read_b64_tr_b16 a[94:95], v95 offset:42112\n"
      "v_mfma_f32_4x4x4_16b_bf16 v[84:87], a[246:247], a[220:221], v[76:79]\n"
      "ds_read_b64_tr_b16 a[240:241], v95 offset:53632\n"
      "v_mfma_f32_4x4x4_16b_bf16 v[88:91], a[246:247], a[222:223], v[80:83]\n"
      "ds_read_b64_tr_b16 a[242:243], v95 offset:54400\n"
      "v_mfma_f32_4x4x4_16b_bf16 v[76:79], a[250:251], a[220:221], v[76:79]\n"
      "ds_read_b64_tr_b16 a[236:237], v95 offset:47488\n"
      "v_mfma_f32_4x4x4_16b_bf16 v[80:83], a[250:251], a[222:223], v[80:83]\n"
      "ds_read_b64_tr_b16 a[238:239], v95 offset:48256\n"
      "v_cvt_pk_bf16_f32 v36, v36, v40\n"
      "v_cvt_pk_bf16_f32 v37, v37, v41\n"
      "v_cvt_pk_bf16_f32 v38, v38, v42\n"
      "v_cvt_pk_bf16_f32 v39, v39, v43\n"
      "v_accvgpr_write_b32 a160, v36\n"
      "v_accvgpr_write_b32 a161, v37\n"
      "v_accvgpr_write_b32 a162, v38\n"
      "v_accvgpr_write_b32 a163, v39\n"
      "v_mfma_f32_16x16x32_bf16 v[72:75], a[180:183], a[212:215], v[72:75]\n"
      "v_cvt_pk_bf16_f32 v28, v28, v32\n"
      "v_cvt_pk_bf16_f32 v29, v29, v33\n"
      "v_mfma_f32_16x16x32_bf16 v[100:103], a[72:75], a[192:195], v[100:103]\n"
      "v_cvt_pk_bf16_f32 v30, v30, v34\n"
      "v_cvt_pk_bf16_f32 v31, v31, v35\n"
      "v_mfma_f32_16x16x32_bf16 v[104:107], a[76:79], a[192:195], v[104:107]\n"
      "s_nop 0\n"
      "v_accvgpr_write_b32 a156, v28\n"
      "v_accvgpr_write_b32 a157, v29\n"
      "v_mfma_f32_16x16x32_bf16 v[108:111], a[80:83], a[192:195], v[108:111]\n"
      "s_waitcnt vmcnt(12)\n"
      "v_accvgpr_write_b32 a158, v30\n"
      "v_accvgpr_write_b32 a159, v31\n"
      "s_barrier\n"
      "s_add_u32 s53, s54, 1379328\n"
      "s_add_u32 m0, s37, 141448\n"
      "v_mfma_f32_4x4x4_16b_bf16 v[28:31], a[244:245], a[88:89], 0\n"
      "v_mfma_f32_4x4x4_16b_bf16 v[32:35], a[244:245], a[90:91], 0\n"
      "buffer_load_dwordx4 v10, s[4:7], s53 offen offset:0 nt lds\n"
      "buffer_load_dwordx4 v11, s[4:7], s53 offen offset:1024 nt lds\n"
      "s_waitcnt lgkmcnt(0)\n"
      "v_mfma_f32_4x4x4_16b_bf16 v[28:31], a[248:249], a[232:233], v[28:31]\n"
      "ds_read_b64_tr_b16 a[96:97], v96 offset:2304\n"
      "v_mfma_f32_4x4x4_16b_bf16 v[32:35], a[248:249], a[234:235], v[32:35]\n"
      "ds_read_b64_tr_b16 a[98:99], v96 offset:3072\n"
      "v_mfma_f32_4x4x4_16b_bf16 v[36:39], a[246:247], a[228:229], v[28:31]\n"
      "ds_read_b64_tr_b16 a[224:225], v96 offset:14592\n"
      "v_mfma_f32_4x4x4_16b_bf16 v[40:43], a[246:247], a[230:231], v[32:35]\n"
      "ds_read_b64_tr_b16 a[226:227], v96 offset:15360\n"
      "v_mfma_f32_4x4x4_16b_bf16 v[28:31], a[250:251], a[228:229], v[28:31]\n"
      "ds_read_b64_tr_b16 a[220:221], v96 offset:8448\n"
      "v_mfma_f32_4x4x4_16b_bf16 v[32:35], a[250:251], a[230:231], v[32:35]\n"
      "ds_read_b64_tr_b16 a[222:223], v96 offset:9216\n"
      "ds_read_b128 a[204:207], v5 offset:55232\n"
      "v_cvt_pk_bf16_f32 v84, v84, v88\n"
      "v_cvt_pk_bf16_f32 v85, v85, v89\n"
      "v_cvt_pk_bf16_f32 v86, v86, v90\n"
      "v_cvt_pk_bf16_f32 v87, v87, v91\n"
      "v_mfma_f32_16x16x32_bf16 v[52:55], a[176:179], a[196:199], v[52:55]\n"
      "v_accvgpr_write_b32 a152, v84\n"
      "v_accvgpr_write_b32 a153, v85\n"
      "v_mfma_f32_16x16x32_bf16 v[56:59], a[168:171], a[196:199], v[56:59]\n"
      "v_accvgpr_write_b32 a154, v86\n"
      "v_accvgpr_write_b32 a155, v87\n"
      "v_mfma_f32_16x16x32_bf16 v[60:63], a[160:163], a[196:199], v[60:63]\n"
      "v_cvt_pk_bf16_f32 v76, v76, v80\n"
      "v_cvt_pk_bf16_f32 v77, v77, v81\n"
      "v_mfma_f32_16x16x32_bf16 v[64:67], a[172:175], a[200:203], v[64:67]\n"
      "v_cvt_pk_bf16_f32 v78, v78, v82\n"
      "v_cvt_pk_bf16_f32 v79, v79, v83\n"
      "v_mfma_f32_16x16x32_bf16 v[68:71], a[164:167], a[200:203], v[68:71]\n"
      "s_add_u32 m0, s37, 145792\n"
      "v_accvgpr_write_b32 a148, v76\n"
      "v_accvgpr_write_b32 a149, v77\n"
      "v_mfma_f32_16x16x32_bf16 v[72:75], a[156:159], a[200:203], v[72:75]\n"
      "s_add_u32 s53, s54, 1575936\n"
      "v_accvgpr_write_b32 a150, v78\n"
      "v_accvgpr_write_b32 a151, v79\n"
      "v_mfma_f32_4x4x4_16b_bf16 v[76:79], a[244:245], a[92:93], 0\n"
      "v_mfma_f32_4x4x4_16b_bf16 v[80:83], a[244:245], a[94:95], 0\n"
      "buffer_load_dwordx4 v10, s[4:7], s53 offen offset:0 nt lds\n"
      "buffer_load_dwordx4 v11, s[4:7], s53 offen offset:1024 nt lds\n"
      "v_mfma_f32_4x4x4_16b_bf16 v[76:79], a[248:249], a[240:241], v[76:79]\n"
      "ds_read_b64_tr_b16 a[100:101], v96 offset:2368\n"
      "v_mfma_f32_4x4x4_16b_bf16 v[80:83], a[248:249], a[242:243], v[80:83]\n"
      "ds_read_b64_tr_b16 a[102:103], v96 offset:3136\n"
      "v_mfma_f32_4x4x4_16b_bf16 v[84:87], a[246:247], a[236:237], v[76:79]\n"
      "ds_read_b64_tr_b16 a[232:233], v96 offset:14656\n"
      "v_mfma_f32_4x4x4_16b_bf16 v[88:91], a[246:247], a[238:239], v[80:83]\n"
      "ds_read_b64_tr_b16 a[234:235], v96 offset:15424\n"
      "v_mfma_f32_4x4x4_16b_bf16 v[76:79], a[250:251], a[236:237], v[76:79]\n"
      "ds_read_b64_tr_b16 a[228:229], v96 offset:8512\n"
      "v_mfma_f32_4x4x4_16b_bf16 v[80:83], a[250:251], a[238:239], v[80:83]\n"
      "ds_read_b64_tr_b16 a[230:231], v96 offset:9280\n"
      "ds_read_b128 a[208:211], v2 offset:64\n"
      "ds_read_b128 a[212:215], v2 offset:4160\n"
      "v_cvt_pk_bf16_f32 v36, v36, v40\n"
      "v_cvt_pk_bf16_f32 v37, v37, v41\n"
      "v_cvt_pk_bf16_f32 v38, v38, v42\n"
      "v_cvt_pk_bf16_f32 v39, v39, v43\n"
      "v_accvgpr_write_b32 a144, v36\n"
      "v_accvgpr_write_b32 a145, v37\n"
      "v_accvgpr_write_b32 a146, v38\n"
      "v_accvgpr_write_b32 a147, v39\n"
      "v_cvt_pk_bf16_f32 v28, v28, v32\n"
      "v_cvt_pk_bf16_f32 v29, v29, v33\n"
      "v_cvt_pk_bf16_f32 v30, v30, v34\n"
      "v_cvt_pk_bf16_f32 v31, v31, v35\n"
      "v_accvgpr_write_b32 a140, v28\n"
      "v_accvgpr_write_b32 a141, v29\n"
      "v_accvgpr_write_b32 a142, v30\n"
      "v_accvgpr_write_b32 a143, v31\n"
      "s_nop 0\n"
      "s_waitcnt lgkmcnt(2)\n"
      "v_mfma_f32_4x4x4_16b_bf16 v[28:31], a[244:245], a[96:97], 0\n"
      "v_mfma_f32_4x4x4_16b_bf16 v[32:35], a[244:245], a[98:99], 0\n"
      "ds_read_b64_tr_b16 a[104:105], v96 offset:2432\n"
      "v_mfma_f32_4x4x4_16b_bf16 v[28:31], a[248:249], a[224:225], v[28:31]\n"
      "ds_read_b64_tr_b16 a[106:107], v96 offset:3200\n"
      "v_mfma_f32_4x4x4_16b_bf16 v[32:35], a[248:249], a[226:227], v[32:35]\n"
      "ds_read_b64_tr_b16 a[240:241], v96 offset:14720\n"
      "v_mfma_f32_4x4x4_16b_bf16 v[36:39], a[246:247], a[220:221], v[28:31]\n"
      "ds_read_b64_tr_b16 a[242:243], v96 offset:15488\n"
      "v_mfma_f32_4x4x4_16b_bf16 v[40:43], a[246:247], a[222:223], v[32:35]\n"
      "ds_read_b64_tr_b16 a[236:237], v96 offset:8576\n"
      "v_mfma_f32_4x4x4_16b_bf16 v[28:31], a[250:251], a[220:221], v[28:31]\n"
      "ds_read_b64_tr_b16 a[238:239], v96 offset:9344\n"
      "v_mfma_f32_4x4x4_16b_bf16 v[32:35], a[250:251], a[222:223], v[32:35]\n"
      "v_cvt_pk_bf16_f32 v84, v84, v88\n"
      "v_cvt_pk_bf16_f32 v85, v85, v89\n"
      "v_cvt_pk_bf16_f32 v86, v86, v90\n"
      "v_cvt_pk_bf16_f32 v87, v87, v91\n"
      "v_accvgpr_write_b32 a136, v84\n"
      "v_accvgpr_write_b32 a137, v85\n"
      "v_accvgpr_write_b32 a138, v86\n"
      "v_accvgpr_write_b32 a139, v87\n"
      "v_cvt_pk_bf16_f32 v76, v76, v80\n"
      "v_cvt_pk_bf16_f32 v77, v77, v81\n"
      "v_mfma_f32_16x16x32_bf16 v[100:103], a[84:87], a[204:207], v[100:103]\n"
      "s_nop 0\n"
      "v_cvt_pk_bf16_f32 v78, v78, v82\n"
      "v_cvt_pk_bf16_f32 v79, v79, v83\n"
      "v_mfma_f32_16x16x32_bf16 v[104:107], a[88:91], a[204:207], v[104:107]\n"
      "s_nop 0\n"
      "v_accvgpr_write_b32 a132, v76\n"
      "v_accvgpr_write_b32 a133, v77\n"
      "v_mfma_f32_16x16x32_bf16 v[108:111], a[92:95], a[204:207], v[108:111]\n"
      "s_nop 0\n"
      "v_accvgpr_write_b32 a134, v78\n"
      "v_accvgpr_write_b32 a135, v79\n"
      "v_mfma_f32_4x4x4_16b_bf16 v[76:79], a[244:245], a[100:101], 0\n"
      "v_mfma_f32_4x4x4_16b_bf16 v[80:83], a[244:245], a[102:103], 0\n"
      "ds_read_b128 a[192:195], v5 offset:55296\n"
      "v_mfma_f32_4x4x4_16b_bf16 v[76:79], a[248:249], a[232:233], v[76:79]\n"
      "v_mfma_f32_4x4x4_16b_bf16 v[80:83], a[248:249], a[234:235], v[80:83]\n"
      "s_nop 0\n"
      "v_mfma_f32_4x4x4_16b_bf16 v[84:87], a[246:247], a[228:229], v[76:79]\n"
      "v_mfma_f32_4x4x4_16b_bf16 v[88:91], a[246:247], a[230:231], v[80:83]\n"
      "v_mfma_f32_4x4x4_16b_bf16 v[76:79], a[250:251], a[228:229], v[76:79]\n"
      "v_mfma_f32_4x4x4_16b_bf16 v[80:83], a[250:251], a[230:231], v[80:83]\n"
      "v_cvt_pk_bf16_f32 v36, v36, v40\n"
      "v_cvt_pk_bf16_f32 v37, v37, v41\n"
      "v_cvt_pk_bf16_f32 v38, v38, v42\n"
      "v_cvt_pk_bf16_f32 v39, v39, v43\n"
      "s_waitcnt lgkmcnt(1)\n"
      "v_mfma_f32_16x16x32_bf16 v[52:55], a[152:155], a[208:211], v[52:55]\n"
      "v_accvgpr_write_b32 a128, v36\n"
      "v_accvgpr_write_b32 a129, v37\n"
      "v_mfma_f32_16x16x32_bf16 v[56:59], a[144:147], a[208:211], v[56:59]\n"
      "v_accvgpr_write_b32 a130, v38\n"
      "v_accvgpr_write_b32 a131, v39\n"
      "v_mfma_f32_16x16x32_bf16 v[60:63], a[136:139], a[208:211], v[60:63]\n"
      "s_nop 0\n"
      "v_cvt_pk_bf16_f32 v28, v28, v32\n"
      "v_cvt_pk_bf16_f32 v29, v29, v33\n"
      "v_mfma_f32_16x16x32_bf16 v[64:67], a[148:151], a[212:215], v[64:67]\n"
      "v_cvt_pk_bf16_f32 v30, v30, v34\n"
      "v_cvt_pk_bf16_f32 v31, v31, v35\n"
      "v_mfma_f32_16x16x32_bf16 v[68:71], a[140:143], a[212:215], v[68:71]\n"
      "v_accvgpr_write_b32 a124, v28\n"
      "v_accvgpr_write_b32 a125, v29\n"
      "v_mfma_f32_16x16x32_bf16 v[72:75], a[132:135], a[212:215], v[72:75]\n"
      "v_accvgpr_write_b32 a126, v30\n"
      "v_accvgpr_write_b32 a127, v31\n"
      "v_mfma_f32_4x4x4_16b_bf16 v[28:31], a[244:245], a[104:105], 0\n"
      "v_mfma_f32_4x4x4_16b_bf16 v[32:35], a[244:245], a[106:107], 0\n"
      "ds_read_b128 a[196:199], v2 offset:128\n"
      "v_mfma_f32_4x4x4_16b_bf16 v[28:31], a[248:249], a[240:241], v[28:31]\n"
      "ds_read_b128 a[200:203], v2 offset:4224\n"
      "v_mfma_f32_4x4x4_16b_bf16 v[32:35], a[248:249], a[242:243], v[32:35]\n"
      "v_mfma_f32_4x4x4_16b_bf16 v[36:39], a[246:247], a[236:237], v[28:31]\n"
      "s_nop 0\n"
      "s_nop 0\n"
      "v_mfma_f32_4x4x4_16b_bf16 v[40:43], a[246:247], a[238:239], v[32:35]\n"
      "v_mfma_f32_4x4x4_16b_bf16 v[28:31], a[250:251], a[236:237], v[28:31]\n"
      "v_mfma_f32_4x4x4_16b_bf16 v[32:35], a[250:251], a[238:239], v[32:35]\n"
      "v_cvt_pk_bf16_f32 v84, v84, v88\n"
      "v_cvt_pk_bf16_f32 v85, v85, v89\n"
      "v_cvt_pk_bf16_f32 v86, v86, v90\n"
      "v_cvt_pk_bf16_f32 v87, v87, v91\n"
      "v_accvgpr_write_b32 a120, v84\n"
      "v_accvgpr_write_b32 a121, v85\n"
      "v_accvgpr_write_b32 a122, v86\n"
      "v_accvgpr_write_b32 a123, v87\n"
      "v_cvt_pk_bf16_f32 v76, v76, v80\n"
      "v_cvt_pk_bf16_f32 v77, v77, v81\n"
      "v_cvt_pk_bf16_f32 v78, v78, v82\n"
      "v_cvt_pk_bf16_f32 v79, v79, v83\n"
      "v_accvgpr_write_b32 a116, v76\n"
      "v_accvgpr_write_b32 a117, v77\n"
      "v_accvgpr_write_b32 a118, v78\n"
      "v_accvgpr_write_b32 a119, v79\n"
      "s_waitcnt lgkmcnt(0)\n"
      "s_waitcnt vmcnt(14)\n"
      "s_barrier\n"
      "s_mov_b32 m0, s64\n"
      "s_nop 0\n"
      "s_nop 0\n"
      "v_mfma_f32_16x16x32_bf16 v[100:103], a[96:99], a[192:195], v[100:103]\n"
      "s_add_u32 s53, s54, 1769472\n"
      "ds_read_b128 a[204:207], v3 offset:59264\n"
      "v_cvt_pk_bf16_f32 v36, v36, v40\n"
      "v_cvt_pk_bf16_f32 v37, v37, v41\n"
      "v_cvt_pk_bf16_f32 v38, v38, v42\n"
      "v_cvt_pk_bf16_f32 v39, v39, v43\n"
      "v_accvgpr_write_b32 a112, v36\n"
      "v_mfma_f32_16x16x32_bf16 v[104:107], a[100:103], a[192:195], v[104:107]\n"
      "s_add_u32 m0, s37, 82432\n"
      "ds_read_b128 a[208:211], v3 offset:59328\n"
      "ds_read_b64_tr_b16 a[220:221], v7 offset:18944\n"
      "ds_read_b64_tr_b16 a[222:223], v7 offset:18952\n"
      "v_accvgpr_write_b32 a113, v37\n"
      "v_accvgpr_write_b32 a114, v38\n"
      "v_accvgpr_write_b32 a115, v39\n"
      "v_mfma_f32_16x16x32_bf16 v[108:111], a[104:107], a[192:195], v[108:111]\n"
      "s_nop 0\n"
      "buffer_load_dwordx4 v8, s[4:7], s53 offen offset:0 nt lds\n"
      "buffer_load_dwordx4 v9, s[4:7], s53 offen offset:1024 nt lds\n"
      "v_cvt_pk_bf16_f32 v28, v28, v32\n"
      "v_cvt_pk_bf16_f32 v29, v29, v33\n"
      "v_mfma_f32_16x16x32_bf16 v[52:55], a[128:131], a[196:199], v[52:55]\n"
      "s_mov_b32 m0, s64\n"
      "ds_read_b128 a[212:215], v3 offset:59392\n"
      "v_cvt_pk_bf16_f32 v30, v30, v34\n"
      "v_cvt_pk_bf16_f32 v31, v31, v35\n"
      "v_accvgpr_write_b32 a108, v28\n"
      "v_accvgpr_write_b32 a109, v29\n"
      "v_accvgpr_write_b32 a110, v30\n"
      "v_accvgpr_write_b32 a111, v31\n"
      "v_mfma_f32_16x16x32_bf16 v[56:59], a[120:123], a[196:199], v[56:59]\n"
      "s_nop 0\n"
      "v_mfma_f32_16x16x32_bf16 v[60:63], a[112:115], a[196:199], v[60:63]\n"
      "s_add_u32 s53, s54, 1966080\n"
      "v_mfma_f32_16x16x32_bf16 v[64:67], a[124:127], a[200:203], v[64:67]\n"
      "s_add_u32 m0, s37, 86536\n"
      "ds_read_b128 a[192:195], v4 offset:59264\n"
      "v_mfma_f32_16x16x32_bf16 v[68:71], a[116:119], a[200:203], v[68:71]\n"
      "s_waitcnt lgkmcnt(1)\n"
      "buffer_load_dwordx4 v8, s[4:7], s53 offen offset:0 nt lds\n"
      "buffer_load_dwordx4 v9, s[4:7], s53 offen offset:1024 nt lds\n"
      "v_mfma_f32_16x16x32_bf16 v[72:75], a[108:111], a[200:203], v[72:75]\n"
      "s_mov_b32 m0, s64\n"
      "v_add_f32 v52, v52, v64\n"
      "v_add_f32 v53, v53, v65\n"
      "v_mfma_f32_4x4x4_16b_bf16 v[20:23], a[216:217], a[220:221], 0\n"
      "v_add_f32 v54, v54, v66\n"
      "v_mfma_f32_4x4x4_16b_bf16 v[24:27], a[216:217], a[222:223], 0\n"
      "v_add_f32 v55, v55, v67\n"
      "v_add_f32 v56, v56, v68\n"
      "v_mfma_f32_16x16x32_bf16 v[112:115], a[0:3], a[204:207], 0\n"
      "ds_read_b128 a[196:199], v4 offset:59328\n"
      "ds_write_addtid_b32 v52 offset:45312\n"
      "v_mfma_f32_16x16x32_bf16 v[116:119], a[4:7], a[204:207], 0\n"
      "s_add_u32 s53, s54, 6488064\n"
      "ds_read_b64_tr_b16 a[224:225], v7 offset:19456\n"
      "ds_read_b64_tr_b16 a[226:227], v7 offset:19464\n"
      "v_mfma_f32_16x16x32_bf16 v[120:123], a[8:11], a[204:207], 0\n"
      "s_add_u32 m0, s37, 150272\n"
      "v_cvt_pk_bf16_f32 v20, v20, v24\n"
      "v_cvt_pk_bf16_f32 v21, v21, v25\n"
      "v_mfma_f32_16x16x32_bf16 v[112:115], a[12:15], a[208:211], v[112:115]\n"
      "buffer_load_dwordx4 v8, s[4:7], s53 offen offset:0 nt lds\n"
      "buffer_load_dwordx4 v9, s[4:7], s53 offen offset:1024 nt lds\n"
      "ds_read_b128 a[200:203], v4 offset:59392\n"
      "v_mfma_f32_16x16x32_bf16 v[116:119], a[16:19], a[208:211], v[116:119]\n"
      "s_mov_b32 m0, s64\n"
      "v_add_f32 v57, v57, v69\n"
      "v_cvt_pk_bf16_f32 v22, v22, v26\n"
      "v_mfma_f32_16x16x32_bf16 v[120:123], a[20:23], a[208:211], v[120:123]\n"
      "ds_write_addtid_b32 v20 offset:18432\n"
      "ds_write_addtid_b32 v21 offset:22528\n"
      "v_mfma_f32_16x16x32_bf16 v[112:115], a[24:27], a[212:215], v[112:115]\n"
      "s_add_u32 s53, s54, 6491136\n"
      "ds_read_b128 a[204:207], v5 offset:59264\n"
      "ds_write_addtid_b32 v53 offset:45824\n"
      "v_mfma_f32_16x16x32_bf16 v[116:119], a[28:31], a[212:215], v[116:119]\n"
      "s_add_u32 m0, s37, 158464\n"
      "v_cvt_pk_bf16_f32 v23, v23, v27\n"
      "v_add_f32 v58, v58, v70\n"
      "v_mfma_f32_16x16x32_bf16 v[120:123], a[32:35], a[212:215], v[120:123]\n"
      "s_waitcnt lgkmcnt(4)\n"
      "buffer_load_dwordx4 v10, s[4:7], s53 offen offset:0 nt lds\n"
      "buffer_load_dwordx4 v11, s[4:7], s53 offen offset:1024 nt lds\n"
      "v_mfma_f32_4x4x4_16b_bf16 v[28:31], a[216:217], a[224:225], 0\n"
      "v_add_f32 v59, v59, v71\n"
      "v_mfma_f32_4x4x4_16b_bf16 v[32:35], a[216:217], a[226:227], 0\n"
      "v_add_f32 v60, v60, v72\n"
      "v_add_f32 v61, v61, v73\n"
      "v_mfma_f32_16x16x32_bf16 v[112:115], a[36:39], a[192:195], v[112:115]\n"
      "s_mov_b32 m0, s64\n"
      "ds_read_b128 a[208:211], v5 offset:59328\n"
      "ds_write_addtid_b32 v22 offset:26624\n"
      "v_mfma_f32_16x16x32_bf16 v[116:119], a[40:43], a[192:195], v[116:119]\n"
      "ds_write_addtid_b32 v23 offset:30720\n"
      "ds_read_b64_tr_b16 a[228:229], v7 offset:19968\n"
      "v_mfma_f32_16x16x32_bf16 v[120:123], a[44:47], a[192:195], v[120:123]\n"
      "ds_write_addtid_b32 v54 offset:46336\n"
      "ds_read_b64_tr_b16 a[230:231], v7 offset:19976\n"
      "v_mfma_f32_16x16x32_bf16 v[112:115], a[48:51], a[196:199], v[112:115]\n"
      "ds_read_b128 a[212:215], v5 offset:59392\n"
      "ds_write_addtid_b32 v55 offset:46848\n"
      "v_mfma_f32_16x16x32_bf16 v[116:119], a[52:55], a[196:199], v[116:119]\n"
      "ds_write_addtid_b32 v56 offset:47360\n"
      "ds_write_addtid_b32 v57 offset:47872\n"
      "v_mfma_f32_16x16x32_bf16 v[120:123], a[56:59], a[196:199], v[120:123]\n"
      "ds_write_addtid_b32 v58 offset:48384\n"
      "ds_write_addtid_b32 v59 offset:48896\n"
      "v_mfma_f32_16x16x32_bf16 v[112:115], a[60:63], a[200:203], v[112:115]\n"
      "ds_read_b128 a[192:195], v3 offset:0\n"
      "ds_write_addtid_b32 v60 offset:49408\n"
      "v_mfma_f32_16x16x32_bf16 v[116:119], a[64:67], a[200:203], v[116:119]\n"
      "s_nop 0\n"
      "ds_write_addtid_b32 v61 offset:49920\n"
      "v_cvt_pk_bf16_f32 v28, v28, v32\n"
      "v_mfma_f32_16x16x32_bf16 v[120:123], a[68:71], a[200:203], v[120:123]\n"
      "s_waitcnt lgkmcnt(7)\n"
      "ds_write_addtid_b32 v28 offset:18944\n"
      "v_cvt_pk_bf16_f32 v29, v29, v33\n"
      "v_mfma_f32_4x4x4_16b_bf16 v[20:23], a[216:217], a[228:229], 0\n"
      "v_cvt_pk_bf16_f32 v30, v30, v34\n"
      "v_mfma_f32_4x4x4_16b_bf16 v[24:27], a[216:217], a[230:231], 0\n"
      "ds_write_addtid_b32 v29 offset:23040\n"
      "ds_write_addtid_b32 v30 offset:27136\n"
      "v_mfma_f32_16x16x32_bf16 v[112:115], a[72:75], a[204:207], v[112:115]\n"
      "ds_read_b128 a[196:199], v3 offset:64\n"
      "v_cvt_pk_bf16_f32 v31, v31, v35\n"
      "v_mfma_f32_16x16x32_bf16 v[116:119], a[76:79], a[204:207], v[116:119]\n"
      "ds_write_addtid_b32 v31 offset:31232\n"
      "ds_read_b64_tr_b16 a[232:233], v7 offset:20480\n"
      "v_mfma_f32_16x16x32_bf16 v[120:123], a[80:83], a[204:207], v[120:123]\n"
      "ds_read_b64_tr_b16 a[234:235], v7 offset:20488\n"
      "v_cvt_pk_bf16_f32 v20, v20, v24\n"
      "v_mfma_f32_16x16x32_bf16 v[112:115], a[84:87], a[208:211], v[112:115]\n"
      "ds_read_b128 a[200:203], v3 offset:128\n"
      "ds_write_addtid_b32 v20 offset:19456\n"
      "v_mfma_f32_16x16x32_bf16 v[116:119], a[88:91], a[208:211], v[116:119]\n"
      "v_cvt_pk_bf16_f32 v21, v21, v25\n"
      "v_cvt_pk_bf16_f32 v22, v22, v26\n"
      "v_mfma_f32_16x16x32_bf16 v[120:123], a[92:95], a[208:211], v[120:123]\n"
      "ds_write_addtid_b32 v21 offset:23552\n"
      "ds_write_addtid_b32 v22 offset:27648\n"
      "v_mfma_f32_16x16x32_bf16 v[112:115], a[96:99], a[212:215], v[112:115]\n"
      "ds_read_b128 a[204:207], v4 offset:0\n"
      "v_cvt_pk_bf16_f32 v23, v23, v27\n"
      "v_mfma_f32_16x16x32_bf16 v[116:119], a[100:103], a[212:215], v[116:119]\n"
      "ds_write_addtid_b32 v23 offset:31744\n"
      "v_add_f32 v62, v62, v74\n"
      "v_mfma_f32_16x16x32_bf16 v[120:123], a[104:107], a[212:215], v[120:123]\n"
      "s_waitcnt lgkmcnt(4)\n"
      "ds_write_addtid_b32 v62 offset:50432\n"
      "v_add_f32_e64 v63, v63, v75\n"
      "v_mfma_f32_4x4x4_16b_bf16 v[28:31], a[216:217], a[232:233], 0\n"
      "v_mfma_f32_4x4x4_16b_bf16 v[32:35], a[216:217], a[234:235], 0\n"
      "ds_write_addtid_b32 v63 offset:50944\n"
      "v_mfma_f32_16x16x32_bf16 v[76:79], a[0:3], a[192:195], 0\n"
      "ds_read_b128 a[208:211], v4 offset:64\n"
      "v_mfma_f32_16x16x32_bf16 v[80:83], a[4:7], a[192:195], 0\n"
      "v_mfma_f32_16x16x32_bf16 v[84:87], a[8:11], a[192:195], 0\n"
      "v_cvt_pk_bf16_f32 v28, v28, v32\n"
      "v_cvt_pk_bf16_f32 v29, v29, v33\n"
      "v_mfma_f32_16x16x32_bf16 v[76:79], a[12:15], a[196:199], v[76:79]\n"
      "ds_read_b128 a[212:215], v4 offset:128\n"
      "ds_write_addtid_b32 v28 offset:19968\n"
      "v_mfma_f32_16x16x32_bf16 v[80:83], a[16:19], a[196:199], v[80:83]\n"
      "ds_write_addtid_b32 v29 offset:24064\n"
      "v_cvt_pk_bf16_f32 v30, v30, v34\n"
      "v_mfma_f32_16x16x32_bf16 v[84:87], a[20:23], a[196:199], v[84:87]\n"
      "ds_write_addtid_b32 v30 offset:28160\n"
      "v_cvt_pk_bf16_f32 v31, v31, v35\n"
      "v_mfma_f32_16x16x32_bf16 v[76:79], a[24:27], a[200:203], v[76:79]\n"
      "ds_read_b128 a[192:195], v5 offset:0\n"
      "ds_write_addtid_b32 v31 offset:32256\n"
      "v_mfma_f32_16x16x32_bf16 v[80:83], a[28:31], a[200:203], v[80:83]\n"
      "v_mfma_f32_16x16x32_bf16 v[84:87], a[32:35], a[200:203], v[84:87]\n"
      "s_waitcnt lgkmcnt(4)\n"
      "v_mfma_f32_16x16x32_bf16 v[76:79], a[36:39], a[204:207], v[76:79]\n"
      "s_nop 0\n"
      "ds_read_b128 a[196:199], v5 offset:64\n"
      "v_mfma_f32_16x16x32_bf16 v[80:83], a[40:43], a[204:207], v[80:83]\n"
      "v_fma_f32 v40, -2.0, v64, v52\n"
      "v_fma_f32 v41, -2.0, v65, v53\n"
      "v_mfma_f32_16x16x32_bf16 v[84:87], a[44:47], a[204:207], v[84:87]\n"
      "v_fma_f32 v42, -2.0, v66, v54\n"
      "v_fma_f32 v43, -2.0, v67, v55\n"
      "v_mfma_f32_16x16x32_bf16 v[76:79], a[48:51], a[208:211], v[76:79]\n"
      "ds_read_b128 a[200:203], v5 offset:128\n"
      "v_fma_f32 v44, -2.0, v68, v56\n"
      "v_mfma_f32_16x16x32_bf16 v[80:83], a[52:55], a[208:211], v[80:83]\n"
      "v_fma_f32 v45, -2.0, v69, v57\n"
      "v_mfma_f32_16x16x32_bf16 v[84:87], a[56:59], a[208:211], v[84:87]\n"
      "v_mfma_f32_16x16x32_bf16 v[76:79], a[60:63], a[212:215], v[76:79]\n"
      "s_nop 0\n"
      "v_mfma_f32_16x16x32_bf16 v[80:83], a[64:67], a[212:215], v[80:83]\n"
      "s_waitcnt vmcnt(14)\n"
      "v_mfma_f32_16x16x32_bf16 v[84:87], a[68:71], a[212:215], v[84:87]\n"
      "s_waitcnt lgkmcnt(0)\n"
      "s_barrier\n"
      "s_add_u32 s54, s54, 1966080\n"
      "s_add_u32 s6, s19, 6295552\n"
      ".p2align 6\n"
      "LDloop_%=:\n"
      "v_mfma_f32_16x16x32_bf16 v[76:79], a[72:75], a[192:195], v[76:79]\n"
      "s_add_u32 s58, s58, 3072\n"
      "ds_read_b128 a[204:207], v0 offset:16384\n"
      "ds_read2st64_b32 v[28:29], v6 offset0:0 offset1:2\n"
      "ds_read2st64_b32 v[30:31], v6 offset0:4 offset1:6\n"
      "v_mfma_f32_16x16x32_bf16 v[80:83], a[76:79], a[192:195], v[80:83]\n"
      "s_add_u32 s59, s58, 196608\n"
      "ds_read_b128 a[208:211], v0 offset:20480\n"
      "ds_read2st64_b32 v[32:33], v6 offset0:8 offset1:10\n"
      "v_mfma_f32_16x16x32_bf16 v[84:87], a[80:83], a[192:195], v[84:87]\n"
      "ds_read2st64_b32 v[34:35], v6 offset0:12 offset1:14\n"
      "v_fma_f32 v46, -2.0, v70, v58\n"
      "v_mfma_f32_16x16x32_bf16 v[76:79], a[84:87], a[196:199], v[76:79]\n"
      "ds_read_b128 a[212:215], v0 offset:16448\n"
      "v_fma_f32 v47, -2.0, v71, v59\n"
      "v_fma_f32 v48, -2.0, v72, v60\n"
      "v_mfma_f32_16x16x32_bf16 v[80:83], a[88:91], a[196:199], v[80:83]\n"
      "ds_read2st64_b32 a[220:221], v12 offset0:181 offset1:183\n"
      "v_fma_f32 v49, -2.0, v73, v61\n"
      "v_mfma_f32_16x16x32_bf16 v[84:87], a[92:95], a[196:199], v[84:87]\n"
      "ds_read2st64_b32 a[222:223], v15 offset0:181 offset1:183\n"
      "v_fma_f32 v50, -2.0, v74, v62\n"
      "v_mfma_f32_16x16x32_bf16 v[76:79], a[96:99], a[200:203], v[76:79]\n"
      "ds_read_b128 a[192:195], v0 offset:20544\n"
      "v_fma_f32 v51, -2.0, v75, v63\n"
      "v_mfma_f32_16x16x32_bf16 v[80:83], a[100:103], a[200:203], v[80:83]\n"
      "ds_read2st64_b32 v[36:37], v6 offset0:16 offset1:18\n"
      "v_mfma_f32_16x16x32_bf16 v[84:87], a[104:107], a[200:203], v[84:87]\n"
      "s_waitcnt lgkmcnt(6)\n"
      "ds_read2st64_b32 v[38:39], v6 offset0:20 offset1:22\n"
      "v_fmac_f32 v40, s46, v28\n"
      "v_fmac_f32 v41, s46, v29\n"
      "v_fmac_f32 v42, s46, v30\n"
      "v_mfma_f32_16x16x32_bf16 v[52:55], v[252:255], a[204:207], 0\n"
      "ds_read_b128 a[196:199], v0 offset:16512\n"
      "v_fmac_f32_e64 v43, s46, v31\n"
      "v_fma_f32 v28, 0.5, v28, v100\n"
      "v_mfma_f32_16x16x32_bf16 v[56:59], v[244:247], a[204:207], 0\n"
      "s_add_u32 s54, s54, -193536\n"
      "ds_read_b64_tr_b16 a[244:245], v7 offset:36864\n"
      "ds_read_b64_tr_b16 a[246:247], v7 offset:36872\n"
      "v_mfma_f32_16x16x32_bf16 v[60:63], v[236:239], a[204:207], 0\n"
      "s_add_u32 m0, s37, 99328\n"
      "v_fma_f32 v29, 0.5, v29, v101\n"
      "v_fma_f32 v30, 0.5, v30, v102\n"
      "v_fma_f32 v31, 0.5, v31, v103\n"
      "v_mfma_f32_16x16x32_bf16 v[64:67], v[248:251], a[208:211], 0\n"
      "buffer_load_dwordx4 v10, s[4:7], s54 offen offset:0 nt lds\n"
      "buffer_load_dwordx4 v11, s[4:7], s54 offen offset:1024 nt lds\n"
      "ds_read_b128 a[200:203], v0 offset:20608\n"
      "v_mfma_f32_16x16x32_bf16 v[68:71], v[240:243], a[208:211], 0\n"
      "s_mov_b32 m0, s64\n"
      "v_fmac_f32 v40, s48, v76\n"
      "v_fmac_f32 v41, s48, v77\n"
      "v_mfma_f32_16x16x32_bf16 v[72:75], v[232:235], a[208:211], 0\n"
      "s_waitcnt lgkmcnt(4)\n"
      "v_fmac_f32 v42, s48, v78\n"
      "v_fmac_f32 v43, s48, v79\n"
      "v_fmac_f32 v28, s45, v40\n"
      "v_mfma_f32_16x16x32_bf16 v[52:55], v[228:231], a[212:215], v[52:55]\n"
      "ds_read_b128 a[204:207], v1 offset:16384\n"
      "v_fmac_f32 v29, s45, v41\n"
      "v_mfma_f32_16x16x32_bf16 v[56:59], v[220:223], a[212:215], v[56:59]\n"
      "v_fmac_f32 v30, s45, v42\n"
      "v_fmac_f32_e64 v31, s45, v43\n"
      "v_mfma_f32_16x16x32_bf16 v[60:63], v[212:215], a[212:215], v[60:63]\n"
      "s_waitcnt lgkmcnt(1)\n"
      "v_fma_f32 v40, s47, v40, -v76\n"
      "v_fma_f32 v41, s47, v41, -v77\n"
      "v_mfma_f32_4x4x4_16b_bf16 v[20:23], a[216:217], a[244:245], 0\n"
      "v_fma_f32 v42, s47, v42, -v78\n"
      "v_mfma_f32_4x4x4_16b_bf16 v[24:27], a[216:217], a[246:247], 0\n"
      "v_fma_f32 v43, s47, v43, -v79\n"
      "v_cvt_pk_bf16_f32 v28, v28, v29\n"
      "v_mfma_f32_16x16x32_bf16 v[64:67], v[224:227], a[192:195], v[64:67]\n"
      "buffer_store_dwordx4 a[220:223], v18, s[8:11], s58 offen offset:0 sc1\n"
      "ds_read_b128 a[208:211], v1 offset:20480\n"
      "v_mfma_f32_16x16x32_bf16 v[68:71], v[216:219], a[192:195], v[68:71]\n"
      "ds_write_addtid_b32 v28 offset:32768\n"
      "v_cvt_pk_bf16_f32 v20, v20, v24\n"
      "v_mfma_f32_16x16x32_bf16 v[72:75], v[208:211], a[192:195], v[72:75]\n"
      "ds_write_addtid_b32 v20 offset:3072\n"
      "v_cvt_pk_bf16_f32 v30, v30, v31\n"
      "v_mfma_f32_16x16x32_bf16 v[52:55], v[204:207], a[196:199], v[52:55]\n"
      "ds_read_b128 a[212:215], v1 offset:16448\n"
      "ds_write_addtid_b32 v30 offset:33280\n"
      "v_cvt_pk_bf16_f32 v40, v40, v41\n"
      "v_cvt_pk_bf16_f32 v21, v21, v25\n"
      "v_mfma_f32_16x16x32_bf16 v[56:59], v[196:199], a[196:199], v[56:59]\n"
      "ds_write_addtid_b32 v40 offset:34816\n"
      "ds_write_addtid_b32 v21 offset:7168\n"
      "ds_read2st64_b32 a[224:225], v13 offset0:181 offset1:183\n"
      "v_mfma_f32_16x16x32_bf16 v[60:63], v[188:191], a[196:199], v[60:63]\n"
      "ds_read2st64_b32 a[226:227], v16 offset0:181 offset1:183\n"
      "v_cvt_pk_bf16_f32 v42, v42, v43\n"
      "v_fmac_f32 v44, s46, v32\n"
      "v_mfma_f32_16x16x32_bf16 v[64:67], v[200:203], a[200:203], v[64:67]\n"
      "ds_read_b128 a[192:195], v1 offset:20544\n"
      "ds_write_addtid_b32 v42 offset:35328\n"
      "v_mfma_f32_16x16x32_bf16 v[68:71], v[192:195], a[200:203], v[68:71]\n"
      "v_fmac_f32_e64 v45, s46, v33\n"
      "v_cvt_pk_bf16_f32 v22, v22, v26\n"
      "v_mfma_f32_16x16x32_bf16 v[72:75], v[184:187], a[200:203], v[72:75]\n"
      "s_waitcnt lgkmcnt(4)\n"
      "ds_write_addtid_b32 v22 offset:11264\n"
      "v_fmac_f32 v46, s46, v34\n"
      "v_fmac_f32 v47, s46, v35\n"
      "v_mfma_f32_16x16x32_bf16 v[52:55], v[180:183], a[204:207], v[52:55]\n"
      "ds_read_b128 a[196:199], v1 offset:16512\n"
      "v_cvt_pk_bf16_f32 v23, v23, v27\n"
      "v_mfma_f32_16x16x32_bf16 v[56:59], v[172:175], a[204:207], v[56:59]\n"
      "s_add_u32 s54, s54, 196608\n"
      "ds_write_addtid_b32 v23 offset:15360\n"
      "ds_read2st64_b32 a[228:229], v14 offset0:181 offset1:183\n"
      "v_mfma_f32_16x16x32_bf16 v[60:63], v[164:167], a[204:207], v[60:63]\n"
      "s_add_u32 m0, s37, 103432\n"
      "ds_read2st64_b32 a[230:231], v17 offset0:181 offset1:183\n"
      "v_fma_f32 v32, 0.5, v32, v104\n"
      "v_fma_f32 v33, 0.5, v33, v105\n"
      "v_mfma_f32_16x16x32_bf16 v[64:67], v[176:179], a[208:211], v[64:67]\n"
      "buffer_load_dwordx4 v10, s[4:7], s54 offen offset:0 nt lds\n"
      "buffer_load_dwordx4 v11, s[4:7], s54 offen offset:1024 nt lds\n"
      "ds_read_b128 a[200:203], v1 offset:20608\n"
      "v_mfma_f32_16x16x32_bf16 v[68:71], v[168:171], a[208:211], v[68:71]\n"
      "s_mov_b32 m0, s64\n"
      "v_fma_f32 v34, 0.5, v34, v106\n"
      "v_fma_f32 v35, 0.5, v35, v107\n"
      "v_mfma_f32_16x16x32_bf16 v[72:75], v[160:163], a[208:211], v[72:75]\n"
      "v_fmac_f32 v44, s48, v80\n"
      "v_fmac_f32_e64 v45, s48, v81\n"
      "v_mfma_f32_16x16x32_bf16 v[52:55], v[156:159], a[212:215], v[52:55]\n"
      "ds_read_b128 a[204:207], v2 offset:16384\n"
      "v_fmac_f32 v46, s48, v82\n"
      "v_mfma_f32_16x16x32_bf16 v[56:59], v[148:151], a[212:215], v[56:59]\n"
      "v_fmac_f32 v47, s48, v83\n"
      "v_fmac_f32 v32, s45, v44\n"
      "v_mfma_f32_16x16x32_bf16 v[60:63], v[140:143], a[212:215], v[60:63]\n"
      "s_waitcnt lgkmcnt(1)\n"
      "v_fmac_f32 v33, s45, v45\n"
      "v_fmac_f32 v34, s45, v46\n"
      "v_mfma_f32_16x16x32_bf16 v[64:67], v[152:155], a[192:195], v[64:67]\n"
      "buffer_store_dwordx4 a[224:227], v18, s[8:11], s58 offen offset:128 sc1\n"
      "ds_read_b128 a[208:211], v2 offset:20480\n"
      "v_mfma_f32_16x16x32_bf16 v[68:71], v[144:147], a[192:195], v[68:71]\n"
      "v_fmac_f32 v35, s45, v47\n"
      "v_fma_f32 v44, s47, v44, -v80\n"
      "v_mfma_f32_16x16x32_bf16 v[72:75], v[136:139], a[192:195], v[72:75]\n"
      "s_nop 0\n"
      "ds_read_b64_tr_b16 a[248:249], v7 offset:37376\n"
      "ds_read_b64_tr_b16 a[250:251], v7 offset:37384\n"
      "v_fma_f32 v45, s47, v45, -v81\n"
      "v_fma_f32 v46, s47, v46, -v82\n"
      "v_mfma_f32_16x16x32_bf16 v[52:55], v[132:135], a[196:199], v[52:55]\n"
      "ds_read_b128 a[212:215], v2 offset:16448\n"
      "v_fma_f32 v47, s47, v47, -v83\n"
      "v_mfma_f32_16x16x32_bf16 v[56:59], v[124:127], a[196:199], v[56:59]\n"
      "v_cvt_pk_bf16_f32 v32, v32, v33\n"
      "v_cvt_pk_bf16_f32 v34, v34, v35\n"
      "v_mfma_f32_16x16x32_bf16 v[60:63], a[184:187], a[196:199], v[60:63]\n"
      "ds_write_addtid_b32 v32 offset:40964\n"
      "ds_write_addtid_b32 v34 offset:41476\n"
      "v_cvt_pk_bf16_f32 v44, v44, v45\n"
      "v_mfma_f32_16x16x32_bf16 v[64:67], v[128:131], a[200:203], v[64:67]\n"
      "buffer_store_dwordx4 a[228:231], v18, s[8:11], s58 offen offset:256 sc1\n"
      "ds_read_b128 a[192:195], v2 offset:20544\n"
      "v_mfma_f32_16x16x32_bf16 v[68:71], a[188:191], a[200:203], v[68:71]\n"
      "ds_write_addtid_b32 v44 offset:43012\n"
      "ds_read2st64_b32 a[232:233], v12 offset0:4 offset1:6\n"
      "ds_read2st64_b32 a[234:235], v15 offset0:4 offset1:6\n"
      "v_cvt_pk_bf16_f32 v46, v46, v47\n"
      "v_mfma_f32_16x16x32_bf16 v[72:75], a[180:183], a[200:203], v[72:75]\n"
      "s_waitcnt lgkmcnt(5)\n"
      "ds_write_addtid_b32 v46 offset:43524\n"
      "v_fmac_f32 v48, s46, v36\n"
      "v_mfma_f32_16x16x32_bf16 v[52:55], a[176:179], a[204:207], v[52:55]\n"
      "ds_read_b128 a[196:199], v2 offset:16512\n"
      "v_fmac_f32 v49, s46, v37\n"
      "v_fmac_f32 v50, s46, v38\n"
      "v_mfma_f32_4x4x4_16b_bf16 v[20:23], a[216:217], a[248:249], 0\n"
      "v_fmac_f32_e64 v51, s46, v39\n"
      "v_mfma_f32_4x4x4_16b_bf16 v[24:27], a[216:217], a[250:251], 0\n"
      "v_fma_f32 v36, 0.5, v36, v108\n"
      "v_fma_f32 v37, 0.5, v37, v109\n"
      "v_mfma_f32_16x16x32_bf16 v[56:59], a[168:171], a[204:207], v[56:59]\n"
      "s_add_u32 s54, s54, 193536\n"
      "v_fma_f32 v38, 0.5, v38, v110\n"
      "v_cvt_pk_bf16_f32 v20, v20, v24\n"
      "v_mfma_f32_16x16x32_bf16 v[60:63], a[160:163], a[204:207], v[60:63]\n"
      "s_add_u32 m0, s37, 90760\n"
      "v_fma_f32 v39, 0.5, v39, v111\n"
      "v_fmac_f32 v48, s48, v84\n"
      "v_mfma_f32_16x16x32_bf16 v[64:67], a[172:175], a[208:211], v[64:67]\n"
      "s_nop 0\n"
      "buffer_load_dwordx4 v8, s[4:7], s54 offen offset:0 nt lds\n"
      "buffer_load_dwordx4 v9, s[4:7], s54 offen offset:1024 nt lds\n"
      "ds_read_b128 a[200:203], v2 offset:20608\n"
      "v_mfma_f32_16x16x32_bf16 v[68:71], a[164:167], a[208:211], v[68:71]\n"
      "s_mov_b32 m0, s64\n"
      "v_fmac_f32 v49, s48, v85\n"
      "v_cvt_pk_bf16_f32 v21, v21, v25\n"
      "v_mfma_f32_16x16x32_bf16 v[72:75], a[156:159], a[208:211], v[72:75]\n"
      "ds_write_addtid_b32 v20 offset:3584\n"
      "ds_write_addtid_b32 v21 offset:7680\n"
      "v_fmac_f32 v50, s48, v86\n"
      "v_mfma_f32_16x16x32_bf16 v[52:55], a[152:155], a[212:215], v[52:55]\n"
      "ds_read_b128 a[204:207], v3 offset:16896\n"
      "v_fmac_f32 v51, s48, v87\n"
      "v_mfma_f32_16x16x32_bf16 v[56:59], a[144:147], a[212:215], v[56:59]\n"
      "v_fmac_f32 v36, s45, v48\n"
      "v_cvt_pk_bf16_f32 v22, v22, v26\n"
      "v_mfma_f32_16x16x32_bf16 v[60:63], a[136:139], a[212:215], v[60:63]\n"
      "s_waitcnt lgkmcnt(3)\n"
      "ds_write_addtid_b32 v22 offset:11776\n"
      "v_fmac_f32 v37, s45, v49\n"
      "v_mfma_f32_16x16x32_bf16 v[64:67], a[148:151], a[192:195], v[64:67]\n"
      "ds_read_b128 a[208:211], v3 offset:16960\n"
      "v_fmac_f32 v38, s45, v50\n"
      "v_mfma_f32_16x16x32_bf16 v[68:71], a[140:143], a[192:195], v[68:71]\n"
      "v_fmac_f32 v39, s45, v51\n"
      "v_cvt_pk_bf16_f32 v23, v23, v27\n"
      "v_mfma_f32_16x16x32_bf16 v[72:75], a[132:135], a[192:195], v[72:75]\n"
      "s_nop 0\n"
      "buffer_store_dwordx4 a[232:235], v18, s[8:11], s59 offen offset:0 sc1\n"
      "ds_write_addtid_b32 v23 offset:15872\n"
      "v_mfma_f32_16x16x32_bf16 v[52:55], a[128:131], a[196:199], v[52:55]\n"
      "ds_read_b128 a[212:215], v3 offset:17024\n"
      "v_fma_f32 v48, s47, v48, -v84\n"
      "v_mfma_f32_16x16x32_bf16 v[56:59], a[120:123], a[196:199], v[56:59]\n"
      "ds_read2st64_b32 a[236:237], v13 offset0:4 offset1:6\n"
      "ds_read2st64_b32 a[238:239], v16 offset0:4 offset1:6\n"
      "v_fma_f32 v49, s47, v49, -v85\n"
      "v_mfma_f32_16x16x32_bf16 v[60:63], a[112:115], a[196:199], v[60:63]\n"
      "v_fma_f32 v50, s47, v50, -v86\n"
      "v_fma_f32 v51, s47, v51, -v87\n"
      "v_mfma_f32_16x16x32_bf16 v[64:67], a[124:127], a[200:203], v[64:67]\n"
      "ds_read_b128 a[192:195], v4 offset:16896\n"
      "v_cvt_pk_bf16_f32 v36, v36, v37\n"
      "v_mfma_f32_16x16x32_bf16 v[68:71], a[116:119], a[200:203], v[68:71]\n"
      "ds_write_addtid_b32 v36 offset:36864\n"
      "v_cvt_pk_bf16_f32 v38, v38, v39\n"
      "v_mfma_f32_16x16x32_bf16 v[72:75], a[108:111], a[200:203], v[72:75]\n"
      "s_waitcnt lgkmcnt(4)\n"
      "ds_write_addtid_b32 v38 offset:37376\n"
      "v_add_f32 v52, v52, v64\n"
      "v_add_f32 v53, v53, v65\n"
      "v_cvt_pk_bf16_f32 v48, v48, v49\n"
      "v_mfma_f32_16x16x32_bf16 v[88:91], a[0:3], a[204:207], 0\n"
      "s_nop 0\n"
      "ds_read_b128 a[196:199], v4 offset:16960\n"
      "ds_write_addtid_b32 v48 offset:38912\n"
      "ds_write_addtid_b32 v52 offset:51456\n"
      "v_add_f32_e64 v54, v54, v66\n"
      "v_mfma_f32_16x16x32_bf16 v[92:95], a[4:7], a[204:207], 0\n"
      "s_add_u32 s54, s54, 196608\n"
      "ds_read2st64_b32 a[240:241], v14 offset0:4 offset1:6\n"
      "ds_write_addtid_b32 v53 offset:51968\n"
      "v_mfma_f32_16x16x32_bf16 v[96:99], a[8:11], a[204:207], 0\n"
      "s_add_u32 m0, s37, 95104\n"
      "ds_read2st64_b32 a[242:243], v17 offset0:4 offset1:6\n"
      "v_add_f32 v55, v55, v67\n"
      "v_mfma_f32_16x16x32_bf16 v[88:91], a[12:15], a[208:211], v[88:91]\n"
      "buffer_load_dwordx4 v8, s[4:7], s54 offen offset:0 nt lds\n"
      "buffer_load_dwordx4 v9, s[4:7], s54 offen offset:1024 nt lds\n"
      "ds_read_b128 a[200:203], v4 offset:17024\n"
      "v_add_f32 v56, v56, v68\n"
      "v_mfma_f32_16x16x32_bf16 v[92:95], a[16:19], a[208:211], v[92:95]\n"
      "s_mov_b32 m0, s64\n"
      "v_add_f32 v57, v57, v69\n"
      "v_cvt_pk_bf16_f32 v50, v50, v51\n"
      "v_mfma_f32_16x16x32_bf16 v[96:99], a[20:23], a[208:211], v[96:99]\n"
      "ds_write_addtid_b32 v54 offset:52480\n"
      "ds_write_addtid_b32 v50 offset:39424\n"
      "ds_write_addtid_b32 v55 offset:52992\n"
      "v_mfma_f32_16x16x32_bf16 v[88:91], a[24:27], a[212:215], v[88:91]\n"
      "ds_read_b128 a[204:207], v5 offset:16896\n"
      "ds_write_addtid_b32 v56 offset:53504\n"
      "v_mfma_f32_16x16x32_bf16 v[92:95], a[28:31], a[212:215], v[92:95]\n"
      "ds_write_addtid_b32 v57 offset:54016\n"
      "v_add_f32 v58, v58, v70\n"
      "v_mfma_f32_16x16x32_bf16 v[96:99], a[32:35], a[212:215], v[96:99]\n"
      "s_waitcnt lgkmcnt(6)\n"
      "ds_write_addtid_b32 v58 offset:54528\n"
      "v_add_f32_e64 v59, v59, v71\n"
      "v_add_f32_e64 v60, v60, v72\n"
      "v_mfma_f32_16x16x32_bf16 v[88:91], a[36:39], a[192:195], v[88:91]\n"
      "s_nop 0\n"
      "buffer_store_dwordx4 a[236:239], v18, s[8:11], s59 offen offset:128 sc1\n"
      "ds_read_b128 a[208:211], v5 offset:16960\n"
      "v_mfma_f32_16x16x32_bf16 v[92:95], a[40:43], a[192:195], v[92:95]\n"
      "s_nop 0\n"
      "ds_write_addtid_b32 v59 offset:55040\n"
      "ds_write_addtid_b32 v60 offset:55552\n"
      "v_mfma_f32_16x16x32_bf16 v[96:99], a[44:47], a[192:195], v[96:99]\n"
      "s_nop 0\n"
      "v_add_f32_e64 v61, v61, v73\n"
      "v_add_f32_e64 v62, v62, v74\n"
      "v_add_f32_e64 v63, v63, v75\n"
      "v_mfma_f32_16x16x32_bf16 v[88:91], a[48:51], a[196:199], v[88:91]\n"
      "s_nop 0\n"
      "ds_read_b128 a[212:215], v5 offset:17024\n"
      "ds_write_addtid_b32 v61 offset:56064\n"
      "v_mfma_f32_16x16x32_bf16 v[92:95], a[52:55], a[196:199], v[92:95]\n"
      "ds_write_addtid_b32 v62 offset:56576\n"
      "ds_write_addtid_b32 v63 offset:57088\n"
      "v_mfma_f32_16x16x32_bf16 v[96:99], a[56:59], a[196:199], v[96:99]\n"
      "s_nop 0\n"
      "buffer_store_dwordx4 a[240:243], v18, s[8:11], s59 offen offset:256 sc1\n"
      "v_fma_f32 v40, -2.0, v64, v52\n"
      "v_mfma_f32_16x16x32_bf16 v[88:91], a[60:63], a[200:203], v[88:91]\n"
      "s_nop 0\n"
      "v_fma_f32 v41, -2.0, v65, v53\n"
      "v_fma_f32 v42, -2.0, v66, v54\n"
      "v_mfma_f32_16x16x32_bf16 v[92:95], a[64:67], a[200:203], v[92:95]\n"
      "s_waitcnt vmcnt(16)\n"
      "v_fma_f32 v43, -2.0, v67, v55\n"
      "v_fma_f32 v44, -2.0, v68, v56\n"
      "v_mfma_f32_16x16x32_bf16 v[96:99], a[68:71], a[200:203], v[96:99]\n"
      "s_waitcnt lgkmcnt(0)\n"
      "v_fma_f32 v45, -2.0, v69, v57\n"
      "s_barrier\n"
      "LDmid_%=:\n"
      "v_mfma_f32_16x16x32_bf16 v[88:91], a[72:75], a[204:207], v[88:91]\n"
      "s_add_u32 s62, s62, 786432\n"
      "ds_read_b128 a[192:195], v0 offset:0\n"
      "ds_read2st64_b32 v[28:29], v6 offset0:24 offset1:26\n"
      "ds_read2st64_b32 v[30:31], v6 offset0:28 offset1:30\n"
      "v_mfma_f32_16x16x32_bf16 v[92:95], a[76:79], a[204:207], v[92:95]\n"
      "s_add_u32 s58, s62, 0\n"
      "ds_read_b128 a[196:199], v0 offset:4096\n"
      "ds_read2st64_b32 v[32:33], v6 offset0:32 offset1:34\n"
      "v_mfma_f32_16x16x32_bf16 v[96:99], a[80:83], a[204:207], v[96:99]\n"
      "s_add_u32 s59, s58, 196608\n"
      "ds_read2st64_b32 v[34:35], v6 offset0:36 offset1:38\n"
      "v_fma_f32 v46, -2.0, v70, v58\n"
      "v_mfma_f32_16x16x32_bf16 v[88:91], a[84:87], a[208:211], v[88:91]\n"
      "ds_read_b128 a[200:203], v0 offset:64\n"
      "v_fma_f32 v47, -2.0, v71, v59\n"
      "v_fma_f32 v48, -2.0, v72, v60\n"
      "v_mfma_f32_16x16x32_bf16 v[92:95], a[88:91], a[208:211], v[92:95]\n"
      "ds_read2st64_b32 a[220:221], v12 offset0:177 offset1:179\n"
      "v_fma_f32 v49, -2.0, v73, v61\n"
      "v_mfma_f32_16x16x32_bf16 v[96:99], a[92:95], a[208:211], v[96:99]\n"
      "ds_read2st64_b32 a[222:223], v15 offset0:177 offset1:179\n"
      "v_fma_f32 v50, -2.0, v74, v62\n"
      "v_mfma_f32_16x16x32_bf16 v[88:91], a[96:99], a[212:215], v[88:91]\n"
      "ds_read_b128 a[204:207], v0 offset:4160\n"
      "v_fma_f32 v51, -2.0, v75, v63\n"
      "v_mfma_f32_16x16x32_bf16 v[92:95], a[100:103], a[212:215], v[92:95]\n"
      "ds_read2st64_b32 v[36:37], v6 offset0:40 offset1:42\n"
      "v_mfma_f32_16x16x32_bf16 v[96:99], a[104:107], a[212:215], v[96:99]\n"
      "s_waitcnt lgkmcnt(6)\n"
      "ds_read2st64_b32 v[38:39], v6 offset0:44 offset1:46\n"
      "v_fmac_f32 v40, s46, v28\n"
      "v_fmac_f32 v41, s46, v29\n"
      "v_fmac_f32 v42, s46, v30\n"
      "v_mfma_f32_16x16x32_bf16 v[52:55], v[252:255], a[192:195], 0\n"
      "ds_read_b128 a[208:211], v0 offset:128\n"
      "v_fmac_f32_e64 v43, s46, v31\n"
      "v_fma_f32 v28, 0.5, v28, v112\n"
      "v_mfma_f32_16x16x32_bf16 v[56:59], v[244:247], a[192:195], 0\n"
      "s_add_u32 s54, s54, -193536\n"
      "ds_read_b64_tr_b16 a[244:245], v7 offset:53760\n"
      "ds_read_b64_tr_b16 a[246:247], v7 offset:53768\n"
      "v_mfma_f32_16x16x32_bf16 v[60:63], v[236:239], a[192:195], 0\n"
      "s_add_u32 m0, s37, 107656\n"
      "v_fma_f32 v29, 0.5, v29, v113\n"
      "v_fma_f32 v30, 0.5, v30, v114\n"
      "v_fma_f32 v31, 0.5, v31, v115\n"
      "v_mfma_f32_16x16x32_bf16 v[64:67], v[248:251], a[196:199], 0\n"
      "buffer_load_dwordx4 v10, s[4:7], s54 offen offset:0 nt lds\n"
      "buffer_load_dwordx4 v11, s[4:7], s54 offen offset:1024 nt lds\n"
      "ds_read_b128 a[212:215], v0 offset:4224\n"
      "v_mfma_f32_16x16x32_bf16 v[68:71], v[240:243], a[196:199], 0\n"
      "s_mov_b32 m0, s64\n"
      "v_fmac_f32 v40, s48, v88\n"
      "v_fmac_f32 v41, s48, v89\n"
      "v_mfma_f32_16x16x32_bf16 v[72:75], v[232:235], a[196:199], 0\n"
      "s_waitcnt lgkmcnt(4)\n"
      "v_fmac_f32 v42, s48, v90\n"
      "v_fmac_f32 v43, s48, v91\n"
      "v_fmac_f32 v28, s45, v40\n"
      "v_mfma_f32_16x16x32_bf16 v[52:55], v[228:231], a[200:203], v[52:55]\n"
      "s_nop 0\n"
      "ds_read_b128 a[192:195], v1 offset:0\n"
      "v_fmac_f32 v29, s45, v41\n"
      "v_mfma_f32_16x16x32_bf16 v[56:59], v[220:223], a[200:203], v[56:59]\n"
      "v_fmac_f32 v30, s45, v42\n"
      "v_fmac_f32 v31, s45, v43\n"
      "v_mfma_f32_16x16x32_bf16 v[60:63], v[212:215], a[200:203], v[60:63]\n"
      "s_waitcnt lgkmcnt(1)\n"
      "v_fma_f32 v40, s47, v40, -v88\n"
      "v_fma_f32 v41, s47, v41, -v89\n"
      "v_mfma_f32_4x4x4_16b_bf16 v[20:23], a[216:217], a[244:245], 0\n"
      "v_fma_f32 v42, s47, v42, -v90\n"
      "v_mfma_f32_4x4x4_16b_bf16 v[24:27], a[216:217], a[246:247], 0\n"
      "v_fma_f32 v43, s47, v43, -v91\n"
      "v_cvt_pk_bf16_f32 v28, v28, v29\n"
      "v_mfma_f32_16x16x32_bf16 v[64:67], v[224:227], a[204:207], v[64:67]\n"
      "buffer_store_dwordx4 a[220:223], v18, s[8:11], s58 offen offset:0 sc1\n"
      "ds_read_b128 a[196:199], v1 offset:4096\n"
      "v_mfma_f32_16x16x32_bf16 v[68:71], v[216:219], a[204:207], v[68:71]\n"
      "ds_write_addtid_b32 v28 offset:33792\n"
      "v_cvt_pk_bf16_f32 v20, v20, v24\n"
      "v_mfma_f32_16x16x32_bf16 v[72:75], v[208:211], a[204:207], v[72:75]\n"
      "ds_write_addtid_b32 v20 offset:19456\n"
      "v_cvt_pk_bf16_f32 v30, v30, v31\n"
      "v_mfma_f32_16x16x32_bf16 v[52:55], v[204:207], a[208:211], v[52:55]\n"
      "ds_read_b128 a[200:203], v1 offset:64\n"
      "ds_write_addtid_b32 v30 offset:34304\n"
      "v_cvt_pk_bf16_f32 v40, v40, v41\n"
      "v_cvt_pk_bf16_f32 v21, v21, v25\n"
      "v_mfma_f32_16x16x32_bf16 v[56:59], v[196:199], a[208:211], v[56:59]\n"
      "ds_write_addtid_b32 v40 offset:35840\n"
      "ds_write_addtid_b32 v21 offset:23552\n"
      "ds_read2st64_b32 a[224:225], v13 offset0:177 offset1:179\n"
      "v_mfma_f32_16x16x32_bf16 v[60:63], v[188:191], a[208:211], v[60:63]\n"
      "ds_read2st64_b32 a[226:227], v16 offset0:177 offset1:179\n"
      "v_cvt_pk_bf16_f32 v42, v42, v43\n"
      "v_fmac_f32 v44, s46, v32\n"
      "v_mfma_f32_16x16x32_bf16 v[64:67], v[200:203], a[212:215], v[64:67]\n"
      "ds_read_b128 a[204:207], v1 offset:4160\n"
      "ds_write_addtid_b32 v42 offset:36352\n"
      "v_mfma_f32_16x16x32_bf16 v[68:71], v[192:195], a[212:215], v[68:71]\n"
      "v_fmac_f32 v45, s46, v33\n"
      "v_cvt_pk_bf16_f32 v22, v22, v26\n"
      "v_mfma_f32_16x16x32_bf16 v[72:75], v[184:187], a[212:215], v[72:75]\n"
      "s_waitcnt lgkmcnt(4)\n"
      "ds_write_addtid_b32 v22 offset:27648\n"
      "v_fmac_f32 v46, s46, v34\n"
      "v_fmac_f32 v47, s46, v35\n"
      "v_mfma_f32_16x16x32_bf16 v[52:55], v[180:183], a[192:195], v[52:55]\n"
      "s_nop 0\n"
      "ds_read_b128 a[208:211], v1 offset:128\n"
      "v_cvt_pk_bf16_f32 v23, v23, v27\n"
      "v_mfma_f32_16x16x32_bf16 v[56:59], v[172:175], a[192:195], v[56:59]\n"
      "s_add_u32 s54, s54, 196608\n"
      "ds_write_addtid_b32 v23 offset:31744\n"
      "ds_read2st64_b32 a[228:229], v14 offset0:177 offset1:179\n"
      "v_mfma_f32_16x16x32_bf16 v[60:63], v[164:167], a[192:195], v[60:63]\n"
      "s_add_u32 m0, s37, 112000\n"
      "ds_read2st64_b32 a[230:231], v17 offset0:177 offset1:179\n"
      "v_fma_f32 v32, 0.5, v32, v116\n"
      "v_fma_f32 v33, 0.5, v33, v117\n"
      "v_mfma_f32_16x16x32_bf16 v[64:67], v[176:179], a[196:199], v[64:67]\n"
      "buffer_load_dwordx4 v10, s[4:7], s54 offen offset:0 nt lds\n"
      "buffer_load_dwordx4 v11, s[4:7], s54 offen offset:1024 nt lds\n"
      "ds_read_b128 a[212:215], v1 offset:4224\n"
      "v_mfma_f32_16x16x32_bf16 v[68:71], v[168:171], a[196:199], v[68:71]\n"
      "s_mov_b32 m0, s64\n"
      "v_fma_f32 v34, 0.5, v34, v118\n"
      "v_fma_f32 v35, 0.5, v35, v119\n"
      "v_mfma_f32_16x16x32_bf16 v[72:75], v[160:163], a[196:199], v[72:75]\n"
      "v_fmac_f32 v44, s48, v92\n"
      "v_fmac_f32 v45, s48, v93\n"
      "v_mfma_f32_16x16x32_bf16 v[52:55], v[156:159], a[200:203], v[52:55]\n"
      "ds_read_b128 a[192:195], v2 offset:0\n"
      "v_fmac_f32 v46, s48, v94\n"
      "v_mfma_f32_16x16x32_bf16 v[56:59], v[148:151], a[200:203], v[56:59]\n"
      "v_fmac_f32 v47, s48, v95\n"
      "v_fmac_f32 v32, s45, v44\n"
      "v_mfma_f32_16x16x32_bf16 v[60:63], v[140:143], a[200:203], v[60:63]\n"
      "s_waitcnt lgkmcnt(1)\n"
      "v_fmac_f32 v33, s45, v45\n"
      "v_fmac_f32_e64 v34, s45, v46\n"
      "v_mfma_f32_16x16x32_bf16 v[64:67], v[152:155], a[204:207], v[64:67]\n"
      "buffer_store_dwordx4 a[224:227], v18, s[8:11], s58 offen offset:128 sc1\n"
      "ds_read_b128 a[196:199], v2 offset:4096\n"
      "v_mfma_f32_16x16x32_bf16 v[68:71], v[144:147], a[204:207], v[68:71]\n"
      "v_fmac_f32 v35, s45, v47\n"
      "v_fma_f32 v44, s47, v44, -v92\n"
      "v_mfma_f32_16x16x32_bf16 v[72:75], v[136:139], a[204:207], v[72:75]\n"
      "s_nop 0\n"
      "ds_read_b64_tr_b16 a[248:249], v7 offset:54272\n"
      "ds_read_b64_tr_b16 a[250:251], v7 offset:54280\n"
      "v_fma_f32 v45, s47, v45, -v93\n"
      "v_fma_f32 v46, s47, v46, -v94\n"
      "v_mfma_f32_16x16x32_bf16 v[52:55], v[132:135], a[208:211], v[52:55]\n"
      "ds_read_b128 a[200:203], v2 offset:64\n"
      "v_fma_f32 v47, s47, v47, -v95\n"
      "v_mfma_f32_16x16x32_bf16 v[56:59], v[124:127], a[208:211], v[56:59]\n"
      "v_cvt_pk_bf16_f32 v32, v32, v33\n"
      "v_cvt_pk_bf16_f32 v34, v34, v35\n"
      "v_mfma_f32_16x16x32_bf16 v[60:63], a[184:187], a[208:211], v[60:63]\n"
      "ds_write_addtid_b32 v32 offset:41988\n"
      "ds_write_addtid_b32 v34 offset:42500\n"
      "v_cvt_pk_bf16_f32 v44, v44, v45\n"
      "v_mfma_f32_16x16x32_bf16 v[64:67], v[128:131], a[212:215], v[64:67]\n"
      "buffer_store_dwordx4 a[228:231], v18, s[8:11], s58 offen offset:256 sc1\n"
      "ds_read_b128 a[204:207], v2 offset:4160\n"
      "v_mfma_f32_16x16x32_bf16 v[68:71], a[188:191], a[212:215], v[68:71]\n"
      "ds_write_addtid_b32 v44 offset:44036\n"
      "ds_read2st64_b32 a[232:233], v12 offset0:0 offset1:2\n"
      "ds_read2st64_b32 a[234:235], v15 offset0:0 offset1:2\n"
      "v_cvt_pk_bf16_f32 v46, v46, v47\n"
      "v_mfma_f32_16x16x32_bf16 v[72:75], a[180:183], a[212:215], v[72:75]\n"
      "s_waitcnt lgkmcnt(5)\n"
      "ds_write_addtid_b32 v46 offset:44548\n"
      "v_fmac_f32 v48, s46, v36\n"
      "v_mfma_f32_16x16x32_bf16 v[52:55], a[176:179], a[192:195], v[52:55]\n"
      "ds_read_b128 a[208:211], v2 offset:128\n"
      "v_fmac_f32 v49, s46, v37\n"
      "v_fmac_f32 v50, s46, v38\n"
      "v_mfma_f32_4x4x4_16b_bf16 v[20:23], a[216:217], a[248:249], 0\n"
      "v_fmac_f32_e64 v51, s46, v39\n"
      "v_mfma_f32_4x4x4_16b_bf16 v[24:27], a[216:217], a[250:251], 0\n"
      "v_fma_f32 v36, 0.5, v36, v120\n"
      "v_fma_f32 v37, 0.5, v37, v121\n"
      "v_mfma_f32_16x16x32_bf16 v[56:59], a[168:171], a[192:195], v[56:59]\n"
      "s_add_u32 s54, s54, 193536\n"
      "v_fma_f32 v38, 0.5, v38, v122\n"
      "v_cvt_pk_bf16_f32 v20, v20, v24\n"
      "v_mfma_f32_16x16x32_bf16 v[60:63], a[160:163], a[192:195], v[60:63]\n"
      "s_add_u32 m0, s37, 116224\n"
      "v_fma_f32 v39, 0.5, v39, v123\n"
      "v_fmac_f32_e64 v48, s48, v96\n"
      "v_mfma_f32_16x16x32_bf16 v[64:67], a[172:175], a[196:199], v[64:67]\n"
      "buffer_load_dwordx4 v8, s[4:7], s54 offen offset:0 nt lds\n"
      "buffer_load_dwordx4 v9, s[4:7], s54 offen offset:1024 nt lds\n"
      "ds_read_b128 a[212:215], v2 offset:4224\n"
      "v_mfma_f32_16x16x32_bf16 v[68:71], a[164:167], a[196:199], v[68:71]\n"
      "s_mov_b32 m0, s64\n"
      "v_fmac_f32 v49, s48, v97\n"
      "v_cvt_pk_bf16_f32 v21, v21, v25\n"
      "v_mfma_f32_16x16x32_bf16 v[72:75], a[156:159], a[196:199], v[72:75]\n"
      "ds_write_addtid_b32 v20 offset:19968\n"
      "ds_write_addtid_b32 v21 offset:24064\n"
      "v_fmac_f32 v50, s48, v98\n"
      "v_mfma_f32_16x16x32_bf16 v[52:55], a[152:155], a[200:203], v[52:55]\n"
      "ds_read_b128 a[192:195], v3 offset:33792\n"
      "v_fmac_f32 v51, s48, v99\n"
      "v_mfma_f32_16x16x32_bf16 v[56:59], a[144:147], a[200:203], v[56:59]\n"
      "v_fmac_f32_e64 v36, s45, v48\n"
      "v_cvt_pk_bf16_f32 v22, v22, v26\n"
      "v_mfma_f32_16x16x32_bf16 v[60:63], a[136:139], a[200:203], v[60:63]\n"
      "s_waitcnt lgkmcnt(3)\n"
      "ds_write_addtid_b32 v22 offset:28160\n"
      "v_fmac_f32 v37, s45, v49\n"
      "v_mfma_f32_16x16x32_bf16 v[64:67], a[148:151], a[204:207], v[64:67]\n"
      "ds_read_b128 a[196:199], v3 offset:33856\n"
      "v_fmac_f32 v38, s45, v50\n"
      "v_mfma_f32_16x16x32_bf16 v[68:71], a[140:143], a[204:207], v[68:71]\n"
      "v_fmac_f32 v39, s45, v51\n"
      "v_cvt_pk_bf16_f32 v23, v23, v27\n"
      "v_mfma_f32_16x16x32_bf16 v[72:75], a[132:135], a[204:207], v[72:75]\n"
      "buffer_store_dwordx4 a[232:235], v18, s[8:11], s59 offen offset:0 sc1\n"
      "ds_write_addtid_b32 v23 offset:32256\n"
      "v_mfma_f32_16x16x32_bf16 v[52:55], a[128:131], a[208:211], v[52:55]\n"
      "ds_read_b128 a[200:203], v3 offset:33920\n"
      "v_fma_f32 v48, s47, v48, -v96\n"
      "v_mfma_f32_16x16x32_bf16 v[56:59], a[120:123], a[208:211], v[56:59]\n"
      "ds_read2st64_b32 a[236:237], v13 offset0:0 offset1:2\n"
      "ds_read2st64_b32 a[238:239], v16 offset0:0 offset1:2\n"
      "v_fma_f32 v49, s47, v49, -v97\n"
      "v_mfma_f32_16x16x32_bf16 v[60:63], a[112:115], a[208:211], v[60:63]\n"
      "v_fma_f32 v50, s47, v50, -v98\n"
      "v_fma_f32 v51, s47, v51, -v99\n"
      "v_mfma_f32_16x16x32_bf16 v[64:67], a[124:127], a[212:215], v[64:67]\n"
      "ds_read_b128 a[204:207], v4 offset:33792\n"
      "v_cvt_pk_bf16_f32 v36, v36, v37\n"
      "v_mfma_f32_16x16x32_bf16 v[68:71], a[116:119], a[212:215], v[68:71]\n"
      "ds_write_addtid_b32 v36 offset:37888\n"
      "v_cvt_pk_bf16_f32 v38, v38, v39\n"
      "v_mfma_f32_16x16x32_bf16 v[72:75], a[108:111], a[212:215], v[72:75]\n"
      "s_waitcnt lgkmcnt(4)\n"
      "ds_write_addtid_b32 v38 offset:38400\n"
      "v_add_f32 v52, v52, v64\n"
      "v_add_f32_e64 v53, v53, v65\n"
      "v_cvt_pk_bf16_f32 v48, v48, v49\n"
      "v_mfma_f32_16x16x32_bf16 v[100:103], a[0:3], a[192:195], 0\n"
      "ds_read_b128 a[208:211], v4 offset:33856\n"
      "ds_write_addtid_b32 v48 offset:39936\n"
      "ds_write_addtid_b32 v52 offset:45312\n"
      "v_add_f32_e64 v54, v54, v66\n"
      "v_mfma_f32_16x16x32_bf16 v[104:107], a[4:7], a[192:195], 0\n"
      "s_add_u32 s54, s54, 196608\n"
      "ds_read2st64_b32 a[240:241], v14 offset0:0 offset1:2\n"
      "ds_write_addtid_b32 v53 offset:45824\n"
      "v_mfma_f32_16x16x32_bf16 v[108:111], a[8:11], a[192:195], 0\n"
      "s_add_u32 m0, s37, 120328\n"
      "ds_read2st64_b32 a[242:243], v17 offset0:0 offset1:2\n"
      "v_add_f32 v55, v55, v67\n"
      "v_mfma_f32_16x16x32_bf16 v[100:103], a[12:15], a[196:199], v[100:103]\n"
      "s_nop 0\n"
      "buffer_load_dwordx4 v8, s[4:7], s54 offen offset:0 nt lds\n"
      "buffer_load_dwordx4 v9, s[4:7], s54 offen offset:1024 nt lds\n"
      "ds_read_b128 a[212:215], v4 offset:33920\n"
      "v_add_f32 v56, v56, v68\n"
      "v_mfma_f32_16x16x32_bf16 v[104:107], a[16:19], a[196:199], v[104:107]\n"
      "s_mov_b32 m0, s64\n"
      "v_add_f32 v57, v57, v69\n"
      "v_cvt_pk_bf16_f32 v50, v50, v51\n"
      "v_mfma_f32_16x16x32_bf16 v[108:111], a[20:23], a[196:199], v[108:111]\n"
      "s_nop 0\n"
      "ds_write_addtid_b32 v54 offset:46336\n"
      "ds_write_addtid_b32 v50 offset:40448\n"
      "ds_write_addtid_b32 v55 offset:46848\n"
      "v_mfma_f32_16x16x32_bf16 v[100:103], a[24:27], a[200:203], v[100:103]\n"
      "ds_read_b128 a[192:195], v5 offset:33792\n"
      "ds_write_addtid_b32 v56 offset:47360\n"
      "v_mfma_f32_16x16x32_bf16 v[104:107], a[28:31], a[200:203], v[104:107]\n"
      "ds_write_addtid_b32 v57 offset:47872\n"
      "v_add_f32 v58, v58, v70\n"
      "v_mfma_f32_16x16x32_bf16 v[108:111], a[32:35], a[200:203], v[108:111]\n"
      "s_waitcnt lgkmcnt(6)\n"
      "ds_write_addtid_b32 v58 offset:48384\n"
      "v_add_f32 v59, v59, v71\n"
      "v_add_f32 v60, v60, v72\n"
      "v_mfma_f32_16x16x32_bf16 v[100:103], a[36:39], a[204:207], v[100:103]\n"
      "s_nop 0\n"
      "buffer_store_dwordx4 a[236:239], v18, s[8:11], s59 offen offset:128 sc1\n"
      "ds_read_b128 a[196:199], v5 offset:33856\n"
      "v_mfma_f32_16x16x32_bf16 v[104:107], a[40:43], a[204:207], v[104:107]\n"
      "s_nop 0\n"
      "ds_write_addtid_b32 v59 offset:48896\n"
      "ds_write_addtid_b32 v60 offset:49408\n"
      "v_mfma_f32_16x16x32_bf16 v[108:111], a[44:47], a[204:207], v[108:111]\n"
      "s_nop 0\n"
      "v_add_f32_e64 v61, v61, v73\n"
      "v_add_f32_e64 v62, v62, v74\n"
      "v_add_f32_e64 v63, v63, v75\n"
      "v_mfma_f32_16x16x32_bf16 v[100:103], a[48:51], a[208:211], v[100:103]\n"
      "s_nop 0\n"
      "ds_read_b128 a[200:203], v5 offset:33920\n"
      "ds_write_addtid_b32 v61 offset:49920\n"
      "v_mfma_f32_16x16x32_bf16 v[104:107], a[52:55], a[208:211], v[104:107]\n"
      "s_nop 0\n"
      "ds_write_addtid_b32 v62 offset:50432\n"
      "ds_write_addtid_b32 v63 offset:50944\n"
      "v_mfma_f32_16x16x32_bf16 v[108:111], a[56:59], a[208:211], v[108:111]\n"
      "s_nop 0\n"
      "buffer_store_dwordx4 a[240:243], v18, s[8:11], s59 offen offset:256 sc1\n"
      "v_fma_f32 v40, -2.0, v64, v52\n"
      "v_mfma_f32_16x16x32_bf16 v[100:103], a[60:63], a[212:215], v[100:103]\n"
      "s_nop 0\n"
      "v_fma_f32 v41, -2.0, v65, v53\n"
      "v_fma_f32 v42, -2.0, v66, v54\n"
      "v_mfma_f32_16x16x32_bf16 v[104:107], a[64:67], a[212:215], v[104:107]\n"
      "s_waitcnt vmcnt(16)\n"
      "v_fma_f32 v43, -2.0, v67, v55\n"
      "v_fma_f32 v44, -2.0, v68, v56\n"
      "v_mfma_f32_16x16x32_bf16 v[108:111], a[68:71], a[212:215], v[108:111]\n"
      "s_waitcnt lgkmcnt(0)\n"
      "v_fma_f32 v45, -2.0, v69, v57\n"
      "s_barrier\n"
      "v_mfma_f32_16x16x32_bf16 v[100:103], a[72:75], a[192:195], v[100:103]\n"
      "s_add_u32 s58, s58, 3072\n"
      "ds_read_b128 a[204:207], v0 offset:16384\n"
      "ds_read2st64_b32 v[28:29], v6 offset0:0 offset1:2\n"
      "ds_read2st64_b32 v[30:31], v6 offset0:4 offset1:6\n"
      "v_mfma_f32_16x16x32_bf16 v[104:107], a[76:79], a[192:195], v[104:107]\n"
      "s_add_u32 s59, s58, 196608\n"
      "ds_read_b128 a[208:211], v0 offset:20480\n"
      "ds_read2st64_b32 v[32:33], v6 offset0:8 offset1:10\n"
      "v_mfma_f32_16x16x32_bf16 v[108:111], a[80:83], a[192:195], v[108:111]\n"
      "ds_read2st64_b32 v[34:35], v6 offset0:12 offset1:14\n"
      "v_fma_f32 v46, -2.0, v70, v58\n"
      "v_mfma_f32_16x16x32_bf16 v[100:103], a[84:87], a[196:199], v[100:103]\n"
      "ds_read_b128 a[212:215], v0 offset:16448\n"
      "v_fma_f32 v47, -2.0, v71, v59\n"
      "v_fma_f32 v48, -2.0, v72, v60\n"
      "v_mfma_f32_16x16x32_bf16 v[104:107], a[88:91], a[196:199], v[104:107]\n"
      "ds_read2st64_b32 a[220:221], v12 offset0:181 offset1:183\n"
      "v_fma_f32 v49, -2.0, v73, v61\n"
      "v_mfma_f32_16x16x32_bf16 v[108:111], a[92:95], a[196:199], v[108:111]\n"
      "ds_read2st64_b32 a[222:223], v15 offset0:181 offset1:183\n"
      "v_fma_f32 v50, -2.0, v74, v62\n"
      "v_mfma_f32_16x16x32_bf16 v[100:103], a[96:99], a[200:203], v[100:103]\n"
      "ds_read_b128 a[192:195], v0 offset:20544\n"
      "v_fma_f32 v51, -2.0, v75, v63\n"
      "v_mfma_f32_16x16x32_bf16 v[104:107], a[100:103], a[200:203], v[104:107]\n"
      "ds_read2st64_b32 v[36:37], v6 offset0:16 offset1:18\n"
      "v_mfma_f32_16x16x32_bf16 v[108:111], a[104:107], a[200:203], v[108:111]\n"
      "s_waitcnt lgkmcnt(6)\n"
      "ds_read2st64_b32 v[38:39], v6 offset0:20 offset1:22\n"
      "v_fmac_f32 v40, s46, v28\n"
      "v_fmac_f32 v41, s46, v29\n"
      "v_fmac_f32 v42, s46, v30\n"
      "v_mfma_f32_16x16x32_bf16 v[52:55], v[252:255], a[204:207], 0\n"
      "ds_read_b128 a[196:199], v0 offset:16512\n"
      "v_fmac_f32_e64 v43, s46, v31\n"
      "v_fma_f32 v28, 0.5, v28, v76\n"
      "v_mfma_f32_16x16x32_bf16 v[56:59], v[244:247], a[204:207], 0\n"
      "s_add_u32 s54, s54, -193536\n"
      "ds_read_b64_tr_b16 a[244:245], v7 offset:3072\n"
      "ds_read_b64_tr_b16 a[246:247], v7 offset:3080\n"
      "v_mfma_f32_16x16x32_bf16 v[60:63], v[236:239], a[204:207], 0\n"
      "s_add_u32 m0, s37, 133120\n"
      "v_fma_f32 v29, 0.5, v29, v77\n"
      "v_fma_f32 v30, 0.5, v30, v78\n"
      "v_fma_f32 v31, 0.5, v31, v79\n"
      "v_mfma_f32_16x16x32_bf16 v[64:67], v[248:251], a[208:211], 0\n"
      "buffer_load_dwordx4 v10, s[4:7], s54 offen offset:0 nt lds\n"
      "buffer_load_dwordx4 v11, s[4:7], s54 offen offset:1024 nt lds\n"
      "ds_read_b128 a[200:203], v0 offset:20608\n"
      "v_mfma_f32_16x16x32_bf16 v[68:71], v[240:243], a[208:211], 0\n"
      "s_mov_b32 m0, s64\n"
      "v_fmac_f32 v40, s48, v100\n"
      "v_fmac_f32 v41, s48, v101\n"
      "v_mfma_f32_16x16x32_bf16 v[72:75], v[232:235], a[208:211], 0\n"
      "s_waitcnt lgkmcnt(4)\n"
      "v_fmac_f32 v42, s48, v102\n"
      "v_fmac_f32 v43, s48, v103\n"
      "v_fmac_f32_e64 v28, s45, v40\n"
      "v_mfma_f32_16x16x32_bf16 v[52:55], v[228:231], a[212:215], v[52:55]\n"
      "ds_read_b128 a[204:207], v1 offset:16384\n"
      "v_fmac_f32 v29, s45, v41\n"
      "v_mfma_f32_16x16x32_bf16 v[56:59], v[220:223], a[212:215], v[56:59]\n"
      "v_fmac_f32 v30, s45, v42\n"
      "v_fmac_f32 v31, s45, v43\n"
      "v_mfma_f32_16x16x32_bf16 v[60:63], v[212:215], a[212:215], v[60:63]\n"
      "s_waitcnt lgkmcnt(1)\n"
      "v_fma_f32 v40, s47, v40, -v100\n"
      "v_fma_f32 v41, s47, v41, -v101\n"
      "v_mfma_f32_4x4x4_16b_bf16 v[20:23], a[216:217], a[244:245], 0\n"
      "v_fma_f32 v42, s47, v42, -v102\n"
      "v_mfma_f32_4x4x4_16b_bf16 v[24:27], a[216:217], a[246:247], 0\n"
      "v_fma_f32 v43, s47, v43, -v103\n"
      "v_cvt_pk_bf16_f32 v28, v28, v29\n"
      "v_mfma_f32_16x16x32_bf16 v[64:67], v[224:227], a[192:195], v[64:67]\n"
      "buffer_store_dwordx4 a[220:223], v18, s[8:11], s58 offen offset:0 sc1\n"
      "ds_read_b128 a[208:211], v1 offset:20480\n"
      "v_mfma_f32_16x16x32_bf16 v[68:71], v[216:219], a[192:195], v[68:71]\n"
      "ds_write_addtid_b32 v28 offset:32768\n"
      "v_cvt_pk_bf16_f32 v20, v20, v24\n"
      "v_mfma_f32_16x16x32_bf16 v[72:75], v[208:211], a[192:195], v[72:75]\n"
      "ds_write_addtid_b32 v20 offset:3072\n"
      "v_cvt_pk_bf16_f32 v30, v30, v31\n"
      "v_mfma_f32_16x16x32_bf16 v[52:55], v[204:207], a[196:199], v[52:55]\n"
      "ds_read_b128 a[212:215], v1 offset:16448\n"
      "ds_write_addtid_b32 v30 offset:33280\n"
      "v_cvt_pk_bf16_f32 v40, v40, v41\n"
      "v_cvt_pk_bf16_f32 v21, v21, v25\n"
      "v_mfma_f32_16x16x32_bf16 v[56:59], v[196:199], a[196:199], v[56:59]\n"
      "ds_write_addtid_b32 v40 offset:34816\n"
      "ds_write_addtid_b32 v21 offset:7168\n"
      "ds_read2st64_b32 a[224:225], v13 offset0:181 offset1:183\n"
      "v_mfma_f32_16x16x32_bf16 v[60:63], v[188:191], a[196:199], v[60:63]\n"
      "ds_read2st64_b32 a[226:227], v16 offset0:181 offset1:183\n"
      "v_cvt_pk_bf16_f32 v42, v42, v43\n"
      "v_fmac_f32 v44, s46, v32\n"
      "v_mfma_f32_16x16x32_bf16 v[64:67], v[200:203], a[200:203], v[64:67]\n"
      "ds_read_b128 a[192:195], v1 offset:20544\n"
      "ds_write_addtid_b32 v42 offset:35328\n"
      "v_mfma_f32_16x16x32_bf16 v[68:71], v[192:195], a[200:203], v[68:71]\n"
      "v_fmac_f32 v45, s46, v33\n"
      "v_cvt_pk_bf16_f32 v22, v22, v26\n"
      "v_mfma_f32_16x16x32_bf16 v[72:75], v[184:187], a[200:203], v[72:75]\n"
      "s_waitcnt lgkmcnt(4)\n"
      "ds_write_addtid_b32 v22 offset:11264\n"
      "v_fmac_f32 v46, s46, v34\n"
      "v_fmac_f32 v47, s46, v35\n"
      "v_mfma_f32_16x16x32_bf16 v[52:55], v[180:183], a[204:207], v[52:55]\n"
      "s_nop 0\n"
      "ds_read_b128 a[196:199], v1 offset:16512\n"
      "v_cvt_pk_bf16_f32 v23, v23, v27\n"
      "v_mfma_f32_16x16x32_bf16 v[56:59], v[172:175], a[204:207], v[56:59]\n"
      "s_add_u32 s54, s54, 196608\n"
      "ds_write_addtid_b32 v23 offset:15360\n"
      "ds_read2st64_b32 a[228:229], v14 offset0:181 offset1:183\n"
      "v_mfma_f32_16x16x32_bf16 v[60:63], v[164:167], a[204:207], v[60:63]\n"
      "s_add_u32 m0, s37, 137224\n"
      "ds_read2st64_b32 a[230:231], v17 offset0:181 offset1:183\n"
      "v_fma_f32 v32, 0.5, v32, v80\n"
      "v_fma_f32 v33, 0.5, v33, v81\n"
      "v_mfma_f32_16x16x32_bf16 v[64:67], v[176:179], a[208:211], v[64:67]\n"
      "s_nop 0\n"
      "buffer_load_dwordx4 v10, s[4:7], s54 offen offset:0 nt lds\n"
      "buffer_load_dwordx4 v11, s[4:7], s54 offen offset:1024 nt lds\n"
      "ds_read_b128 a[200:203], v1 offset:20608\n"
      "v_mfma_f32_16x16x32_bf16 v[68:71], v[168:171], a[208:211], v[68:71]\n"
      "s_mov_b32 m0, s64\n"
      "v_fma_f32 v34, 0.5, v34, v82\n"
      "v_fma_f32 v35, 0.5, v35, v83\n"
      "v_mfma_f32_16x16x32_bf16 v[72:75], v[160:163], a[208:211], v[72:75]\n"
      "v_fmac_f32 v44, s48, v104\n"
      "v_fmac_f32 v45, s48, v105\n"
      "v_mfma_f32_16x16x32_bf16 v[52:55], v[156:159], a[212:215], v[52:55]\n"
      "ds_read_b128 a[204:207], v2 offset:16384\n"
      "v_fmac_f32 v46, s48, v106\n"
      "v_mfma_f32_16x16x32_bf16 v[56:59], v[148:151], a[212:215], v[56:59]\n"
      "v_fmac_f32 v47, s48, v107\n"
      "v_fmac_f32 v32, s45, v44\n"
      "v_mfma_f32_16x16x32_bf16 v[60:63], v[140:143], a[212:215], v[60:63]\n"
      "s_waitcnt lgkmcnt(1)\n"
      "v_fmac_f32 v33, s45, v45\n"
      "v_fmac_f32 v34, s45, v46\n"
      "v_mfma_f32_16x16x32_bf16 v[64:67], v[152:155], a[192:195], v[64:67]\n"
      "buffer_store_dwordx4 a[224:227], v18, s[8:11], s58 offen offset:128 sc1\n"
      "ds_read_b128 a[208:211], v2 offset:20480\n"
      "v_mfma_f32_16x16x32_bf16 v[68:71], v[144:147], a[192:195], v[68:71]\n"
      "v_fmac_f32 v35, s45, v47\n"
      "v_fma_f32 v44, s47, v44, -v104\n"
      "v_mfma_f32_16x16x32_bf16 v[72:75], v[136:139], a[192:195], v[72:75]\n"
      "s_nop 0\n"
      "ds_read_b64_tr_b16 a[248:249], v7 offset:3584\n"
      "ds_read_b64_tr_b16 a[250:251], v7 offset:3592\n"
      "v_fma_f32 v45, s47, v45, -v105\n"
      "v_fma_f32 v46, s47, v46, -v106\n"
      "v_mfma_f32_16x16x32_bf16 v[52:55], v[132:135], a[196:199], v[52:55]\n"
      "ds_read_b128 a[212:215], v2 offset:16448\n"
      "v_fma_f32 v47, s47, v47, -v107\n"
      "v_mfma_f32_16x16x32_bf16 v[56:59], v[124:127], a[196:199], v[56:59]\n"
      "v_cvt_pk_bf16_f32 v32, v32, v33\n"
      "v_cvt_pk_bf16_f32 v34, v34, v35\n"
      "v_mfma_f32_16x16x32_bf16 v[60:63], a[184:187], a[196:199], v[60:63]\n"
      "ds_write_addtid_b32 v32 offset:40964\n"
      "ds_write_addtid_b32 v34 offset:41476\n"
      "v_cvt_pk_bf16_f32 v44, v44, v45\n"
      "v_mfma_f32_16x16x32_bf16 v[64:67], v[128:131], a[200:203], v[64:67]\n"
      "buffer_store_dwordx4 a[228:231], v18, s[8:11], s58 offen offset:256 sc1\n"
      "ds_read_b128 a[192:195], v2 offset:20544\n"
      "v_mfma_f32_16x16x32_bf16 v[68:71], a[188:191], a[200:203], v[68:71]\n"
      "s_nop 0\n"
      "ds_write_addtid_b32 v44 offset:43012\n"
      "ds_read2st64_b32 a[232:233], v12 offset0:4 offset1:6\n"
      "ds_read2st64_b32 a[234:235], v15 offset0:4 offset1:6\n"
      "v_cvt_pk_bf16_f32 v46, v46, v47\n"
      "v_mfma_f32_16x16x32_bf16 v[72:75], a[180:183], a[200:203], v[72:75]\n"
      "s_waitcnt lgkmcnt(5)\n"
      "ds_write_addtid_b32 v46 offset:43524\n"
      "v_fmac_f32 v48, s46, v36\n"
      "v_mfma_f32_16x16x32_bf16 v[52:55], a[176:179], a[204:207], v[52:55]\n"
      "ds_read_b128 a[196:199], v2 offset:16512\n"
      "v_fmac_f32 v49, s46, v37\n"
      "v_fmac_f32 v50, s46, v38\n"
      "v_mfma_f32_4x4x4_16b_bf16 v[20:23], a[216:217], a[248:249], 0\n"
      "v_fmac_f32 v51, s46, v39\n"
      "v_mfma_f32_4x4x4_16b_bf16 v[24:27], a[216:217], a[250:251], 0\n"
      "v_fma_f32 v36, 0.5, v36, v84\n"
      "v_fma_f32 v37, 0.5, v37, v85\n"
      "v_mfma_f32_16x16x32_bf16 v[56:59], a[168:171], a[204:207], v[56:59]\n"
      "s_add_u32 s54, s54, 193536\n"
      "v_fma_f32 v38, 0.5, v38, v86\n"
      "v_cvt_pk_bf16_f32 v20, v20, v24\n"
      "v_mfma_f32_16x16x32_bf16 v[60:63], a[160:163], a[204:207], v[60:63]\n"
      "s_add_u32 m0, s37, 124552\n"
      "v_fma_f32 v39, 0.5, v39, v87\n"
      "v_fmac_f32 v48, s48, v108\n"
      "v_mfma_f32_16x16x32_bf16 v[64:67], a[172:175], a[208:211], v[64:67]\n"
      "buffer_load_dwordx4 v8, s[4:7], s54 offen offset:0 nt lds\n"
      "buffer_load_dwordx4 v9, s[4:7], s54 offen offset:1024 nt lds\n"
      "ds_read_b128 a[200:203], v2 offset:20608\n"
      "v_mfma_f32_16x16x32_bf16 v[68:71], a[164:167], a[208:211], v[68:71]\n"
      "s_mov_b32 m0, s64\n"
      "v_fmac_f32_e64 v49, s48, v109\n"
      "v_cvt_pk_bf16_f32 v21, v21, v25\n"
      "v_mfma_f32_16x16x32_bf16 v[72:75], a[156:159], a[208:211], v[72:75]\n"
      "ds_write_addtid_b32 v20 offset:3584\n"
      "ds_write_addtid_b32 v21 offset:7680\n"
      "v_fmac_f32 v50, s48, v110\n"
      "v_mfma_f32_16x16x32_bf16 v[52:55], a[152:155], a[212:215], v[52:55]\n"
      "ds_read_b128 a[204:207], v3 offset:50688\n"
      "v_fmac_f32 v51, s48, v111\n"
      "v_mfma_f32_16x16x32_bf16 v[56:59], a[144:147], a[212:215], v[56:59]\n"
      "v_fmac_f32_e64 v36, s45, v48\n"
      "v_cvt_pk_bf16_f32 v22, v22, v26\n"
      "v_mfma_f32_16x16x32_bf16 v[60:63], a[136:139], a[212:215], v[60:63]\n"
      "s_waitcnt lgkmcnt(3)\n"
      "ds_write_addtid_b32 v22 offset:11776\n"
      "v_fmac_f32 v37, s45, v49\n"
      "v_mfma_f32_16x16x32_bf16 v[64:67], a[148:151], a[192:195], v[64:67]\n"
      "ds_read_b128 a[208:211], v3 offset:50752\n"
      "v_fmac_f32 v38, s45, v50\n"
      "v_mfma_f32_16x16x32_bf16 v[68:71], a[140:143], a[192:195], v[68:71]\n"
      "v_fmac_f32 v39, s45, v51\n"
      "v_cvt_pk_bf16_f32 v23, v23, v27\n"
      "v_mfma_f32_16x16x32_bf16 v[72:75], a[132:135], a[192:195], v[72:75]\n"
      "buffer_store_dwordx4 a[232:235], v18, s[8:11], s59 offen offset:0 sc1\n"
      "ds_write_addtid_b32 v23 offset:15872\n"
      "v_mfma_f32_16x16x32_bf16 v[52:55], a[128:131], a[196:199], v[52:55]\n"
      "ds_read_b128 a[212:215], v3 offset:50816\n"
      "v_fma_f32 v48, s47, v48, -v108\n"
      "v_mfma_f32_16x16x32_bf16 v[56:59], a[120:123], a[196:199], v[56:59]\n"
      "ds_read2st64_b32 a[236:237], v13 offset0:4 offset1:6\n"
      "ds_read2st64_b32 a[238:239], v16 offset0:4 offset1:6\n"
      "v_fma_f32 v49, s47, v49, -v109\n"
      "v_mfma_f32_16x16x32_bf16 v[60:63], a[112:115], a[196:199], v[60:63]\n"
      "v_fma_f32 v50, s47, v50, -v110\n"
      "v_fma_f32 v51, s47, v51, -v111\n"
      "v_mfma_f32_16x16x32_bf16 v[64:67], a[124:127], a[200:203], v[64:67]\n"
      "ds_read_b128 a[192:195], v4 offset:50688\n"
      "v_cvt_pk_bf16_f32 v36, v36, v37\n"
      "v_mfma_f32_16x16x32_bf16 v[68:71], a[116:119], a[200:203], v[68:71]\n"
      "ds_write_addtid_b32 v36 offset:36864\n"
      "v_cvt_pk_bf16_f32 v38, v38, v39\n"
      "v_mfma_f32_16x16x32_bf16 v[72:75], a[108:111], a[200:203], v[72:75]\n"
      "s_waitcnt lgkmcnt(4)\n"
      "ds_write_addtid_b32 v38 offset:37376\n"
      "v_add_f32 v52, v52, v64\n"
      "v_add_f32_e64 v53, v53, v65\n"
      "v_cvt_pk_bf16_f32 v48, v48, v49\n"
      "v_mfma_f32_16x16x32_bf16 v[112:115], a[0:3], a[204:207], 0\n"
      "ds_read_b128 a[196:199], v4 offset:50752\n"
      "ds_write_addtid_b32 v48 offset:38912\n"
      "ds_write_addtid_b32 v52 offset:51456\n"
      "v_add_f32_e64 v54, v54, v66\n"
      "v_mfma_f32_16x16x32_bf16 v[116:119], a[4:7], a[204:207], 0\n"
      "s_add_u32 s54, s54, 196608\n"
      "ds_read2st64_b32 a[240:241], v14 offset0:4 offset1:6\n"
      "ds_write_addtid_b32 v53 offset:51968\n"
      "v_mfma_f32_16x16x32_bf16 v[120:123], a[8:11], a[204:207], 0\n"
      "s_add_u32 m0, s37, 128896\n"
      "ds_read2st64_b32 a[242:243], v17 offset0:4 offset1:6\n"
      "v_add_f32 v55, v55, v67\n"
      "v_mfma_f32_16x16x32_bf16 v[112:115], a[12:15], a[208:211], v[112:115]\n"
      "s_nop 0\n"
      "buffer_load_dwordx4 v8, s[4:7], s54 offen offset:0 nt lds\n"
      "buffer_load_dwordx4 v9, s[4:7], s54 offen offset:1024 nt lds\n"
      "ds_read_b128 a[200:203], v4 offset:50816\n"
      "v_add_f32 v56, v56, v68\n"
      "v_mfma_f32_16x16x32_bf16 v[116:119], a[16:19], a[208:211], v[116:119]\n"
      "s_mov_b32 m0, s64\n"
      "v_add_f32 v57, v57, v69\n"
      "v_cvt_pk_bf16_f32 v50, v50, v51\n"
      "v_mfma_f32_16x16x32_bf16 v[120:123], a[20:23], a[208:211], v[120:123]\n"
      "s_nop 0\n"
      "ds_write_addtid_b32 v54 offset:52480\n"
      "ds_write_addtid_b32 v50 offset:39424\n"
      "ds_write_addtid_b32 v55 offset:52992\n"
      "v_mfma_f32_16x16x32_bf16 v[112:115], a[24:27], a[212:215], v[112:115]\n"
      "ds_read_b128 a[204:207], v5 offset:50688\n"
      "ds_write_addtid_b32 v56 offset:53504\n"
      "v_mfma_f32_16x16x32_bf16 v[116:119], a[28:31], a[212:215], v[116:119]\n"
      "ds_write_addtid_b32 v57 offset:54016\n"
      "v_add_f32_e64 v58, v58, v70\n"
      "v_mfma_f32_16x16x32_bf16 v[120:123], a[32:35], a[212:215], v[120:123]\n"
      "s_waitcnt lgkmcnt(6)\n"
      "ds_write_addtid_b32 v58 offset:54528\n"
      "v_add_f32 v59, v59, v71\n"
      "v_add_f32 v60, v60, v72\n"
      "v_mfma_f32_16x16x32_bf16 v[112:115], a[36:39], a[192:195], v[112:115]\n"
      "buffer_store_dwordx4 a[236:239], v18, s[8:11], s59 offen offset:128 sc1\n"
      "ds_read_b128 a[208:211], v5 offset:50752\n"
      "v_mfma_f32_16x16x32_bf16 v[116:119], a[40:43], a[192:195], v[116:119]\n"
      "s_nop 0\n"
      "ds_write_addtid_b32 v59 offset:55040\n"
      "ds_write_addtid_b32 v60 offset:55552\n"
      "v_mfma_f32_16x16x32_bf16 v[120:123], a[44:47], a[192:195], v[120:123]\n"
      "v_add_f32 v61, v61, v73\n"
      "v_add_f32 v62, v62, v74\n"
      "v_add_f32 v63, v63, v75\n"
      "v_mfma_f32_16x16x32_bf16 v[112:115], a[48:51], a[196:199], v[112:115]\n"
      "s_nop 0\n"
      "ds_read_b128 a[212:215], v5 offset:50816\n"
      "ds_write_addtid_b32 v61 offset:56064\n"
      "v_mfma_f32_16x16x32_bf16 v[116:119], a[52:55], a[196:199], v[116:119]\n"
      "ds_write_addtid_b32 v62 offset:56576\n"
      "ds_write_addtid_b32 v63 offset:57088\n"
      "v_mfma_f32_16x16x32_bf16 v[120:123], a[56:59], a[196:199], v[120:123]\n"
      "s_nop 0\n"
      "buffer_store_dwordx4 a[240:243], v18, s[8:11], s59 offen offset:256 sc1\n"
      "v_fma_f32 v40, -2.0, v64, v52\n"
      "v_mfma_f32_16x16x32_bf16 v[112:115], a[60:63], a[200:203], v[112:115]\n"
      "s_nop 0\n"
      "v_fma_f32 v41, -2.0, v65, v53\n"
      "v_fma_f32 v42, -2.0, v66, v54\n"
      "v_mfma_f32_16x16x32_bf16 v[116:119], a[64:67], a[200:203], v[116:119]\n"
      "s_waitcnt vmcnt(16)\n"
      "v_fma_f32 v43, -2.0, v67, v55\n"
      "v_fma_f32 v44, -2.0, v68, v56\n"
      "v_mfma_f32_16x16x32_bf16 v[120:123], a[68:71], a[200:203], v[120:123]\n"
      "s_waitcnt lgkmcnt(0)\n"
      "v_fma_f32 v45, -2.0, v69, v57\n"
      "s_barrier\n"
      "v_mfma_f32_16x16x32_bf16 v[112:115], a[72:75], a[204:207], v[112:115]\n"
      "s_add_u32 s62, s62, 786432\n"
      "ds_read_b128 a[192:195], v0 offset:0\n"
      "ds_read2st64_b32 v[28:29], v6 offset0:24 offset1:26\n"
      "ds_read2st64_b32 v[30:31], v6 offset0:28 offset1:30\n"
      "v_mfma_f32_16x16x32_bf16 v[116:119], a[76:79], a[204:207], v[116:119]\n"
      "s_add_u32 s58, s62, 0\n"
      "ds_read_b128 a[196:199], v0 offset:4096\n"
      "ds_read2st64_b32 v[32:33], v6 offset0:32 offset1:34\n"
      "v_mfma_f32_16x16x32_bf16 v[120:123], a[80:83], a[204:207], v[120:123]\n"
      "s_add_u32 s59, s58, 196608\n"
      "ds_read2st64_b32 v[34:35], v6 offset0:36 offset1:38\n"
      "v_fma_f32 v46, -2.0, v70, v58\n"
      "v_mfma_f32_16x16x32_bf16 v[112:115], a[84:87], a[208:211], v[112:115]\n"
      "ds_read_b128 a[200:203], v0 offset:64\n"
      "v_fma_f32 v47, -2.0, v71, v59\n"
      "v_fma_f32 v48, -2.0, v72, v60\n"
      "v_mfma_f32_16x16x32_bf16 v[116:119], a[88:91], a[208:211], v[116:119]\n"
      "ds_read2st64_b32 a[220:221], v12 offset0:177 offset1:179\n"
      "v_fma_f32 v49, -2.0, v73, v61\n"
      "v_mfma_f32_16x16x32_bf16 v[120:123], a[92:95], a[208:211], v[120:123]\n"
      "ds_read2st64_b32 a[222:223], v15 offset0:177 offset1:179\n"
      "v_fma_f32 v50, -2.0, v74, v62\n"
      "v_mfma_f32_16x16x32_bf16 v[112:115], a[96:99], a[212:215], v[112:115]\n"
      "ds_read_b128 a[204:207], v0 offset:4160\n"
      "v_fma_f32 v51, -2.0, v75, v63\n"
      "v_mfma_f32_16x16x32_bf16 v[116:119], a[100:103], a[212:215], v[116:119]\n"
      "ds_read2st64_b32 v[36:37], v6 offset0:40 offset1:42\n"
      "v_mfma_f32_16x16x32_bf16 v[120:123], a[104:107], a[212:215], v[120:123]\n"
      "s_waitcnt lgkmcnt(6)\n"
      "ds_read2st64_b32 v[38:39], v6 offset0:44 offset1:46\n"
      "v_fmac_f32 v40, s46, v28\n"
      "v_fmac_f32 v41, s46, v29\n"
      "v_fmac_f32 v42, s46, v30\n"
      "v_mfma_f32_16x16x32_bf16 v[52:55], v[252:255], a[192:195], 0\n"
      "ds_read_b128 a[208:211], v0 offset:128\n"
      "v_fmac_f32_e64 v43, s46, v31\n"
      "v_fma_f32 v28, 0.5, v28, v88\n"
      "v_mfma_f32_16x16x32_bf16 v[56:59], v[244:247], a[192:195], 0\n"
      "s_add_u32 s54, s54, -193536\n"
      "ds_read_b64_tr_b16 a[244:245], v7 offset:19968\n"
      "ds_read_b64_tr_b16 a[246:247], v7 offset:19976\n"
      "v_mfma_f32_16x16x32_bf16 v[60:63], v[236:239], a[192:195], 0\n"
      "s_add_u32 m0, s37, 141448\n"
      "v_fma_f32 v29, 0.5, v29, v89\n"
      "v_fma_f32 v30, 0.5, v30, v90\n"
      "v_fma_f32 v31, 0.5, v31, v91\n"
      "v_mfma_f32_16x16x32_bf16 v[64:67], v[248:251], a[196:199], 0\n"
      "buffer_load_dwordx4 v10, s[4:7], s54 offen offset:0 nt lds\n"
      "buffer_load_dwordx4 v11, s[4:7], s54 offen offset:1024 nt lds\n"
      "ds_read_b128 a[212:215], v0 offset:4224\n"
      "v_mfma_f32_16x16x32_bf16 v[68:71], v[240:243], a[196:199], 0\n"
      "s_mov_b32 m0, s64\n"
      "v_fmac_f32 v40, s48, v112\n"
      "v_fmac_f32 v41, s48, v113\n"
      "v_mfma_f32_16x16x32_bf16 v[72:75], v[232:235], a[196:199], 0\n"
      "s_waitcnt lgkmcnt(4)\n"
      "v_fmac_f32 v42, s48, v114\n"
      "v_fmac_f32 v43, s48, v115\n"
      "v_fmac_f32 v28, s45, v40\n"
      "v_mfma_f32_16x16x32_bf16 v[52:55], v[228:231], a[200:203], v[52:55]\n"
      "s_nop 0\n"
      "ds_read_b128 a[192:195], v1 offset:0\n"
      "v_fmac_f32 v29, s45, v41\n"
      "v_mfma_f32_16x16x32_bf16 v[56:59], v[220:223], a[200:203], v[56:59]\n"
      "v_fmac_f32 v30, s45, v42\n"
      "v_fmac_f32 v31, s45, v43\n"
      "v_mfma_f32_16x16x32_bf16 v[60:63], v[212:215], a[200:203], v[60:63]\n"
      "s_waitcnt lgkmcnt(1)\n"
      "v_fma_f32 v40, s47, v40, -v112\n"
      "v_fma_f32 v41, s47, v41, -v113\n"
      "v_mfma_f32_4x4x4_16b_bf16 v[20:23], a[216:217], a[244:245], 0\n"
      "v_fma_f32 v42, s47, v42, -v114\n"
      "v_mfma_f32_4x4x4_16b_bf16 v[24:27], a[216:217], a[246:247], 0\n"
      "v_fma_f32 v43, s47, v43, -v115\n"
      "v_cvt_pk_bf16_f32 v28, v28, v29\n"
      "v_mfma_f32_16x16x32_bf16 v[64:67], v[224:227], a[204:207], v[64:67]\n"
      "buffer_store_dwordx4 a[220:223], v18, s[8:11], s58 offen offset:0 sc1\n"
      "ds_read_b128 a[196:199], v1 offset:4096\n"
      "v_mfma_f32_16x16x32_bf16 v[68:71], v[216:219], a[204:207], v[68:71]\n"
      "ds_write_addtid_b32 v28 offset:33792\n"
      "v_cvt_pk_bf16_f32 v20, v20, v24\n"
      "v_mfma_f32_16x16x32_bf16 v[72:75], v[208:211], a[204:207], v[72:75]\n"
      "ds_write_addtid_b32 v20 offset:19456\n"
      "v_cvt_pk_bf16_f32 v30, v30, v31\n"
      "v_mfma_f32_16x16x32_bf16 v[52:55], v[204:207], a[208:211], v[52:55]\n"
      "ds_read_b128 a[200:203], v1 offset:64\n"
      "ds_write_addtid_b32 v30 offset:34304\n"
      "v_cvt_pk_bf16_f32 v40, v40, v41\n"
      "v_cvt_pk_bf16_f32 v21, v21, v25\n"
      "v_mfma_f32_16x16x32_bf16 v[56:59], v[196:199], a[208:211], v[56:59]\n"
      "ds_write_addtid_b32 v40 offset:35840\n"
      "ds_write_addtid_b32 v21 offset:23552\n"
      "ds_read2st64_b32 a[224:225], v13 offset0:177 offset1:179\n"
      "v_mfma_f32_16x16x32_bf16 v[60:63], v[188:191], a[208:211], v[60:63]\n"
      "ds_read2st64_b32 a[226:227], v16 offset0:177 offset1:179\n"
      "v_cvt_pk_bf16_f32 v42, v42, v43\n"
      "v_fmac_f32 v44, s46, v32\n"
      "v_mfma_f32_16x16x32_bf16 v[64:67], v[200:203], a[212:215], v[64:67]\n"
      "ds_read_b128 a[204:207], v1 offset:4160\n"
      "ds_write_addtid_b32 v42 offset:36352\n"
      "v_mfma_f32_16x16x32_bf16 v[68:71], v[192:195], a[212:215], v[68:71]\n"
      "v_fmac_f32 v45, s46, v33\n"
      "v_cvt_pk_bf16_f32 v22, v22, v26\n"
      "v_mfma_f32_16x16x32_bf16 v[72:75], v[184:187], a[212:215], v[72:75]\n"
      "s_waitcnt lgkmcnt(4)\n"
      "ds_write_addtid_b32 v22 offset:27648\n"
      "v_fmac_f32 v46, s46, v34\n"
      "v_fmac_f32 v47, s46, v35\n"
      "v_mfma_f32_16x16x32_bf16 v[52:55], v[180:183], a[192:195], v[52:55]\n"
      "s_nop 0\n"
      "ds_read_b128 a[208:211], v1 offset:128\n"
      "v_cvt_pk_bf16_f32 v23, v23, v27\n"
      "v_mfma_f32_16x16x32_bf16 v[56:59], v[172:175], a[192:195], v[56:59]\n"
      "s_add_u32 s54, s54, 196608\n"
      "ds_write_addtid_b32 v23 offset:31744\n"
      "ds_read2st64_b32 a[228:229], v14 offset0:177 offset1:179\n"
      "v_mfma_f32_16x16x32_bf16 v[60:63], v[164:167], a[192:195], v[60:63]\n"
      "s_add_u32 m0, s37, 145792\n"
      "ds_read2st64_b32 a[230:231], v17 offset0:177 offset1:179\n"
      "v_fma_f32 v32, 0.5, v32, v92\n"
      "v_fma_f32 v33, 0.5, v33, v93\n"
      "v_mfma_f32_16x16x32_bf16 v[64:67], v[176:179], a[196:199], v[64:67]\n"
      "buffer_load_dwordx4 v10, s[4:7], s54 offen offset:0 nt lds\n"
      "buffer_load_dwordx4 v11, s[4:7], s54 offen offset:1024 nt lds\n"
      "ds_read_b128 a[212:215], v1 offset:4224\n"
      "v_mfma_f32_16x16x32_bf16 v[68:71], v[168:171], a[196:199], v[68:71]\n"
      "s_mov_b32 m0, s64\n"
      "v_fma_f32 v34, 0.5, v34, v94\n"
      "v_fma_f32 v35, 0.5, v35, v95\n"
      "v_mfma_f32_16x16x32_bf16 v[72:75], v[160:163], a[196:199], v[72:75]\n"
      "v_fmac_f32 v44, s48, v116\n"
      "v_fmac_f32 v45, s48, v117\n"
      "v_mfma_f32_16x16x32_bf16 v[52:55], v[156:159], a[200:203], v[52:55]\n"
      "ds_read_b128 a[192:195], v2 offset:0\n"
      "v_fmac_f32 v46, s48, v118\n"
      "v_mfma_f32_16x16x32_bf16 v[56:59], v[148:151], a[200:203], v[56:59]\n"
      "v_fmac_f32 v47, s48, v119\n"
      "v_fmac_f32 v32, s45, v44\n"
      "v_mfma_f32_16x16x32_bf16 v[60:63], v[140:143], a[200:203], v[60:63]\n"
      "s_waitcnt lgkmcnt(1)\n"
      "v_fmac_f32 v33, s45, v45\n"
      "v_fmac_f32_e64 v34, s45, v46\n"
      "v_mfma_f32_16x16x32_bf16 v[64:67], v[152:155], a[204:207], v[64:67]\n"
      "buffer_store_dwordx4 a[224:227], v18, s[8:11], s58 offen offset:128 sc1\n"
      "ds_read_b128 a[196:199], v2 offset:4096\n"
      "v_mfma_f32_16x16x32_bf16 v[68:71], v[144:147], a[204:207], v[68:71]\n"
      "v_fmac_f32 v35, s45, v47\n"
      "v_fma_f32 v44, s47, v44, -v116\n"
      "v_mfma_f32_16x16x32_bf16 v[72:75], v[136:139], a[204:207], v[72:75]\n"
      "s_nop 0\n"
      "ds_read_b64_tr_b16 a[248:249], v7 offset:20480\n"
      "ds_read_b64_tr_b16 a[250:251], v7 offset:20488\n"
      "v_fma_f32 v45, s47, v45, -v117\n"
      "v_fma_f32 v46, s47, v46, -v118\n"
      "v_mfma_f32_16x16x32_bf16 v[52:55], v[132:135], a[208:211], v[52:55]\n"
      "ds_read_b128 a[200:203], v2 offset:64\n"
      "v_fma_f32 v47, s47, v47, -v119\n"
      "v_mfma_f32_16x16x32_bf16 v[56:59], v[124:127], a[208:211], v[56:59]\n"
      "v_cvt_pk_bf16_f32 v32, v32, v33\n"
      "v_cvt_pk_bf16_f32 v34, v34, v35\n"
      "v_mfma_f32_16x16x32_bf16 v[60:63], a[184:187], a[208:211], v[60:63]\n"
      "ds_write_addtid_b32 v32 offset:41988\n"
      "ds_write_addtid_b32 v34 offset:42500\n"
      "v_cvt_pk_bf16_f32 v44, v44, v45\n"
      "v_mfma_f32_16x16x32_bf16 v[64:67], v[128:131], a[212:215], v[64:67]\n"
      "buffer_store_dwordx4 a[228:231], v18, s[8:11], s58 offen offset:256 sc1\n"
      "ds_read_b128 a[204:207], v2 offset:4160\n"
      "v_mfma_f32_16x16x32_bf16 v[68:71], a[188:191], a[212:215], v[68:71]\n"
      "ds_write_addtid_b32 v44 offset:44036\n"
      "ds_read2st64_b32 a[232:233], v12 offset0:0 offset1:2\n"
      "ds_read2st64_b32 a[234:235], v15 offset0:0 offset1:2\n"
      "v_cvt_pk_bf16_f32 v46, v46, v47\n"
      "v_mfma_f32_16x16x32_bf16 v[72:75], a[180:183], a[212:215], v[72:75]\n"
      "s_waitcnt lgkmcnt(5)\n"
      "ds_write_addtid_b32 v46 offset:44548\n"
      "v_fmac_f32 v48, s46, v36\n"
      "v_mfma_f32_16x16x32_bf16 v[52:55], a[176:179], a[192:195], v[52:55]\n"
      "ds_read_b128 a[208:211], v2 offset:128\n"
      "v_fmac_f32 v49, s46, v37\n"
      "v_fmac_f32 v50, s46, v38\n"
      "v_mfma_f32_4x4x4_16b_bf16 v[20:23], a[216:217], a[248:249], 0\n"
      "v_fmac_f32_e64 v51, s46, v39\n"
      "v_mfma_f32_4x4x4_16b_bf16 v[24:27], a[216:217], a[250:251], 0\n"
      "v_fma_f32 v36, 0.5, v36, v96\n"
      "v_fma_f32 v37, 0.5, v37, v97\n"
      "v_mfma_f32_16x16x32_bf16 v[56:59], a[168:171], a[192:195], v[56:59]\n"
      "s_add_u32 s54, s54, 193536\n"
      "v_fma_f32 v38, 0.5, v38, v98\n"
      "v_cvt_pk_bf16_f32 v20, v20, v24\n"
      "v_mfma_f32_16x16x32_bf16 v[60:63], a[160:163], a[192:195], v[60:63]\n"
      "s_add_u32 m0, s37, 82432\n"
      "v_fma_f32 v39, 0.5, v39, v99\n"
      "v_fmac_f32_e64 v48, s48, v120\n"
      "v_mfma_f32_16x16x32_bf16 v[64:67], a[172:175], a[196:199], v[64:67]\n"
      "buffer_load_dwordx4 v8, s[4:7], s54 offen offset:0 nt lds\n"
      "buffer_load_dwordx4 v9, s[4:7], s54 offen offset:1024 nt lds\n"
      "ds_read_b128 a[212:215], v2 offset:4224\n"
      "v_mfma_f32_16x16x32_bf16 v[68:71], a[164:167], a[196:199], v[68:71]\n"
      "s_mov_b32 m0, s64\n"
      "v_fmac_f32 v49, s48, v121\n"
      "v_cvt_pk_bf16_f32 v21, v21, v25\n"
      "v_mfma_f32_16x16x32_bf16 v[72:75], a[156:159], a[196:199], v[72:75]\n"
      "ds_write_addtid_b32 v20 offset:19968\n"
      "ds_write_addtid_b32 v21 offset:24064\n"
      "v_fmac_f32 v50, s48, v122\n"
      "v_mfma_f32_16x16x32_bf16 v[52:55], a[152:155], a[200:203], v[52:55]\n"
      "ds_read_b128 a[192:195], v3 offset:0\n"
      "v_fmac_f32 v51, s48, v123\n"
      "v_mfma_f32_16x16x32_bf16 v[56:59], a[144:147], a[200:203], v[56:59]\n"
      "v_fmac_f32_e64 v36, s45, v48\n"
      "v_cvt_pk_bf16_f32 v22, v22, v26\n"
      "v_mfma_f32_16x16x32_bf16 v[60:63], a[136:139], a[200:203], v[60:63]\n"
      "s_waitcnt lgkmcnt(3)\n"
      "ds_write_addtid_b32 v22 offset:28160\n"
      "v_fmac_f32 v37, s45, v49\n"
      "v_mfma_f32_16x16x32_bf16 v[64:67], a[148:151], a[204:207], v[64:67]\n"
      "ds_read_b128 a[196:199], v3 offset:64\n"
      "v_fmac_f32 v38, s45, v50\n"
      "v_mfma_f32_16x16x32_bf16 v[68:71], a[140:143], a[204:207], v[68:71]\n"
      "v_fmac_f32 v39, s45, v51\n"
      "v_cvt_pk_bf16_f32 v23, v23, v27\n"
      "v_mfma_f32_16x16x32_bf16 v[72:75], a[132:135], a[204:207], v[72:75]\n"
      "buffer_store_dwordx4 a[232:235], v18, s[8:11], s59 offen offset:0 sc1\n"
      "ds_write_addtid_b32 v23 offset:32256\n"
      "v_mfma_f32_16x16x32_bf16 v[52:55], a[128:131], a[208:211], v[52:55]\n"
      "ds_read_b128 a[200:203], v3 offset:128\n"
      "v_fma_f32 v48, s47, v48, -v120\n"
      "v_mfma_f32_16x16x32_bf16 v[56:59], a[120:123], a[208:211], v[56:59]\n"
      "ds_read2st64_b32 a[236:237], v13 offset0:0 offset1:2\n"
      "ds_read2st64_b32 a[238:239], v16 offset0:0 offset1:2\n"
      "v_fma_f32 v49, s47, v49, -v121\n"
      "v_mfma_f32_16x16x32_bf16 v[60:63], a[112:115], a[208:211], v[60:63]\n"
      "v_fma_f32 v50, s47, v50, -v122\n"
      "v_fma_f32 v51, s47, v51, -v123\n"
      "v_mfma_f32_16x16x32_bf16 v[64:67], a[124:127], a[212:215], v[64:67]\n"
      "ds_read_b128 a[204:207], v4 offset:0\n"
      "v_cvt_pk_bf16_f32 v36, v36, v37\n"
      "v_mfma_f32_16x16x32_bf16 v[68:71], a[116:119], a[212:215], v[68:71]\n"
      "ds_write_addtid_b32 v36 offset:37888\n"
      "v_cvt_pk_bf16_f32 v38, v38, v39\n"
      "v_mfma_f32_16x16x32_bf16 v[72:75], a[108:111], a[212:215], v[72:75]\n"
      "s_waitcnt lgkmcnt(4)\n"
      "ds_write_addtid_b32 v38 offset:38400\n"
      "v_add_f32 v52, v52, v64\n"
      "v_add_f32_e64 v53, v53, v65\n"
      "v_cvt_pk_bf16_f32 v48, v48, v49\n"
      "v_mfma_f32_16x16x32_bf16 v[76:79], a[0:3], a[192:195], 0\n"
      "ds_read_b128 a[208:211], v4 offset:64\n"
      "ds_write_addtid_b32 v48 offset:39936\n"
      "ds_write_addtid_b32 v52 offset:45312\n"
      "v_add_f32_e64 v54, v54, v66\n"
      "v_mfma_f32_16x16x32_bf16 v[80:83], a[4:7], a[192:195], 0\n"
      "s_add_u32 s54, s54, 196608\n"
      "ds_read2st64_b32 a[240:241], v14 offset0:0 offset1:2\n"
      "ds_write_addtid_b32 v53 offset:45824\n"
      "v_mfma_f32_16x16x32_bf16 v[84:87], a[8:11], a[192:195], 0\n"
      "s_add_u32 m0, s37, 86536\n"
      "ds_read2st64_b32 a[242:243], v17 offset0:0 offset1:2\n"
      "v_add_f32 v55, v55, v67\n"
      "v_mfma_f32_16x16x32_bf16 v[76:79], a[12:15], a[196:199], v[76:79]\n"
      "s_nop 0\n"
      "buffer_load_dwordx4 v8, s[4:7], s54 offen offset:0 nt lds\n"
      "buffer_load_dwordx4 v9, s[4:7], s54 offen offset:1024 nt lds\n"
      "ds_read_b128 a[212:215], v4 offset:128\n"
      "v_add_f32 v56, v56, v68\n"
      "v_mfma_f32_16x16x32_bf16 v[80:83], a[16:19], a[196:199], v[80:83]\n"
      "s_mov_b32 m0, s64\n"
      "v_add_f32 v57, v57, v69\n"
      "v_cvt_pk_bf16_f32 v50, v50, v51\n"
      "v_mfma_f32_16x16x32_bf16 v[84:87], a[20:23], a[196:199], v[84:87]\n"
      "s_nop 0\n"
      "ds_write_addtid_b32 v54 offset:46336\n"
      "ds_write_addtid_b32 v50 offset:40448\n"
      "ds_write_addtid_b32 v55 offset:46848\n"
      "v_mfma_f32_16x16x32_bf16 v[76:79], a[24:27], a[200:203], v[76:79]\n"
      "ds_read_b128 a[192:195], v5 offset:0\n"
      "ds_write_addtid_b32 v56 offset:47360\n"
      "v_mfma_f32_16x16x32_bf16 v[80:83], a[28:31], a[200:203], v[80:83]\n"
      "ds_write_addtid_b32 v57 offset:47872\n"
      "v_add_f32 v58, v58, v70\n"
      "v_mfma_f32_16x16x32_bf16 v[84:87], a[32:35], a[200:203], v[84:87]\n"
      "s_waitcnt lgkmcnt(6)\n"
      "ds_write_addtid_b32 v58 offset:48384\n"
      "v_add_f32 v59, v59, v71\n"
      "v_add_f32 v60, v60, v72\n"
      "v_mfma_f32_16x16x32_bf16 v[76:79], a[36:39], a[204:207], v[76:79]\n"
      "buffer_store_dwordx4 a[236:239], v18, s[8:11], s59 offen offset:128 sc1\n"
      "ds_read_b128 a[196:199], v5 offset:64\n"
      "v_mfma_f32_16x16x32_bf16 v[80:83], a[40:43], a[204:207], v[80:83]\n"
      "ds_write_addtid_b32 v59 offset:48896\n"
      "ds_write_addtid_b32 v60 offset:49408\n"
      "v_mfma_f32_16x16x32_bf16 v[84:87], a[44:47], a[204:207], v[84:87]\n"
      "v_add_f32 v61, v61, v73\n"
      "v_add_f32 v62, v62, v74\n"
      "v_add_f32 v63, v63, v75\n"
      "v_mfma_f32_16x16x32_bf16 v[76:79], a[48:51], a[208:211], v[76:79]\n"
      "ds_read_b128 a[200:203], v5 offset:128\n"
      "ds_write_addtid_b32 v61 offset:49920\n"
      "v_mfma_f32_16x16x32_bf16 v[80:83], a[52:55], a[208:211], v[80:83]\n"
      "s_nop 0\n"
      "ds_write_addtid_b32 v62 offset:50432\n"
      "ds_write_addtid_b32 v63 offset:50944\n"
      "v_mfma_f32_16x16x32_bf16 v[84:87], a[56:59], a[208:211], v[84:87]\n"
      "buffer_store_dwordx4 a[240:243], v18, s[8:11], s59 offen offset:256 sc1\n"
      "v_fma_f32 v40, -2.0, v64, v52\n"
      "v_mfma_f32_16x16x32_bf16 v[76:79], a[60:63], a[212:215], v[76:79]\n"
      "v_fma_f32 v41, -2.0, v65, v53\n"
      "v_fma_f32 v42, -2.0, v66, v54\n"
      "v_mfma_f32_16x16x32_bf16 v[80:83], a[64:67], a[212:215], v[80:83]\n"
      "s_waitcnt vmcnt(16)\n"
      "v_fma_f32 v43, -2.0, v67, v55\n"
      "v_fma_f32 v44, -2.0, v68, v56\n"
      "v_mfma_f32_16x16x32_bf16 v[84:87], a[68:71], a[212:215], v[84:87]\n"
      "s_waitcnt lgkmcnt(0)\n"
      "v_fma_f32 v45, -2.0, v69, v57\n"
      "s_barrier\n"
      "s_add_u32 s55, s55, 2\n"
      "s_cmp_lt_u32 s55, 6\n"
      "s_cbranch_scc1 LDloop_%=\n"
      "LDlast_%=:\n"
      "v_mfma_f32_16x16x32_bf16 v[76:79], a[72:75], a[192:195], v[76:79]\n"
      "s_add_u32 s58, s58, 3072\n"
      "ds_read_b128 a[204:207], v0 offset:16384\n"
      "ds_read2st64_b32 v[28:29], v6 offset0:0 offset1:2\n"
      "v_fma_f32 v46, -2.0, v70, v58\n"
      "v_mfma_f32_16x16x32_bf16 v[80:83], a[76:79], a[192:195], v[80:83]\n"
      "s_add_u32 s59, s58, 196608\n"
      "ds_read_b128 a[208:211], v0 offset:20480\n"
      "ds_read2st64_b32 v[30:31], v6 offset0:4 offset1:6\n"
      "v_mfma_f32_16x16x32_bf16 v[84:87], a[80:83], a[192:195], v[84:87]\n"
      "ds_read2st64_b32 v[32:33], v6 offset0:8 offset1:10\n"
      "ds_read2st64_b32 v[34:35], v6 offset0:12 offset1:14\n"
      "v_mfma_f32_16x16x32_bf16 v[76:79], a[84:87], a[196:199], v[76:79]\n"
      "ds_read_b128 a[212:215], v0 offset:16448\n"
      "v_fma_f32 v47, -2.0, v71, v59\n"
      "v_mfma_f32_16x16x32_bf16 v[80:83], a[88:91], a[196:199], v[80:83]\n"
      "ds_read2st64_b32 v[36:37], v6 offset0:16 offset1:18\n"
      "v_fma_f32 v48, -2.0, v72, v60\n"
      "v_fma_f32 v49, -2.0, v73, v61\n"
      "v_mfma_f32_16x16x32_bf16 v[84:87], a[92:95], a[196:199], v[84:87]\n"
      "ds_read2st64_b32 v[38:39], v6 offset0:20 offset1:22\n"
      "v_fma_f32 v50, -2.0, v74, v62\n"
      "v_mfma_f32_16x16x32_bf16 v[76:79], a[96:99], a[200:203], v[76:79]\n"
      "ds_read_b128 a[192:195], v0 offset:20544\n"
      "v_fma_f32 v51, -2.0, v75, v63\n"
      "v_mfma_f32_16x16x32_bf16 v[80:83], a[100:103], a[200:203], v[80:83]\n"
      "v_mfma_f32_16x16x32_bf16 v[84:87], a[104:107], a[200:203], v[84:87]\n"
      "s_waitcnt lgkmcnt(6)\n"
      "ds_read_b64_tr_b16 a[244:245], v7 offset:36864\n"
      "ds_read_b64_tr_b16 a[246:247], v7 offset:36872\n"
      "v_fmac_f32 v40, s46, v28\n"
      "v_fmac_f32 v41, s46, v29\n"
      "v_fmac_f32 v42, s46, v30\n"
      "v_mfma_f32_16x16x32_bf16 v[52:55], v[252:255], a[204:207], 0\n"
      "ds_read_b128 a[196:199], v0 offset:16512\n"
      "v_fmac_f32 v43, s46, v31\n"
      "v_mfma_f32_16x16x32_bf16 v[56:59], v[244:247], a[204:207], 0\n"
      "v_fma_f32 v28, 0.5, v28, v100\n"
      "v_fma_f32 v29, 0.5, v29, v101\n"
      "v_mfma_f32_16x16x32_bf16 v[60:63], v[236:239], a[204:207], 0\n"
      "v_fma_f32 v30, 0.5, v30, v102\n"
      "v_fma_f32 v31, 0.5, v31, v103\n"
      "v_fmac_f32 v40, s48, v76\n"
      "v_mfma_f32_16x16x32_bf16 v[64:67], v[248:251], a[208:211], 0\n"
      "ds_read_b128 a[200:203], v0 offset:20608\n"
      "v_fmac_f32 v41, s48, v77\n"
      "v_mfma_f32_16x16x32_bf16 v[68:71], v[240:243], a[208:211], 0\n"
      "v_fmac_f32 v42, s48, v78\n"
      "v_fmac_f32 v43, s48, v79\n"
      "v_mfma_f32_16x16x32_bf16 v[72:75], v[232:235], a[208:211], 0\n"
      "s_waitcnt lgkmcnt(2)\n"
      "v_fmac_f32 v28, s45, v40\n"
      "v_fmac_f32 v29, s45, v41\n"
      "v_mfma_f32_16x16x32_bf16 v[52:55], v[228:231], a[212:215], v[52:55]\n"
      "ds_read_b128 a[204:207], v1 offset:16384\n"
      "v_fmac_f32 v30, s45, v42\n"
      "v_mfma_f32_4x4x4_16b_bf16 v[20:23], a[216:217], a[244:245], 0\n"
      "v_fmac_f32 v31, s45, v43\n"
      "v_mfma_f32_4x4x4_16b_bf16 v[24:27], a[216:217], a[246:247], 0\n"
      "v_fma_f32 v40, s47, v40, -v76\n"
      "v_fma_f32 v41, s47, v41, -v77\n"
      "v_mfma_f32_16x16x32_bf16 v[56:59], v[220:223], a[212:215], v[56:59]\n"
      "v_fma_f32 v42, s47, v42, -v78\n"
      "v_fma_f32 v43, s47, v43, -v79\n"
      "v_cvt_pk_bf16_f32 v20, v20, v24\n"
      "v_mfma_f32_16x16x32_bf16 v[60:63], v[212:215], a[212:215], v[60:63]\n"
      "ds_write_addtid_b32 v20 offset:3072\n"
      "v_cvt_pk_bf16_f32 v21, v21, v25\n"
      "v_mfma_f32_16x16x32_bf16 v[64:67], v[224:227], a[192:195], v[64:67]\n"
      "ds_read_b128 a[208:211], v1 offset:20480\n"
      "ds_write_addtid_b32 v21 offset:7168\n"
      "v_mfma_f32_16x16x32_bf16 v[68:71], v[216:219], a[192:195], v[68:71]\n"
      "v_cvt_pk_bf16_f32 v28, v28, v29\n"
      "v_cvt_pk_bf16_f32 v30, v30, v31\n"
      "v_mfma_f32_16x16x32_bf16 v[72:75], v[208:211], a[192:195], v[72:75]\n"
      "s_waitcnt lgkmcnt(3)\n"
      "ds_write_addtid_b32 v28 offset:32768\n"
      "ds_write_addtid_b32 v30 offset:33280\n"
      "v_cvt_pk_bf16_f32 v40, v40, v41\n"
      "v_mfma_f32_16x16x32_bf16 v[52:55], v[204:207], a[196:199], v[52:55]\n"
      "s_nop 0\n"
      "ds_read_b128 a[212:215], v1 offset:16448\n"
      "ds_write_addtid_b32 v40 offset:34816\n"
      "v_mfma_f32_16x16x32_bf16 v[56:59], v[196:199], a[196:199], v[56:59]\n"
      "ds_read2st64_b32 a[220:221], v12 offset0:181 offset1:183\n"
      "ds_read2st64_b32 a[222:223], v15 offset0:181 offset1:183\n"
      "v_cvt_pk_bf16_f32 v42, v42, v43\n"
      "v_mfma_f32_16x16x32_bf16 v[60:63], v[188:191], a[196:199], v[60:63]\n"
      "ds_write_addtid_b32 v42 offset:35328\n"
      "v_fmac_f32 v44, s46, v32\n"
      "v_mfma_f32_16x16x32_bf16 v[64:67], v[200:203], a[200:203], v[64:67]\n"
      "ds_read_b128 a[192:195], v1 offset:20544\n"
      "v_cvt_pk_bf16_f32 v22, v22, v26\n"
      "v_mfma_f32_16x16x32_bf16 v[68:71], v[192:195], a[200:203], v[68:71]\n"
      "ds_write_addtid_b32 v22 offset:11264\n"
      "v_fmac_f32 v45, s46, v33\n"
      "v_fmac_f32 v46, s46, v34\n"
      "v_mfma_f32_16x16x32_bf16 v[72:75], v[184:187], a[200:203], v[72:75]\n"
      "v_fmac_f32 v47, s46, v35\n"
      "v_cvt_pk_bf16_f32 v23, v23, v27\n"
      "v_mfma_f32_16x16x32_bf16 v[52:55], v[180:183], a[204:207], v[52:55]\n"
      "ds_read_b128 a[196:199], v1 offset:16512\n"
      "ds_write_addtid_b32 v23 offset:15360\n"
      "v_mfma_f32_16x16x32_bf16 v[56:59], v[172:175], a[204:207], v[56:59]\n"
      "ds_read2st64_b32 a[224:225], v13 offset0:181 offset1:183\n"
      "ds_read2st64_b32 a[226:227], v16 offset0:181 offset1:183\n"
      "v_fma_f32 v32, 0.5, v32, v104\n"
      "v_mfma_f32_16x16x32_bf16 v[60:63], v[164:167], a[204:207], v[60:63]\n"
      "s_waitcnt lgkmcnt(5)\n"
      "v_fma_f32 v33, 0.5, v33, v105\n"
      "v_fma_f32 v34, 0.5, v34, v106\n"
      "v_mfma_f32_16x16x32_bf16 v[64:67], v[176:179], a[208:211], v[64:67]\n"
      "ds_read_b128 a[200:203], v1 offset:20608\n"
      "v_fma_f32 v35, 0.5, v35, v107\n"
      "v_mfma_f32_16x16x32_bf16 v[68:71], v[168:171], a[208:211], v[68:71]\n"
      "v_fmac_f32 v44, s48, v80\n"
      "v_fmac_f32 v45, s48, v81\n"
      "v_mfma_f32_16x16x32_bf16 v[72:75], v[160:163], a[208:211], v[72:75]\n"
      "v_fmac_f32 v46, s48, v82\n"
      "v_fmac_f32 v47, s48, v83\n"
      "v_mfma_f32_16x16x32_bf16 v[52:55], v[156:159], a[212:215], v[52:55]\n"
      "ds_read_b128 a[204:207], v2 offset:16384\n"
      "v_fmac_f32 v32, s45, v44\n"
      "v_mfma_f32_16x16x32_bf16 v[56:59], v[148:151], a[212:215], v[56:59]\n"
      "v_fmac_f32 v33, s45, v45\n"
      "v_fmac_f32 v34, s45, v46\n"
      "v_mfma_f32_16x16x32_bf16 v[60:63], v[140:143], a[212:215], v[60:63]\n"
      "v_fmac_f32 v35, s45, v47\n"
      "v_fma_f32 v44, s47, v44, -v80\n"
      "v_mfma_f32_16x16x32_bf16 v[64:67], v[152:155], a[192:195], v[64:67]\n"
      "buffer_store_dwordx4 a[220:223], v18, s[8:11], s58 offen offset:0 sc1\n"
      "ds_read_b128 a[208:211], v2 offset:20480\n"
      "v_mfma_f32_16x16x32_bf16 v[68:71], v[144:147], a[192:195], v[68:71]\n"
      "s_nop 0\n"
      "ds_read_b64_tr_b16 a[248:249], v7 offset:37376\n"
      "ds_read_b64_tr_b16 a[250:251], v7 offset:37384\n"
      "v_fma_f32 v45, s47, v45, -v81\n"
      "v_mfma_f32_16x16x32_bf16 v[72:75], v[136:139], a[192:195], v[72:75]\n"
      "s_waitcnt lgkmcnt(3)\n"
      "v_fma_f32 v46, s47, v46, -v82\n"
      "v_fma_f32 v47, s47, v47, -v83\n"
      "v_mfma_f32_16x16x32_bf16 v[52:55], v[132:135], a[196:199], v[52:55]\n"
      "s_nop 0\n"
      "ds_read_b128 a[212:215], v2 offset:16448\n"
      "v_cvt_pk_bf16_f32 v32, v32, v33\n"
      "v_mfma_f32_16x16x32_bf16 v[56:59], v[124:127], a[196:199], v[56:59]\n"
      "ds_write_addtid_b32 v32 offset:40964\n"
      "v_cvt_pk_bf16_f32 v34, v34, v35\n"
      "v_mfma_f32_16x16x32_bf16 v[60:63], a[184:187], a[196:199], v[60:63]\n"
      "ds_write_addtid_b32 v34 offset:41476\n"
      "v_cvt_pk_bf16_f32 v44, v44, v45\n"
      "v_cvt_pk_bf16_f32 v46, v46, v47\n"
      "v_mfma_f32_16x16x32_bf16 v[64:67], v[128:131], a[200:203], v[64:67]\n"
      "buffer_store_dwordx4 a[224:227], v18, s[8:11], s58 offen offset:128 sc1\n"
      "ds_read_b128 a[192:195], v2 offset:20544\n"
      "v_mfma_f32_16x16x32_bf16 v[68:71], a[188:191], a[200:203], v[68:71]\n"
      "s_waitcnt lgkmcnt(2)\n"
      "ds_write_addtid_b32 v44 offset:43012\n"
      "ds_write_addtid_b32 v46 offset:43524\n"
      "v_fmac_f32 v48, s46, v36\n"
      "v_mfma_f32_4x4x4_16b_bf16 v[20:23], a[216:217], a[248:249], 0\n"
      "v_fmac_f32_e64 v49, s46, v37\n"
      "v_mfma_f32_4x4x4_16b_bf16 v[24:27], a[216:217], a[250:251], 0\n"
      "ds_read2st64_b32 a[228:229], v14 offset0:181 offset1:183\n"
      "ds_read2st64_b32 a[230:231], v17 offset0:181 offset1:183\n"
      "v_fmac_f32 v50, s46, v38\n"
      "v_mfma_f32_16x16x32_bf16 v[72:75], a[180:183], a[200:203], v[72:75]\n"
      "v_fmac_f32 v51, s46, v39\n"
      "v_fma_f32 v36, 0.5, v36, v108\n"
      "v_cvt_pk_bf16_f32 v20, v20, v24\n"
      "v_mfma_f32_16x16x32_bf16 v[52:55], a[176:179], a[204:207], v[52:55]\n"
      "ds_read_b128 a[196:199], v2 offset:16512\n"
      "ds_write_addtid_b32 v20 offset:3584\n"
      "v_mfma_f32_16x16x32_bf16 v[56:59], a[168:171], a[204:207], v[56:59]\n"
      "v_fma_f32 v37, 0.5, v37, v109\n"
      "v_fma_f32 v38, 0.5, v38, v110\n"
      "v_mfma_f32_16x16x32_bf16 v[60:63], a[160:163], a[204:207], v[60:63]\n"
      "v_fma_f32 v39, 0.5, v39, v111\n"
      "v_cvt_pk_bf16_f32 v21, v21, v25\n"
      "v_mfma_f32_16x16x32_bf16 v[64:67], a[172:175], a[208:211], v[64:67]\n"
      "ds_read_b128 a[200:203], v2 offset:20608\n"
      "ds_write_addtid_b32 v21 offset:7680\n"
      "v_fmac_f32 v48, s48, v84\n"
      "v_mfma_f32_16x16x32_bf16 v[68:71], a[164:167], a[208:211], v[68:71]\n"
      "v_fmac_f32 v49, s48, v85\n"
      "v_cvt_pk_bf16_f32 v22, v22, v26\n"
      "v_mfma_f32_16x16x32_bf16 v[72:75], a[156:159], a[208:211], v[72:75]\n"
      "ds_write_addtid_b32 v22 offset:11776\n"
      "v_fmac_f32 v50, s48, v86\n"
      "v_mfma_f32_16x16x32_bf16 v[52:55], a[152:155], a[212:215], v[52:55]\n"
      "ds_read_b128 a[204:207], v3 offset:16896\n"
      "v_fmac_f32 v51, s48, v87\n"
      "v_mfma_f32_16x16x32_bf16 v[56:59], a[144:147], a[212:215], v[56:59]\n"
      "v_fmac_f32 v36, s45, v48\n"
      "v_cvt_pk_bf16_f32 v23, v23, v27\n"
      "v_mfma_f32_16x16x32_bf16 v[60:63], a[136:139], a[212:215], v[60:63]\n"
      "s_waitcnt lgkmcnt(2)\n"
      "ds_write_addtid_b32 v23 offset:15872\n"
      "v_fmac_f32 v37, s45, v49\n"
      "v_mfma_f32_16x16x32_bf16 v[64:67], a[148:151], a[192:195], v[64:67]\n"
      "ds_read_b128 a[208:211], v3 offset:16960\n"
      "v_fmac_f32 v38, s45, v50\n"
      "v_mfma_f32_16x16x32_bf16 v[68:71], a[140:143], a[192:195], v[68:71]\n"
      "v_fmac_f32 v39, s45, v51\n"
      "v_fma_f32 v48, s47, v48, -v84\n"
      "v_mfma_f32_16x16x32_bf16 v[72:75], a[132:135], a[192:195], v[72:75]\n"
      "s_nop 0\n"
      "buffer_store_dwordx4 a[228:231], v18, s[8:11], s58 offen offset:256 sc1\n"
      "v_mfma_f32_16x16x32_bf16 v[52:55], a[128:131], a[196:199], v[52:55]\n"
      "ds_read_b128 a[212:215], v3 offset:17024\n"
      "v_fma_f32 v49, s47, v49, -v85\n"
      "v_mfma_f32_16x16x32_bf16 v[56:59], a[120:123], a[196:199], v[56:59]\n"
      "ds_read2st64_b32 a[232:233], v12 offset0:4 offset1:6\n"
      "ds_read2st64_b32 a[234:235], v15 offset0:4 offset1:6\n"
      "v_fma_f32 v50, s47, v50, -v86\n"
      "v_mfma_f32_16x16x32_bf16 v[60:63], a[112:115], a[196:199], v[60:63]\n"
      "v_fma_f32 v51, s47, v51, -v87\n"
      "v_cvt_pk_bf16_f32 v36, v36, v37\n"
      "v_mfma_f32_16x16x32_bf16 v[64:67], a[124:127], a[200:203], v[64:67]\n"
      "ds_read_b128 a[192:195], v4 offset:16896\n"
      "ds_write_addtid_b32 v36 offset:36864\n"
      "v_mfma_f32_16x16x32_bf16 v[68:71], a[116:119], a[200:203], v[68:71]\n"
      "v_cvt_pk_bf16_f32 v38, v38, v39\n"
      "v_cvt_pk_bf16_f32 v48, v48, v49\n"
      "v_mfma_f32_16x16x32_bf16 v[72:75], a[108:111], a[200:203], v[72:75]\n"
      "s_waitcnt lgkmcnt(4)\n"
      "ds_write_addtid_b32 v38 offset:37376\n"
      "ds_write_addtid_b32 v48 offset:38912\n"
      "v_add_f32 v52, v52, v64\n"
      "v_add_f32 v53, v53, v65\n"
      "v_mfma_f32_16x16x32_bf16 v[88:91], a[0:3], a[204:207], 0\n"
      "ds_read_b128 a[196:199], v4 offset:16960\n"
      "ds_write_addtid_b32 v52 offset:51456\n"
      "v_add_f32 v54, v54, v66\n"
      "v_mfma_f32_16x16x32_bf16 v[92:95], a[4:7], a[204:207], 0\n"
      "ds_read2st64_b32 a[236:237], v13 offset0:4 offset1:6\n"
      "ds_write_addtid_b32 v53 offset:51968\n"
      "v_mfma_f32_16x16x32_bf16 v[96:99], a[8:11], a[204:207], 0\n"
      "ds_write_addtid_b32 v54 offset:52480\n"
      "ds_read2st64_b32 a[238:239], v16 offset0:4 offset1:6\n"
      "v_mfma_f32_16x16x32_bf16 v[88:91], a[12:15], a[208:211], v[88:91]\n"
      "ds_read_b128 a[200:203], v4 offset:17024\n"
      "v_add_f32 v55, v55, v67\n"
      "v_add_f32 v56, v56, v68\n"
      "v_mfma_f32_16x16x32_bf16 v[92:95], a[16:19], a[208:211], v[92:95]\n"
      "ds_write_addtid_b32 v55 offset:52992\n"
      "ds_write_addtid_b32 v56 offset:53504\n"
      "v_mfma_f32_16x16x32_bf16 v[96:99], a[20:23], a[208:211], v[96:99]\n"
      "v_add_f32 v57, v57, v69\n"
      "v_add_f32 v58, v58, v70\n"
      "v_mfma_f32_16x16x32_bf16 v[88:91], a[24:27], a[212:215], v[88:91]\n"
      "ds_read_b128 a[204:207], v5 offset:16896\n"
      "ds_write_addtid_b32 v57 offset:54016\n"
      "v_mfma_f32_16x16x32_bf16 v[92:95], a[28:31], a[212:215], v[92:95]\n"
      "s_nop 0\n"
      "ds_write_addtid_b32 v58 offset:54528\n"
      "v_add_f32_e64 v59, v59, v71\n"
      "v_cvt_pk_bf16_f32 v50, v50, v51\n"
      "v_mfma_f32_16x16x32_bf16 v[96:99], a[32:35], a[212:215], v[96:99]\n"
      "s_waitcnt lgkmcnt(5)\n"
      "ds_write_addtid_b32 v50 offset:39424\n"
      "ds_write_addtid_b32 v59 offset:55040\n"
      "v_mfma_f32_16x16x32_bf16 v[88:91], a[36:39], a[192:195], v[88:91]\n"
      "s_nop 0\n"
      "buffer_store_dwordx4 a[232:235], v18, s[8:11], s59 offen offset:0 sc1\n"
      "ds_read_b128 a[208:211], v5 offset:16960\n"
      "v_mfma_f32_16x16x32_bf16 v[92:95], a[40:43], a[192:195], v[92:95]\n"
      "s_nop 0\n"
      "v_add_f32_e64 v60, v60, v72\n"
      "v_add_f32_e64 v61, v61, v73\n"
      "v_add_f32_e64 v62, v62, v74\n"
      "v_mfma_f32_16x16x32_bf16 v[96:99], a[44:47], a[192:195], v[96:99]\n"
      "s_nop 0\n"
      "ds_write_addtid_b32 v60 offset:55552\n"
      "ds_write_addtid_b32 v61 offset:56064\n"
      "v_add_f32_e64 v63, v63, v75\n"
      "v_mfma_f32_16x16x32_bf16 v[88:91], a[48:51], a[196:199], v[88:91]\n"
      "s_nop 0\n"
      "ds_read_b128 a[212:215], v5 offset:17024\n"
      "ds_write_addtid_b32 v62 offset:56576\n"
      "v_mfma_f32_16x16x32_bf16 v[92:95], a[52:55], a[196:199], v[92:95]\n"
      "ds_write_addtid_b32 v63 offset:57088\n"
      "v_fma_f32 v40, -2.0, v64, v52\n"
      "v_mfma_f32_16x16x32_bf16 v[96:99], a[56:59], a[196:199], v[96:99]\n"
      "s_nop 0\n"
      "buffer_store_dwordx4 a[236:239], v18, s[8:11], s59 offen offset:128 sc1\n"
      "v_mfma_f32_16x16x32_bf16 v[88:91], a[60:63], a[200:203], v[88:91]\n"
      "s_nop 0\n"
      "v_fma_f32 v41, -2.0, v65, v53\n"
      "v_fma_f32 v42, -2.0, v66, v54\n"
      "v_mfma_f32_16x16x32_bf16 v[92:95], a[64:67], a[200:203], v[92:95]\n"
      "s_waitcnt vmcnt(14)\n"
      "v_fma_f32 v43, -2.0, v67, v55\n"
      "v_fma_f32 v44, -2.0, v68, v56\n"
      "v_mfma_f32_16x16x32_bf16 v[96:99], a[68:71], a[200:203], v[96:99]\n"
      "s_waitcnt lgkmcnt(0)\n"
      "v_fma_f32 v45, -2.0, v69, v57\n"
      "s_barrier\n"
      "v_mfma_f32_16x16x32_bf16 v[88:91], a[72:75], a[204:207], v[88:91]\n"
      "s_add_u32 s62, s62, 786432\n"
      "ds_read_b128 a[192:195], v0 offset:0\n"
      "ds_read2st64_b32 v[28:29], v6 offset0:24 offset1:26\n"
      "v_fma_f32 v46, -2.0, v70, v58\n"
      "v_mfma_f32_16x16x32_bf16 v[92:95], a[76:79], a[204:207], v[92:95]\n"
      "s_add_u32 s58, s62, 0\n"
      "ds_read_b128 a[196:199], v0 offset:4096\n"
      "ds_read2st64_b32 v[30:31], v6 offset0:28 offset1:30\n"
      "v_mfma_f32_16x16x32_bf16 v[96:99], a[80:83], a[204:207], v[96:99]\n"
      "s_add_u32 s59, s58, 196608\n"
      "ds_read2st64_b32 v[32:33], v6 offset0:32 offset1:34\n"
      "ds_read2st64_b32 v[34:35], v6 offset0:36 offset1:38\n"
      "v_mfma_f32_16x16x32_bf16 v[88:91], a[84:87], a[208:211], v[88:91]\n"
      "ds_read_b128 a[200:203], v0 offset:64\n"
      "v_fma_f32 v47, -2.0, v71, v59\n"
      "v_mfma_f32_16x16x32_bf16 v[92:95], a[88:91], a[208:211], v[92:95]\n"
      "ds_read2st64_b32 v[36:37], v6 offset0:40 offset1:42\n"
      "v_fma_f32 v48, -2.0, v72, v60\n"
      "v_fma_f32 v49, -2.0, v73, v61\n"
      "v_mfma_f32_16x16x32_bf16 v[96:99], a[92:95], a[208:211], v[96:99]\n"
      "ds_read2st64_b32 v[38:39], v6 offset0:44 offset1:46\n"
      "v_fma_f32 v50, -2.0, v74, v62\n"
      "v_mfma_f32_16x16x32_bf16 v[88:91], a[96:99], a[212:215], v[88:91]\n"
      "ds_read_b128 a[204:207], v0 offset:4160\n"
      "v_fma_f32 v51, -2.0, v75, v63\n"
      "v_mfma_f32_16x16x32_bf16 v[92:95], a[100:103], a[212:215], v[92:95]\n"
      "v_mfma_f32_16x16x32_bf16 v[96:99], a[104:107], a[212:215], v[96:99]\n"
      "s_waitcnt lgkmcnt(6)\n"
      "ds_read_b64_tr_b16 a[244:245], v7 offset:53760\n"
      "ds_read_b64_tr_b16 a[246:247], v7 offset:53768\n"
      "v_fmac_f32 v40, s46, v28\n"
      "v_fmac_f32 v41, s46, v29\n"
      "v_fmac_f32 v42, s46, v30\n"
      "v_mfma_f32_16x16x32_bf16 v[52:55], v[252:255], a[192:195], 0\n"
      "ds_read_b128 a[208:211], v0 offset:128\n"
      "v_fmac_f32 v43, s46, v31\n"
      "v_mfma_f32_16x16x32_bf16 v[56:59], v[244:247], a[192:195], 0\n"
      "v_fma_f32 v28, 0.5, v28, v112\n"
      "v_fma_f32 v29, 0.5, v29, v113\n"
      "v_mfma_f32_16x16x32_bf16 v[60:63], v[236:239], a[192:195], 0\n"
      "s_nop 0\n"
      "v_fma_f32 v30, 0.5, v30, v114\n"
      "v_fma_f32 v31, 0.5, v31, v115\n"
      "v_fmac_f32 v40, s48, v88\n"
      "v_mfma_f32_16x16x32_bf16 v[64:67], v[248:251], a[196:199], 0\n"
      "ds_read_b128 a[212:215], v0 offset:4224\n"
      "v_fmac_f32 v41, s48, v89\n"
      "v_mfma_f32_16x16x32_bf16 v[68:71], v[240:243], a[196:199], 0\n"
      "v_fmac_f32 v42, s48, v90\n"
      "v_fmac_f32 v43, s48, v91\n"
      "v_mfma_f32_16x16x32_bf16 v[72:75], v[232:235], a[196:199], 0\n"
      "s_waitcnt lgkmcnt(2)\n"
      "v_fmac_f32 v28, s45, v40\n"
      "v_fmac_f32 v29, s45, v41\n"
      "v_mfma_f32_16x16x32_bf16 v[52:55], v[228:231], a[200:203], v[52:55]\n"
      "ds_read_b128 a[192:195], v1 offset:0\n"
      "v_fmac_f32 v30, s45, v42\n"
      "v_mfma_f32_4x4x4_16b_bf16 v[20:23], a[216:217], a[244:245], 0\n"
      "v_fmac_f32_e64 v31, s45, v43\n"
      "v_mfma_f32_4x4x4_16b_bf16 v[24:27], a[216:217], a[246:247], 0\n"
      "v_fma_f32 v40, s47, v40, -v88\n"
      "v_fma_f32 v41, s47, v41, -v89\n"
      "v_mfma_f32_16x16x32_bf16 v[56:59], v[220:223], a[200:203], v[56:59]\n"
      "v_fma_f32 v42, s47, v42, -v90\n"
      "v_fma_f32 v43, s47, v43, -v91\n"
      "v_cvt_pk_bf16_f32 v20, v20, v24\n"
      "v_mfma_f32_16x16x32_bf16 v[60:63], v[212:215], a[200:203], v[60:63]\n"
      "ds_write_addtid_b32 v20 offset:19456\n"
      "v_cvt_pk_bf16_f32 v21, v21, v25\n"
      "v_mfma_f32_16x16x32_bf16 v[64:67], v[224:227], a[204:207], v[64:67]\n"
      "ds_read_b128 a[196:199], v1 offset:4096\n"
      "ds_write_addtid_b32 v21 offset:23552\n"
      "v_mfma_f32_16x16x32_bf16 v[68:71], v[216:219], a[204:207], v[68:71]\n"
      "s_nop 0\n"
      "v_cvt_pk_bf16_f32 v28, v28, v29\n"
      "v_cvt_pk_bf16_f32 v30, v30, v31\n"
      "v_mfma_f32_16x16x32_bf16 v[72:75], v[208:211], a[204:207], v[72:75]\n"
      "s_waitcnt lgkmcnt(3)\n"
      "ds_write_addtid_b32 v28 offset:33792\n"
      "ds_write_addtid_b32 v30 offset:34304\n"
      "v_cvt_pk_bf16_f32 v40, v40, v41\n"
      "v_mfma_f32_16x16x32_bf16 v[52:55], v[204:207], a[208:211], v[52:55]\n"
      "ds_read_b128 a[200:203], v1 offset:64\n"
      "ds_write_addtid_b32 v40 offset:35840\n"
      "v_mfma_f32_16x16x32_bf16 v[56:59], v[196:199], a[208:211], v[56:59]\n"
      "ds_read2st64_b32 a[220:221], v12 offset0:177 offset1:179\n"
      "ds_read2st64_b32 a[222:223], v15 offset0:177 offset1:179\n"
      "v_cvt_pk_bf16_f32 v42, v42, v43\n"
      "v_mfma_f32_16x16x32_bf16 v[60:63], v[188:191], a[208:211], v[60:63]\n"
      "ds_write_addtid_b32 v42 offset:36352\n"
      "v_fmac_f32 v44, s46, v32\n"
      "v_mfma_f32_16x16x32_bf16 v[64:67], v[200:203], a[212:215], v[64:67]\n"
      "ds_read_b128 a[204:207], v1 offset:4160\n"
      "v_cvt_pk_bf16_f32 v22, v22, v26\n"
      "v_mfma_f32_16x16x32_bf16 v[68:71], v[192:195], a[212:215], v[68:71]\n"
      "s_nop 0\n"
      "ds_write_addtid_b32 v22 offset:27648\n"
      "v_fmac_f32 v45, s46, v33\n"
      "v_fmac_f32 v46, s46, v34\n"
      "v_mfma_f32_16x16x32_bf16 v[72:75], v[184:187], a[212:215], v[72:75]\n"
      "v_fmac_f32 v47, s46, v35\n"
      "v_cvt_pk_bf16_f32 v23, v23, v27\n"
      "v_mfma_f32_16x16x32_bf16 v[52:55], v[180:183], a[192:195], v[52:55]\n"
      "s_nop 0\n"
      "ds_read_b128 a[208:211], v1 offset:128\n"
      "ds_write_addtid_b32 v23 offset:31744\n"
      "v_mfma_f32_16x16x32_bf16 v[56:59], v[172:175], a[192:195], v[56:59]\n"
      "s_nop 0\n"
      "ds_read2st64_b32 a[224:225], v13 offset0:177 offset1:179\n"
      "ds_read2st64_b32 a[226:227], v16 offset0:177 offset1:179\n"
      "v_fma_f32 v32, 0.5, v32, v116\n"
      "v_mfma_f32_16x16x32_bf16 v[60:63], v[164:167], a[192:195], v[60:63]\n"
      "s_waitcnt lgkmcnt(6)\n"
      "v_fma_f32 v33, 0.5, v33, v117\n"
      "v_fma_f32 v34, 0.5, v34, v118\n"
      "v_mfma_f32_16x16x32_bf16 v[64:67], v[176:179], a[196:199], v[64:67]\n"
      "ds_read_b128 a[212:215], v1 offset:4224\n"
      "v_fma_f32 v35, 0.5, v35, v119\n"
      "v_mfma_f32_16x16x32_bf16 v[68:71], v[168:171], a[196:199], v[68:71]\n"
      "v_fmac_f32 v44, s48, v92\n"
      "v_fmac_f32 v45, s48, v93\n"
      "v_mfma_f32_16x16x32_bf16 v[72:75], v[160:163], a[196:199], v[72:75]\n"
      "v_fmac_f32 v46, s48, v94\n"
      "v_fmac_f32 v47, s48, v95\n"
      "v_mfma_f32_16x16x32_bf16 v[52:55], v[156:159], a[200:203], v[52:55]\n"
      "ds_read_b128 a[192:195], v2 offset:0\n"
      "v_fmac_f32 v32, s45, v44\n"
      "v_mfma_f32_16x16x32_bf16 v[56:59], v[148:151], a[200:203], v[56:59]\n"
      "v_fmac_f32 v33, s45, v45\n"
      "v_fmac_f32 v34, s45, v46\n"
      "v_mfma_f32_16x16x32_bf16 v[60:63], v[140:143], a[200:203], v[60:63]\n"
      "s_waitcnt lgkmcnt(1)\n"
      "v_fmac_f32 v35, s45, v47\n"
      "v_fma_f32 v44, s47, v44, -v92\n"
      "v_mfma_f32_16x16x32_bf16 v[64:67], v[152:155], a[204:207], v[64:67]\n"
      "s_nop 0\n"
      "buffer_store_dwordx4 a[220:223], v18, s[8:11], s58 offen offset:0 sc1\n"
      "ds_read_b128 a[196:199], v2 offset:4096\n"
      "v_mfma_f32_16x16x32_bf16 v[68:71], v[144:147], a[204:207], v[68:71]\n"
      "ds_read_b64_tr_b16 a[248:249], v7 offset:54272\n"
      "ds_read_b64_tr_b16 a[250:251], v7 offset:54280\n"
      "v_fma_f32 v45, s47, v45, -v93\n"
      "v_mfma_f32_16x16x32_bf16 v[72:75], v[136:139], a[204:207], v[72:75]\n"
      "v_fma_f32 v46, s47, v46, -v94\n"
      "v_fma_f32 v47, s47, v47, -v95\n"
      "v_mfma_f32_16x16x32_bf16 v[52:55], v[132:135], a[208:211], v[52:55]\n"
      "ds_read_b128 a[200:203], v2 offset:64\n"
      "v_cvt_pk_bf16_f32 v32, v32, v33\n"
      "v_mfma_f32_16x16x32_bf16 v[56:59], v[124:127], a[208:211], v[56:59]\n"
      "ds_write_addtid_b32 v32 offset:41988\n"
      "v_cvt_pk_bf16_f32 v34, v34, v35\n"
      "v_mfma_f32_16x16x32_bf16 v[60:63], a[184:187], a[208:211], v[60:63]\n"
      "ds_write_addtid_b32 v34 offset:42500\n"
      "v_cvt_pk_bf16_f32 v44, v44, v45\n"
      "v_cvt_pk_bf16_f32 v46, v46, v47\n"
      "v_mfma_f32_16x16x32_bf16 v[64:67], v[128:131], a[212:215], v[64:67]\n"
      "s_nop 0\n"
      "buffer_store_dwordx4 a[224:227], v18, s[8:11], s58 offen offset:128 sc1\n"
      "ds_read_b128 a[204:207], v2 offset:4160\n"
      "v_mfma_f32_16x16x32_bf16 v[68:71], a[188:191], a[212:215], v[68:71]\n"
      "s_waitcnt lgkmcnt(3)\n"
      "ds_write_addtid_b32 v44 offset:44036\n"
      "ds_write_addtid_b32 v46 offset:44548\n"
      "v_fmac_f32 v48, s46, v36\n"
      "v_mfma_f32_4x4x4_16b_bf16 v[20:23], a[216:217], a[248:249], 0\n"
      "v_fmac_f32 v49, s46, v37\n"
      "v_mfma_f32_4x4x4_16b_bf16 v[24:27], a[216:217], a[250:251], 0\n"
      "ds_read2st64_b32 a[228:229], v14 offset0:177 offset1:179\n"
      "ds_read2st64_b32 a[230:231], v17 offset0:177 offset1:179\n"
      "v_fmac_f32_e64 v50, s46, v38\n"
      "v_mfma_f32_16x16x32_bf16 v[72:75], a[180:183], a[212:215], v[72:75]\n"
      "v_fmac_f32 v51, s46, v39\n"
      "v_fma_f32 v36, 0.5, v36, v120\n"
      "v_cvt_pk_bf16_f32 v20, v20, v24\n"
      "v_mfma_f32_16x16x32_bf16 v[52:55], a[176:179], a[192:195], v[52:55]\n"
      "ds_read_b128 a[208:211], v2 offset:128\n"
      "ds_write_addtid_b32 v20 offset:19968\n"
      "v_mfma_f32_16x16x32_bf16 v[56:59], a[168:171], a[192:195], v[56:59]\n"
      "s_nop 0\n"
      "v_fma_f32 v37, 0.5, v37, v121\n"
      "v_fma_f32 v38, 0.5, v38, v122\n"
      "v_mfma_f32_16x16x32_bf16 v[60:63], a[160:163], a[192:195], v[60:63]\n"
      "v_fma_f32 v39, 0.5, v39, v123\n"
      "v_cvt_pk_bf16_f32 v21, v21, v25\n"
      "v_mfma_f32_16x16x32_bf16 v[64:67], a[172:175], a[196:199], v[64:67]\n"
      "ds_read_b128 a[212:215], v2 offset:4224\n"
      "ds_write_addtid_b32 v21 offset:24064\n"
      "v_fmac_f32 v48, s48, v96\n"
      "v_mfma_f32_16x16x32_bf16 v[68:71], a[164:167], a[196:199], v[68:71]\n"
      "v_fmac_f32 v49, s48, v97\n"
      "v_cvt_pk_bf16_f32 v22, v22, v26\n"
      "v_mfma_f32_16x16x32_bf16 v[72:75], a[156:159], a[196:199], v[72:75]\n"
      "ds_write_addtid_b32 v22 offset:28160\n"
      "v_fmac_f32 v50, s48, v98\n"
      "v_mfma_f32_16x16x32_bf16 v[52:55], a[152:155], a[200:203], v[52:55]\n"
      "ds_read_b128 a[192:195], v3 offset:33792\n"
      "v_fmac_f32 v51, s48, v99\n"
      "v_mfma_f32_16x16x32_bf16 v[56:59], a[144:147], a[200:203], v[56:59]\n"
      "v_fmac_f32 v36, s45, v48\n"
      "v_cvt_pk_bf16_f32 v23, v23, v27\n"
      "v_mfma_f32_16x16x32_bf16 v[60:63], a[136:139], a[200:203], v[60:63]\n"
      "s_waitcnt lgkmcnt(2)\n"
      "ds_write_addtid_b32 v23 offset:32256\n"
      "v_fmac_f32 v37, s45, v49\n"
      "v_mfma_f32_16x16x32_bf16 v[64:67], a[148:151], a[204:207], v[64:67]\n"
      "ds_read_b128 a[196:199], v3 offset:33856\n"
      "v_fmac_f32 v38, s45, v50\n"
      "v_mfma_f32_16x16x32_bf16 v[68:71], a[140:143], a[204:207], v[68:71]\n"
      "v_fmac_f32 v39, s45, v51\n"
      "v_fma_f32 v48, s47, v48, -v96\n"
      "v_mfma_f32_16x16x32_bf16 v[72:75], a[132:135], a[204:207], v[72:75]\n"
      "buffer_store_dwordx4 a[228:231], v18, s[8:11], s58 offen offset:256 sc1\n"
      "v_mfma_f32_16x16x32_bf16 v[52:55], a[128:131], a[208:211], v[52:55]\n"
      "s_nop 0\n"
      "ds_read_b128 a[200:203], v3 offset:33920\n"
      "v_fma_f32 v49, s47, v49, -v97\n"
      "v_mfma_f32_16x16x32_bf16 v[56:59], a[120:123], a[208:211], v[56:59]\n"
      "ds_read2st64_b32 a[232:233], v12 offset0:0 offset1:2\n"
      "ds_read2st64_b32 a[234:235], v15 offset0:0 offset1:2\n"
      "v_fma_f32 v50, s47, v50, -v98\n"
      "v_mfma_f32_16x16x32_bf16 v[60:63], a[112:115], a[208:211], v[60:63]\n"
      "v_fma_f32 v51, s47, v51, -v99\n"
      "v_cvt_pk_bf16_f32 v36, v36, v37\n"
      "v_mfma_f32_16x16x32_bf16 v[64:67], a[124:127], a[212:215], v[64:67]\n"
      "ds_read_b128 a[204:207], v4 offset:33792\n"
      "ds_write_addtid_b32 v36 offset:37888\n"
      "v_mfma_f32_16x16x32_bf16 v[68:71], a[116:119], a[212:215], v[68:71]\n"
      "s_nop 0\n"
      "v_cvt_pk_bf16_f32 v38, v38, v39\n"
      "v_cvt_pk_bf16_f32 v48, v48, v49\n"
      "v_mfma_f32_16x16x32_bf16 v[72:75], a[108:111], a[212:215], v[72:75]\n"
      "s_waitcnt lgkmcnt(4)\n"
      "ds_write_addtid_b32 v38 offset:38400\n"
      "ds_write_addtid_b32 v48 offset:39936\n"
      "v_add_f32 v52, v52, v64\n"
      "v_add_f32 v53, v53, v65\n"
      "v_mfma_f32_16x16x32_bf16 v[100:103], a[0:3], a[192:195], 0\n"
      "ds_read_b128 a[208:211], v4 offset:33856\n"
      "ds_write_addtid_b32 v52 offset:45312\n"
      "v_add_f32 v54, v54, v66\n"
      "v_mfma_f32_16x16x32_bf16 v[104:107], a[4:7], a[192:195], 0\n"
      "s_nop 0\n"
      "ds_read2st64_b32 a[236:237], v13 offset0:0 offset1:2\n"
      "ds_write_addtid_b32 v53 offset:45824\n"
      "v_mfma_f32_16x16x32_bf16 v[108:111], a[8:11], a[192:195], 0\n"
      "ds_write_addtid_b32 v54 offset:46336\n"
      "ds_read2st64_b32 a[238:239], v16 offset0:0 offset1:2\n"
      "v_mfma_f32_16x16x32_bf16 v[100:103], a[12:15], a[196:199], v[100:103]\n"
      "ds_read_b128 a[212:215], v4 offset:33920\n"
      "v_add_f32 v55, v55, v67\n"
      "v_add_f32 v56, v56, v68\n"
      "v_mfma_f32_16x16x32_bf16 v[104:107], a[16:19], a[196:199], v[104:107]\n"
      "ds_write_addtid_b32 v55 offset:46848\n"
      "ds_write_addtid_b32 v56 offset:47360\n"
      "v_mfma_f32_16x16x32_bf16 v[108:111], a[20:23], a[196:199], v[108:111]\n"
      "v_add_f32 v57, v57, v69\n"
      "v_add_f32 v58, v58, v70\n"
      "v_mfma_f32_16x16x32_bf16 v[100:103], a[24:27], a[200:203], v[100:103]\n"
      "ds_read_b128 a[192:195], v5 offset:33792\n"
      "ds_write_addtid_b32 v57 offset:47872\n"
      "v_mfma_f32_16x16x32_bf16 v[104:107], a[28:31], a[200:203], v[104:107]\n"
      "ds_write_addtid_b32 v58 offset:48384\n"
      "v_add_f32 v59, v59, v71\n"
      "v_cvt_pk_bf16_f32 v50, v50, v51\n"
      "v_mfma_f32_16x16x32_bf16 v[108:111], a[32:35], a[200:203], v[108:111]\n"
      "s_waitcnt lgkmcnt(5)\n"
      "ds_write_addtid_b32 v50 offset:40448\n"
      "ds_write_addtid_b32 v59 offset:48896\n"
      "v_mfma_f32_16x16x32_bf16 v[100:103], a[36:39], a[204:207], v[100:103]\n"
      "buffer_store_dwordx4 a[232:235], v18, s[8:11], s59 offen offset:0 sc1\n"
      "ds_read_b128 a[196:199], v5 offset:33856\n"
      "v_mfma_f32_16x16x32_bf16 v[104:107], a[40:43], a[204:207], v[104:107]\n"
      "v_add_f32 v60, v60, v72\n"
      "v_add_f32 v61, v61, v73\n"
      "v_add_f32 v62, v62, v74\n"
      "v_mfma_f32_16x16x32_bf16 v[108:111], a[44:47], a[204:207], v[108:111]\n"
      "ds_write_addtid_b32 v60 offset:49408\n"
      "ds_write_addtid_b32 v61 offset:49920\n"
      "v_add_f32 v63, v63, v75\n"
      "v_mfma_f32_16x16x32_bf16 v[100:103], a[48:51], a[208:211], v[100:103]\n"
      "ds_read_b128 a[200:203], v5 offset:33920\n"
      "ds_write_addtid_b32 v62 offset:50432\n"
      "v_mfma_f32_16x16x32_bf16 v[104:107], a[52:55], a[208:211], v[104:107]\n"
      "ds_write_addtid_b32 v63 offset:50944\n"
      "v_fma_f32 v40, -2.0, v64, v52\n"
      "v_mfma_f32_16x16x32_bf16 v[108:111], a[56:59], a[208:211], v[108:111]\n"
      "buffer_store_dwordx4 a[236:239], v18, s[8:11], s59 offen offset:128 sc1\n"
      "v_mfma_f32_16x16x32_bf16 v[100:103], a[60:63], a[212:215], v[100:103]\n"
      "v_fma_f32 v41, -2.0, v65, v53\n"
      "v_fma_f32 v42, -2.0, v66, v54\n"
      "v_mfma_f32_16x16x32_bf16 v[104:107], a[64:67], a[212:215], v[104:107]\n"
      "v_fma_f32 v43, -2.0, v67, v55\n"
      "v_fma_f32 v44, -2.0, v68, v56\n"
      "v_mfma_f32_16x16x32_bf16 v[108:111], a[68:71], a[212:215], v[108:111]\n"
      "s_waitcnt lgkmcnt(0)\n"
      "v_fma_f32 v45, -2.0, v69, v57\n"
      "s_barrier\n"
      "v_mfma_f32_16x16x32_bf16 v[100:103], a[72:75], a[192:195], v[100:103]\n"
      "s_add_u32 s58, s58, 3072\n"
      "ds_read_b128 a[204:207], v0 offset:16384\n"
      "ds_read2st64_b32 v[28:29], v6 offset0:0 offset1:2\n"
      "v_fma_f32 v46, -2.0, v70, v58\n"
      "v_mfma_f32_16x16x32_bf16 v[104:107], a[76:79], a[192:195], v[104:107]\n"
      "ds_read_b128 a[208:211], v0 offset:20480\n"
      "ds_read2st64_b32 v[30:31], v6 offset0:4 offset1:6\n"
      "v_mfma_f32_16x16x32_bf16 v[108:111], a[80:83], a[192:195], v[108:111]\n"
      "ds_read2st64_b32 v[32:33], v6 offset0:8 offset1:10\n"
      "ds_read2st64_b32 v[34:35], v6 offset0:12 offset1:14\n"
      "v_mfma_f32_16x16x32_bf16 v[100:103], a[84:87], a[196:199], v[100:103]\n"
      "ds_read_b128 a[212:215], v0 offset:16448\n"
      "v_fma_f32 v47, -2.0, v71, v59\n"
      "v_mfma_f32_16x16x32_bf16 v[104:107], a[88:91], a[196:199], v[104:107]\n"
      "v_fma_f32 v48, -2.0, v72, v60\n"
      "v_fma_f32 v49, -2.0, v73, v61\n"
      "v_mfma_f32_16x16x32_bf16 v[108:111], a[92:95], a[196:199], v[108:111]\n"
      "ds_read2st64_b32 v[36:37], v6 offset0:16 offset1:18\n"
      "ds_read2st64_b32 v[38:39], v6 offset0:20 offset1:22\n"
      "v_mfma_f32_16x16x32_bf16 v[100:103], a[96:99], a[200:203], v[100:103]\n"
      "ds_read_b128 a[192:195], v0 offset:20544\n"
      "ds_read2st64_b32 a[220:221], v12 offset0:181 offset1:183\n"
      "v_mfma_f32_16x16x32_bf16 v[104:107], a[100:103], a[200:203], v[104:107]\n"
      "ds_read2st64_b32 a[222:223], v15 offset0:181 offset1:183\n"
      "v_fma_f32 v50, -2.0, v74, v62\n"
      "v_fma_f32 v51, -2.0, v75, v63\n"
      "v_mfma_f32_16x16x32_bf16 v[108:111], a[104:107], a[200:203], v[108:111]\n"
      "s_waitcnt lgkmcnt(8)\n"
      "v_fmac_f32 v40, s46, v28\n"
      "v_fmac_f32 v41, s46, v29\n"
      "v_mfma_f32_16x16x32_bf16 v[52:55], v[252:255], a[204:207], 0\n"
      "ds_read_b128 a[196:199], v0 offset:16512\n"
      "v_fmac_f32 v42, s46, v30\n"
      "v_mfma_f32_16x16x32_bf16 v[56:59], v[244:247], a[204:207], 0\n"
      "v_fmac_f32 v43, s46, v31\n"
      "v_fma_f32 v28, 0.5, v28, v76\n"
      "v_mfma_f32_16x16x32_bf16 v[60:63], v[236:239], a[204:207], 0\n"
      "v_fma_f32 v29, 0.5, v29, v77\n"
      "v_fma_f32 v30, 0.5, v30, v78\n"
      "v_mfma_f32_16x16x32_bf16 v[64:67], v[248:251], a[208:211], 0\n"
      "s_nop 0\n"
      "ds_read_b128 a[200:203], v0 offset:20608\n"
      "v_fma_f32 v31, 0.5, v31, v79\n"
      "v_mfma_f32_16x16x32_bf16 v[68:71], v[240:243], a[208:211], 0\n"
      "v_fmac_f32 v40, s48, v100\n"
      "v_fmac_f32 v41, s48, v101\n"
      "v_mfma_f32_16x16x32_bf16 v[72:75], v[232:235], a[208:211], 0\n"
      "s_waitcnt lgkmcnt(2)\n"
      "v_fmac_f32 v42, s48, v102\n"
      "v_fmac_f32 v43, s48, v103\n"
      "v_mfma_f32_16x16x32_bf16 v[52:55], v[228:231], a[212:215], v[52:55]\n"
      "ds_read_b128 a[204:207], v1 offset:16384\n"
      "v_fmac_f32 v28, s45, v40\n"
      "v_mfma_f32_16x16x32_bf16 v[56:59], v[220:223], a[212:215], v[56:59]\n"
      "v_fmac_f32 v29, s45, v41\n"
      "v_fmac_f32 v30, s45, v42\n"
      "v_mfma_f32_16x16x32_bf16 v[60:63], v[212:215], a[212:215], v[60:63]\n"
      "v_fmac_f32 v31, s45, v43\n"
      "v_fma_f32 v40, s47, v40, -v100\n"
      "v_mfma_f32_16x16x32_bf16 v[64:67], v[224:227], a[192:195], v[64:67]\n"
      "s_nop 0\n"
      "ds_read_b128 a[208:211], v1 offset:20480\n"
      "v_fma_f32 v41, s47, v41, -v101\n"
      "v_mfma_f32_16x16x32_bf16 v[68:71], v[216:219], a[192:195], v[68:71]\n"
      "v_fma_f32 v42, s47, v42, -v102\n"
      "v_fma_f32 v43, s47, v43, -v103\n"
      "v_mfma_f32_16x16x32_bf16 v[72:75], v[208:211], a[192:195], v[72:75]\n"
      "s_waitcnt lgkmcnt(1)\n"
      "buffer_store_dwordx4 a[220:223], v18, s[8:11], s58 offen offset:0 sc1\n"
      "v_mfma_f32_16x16x32_bf16 v[52:55], v[204:207], a[196:199], v[52:55]\n"
      "s_nop 0\n"
      "ds_read_b128 a[212:215], v1 offset:16448\n"
      "v_cvt_pk_bf16_f32 v28, v28, v29\n"
      "v_mfma_f32_16x16x32_bf16 v[56:59], v[196:199], a[196:199], v[56:59]\n"
      "ds_write_addtid_b32 v28 offset:32768\n"
      "ds_read2st64_b32 a[224:225], v13 offset0:181 offset1:183\n"
      "v_cvt_pk_bf16_f32 v30, v30, v31\n"
      "v_mfma_f32_16x16x32_bf16 v[60:63], v[188:191], a[196:199], v[60:63]\n"
      "ds_write_addtid_b32 v30 offset:33280\n"
      "ds_read2st64_b32 a[226:227], v16 offset0:181 offset1:183\n"
      "v_cvt_pk_bf16_f32 v40, v40, v41\n"
      "v_mfma_f32_16x16x32_bf16 v[64:67], v[200:203], a[200:203], v[64:67]\n"
      "ds_read_b128 a[192:195], v1 offset:20544\n"
      "ds_write_addtid_b32 v40 offset:34816\n"
      "v_mfma_f32_16x16x32_bf16 v[68:71], v[192:195], a[200:203], v[68:71]\n"
      "v_cvt_pk_bf16_f32 v42, v42, v43\n"
      "v_fmac_f32 v44, s46, v32\n"
      "v_mfma_f32_16x16x32_bf16 v[72:75], v[184:187], a[200:203], v[72:75]\n"
      "s_nop 0\n"
      "ds_write_addtid_b32 v42 offset:35328\n"
      "v_fmac_f32 v45, s46, v33\n"
      "v_mfma_f32_16x16x32_bf16 v[52:55], v[180:183], a[204:207], v[52:55]\n"
      "ds_read_b128 a[196:199], v1 offset:16512\n"
      "v_fmac_f32 v46, s46, v34\n"
      "v_mfma_f32_16x16x32_bf16 v[56:59], v[172:175], a[204:207], v[56:59]\n"
      "v_fmac_f32 v47, s46, v35\n"
      "v_fma_f32 v32, 0.5, v32, v80\n"
      "v_mfma_f32_16x16x32_bf16 v[60:63], v[164:167], a[204:207], v[60:63]\n"
      "s_waitcnt lgkmcnt(4)\n"
      "v_fma_f32 v33, 0.5, v33, v81\n"
      "v_fma_f32 v34, 0.5, v34, v82\n"
      "v_mfma_f32_16x16x32_bf16 v[64:67], v[176:179], a[208:211], v[64:67]\n"
      "ds_read_b128 a[200:203], v1 offset:20608\n"
      "v_fma_f32 v35, 0.5, v35, v83\n"
      "v_fmac_f32 v44, s48, v104\n"
      "v_mfma_f32_16x16x32_bf16 v[68:71], v[168:171], a[208:211], v[68:71]\n"
      "v_fmac_f32 v45, s48, v105\n"
      "v_fmac_f32_e64 v46, s48, v106\n"
      "v_mfma_f32_16x16x32_bf16 v[72:75], v[160:163], a[208:211], v[72:75]\n"
      "v_fmac_f32 v47, s48, v107\n"
      "v_fmac_f32 v32, s45, v44\n"
      "v_mfma_f32_16x16x32_bf16 v[52:55], v[156:159], a[212:215], v[52:55]\n"
      "ds_read_b128 a[204:207], v2 offset:16384\n"
      "v_fmac_f32 v33, s45, v45\n"
      "v_mfma_f32_16x16x32_bf16 v[56:59], v[148:151], a[212:215], v[56:59]\n"
      "v_fmac_f32 v34, s45, v46\n"
      "v_fmac_f32 v35, s45, v47\n"
      "v_mfma_f32_16x16x32_bf16 v[60:63], v[140:143], a[212:215], v[60:63]\n"
      "s_waitcnt lgkmcnt(1)\n"
      "v_fma_f32 v44, s47, v44, -v104\n"
      "v_fma_f32 v45, s47, v45, -v105\n"
      "v_mfma_f32_16x16x32_bf16 v[64:67], v[152:155], a[192:195], v[64:67]\n"
      "buffer_store_dwordx4 a[224:227], v18, s[8:11], s58 offen offset:128 sc1\n"
      "ds_read_b128 a[208:211], v2 offset:20480\n"
      "v_mfma_f32_16x16x32_bf16 v[68:71], v[144:147], a[192:195], v[68:71]\n"
      "v_fma_f32 v46, s47, v46, -v106\n"
      "v_fma_f32 v47, s47, v47, -v107\n"
      "v_mfma_f32_16x16x32_bf16 v[72:75], v[136:139], a[192:195], v[72:75]\n"
      "ds_read2st64_b32 a[228:229], v14 offset0:181 offset1:183\n"
      "ds_read2st64_b32 a[230:231], v17 offset0:181 offset1:183\n"
      "v_cvt_pk_bf16_f32 v32, v32, v33\n"
      "v_mfma_f32_16x16x32_bf16 v[52:55], v[132:135], a[196:199], v[52:55]\n"
      "ds_read_b128 a[212:215], v2 offset:16448\n"
      "ds_write_addtid_b32 v32 offset:40964\n"
      "v_mfma_f32_16x16x32_bf16 v[56:59], v[124:127], a[196:199], v[56:59]\n"
      "v_cvt_pk_bf16_f32 v34, v34, v35\n"
      "v_cvt_pk_bf16_f32 v44, v44, v45\n"
      "v_mfma_f32_16x16x32_bf16 v[60:63], a[184:187], a[196:199], v[60:63]\n"
      "ds_write_addtid_b32 v34 offset:41476\n"
      "ds_write_addtid_b32 v44 offset:43012\n"
      "v_mfma_f32_16x16x32_bf16 v[64:67], v[128:131], a[200:203], v[64:67]\n"
      "ds_read_b128 a[192:195], v2 offset:20544\n"
      "v_cvt_pk_bf16_f32 v46, v46, v47\n"
      "v_mfma_f32_16x16x32_bf16 v[68:71], a[188:191], a[200:203], v[68:71]\n"
      "ds_write_addtid_b32 v46 offset:43524\n"
      "v_fmac_f32 v48, s46, v36\n"
      "v_fmac_f32 v49, s46, v37\n"
      "v_mfma_f32_16x16x32_bf16 v[72:75], a[180:183], a[200:203], v[72:75]\n"
      "s_waitcnt lgkmcnt(4)\n"
      "v_fmac_f32 v50, s46, v38\n"
      "v_fmac_f32 v51, s46, v39\n"
      "v_mfma_f32_16x16x32_bf16 v[52:55], a[176:179], a[204:207], v[52:55]\n"
      "s_nop 0\n"
      "ds_read_b128 a[196:199], v2 offset:16512\n"
      "v_fma_f32 v36, 0.5, v36, v84\n"
      "v_mfma_f32_16x16x32_bf16 v[56:59], a[168:171], a[204:207], v[56:59]\n"
      "v_fma_f32 v37, 0.5, v37, v85\n"
      "v_fma_f32 v38, 0.5, v38, v86\n"
      "v_mfma_f32_16x16x32_bf16 v[60:63], a[160:163], a[204:207], v[60:63]\n"
      "v_fma_f32 v39, 0.5, v39, v87\n"
      "v_fmac_f32 v48, s48, v108\n"
      "v_mfma_f32_16x16x32_bf16 v[64:67], a[172:175], a[208:211], v[64:67]\n"
      "s_nop 0\n"
      "ds_read_b128 a[200:203], v2 offset:20608\n"
      "v_fmac_f32 v49, s48, v109\n"
      "v_mfma_f32_16x16x32_bf16 v[68:71], a[164:167], a[208:211], v[68:71]\n"
      "v_fmac_f32 v50, s48, v110\n"
      "v_fmac_f32 v51, s48, v111\n"
      "v_mfma_f32_16x16x32_bf16 v[72:75], a[156:159], a[208:211], v[72:75]\n"
      "v_fmac_f32 v36, s45, v48\n"
      "v_fmac_f32 v37, s45, v49\n"
      "v_mfma_f32_16x16x32_bf16 v[52:55], a[152:155], a[212:215], v[52:55]\n"
      "ds_read_b128 a[204:207], v3 offset:50688\n"
      "v_fmac_f32 v38, s45, v50\n"
      "v_fmac_f32 v39, s45, v51\n"
      "v_mfma_f32_16x16x32_bf16 v[56:59], a[144:147], a[212:215], v[56:59]\n"
      "buffer_store_dwordx4 a[228:231], v18, s[8:11], s58 offen offset:256 sc1\n"
      "v_mfma_f32_16x16x32_bf16 v[60:63], a[136:139], a[212:215], v[60:63]\n"
      "s_waitcnt lgkmcnt(1)\n"
      "v_fma_f32 v48, s47, v48, -v108\n"
      "v_fma_f32 v49, s47, v49, -v109\n"
      "v_mfma_f32_16x16x32_bf16 v[64:67], a[148:151], a[192:195], v[64:67]\n"
      "ds_read_b128 a[208:211], v3 offset:50752\n"
      "v_fma_f32 v50, s47, v50, -v110\n"
      "v_mfma_f32_16x16x32_bf16 v[68:71], a[140:143], a[192:195], v[68:71]\n"
      "v_fma_f32 v51, s47, v51, -v111\n"
      "v_cvt_pk_bf16_f32 v36, v36, v37\n"
      "v_mfma_f32_16x16x32_bf16 v[72:75], a[132:135], a[192:195], v[72:75]\n"
      "ds_write_addtid_b32 v36 offset:36864\n"
      "v_cvt_pk_bf16_f32 v38, v38, v39\n"
      "v_mfma_f32_16x16x32_bf16 v[52:55], a[128:131], a[196:199], v[52:55]\n"
      "ds_read_b128 a[212:215], v3 offset:50816\n"
      "ds_write_addtid_b32 v38 offset:37376\n"
      "v_cvt_pk_bf16_f32 v48, v48, v49\n"
      "v_cvt_pk_bf16_f32 v50, v50, v51\n"
      "v_mfma_f32_16x16x32_bf16 v[56:59], a[120:123], a[196:199], v[56:59]\n"
      "ds_write_addtid_b32 v48 offset:38912\n"
      "ds_write_addtid_b32 v50 offset:39424\n"
      "v_mfma_f32_16x16x32_bf16 v[60:63], a[112:115], a[196:199], v[60:63]\n"
      "v_mfma_f32_16x16x32_bf16 v[64:67], a[124:127], a[200:203], v[64:67]\n"
      "ds_read_b128 a[192:195], v4 offset:50688\n"
      "v_mfma_f32_16x16x32_bf16 v[68:71], a[116:119], a[200:203], v[68:71]\n"
      "v_mfma_f32_16x16x32_bf16 v[72:75], a[108:111], a[200:203], v[72:75]\n"
      "s_waitcnt lgkmcnt(3)\n"
      "v_mfma_f32_16x16x32_bf16 v[112:115], a[0:3], a[204:207], 0\n"
      "ds_read_b128 a[196:199], v4 offset:50752\n"
      "v_mfma_f32_16x16x32_bf16 v[116:119], a[4:7], a[204:207], 0\n"
      "s_nop 0\n"
      "v_mfma_f32_16x16x32_bf16 v[120:123], a[8:11], a[204:207], 0\n"
      "v_add_f32 v52, v52, v64\n"
      "v_add_f32 v53, v53, v65\n"
      "v_add_f32 v54, v54, v66\n"
      "v_add_f32 v55, v55, v67\n"
      "v_add_f32 v56, v56, v68\n"
      "v_add_f32 v57, v57, v69\n"
      "v_add_f32 v58, v58, v70\n"
      "v_mfma_f32_16x16x32_bf16 v[112:115], a[12:15], a[208:211], v[112:115]\n"
      "s_nop 0\n"
      "ds_read_b128 a[200:203], v4 offset:50816\n"
      "ds_write_addtid_b32 v52 offset:51456\n"
      "ds_write_addtid_b32 v53 offset:51968\n"
      "v_add_f32 v59, v59, v71\n"
      "v_add_f32 v60, v60, v72\n"
      "v_add_f32 v61, v61, v73\n"
      "v_add_f32 v62, v62, v74\n"
      "v_mfma_f32_16x16x32_bf16 v[116:119], a[16:19], a[208:211], v[116:119]\n"
      "ds_write_addtid_b32 v54 offset:52480\n"
      "ds_write_addtid_b32 v55 offset:52992\n"
      "ds_write_addtid_b32 v56 offset:53504\n"
      "v_mfma_f32_16x16x32_bf16 v[120:123], a[20:23], a[208:211], v[120:123]\n"
      "ds_write_addtid_b32 v57 offset:54016\n"
      "ds_write_addtid_b32 v58 offset:54528\n"
      "ds_write_addtid_b32 v59 offset:55040\n"
      "v_add_f32 v63, v63, v75\n"
      "v_mfma_f32_16x16x32_bf16 v[112:115], a[24:27], a[212:215], v[112:115]\n"
      "ds_write_addtid_b32 v60 offset:55552\n"
      "ds_write_addtid_b32 v61 offset:56064\n"
      "v_mfma_f32_16x16x32_bf16 v[116:119], a[28:31], a[212:215], v[116:119]\n"
      "s_waitcnt vmcnt(15)\n"
      "ds_write_addtid_b32 v62 offset:56576\n"
      "ds_write_addtid_b32 v63 offset:57088\n"
      "v_mfma_f32_16x16x32_bf16 v[120:123], a[32:35], a[212:215], v[120:123]\n"
      "s_waitcnt lgkmcnt(0)\n"
      "s_barrier\n"
      "v_mfma_f32_16x16x32_bf16 v[112:115], a[36:39], a[192:195], v[112:115]\n"
      "s_add_u32 s58, s58, 783360\n"
      "ds_read_b128 a[204:207], v5 offset:50688\n"
      "ds_read2st64_b32 v[28:29], v6 offset0:24 offset1:26\n"
      "v_mfma_f32_16x16x32_bf16 v[116:119], a[40:43], a[192:195], v[116:119]\n"
      "ds_read2st64_b32 v[30:31], v6 offset0:28 offset1:30\n"
      "ds_read2st64_b32 v[32:33], v6 offset0:32 offset1:34\n"
      "v_mfma_f32_16x16x32_bf16 v[120:123], a[44:47], a[192:195], v[120:123]\n"
      "ds_read2st64_b32 v[34:35], v6 offset0:36 offset1:38\n"
      "ds_read2st64_b32 v[36:37], v6 offset0:40 offset1:42\n"
      "v_mfma_f32_16x16x32_bf16 v[112:115], a[48:51], a[196:199], v[112:115]\n"
      "ds_read_b128 a[208:211], v5 offset:50752\n"
      "ds_read2st64_b32 v[38:39], v6 offset0:44 offset1:46\n"
      "v_mfma_f32_16x16x32_bf16 v[116:119], a[52:55], a[196:199], v[116:119]\n"
      "v_fma_f32 v40, -2.0, v64, v52\n"
      "v_fma_f32 v41, -2.0, v65, v53\n"
      "v_mfma_f32_16x16x32_bf16 v[120:123], a[56:59], a[196:199], v[120:123]\n"
      "ds_read2st64_b32 a[220:221], v12 offset0:177 offset1:179\n"
      "ds_read2st64_b32 a[222:223], v15 offset0:177 offset1:179\n"
      "v_mfma_f32_16x16x32_bf16 v[112:115], a[60:63], a[200:203], v[112:115]\n"
      "ds_read_b128 a[212:215], v5 offset:50816\n"
      "v_fma_f32 v42, -2.0, v66, v54\n"
      "v_mfma_f32_16x16x32_bf16 v[116:119], a[64:67], a[200:203], v[116:119]\n"
      "s_nop 0\n"
      "ds_read2st64_b32 a[224:225], v13 offset0:177 offset1:179\n"
      "ds_read2st64_b32 a[226:227], v16 offset0:177 offset1:179\n"
      "v_mfma_f32_16x16x32_bf16 v[120:123], a[68:71], a[200:203], v[120:123]\n"
      "s_waitcnt lgkmcnt(9)\n"
      "v_fma_f32 v43, -2.0, v67, v55\n"
      "v_fma_f32 v44, -2.0, v68, v56\n"
      "v_mfma_f32_16x16x32_bf16 v[112:115], a[72:75], a[204:207], v[112:115]\n"
      "s_waitcnt lgkmcnt(3)\n"
      "buffer_store_dwordx4 a[220:223], v18, s[8:11], s58 offen offset:0 sc1\n"
      "v_mfma_f32_16x16x32_bf16 v[116:119], a[76:79], a[204:207], v[116:119]\n"
      "v_fma_f32 v45, -2.0, v69, v57\n"
      "v_fma_f32 v46, -2.0, v70, v58\n"
      "v_mfma_f32_16x16x32_bf16 v[120:123], a[80:83], a[204:207], v[120:123]\n"
      "s_nop 0\n"
      "ds_read2st64_b32 a[228:229], v14 offset0:177 offset1:179\n"
      "ds_read2st64_b32 a[230:231], v17 offset0:177 offset1:179\n"
      "v_mfma_f32_16x16x32_bf16 v[112:115], a[84:87], a[208:211], v[112:115]\n"
      "s_waitcnt lgkmcnt(2)\n"
      "buffer_store_dwordx4 a[224:227], v18, s[8:11], s58 offen offset:128 sc1\n"
      "v_mfma_f32_16x16x32_bf16 v[116:119], a[88:91], a[208:211], v[116:119]\n"
      "s_nop 0\n"
      "v_fma_f32 v47, -2.0, v71, v59\n"
      "v_fma_f32 v48, -2.0, v72, v60\n"
      "v_mfma_f32_16x16x32_bf16 v[120:123], a[92:95], a[208:211], v[120:123]\n"
      "v_fma_f32 v49, -2.0, v73, v61\n"
      "v_fma_f32 v50, -2.0, v74, v62\n"
      "v_mfma_f32_16x16x32_bf16 v[112:115], a[96:99], a[212:215], v[112:115]\n"
      "s_waitcnt lgkmcnt(0)\n"
      "buffer_store_dwordx4 a[228:231], v18, s[8:11], s58 offen offset:256 sc1\n"
      "v_mfma_f32_16x16x32_bf16 v[116:119], a[100:103], a[212:215], v[116:119]\n"
      "v_fma_f32 v51, -2.0, v75, v63\n"
      "v_fmac_f32 v40, s46, v28\n"
      "v_mfma_f32_16x16x32_bf16 v[120:123], a[104:107], a[212:215], v[120:123]\n"
      "v_fmac_f32 v41, s46, v29\n"
      "v_fmac_f32 v42, s46, v30\n"
      "v_fmac_f32 v43, s46, v31\n"
      "v_fma_f32 v28, 0.5, v28, v88\n"
      "v_fma_f32 v29, 0.5, v29, v89\n"
      "v_fma_f32 v30, 0.5, v30, v90\n"
      "v_fma_f32 v31, 0.5, v31, v91\n"
      "v_fmac_f32 v44, s46, v32\n"
      "v_fmac_f32_e64 v45, s46, v33\n"
      "v_pk_fma_f32 v[46:47], v[34:35], s[46:47], v[46:47] op_sel:[0,0,0] op_sel_hi:[1,0,1]\n"
      "v_pk_fma_f32 v[32:33], v[32:33], s[48:49], v[92:93] op_sel:[0,1,0] op_sel_hi:[1,1,1]\n"
      "v_pk_fma_f32 v[34:35], v[34:35], s[48:49], v[94:95] op_sel:[0,1,0] op_sel_hi:[1,1,1]\n"
      "v_pk_fma_f32 v[48:49], v[36:37], s[46:47], v[48:49] op_sel:[0,0,0] op_sel_hi:[1,0,1]\n"
      "v_pk_fma_f32 v[50:51], v[38:39], s[46:47], v[50:51] op_sel:[0,0,0] op_sel_hi:[1,0,1]\n"
      "v_pk_fma_f32 v[36:37], v[36:37], s[48:49], v[96:97] op_sel:[0,1,0] op_sel_hi:[1,1,1]\n"
      "v_pk_fma_f32 v[38:39], v[38:39], s[48:49], v[98:99] op_sel:[0,1,0] op_sel_hi:[1,1,1]\n"
      "v_pk_fma_f32 v[40:41], v[112:113], s[48:49], v[40:41] op_sel:[0,0,0] op_sel_hi:[1,0,1]\n"
      "v_pk_fma_f32 v[42:43], v[114:115], s[48:49], v[42:43] op_sel:[0,0,0] op_sel_hi:[1,0,1]\n"
      "v_pk_fma_f32 v[28:29], v[40:41], s[44:45], v[28:29] op_sel:[0,1,0] op_sel_hi:[1,1,1]\n"
      "v_pk_fma_f32 v[30:31], v[42:43], s[44:45], v[30:31] op_sel:[0,1,0] op_sel_hi:[1,1,1]\n"
      "v_pk_fma_f32 v[40:41], v[40:41], s[46:47], v[112:113] op_sel:[0,1,0] op_sel_hi:[1,1,1] neg_lo:[0,0,1] neg_hi:[0,0,1]\n"
      "v_pk_fma_f32 v[42:43], v[42:43], s[46:47], v[114:115] op_sel:[0,1,0] op_sel_hi:[1,1,1] neg_lo:[0,0,1] neg_hi:[0,0,1]\n"
      "v_cvt_pk_bf16_f32 v28, v28, v29\n"
      "v_cvt_pk_bf16_f32 v30, v30, v31\n"
      "v_cvt_pk_bf16_f32 v40, v40, v41\n"
      "v_cvt_pk_bf16_f32 v42, v42, v43\n"
      "v_pk_fma_f32 v[44:45], v[116:117], s[48:49], v[44:45] op_sel:[0,0,0] op_sel_hi:[1,0,1]\n"
      "v_pk_fma_f32 v[46:47], v[118:119], s[48:49], v[46:47] op_sel:[0,0,0] op_sel_hi:[1,0,1]\n"
      "v_pk_fma_f32 v[32:33], v[44:45], s[44:45], v[32:33] op_sel:[0,1,0] op_sel_hi:[1,1,1]\n"
      "v_pk_fma_f32 v[34:35], v[46:47], s[44:45], v[34:35] op_sel:[0,1,0] op_sel_hi:[1,1,1]\n"
      "v_pk_fma_f32 v[44:45], v[44:45], s[46:47], v[116:117] op_sel:[0,1,0] op_sel_hi:[1,1,1] neg_lo:[0,0,1] neg_hi:[0,0,1]\n"
      "v_pk_fma_f32 v[46:47], v[46:47], s[46:47], v[118:119] op_sel:[0,1,0] op_sel_hi:[1,1,1] neg_lo:[0,0,1] neg_hi:[0,0,1]\n"
      "v_cvt_pk_bf16_f32 v32, v32, v33\n"
      "v_cvt_pk_bf16_f32 v34, v34, v35\n"
      "ds_write_addtid_b32 v28 offset:33792\n"
      "ds_write_addtid_b32 v30 offset:34304\n"
      "ds_write_addtid_b32 v40 offset:35840\n"
      "ds_write_addtid_b32 v42 offset:36352\n"
      "ds_write_addtid_b32 v32 offset:41988\n"
      "ds_write_addtid_b32 v34 offset:42500\n"
      "v_cvt_pk_bf16_f32 v44, v44, v45\n"
      "v_cvt_pk_bf16_f32 v46, v46, v47\n"
      "v_pk_fma_f32 v[48:49], v[120:121], s[48:49], v[48:49] op_sel:[0,0,0] op_sel_hi:[1,0,1]\n"
      "v_pk_fma_f32 v[50:51], v[122:123], s[48:49], v[50:51] op_sel:[0,0,0] op_sel_hi:[1,0,1]\n"
      "v_pk_fma_f32 v[36:37], v[48:49], s[44:45], v[36:37] op_sel:[0,1,0] op_sel_hi:[1,1,1]\n"
      "v_pk_fma_f32 v[38:39], v[50:51], s[44:45], v[38:39] op_sel:[0,1,0] op_sel_hi:[1,1,1]\n"
      "v_pk_fma_f32 v[48:49], v[48:49], s[46:47], v[120:121] op_sel:[0,1,0] op_sel_hi:[1,1,1] neg_lo:[0,0,1] neg_hi:[0,0,1]\n"
      "v_pk_fma_f32 v[50:51], v[50:51], s[46:47], v[122:123] op_sel:[0,1,0] op_sel_hi:[1,1,1] neg_lo:[0,0,1] neg_hi:[0,0,1]\n"
      "v_cvt_pk_bf16_f32 v36, v36, v37\n"
      "v_cvt_pk_bf16_f32 v38, v38, v39\n"
      "v_cvt_pk_bf16_f32 v48, v48, v49\n"
      "v_cvt_pk_bf16_f32 v50, v50, v51\n"
      "ds_write_addtid_b32 v44 offset:44036\n"
      "ds_write_addtid_b32 v46 offset:44548\n"
      "ds_write_addtid_b32 v36 offset:37888\n"
      "ds_write_addtid_b32 v38 offset:38400\n"
      "ds_write_addtid_b32 v48 offset:39936\n"
      "ds_write_addtid_b32 v50 offset:40448\n"
      "s_waitcnt lgkmcnt(0)\n"
      "s_barrier\n"
      "ds_read2st64_b32 a[220:221], v12 offset0:181 offset1:183\n"
      "ds_read2st64_b32 a[222:223], v15 offset0:181 offset1:183\n"
      "ds_read2st64_b32 a[224:225], v13 offset0:181 offset1:183\n"
      "ds_read2st64_b32 a[226:227], v16 offset0:181 offset1:183\n"
      "ds_read2st64_b32 a[228:229], v14 offset0:181 offset1:183\n"
      "ds_read2st64_b32 a[230:231], v17 offset0:181 offset1:183\n"
      "s_add_u32 s58, s58, 3072\n"
      "s_waitcnt lgkmcnt(4)\n"
      "buffer_store_dwordx4 a[220:223], v18, s[8:11], s58 offen offset:0 sc1\n"
      "s_waitcnt lgkmcnt(2)\n"
      "buffer_store_dwordx4 a[224:227], v18, s[8:11], s58 offen offset:128 sc1\n"
      "s_waitcnt lgkmcnt(0)\n"
      "buffer_store_dwordx4 a[228:231], v18, s[8:11], s58 offen offset:256 sc1\n"
      "LDend_%=:\n"
      "s_branch Lfin_%=\n"
      ".p2align 6\n"
      "LS_%=:\n"
      "v_lshlrev_b32 v43, 4, v37\n"
      "v_lshlrev_b32 v44, 2, v37\n"
      "v_and_b32 v43, 16, v43\n"
      "v_and_or_b32 v43, v44, 8, v43\n"
      "v_mul_u32_u24 v44, 192, v38\n"
      "v_mul_u32_u24 v45, 1536, v39\n"
      "s_lshl_b32 s27, s18, 5\n"
      "v_lshlrev_b32 v46, 1, v36\n"
      "v_add3_u32 v43, v43, v44, v45\n"
      "v_bitop3_b32 v46, v46, s27, 32 bitop3:0x6c\n"
      "v_add_u32 v95, v43, v46\n"
      "v_add_u32 v96, 57344, v95\n"
      "v_add_u32 v97, 114688, v95\n"
      "s_lshl_b32 s26, s18, 3\n"
      "v_and_b32 v42, 15, v36\n"
      "v_xor_b32 v42, s26, v42\n"
      "s_lshl_b32 s26, s17, 13\n"
      "s_add_u32 s26, s26, 24832\n"
      "s_mul_i32 s27, s17, 12672\n"
      "v_mul_u32_u24 v44, 192, v42\n"
      "v_lshlrev_b32 v43, 3, v42\n"
      "v_and_b32 v45, 48, v36\n"
      "s_add_u32 s27, s27, 82432\n"
      "v_bitop3_b32 v46, v43, v45, 32 bitop3:0x6c\n"
      "v_add3_u32 v0, v46, v44, s26\n"
      "v_add3_u32 v3, v46, v44, s27\n"
      "v_add_u32 v43, 16, v43\n"
      "v_add_u32 v44, 384, v44\n"
      "v_bitop3_b32 v46, v43, v45, 32 bitop3:0x6c\n"
      "v_add3_u32 v1, v46, v44, s26\n"
      "v_add3_u32 v4, v46, v44, s27\n"
      "v_add_u32 v43, 16, v43\n"
      "v_add_u32 v44, 384, v44\n"
      "v_bitop3_b32 v46, v43, v45, 32 bitop3:0x6c\n"
      "v_add3_u32 v2, v46, v44, s26\n"
      "v_add3_u32 v5, v46, v44, s27\n"
      "v_lshlrev_b32 v43, 31, v36\n"
      "v_and_b32 v44, 2, v36\n"
      "v_or_b32 v43, 0x3f803f80, v43\n"
      "v_mul_u32_u24 v45, 0xc00080, v44\n"
      "v_lshl_add_u32 v44, v44, 22, v43\n"
      "v_add_u32 v45, v45, v43\n"
      "v_accvgpr_write_b32 a216, v44\n"
      "v_accvgpr_write_b32 a217, v45\n"
      "v_lshlrev_b32 v43, 3, v38\n"
      "s_mov_b32 s26, 806420736\n"
      "v_bfe_u32 v43, s26, v43, 8\n"
      "v_lshlrev_b32 v44, 12, v38\n"
      "s_lshl_b32 s28, s18, 8\n"
      "s_add_u32 s28, s28, 82432\n"
      "v_lshl_add_u32 v43, v43, 3, v44\n"
      "v_lshl_add_u32 v44, v39, 2, v37\n"
      "v_lshl_add_u32 v43, v44, 4, v43\n"
      "v_add_u32 v7, s28, v43\n"
      "s_mul_i32 s26, s17, 70144\n"
      "s_lshl_b32 s27, s18, 8\n"
      "s_sub_u32 s26, 70144, s26\n"
      "s_add_u32 s26, s26, s27\n"
      "v_lshl_add_u32 v6, v36, 2, s26\n"
      "v_lshrrev_b32 v43, 3, v36\n"
      "v_and_b32 v44, 7, v36\n"
      "v_mul_u32_u24 v45, 384, v43\n"
      "v_lshl_add_u32 v11, v44, 4, v45\n"
      "v_and_b32 v44, 2, v36\n"
      "v_lshlrev_b32 v43, 3, v43\n"
      "v_lshlrev_b32 v45, 4, v44\n"
      "v_xor_b32 v43, v43, v45\n"
      "v_lshl_add_u32 v43, v44, 7, v43\n"
      "v_and_b32 v44, 1, v36\n"
      "s_lshl_b32 s26, s18, 11\n"
      "v_lshl_add_u32 v43, v44, 6, v43\n"
      "s_add_u32 s26, s26, 12288\n"
      "v_bfe_u32 v44, v36, 2, 1\n"
      "v_add_u32 v43, s26, v43\n"
      "v_mul_i32_i24 v45, -4100, v44\n"
      "s_mov_b32 s27, 8200\n"
      "v_add3_u32 v8, v43, v45, s27\n"
      "v_add_u32 v9, 128, v8\n"
      "s_mul_i32 s62, s18, 393216\n"
      "s_add_u32 s62, s62, s19\n"
      "s_add_u32 s62, s62, s21\n"
      "s_sub_u32 s62, s62, 786432\n"
      "s_mov_b32 s58, 0x80000000\n"
      "s_mov_b32 s59, 0x80000000\n"
      "s_and_b32 s9, s9, 0xffff\n"
      "s_mov_b32 s45, 0xbeaaaaab\n"
      "s_mov_b32 s46, 0xbe800000\n"
      "s_mov_b32 s47, 0x3eaaaaab\n"
      "s_add_u32 m0, s63, 12288\n"
      "s_add_u32 s52, s63, 61440\n"
      "buffer_load_dwordx4 v92, s[12:15], s52 offen offset:0 lds\n"
      "buffer_load_dwordx4 v93, s[12:15], s52 offen offset:1024 lds\n"
      "buffer_load_dwordx4 v94, s[12:15], s52 offen offset:2048 lds\n"
      "s_add_u32 m0, s63, 18432\n"
      "s_add_u32 s52, s63, 116736\n"
      "buffer_load_dwordx4 v92, s[12:15], s52 offen offset:0 lds\n"
      "buffer_load_dwordx4 v93, s[12:15], s52 offen offset:1024 lds\n"
      "buffer_load_dwordx4 v94, s[12:15], s52 offen offset:2048 lds\n"
      "LSpro_%=:\n"
      "s_waitcnt vmcnt(6)\n"
      "s_barrier\n"
      "ds_read_b64_tr_b16 a[224:225], v95 offset:24832\n"
      "ds_read_b64_tr_b16 a[226:227], v95 offset:25600\n"
      "ds_read_b64_tr_b16 a[0:1], v95 offset:0\n"
      "ds_read_b64_tr_b16 a[2:3], v95 offset:768\n"
      "ds_read_b64_tr_b16 a[220:221], v95 offset:30976\n"
      "ds_read_b64_tr_b16 a[222:223], v95 offset:31744\n"
      "ds_read_b64_tr_b16 a[232:233], v95 offset:24896\n"
      "ds_read_b64_tr_b16 a[234:235], v95 offset:25664\n"
      "ds_read_b64_tr_b16 a[4:5], v95 offset:64\n"
      "ds_read_b64_tr_b16 a[6:7], v95 offset:832\n"
      "ds_read_b64_tr_b16 a[228:229], v95 offset:31040\n"
      "ds_read_b64_tr_b16 a[230:231], v95 offset:31808\n"
      "s_add_u32 m0, s63, 41216\n"
      "s_add_u32 s52, s63, 12288\n"
      "s_waitcnt lgkmcnt(10)\n"
      "v_mfma_f32_4x4x4_16b_bf16 v[28:31], a[244:245], a[224:225], 0\n"
      "v_mfma_f32_4x4x4_16b_bf16 v[32:35], a[244:245], a[226:227], 0\n"
      "buffer_load_dwordx4 v92, s[12:15], s52 offen offset:0 lds\n"
      "s_waitcnt lgkmcnt(8)\n"
      "v_mfma_f32_4x4x4_16b_bf16 v[28:31], a[248:249], a[0:1], v[28:31]\n"
      "ds_read_b64_tr_b16 a[240:241], v95 offset:24960\n"
      "v_mfma_f32_4x4x4_16b_bf16 v[32:35], a[248:249], a[2:3], v[32:35]\n"
      "buffer_load_dwordx4 v93, s[12:15], s52 offen offset:1024 lds\n"
      "s_waitcnt lgkmcnt(7)\n"
      "v_mfma_f32_4x4x4_16b_bf16 v[36:39], a[246:247], a[220:221], v[28:31]\n"
      "ds_read_b64_tr_b16 a[242:243], v95 offset:25728\n"
      "v_mfma_f32_4x4x4_16b_bf16 v[40:43], a[246:247], a[222:223], v[32:35]\n"
      "buffer_load_dwordx4 v94, s[12:15], s52 offen offset:2048 lds\n"
      "v_mfma_f32_4x4x4_16b_bf16 v[28:31], a[250:251], a[220:221], v[28:31]\n"
      "ds_read_b64_tr_b16 a[8:9], v95 offset:128\n"
      "v_mfma_f32_4x4x4_16b_bf16 v[32:35], a[250:251], a[222:223], v[32:35]\n"
      "ds_read_b64_tr_b16 a[10:11], v95 offset:896\n"
      "ds_read_b64_tr_b16 a[236:237], v95 offset:31104\n"
      "ds_read_b64_tr_b16 a[238:239], v95 offset:31872\n"
      "s_add_u32 m0, s63, 47360\n"
      "s_add_u32 s52, s63, 67584\n"
      "s_waitcnt lgkmcnt(5)\n"
      "v_mfma_f32_4x4x4_16b_bf16 v[76:79], a[244:245], a[232:233], 0\n"
      "v_mfma_f32_4x4x4_16b_bf16 v[80:83], a[244:245], a[234:235], 0\n"
      "buffer_load_dwordx4 v92, s[12:15], s52 offen offset:0 lds\n"
      "v_mfma_f32_4x4x4_16b_bf16 v[76:79], a[248:249], a[4:5], v[76:79]\n"
      "buffer_load_dwordx4 v93, s[12:15], s52 offen offset:1024 lds\n"
      "v_mfma_f32_4x4x4_16b_bf16 v[80:83], a[248:249], a[6:7], v[80:83]\n"
      "buffer_load_dwordx4 v94, s[12:15], s52 offen offset:2048 lds\n"
      "v_mfma_f32_4x4x4_16b_bf16 v[84:87], a[246:247], a[228:229], v[76:79]\n"
      "v_mfma_f32_4x4x4_16b_bf16 v[88:91], a[246:247], a[230:231], v[80:83]\n"
      "v_mfma_f32_4x4x4_16b_bf16 v[76:79], a[250:251], a[228:229], v[76:79]\n"
      "v_mfma_f32_4x4x4_16b_bf16 v[80:83], a[250:251], a[230:231], v[80:83]\n"
      "v_cvt_pk_bf16_f32 v252, v36, v40\n"
      "v_cvt_pk_bf16_f32 v253, v37, v41\n"
      "v_cvt_pk_bf16_f32 v254, v38, v42\n"
      "v_cvt_pk_bf16_f32 v255, v39, v43\n"
      "v_cvt_pk_bf16_f32 v248, v28, v32\n"
      "v_cvt_pk_bf16_f32 v249, v29, v33\n"
      "v_cvt_pk_bf16_f32 v250, v30, v34\n"
      "v_cvt_pk_bf16_f32 v251, v31, v35\n"
      "s_add_u32 m0, s63, 59648\n"
      "s_add_u32 s52, s63, 18432\n"
      "s_waitcnt lgkmcnt(0)\n"
      "v_mfma_f32_4x4x4_16b_bf16 v[28:31], a[244:245], a[240:241], 0\n"
      "v_mfma_f32_4x4x4_16b_bf16 v[32:35], a[244:245], a[242:243], 0\n"
      "buffer_load_dwordx4 v92, s[12:15], s52 offen offset:0 lds\n"
      "v_mfma_f32_4x4x4_16b_bf16 v[28:31], a[248:249], a[8:9], v[28:31]\n"
      "buffer_load_dwordx4 v93, s[12:15], s52 offen offset:1024 lds\n"
      "v_mfma_f32_4x4x4_16b_bf16 v[32:35], a[248:249], a[10:11], v[32:35]\n"
      "buffer_load_dwordx4 v94, s[12:15], s52 offen offset:2048 lds\n"
      "v_mfma_f32_4x4x4_16b_bf16 v[36:39], a[246:247], a[236:237], v[28:31]\n"
      "v_mfma_f32_4x4x4_16b_bf16 v[40:43], a[246:247], a[238:239], v[32:35]\n"
      "v_mfma_f32_4x4x4_16b_bf16 v[28:31], a[250:251], a[236:237], v[28:31]\n"
      "v_mfma_f32_4x4x4_16b_bf16 v[32:35], a[250:251], a[238:239], v[32:35]\n"
      "v_cvt_pk_bf16_f32 v244, v84, v88\n"
      "v_cvt_pk_bf16_f32 v245, v85, v89\n"
      "v_cvt_pk_bf16_f32 v246, v86, v90\n"
      "v_cvt_pk_bf16_f32 v247, v87, v91\n"
      "v_cvt_pk_bf16_f32 v240, v76, v80\n"
      "v_cvt_pk_bf16_f32 v241, v77, v81\n"
      "v_cvt_pk_bf16_f32 v242, v78, v82\n"
      "v_cvt_pk_bf16_f32 v243, v79, v83\n"
      "s_waitcnt vmcnt(9)\n"
      "s_barrier\n"
      "ds_read_b64_tr_b16 a[224:225], v95 offset:6144\n"
      "ds_read_b64_tr_b16 a[226:227], v95 offset:6912\n"
      "ds_read_b64_tr_b16 a[12:13], v95 offset:18432\n"
      "ds_read_b64_tr_b16 a[14:15], v95 offset:19200\n"
      "ds_read_b64_tr_b16 a[220:221], v95 offset:12288\n"
      "ds_read_b64_tr_b16 a[222:223], v95 offset:13056\n"
      "s_add_u32 m0, s63, 65792\n"
      "s_add_u32 s52, s63, 73728\n"
      "s_waitcnt lgkmcnt(4)\n"
      "v_mfma_f32_4x4x4_16b_bf16 v[76:79], a[244:245], a[224:225], 0\n"
      "v_mfma_f32_4x4x4_16b_bf16 v[80:83], a[244:245], a[226:227], 0\n"
      "buffer_load_dwordx4 v92, s[12:15], s52 offen offset:0 lds\n"
      "s_waitcnt lgkmcnt(2)\n"
      "v_mfma_f32_4x4x4_16b_bf16 v[76:79], a[248:249], a[12:13], v[76:79]\n"
      "ds_read_b64_tr_b16 a[232:233], v95 offset:6208\n"
      "v_mfma_f32_4x4x4_16b_bf16 v[80:83], a[248:249], a[14:15], v[80:83]\n"
      "buffer_load_dwordx4 v93, s[12:15], s52 offen offset:1024 lds\n"
      "s_waitcnt lgkmcnt(1)\n"
      "v_mfma_f32_4x4x4_16b_bf16 v[84:87], a[246:247], a[220:221], v[76:79]\n"
      "ds_read_b64_tr_b16 a[234:235], v95 offset:6976\n"
      "v_mfma_f32_4x4x4_16b_bf16 v[88:91], a[246:247], a[222:223], v[80:83]\n"
      "buffer_load_dwordx4 v94, s[12:15], s52 offen offset:2048 lds\n"
      "v_mfma_f32_4x4x4_16b_bf16 v[76:79], a[250:251], a[220:221], v[76:79]\n"
      "ds_read_b64_tr_b16 a[16:17], v95 offset:18496\n"
      "v_mfma_f32_4x4x4_16b_bf16 v[80:83], a[250:251], a[222:223], v[80:83]\n"
      "ds_read_b64_tr_b16 a[18:19], v95 offset:19264\n"
      "ds_read_b64_tr_b16 a[228:229], v95 offset:12352\n"
      "ds_read_b64_tr_b16 a[230:231], v95 offset:13120\n"
      "ds_read_b64_tr_b16 a[240:241], v95 offset:6272\n"
      "ds_read_b64_tr_b16 a[242:243], v95 offset:7040\n"
      "ds_read_b64_tr_b16 a[20:21], v95 offset:18560\n"
      "ds_read_b64_tr_b16 a[22:23], v95 offset:19328\n"
      "ds_read_b64_tr_b16 a[236:237], v95 offset:12416\n"
      "ds_read_b64_tr_b16 a[238:239], v95 offset:13184\n"
      "ds_read_b64_tr_b16 a[188:189], v7 offset:0\n"
      "ds_read_b64_tr_b16 a[190:191], v7 offset:8\n"
      "ds_read_b128 a[192:195], v3 offset:0\n"
      "v_cvt_pk_bf16_f32 v236, v36, v40\n"
      "v_cvt_pk_bf16_f32 v237, v37, v41\n"
      "v_cvt_pk_bf16_f32 v238, v38, v42\n"
      "v_cvt_pk_bf16_f32 v239, v39, v43\n"
      "v_cvt_pk_bf16_f32 v232, v28, v32\n"
      "v_cvt_pk_bf16_f32 v233, v29, v33\n"
      "v_cvt_pk_bf16_f32 v234, v30, v34\n"
      "v_cvt_pk_bf16_f32 v235, v31, v35\n"
      "s_waitcnt vmcnt(6)\n"
      "s_barrier\n"
      "s_add_u32 m0, s63, 116224\n"
      "s_add_u32 s52, s63, 24576\n"
      "s_waitcnt lgkmcnt(7)\n"
      "v_mfma_f32_4x4x4_16b_bf16 v[28:31], a[244:245], a[232:233], 0\n"
      "v_mfma_f32_4x4x4_16b_bf16 v[32:35], a[244:245], a[234:235], 0\n"
      "buffer_load_dwordx4 v92, s[12:15], s52 offen offset:0 lds\n"
      "v_mfma_f32_4x4x4_16b_bf16 v[28:31], a[248:249], a[16:17], v[28:31]\n"
      "ds_read_b64_tr_b16 a[224:225], v95 offset:41216\n"
      "v_mfma_f32_4x4x4_16b_bf16 v[32:35], a[248:249], a[18:19], v[32:35]\n"
      "buffer_load_dwordx4 v93, s[12:15], s52 offen offset:1024 lds\n"
      "v_mfma_f32_4x4x4_16b_bf16 v[36:39], a[246:247], a[228:229], v[28:31]\n"
      "ds_read_b64_tr_b16 a[226:227], v95 offset:41984\n"
      "v_mfma_f32_4x4x4_16b_bf16 v[40:43], a[246:247], a[230:231], v[32:35]\n"
      "buffer_load_dwordx4 v94, s[12:15], s52 offen offset:2048 lds\n"
      "v_mfma_f32_4x4x4_16b_bf16 v[28:31], a[250:251], a[228:229], v[28:31]\n"
      "ds_read_b64_tr_b16 a[24:25], v95 offset:53504\n"
      "v_mfma_f32_4x4x4_16b_bf16 v[32:35], a[250:251], a[230:231], v[32:35]\n"
      "ds_read_b64_tr_b16 a[26:27], v95 offset:54272\n"
      "s_waitcnt lgkmcnt(4)\n"
      "v_mfma_f32_4x4x4_16b_bf16 v[44:47], a[216:217], a[188:189], 0\n"
      "ds_read_b64_tr_b16 a[220:221], v95 offset:47360\n"
      "v_mfma_f32_4x4x4_16b_bf16 v[48:51], a[216:217], a[190:191], 0\n"
      "ds_read_b64_tr_b16 a[222:223], v95 offset:48128\n"
      "ds_read_b64_tr_b16 a[184:185], v7 offset:512\n"
      "ds_read_b64_tr_b16 a[186:187], v7 offset:520\n"
      "ds_read_b128 a[196:199], v3 offset:64\n"
      "v_cvt_pk_bf16_f32 v228, v84, v88\n"
      "v_cvt_pk_bf16_f32 v229, v85, v89\n"
      "v_cvt_pk_bf16_f32 v230, v86, v90\n"
      "v_cvt_pk_bf16_f32 v231, v87, v91\n"
      "v_cvt_pk_bf16_f32 v224, v76, v80\n"
      "v_cvt_pk_bf16_f32 v225, v77, v81\n"
      "v_cvt_pk_bf16_f32 v226, v78, v82\n"
      "v_cvt_pk_bf16_f32 v227, v79, v83\n"
      "s_add_u32 m0, s63, 122368\n"
      "s_add_u32 s52, s63, 79872\n"
      "v_mfma_f32_4x4x4_16b_bf16 v[76:79], a[244:245], a[240:241], 0\n"
      "v_mfma_f32_4x4x4_16b_bf16 v[80:83], a[244:245], a[242:243], 0\n"
      "buffer_load_dwordx4 v92, s[12:15], s52 offen offset:0 lds\n"
      "v_mfma_f32_4x4x4_16b_bf16 v[76:79], a[248:249], a[20:21], v[76:79]\n"
      "ds_read_b64_tr_b16 a[232:233], v95 offset:41280\n"
      "v_mfma_f32_4x4x4_16b_bf16 v[80:83], a[248:249], a[22:23], v[80:83]\n"
      "buffer_load_dwordx4 v93, s[12:15], s52 offen offset:1024 lds\n"
      "v_mfma_f32_4x4x4_16b_bf16 v[84:87], a[246:247], a[236:237], v[76:79]\n"
      "ds_read_b64_tr_b16 a[234:235], v95 offset:42048\n"
      "v_mfma_f32_4x4x4_16b_bf16 v[88:91], a[246:247], a[238:239], v[80:83]\n"
      "buffer_load_dwordx4 v94, s[12:15], s52 offen offset:2048 lds\n"
      "v_mfma_f32_4x4x4_16b_bf16 v[76:79], a[250:251], a[236:237], v[76:79]\n"
      "ds_read_b64_tr_b16 a[28:29], v95 offset:53568\n"
      "v_mfma_f32_4x4x4_16b_bf16 v[80:83], a[250:251], a[238:239], v[80:83]\n"
      "ds_read_b64_tr_b16 a[30:31], v95 offset:54336\n"
      "s_nop 0\n"
      "s_waitcnt lgkmcnt(4)\n"
      "v_mfma_f32_4x4x4_16b_bf16 v[112:115], a[216:217], a[184:185], 0\n"
      "ds_read_b64_tr_b16 a[228:229], v95 offset:47424\n"
      "v_mfma_f32_4x4x4_16b_bf16 v[116:119], a[216:217], a[186:187], 0\n"
      "ds_read_b64_tr_b16 a[230:231], v95 offset:48192\n"
      "ds_read_b64_tr_b16 a[180:181], v7 offset:1024\n"
      "ds_read_b64_tr_b16 a[182:183], v7 offset:1032\n"
      "v_cvt_pk_bf16_f32 v220, v36, v40\n"
      "v_cvt_pk_bf16_f32 v221, v37, v41\n"
      "v_cvt_pk_bf16_f32 v222, v38, v42\n"
      "v_cvt_pk_bf16_f32 v223, v39, v43\n"
      "v_cvt_pk_bf16_f32 v216, v28, v32\n"
      "v_cvt_pk_bf16_f32 v217, v29, v33\n"
      "v_mfma_f32_16x16x32_bf16 v[100:103], a[0:3], a[192:195], 0\n"
      "v_cvt_pk_bf16_f32 v218, v30, v34\n"
      "v_cvt_pk_bf16_f32 v219, v31, v35\n"
      "v_mfma_f32_16x16x32_bf16 v[104:107], a[4:7], a[192:195], 0\n"
      "v_cvt_pk_bf16_f32 v44, v44, v48\n"
      "v_cvt_pk_bf16_f32 v45, v45, v49\n"
      "v_mfma_f32_16x16x32_bf16 v[108:111], a[8:11], a[192:195], 0\n"
      "s_add_u32 m0, s63, 134656\n"
      "v_cvt_pk_bf16_f32 v46, v46, v50\n"
      "v_cvt_pk_bf16_f32 v47, v47, v51\n"
      "s_add_u32 s52, s63, 30720\n"
      "v_mfma_f32_4x4x4_16b_bf16 v[28:31], a[244:245], a[224:225], 0\n"
      "v_mfma_f32_4x4x4_16b_bf16 v[32:35], a[244:245], a[226:227], 0\n"
      "buffer_load_dwordx4 v92, s[12:15], s52 offen offset:0 lds\n"
      "v_mfma_f32_4x4x4_16b_bf16 v[28:31], a[248:249], a[24:25], v[28:31]\n"
      "ds_read_b64_tr_b16 a[240:241], v95 offset:41344\n"
      "v_mfma_f32_4x4x4_16b_bf16 v[32:35], a[248:249], a[26:27], v[32:35]\n"
      "buffer_load_dwordx4 v93, s[12:15], s52 offen offset:1024 lds\n"
      "v_mfma_f32_4x4x4_16b_bf16 v[36:39], a[246:247], a[220:221], v[28:31]\n"
      "ds_read_b64_tr_b16 a[242:243], v95 offset:42112\n"
      "v_mfma_f32_4x4x4_16b_bf16 v[40:43], a[246:247], a[222:223], v[32:35]\n"
      "buffer_load_dwordx4 v94, s[12:15], s52 offen offset:2048 lds\n"
      "v_mfma_f32_4x4x4_16b_bf16 v[28:31], a[250:251], a[220:221], v[28:31]\n"
      "ds_read_b64_tr_b16 a[32:33], v95 offset:53632\n"
      "v_mfma_f32_4x4x4_16b_bf16 v[32:35], a[250:251], a[222:223], v[32:35]\n"
      "ds_read_b64_tr_b16 a[34:35], v95 offset:54400\n"
      "s_nop 0\n"
      "s_waitcnt lgkmcnt(4)\n"
      "v_mfma_f32_4x4x4_16b_bf16 v[20:23], a[216:217], a[180:181], 0\n"
      "ds_read_b64_tr_b16 a[236:237], v95 offset:47488\n"
      "v_mfma_f32_4x4x4_16b_bf16 v[24:27], a[216:217], a[182:183], 0\n"
      "ds_read_b64_tr_b16 a[238:239], v95 offset:48256\n"
      "ds_read_b64_tr_b16 a[176:177], v7 offset:1536\n"
      "ds_read_b64_tr_b16 a[178:179], v7 offset:1544\n"
      "s_mov_b32 m0, s64\n"
      "s_nop 0\n"
      "ds_write_addtid_b32 v44 offset:24832\n"
      "ds_write_addtid_b32 v45 offset:28928\n"
      "ds_write_addtid_b32 v46 offset:33024\n"
      "ds_write_addtid_b32 v47 offset:37120\n"
      "v_cvt_pk_bf16_f32 v212, v84, v88\n"
      "v_cvt_pk_bf16_f32 v213, v85, v89\n"
      "v_cvt_pk_bf16_f32 v214, v86, v90\n"
      "v_cvt_pk_bf16_f32 v215, v87, v91\n"
      "v_cvt_pk_bf16_f32 v208, v76, v80\n"
      "v_cvt_pk_bf16_f32 v209, v77, v81\n"
      "v_mfma_f32_16x16x32_bf16 v[100:103], a[12:15], a[196:199], v[100:103]\n"
      "v_cvt_pk_bf16_f32 v210, v78, v82\n"
      "v_cvt_pk_bf16_f32 v211, v79, v83\n"
      "v_mfma_f32_16x16x32_bf16 v[104:107], a[16:19], a[196:199], v[104:107]\n"
      "v_cvt_pk_bf16_f32 v112, v112, v116\n"
      "v_cvt_pk_bf16_f32 v113, v113, v117\n"
      "v_mfma_f32_16x16x32_bf16 v[108:111], a[20:23], a[196:199], v[108:111]\n"
      "s_waitcnt vmcnt(9)\n"
      "v_cvt_pk_bf16_f32 v114, v114, v118\n"
      "v_cvt_pk_bf16_f32 v115, v115, v119\n"
      "s_barrier\n"
      "s_add_u32 m0, s63, 140800\n"
      "s_add_u32 s52, s63, 86016\n"
      "v_mfma_f32_4x4x4_16b_bf16 v[76:79], a[244:245], a[232:233], 0\n"
      "v_mfma_f32_4x4x4_16b_bf16 v[80:83], a[244:245], a[234:235], 0\n"
      "buffer_load_dwordx4 v92, s[12:15], s52 offen offset:0 lds\n"
      "v_mfma_f32_4x4x4_16b_bf16 v[76:79], a[248:249], a[28:29], v[76:79]\n"
      "ds_read_b64_tr_b16 a[224:225], v96 offset:2304\n"
      "v_mfma_f32_4x4x4_16b_bf16 v[80:83], a[248:249], a[30:31], v[80:83]\n"
      "buffer_load_dwordx4 v93, s[12:15], s52 offen offset:1024 lds\n"
      "v_mfma_f32_4x4x4_16b_bf16 v[84:87], a[246:247], a[228:229], v[76:79]\n"
      "ds_read_b64_tr_b16 a[226:227], v96 offset:3072\n"
      "v_mfma_f32_4x4x4_16b_bf16 v[88:91], a[246:247], a[230:231], v[80:83]\n"
      "buffer_load_dwordx4 v94, s[12:15], s52 offen offset:2048 lds\n"
      "v_mfma_f32_4x4x4_16b_bf16 v[76:79], a[250:251], a[228:229], v[76:79]\n"
      "ds_read_b64_tr_b16 a[36:37], v96 offset:14592\n"
      "v_mfma_f32_4x4x4_16b_bf16 v[80:83], a[250:251], a[230:231], v[80:83]\n"
      "ds_read_b64_tr_b16 a[38:39], v96 offset:15360\n"
      "s_waitcnt lgkmcnt(4)\n"
      "v_mfma_f32_4x4x4_16b_bf16 v[12:15], a[216:217], a[176:177], 0\n"
      "ds_read_b64_tr_b16 a[220:221], v96 offset:8448\n"
      "v_mfma_f32_4x4x4_16b_bf16 v[16:19], a[216:217], a[178:179], 0\n"
      "ds_read_b64_tr_b16 a[222:223], v96 offset:9216\n"
      "s_mov_b32 m0, s64\n"
      "s_nop 0\n"
      "s_nop 0\n"
      "ds_write_addtid_b32 v112 offset:25344\n"
      "ds_write_addtid_b32 v113 offset:29440\n"
      "ds_write_addtid_b32 v114 offset:33536\n"
      "ds_write_addtid_b32 v115 offset:37632\n"
      "ds_read_b128 a[200:203], v3 offset:128\n"
      "v_cvt_pk_bf16_f32 v204, v36, v40\n"
      "v_cvt_pk_bf16_f32 v205, v37, v41\n"
      "v_cvt_pk_bf16_f32 v206, v38, v42\n"
      "v_cvt_pk_bf16_f32 v207, v39, v43\n"
      "v_cvt_pk_bf16_f32 v200, v28, v32\n"
      "v_cvt_pk_bf16_f32 v201, v29, v33\n"
      "v_cvt_pk_bf16_f32 v202, v30, v34\n"
      "v_cvt_pk_bf16_f32 v203, v31, v35\n"
      "v_cvt_pk_bf16_f32 v20, v20, v24\n"
      "v_cvt_pk_bf16_f32 v21, v21, v25\n"
      "v_cvt_pk_bf16_f32 v22, v22, v26\n"
      "v_cvt_pk_bf16_f32 v23, v23, v27\n"
      "s_add_u32 m0, s63, 6144\n"
      "s_add_u32 s52, s63, 36864\n"
      "v_mfma_f32_4x4x4_16b_bf16 v[28:31], a[244:245], a[240:241], 0\n"
      "v_mfma_f32_4x4x4_16b_bf16 v[32:35], a[244:245], a[242:243], 0\n"
      "buffer_load_dwordx4 v92, s[12:15], s52 offen offset:0 lds\n"
      "v_mfma_f32_4x4x4_16b_bf16 v[28:31], a[248:249], a[32:33], v[28:31]\n"
      "ds_read_b64_tr_b16 a[232:233], v96 offset:2368\n"
      "v_mfma_f32_4x4x4_16b_bf16 v[32:35], a[248:249], a[34:35], v[32:35]\n"
      "buffer_load_dwordx4 v93, s[12:15], s52 offen offset:1024 lds\n"
      "v_mfma_f32_4x4x4_16b_bf16 v[36:39], a[246:247], a[236:237], v[28:31]\n"
      "ds_read_b64_tr_b16 a[234:235], v96 offset:3136\n"
      "v_mfma_f32_4x4x4_16b_bf16 v[40:43], a[246:247], a[238:239], v[32:35]\n"
      "ds_read_b64_tr_b16 a[40:41], v96 offset:14656\n"
      "v_mfma_f32_4x4x4_16b_bf16 v[28:31], a[250:251], a[236:237], v[28:31]\n"
      "ds_read_b64_tr_b16 a[42:43], v96 offset:15424\n"
      "v_mfma_f32_4x4x4_16b_bf16 v[32:35], a[250:251], a[238:239], v[32:35]\n"
      "ds_read_b64_tr_b16 a[228:229], v96 offset:8512\n"
      "ds_read_b64_tr_b16 a[230:231], v96 offset:9280\n"
      "s_mov_b32 m0, s64\n"
      "s_nop 0\n"
      "ds_write_addtid_b32 v20 offset:25856\n"
      "ds_write_addtid_b32 v21 offset:29952\n"
      "ds_write_addtid_b32 v22 offset:34048\n"
      "ds_write_addtid_b32 v23 offset:38144\n"
      "v_cvt_pk_bf16_f32 v196, v84, v88\n"
      "v_cvt_pk_bf16_f32 v197, v85, v89\n"
      "v_cvt_pk_bf16_f32 v198, v86, v90\n"
      "v_cvt_pk_bf16_f32 v199, v87, v91\n"
      "v_cvt_pk_bf16_f32 v192, v76, v80\n"
      "v_cvt_pk_bf16_f32 v193, v77, v81\n"
      "v_cvt_pk_bf16_f32 v194, v78, v82\n"
      "v_cvt_pk_bf16_f32 v195, v79, v83\n"
      "v_cvt_pk_bf16_f32 v12, v12, v16\n"
      "v_cvt_pk_bf16_f32 v13, v13, v17\n"
      "v_cvt_pk_bf16_f32 v14, v14, v18\n"
      "v_cvt_pk_bf16_f32 v15, v15, v19\n"
      "s_waitcnt lgkmcnt(4)\n"
      "v_mfma_f32_4x4x4_16b_bf16 v[76:79], a[244:245], a[224:225], 0\n"
      "v_mfma_f32_4x4x4_16b_bf16 v[80:83], a[244:245], a[226:227], 0\n"
      "s_add_u32 m0, s63, 6144\n"
      "s_nop 0\n"
      "buffer_load_dwordx4 v94, s[12:15], s52 offen offset:2048 lds\n"
      "v_mfma_f32_4x4x4_16b_bf16 v[76:79], a[248:249], a[36:37], v[76:79]\n"
      "ds_read_b64_tr_b16 a[240:241], v96 offset:2432\n"
      "v_mfma_f32_4x4x4_16b_bf16 v[80:83], a[248:249], a[38:39], v[80:83]\n"
      "s_add_u32 m0, s63, 12288\n"
      "v_mfma_f32_4x4x4_16b_bf16 v[84:87], a[246:247], a[220:221], v[76:79]\n"
      "ds_read_b64_tr_b16 a[242:243], v96 offset:3200\n"
      "v_mfma_f32_4x4x4_16b_bf16 v[88:91], a[246:247], a[222:223], v[80:83]\n"
      "s_add_u32 s52, s63, 92160\n"
      "v_mfma_f32_4x4x4_16b_bf16 v[76:79], a[250:251], a[220:221], v[76:79]\n"
      "ds_read_b64_tr_b16 a[44:45], v96 offset:14720\n"
      "v_mfma_f32_4x4x4_16b_bf16 v[80:83], a[250:251], a[222:223], v[80:83]\n"
      "buffer_load_dwordx4 v92, s[12:15], s52 offen offset:0 lds\n"
      "ds_read_b64_tr_b16 a[46:47], v96 offset:15488\n"
      "ds_read_b64_tr_b16 a[236:237], v96 offset:8576\n"
      "ds_read_b64_tr_b16 a[238:239], v96 offset:9344\n"
      "s_mov_b32 m0, s64\n"
      "s_nop 0\n"
      "ds_write_addtid_b32 v12 offset:26368\n"
      "ds_write_addtid_b32 v13 offset:30464\n"
      "ds_write_addtid_b32 v14 offset:34560\n"
      "ds_write_addtid_b32 v15 offset:38656\n"
      "v_cvt_pk_bf16_f32 v188, v36, v40\n"
      "v_cvt_pk_bf16_f32 v189, v37, v41\n"
      "v_mfma_f32_16x16x32_bf16 v[100:103], a[24:27], a[200:203], v[100:103]\n"
      "v_cvt_pk_bf16_f32 v190, v38, v42\n"
      "v_cvt_pk_bf16_f32 v191, v39, v43\n"
      "v_mfma_f32_16x16x32_bf16 v[104:107], a[28:31], a[200:203], v[104:107]\n"
      "s_waitcnt vmcnt(10)\n"
      "v_cvt_pk_bf16_f32 v184, v28, v32\n"
      "v_cvt_pk_bf16_f32 v185, v29, v33\n"
      "v_mfma_f32_16x16x32_bf16 v[108:111], a[32:35], a[200:203], v[108:111]\n"
      "s_waitcnt lgkmcnt(0)\n"
      "v_cvt_pk_bf16_f32 v186, v30, v34\n"
      "v_cvt_pk_bf16_f32 v187, v31, v35\n"
      "s_barrier\n"
      "v_mfma_f32_4x4x4_16b_bf16 v[28:31], a[244:245], a[232:233], 0\n"
      "v_mfma_f32_4x4x4_16b_bf16 v[32:35], a[244:245], a[234:235], 0\n"
      "s_add_u32 m0, s63, 12288\n"
      "s_nop 0\n"
      "buffer_load_dwordx4 v93, s[12:15], s52 offen offset:1024 lds\n"
      "v_mfma_f32_4x4x4_16b_bf16 v[28:31], a[248:249], a[40:41], v[28:31]\n"
      "ds_read_b64_tr_b16 a[224:225], v97 offset:1536\n"
      "v_mfma_f32_4x4x4_16b_bf16 v[32:35], a[248:249], a[42:43], v[32:35]\n"
      "buffer_load_dwordx4 v94, s[12:15], s52 offen offset:2048 lds\n"
      "v_mfma_f32_4x4x4_16b_bf16 v[36:39], a[246:247], a[228:229], v[28:31]\n"
      "ds_read_b64_tr_b16 a[226:227], v97 offset:2304\n"
      "v_mfma_f32_4x4x4_16b_bf16 v[40:43], a[246:247], a[230:231], v[32:35]\n"
      "ds_read_b64_tr_b16 a[48:49], v97 offset:13824\n"
      "v_mfma_f32_4x4x4_16b_bf16 v[28:31], a[250:251], a[228:229], v[28:31]\n"
      "ds_read_b64_tr_b16 a[50:51], v97 offset:14592\n"
      "v_mfma_f32_4x4x4_16b_bf16 v[32:35], a[250:251], a[230:231], v[32:35]\n"
      "ds_read_b64_tr_b16 a[220:221], v97 offset:7680\n"
      "ds_read_b64_tr_b16 a[222:223], v97 offset:8448\n"
      "ds_read_b128 a[204:207], v4 offset:0\n"
      "ds_read_b128 a[208:211], v0 offset:0\n"
      "ds_read_b128 a[212:215], v0 offset:4096\n"
      "v_cvt_pk_bf16_f32 v180, v84, v88\n"
      "v_cvt_pk_bf16_f32 v181, v85, v89\n"
      "v_cvt_pk_bf16_f32 v182, v86, v90\n"
      "v_cvt_pk_bf16_f32 v183, v87, v91\n"
      "v_cvt_pk_bf16_f32 v176, v76, v80\n"
      "v_cvt_pk_bf16_f32 v177, v77, v81\n"
      "v_cvt_pk_bf16_f32 v178, v78, v82\n"
      "v_cvt_pk_bf16_f32 v179, v79, v83\n"
      "s_add_u32 m0, s63, 41216\n"
      "s_add_u32 s52, s63, 43008\n"
      "v_mfma_f32_4x4x4_16b_bf16 v[76:79], a[244:245], a[240:241], 0\n"
      "v_mfma_f32_4x4x4_16b_bf16 v[80:83], a[244:245], a[242:243], 0\n"
      "buffer_load_dwordx4 v92, s[12:15], s52 offen offset:0 lds\n"
      "v_mfma_f32_4x4x4_16b_bf16 v[76:79], a[248:249], a[44:45], v[76:79]\n"
      "ds_read_b64_tr_b16 a[232:233], v97 offset:1600\n"
      "v_mfma_f32_4x4x4_16b_bf16 v[80:83], a[248:249], a[46:47], v[80:83]\n"
      "buffer_load_dwordx4 v93, s[12:15], s52 offen offset:1024 lds\n"
      "v_mfma_f32_4x4x4_16b_bf16 v[84:87], a[246:247], a[236:237], v[76:79]\n"
      "ds_read_b64_tr_b16 a[234:235], v97 offset:2368\n"
      "v_mfma_f32_4x4x4_16b_bf16 v[88:91], a[246:247], a[238:239], v[80:83]\n"
      "ds_read_b64_tr_b16 a[52:53], v97 offset:13888\n"
      "v_mfma_f32_4x4x4_16b_bf16 v[76:79], a[250:251], a[236:237], v[76:79]\n"
      "ds_read_b64_tr_b16 a[54:55], v97 offset:14656\n"
      "v_mfma_f32_4x4x4_16b_bf16 v[80:83], a[250:251], a[238:239], v[80:83]\n"
      "ds_read_b64_tr_b16 a[228:229], v97 offset:7744\n"
      "ds_read_b64_tr_b16 a[230:231], v97 offset:8512\n"
      "ds_read_b128 a[192:195], v0 offset:64\n"
      "ds_read_b128 a[196:199], v0 offset:4160\n"
      "ds_read_b128 a[200:203], v0 offset:128\n"
      "v_cvt_pk_bf16_f32 v172, v36, v40\n"
      "v_cvt_pk_bf16_f32 v173, v37, v41\n"
      "v_cvt_pk_bf16_f32 v174, v38, v42\n"
      "v_cvt_pk_bf16_f32 v175, v39, v43\n"
      "v_cvt_pk_bf16_f32 v168, v28, v32\n"
      "v_cvt_pk_bf16_f32 v169, v29, v33\n"
      "v_cvt_pk_bf16_f32 v170, v30, v34\n"
      "v_cvt_pk_bf16_f32 v171, v31, v35\n"
      "s_nop 0\n"
      "s_waitcnt lgkmcnt(6)\n"
      "v_mfma_f32_4x4x4_16b_bf16 v[28:31], a[244:245], a[224:225], 0\n"
      "v_mfma_f32_4x4x4_16b_bf16 v[32:35], a[244:245], a[226:227], 0\n"
      "buffer_load_dwordx4 v94, s[12:15], s52 offen offset:2048 lds\n"
      "v_mfma_f32_4x4x4_16b_bf16 v[28:31], a[248:249], a[48:49], v[28:31]\n"
      "ds_read_b64_tr_b16 a[240:241], v97 offset:1664\n"
      "v_mfma_f32_4x4x4_16b_bf16 v[32:35], a[248:249], a[50:51], v[32:35]\n"
      "s_add_u32 m0, s63, 47360\n"
      "v_mfma_f32_4x4x4_16b_bf16 v[36:39], a[246:247], a[220:221], v[28:31]\n"
      "ds_read_b64_tr_b16 a[242:243], v97 offset:2432\n"
      "v_mfma_f32_4x4x4_16b_bf16 v[40:43], a[246:247], a[222:223], v[32:35]\n"
      "s_add_u32 s52, s63, 98304\n"
      "v_mfma_f32_4x4x4_16b_bf16 v[28:31], a[250:251], a[220:221], v[28:31]\n"
      "ds_read_b64_tr_b16 a[56:57], v97 offset:13952\n"
      "v_mfma_f32_4x4x4_16b_bf16 v[32:35], a[250:251], a[222:223], v[32:35]\n"
      "buffer_load_dwordx4 v92, s[12:15], s52 offen offset:0 lds\n"
      "ds_read_b64_tr_b16 a[58:59], v97 offset:14720\n"
      "ds_read_b64_tr_b16 a[236:237], v97 offset:7808\n"
      "ds_read_b64_tr_b16 a[238:239], v97 offset:8576\n"
      "v_mfma_f32_16x16x32_bf16 v[100:103], a[36:39], a[204:207], v[100:103]\n"
      "v_cvt_pk_bf16_f32 v164, v84, v88\n"
      "v_cvt_pk_bf16_f32 v165, v85, v89\n"
      "v_mfma_f32_16x16x32_bf16 v[104:107], a[40:43], a[204:207], v[104:107]\n"
      "v_cvt_pk_bf16_f32 v166, v86, v90\n"
      "v_cvt_pk_bf16_f32 v167, v87, v91\n"
      "v_mfma_f32_16x16x32_bf16 v[108:111], a[44:47], a[204:207], v[108:111]\n"
      "s_nop 0\n"
      "v_cvt_pk_bf16_f32 v160, v76, v80\n"
      "v_cvt_pk_bf16_f32 v161, v77, v81\n"
      "v_mfma_f32_16x16x32_bf16 v[52:55], v[252:255], a[208:211], 0\n"
      "s_waitcnt vmcnt(10)\n"
      "v_cvt_pk_bf16_f32 v162, v78, v82\n"
      "v_cvt_pk_bf16_f32 v163, v79, v83\n"
      "s_barrier\n"
      "v_mfma_f32_4x4x4_16b_bf16 v[76:79], a[244:245], a[232:233], 0\n"
      "v_mfma_f32_4x4x4_16b_bf16 v[80:83], a[244:245], a[234:235], 0\n"
      "buffer_load_dwordx4 v93, s[12:15], s52 offen offset:1024 lds\n"
      "s_waitcnt lgkmcnt(0)\n"
      "v_mfma_f32_4x4x4_16b_bf16 v[76:79], a[248:249], a[52:53], v[76:79]\n"
      "ds_read_b64_tr_b16 a[224:225], v97 offset:19968\n"
      "v_mfma_f32_4x4x4_16b_bf16 v[80:83], a[248:249], a[54:55], v[80:83]\n"
      "buffer_load_dwordx4 v94, s[12:15], s52 offen offset:2048 lds\n"
      "v_mfma_f32_4x4x4_16b_bf16 v[84:87], a[246:247], a[228:229], v[76:79]\n"
      "ds_read_b64_tr_b16 a[226:227], v97 offset:20736\n"
      "v_mfma_f32_4x4x4_16b_bf16 v[88:91], a[246:247], a[230:231], v[80:83]\n"
      "ds_read_b64_tr_b16 a[60:61], v95 offset:0\n"
      "v_mfma_f32_4x4x4_16b_bf16 v[76:79], a[250:251], a[228:229], v[76:79]\n"
      "ds_read_b64_tr_b16 a[62:63], v95 offset:768\n"
      "v_mfma_f32_4x4x4_16b_bf16 v[80:83], a[250:251], a[230:231], v[80:83]\n"
      "ds_read_b64_tr_b16 a[220:221], v97 offset:26112\n"
      "ds_read_b64_tr_b16 a[222:223], v97 offset:26880\n"
      "ds_read_b128 a[204:207], v0 offset:4224\n"
      "v_mfma_f32_16x16x32_bf16 v[56:59], v[244:247], a[208:211], 0\n"
      "v_cvt_pk_bf16_f32 v156, v36, v40\n"
      "v_cvt_pk_bf16_f32 v157, v37, v41\n"
      "v_mfma_f32_16x16x32_bf16 v[60:63], v[236:239], a[208:211], 0\n"
      "v_cvt_pk_bf16_f32 v158, v38, v42\n"
      "v_cvt_pk_bf16_f32 v159, v39, v43\n"
      "v_mfma_f32_16x16x32_bf16 v[64:67], v[248:251], a[212:215], 0\n"
      "v_cvt_pk_bf16_f32 v152, v28, v32\n"
      "v_cvt_pk_bf16_f32 v153, v29, v33\n"
      "v_mfma_f32_16x16x32_bf16 v[68:71], v[240:243], a[212:215], 0\n"
      "s_add_u32 m0, s63, 59648\n"
      "v_cvt_pk_bf16_f32 v154, v30, v34\n"
      "v_cvt_pk_bf16_f32 v155, v31, v35\n"
      "s_add_u32 s52, s63, 49152\n"
      "v_mfma_f32_4x4x4_16b_bf16 v[28:31], a[244:245], a[240:241], 0\n"
      "v_mfma_f32_4x4x4_16b_bf16 v[32:35], a[244:245], a[242:243], 0\n"
      "buffer_load_dwordx4 v92, s[12:15], s52 offen offset:0 lds\n"
      "v_mfma_f32_4x4x4_16b_bf16 v[28:31], a[248:249], a[56:57], v[28:31]\n"
      "ds_read_b64_tr_b16 a[232:233], v97 offset:20032\n"
      "v_mfma_f32_4x4x4_16b_bf16 v[32:35], a[248:249], a[58:59], v[32:35]\n"
      "buffer_load_dwordx4 v93, s[12:15], s52 offen offset:1024 lds\n"
      "v_mfma_f32_4x4x4_16b_bf16 v[36:39], a[246:247], a[236:237], v[28:31]\n"
      "ds_read_b64_tr_b16 a[234:235], v97 offset:20800\n"
      "v_mfma_f32_4x4x4_16b_bf16 v[40:43], a[246:247], a[238:239], v[32:35]\n"
      "ds_read_b64_tr_b16 a[64:65], v95 offset:64\n"
      "v_mfma_f32_4x4x4_16b_bf16 v[28:31], a[250:251], a[236:237], v[28:31]\n"
      "ds_read_b64_tr_b16 a[66:67], v95 offset:832\n"
      "v_mfma_f32_4x4x4_16b_bf16 v[32:35], a[250:251], a[238:239], v[32:35]\n"
      "ds_read_b64_tr_b16 a[228:229], v97 offset:26176\n"
      "ds_read_b64_tr_b16 a[230:231], v97 offset:26944\n"
      "ds_read_b128 a[208:211], v1 offset:0\n"
      "v_mfma_f32_16x16x32_bf16 v[72:75], v[232:235], a[212:215], 0\n"
      "s_nop 0\n"
      "v_cvt_pk_bf16_f32 v148, v84, v88\n"
      "v_cvt_pk_bf16_f32 v149, v85, v89\n"
      "v_mfma_f32_16x16x32_bf16 v[52:55], v[228:231], a[192:195], v[52:55]\n"
      "s_nop 0\n"
      "v_cvt_pk_bf16_f32 v150, v86, v90\n"
      "v_cvt_pk_bf16_f32 v151, v87, v91\n"
      "v_mfma_f32_16x16x32_bf16 v[56:59], v[220:223], a[192:195], v[56:59]\n"
      "s_nop 0\n"
      "v_cvt_pk_bf16_f32 v144, v76, v80\n"
      "v_cvt_pk_bf16_f32 v145, v77, v81\n"
      "v_mfma_f32_16x16x32_bf16 v[60:63], v[212:215], a[192:195], v[60:63]\n"
      "s_waitcnt lgkmcnt(2)\n"
      "v_cvt_pk_bf16_f32 v146, v78, v82\n"
      "v_cvt_pk_bf16_f32 v147, v79, v83\n"
      "v_mfma_f32_4x4x4_16b_bf16 v[76:79], a[244:245], a[224:225], 0\n"
      "v_mfma_f32_4x4x4_16b_bf16 v[80:83], a[244:245], a[226:227], 0\n"
      "buffer_load_dwordx4 v94, s[12:15], s52 offen offset:2048 lds\n"
      "v_mfma_f32_4x4x4_16b_bf16 v[76:79], a[248:249], a[60:61], v[76:79]\n"
      "ds_read_b64_tr_b16 a[240:241], v97 offset:20096\n"
      "v_mfma_f32_4x4x4_16b_bf16 v[80:83], a[248:249], a[62:63], v[80:83]\n"
      "s_add_u32 m0, s63, 65792\n"
      "v_mfma_f32_4x4x4_16b_bf16 v[84:87], a[246:247], a[220:221], v[76:79]\n"
      "ds_read_b64_tr_b16 a[242:243], v97 offset:20864\n"
      "v_mfma_f32_4x4x4_16b_bf16 v[88:91], a[246:247], a[222:223], v[80:83]\n"
      "s_add_u32 s52, s63, 104448\n"
      "v_mfma_f32_4x4x4_16b_bf16 v[76:79], a[250:251], a[220:221], v[76:79]\n"
      "ds_read_b64_tr_b16 a[68:69], v95 offset:128\n"
      "v_mfma_f32_4x4x4_16b_bf16 v[80:83], a[250:251], a[222:223], v[80:83]\n"
      "buffer_load_dwordx4 v92, s[12:15], s52 offen offset:0 lds\n"
      "ds_read_b64_tr_b16 a[70:71], v95 offset:896\n"
      "ds_read_b64_tr_b16 a[236:237], v97 offset:26240\n"
      "ds_read_b64_tr_b16 a[238:239], v97 offset:27008\n"
      "ds_read_b128 a[212:215], v1 offset:4096\n"
      "ds_read_b128 a[192:195], v4 offset:64\n"
      "v_mfma_f32_16x16x32_bf16 v[64:67], v[224:227], a[196:199], v[64:67]\n"
      "v_cvt_pk_bf16_f32 v140, v36, v40\n"
      "v_cvt_pk_bf16_f32 v141, v37, v41\n"
      "v_mfma_f32_16x16x32_bf16 v[68:71], v[216:219], a[196:199], v[68:71]\n"
      "s_nop 0\n"
      "v_cvt_pk_bf16_f32 v142, v38, v42\n"
      "v_cvt_pk_bf16_f32 v143, v39, v43\n"
      "v_mfma_f32_16x16x32_bf16 v[72:75], v[208:211], a[196:199], v[72:75]\n"
      "s_nop 0\n"
      "v_cvt_pk_bf16_f32 v136, v28, v32\n"
      "v_cvt_pk_bf16_f32 v137, v29, v33\n"
      "v_mfma_f32_16x16x32_bf16 v[52:55], v[204:207], a[200:203], v[52:55]\n"
      "s_waitcnt vmcnt(10)\n"
      "v_cvt_pk_bf16_f32 v138, v30, v34\n"
      "v_cvt_pk_bf16_f32 v139, v31, v35\n"
      "s_barrier\n"
      "v_mfma_f32_4x4x4_16b_bf16 v[28:31], a[244:245], a[232:233], 0\n"
      "v_mfma_f32_4x4x4_16b_bf16 v[32:35], a[244:245], a[234:235], 0\n"
      "buffer_load_dwordx4 v93, s[12:15], s52 offen offset:1024 lds\n"
      "v_mfma_f32_4x4x4_16b_bf16 v[28:31], a[248:249], a[64:65], v[28:31]\n"
      "ds_read_b64_tr_b16 a[224:225], v95 offset:6144\n"
      "v_mfma_f32_4x4x4_16b_bf16 v[32:35], a[248:249], a[66:67], v[32:35]\n"
      "buffer_load_dwordx4 v94, s[12:15], s52 offen offset:2048 lds\n"
      "s_nop 0\n"
      "s_waitcnt lgkmcnt(1)\n"
      "v_mfma_f32_4x4x4_16b_bf16 v[36:39], a[246:247], a[228:229], v[28:31]\n"
      "ds_read_b64_tr_b16 a[226:227], v95 offset:6912\n"
      "v_mfma_f32_4x4x4_16b_bf16 v[40:43], a[246:247], a[230:231], v[32:35]\n"
      "ds_read_b64_tr_b16 a[72:73], v95 offset:18432\n"
      "v_mfma_f32_4x4x4_16b_bf16 v[28:31], a[250:251], a[228:229], v[28:31]\n"
      "ds_read_b64_tr_b16 a[74:75], v95 offset:19200\n"
      "v_mfma_f32_4x4x4_16b_bf16 v[32:35], a[250:251], a[230:231], v[32:35]\n"
      "ds_read_b64_tr_b16 a[220:221], v95 offset:12288\n"
      "ds_read_b64_tr_b16 a[222:223], v95 offset:13056\n"
      "ds_read_b128 a[196:199], v1 offset:64\n"
      "v_mfma_f32_16x16x32_bf16 v[56:59], v[196:199], a[200:203], v[56:59]\n"
      "v_cvt_pk_bf16_f32 v132, v84, v88\n"
      "v_cvt_pk_bf16_f32 v133, v85, v89\n"
      "v_mfma_f32_16x16x32_bf16 v[60:63], v[188:191], a[200:203], v[60:63]\n"
      "v_cvt_pk_bf16_f32 v134, v86, v90\n"
      "v_cvt_pk_bf16_f32 v135, v87, v91\n"
      "v_mfma_f32_16x16x32_bf16 v[64:67], v[200:203], a[204:207], v[64:67]\n"
      "s_nop 0\n"
      "v_cvt_pk_bf16_f32 v128, v76, v80\n"
      "v_cvt_pk_bf16_f32 v129, v77, v81\n"
      "v_mfma_f32_16x16x32_bf16 v[68:71], v[192:195], a[204:207], v[68:71]\n"
      "s_nop 0\n"
      "v_cvt_pk_bf16_f32 v130, v78, v82\n"
      "v_cvt_pk_bf16_f32 v131, v79, v83\n"
      "v_mfma_f32_4x4x4_16b_bf16 v[76:79], a[244:245], a[240:241], 0\n"
      "v_mfma_f32_4x4x4_16b_bf16 v[80:83], a[244:245], a[242:243], 0\n"
      "ds_read_b64_tr_b16 a[232:233], v95 offset:6208\n"
      "v_mfma_f32_4x4x4_16b_bf16 v[76:79], a[248:249], a[68:69], v[76:79]\n"
      "ds_read_b64_tr_b16 a[234:235], v95 offset:6976\n"
      "v_mfma_f32_4x4x4_16b_bf16 v[80:83], a[248:249], a[70:71], v[80:83]\n"
      "ds_read_b64_tr_b16 a[76:77], v95 offset:18496\n"
      "v_mfma_f32_4x4x4_16b_bf16 v[84:87], a[246:247], a[236:237], v[76:79]\n"
      "ds_read_b64_tr_b16 a[78:79], v95 offset:19264\n"
      "v_mfma_f32_4x4x4_16b_bf16 v[88:91], a[246:247], a[238:239], v[80:83]\n"
      "ds_read_b64_tr_b16 a[228:229], v95 offset:12352\n"
      "v_mfma_f32_4x4x4_16b_bf16 v[76:79], a[250:251], a[236:237], v[76:79]\n"
      "ds_read_b64_tr_b16 a[230:231], v95 offset:13120\n"
      "v_mfma_f32_4x4x4_16b_bf16 v[80:83], a[250:251], a[238:239], v[80:83]\n"
      "ds_read_b128 a[200:203], v1 offset:4160\n"
      "v_mfma_f32_16x16x32_bf16 v[72:75], v[184:187], a[204:207], v[72:75]\n"
      "s_nop 0\n"
      "v_cvt_pk_bf16_f32 v124, v36, v40\n"
      "v_cvt_pk_bf16_f32 v125, v37, v41\n"
      "v_mfma_f32_16x16x32_bf16 v[52:55], v[180:183], a[208:211], v[52:55]\n"
      "s_nop 0\n"
      "v_cvt_pk_bf16_f32 v126, v38, v42\n"
      "v_cvt_pk_bf16_f32 v127, v39, v43\n"
      "v_mfma_f32_16x16x32_bf16 v[56:59], v[172:175], a[208:211], v[56:59]\n"
      "s_nop 0\n"
      "v_cvt_pk_bf16_f32 v28, v28, v32\n"
      "v_cvt_pk_bf16_f32 v29, v29, v33\n"
      "v_mfma_f32_16x16x32_bf16 v[60:63], v[164:167], a[208:211], v[60:63]\n"
      "s_nop 0\n"
      "v_cvt_pk_bf16_f32 v30, v30, v34\n"
      "v_cvt_pk_bf16_f32 v31, v31, v35\n"
      "v_mfma_f32_16x16x32_bf16 v[64:67], v[176:179], a[212:215], v[64:67]\n"
      "s_nop 0\n"
      "v_accvgpr_write_b32 a188, v28\n"
      "v_accvgpr_write_b32 a189, v29\n"
      "v_mfma_f32_16x16x32_bf16 v[68:71], v[168:171], a[212:215], v[68:71]\n"
      "s_waitcnt lgkmcnt(0)\n"
      "v_accvgpr_write_b32 a190, v30\n"
      "v_accvgpr_write_b32 a191, v31\n"
      "v_mfma_f32_4x4x4_16b_bf16 v[28:31], a[244:245], a[224:225], 0\n"
      "v_mfma_f32_4x4x4_16b_bf16 v[32:35], a[244:245], a[226:227], 0\n"
      "ds_read_b64_tr_b16 a[240:241], v95 offset:6272\n"
      "v_mfma_f32_4x4x4_16b_bf16 v[28:31], a[248:249], a[72:73], v[28:31]\n"
      "ds_read_b64_tr_b16 a[242:243], v95 offset:7040\n"
      "v_mfma_f32_4x4x4_16b_bf16 v[32:35], a[248:249], a[74:75], v[32:35]\n"
      "ds_read_b64_tr_b16 a[80:81], v95 offset:18560\n"
      "v_mfma_f32_4x4x4_16b_bf16 v[36:39], a[246:247], a[220:221], v[28:31]\n"
      "ds_read_b64_tr_b16 a[82:83], v95 offset:19328\n"
      "v_mfma_f32_4x4x4_16b_bf16 v[40:43], a[246:247], a[222:223], v[32:35]\n"
      "ds_read_b64_tr_b16 a[236:237], v95 offset:12416\n"
      "v_mfma_f32_4x4x4_16b_bf16 v[28:31], a[250:251], a[220:221], v[28:31]\n"
      "ds_read_b64_tr_b16 a[238:239], v95 offset:13184\n"
      "v_mfma_f32_4x4x4_16b_bf16 v[32:35], a[250:251], a[222:223], v[32:35]\n"
      "ds_read_b128 a[204:207], v4 offset:128\n"
      "ds_read_b128 a[208:211], v1 offset:128\n"
      "v_cvt_pk_bf16_f32 v84, v84, v88\n"
      "v_cvt_pk_bf16_f32 v85, v85, v89\n"
      "v_cvt_pk_bf16_f32 v86, v86, v90\n"
      "v_cvt_pk_bf16_f32 v87, v87, v91\n"
      "v_mfma_f32_16x16x32_bf16 v[72:75], v[160:163], a[212:215], v[72:75]\n"
      "v_accvgpr_write_b32 a184, v84\n"
      "v_accvgpr_write_b32 a185, v85\n"
      "v_mfma_f32_16x16x32_bf16 v[100:103], a[48:51], a[192:195], v[100:103]\n"
      "v_accvgpr_write_b32 a186, v86\n"
      "v_accvgpr_write_b32 a187, v87\n"
      "v_mfma_f32_16x16x32_bf16 v[104:107], a[52:55], a[192:195], v[104:107]\n"
      "v_cvt_pk_bf16_f32 v76, v76, v80\n"
      "v_cvt_pk_bf16_f32 v77, v77, v81\n"
      "v_mfma_f32_16x16x32_bf16 v[108:111], a[56:59], a[192:195], v[108:111]\n"
      "v_cvt_pk_bf16_f32 v78, v78, v82\n"
      "v_cvt_pk_bf16_f32 v79, v79, v83\n"
      "v_mfma_f32_16x16x32_bf16 v[52:55], v[156:159], a[196:199], v[52:55]\n"
      "v_accvgpr_write_b32 a180, v76\n"
      "v_accvgpr_write_b32 a181, v77\n"
      "v_mfma_f32_16x16x32_bf16 v[56:59], v[148:151], a[196:199], v[56:59]\n"
      "s_waitcnt vmcnt(6)\n"
      "v_accvgpr_write_b32 a182, v78\n"
      "v_accvgpr_write_b32 a183, v79\n"
      "s_barrier\n"
      "v_mfma_f32_4x4x4_16b_bf16 v[76:79], a[244:245], a[232:233], 0\n"
      "v_mfma_f32_4x4x4_16b_bf16 v[80:83], a[244:245], a[234:235], 0\n"
      "ds_read_b64_tr_b16 a[224:225], v95 offset:41216\n"
      "v_mfma_f32_4x4x4_16b_bf16 v[76:79], a[248:249], a[76:77], v[76:79]\n"
      "ds_read_b64_tr_b16 a[226:227], v95 offset:41984\n"
      "v_mfma_f32_4x4x4_16b_bf16 v[80:83], a[248:249], a[78:79], v[80:83]\n"
      "ds_read_b64_tr_b16 a[84:85], v95 offset:53504\n"
      "v_mfma_f32_4x4x4_16b_bf16 v[84:87], a[246:247], a[228:229], v[76:79]\n"
      "ds_read_b64_tr_b16 a[86:87], v95 offset:54272\n"
      "v_mfma_f32_4x4x4_16b_bf16 v[88:91], a[246:247], a[230:231], v[80:83]\n"
      "ds_read_b64_tr_b16 a[220:221], v95 offset:47360\n"
      "v_mfma_f32_4x4x4_16b_bf16 v[76:79], a[250:251], a[228:229], v[76:79]\n"
      "ds_read_b64_tr_b16 a[222:223], v95 offset:48128\n"
      "v_mfma_f32_4x4x4_16b_bf16 v[80:83], a[250:251], a[230:231], v[80:83]\n"
      "ds_read_b128 a[212:215], v1 offset:4224\n"
      "ds_read_b128 a[192:195], v5 offset:0\n"
      "v_cvt_pk_bf16_f32 v36, v36, v40\n"
      "v_cvt_pk_bf16_f32 v37, v37, v41\n"
      "v_cvt_pk_bf16_f32 v38, v38, v42\n"
      "v_cvt_pk_bf16_f32 v39, v39, v43\n"
      "v_mfma_f32_16x16x32_bf16 v[60:63], v[140:143], a[196:199], v[60:63]\n"
      "v_accvgpr_write_b32 a176, v36\n"
      "v_accvgpr_write_b32 a177, v37\n"
      "v_mfma_f32_16x16x32_bf16 v[64:67], v[152:155], a[200:203], v[64:67]\n"
      "v_accvgpr_write_b32 a178, v38\n"
      "v_accvgpr_write_b32 a179, v39\n"
      "v_mfma_f32_16x16x32_bf16 v[68:71], v[144:147], a[200:203], v[68:71]\n"
      "v_cvt_pk_bf16_f32 v28, v28, v32\n"
      "v_cvt_pk_bf16_f32 v29, v29, v33\n"
      "v_mfma_f32_16x16x32_bf16 v[72:75], v[136:139], a[200:203], v[72:75]\n"
      "s_waitcnt lgkmcnt(1)\n"
      "v_cvt_pk_bf16_f32 v30, v30, v34\n"
      "v_cvt_pk_bf16_f32 v31, v31, v35\n"
      "v_mfma_f32_16x16x32_bf16 v[100:103], a[60:63], a[204:207], v[100:103]\n"
      "s_nop 0\n"
      "v_accvgpr_write_b32 a172, v28\n"
      "v_accvgpr_write_b32 a173, v29\n"
      "v_mfma_f32_16x16x32_bf16 v[104:107], a[64:67], a[204:207], v[104:107]\n"
      "v_accvgpr_write_b32 a174, v30\n"
      "v_accvgpr_write_b32 a175, v31\n"
      "v_mfma_f32_4x4x4_16b_bf16 v[28:31], a[244:245], a[240:241], 0\n"
      "v_mfma_f32_4x4x4_16b_bf16 v[32:35], a[244:245], a[242:243], 0\n"
      "ds_read_b64_tr_b16 a[232:233], v95 offset:41280\n"
      "v_mfma_f32_4x4x4_16b_bf16 v[28:31], a[248:249], a[80:81], v[28:31]\n"
      "ds_read_b64_tr_b16 a[234:235], v95 offset:42048\n"
      "v_mfma_f32_4x4x4_16b_bf16 v[32:35], a[248:249], a[82:83], v[32:35]\n"
      "ds_read_b64_tr_b16 a[88:89], v95 offset:53568\n"
      "v_mfma_f32_4x4x4_16b_bf16 v[36:39], a[246:247], a[236:237], v[28:31]\n"
      "ds_read_b64_tr_b16 a[90:91], v95 offset:54336\n"
      "v_mfma_f32_4x4x4_16b_bf16 v[40:43], a[246:247], a[238:239], v[32:35]\n"
      "ds_read_b64_tr_b16 a[228:229], v95 offset:47424\n"
      "v_mfma_f32_4x4x4_16b_bf16 v[28:31], a[250:251], a[236:237], v[28:31]\n"
      "ds_read_b64_tr_b16 a[230:231], v95 offset:48192\n"
      "v_mfma_f32_4x4x4_16b_bf16 v[32:35], a[250:251], a[238:239], v[32:35]\n"
      "ds_read_b128 a[196:199], v2 offset:0\n"
      "ds_read_b128 a[200:203], v2 offset:4096\n"
      "v_cvt_pk_bf16_f32 v84, v84, v88\n"
      "v_cvt_pk_bf16_f32 v85, v85, v89\n"
      "v_cvt_pk_bf16_f32 v86, v86, v90\n"
      "v_cvt_pk_bf16_f32 v87, v87, v91\n"
      "v_mfma_f32_16x16x32_bf16 v[108:111], a[68:71], a[204:207], v[108:111]\n"
      "v_accvgpr_write_b32 a168, v84\n"
      "v_accvgpr_write_b32 a169, v85\n"
      "v_mfma_f32_16x16x32_bf16 v[52:55], v[132:135], a[208:211], v[52:55]\n"
      "v_accvgpr_write_b32 a170, v86\n"
      "v_accvgpr_write_b32 a171, v87\n"
      "v_mfma_f32_16x16x32_bf16 v[56:59], v[124:127], a[208:211], v[56:59]\n"
      "v_cvt_pk_bf16_f32 v76, v76, v80\n"
      "v_cvt_pk_bf16_f32 v77, v77, v81\n"
      "v_mfma_f32_16x16x32_bf16 v[60:63], a[184:187], a[208:211], v[60:63]\n"
      "s_nop 0\n"
      "v_cvt_pk_bf16_f32 v78, v78, v82\n"
      "v_cvt_pk_bf16_f32 v79, v79, v83\n"
      "v_mfma_f32_16x16x32_bf16 v[64:67], v[128:131], a[212:215], v[64:67]\n"
      "s_nop 0\n"
      "v_accvgpr_write_b32 a164, v76\n"
      "v_accvgpr_write_b32 a165, v77\n"
      "v_mfma_f32_16x16x32_bf16 v[68:71], a[188:191], a[212:215], v[68:71]\n"
      "v_accvgpr_write_b32 a166, v78\n"
      "v_accvgpr_write_b32 a167, v79\n"
      "v_mfma_f32_4x4x4_16b_bf16 v[76:79], a[244:245], a[224:225], 0\n"
      "v_mfma_f32_4x4x4_16b_bf16 v[80:83], a[244:245], a[226:227], 0\n"
      "ds_read_b64_tr_b16 a[240:241], v95 offset:41344\n"
      "v_mfma_f32_4x4x4_16b_bf16 v[76:79], a[248:249], a[84:85], v[76:79]\n"
      "ds_read_b64_tr_b16 a[242:243], v95 offset:42112\n"
      "v_mfma_f32_4x4x4_16b_bf16 v[80:83], a[248:249], a[86:87], v[80:83]\n"
      "ds_read_b64_tr_b16 a[92:93], v95 offset:53632\n"
      "v_mfma_f32_4x4x4_16b_bf16 v[84:87], a[246:247], a[220:221], v[76:79]\n"
      "ds_read_b64_tr_b16 a[94:95], v95 offset:54400\n"
      "v_mfma_f32_4x4x4_16b_bf16 v[88:91], a[246:247], a[222:223], v[80:83]\n"
      "ds_read_b64_tr_b16 a[236:237], v95 offset:47488\n"
      "v_mfma_f32_4x4x4_16b_bf16 v[76:79], a[250:251], a[220:221], v[76:79]\n"
      "ds_read_b64_tr_b16 a[238:239], v95 offset:48256\n"
      "v_mfma_f32_4x4x4_16b_bf16 v[80:83], a[250:251], a[222:223], v[80:83]\n"
      "v_cvt_pk_bf16_f32 v36, v36, v40\n"
      "v_cvt_pk_bf16_f32 v37, v37, v41\n"
      "v_cvt_pk_bf16_f32 v38, v38, v42\n"
      "v_cvt_pk_bf16_f32 v39, v39, v43\n"
      "v_accvgpr_write_b32 a160, v36\n"
      "v_accvgpr_write_b32 a161, v37\n"
      "v_accvgpr_write_b32 a162, v38\n"
      "v_accvgpr_write_b32 a163, v39\n"
      "v_mfma_f32_16x16x32_bf16 v[72:75], a[180:183], a[212:215], v[72:75]\n"
      "s_waitcnt lgkmcnt(2)\n"
      "v_cvt_pk_bf16_f32 v28, v28, v32\n"
      "v_cvt_pk_bf16_f32 v29, v29, v33\n"
      "v_mfma_f32_16x16x32_bf16 v[100:103], a[72:75], a[192:195], v[100:103]\n"
      "s_nop 0\n"
      "v_cvt_pk_bf16_f32 v30, v30, v34\n"
      "v_cvt_pk_bf16_f32 v31, v31, v35\n"
      "v_mfma_f32_16x16x32_bf16 v[104:107], a[76:79], a[192:195], v[104:107]\n"
      "v_accvgpr_write_b32 a156, v28\n"
      "v_accvgpr_write_b32 a157, v29\n"
      "v_mfma_f32_16x16x32_bf16 v[108:111], a[80:83], a[192:195], v[108:111]\n"
      "s_waitcnt vmcnt(0)\n"
      "v_accvgpr_write_b32 a158, v30\n"
      "v_accvgpr_write_b32 a159, v31\n"
      "s_barrier\n"
      "v_mfma_f32_4x4x4_16b_bf16 v[28:31], a[244:245], a[232:233], 0\n"
      "v_mfma_f32_4x4x4_16b_bf16 v[32:35], a[244:245], a[234:235], 0\n"
      "ds_read_b64_tr_b16 a[224:225], v96 offset:2304\n"
      "v_mfma_f32_4x4x4_16b_bf16 v[28:31], a[248:249], a[88:89], v[28:31]\n"
      "ds_read_b64_tr_b16 a[226:227], v96 offset:3072\n"
      "v_mfma_f32_4x4x4_16b_bf16 v[32:35], a[248:249], a[90:91], v[32:35]\n"
      "ds_read_b64_tr_b16 a[96:97], v96 offset:14592\n"
      "v_mfma_f32_4x4x4_16b_bf16 v[36:39], a[246:247], a[228:229], v[28:31]\n"
      "ds_read_b64_tr_b16 a[98:99], v96 offset:15360\n"
      "v_mfma_f32_4x4x4_16b_bf16 v[40:43], a[246:247], a[230:231], v[32:35]\n"
      "ds_read_b64_tr_b16 a[220:221], v96 offset:8448\n"
      "v_mfma_f32_4x4x4_16b_bf16 v[28:31], a[250:251], a[228:229], v[28:31]\n"
      "ds_read_b64_tr_b16 a[222:223], v96 offset:9216\n"
      "v_mfma_f32_4x4x4_16b_bf16 v[32:35], a[250:251], a[230:231], v[32:35]\n"
      "ds_read_b128 a[204:207], v5 offset:64\n"
      "v_cvt_pk_bf16_f32 v84, v84, v88\n"
      "v_cvt_pk_bf16_f32 v85, v85, v89\n"
      "v_cvt_pk_bf16_f32 v86, v86, v90\n"
      "v_cvt_pk_bf16_f32 v87, v87, v91\n"
      "v_mfma_f32_16x16x32_bf16 v[52:55], a[176:179], a[196:199], v[52:55]\n"
      "s_nop 0\n"
      "v_accvgpr_write_b32 a152, v84\n"
      "v_accvgpr_write_b32 a153, v85\n"
      "v_mfma_f32_16x16x32_bf16 v[56:59], a[168:171], a[196:199], v[56:59]\n"
      "s_nop 0\n"
      "v_accvgpr_write_b32 a154, v86\n"
      "v_accvgpr_write_b32 a155, v87\n"
      "v_mfma_f32_16x16x32_bf16 v[60:63], a[160:163], a[196:199], v[60:63]\n"
      "s_nop 0\n"
      "v_cvt_pk_bf16_f32 v76, v76, v80\n"
      "v_cvt_pk_bf16_f32 v77, v77, v81\n"
      "v_mfma_f32_16x16x32_bf16 v[64:67], a[172:175], a[200:203], v[64:67]\n"
      "s_nop 0\n"
      "v_cvt_pk_bf16_f32 v78, v78, v82\n"
      "v_cvt_pk_bf16_f32 v79, v79, v83\n"
      "v_mfma_f32_16x16x32_bf16 v[68:71], a[164:167], a[200:203], v[68:71]\n"
      "s_nop 0\n"
      "v_accvgpr_write_b32 a148, v76\n"
      "v_accvgpr_write_b32 a149, v77\n"
      "v_mfma_f32_16x16x32_bf16 v[72:75], a[156:159], a[200:203], v[72:75]\n"
      "s_nop 0\n"
      "v_accvgpr_write_b32 a150, v78\n"
      "v_accvgpr_write_b32 a151, v79\n"
      "v_mfma_f32_4x4x4_16b_bf16 v[76:79], a[244:245], a[240:241], 0\n"
      "v_mfma_f32_4x4x4_16b_bf16 v[80:83], a[244:245], a[242:243], 0\n"
      "ds_read_b64_tr_b16 a[232:233], v96 offset:2368\n"
      "v_mfma_f32_4x4x4_16b_bf16 v[76:79], a[248:249], a[92:93], v[76:79]\n"
      "ds_read_b64_tr_b16 a[234:235], v96 offset:3136\n"
      "v_mfma_f32_4x4x4_16b_bf16 v[80:83], a[248:249], a[94:95], v[80:83]\n"
      "ds_read_b64_tr_b16 a[100:101], v96 offset:14656\n"
      "s_nop 0\n"
      "s_waitcnt lgkmcnt(3)\n"
      "v_mfma_f32_4x4x4_16b_bf16 v[84:87], a[246:247], a[236:237], v[76:79]\n"
      "ds_read_b64_tr_b16 a[102:103], v96 offset:15424\n"
      "v_mfma_f32_4x4x4_16b_bf16 v[88:91], a[246:247], a[238:239], v[80:83]\n"
      "ds_read_b64_tr_b16 a[228:229], v96 offset:8512\n"
      "v_mfma_f32_4x4x4_16b_bf16 v[76:79], a[250:251], a[236:237], v[76:79]\n"
      "ds_read_b64_tr_b16 a[230:231], v96 offset:9280\n"
      "v_mfma_f32_4x4x4_16b_bf16 v[80:83], a[250:251], a[238:239], v[80:83]\n"
      "ds_read_b128 a[208:211], v2 offset:64\n"
      "ds_read_b128 a[212:215], v2 offset:4160\n"
      "v_cvt_pk_bf16_f32 v36, v36, v40\n"
      "v_cvt_pk_bf16_f32 v37, v37, v41\n"
      "v_cvt_pk_bf16_f32 v38, v38, v42\n"
      "v_cvt_pk_bf16_f32 v39, v39, v43\n"
      "v_accvgpr_write_b32 a144, v36\n"
      "v_accvgpr_write_b32 a145, v37\n"
      "v_accvgpr_write_b32 a146, v38\n"
      "v_accvgpr_write_b32 a147, v39\n"
      "v_cvt_pk_bf16_f32 v28, v28, v32\n"
      "v_cvt_pk_bf16_f32 v29, v29, v33\n"
      "v_cvt_pk_bf16_f32 v30, v30, v34\n"
      "v_cvt_pk_bf16_f32 v31, v31, v35\n"
      "v_accvgpr_write_b32 a140, v28\n"
      "v_accvgpr_write_b32 a141, v29\n"
      "v_accvgpr_write_b32 a142, v30\n"
      "v_accvgpr_write_b32 a143, v31\n"
      "v_mfma_f32_4x4x4_16b_bf16 v[28:31], a[244:245], a[224:225], 0\n"
      "v_mfma_f32_4x4x4_16b_bf16 v[32:35], a[244:245], a[226:227], 0\n"
      "ds_read_b64_tr_b16 a[240:241], v96 offset:2432\n"
      "v_mfma_f32_4x4x4_16b_bf16 v[28:31], a[248:249], a[96:97], v[28:31]\n"
      "ds_read_b64_tr_b16 a[242:243], v96 offset:3200\n"
      "v_mfma_f32_4x4x4_16b_bf16 v[32:35], a[248:249], a[98:99], v[32:35]\n"
      "ds_read_b64_tr_b16 a[104:105], v96 offset:14720\n"
      "v_mfma_f32_4x4x4_16b_bf16 v[36:39], a[246:247], a[220:221], v[28:31]\n"
      "ds_read_b64_tr_b16 a[106:107], v96 offset:15488\n"
      "v_mfma_f32_4x4x4_16b_bf16 v[40:43], a[246:247], a[222:223], v[32:35]\n"
      "ds_read_b64_tr_b16 a[236:237], v96 offset:8576\n"
      "v_mfma_f32_4x4x4_16b_bf16 v[28:31], a[250:251], a[220:221], v[28:31]\n"
      "ds_read_b64_tr_b16 a[238:239], v96 offset:9344\n"
      "v_mfma_f32_4x4x4_16b_bf16 v[32:35], a[250:251], a[222:223], v[32:35]\n"
      "v_cvt_pk_bf16_f32 v84, v84, v88\n"
      "v_cvt_pk_bf16_f32 v85, v85, v89\n"
      "v_cvt_pk_bf16_f32 v86, v86, v90\n"
      "v_cvt_pk_bf16_f32 v87, v87, v91\n"
      "v_accvgpr_write_b32 a136, v84\n"
      "v_accvgpr_write_b32 a137, v85\n"
      "v_accvgpr_write_b32 a138, v86\n"
      "v_accvgpr_write_b32 a139, v87\n"
      "v_cvt_pk_bf16_f32 v76, v76, v80\n"
      "v_cvt_pk_bf16_f32 v77, v77, v81\n"
      "v_mfma_f32_16x16x32_bf16 v[100:103], a[84:87], a[204:207], v[100:103]\n"
      "s_nop 0\n"
      "v_cvt_pk_bf16_f32 v78, v78, v82\n"
      "v_cvt_pk_bf16_f32 v79, v79, v83\n"
      "v_mfma_f32_16x16x32_bf16 v[104:107], a[88:91], a[204:207], v[104:107]\n"
      "s_nop 0\n"
      "v_accvgpr_write_b32 a132, v76\n"
      "v_accvgpr_write_b32 a133, v77\n"
      "v_mfma_f32_16x16x32_bf16 v[108:111], a[92:95], a[204:207], v[108:111]\n"
      "s_waitcnt lgkmcnt(0)\n"
      "v_accvgpr_write_b32 a134, v78\n"
      "v_accvgpr_write_b32 a135, v79\n"
      "v_mfma_f32_4x4x4_16b_bf16 v[76:79], a[244:245], a[232:233], 0\n"
      "v_mfma_f32_4x4x4_16b_bf16 v[80:83], a[244:245], a[234:235], 0\n"
      "ds_read_b128 a[192:195], v5 offset:128\n"
      "v_mfma_f32_4x4x4_16b_bf16 v[76:79], a[248:249], a[100:101], v[76:79]\n"
      "v_mfma_f32_4x4x4_16b_bf16 v[80:83], a[248:249], a[102:103], v[80:83]\n"
      "s_nop 0\n"
      "v_mfma_f32_4x4x4_16b_bf16 v[84:87], a[246:247], a[228:229], v[76:79]\n"
      "v_mfma_f32_4x4x4_16b_bf16 v[88:91], a[246:247], a[230:231], v[80:83]\n"
      "v_mfma_f32_4x4x4_16b_bf16 v[76:79], a[250:251], a[228:229], v[76:79]\n"
      "v_mfma_f32_4x4x4_16b_bf16 v[80:83], a[250:251], a[230:231], v[80:83]\n"
      "v_cvt_pk_bf16_f32 v36, v36, v40\n"
      "v_cvt_pk_bf16_f32 v37, v37, v41\n"
      "v_cvt_pk_bf16_f32 v38, v38, v42\n"
      "v_cvt_pk_bf16_f32 v39, v39, v43\n"
      "v_mfma_f32_16x16x32_bf16 v[52:55], a[152:155], a[208:211], v[52:55]\n"
      "v_accvgpr_write_b32 a128, v36\n"
      "v_accvgpr_write_b32 a129, v37\n"
      "v_mfma_f32_16x16x32_bf16 v[56:59], a[144:147], a[208:211], v[56:59]\n"
      "v_accvgpr_write_b32 a130, v38\n"
      "v_accvgpr_write_b32 a131, v39\n"
      "v_mfma_f32_16x16x32_bf16 v[60:63], a[136:139], a[208:211], v[60:63]\n"
      "v_cvt_pk_bf16_f32 v28, v28, v32\n"
      "v_cvt_pk_bf16_f32 v29, v29, v33\n"
      "v_mfma_f32_16x16x32_bf16 v[64:67], a[148:151], a[212:215], v[64:67]\n"
      "v_cvt_pk_bf16_f32 v30, v30, v34\n"
      "v_cvt_pk_bf16_f32 v31, v31, v35\n"
      "v_mfma_f32_16x16x32_bf16 v[68:71], a[140:143], a[212:215], v[68:71]\n"
      "v_accvgpr_write_b32 a124, v28\n"
      "v_accvgpr_write_b32 a125, v29\n"
      "v_mfma_f32_16x16x32_bf16 v[72:75], a[132:135], a[212:215], v[72:75]\n"
      "v_accvgpr_write_b32 a126, v30\n"
      "v_accvgpr_write_b32 a127, v31\n"
      "v_mfma_f32_4x4x4_16b_bf16 v[28:31], a[244:245], a[240:241], 0\n"
      "v_mfma_f32_4x4x4_16b_bf16 v[32:35], a[244:245], a[242:243], 0\n"
      "ds_read_b128 a[196:199], v2 offset:128\n"
      "v_mfma_f32_4x4x4_16b_bf16 v[28:31], a[248:249], a[104:105], v[28:31]\n"
      "ds_read_b128 a[200:203], v2 offset:4224\n"
      "v_mfma_f32_4x4x4_16b_bf16 v[32:35], a[248:249], a[106:107], v[32:35]\n"
      "v_mfma_f32_4x4x4_16b_bf16 v[36:39], a[246:247], a[236:237], v[28:31]\n"
      "s_nop 0\n"
      "s_nop 0\n"
      "v_mfma_f32_4x4x4_16b_bf16 v[40:43], a[246:247], a[238:239], v[32:35]\n"
      "v_mfma_f32_4x4x4_16b_bf16 v[28:31], a[250:251], a[236:237], v[28:31]\n"
      "v_mfma_f32_4x4x4_16b_bf16 v[32:35], a[250:251], a[238:239], v[32:35]\n"
      "v_cvt_pk_bf16_f32 v84, v84, v88\n"
      "v_cvt_pk_bf16_f32 v85, v85, v89\n"
      "v_cvt_pk_bf16_f32 v86, v86, v90\n"
      "v_cvt_pk_bf16_f32 v87, v87, v91\n"
      "v_accvgpr_write_b32 a120, v84\n"
      "v_accvgpr_write_b32 a121, v85\n"
      "v_accvgpr_write_b32 a122, v86\n"
      "v_accvgpr_write_b32 a123, v87\n"
      "v_cvt_pk_bf16_f32 v76, v76, v80\n"
      "v_cvt_pk_bf16_f32 v77, v77, v81\n"
      "v_cvt_pk_bf16_f32 v78, v78, v82\n"
      "v_cvt_pk_bf16_f32 v79, v79, v83\n"
      "v_accvgpr_write_b32 a116, v76\n"
      "v_accvgpr_write_b32 a117, v77\n"
      "v_accvgpr_write_b32 a118, v78\n"
      "v_accvgpr_write_b32 a119, v79\n"
      "s_waitcnt lgkmcnt(0)\n"
      "s_barrier\n"
      "s_mov_b32 m0, s64\n"
      "s_nop 0\n"
      "v_mfma_f32_16x16x32_bf16 v[100:103], a[96:99], a[192:195], v[100:103]\n"
      "ds_read_b128 a[204:207], v3 offset:16896\n"
      "v_cvt_pk_bf16_f32 v36, v36, v40\n"
      "v_cvt_pk_bf16_f32 v37, v37, v41\n"
      "v_cvt_pk_bf16_f32 v38, v38, v42\n"
      "v_cvt_pk_bf16_f32 v39, v39, v43\n"
      "v_mfma_f32_16x16x32_bf16 v[104:107], a[100:103], a[192:195], v[104:107]\n"
      "ds_read_b128 a[208:211], v3 offset:16960\n"
      "ds_read_b64_tr_b16 a[220:221], v7 offset:16896\n"
      "ds_read_b64_tr_b16 a[222:223], v7 offset:16904\n"
      "v_accvgpr_write_b32 a112, v36\n"
      "v_mfma_f32_16x16x32_bf16 v[108:111], a[104:107], a[192:195], v[108:111]\n"
      "v_accvgpr_write_b32 a113, v37\n"
      "v_accvgpr_write_b32 a114, v38\n"
      "v_accvgpr_write_b32 a115, v39\n"
      "v_cvt_pk_bf16_f32 v28, v28, v32\n"
      "v_cvt_pk_bf16_f32 v29, v29, v33\n"
      "v_cvt_pk_bf16_f32 v30, v30, v34\n"
      "v_mfma_f32_16x16x32_bf16 v[52:55], a[128:131], a[196:199], v[52:55]\n"
      "ds_read_b128 a[212:215], v3 offset:17024\n"
      "v_cvt_pk_bf16_f32 v31, v31, v35\n"
      "v_accvgpr_write_b32 a108, v28\n"
      "v_accvgpr_write_b32 a109, v29\n"
      "v_accvgpr_write_b32 a110, v30\n"
      "v_accvgpr_write_b32 a111, v31\n"
      "v_mfma_f32_16x16x32_bf16 v[56:59], a[120:123], a[196:199], v[56:59]\n"
      "v_mfma_f32_16x16x32_bf16 v[60:63], a[112:115], a[196:199], v[60:63]\n"
      "v_mfma_f32_16x16x32_bf16 v[64:67], a[124:127], a[200:203], v[64:67]\n"
      "ds_read_b128 a[192:195], v4 offset:16896\n"
      "v_mfma_f32_16x16x32_bf16 v[68:71], a[116:119], a[200:203], v[68:71]\n"
      "s_nop 0\n"
      "v_mfma_f32_16x16x32_bf16 v[72:75], a[108:111], a[200:203], v[72:75]\n"
      "s_waitcnt lgkmcnt(1)\n"
      "v_mfma_f32_4x4x4_16b_bf16 v[20:23], a[216:217], a[220:221], 0\n"
      "v_mfma_f32_4x4x4_16b_bf16 v[24:27], a[216:217], a[222:223], 0\n"
      "v_mfma_f32_16x16x32_bf16 v[112:115], a[0:3], a[204:207], 0\n"
      "ds_read_b128 a[196:199], v4 offset:16960\n"
      "v_sub_f32 v52, v64, v52\n"
      "v_sub_f32 v53, v65, v53\n"
      "v_sub_f32 v54, v66, v54\n"
      "v_sub_f32 v55, v67, v55\n"
      "v_sub_f32 v56, v68, v56\n"
      "v_mfma_f32_16x16x32_bf16 v[116:119], a[4:7], a[204:207], 0\n"
      "s_nop 0\n"
      "ds_read_b64_tr_b16 a[224:225], v7 offset:17408\n"
      "ds_write_addtid_b32 v52 offset:0\n"
      "v_cvt_pk_bf16_f32 v20, v20, v24\n"
      "v_mfma_f32_16x16x32_bf16 v[120:123], a[8:11], a[204:207], 0\n"
      "ds_write_addtid_b32 v20 offset:41216\n"
      "ds_read_b64_tr_b16 a[226:227], v7 offset:17416\n"
      "v_mfma_f32_16x16x32_bf16 v[112:115], a[12:15], a[208:211], v[112:115]\n"
      "ds_read_b128 a[200:203], v4 offset:17024\n"
      "ds_write_addtid_b32 v53 offset:512\n"
      "v_mfma_f32_16x16x32_bf16 v[116:119], a[16:19], a[208:211], v[116:119]\n"
      "ds_write_addtid_b32 v54 offset:1024\n"
      "ds_write_addtid_b32 v55 offset:1536\n"
      "v_mfma_f32_16x16x32_bf16 v[120:123], a[20:23], a[208:211], v[120:123]\n"
      "ds_write_addtid_b32 v56 offset:2048\n"
      "v_add_f32 v64, v64, v100\n"
      "v_mfma_f32_16x16x32_bf16 v[112:115], a[24:27], a[212:215], v[112:115]\n"
      "s_nop 0\n"
      "ds_read_b128 a[204:207], v5 offset:16896\n"
      "v_cvt_pk_bf16_f32 v21, v21, v25\n"
      "v_mfma_f32_16x16x32_bf16 v[116:119], a[28:31], a[212:215], v[116:119]\n"
      "ds_write_addtid_b32 v21 offset:45312\n"
      "v_sub_f32 v57, v69, v57\n"
      "v_mfma_f32_16x16x32_bf16 v[120:123], a[32:35], a[212:215], v[120:123]\n"
      "s_waitcnt lgkmcnt(5)\n"
      "ds_write_addtid_b32 v57 offset:2560\n"
      "v_add_f32_e64 v65, v65, v101\n"
      "v_mfma_f32_4x4x4_16b_bf16 v[28:31], a[216:217], a[224:225], 0\n"
      "v_cvt_pk_bf16_f32 v22, v22, v26\n"
      "v_mfma_f32_4x4x4_16b_bf16 v[32:35], a[216:217], a[226:227], 0\n"
      "ds_write_addtid_b32 v22 offset:49408\n"
      "v_add_f32 v66, v66, v102\n"
      "v_mfma_f32_16x16x32_bf16 v[112:115], a[36:39], a[192:195], v[112:115]\n"
      "ds_read_b128 a[208:211], v5 offset:16960\n"
      "v_cvt_pk_bf16_f32 v23, v23, v27\n"
      "v_mfma_f32_16x16x32_bf16 v[116:119], a[40:43], a[192:195], v[116:119]\n"
      "s_nop 0\n"
      "ds_write_addtid_b32 v23 offset:53504\n"
      "ds_read_b64_tr_b16 a[228:229], v7 offset:17920\n"
      "v_mfma_f32_16x16x32_bf16 v[120:123], a[44:47], a[192:195], v[120:123]\n"
      "ds_read_b64_tr_b16 a[230:231], v7 offset:17928\n"
      "v_sub_f32 v58, v70, v58\n"
      "v_mfma_f32_16x16x32_bf16 v[112:115], a[48:51], a[196:199], v[112:115]\n"
      "s_nop 0\n"
      "ds_read_b128 a[212:215], v5 offset:17024\n"
      "ds_write_addtid_b32 v58 offset:3072\n"
      "v_mfma_f32_16x16x32_bf16 v[116:119], a[52:55], a[196:199], v[116:119]\n"
      "v_cvt_pk_bf16_f32 v28, v28, v32\n"
      "v_add_f32 v67, v67, v103\n"
      "v_mfma_f32_16x16x32_bf16 v[120:123], a[56:59], a[196:199], v[120:123]\n"
      "ds_write_addtid_b32 v28 offset:41728\n"
      "v_cvt_pk_bf16_f32 v29, v29, v33\n"
      "v_mfma_f32_16x16x32_bf16 v[112:115], a[60:63], a[200:203], v[112:115]\n"
      "s_nop 0\n"
      "ds_read_b128 a[192:195], v3 offset:33792\n"
      "ds_write_addtid_b32 v29 offset:45824\n"
      "v_mfma_f32_16x16x32_bf16 v[116:119], a[64:67], a[200:203], v[116:119]\n"
      "v_sub_f32 v59, v71, v59\n"
      "v_cvt_pk_bf16_f32 v30, v30, v34\n"
      "v_mfma_f32_16x16x32_bf16 v[120:123], a[68:71], a[200:203], v[120:123]\n"
      "s_waitcnt lgkmcnt(3)\n"
      "ds_write_addtid_b32 v30 offset:49920\n"
      "ds_write_addtid_b32 v59 offset:3584\n"
      "v_mfma_f32_4x4x4_16b_bf16 v[20:23], a[216:217], a[228:229], 0\n"
      "v_add_f32 v68, v68, v104\n"
      "v_mfma_f32_4x4x4_16b_bf16 v[24:27], a[216:217], a[230:231], 0\n"
      "v_cvt_pk_bf16_f32 v31, v31, v35\n"
      "v_sub_f32 v60, v72, v60\n"
      "v_mfma_f32_16x16x32_bf16 v[112:115], a[72:75], a[204:207], v[112:115]\n"
      "ds_read_b128 a[196:199], v3 offset:33856\n"
      "ds_write_addtid_b32 v31 offset:54016\n"
      "v_mfma_f32_16x16x32_bf16 v[116:119], a[76:79], a[204:207], v[116:119]\n"
      "ds_read_b64_tr_b16 a[232:233], v7 offset:18432\n"
      "ds_read_b64_tr_b16 a[234:235], v7 offset:18440\n"
      "v_mfma_f32_16x16x32_bf16 v[120:123], a[80:83], a[204:207], v[120:123]\n"
      "ds_write_addtid_b32 v60 offset:4096\n"
      "v_cvt_pk_bf16_f32 v20, v20, v24\n"
      "v_mfma_f32_16x16x32_bf16 v[112:115], a[84:87], a[208:211], v[112:115]\n"
      "ds_read_b128 a[200:203], v3 offset:33920\n"
      "ds_write_addtid_b32 v20 offset:42240\n"
      "v_mfma_f32_16x16x32_bf16 v[116:119], a[88:91], a[208:211], v[116:119]\n"
      "v_add_f32 v69, v69, v105\n"
      "v_cvt_pk_bf16_f32 v21, v21, v25\n"
      "v_mfma_f32_16x16x32_bf16 v[120:123], a[92:95], a[208:211], v[120:123]\n"
      "ds_write_addtid_b32 v21 offset:46336\n"
      "v_sub_f32 v61, v73, v61\n"
      "v_mfma_f32_16x16x32_bf16 v[112:115], a[96:99], a[212:215], v[112:115]\n"
      "ds_read_b128 a[204:207], v4 offset:33792\n"
      "ds_write_addtid_b32 v61 offset:4608\n"
      "v_mfma_f32_16x16x32_bf16 v[116:119], a[100:103], a[212:215], v[116:119]\n"
      "v_add_f32 v70, v70, v106\n"
      "v_cvt_pk_bf16_f32 v22, v22, v26\n"
      "v_mfma_f32_16x16x32_bf16 v[120:123], a[104:107], a[212:215], v[120:123]\n"
      "s_waitcnt lgkmcnt(3)\n"
      "ds_write_addtid_b32 v22 offset:50432\n"
      "v_sub_f32_e64 v62, v74, v62\n"
      "v_mfma_f32_4x4x4_16b_bf16 v[28:31], a[216:217], a[232:233], 0\n"
      "v_cvt_pk_bf16_f32 v23, v23, v27\n"
      "v_mfma_f32_4x4x4_16b_bf16 v[32:35], a[216:217], a[234:235], 0\n"
      "ds_write_addtid_b32 v23 offset:54528\n"
      "ds_write_addtid_b32 v62 offset:5120\n"
      "v_mfma_f32_16x16x32_bf16 v[76:79], a[0:3], a[192:195], 0\n"
      "ds_read_b128 a[208:211], v4 offset:33856\n"
      "v_add_f32 v71, v71, v107\n"
      "v_mfma_f32_16x16x32_bf16 v[80:83], a[4:7], a[192:195], 0\n"
      "s_nop 0\n"
      "v_cvt_pk_bf16_f32 v28, v28, v32\n"
      "v_cvt_pk_bf16_f32 v29, v29, v33\n"
      "v_mfma_f32_16x16x32_bf16 v[84:87], a[8:11], a[192:195], 0\n"
      "ds_write_addtid_b32 v28 offset:42752\n"
      "ds_write_addtid_b32 v29 offset:46848\n"
      "v_mfma_f32_16x16x32_bf16 v[76:79], a[12:15], a[196:199], v[76:79]\n"
      "ds_read_b128 a[212:215], v4 offset:33920\n"
      "v_add_f32 v72, v72, v108\n"
      "v_mfma_f32_16x16x32_bf16 v[80:83], a[16:19], a[196:199], v[80:83]\n"
      "v_sub_f32 v63, v75, v63\n"
      "v_cvt_pk_bf16_f32 v30, v30, v34\n"
      "v_mfma_f32_16x16x32_bf16 v[84:87], a[20:23], a[196:199], v[84:87]\n"
      "ds_write_addtid_b32 v30 offset:50944\n"
      "ds_write_addtid_b32 v63 offset:5632\n"
      "v_mfma_f32_16x16x32_bf16 v[76:79], a[24:27], a[200:203], v[76:79]\n"
      "ds_read_b128 a[192:195], v5 offset:33792\n"
      "v_add_f32 v73, v73, v109\n"
      "v_mfma_f32_16x16x32_bf16 v[80:83], a[28:31], a[200:203], v[80:83]\n"
      "v_cvt_pk_bf16_f32 v31, v31, v35\n"
      "v_add_f32 v74, v74, v110\n"
      "v_mfma_f32_16x16x32_bf16 v[84:87], a[32:35], a[200:203], v[84:87]\n"
      "s_waitcnt lgkmcnt(3)\n"
      "ds_write_addtid_b32 v31 offset:55040\n"
      "v_add_f32 v75, v75, v111\n"
      "v_mfma_f32_16x16x32_bf16 v[76:79], a[36:39], a[204:207], v[76:79]\n"
      "ds_read_b128 a[196:199], v5 offset:33856\n"
      "v_mfma_f32_16x16x32_bf16 v[80:83], a[40:43], a[204:207], v[80:83]\n"
      "v_fma_f32 v40, 0.5, v52, -v64\n"
      "v_fma_f32 v41, 0.5, v53, -v65\n"
      "v_mfma_f32_16x16x32_bf16 v[84:87], a[44:47], a[204:207], v[84:87]\n"
      "v_fma_f32 v42, 0.5, v54, -v66\n"
      "v_fma_f32 v43, 0.5, v55, -v67\n"
      "v_mfma_f32_16x16x32_bf16 v[76:79], a[48:51], a[208:211], v[76:79]\n"
      "ds_read_b128 a[200:203], v5 offset:33920\n"
      "v_fma_f32 v44, 0.5, v56, -v68\n"
      "v_mfma_f32_16x16x32_bf16 v[80:83], a[52:55], a[208:211], v[80:83]\n"
      "v_fma_f32 v45, 0.5, v57, -v69\n"
      "v_mfma_f32_16x16x32_bf16 v[84:87], a[56:59], a[208:211], v[84:87]\n"
      "v_mfma_f32_16x16x32_bf16 v[76:79], a[60:63], a[212:215], v[76:79]\n"
      "v_mfma_f32_16x16x32_bf16 v[80:83], a[64:67], a[212:215], v[80:83]\n"
      "v_mfma_f32_16x16x32_bf16 v[84:87], a[68:71], a[212:215], v[84:87]\n"
      "s_waitcnt lgkmcnt(0)\n"
      "s_barrier\n"
      ".p2align 6\n"
      "LSloop_%=:\n"
      "v_mfma_f32_16x16x32_bf16 v[76:79], a[72:75], a[192:195], v[76:79]\n"
      "ds_read_b128 a[204:207], v0 offset:16384\n"
      "ds_read2st64_b32 v[28:29], v6 offset0:0 offset1:2\n"
      "v_fma_f32 v46, 0.5, v58, -v70\n"
      "v_mfma_f32_16x16x32_bf16 v[80:83], a[76:79], a[192:195], v[80:83]\n"
      "ds_read_b128 a[208:211], v0 offset:20480\n"
      "ds_read2st64_b32 v[30:31], v6 offset0:4 offset1:6\n"
      "v_mfma_f32_16x16x32_bf16 v[84:87], a[80:83], a[192:195], v[84:87]\n"
      "v_fma_f32 v47, 0.5, v59, -v71\n"
      "v_fma_f32 v48, 0.5, v60, -v72\n"
      "v_mfma_f32_16x16x32_bf16 v[76:79], a[84:87], a[196:199], v[76:79]\n"
      "ds_read_b128 a[212:215], v0 offset:16448\n"
      "ds_read_b64_tr_b16 a[220:221], v7 offset:33792\n"
      "v_mfma_f32_16x16x32_bf16 v[80:83], a[88:91], a[196:199], v[80:83]\n"
      "ds_read_b64_tr_b16 a[222:223], v7 offset:33800\n"
      "ds_read2st64_b32 v[32:33], v6 offset0:8 offset1:10\n"
      "v_fma_f32 v49, 0.5, v61, -v73\n"
      "v_mfma_f32_16x16x32_bf16 v[84:87], a[92:95], a[196:199], v[84:87]\n"
      "ds_read2st64_b32 v[34:35], v6 offset0:12 offset1:14\n"
      "v_fma_f32 v50, 0.5, v62, -v74\n"
      "v_mfma_f32_16x16x32_bf16 v[76:79], a[96:99], a[200:203], v[76:79]\n"
      "ds_read_b128 a[192:195], v0 offset:20544\n"
      "v_fma_f32 v51, 0.5, v63, -v75\n"
      "v_mfma_f32_16x16x32_bf16 v[80:83], a[100:103], a[200:203], v[80:83]\n"
      "ds_read2st64_b32 v[36:37], v6 offset0:16 offset1:18\n"
      "v_mfma_f32_16x16x32_bf16 v[84:87], a[104:107], a[200:203], v[84:87]\n"
      "s_waitcnt lgkmcnt(7)\n"
      "ds_read2st64_b32 v[38:39], v6 offset0:20 offset1:22\n"
      "v_sub_f32 v28, v28, v40\n"
      "v_sub_f32 v29, v29, v41\n"
      "v_sub_f32 v30, v30, v42\n"
      "v_mfma_f32_16x16x32_bf16 v[52:55], v[252:255], a[204:207], v[112:115]\n"
      "s_waitcnt lgkmcnt(5)\n"
      "ds_read_b128 a[196:199], v0 offset:16512\n"
      "v_sub_f32 v31, v31, v43\n"
      "v_mfma_f32_4x4x4_16b_bf16 v[12:15], a[216:217], a[220:221], 0\n"
      "v_fmac_f32 v28, s46, v100\n"
      "v_mfma_f32_4x4x4_16b_bf16 v[16:19], a[216:217], a[222:223], 0\n"
      "v_fmac_f32 v29, s46, v101\n"
      "v_fmac_f32 v30, s46, v102\n"
      "v_mfma_f32_16x16x32_bf16 v[56:59], v[244:247], a[204:207], v[116:119]\n"
      "v_fmac_f32 v31, s46, v103\n"
      "v_fmac_f32 v40, s45, v28\n"
      "v_mfma_f32_16x16x32_bf16 v[60:63], v[236:239], a[204:207], v[120:123]\n"
      "v_fmac_f32 v41, s45, v29\n"
      "v_cvt_pk_bf16_f32 v12, v12, v16\n"
      "v_fmac_f32 v42, s45, v30\n"
      "v_mfma_f32_16x16x32_bf16 v[64:67], v[248:251], a[208:211], v[112:115]\n"
      "ds_read_b128 a[200:203], v0 offset:20608\n"
      "ds_write_addtid_b32 v12 offset:24832\n"
      "v_mfma_f32_16x16x32_bf16 v[68:71], v[240:243], a[208:211], v[116:119]\n"
      "ds_read_b64_tr_b16 a[224:225], v7 offset:34304\n"
      "ds_read_b64_tr_b16 a[226:227], v7 offset:34312\n"
      "v_mfma_f32_16x16x32_bf16 v[72:75], v[232:235], a[208:211], v[120:123]\n"
      "v_fmac_f32 v43, s45, v31\n"
      "v_cvt_pk_bf16_f32 v13, v13, v17\n"
      "v_mfma_f32_16x16x32_bf16 v[52:55], v[228:231], a[212:215], v[52:55]\n"
      "ds_read_b128 a[204:207], v1 offset:16384\n"
      "ds_write_addtid_b32 v13 offset:28928\n"
      "v_fma_f32 v28, s47, v28, v76\n"
      "v_mfma_f32_16x16x32_bf16 v[56:59], v[220:223], a[212:215], v[56:59]\n"
      "v_fma_f32 v29, s47, v29, v77\n"
      "v_cvt_pk_bf16_f32 v14, v14, v18\n"
      "v_mfma_f32_16x16x32_bf16 v[60:63], v[212:215], a[212:215], v[60:63]\n"
      "s_waitcnt lgkmcnt(3)\n"
      "ds_write_addtid_b32 v14 offset:33024\n"
      "v_fma_f32 v30, s47, v30, v78\n"
      "v_mfma_f32_16x16x32_bf16 v[64:67], v[224:227], a[192:195], v[64:67]\n"
      "s_nop 0\n"
      "ds_read_b128 a[208:211], v1 offset:20480\n"
      "v_fma_f32 v31, s47, v31, v79\n"
      "v_mfma_f32_16x16x32_bf16 v[68:71], v[216:219], a[192:195], v[68:71]\n"
      "s_nop 0\n"
      "v_cvt_pk_bf16_f32 v40, v40, v41\n"
      "v_cvt_pk_bf16_f32 v15, v15, v19\n"
      "v_mfma_f32_16x16x32_bf16 v[72:75], v[208:211], a[192:195], v[72:75]\n"
      "s_waitcnt lgkmcnt(2)\n"
      "ds_write_addtid_b32 v40 offset:12288\n"
      "ds_write_addtid_b32 v15 offset:37120\n"
      "v_cvt_pk_bf16_f32 v42, v42, v43\n"
      "v_mfma_f32_4x4x4_16b_bf16 v[20:23], a[216:217], a[224:225], 0\n"
      "v_cvt_pk_bf16_f32 v28, v28, v29\n"
      "v_mfma_f32_4x4x4_16b_bf16 v[24:27], a[216:217], a[226:227], 0\n"
      "ds_write_addtid_b32 v42 offset:12800\n"
      "ds_write_addtid_b32 v28 offset:14336\n"
      "v_mfma_f32_16x16x32_bf16 v[52:55], v[204:207], a[196:199], v[52:55]\n"
      "ds_read_b128 a[212:215], v1 offset:16448\n"
      "v_cvt_pk_bf16_f32 v30, v30, v31\n"
      "v_mfma_f32_16x16x32_bf16 v[56:59], v[196:199], a[196:199], v[56:59]\n"
      "ds_write_addtid_b32 v30 offset:14848\n"
      "v_sub_f32 v32, v32, v44\n"
      "v_cvt_pk_bf16_f32 v20, v20, v24\n"
      "v_mfma_f32_16x16x32_bf16 v[60:63], v[188:191], a[196:199], v[60:63]\n"
      "ds_write_addtid_b32 v20 offset:25344\n"
      "v_sub_f32 v33, v33, v45\n"
      "v_mfma_f32_16x16x32_bf16 v[64:67], v[200:203], a[200:203], v[64:67]\n"
      "ds_read_b128 a[192:195], v1 offset:20544\n"
      "ds_read_b64_tr_b16 a[228:229], v7 offset:34816\n"
      "v_mfma_f32_16x16x32_bf16 v[68:71], v[192:195], a[200:203], v[68:71]\n"
      "ds_read_b64_tr_b16 a[230:231], v7 offset:34824\n"
      "v_sub_f32 v34, v34, v46\n"
      "v_mfma_f32_16x16x32_bf16 v[72:75], v[184:187], a[200:203], v[72:75]\n"
      "v_sub_f32 v35, v35, v47\n"
      "v_cvt_pk_bf16_f32 v21, v21, v25\n"
      "v_mfma_f32_16x16x32_bf16 v[52:55], v[180:183], a[204:207], v[52:55]\n"
      "ds_read_b128 a[196:199], v1 offset:16512\n"
      "ds_write_addtid_b32 v21 offset:29440\n"
      "v_fmac_f32 v32, s46, v104\n"
      "v_mfma_f32_16x16x32_bf16 v[56:59], v[172:175], a[204:207], v[56:59]\n"
      "v_fmac_f32 v33, s46, v105\n"
      "v_cvt_pk_bf16_f32 v22, v22, v26\n"
      "v_mfma_f32_16x16x32_bf16 v[60:63], v[164:167], a[204:207], v[60:63]\n"
      "s_waitcnt lgkmcnt(3)\n"
      "ds_write_addtid_b32 v22 offset:33536\n"
      "v_fmac_f32 v34, s46, v106\n"
      "v_mfma_f32_16x16x32_bf16 v[64:67], v[176:179], a[208:211], v[64:67]\n"
      "ds_read_b128 a[200:203], v1 offset:20608\n"
      "v_fmac_f32 v35, s46, v107\n"
      "v_mfma_f32_16x16x32_bf16 v[68:71], v[168:171], a[208:211], v[68:71]\n"
      "s_waitcnt lgkmcnt(4)\n"
      "v_fmac_f32 v44, s45, v32\n"
      "v_cvt_pk_bf16_f32 v23, v23, v27\n"
      "v_mfma_f32_4x4x4_16b_bf16 v[12:15], a[216:217], a[228:229], 0\n"
      "v_fmac_f32 v45, s45, v33\n"
      "v_mfma_f32_4x4x4_16b_bf16 v[16:19], a[216:217], a[230:231], 0\n"
      "ds_write_addtid_b32 v23 offset:37632\n"
      "v_fmac_f32_e64 v46, s45, v34\n"
      "v_mfma_f32_16x16x32_bf16 v[72:75], v[160:163], a[208:211], v[72:75]\n"
      "v_fmac_f32 v47, s45, v35\n"
      "v_fma_f32 v32, s47, v32, v80\n"
      "v_cvt_pk_bf16_f32 v12, v12, v16\n"
      "v_mfma_f32_16x16x32_bf16 v[52:55], v[156:159], a[212:215], v[52:55]\n"
      "ds_read_b128 a[204:207], v2 offset:16384\n"
      "ds_write_addtid_b32 v12 offset:25856\n"
      "v_mfma_f32_16x16x32_bf16 v[56:59], v[148:151], a[212:215], v[56:59]\n"
      "s_nop 0\n"
      "v_fma_f32 v33, s47, v33, v81\n"
      "v_cvt_pk_bf16_f32 v13, v13, v17\n"
      "v_mfma_f32_16x16x32_bf16 v[60:63], v[140:143], a[212:215], v[60:63]\n"
      "ds_read_b64_tr_b16 a[232:233], v7 offset:35328\n"
      "ds_write_addtid_b32 v13 offset:29952\n"
      "v_fma_f32 v34, s47, v34, v82\n"
      "v_mfma_f32_16x16x32_bf16 v[64:67], v[152:155], a[192:195], v[64:67]\n"
      "ds_read_b128 a[208:211], v2 offset:20480\n"
      "ds_read_b64_tr_b16 a[234:235], v7 offset:35336\n"
      "v_mfma_f32_16x16x32_bf16 v[68:71], v[144:147], a[192:195], v[68:71]\n"
      "s_nop 0\n"
      "v_fma_f32 v35, s47, v35, v83\n"
      "v_cvt_pk_bf16_f32 v44, v44, v45\n"
      "v_mfma_f32_16x16x32_bf16 v[72:75], v[136:139], a[192:195], v[72:75]\n"
      "s_waitcnt lgkmcnt(4)\n"
      "ds_write_addtid_b32 v44 offset:20484\n"
      "v_cvt_pk_bf16_f32 v14, v14, v18\n"
      "v_mfma_f32_16x16x32_bf16 v[52:55], v[132:135], a[196:199], v[52:55]\n"
      "ds_read_b128 a[212:215], v2 offset:16448\n"
      "ds_write_addtid_b32 v14 offset:34048\n"
      "v_cvt_pk_bf16_f32 v46, v46, v47\n"
      "v_mfma_f32_16x16x32_bf16 v[56:59], v[124:127], a[196:199], v[56:59]\n"
      "ds_write_addtid_b32 v46 offset:20996\n"
      "v_cvt_pk_bf16_f32 v32, v32, v33\n"
      "v_mfma_f32_16x16x32_bf16 v[60:63], a[184:187], a[196:199], v[60:63]\n"
      "ds_write_addtid_b32 v32 offset:22532\n"
      "v_cvt_pk_bf16_f32 v34, v34, v35\n"
      "v_mfma_f32_16x16x32_bf16 v[64:67], v[128:131], a[200:203], v[64:67]\n"
      "s_waitcnt lgkmcnt(5)\n"
      "ds_read_b128 a[192:195], v2 offset:20544\n"
      "ds_write_addtid_b32 v34 offset:23044\n"
      "v_sub_f32 v36, v36, v48\n"
      "v_mfma_f32_4x4x4_16b_bf16 v[20:23], a[216:217], a[232:233], 0\n"
      "v_sub_f32_e64 v37, v37, v49\n"
      "v_mfma_f32_4x4x4_16b_bf16 v[24:27], a[216:217], a[234:235], 0\n"
      "v_sub_f32 v38, v38, v50\n"
      "v_cvt_pk_bf16_f32 v15, v15, v19\n"
      "v_mfma_f32_16x16x32_bf16 v[68:71], a[188:191], a[200:203], v[68:71]\n"
      "ds_write_addtid_b32 v15 offset:38144\n"
      "v_sub_f32 v39, v39, v51\n"
      "v_cvt_pk_bf16_f32 v20, v20, v24\n"
      "v_mfma_f32_16x16x32_bf16 v[72:75], a[180:183], a[200:203], v[72:75]\n"
      "ds_write_addtid_b32 v20 offset:26368\n"
      "v_fmac_f32 v36, s46, v108\n"
      "v_mfma_f32_16x16x32_bf16 v[52:55], a[176:179], a[204:207], v[52:55]\n"
      "ds_read_b128 a[196:199], v2 offset:16512\n"
      "v_fmac_f32 v37, s46, v109\n"
      "v_mfma_f32_16x16x32_bf16 v[56:59], a[168:171], a[204:207], v[56:59]\n"
      "ds_read_b64_tr_b16 a[220:221], v7 offset:35840\n"
      "ds_read_b64_tr_b16 a[222:223], v7 offset:35848\n"
      "v_mfma_f32_16x16x32_bf16 v[60:63], a[160:163], a[204:207], v[60:63]\n"
      "v_fmac_f32_e64 v38, s46, v110\n"
      "v_cvt_pk_bf16_f32 v21, v21, v25\n"
      "v_mfma_f32_16x16x32_bf16 v[64:67], a[172:175], a[208:211], v[64:67]\n"
      "ds_read_b128 a[200:203], v2 offset:20608\n"
      "ds_write_addtid_b32 v21 offset:30464\n"
      "v_fmac_f32 v39, s46, v111\n"
      "v_mfma_f32_16x16x32_bf16 v[68:71], a[164:167], a[208:211], v[68:71]\n"
      "s_nop 0\n"
      "v_fmac_f32_e64 v48, s45, v36\n"
      "v_cvt_pk_bf16_f32 v22, v22, v26\n"
      "v_mfma_f32_16x16x32_bf16 v[72:75], a[156:159], a[208:211], v[72:75]\n"
      "s_waitcnt lgkmcnt(4)\n"
      "ds_write_addtid_b32 v22 offset:34560\n"
      "v_fmac_f32 v49, s45, v37\n"
      "v_mfma_f32_16x16x32_bf16 v[52:55], a[152:155], a[212:215], v[52:55]\n"
      "s_nop 0\n"
      "ds_read_b128 a[204:207], v3 offset:50688\n"
      "v_fmac_f32_e64 v50, s45, v38\n"
      "v_mfma_f32_16x16x32_bf16 v[56:59], a[144:147], a[212:215], v[56:59]\n"
      "s_nop 0\n"
      "v_fmac_f32 v51, s45, v39\n"
      "v_cvt_pk_bf16_f32 v23, v23, v27\n"
      "v_mfma_f32_16x16x32_bf16 v[60:63], a[136:139], a[212:215], v[60:63]\n"
      "s_waitcnt lgkmcnt(2)\n"
      "ds_write_addtid_b32 v23 offset:38656\n"
      "v_fma_f32 v36, s47, v36, v84\n"
      "v_mfma_f32_4x4x4_16b_bf16 v[12:15], a[216:217], a[220:221], 0\n"
      "v_fma_f32 v37, s47, v37, v85\n"
      "v_mfma_f32_4x4x4_16b_bf16 v[16:19], a[216:217], a[222:223], 0\n"
      "v_fma_f32 v38, s47, v38, v86\n"
      "v_fma_f32 v39, s47, v39, v87\n"
      "v_mfma_f32_16x16x32_bf16 v[64:67], a[148:151], a[192:195], v[64:67]\n"
      "ds_read_b128 a[208:211], v3 offset:50752\n"
      "v_cvt_pk_bf16_f32 v48, v48, v49\n"
      "v_cvt_pk_bf16_f32 v12, v12, v16\n"
      "v_mfma_f32_16x16x32_bf16 v[68:71], a[140:143], a[192:195], v[68:71]\n"
      "ds_write_addtid_b32 v48 offset:16384\n"
      "ds_write_addtid_b32 v12 offset:26880\n"
      "v_mfma_f32_16x16x32_bf16 v[72:75], a[132:135], a[192:195], v[72:75]\n"
      "v_cvt_pk_bf16_f32 v13, v13, v17\n"
      "v_cvt_pk_bf16_f32 v14, v14, v18\n"
      "v_mfma_f32_16x16x32_bf16 v[52:55], a[128:131], a[196:199], v[52:55]\n"
      "ds_read_b128 a[212:215], v3 offset:50816\n"
      "ds_write_addtid_b32 v13 offset:30976\n"
      "v_mfma_f32_16x16x32_bf16 v[56:59], a[120:123], a[196:199], v[56:59]\n"
      "ds_read_b64_tr_b16 a[224:225], v7 offset:36352\n"
      "ds_write_addtid_b32 v14 offset:35072\n"
      "v_cvt_pk_bf16_f32 v50, v50, v51\n"
      "v_mfma_f32_16x16x32_bf16 v[60:63], a[112:115], a[196:199], v[60:63]\n"
      "ds_write_addtid_b32 v50 offset:16896\n"
      "ds_read_b64_tr_b16 a[226:227], v7 offset:36360\n"
      "v_mfma_f32_16x16x32_bf16 v[64:67], a[124:127], a[200:203], v[64:67]\n"
      "ds_read_b128 a[192:195], v4 offset:50688\n"
      "v_cvt_pk_bf16_f32 v36, v36, v37\n"
      "v_mfma_f32_16x16x32_bf16 v[68:71], a[116:119], a[200:203], v[68:71]\n"
      "ds_write_addtid_b32 v36 offset:18432\n"
      "v_cvt_pk_bf16_f32 v15, v15, v19\n"
      "v_mfma_f32_16x16x32_bf16 v[72:75], a[108:111], a[200:203], v[72:75]\n"
      "s_waitcnt lgkmcnt(6)\n"
      "ds_write_addtid_b32 v15 offset:39168\n"
      "v_sub_f32 v52, v64, v52\n"
      "v_sub_f32_e64 v53, v65, v53\n"
      "v_cvt_pk_bf16_f32 v38, v38, v39\n"
      "v_mfma_f32_16x16x32_bf16 v[88:91], a[0:3], a[204:207], 0\n"
      "s_nop 0\n"
      "ds_read_b128 a[196:199], v4 offset:50752\n"
      "ds_write_addtid_b32 v38 offset:18944\n"
      "v_mfma_f32_16x16x32_bf16 v[92:95], a[4:7], a[204:207], 0\n"
      "s_waitcnt lgkmcnt(5)\n"
      "ds_write_addtid_b32 v52 offset:6144\n"
      "ds_write_addtid_b32 v53 offset:6656\n"
      "v_mfma_f32_4x4x4_16b_bf16 v[20:23], a[216:217], a[224:225], 0\n"
      "v_sub_f32 v54, v66, v54\n"
      "v_mfma_f32_4x4x4_16b_bf16 v[24:27], a[216:217], a[226:227], 0\n"
      "ds_write_addtid_b32 v54 offset:7168\n"
      "v_sub_f32 v55, v67, v55\n"
      "v_mfma_f32_16x16x32_bf16 v[96:99], a[8:11], a[204:207], 0\n"
      "ds_write_addtid_b32 v55 offset:7680\n"
      "v_sub_f32 v56, v68, v56\n"
      "v_sub_f32 v57, v69, v57\n"
      "v_mfma_f32_16x16x32_bf16 v[88:91], a[12:15], a[208:211], v[88:91]\n"
      "ds_read_b128 a[200:203], v4 offset:50816\n"
      "ds_write_addtid_b32 v56 offset:8192\n"
      "v_mfma_f32_16x16x32_bf16 v[92:95], a[16:19], a[208:211], v[92:95]\n"
      "ds_write_addtid_b32 v57 offset:8704\n"
      "v_sub_f32 v58, v70, v58\n"
      "v_mfma_f32_16x16x32_bf16 v[96:99], a[20:23], a[208:211], v[96:99]\n"
      "s_nop 0\n"
      "ds_write_addtid_b32 v58 offset:9216\n"
      "v_cvt_pk_bf16_f32 v20, v20, v24\n"
      "v_mfma_f32_16x16x32_bf16 v[88:91], a[24:27], a[212:215], v[88:91]\n"
      "ds_read_b128 a[204:207], v5 offset:50688\n"
      "ds_write_addtid_b32 v20 offset:27392\n"
      "v_sub_f32 v59, v71, v59\n"
      "v_mfma_f32_16x16x32_bf16 v[92:95], a[28:31], a[212:215], v[92:95]\n"
      "ds_write_addtid_b32 v59 offset:9728\n"
      "v_sub_f32_e64 v60, v72, v60\n"
      "v_mfma_f32_16x16x32_bf16 v[96:99], a[32:35], a[212:215], v[96:99]\n"
      "s_waitcnt lgkmcnt(5)\n"
      "ds_write_addtid_b32 v60 offset:10240\n"
      "v_cvt_pk_bf16_f32 v21, v21, v25\n"
      "v_mfma_f32_16x16x32_bf16 v[88:91], a[36:39], a[192:195], v[88:91]\n"
      "ds_read_b128 a[208:211], v5 offset:50752\n"
      "ds_write_addtid_b32 v21 offset:31488\n"
      "v_mfma_f32_16x16x32_bf16 v[92:95], a[40:43], a[192:195], v[92:95]\n"
      "v_sub_f32 v61, v73, v61\n"
      "v_sub_f32 v62, v74, v62\n"
      "v_cvt_pk_bf16_f32 v22, v22, v26\n"
      "v_mfma_f32_16x16x32_bf16 v[96:99], a[44:47], a[192:195], v[96:99]\n"
      "ds_write_addtid_b32 v22 offset:35584\n"
      "ds_write_addtid_b32 v61 offset:10752\n"
      "v_sub_f32 v63, v75, v63\n"
      "v_mfma_f32_16x16x32_bf16 v[88:91], a[48:51], a[196:199], v[88:91]\n"
      "ds_read_b128 a[212:215], v5 offset:50816\n"
      "ds_write_addtid_b32 v62 offset:11264\n"
      "v_mfma_f32_16x16x32_bf16 v[92:95], a[52:55], a[196:199], v[92:95]\n"
      "s_nop 0\n"
      "ds_write_addtid_b32 v63 offset:11776\n"
      "v_cvt_pk_bf16_f32 v23, v23, v27\n"
      "v_mfma_f32_16x16x32_bf16 v[96:99], a[56:59], a[196:199], v[96:99]\n"
      "ds_write_addtid_b32 v23 offset:39680\n"
      "v_fma_f32 v40, 0.5, v52, -v64\n"
      "v_mfma_f32_16x16x32_bf16 v[88:91], a[60:63], a[200:203], v[88:91]\n"
      "v_fma_f32 v41, 0.5, v53, -v65\n"
      "v_fma_f32 v42, 0.5, v54, -v66\n"
      "v_mfma_f32_16x16x32_bf16 v[92:95], a[64:67], a[200:203], v[92:95]\n"
      "v_fma_f32 v43, 0.5, v55, -v67\n"
      "v_fma_f32 v44, 0.5, v56, -v68\n"
      "v_mfma_f32_16x16x32_bf16 v[96:99], a[68:71], a[200:203], v[96:99]\n"
      "s_waitcnt lgkmcnt(0)\n"
      "v_fma_f32 v45, 0.5, v57, -v69\n"
      "s_barrier\n"
      "LSmid_%=:\n"
      "v_mfma_f32_16x16x32_bf16 v[88:91], a[72:75], a[204:207], v[88:91]\n"
      "ds_read_b128 a[192:195], v0 offset:0\n"
      "ds_read2st64_b32 v[28:29], v6 offset0:24 offset1:26\n"
      "v_fma_f32 v46, 0.5, v58, -v70\n"
      "v_mfma_f32_16x16x32_bf16 v[92:95], a[76:79], a[204:207], v[92:95]\n"
      "ds_read_b128 a[196:199], v0 offset:4096\n"
      "ds_read2st64_b32 v[30:31], v6 offset0:28 offset1:30\n"
      "v_mfma_f32_16x16x32_bf16 v[96:99], a[80:83], a[204:207], v[96:99]\n"
      "v_fma_f32 v47, 0.5, v59, -v71\n"
      "v_fma_f32 v48, 0.5, v60, -v72\n"
      "v_mfma_f32_16x16x32_bf16 v[88:91], a[84:87], a[208:211], v[88:91]\n"
      "ds_read_b128 a[200:203], v0 offset:64\n"
      "ds_read_b64_tr_b16 a[220:221], v7 offset:50688\n"
      "v_mfma_f32_16x16x32_bf16 v[92:95], a[88:91], a[208:211], v[92:95]\n"
      "ds_read_b64_tr_b16 a[222:223], v7 offset:50696\n"
      "ds_read2st64_b32 v[32:33], v6 offset0:32 offset1:34\n"
      "v_fma_f32 v49, 0.5, v61, -v73\n"
      "v_mfma_f32_16x16x32_bf16 v[96:99], a[92:95], a[208:211], v[96:99]\n"
      "ds_read2st64_b32 v[34:35], v6 offset0:36 offset1:38\n"
      "v_fma_f32 v50, 0.5, v62, -v74\n"
      "v_mfma_f32_16x16x32_bf16 v[88:91], a[96:99], a[212:215], v[88:91]\n"
      "ds_read_b128 a[204:207], v0 offset:4160\n"
      "v_fma_f32 v51, 0.5, v63, -v75\n"
      "v_mfma_f32_16x16x32_bf16 v[92:95], a[100:103], a[212:215], v[92:95]\n"
      "ds_read2st64_b32 v[36:37], v6 offset0:40 offset1:42\n"
      "v_mfma_f32_16x16x32_bf16 v[96:99], a[104:107], a[212:215], v[96:99]\n"
      "s_waitcnt lgkmcnt(7)\n"
      "ds_read2st64_b32 v[38:39], v6 offset0:44 offset1:46\n"
      "v_sub_f32 v28, v28, v40\n"
      "v_sub_f32 v29, v29, v41\n"
      "v_sub_f32 v30, v30, v42\n"
      "v_mfma_f32_16x16x32_bf16 v[52:55], v[252:255], a[192:195], v[76:79]\n"
      "s_waitcnt lgkmcnt(5)\n"
      "ds_read_b128 a[208:211], v0 offset:128\n"
      "v_sub_f32 v31, v31, v43\n"
      "v_mfma_f32_4x4x4_16b_bf16 v[12:15], a[216:217], a[220:221], 0\n"
      "v_fmac_f32_e64 v28, s46, v112\n"
      "v_mfma_f32_4x4x4_16b_bf16 v[16:19], a[216:217], a[222:223], 0\n"
      "v_fmac_f32 v29, s46, v113\n"
      "v_fmac_f32 v30, s46, v114\n"
      "v_mfma_f32_16x16x32_bf16 v[56:59], v[244:247], a[192:195], v[80:83]\n"
      "v_fmac_f32 v31, s46, v115\n"
      "v_fmac_f32 v40, s45, v28\n"
      "v_mfma_f32_16x16x32_bf16 v[60:63], v[236:239], a[192:195], v[84:87]\n"
      "v_fmac_f32 v41, s45, v29\n"
      "v_cvt_pk_bf16_f32 v12, v12, v16\n"
      "v_fmac_f32 v42, s45, v30\n"
      "v_mfma_f32_16x16x32_bf16 v[64:67], v[248:251], a[196:199], v[76:79]\n"
      "ds_read_b128 a[212:215], v0 offset:4224\n"
      "ds_write_addtid_b32 v12 offset:41216\n"
      "v_mfma_f32_16x16x32_bf16 v[68:71], v[240:243], a[196:199], v[80:83]\n"
      "ds_read_b64_tr_b16 a[224:225], v7 offset:51200\n"
      "ds_read_b64_tr_b16 a[226:227], v7 offset:51208\n"
      "v_mfma_f32_16x16x32_bf16 v[72:75], v[232:235], a[196:199], v[84:87]\n"
      "v_fmac_f32_e64 v43, s45, v31\n"
      "v_cvt_pk_bf16_f32 v13, v13, v17\n"
      "v_mfma_f32_16x16x32_bf16 v[52:55], v[228:231], a[200:203], v[52:55]\n"
      "s_nop 0\n"
      "ds_read_b128 a[192:195], v1 offset:0\n"
      "ds_write_addtid_b32 v13 offset:45312\n"
      "v_fma_f32 v28, s47, v28, v88\n"
      "v_mfma_f32_16x16x32_bf16 v[56:59], v[220:223], a[200:203], v[56:59]\n"
      "s_nop 0\n"
      "v_fma_f32 v29, s47, v29, v89\n"
      "v_cvt_pk_bf16_f32 v14, v14, v18\n"
      "v_mfma_f32_16x16x32_bf16 v[60:63], v[212:215], a[200:203], v[60:63]\n"
      "s_waitcnt lgkmcnt(3)\n"
      "ds_write_addtid_b32 v14 offset:49408\n"
      "v_fma_f32 v30, s47, v30, v90\n"
      "v_mfma_f32_16x16x32_bf16 v[64:67], v[224:227], a[204:207], v[64:67]\n"
      "s_nop 0\n"
      "ds_read_b128 a[196:199], v1 offset:4096\n"
      "v_fma_f32 v31, s47, v31, v91\n"
      "v_mfma_f32_16x16x32_bf16 v[68:71], v[216:219], a[204:207], v[68:71]\n"
      "s_nop 0\n"
      "v_cvt_pk_bf16_f32 v40, v40, v41\n"
      "v_cvt_pk_bf16_f32 v15, v15, v19\n"
      "v_mfma_f32_16x16x32_bf16 v[72:75], v[208:211], a[204:207], v[72:75]\n"
      "s_waitcnt lgkmcnt(2)\n"
      "ds_write_addtid_b32 v40 offset:13312\n"
      "ds_write_addtid_b32 v15 offset:53504\n"
      "v_cvt_pk_bf16_f32 v42, v42, v43\n"
      "v_mfma_f32_4x4x4_16b_bf16 v[20:23], a[216:217], a[224:225], 0\n"
      "v_cvt_pk_bf16_f32 v28, v28, v29\n"
      "v_mfma_f32_4x4x4_16b_bf16 v[24:27], a[216:217], a[226:227], 0\n"
      "ds_write_addtid_b32 v42 offset:13824\n"
      "ds_write_addtid_b32 v28 offset:15360\n"
      "v_mfma_f32_16x16x32_bf16 v[52:55], v[204:207], a[208:211], v[52:55]\n"
      "ds_read_b128 a[200:203], v1 offset:64\n"
      "v_cvt_pk_bf16_f32 v30, v30, v31\n"
      "v_mfma_f32_16x16x32_bf16 v[56:59], v[196:199], a[208:211], v[56:59]\n"
      "ds_write_addtid_b32 v30 offset:15872\n"
      "v_sub_f32 v32, v32, v44\n"
      "v_cvt_pk_bf16_f32 v20, v20, v24\n"
      "v_mfma_f32_16x16x32_bf16 v[60:63], v[188:191], a[208:211], v[60:63]\n"
      "ds_write_addtid_b32 v20 offset:41728\n"
      "v_sub_f32 v33, v33, v45\n"
      "v_mfma_f32_16x16x32_bf16 v[64:67], v[200:203], a[212:215], v[64:67]\n"
      "ds_read_b128 a[204:207], v1 offset:4160\n"
      "ds_read_b64_tr_b16 a[228:229], v7 offset:51712\n"
      "v_mfma_f32_16x16x32_bf16 v[68:71], v[192:195], a[212:215], v[68:71]\n"
      "ds_read_b64_tr_b16 a[230:231], v7 offset:51720\n"
      "v_sub_f32 v34, v34, v46\n"
      "v_mfma_f32_16x16x32_bf16 v[72:75], v[184:187], a[212:215], v[72:75]\n"
      "v_sub_f32 v35, v35, v47\n"
      "v_cvt_pk_bf16_f32 v21, v21, v25\n"
      "v_mfma_f32_16x16x32_bf16 v[52:55], v[180:183], a[192:195], v[52:55]\n"
      "ds_read_b128 a[208:211], v1 offset:128\n"
      "ds_write_addtid_b32 v21 offset:45824\n"
      "v_fmac_f32 v32, s46, v116\n"
      "v_mfma_f32_16x16x32_bf16 v[56:59], v[172:175], a[192:195], v[56:59]\n"
      "v_fmac_f32 v33, s46, v117\n"
      "v_cvt_pk_bf16_f32 v22, v22, v26\n"
      "v_mfma_f32_16x16x32_bf16 v[60:63], v[164:167], a[192:195], v[60:63]\n"
      "s_waitcnt lgkmcnt(3)\n"
      "ds_write_addtid_b32 v22 offset:49920\n"
      "v_fmac_f32 v34, s46, v118\n"
      "v_mfma_f32_16x16x32_bf16 v[64:67], v[176:179], a[196:199], v[64:67]\n"
      "ds_read_b128 a[212:215], v1 offset:4224\n"
      "v_fmac_f32 v35, s46, v119\n"
      "v_mfma_f32_16x16x32_bf16 v[68:71], v[168:171], a[196:199], v[68:71]\n"
      "s_waitcnt lgkmcnt(4)\n"
      "v_fmac_f32 v44, s45, v32\n"
      "v_cvt_pk_bf16_f32 v23, v23, v27\n"
      "v_mfma_f32_4x4x4_16b_bf16 v[12:15], a[216:217], a[228:229], 0\n"
      "v_fmac_f32 v45, s45, v33\n"
      "v_mfma_f32_4x4x4_16b_bf16 v[16:19], a[216:217], a[230:231], 0\n"
      "ds_write_addtid_b32 v23 offset:54016\n"
      "v_fmac_f32_e64 v46, s45, v34\n"
      "v_mfma_f32_16x16x32_bf16 v[72:75], v[160:163], a[196:199], v[72:75]\n"
      "v_fmac_f32 v47, s45, v35\n"
      "v_fma_f32 v32, s47, v32, v92\n"
      "v_cvt_pk_bf16_f32 v12, v12, v16\n"
      "v_mfma_f32_16x16x32_bf16 v[52:55], v[156:159], a[200:203], v[52:55]\n"
      "ds_read_b128 a[192:195], v2 offset:0\n"
      "ds_write_addtid_b32 v12 offset:42240\n"
      "v_mfma_f32_16x16x32_bf16 v[56:59], v[148:151], a[200:203], v[56:59]\n"
      "s_nop 0\n"
      "v_fma_f32 v33, s47, v33, v93\n"
      "v_cvt_pk_bf16_f32 v13, v13, v17\n"
      "v_mfma_f32_16x16x32_bf16 v[60:63], v[140:143], a[200:203], v[60:63]\n"
      "ds_read_b64_tr_b16 a[232:233], v7 offset:52224\n"
      "ds_write_addtid_b32 v13 offset:46336\n"
      "v_fma_f32 v34, s47, v34, v94\n"
      "v_mfma_f32_16x16x32_bf16 v[64:67], v[152:155], a[204:207], v[64:67]\n"
      "ds_read_b128 a[196:199], v2 offset:4096\n"
      "ds_read_b64_tr_b16 a[234:235], v7 offset:52232\n"
      "v_mfma_f32_16x16x32_bf16 v[68:71], v[144:147], a[204:207], v[68:71]\n"
      "s_nop 0\n"
      "v_fma_f32 v35, s47, v35, v95\n"
      "v_cvt_pk_bf16_f32 v44, v44, v45\n"
      "v_mfma_f32_16x16x32_bf16 v[72:75], v[136:139], a[204:207], v[72:75]\n"
      "s_waitcnt lgkmcnt(4)\n"
      "ds_write_addtid_b32 v44 offset:21508\n"
      "v_cvt_pk_bf16_f32 v14, v14, v18\n"
      "v_mfma_f32_16x16x32_bf16 v[52:55], v[132:135], a[208:211], v[52:55]\n"
      "ds_read_b128 a[200:203], v2 offset:64\n"
      "ds_write_addtid_b32 v14 offset:50432\n"
      "v_cvt_pk_bf16_f32 v46, v46, v47\n"
      "v_mfma_f32_16x16x32_bf16 v[56:59], v[124:127], a[208:211], v[56:59]\n"
      "ds_write_addtid_b32 v46 offset:22020\n"
      "v_cvt_pk_bf16_f32 v32, v32, v33\n"
      "v_mfma_f32_16x16x32_bf16 v[60:63], a[184:187], a[208:211], v[60:63]\n"
      "ds_write_addtid_b32 v32 offset:23556\n"
      "v_cvt_pk_bf16_f32 v34, v34, v35\n"
      "v_mfma_f32_16x16x32_bf16 v[64:67], v[128:131], a[212:215], v[64:67]\n"
      "s_waitcnt lgkmcnt(5)\n"
      "ds_read_b128 a[204:207], v2 offset:4160\n"
      "ds_write_addtid_b32 v34 offset:24068\n"
      "v_sub_f32 v36, v36, v48\n"
      "v_mfma_f32_4x4x4_16b_bf16 v[20:23], a[216:217], a[232:233], 0\n"
      "v_sub_f32_e64 v37, v37, v49\n"
      "v_mfma_f32_4x4x4_16b_bf16 v[24:27], a[216:217], a[234:235], 0\n"
      "v_sub_f32 v38, v38, v50\n"
      "v_cvt_pk_bf16_f32 v15, v15, v19\n"
      "v_mfma_f32_16x16x32_bf16 v[68:71], a[188:191], a[212:215], v[68:71]\n"
      "ds_write_addtid_b32 v15 offset:54528\n"
      "v_sub_f32 v39, v39, v51\n"
      "v_cvt_pk_bf16_f32 v20, v20, v24\n"
      "v_mfma_f32_16x16x32_bf16 v[72:75], a[180:183], a[212:215], v[72:75]\n"
      "ds_write_addtid_b32 v20 offset:42752\n"
      "v_fmac_f32 v36, s46, v120\n"
      "v_mfma_f32_16x16x32_bf16 v[52:55], a[176:179], a[192:195], v[52:55]\n"
      "ds_read_b128 a[208:211], v2 offset:128\n"
      "v_fmac_f32 v37, s46, v121\n"
      "v_mfma_f32_16x16x32_bf16 v[56:59], a[168:171], a[192:195], v[56:59]\n"
      "ds_read_b64_tr_b16 a[220:221], v7 offset:52736\n"
      "ds_read_b64_tr_b16 a[222:223], v7 offset:52744\n"
      "v_mfma_f32_16x16x32_bf16 v[60:63], a[160:163], a[192:195], v[60:63]\n"
      "v_fmac_f32_e64 v38, s46, v122\n"
      "v_cvt_pk_bf16_f32 v21, v21, v25\n"
      "v_mfma_f32_16x16x32_bf16 v[64:67], a[172:175], a[196:199], v[64:67]\n"
      "ds_read_b128 a[212:215], v2 offset:4224\n"
      "ds_write_addtid_b32 v21 offset:46848\n"
      "v_fmac_f32 v39, s46, v123\n"
      "v_mfma_f32_16x16x32_bf16 v[68:71], a[164:167], a[196:199], v[68:71]\n"
      "s_nop 0\n"
      "v_fmac_f32_e64 v48, s45, v36\n"
      "v_cvt_pk_bf16_f32 v22, v22, v26\n"
      "v_mfma_f32_16x16x32_bf16 v[72:75], a[156:159], a[196:199], v[72:75]\n"
      "s_waitcnt lgkmcnt(4)\n"
      "ds_write_addtid_b32 v22 offset:50944\n"
      "v_fmac_f32 v49, s45, v37\n"
      "v_mfma_f32_16x16x32_bf16 v[52:55], a[152:155], a[200:203], v[52:55]\n"
      "s_nop 0\n"
      "ds_read_b128 a[192:195], v3 offset:0\n"
      "v_fmac_f32_e64 v50, s45, v38\n"
      "v_mfma_f32_16x16x32_bf16 v[56:59], a[144:147], a[200:203], v[56:59]\n"
      "s_nop 0\n"
      "v_fmac_f32 v51, s45, v39\n"
      "v_cvt_pk_bf16_f32 v23, v23, v27\n"
      "v_mfma_f32_16x16x32_bf16 v[60:63], a[136:139], a[200:203], v[60:63]\n"
      "s_waitcnt lgkmcnt(2)\n"
      "ds_write_addtid_b32 v23 offset:55040\n"
      "v_fma_f32 v36, s47, v36, v96\n"
      "v_mfma_f32_4x4x4_16b_bf16 v[12:15], a[216:217], a[220:221], 0\n"
      "v_fma_f32 v37, s47, v37, v97\n"
      "v_mfma_f32_4x4x4_16b_bf16 v[16:19], a[216:217], a[222:223], 0\n"
      "v_fma_f32 v38, s47, v38, v98\n"
      "v_fma_f32 v39, s47, v39, v99\n"
      "v_mfma_f32_16x16x32_bf16 v[64:67], a[148:151], a[204:207], v[64:67]\n"
      "ds_read_b128 a[196:199], v3 offset:64\n"
      "v_cvt_pk_bf16_f32 v48, v48, v49\n"
      "v_cvt_pk_bf16_f32 v12, v12, v16\n"
      "v_mfma_f32_16x16x32_bf16 v[68:71], a[140:143], a[204:207], v[68:71]\n"
      "ds_write_addtid_b32 v48 offset:17408\n"
      "ds_write_addtid_b32 v12 offset:43264\n"
      "v_mfma_f32_16x16x32_bf16 v[72:75], a[132:135], a[204:207], v[72:75]\n"
      "v_cvt_pk_bf16_f32 v13, v13, v17\n"
      "v_cvt_pk_bf16_f32 v14, v14, v18\n"
      "v_mfma_f32_16x16x32_bf16 v[52:55], a[128:131], a[208:211], v[52:55]\n"
      "ds_read_b128 a[200:203], v3 offset:128\n"
      "ds_write_addtid_b32 v13 offset:47360\n"
      "v_mfma_f32_16x16x32_bf16 v[56:59], a[120:123], a[208:211], v[56:59]\n"
      "ds_read_b64_tr_b16 a[224:225], v7 offset:53248\n"
      "ds_write_addtid_b32 v14 offset:51456\n"
      "v_cvt_pk_bf16_f32 v50, v50, v51\n"
      "v_mfma_f32_16x16x32_bf16 v[60:63], a[112:115], a[208:211], v[60:63]\n"
      "ds_write_addtid_b32 v50 offset:17920\n"
      "ds_read_b64_tr_b16 a[226:227], v7 offset:53256\n"
      "v_mfma_f32_16x16x32_bf16 v[64:67], a[124:127], a[212:215], v[64:67]\n"
      "ds_read_b128 a[204:207], v4 offset:0\n"
      "v_cvt_pk_bf16_f32 v36, v36, v37\n"
      "v_mfma_f32_16x16x32_bf16 v[68:71], a[116:119], a[212:215], v[68:71]\n"
      "ds_write_addtid_b32 v36 offset:19456\n"
      "v_cvt_pk_bf16_f32 v15, v15, v19\n"
      "v_mfma_f32_16x16x32_bf16 v[72:75], a[108:111], a[212:215], v[72:75]\n"
      "s_waitcnt lgkmcnt(6)\n"
      "ds_write_addtid_b32 v15 offset:55552\n"
      "v_sub_f32 v52, v64, v52\n"
      "v_sub_f32_e64 v53, v65, v53\n"
      "v_cvt_pk_bf16_f32 v38, v38, v39\n"
      "v_mfma_f32_16x16x32_bf16 v[100:103], a[0:3], a[192:195], 0\n"
      "s_nop 0\n"
      "ds_read_b128 a[208:211], v4 offset:64\n"
      "ds_write_addtid_b32 v38 offset:19968\n"
      "v_mfma_f32_16x16x32_bf16 v[104:107], a[4:7], a[192:195], 0\n"
      "s_waitcnt lgkmcnt(5)\n"
      "ds_write_addtid_b32 v52 offset:0\n"
      "ds_write_addtid_b32 v53 offset:512\n"
      "v_mfma_f32_4x4x4_16b_bf16 v[20:23], a[216:217], a[224:225], 0\n"
      "v_sub_f32 v54, v66, v54\n"
      "v_mfma_f32_4x4x4_16b_bf16 v[24:27], a[216:217], a[226:227], 0\n"
      "ds_write_addtid_b32 v54 offset:1024\n"
      "v_sub_f32 v55, v67, v55\n"
      "v_mfma_f32_16x16x32_bf16 v[108:111], a[8:11], a[192:195], 0\n"
      "ds_write_addtid_b32 v55 offset:1536\n"
      "v_sub_f32 v56, v68, v56\n"
      "v_sub_f32 v57, v69, v57\n"
      "v_mfma_f32_16x16x32_bf16 v[100:103], a[12:15], a[196:199], v[100:103]\n"
      "ds_read_b128 a[212:215], v4 offset:128\n"
      "ds_write_addtid_b32 v56 offset:2048\n"
      "v_mfma_f32_16x16x32_bf16 v[104:107], a[16:19], a[196:199], v[104:107]\n"
      "ds_write_addtid_b32 v57 offset:2560\n"
      "v_sub_f32 v58, v70, v58\n"
      "v_mfma_f32_16x16x32_bf16 v[108:111], a[20:23], a[196:199], v[108:111]\n"
      "s_nop 0\n"
      "ds_write_addtid_b32 v58 offset:3072\n"
      "v_cvt_pk_bf16_f32 v20, v20, v24\n"
      "v_mfma_f32_16x16x32_bf16 v[100:103], a[24:27], a[200:203], v[100:103]\n"
      "ds_read_b128 a[192:195], v5 offset:0\n"
      "ds_write_addtid_b32 v20 offset:43776\n"
      "v_sub_f32 v59, v71, v59\n"
      "v_mfma_f32_16x16x32_bf16 v[104:107], a[28:31], a[200:203], v[104:107]\n"
      "ds_write_addtid_b32 v59 offset:3584\n"
      "v_sub_f32_e64 v60, v72, v60\n"
      "v_mfma_f32_16x16x32_bf16 v[108:111], a[32:35], a[200:203], v[108:111]\n"
      "s_waitcnt lgkmcnt(5)\n"
      "ds_write_addtid_b32 v60 offset:4096\n"
      "v_cvt_pk_bf16_f32 v21, v21, v25\n"
      "v_mfma_f32_16x16x32_bf16 v[100:103], a[36:39], a[204:207], v[100:103]\n"
      "ds_read_b128 a[196:199], v5 offset:64\n"
      "ds_write_addtid_b32 v21 offset:47872\n"
      "v_mfma_f32_16x16x32_bf16 v[104:107], a[40:43], a[204:207], v[104:107]\n"
      "v_sub_f32 v61, v73, v61\n"
      "v_sub_f32 v62, v74, v62\n"
      "v_cvt_pk_bf16_f32 v22, v22, v26\n"
      "v_mfma_f32_16x16x32_bf16 v[108:111], a[44:47], a[204:207], v[108:111]\n"
      "ds_write_addtid_b32 v22 offset:51968\n"
      "ds_write_addtid_b32 v61 offset:4608\n"
      "v_sub_f32 v63, v75, v63\n"
      "v_mfma_f32_16x16x32_bf16 v[100:103], a[48:51], a[208:211], v[100:103]\n"
      "ds_read_b128 a[200:203], v5 offset:128\n"
      "ds_write_addtid_b32 v62 offset:5120\n"
      "v_mfma_f32_16x16x32_bf16 v[104:107], a[52:55], a[208:211], v[104:107]\n"
      "s_nop 0\n"
      "ds_write_addtid_b32 v63 offset:5632\n"
      "v_cvt_pk_bf16_f32 v23, v23, v27\n"
      "v_mfma_f32_16x16x32_bf16 v[108:111], a[56:59], a[208:211], v[108:111]\n"
      "ds_write_addtid_b32 v23 offset:56064\n"
      "v_fma_f32 v40, 0.5, v52, -v64\n"
      "v_mfma_f32_16x16x32_bf16 v[100:103], a[60:63], a[212:215], v[100:103]\n"
      "v_fma_f32 v41, 0.5, v53, -v65\n"
      "v_fma_f32 v42, 0.5, v54, -v66\n"
      "v_mfma_f32_16x16x32_bf16 v[104:107], a[64:67], a[212:215], v[104:107]\n"
      "v_fma_f32 v43, 0.5, v55, -v67\n"
      "v_fma_f32 v44, 0.5, v56, -v68\n"
      "v_mfma_f32_16x16x32_bf16 v[108:111], a[68:71], a[212:215], v[108:111]\n"
      "s_waitcnt lgkmcnt(0)\n"
      "v_fma_f32 v45, 0.5, v57, -v69\n"
      "s_barrier\n"
      "v_mfma_f32_16x16x32_bf16 v[100:103], a[72:75], a[192:195], v[100:103]\n"
      "ds_read_b128 a[204:207], v0 offset:16384\n"
      "ds_read2st64_b32 v[28:29], v6 offset0:0 offset1:2\n"
      "v_fma_f32 v46, 0.5, v58, -v70\n"
      "v_mfma_f32_16x16x32_bf16 v[104:107], a[76:79], a[192:195], v[104:107]\n"
      "ds_read_b128 a[208:211], v0 offset:20480\n"
      "ds_read2st64_b32 v[30:31], v6 offset0:4 offset1:6\n"
      "v_mfma_f32_16x16x32_bf16 v[108:111], a[80:83], a[192:195], v[108:111]\n"
      "v_fma_f32 v47, 0.5, v59, -v71\n"
      "v_fma_f32 v48, 0.5, v60, -v72\n"
      "v_mfma_f32_16x16x32_bf16 v[100:103], a[84:87], a[196:199], v[100:103]\n"
      "ds_read_b128 a[212:215], v0 offset:16448\n"
      "ds_read_b64_tr_b16 a[220:221], v7 offset:0\n"
      "v_mfma_f32_16x16x32_bf16 v[104:107], a[88:91], a[196:199], v[104:107]\n"
      "ds_read_b64_tr_b16 a[222:223], v7 offset:8\n"
      "ds_read2st64_b32 v[32:33], v6 offset0:8 offset1:10\n"
      "v_fma_f32 v49, 0.5, v61, -v73\n"
      "v_mfma_f32_16x16x32_bf16 v[108:111], a[92:95], a[196:199], v[108:111]\n"
      "ds_read2st64_b32 v[34:35], v6 offset0:12 offset1:14\n"
      "v_fma_f32 v50, 0.5, v62, -v74\n"
      "v_mfma_f32_16x16x32_bf16 v[100:103], a[96:99], a[200:203], v[100:103]\n"
      "ds_read_b128 a[192:195], v0 offset:20544\n"
      "v_fma_f32 v51, 0.5, v63, -v75\n"
      "v_mfma_f32_16x16x32_bf16 v[104:107], a[100:103], a[200:203], v[104:107]\n"
      "ds_read2st64_b32 v[36:37], v6 offset0:16 offset1:18\n"
      "v_mfma_f32_16x16x32_bf16 v[108:111], a[104:107], a[200:203], v[108:111]\n"
      "s_waitcnt lgkmcnt(7)\n"
      "ds_read2st64_b32 v[38:39], v6 offset0:20 offset1:22\n"
      "v_sub_f32 v28, v28, v40\n"
      "v_sub_f32 v29, v29, v41\n"
      "v_sub_f32 v30, v30, v42\n"
      "v_mfma_f32_16x16x32_bf16 v[52:55], v[252:255], a[204:207], v[88:91]\n"
      "s_waitcnt lgkmcnt(5)\n"
      "ds_read_b128 a[196:199], v0 offset:16512\n"
      "v_sub_f32 v31, v31, v43\n"
      "v_mfma_f32_4x4x4_16b_bf16 v[12:15], a[216:217], a[220:221], 0\n"
      "v_fmac_f32_e64 v28, s46, v76\n"
      "v_mfma_f32_4x4x4_16b_bf16 v[16:19], a[216:217], a[222:223], 0\n"
      "v_fmac_f32 v29, s46, v77\n"
      "v_fmac_f32 v30, s46, v78\n"
      "v_mfma_f32_16x16x32_bf16 v[56:59], v[244:247], a[204:207], v[92:95]\n"
      "v_fmac_f32 v31, s46, v79\n"
      "v_fmac_f32 v40, s45, v28\n"
      "v_mfma_f32_16x16x32_bf16 v[60:63], v[236:239], a[204:207], v[96:99]\n"
      "v_fmac_f32 v41, s45, v29\n"
      "v_cvt_pk_bf16_f32 v12, v12, v16\n"
      "v_fmac_f32 v42, s45, v30\n"
      "v_mfma_f32_16x16x32_bf16 v[64:67], v[248:251], a[208:211], v[88:91]\n"
      "ds_read_b128 a[200:203], v0 offset:20608\n"
      "ds_write_addtid_b32 v12 offset:24832\n"
      "v_mfma_f32_16x16x32_bf16 v[68:71], v[240:243], a[208:211], v[92:95]\n"
      "ds_read_b64_tr_b16 a[224:225], v7 offset:512\n"
      "ds_read_b64_tr_b16 a[226:227], v7 offset:520\n"
      "v_mfma_f32_16x16x32_bf16 v[72:75], v[232:235], a[208:211], v[96:99]\n"
      "v_fmac_f32_e64 v43, s45, v31\n"
      "v_cvt_pk_bf16_f32 v13, v13, v17\n"
      "v_mfma_f32_16x16x32_bf16 v[52:55], v[228:231], a[212:215], v[52:55]\n"
      "s_nop 0\n"
      "ds_read_b128 a[204:207], v1 offset:16384\n"
      "ds_write_addtid_b32 v13 offset:28928\n"
      "v_fma_f32 v28, s47, v28, v100\n"
      "v_mfma_f32_16x16x32_bf16 v[56:59], v[220:223], a[212:215], v[56:59]\n"
      "s_nop 0\n"
      "v_fma_f32 v29, s47, v29, v101\n"
      "v_cvt_pk_bf16_f32 v14, v14, v18\n"
      "v_mfma_f32_16x16x32_bf16 v[60:63], v[212:215], a[212:215], v[60:63]\n"
      "s_waitcnt lgkmcnt(3)\n"
      "ds_write_addtid_b32 v14 offset:33024\n"
      "v_fma_f32 v30, s47, v30, v102\n"
      "v_mfma_f32_16x16x32_bf16 v[64:67], v[224:227], a[192:195], v[64:67]\n"
      "s_nop 0\n"
      "ds_read_b128 a[208:211], v1 offset:20480\n"
      "v_fma_f32 v31, s47, v31, v103\n"
      "v_mfma_f32_16x16x32_bf16 v[68:71], v[216:219], a[192:195], v[68:71]\n"
      "s_nop 0\n"
      "v_cvt_pk_bf16_f32 v40, v40, v41\n"
      "v_cvt_pk_bf16_f32 v15, v15, v19\n"
      "v_mfma_f32_16x16x32_bf16 v[72:75], v[208:211], a[192:195], v[72:75]\n"
      "s_waitcnt lgkmcnt(2)\n"
      "ds_write_addtid_b32 v40 offset:12288\n"
      "ds_write_addtid_b32 v15 offset:37120\n"
      "v_cvt_pk_bf16_f32 v42, v42, v43\n"
      "v_mfma_f32_4x4x4_16b_bf16 v[20:23], a[216:217], a[224:225], 0\n"
      "v_cvt_pk_bf16_f32 v28, v28, v29\n"
      "v_mfma_f32_4x4x4_16b_bf16 v[24:27], a[216:217], a[226:227], 0\n"
      "ds_write_addtid_b32 v42 offset:12800\n"
      "ds_write_addtid_b32 v28 offset:14336\n"
      "v_mfma_f32_16x16x32_bf16 v[52:55], v[204:207], a[196:199], v[52:55]\n"
      "ds_read_b128 a[212:215], v1 offset:16448\n"
      "v_cvt_pk_bf16_f32 v30, v30, v31\n"
      "v_mfma_f32_16x16x32_bf16 v[56:59], v[196:199], a[196:199], v[56:59]\n"
      "ds_write_addtid_b32 v30 offset:14848\n"
      "v_sub_f32 v32, v32, v44\n"
      "v_cvt_pk_bf16_f32 v20, v20, v24\n"
      "v_mfma_f32_16x16x32_bf16 v[60:63], v[188:191], a[196:199], v[60:63]\n"
      "ds_write_addtid_b32 v20 offset:25344\n"
      "v_sub_f32 v33, v33, v45\n"
      "v_mfma_f32_16x16x32_bf16 v[64:67], v[200:203], a[200:203], v[64:67]\n"
      "ds_read_b128 a[192:195], v1 offset:20544\n"
      "ds_read_b64_tr_b16 a[228:229], v7 offset:1024\n"
      "v_mfma_f32_16x16x32_bf16 v[68:71], v[192:195], a[200:203], v[68:71]\n"
      "ds_read_b64_tr_b16 a[230:231], v7 offset:1032\n"
      "v_sub_f32 v34, v34, v46\n"
      "v_mfma_f32_16x16x32_bf16 v[72:75], v[184:187], a[200:203], v[72:75]\n"
      "v_sub_f32 v35, v35, v47\n"
      "v_cvt_pk_bf16_f32 v21, v21, v25\n"
      "v_mfma_f32_16x16x32_bf16 v[52:55], v[180:183], a[204:207], v[52:55]\n"
      "ds_read_b128 a[196:199], v1 offset:16512\n"
      "ds_write_addtid_b32 v21 offset:29440\n"
      "v_fmac_f32 v32, s46, v80\n"
      "v_mfma_f32_16x16x32_bf16 v[56:59], v[172:175], a[204:207], v[56:59]\n"
      "v_fmac_f32 v33, s46, v81\n"
      "v_cvt_pk_bf16_f32 v22, v22, v26\n"
      "v_mfma_f32_16x16x32_bf16 v[60:63], v[164:167], a[204:207], v[60:63]\n"
      "s_waitcnt lgkmcnt(3)\n"
      "ds_write_addtid_b32 v22 offset:33536\n"
      "v_fmac_f32 v34, s46, v82\n"
      "v_mfma_f32_16x16x32_bf16 v[64:67], v[176:179], a[208:211], v[64:67]\n"
      "ds_read_b128 a[200:203], v1 offset:20608\n"
      "v_fmac_f32 v35, s46, v83\n"
      "v_mfma_f32_16x16x32_bf16 v[68:71], v[168:171], a[208:211], v[68:71]\n"
      "s_waitcnt lgkmcnt(4)\n"
      "v_fmac_f32 v44, s45, v32\n"
      "v_cvt_pk_bf16_f32 v23, v23, v27\n"
      "v_mfma_f32_4x4x4_16b_bf16 v[12:15], a[216:217], a[228:229], 0\n"
      "v_fmac_f32 v45, s45, v33\n"
      "v_mfma_f32_4x4x4_16b_bf16 v[16:19], a[216:217], a[230:231], 0\n"
      "ds_write_addtid_b32 v23 offset:37632\n"
      "v_fmac_f32_e64 v46, s45, v34\n"
      "v_mfma_f32_16x16x32_bf16 v[72:75], v[160:163], a[208:211], v[72:75]\n"
      "v_fmac_f32 v47, s45, v35\n"
      "v_fma_f32 v32, s47, v32, v104\n"
      "v_cvt_pk_bf16_f32 v12, v12, v16\n"
      "v_mfma_f32_16x16x32_bf16 v[52:55], v[156:159], a[212:215], v[52:55]\n"
      "ds_read_b128 a[204:207], v2 offset:16384\n"
      "ds_write_addtid_b32 v12 offset:25856\n"
      "v_mfma_f32_16x16x32_bf16 v[56:59], v[148:151], a[212:215], v[56:59]\n"
      "s_nop 0\n"
      "v_fma_f32 v33, s47, v33, v105\n"
      "v_cvt_pk_bf16_f32 v13, v13, v17\n"
      "v_mfma_f32_16x16x32_bf16 v[60:63], v[140:143], a[212:215], v[60:63]\n"
      "ds_read_b64_tr_b16 a[232:233], v7 offset:1536\n"
      "ds_write_addtid_b32 v13 offset:29952\n"
      "v_fma_f32 v34, s47, v34, v106\n"
      "v_mfma_f32_16x16x32_bf16 v[64:67], v[152:155], a[192:195], v[64:67]\n"
      "ds_read_b128 a[208:211], v2 offset:20480\n"
      "ds_read_b64_tr_b16 a[234:235], v7 offset:1544\n"
      "v_mfma_f32_16x16x32_bf16 v[68:71], v[144:147], a[192:195], v[68:71]\n"
      "s_nop 0\n"
      "v_fma_f32 v35, s47, v35, v107\n"
      "v_cvt_pk_bf16_f32 v44, v44, v45\n"
      "v_mfma_f32_16x16x32_bf16 v[72:75], v[136:139], a[192:195], v[72:75]\n"
      "s_waitcnt lgkmcnt(4)\n"
      "ds_write_addtid_b32 v44 offset:20484\n"
      "v_cvt_pk_bf16_f32 v14, v14, v18\n"
      "v_mfma_f32_16x16x32_bf16 v[52:55], v[132:135], a[196:199], v[52:55]\n"
      "ds_read_b128 a[212:215], v2 offset:16448\n"
      "ds_write_addtid_b32 v14 offset:34048\n"
      "v_cvt_pk_bf16_f32 v46, v46, v47\n"
      "v_mfma_f32_16x16x32_bf16 v[56:59], v[124:127], a[196:199], v[56:59]\n"
      "ds_write_addtid_b32 v46 offset:20996\n"
      "v_cvt_pk_bf16_f32 v32, v32, v33\n"
      "v_mfma_f32_16x16x32_bf16 v[60:63], a[184:187], a[196:199], v[60:63]\n"
      "ds_write_addtid_b32 v32 offset:22532\n"
      "v_cvt_pk_bf16_f32 v34, v34, v35\n"
      "v_mfma_f32_16x16x32_bf16 v[64:67], v[128:131], a[200:203], v[64:67]\n"
      "s_waitcnt lgkmcnt(5)\n"
      "ds_read_b128 a[192:195], v2 offset:20544\n"
      "ds_write_addtid_b32 v34 offset:23044\n"
      "v_sub_f32 v36, v36, v48\n"
      "v_mfma_f32_4x4x4_16b_bf16 v[20:23], a[216:217], a[232:233], 0\n"
      "v_sub_f32_e64 v37, v37, v49\n"
      "v_mfma_f32_4x4x4_16b_bf16 v[24:27], a[216:217], a[234:235], 0\n"
      "v_sub_f32 v38, v38, v50\n"
      "v_cvt_pk_bf16_f32 v15, v15, v19\n"
      "v_mfma_f32_16x16x32_bf16 v[68:71], a[188:191], a[200:203], v[68:71]\n"
      "ds_write_addtid_b32 v15 offset:38144\n"
      "v_sub_f32 v39, v39, v51\n"
      "v_cvt_pk_bf16_f32 v20, v20, v24\n"
      "v_mfma_f32_16x16x32_bf16 v[72:75], a[180:183], a[200:203], v[72:75]\n"
      "ds_write_addtid_b32 v20 offset:26368\n"
      "v_fmac_f32 v36, s46, v84\n"
      "v_mfma_f32_16x16x32_bf16 v[52:55], a[176:179], a[204:207], v[52:55]\n"
      "ds_read_b128 a[196:199], v2 offset:16512\n"
      "v_fmac_f32 v37, s46, v85\n"
      "v_mfma_f32_16x16x32_bf16 v[56:59], a[168:171], a[204:207], v[56:59]\n"
      "ds_read_b64_tr_b16 a[220:221], v7 offset:2048\n"
      "ds_read_b64_tr_b16 a[222:223], v7 offset:2056\n"
      "v_mfma_f32_16x16x32_bf16 v[60:63], a[160:163], a[204:207], v[60:63]\n"
      "v_fmac_f32_e64 v38, s46, v86\n"
      "v_cvt_pk_bf16_f32 v21, v21, v25\n"
      "v_mfma_f32_16x16x32_bf16 v[64:67], a[172:175], a[208:211], v[64:67]\n"
      "ds_read_b128 a[200:203], v2 offset:20608\n"
      "ds_write_addtid_b32 v21 offset:30464\n"
      "v_fmac_f32 v39, s46, v87\n"
      "v_mfma_f32_16x16x32_bf16 v[68:71], a[164:167], a[208:211], v[68:71]\n"
      "s_nop 0\n"
      "v_fmac_f32_e64 v48, s45, v36\n"
      "v_cvt_pk_bf16_f32 v22, v22, v26\n"
      "v_mfma_f32_16x16x32_bf16 v[72:75], a[156:159], a[208:211], v[72:75]\n"
      "s_waitcnt lgkmcnt(4)\n"
      "ds_write_addtid_b32 v22 offset:34560\n"
      "v_fmac_f32 v49, s45, v37\n"
      "v_mfma_f32_16x16x32_bf16 v[52:55], a[152:155], a[212:215], v[52:55]\n"
      "s_nop 0\n"
      "ds_read_b128 a[204:207], v3 offset:16896\n"
      "v_fmac_f32_e64 v50, s45, v38\n"
      "v_mfma_f32_16x16x32_bf16 v[56:59], a[144:147], a[212:215], v[56:59]\n"
      "s_nop 0\n"
      "v_fmac_f32 v51, s45, v39\n"
      "v_cvt_pk_bf16_f32 v23, v23, v27\n"
      "v_mfma_f32_16x16x32_bf16 v[60:63], a[136:139], a[212:215], v[60:63]\n"
      "s_waitcnt lgkmcnt(2)\n"
      "ds_write_addtid_b32 v23 offset:38656\n"
      "v_fma_f32 v36, s47, v36, v108\n"
      "v_mfma_f32_4x4x4_16b_bf16 v[12:15], a[216:217], a[220:221], 0\n"
      "v_fma_f32 v37, s47, v37, v109\n"
      "v_mfma_f32_4x4x4_16b_bf16 v[16:19], a[216:217], a[222:223], 0\n"
      "v_fma_f32 v38, s47, v38, v110\n"
      "v_fma_f32 v39, s47, v39, v111\n"
      "v_mfma_f32_16x16x32_bf16 v[64:67], a[148:151], a[192:195], v[64:67]\n"
      "ds_read_b128 a[208:211], v3 offset:16960\n"
      "v_cvt_pk_bf16_f32 v48, v48, v49\n"
      "v_cvt_pk_bf16_f32 v12, v12, v16\n"
      "v_mfma_f32_16x16x32_bf16 v[68:71], a[140:143], a[192:195], v[68:71]\n"
      "ds_write_addtid_b32 v48 offset:16384\n"
      "ds_write_addtid_b32 v12 offset:26880\n"
      "v_mfma_f32_16x16x32_bf16 v[72:75], a[132:135], a[192:195], v[72:75]\n"
      "v_cvt_pk_bf16_f32 v13, v13, v17\n"
      "v_cvt_pk_bf16_f32 v14, v14, v18\n"
      "v_mfma_f32_16x16x32_bf16 v[52:55], a[128:131], a[196:199], v[52:55]\n"
      "ds_read_b128 a[212:215], v3 offset:17024\n"
      "ds_write_addtid_b32 v13 offset:30976\n"
      "v_mfma_f32_16x16x32_bf16 v[56:59], a[120:123], a[196:199], v[56:59]\n"
      "ds_read_b64_tr_b16 a[224:225], v7 offset:2560\n"
      "ds_write_addtid_b32 v14 offset:35072\n"
      "v_cvt_pk_bf16_f32 v50, v50, v51\n"
      "v_mfma_f32_16x16x32_bf16 v[60:63], a[112:115], a[196:199], v[60:63]\n"
      "ds_write_addtid_b32 v50 offset:16896\n"
      "ds_read_b64_tr_b16 a[226:227], v7 offset:2568\n"
      "v_mfma_f32_16x16x32_bf16 v[64:67], a[124:127], a[200:203], v[64:67]\n"
      "ds_read_b128 a[192:195], v4 offset:16896\n"
      "v_cvt_pk_bf16_f32 v36, v36, v37\n"
      "v_mfma_f32_16x16x32_bf16 v[68:71], a[116:119], a[200:203], v[68:71]\n"
      "ds_write_addtid_b32 v36 offset:18432\n"
      "v_cvt_pk_bf16_f32 v15, v15, v19\n"
      "v_mfma_f32_16x16x32_bf16 v[72:75], a[108:111], a[200:203], v[72:75]\n"
      "s_waitcnt lgkmcnt(6)\n"
      "ds_write_addtid_b32 v15 offset:39168\n"
      "v_sub_f32 v52, v64, v52\n"
      "v_sub_f32_e64 v53, v65, v53\n"
      "v_cvt_pk_bf16_f32 v38, v38, v39\n"
      "v_mfma_f32_16x16x32_bf16 v[112:115], a[0:3], a[204:207], 0\n"
      "s_nop 0\n"
      "ds_read_b128 a[196:199], v4 offset:16960\n"
      "ds_write_addtid_b32 v38 offset:18944\n"
      "v_mfma_f32_16x16x32_bf16 v[116:119], a[4:7], a[204:207], 0\n"
      "s_waitcnt lgkmcnt(5)\n"
      "ds_write_addtid_b32 v52 offset:6144\n"
      "ds_write_addtid_b32 v53 offset:6656\n"
      "v_mfma_f32_4x4x4_16b_bf16 v[20:23], a[216:217], a[224:225], 0\n"
      "v_sub_f32 v54, v66, v54\n"
      "v_mfma_f32_4x4x4_16b_bf16 v[24:27], a[216:217], a[226:227], 0\n"
      "ds_write_addtid_b32 v54 offset:7168\n"
      "v_sub_f32 v55, v67, v55\n"
      "v_mfma_f32_16x16x32_bf16 v[120:123], a[8:11], a[204:207], 0\n"
      "ds_write_addtid_b32 v55 offset:7680\n"
      "v_sub_f32 v56, v68, v56\n"
      "v_sub_f32 v57, v69, v57\n"
      "v_mfma_f32_16x16x32_bf16 v[112:115], a[12:15], a[208:211], v[112:115]\n"
      "ds_read_b128 a[200:203], v4 offset:17024\n"
      "ds_write_addtid_b32 v56 offset:8192\n"
      "v_mfma_f32_16x16x32_bf16 v[116:119], a[16:19], a[208:211], v[116:119]\n"
      "ds_write_addtid_b32 v57 offset:8704\n"
      "v_sub_f32 v58, v70, v58\n"
      "v_mfma_f32_16x16x32_bf16 v[120:123], a[20:23], a[208:211], v[120:123]\n"
      "s_nop 0\n"
      "ds_write_addtid_b32 v58 offset:9216\n"
      "v_cvt_pk_bf16_f32 v20, v20, v24\n"
      "v_mfma_f32_16x16x32_bf16 v[112:115], a[24:27], a[212:215], v[112:115]\n"
      "ds_read_b128 a[204:207], v5 offset:16896\n"
      "ds_write_addtid_b32 v20 offset:27392\n"
      "v_sub_f32 v59, v71, v59\n"
      "v_mfma_f32_16x16x32_bf16 v[116:119], a[28:31], a[212:215], v[116:119]\n"
      "ds_write_addtid_b32 v59 offset:9728\n"
      "v_sub_f32_e64 v60, v72, v60\n"
      "v_mfma_f32_16x16x32_bf16 v[120:123], a[32:35], a[212:215], v[120:123]\n"
      "s_waitcnt lgkmcnt(5)\n"
      "ds_write_addtid_b32 v60 offset:10240\n"
      "v_cvt_pk_bf16_f32 v21, v21, v25\n"
      "v_mfma_f32_16x16x32_bf16 v[112:115], a[36:39], a[192:195], v[112:115]\n"
      "ds_read_b128 a[208:211], v5 offset:16960\n"
      "ds_write_addtid_b32 v21 offset:31488\n"
      "v_mfma_f32_16x16x32_bf16 v[116:119], a[40:43], a[192:195], v[116:119]\n"
      "v_sub_f32 v61, v73, v61\n"
      "v_sub_f32 v62, v74, v62\n"
      "v_cvt_pk_bf16_f32 v22, v22, v26\n"
      "v_mfma_f32_16x16x32_bf16 v[120:123], a[44:47], a[192:195], v[120:123]\n"
      "ds_write_addtid_b32 v22 offset:35584\n"
      "ds_write_addtid_b32 v61 offset:10752\n"
      "v_sub_f32 v63, v75, v63\n"
      "v_mfma_f32_16x16x32_bf16 v[112:115], a[48:51], a[196:199], v[112:115]\n"
      "ds_read_b128 a[212:215], v5 offset:17024\n"
      "ds_write_addtid_b32 v62 offset:11264\n"
      "v_mfma_f32_16x16x32_bf16 v[116:119], a[52:55], a[196:199], v[116:119]\n"
      "s_nop 0\n"
      "ds_write_addtid_b32 v63 offset:11776\n"
      "v_cvt_pk_bf16_f32 v23, v23, v27\n"
      "v_mfma_f32_16x16x32_bf16 v[120:123], a[56:59], a[196:199], v[120:123]\n"
      "ds_write_addtid_b32 v23 offset:39680\n"
      "v_fma_f32 v40, 0.5, v52, -v64\n"
      "v_mfma_f32_16x16x32_bf16 v[112:115], a[60:63], a[200:203], v[112:115]\n"
      "v_fma_f32 v41, 0.5, v53, -v65\n"
      "v_fma_f32 v42, 0.5, v54, -v66\n"
      "v_mfma_f32_16x16x32_bf16 v[116:119], a[64:67], a[200:203], v[116:119]\n"
      "v_fma_f32 v43, 0.5, v55, -v67\n"
      "v_fma_f32 v44, 0.5, v56, -v68\n"
      "v_mfma_f32_16x16x32_bf16 v[120:123], a[68:71], a[200:203], v[120:123]\n"
      "s_waitcnt lgkmcnt(0)\n"
      "v_fma_f32 v45, 0.5, v57, -v69\n"
      "s_barrier\n"
      "v_mfma_f32_16x16x32_bf16 v[112:115], a[72:75], a[204:207], v[112:115]\n"
      "ds_read_b128 a[192:195], v0 offset:0\n"
      "ds_read2st64_b32 v[28:29], v6 offset0:24 offset1:26\n"
      "v_fma_f32 v46, 0.5, v58, -v70\n"
      "v_mfma_f32_16x16x32_bf16 v[116:119], a[76:79], a[204:207], v[116:119]\n"
      "ds_read_b128 a[196:199], v0 offset:4096\n"
      "ds_read2st64_b32 v[30:31], v6 offset0:28 offset1:30\n"
      "v_mfma_f32_16x16x32_bf16 v[120:123], a[80:83], a[204:207], v[120:123]\n"
      "v_fma_f32 v47, 0.5, v59, -v71\n"
      "v_fma_f32 v48, 0.5, v60, -v72\n"
      "v_mfma_f32_16x16x32_bf16 v[112:115], a[84:87], a[208:211], v[112:115]\n"
      "ds_read_b128 a[200:203], v0 offset:64\n"
      "ds_read_b64_tr_b16 a[220:221], v7 offset:16896\n"
      "v_mfma_f32_16x16x32_bf16 v[116:119], a[88:91], a[208:211], v[116:119]\n"
      "ds_read_b64_tr_b16 a[222:223], v7 offset:16904\n"
      "ds_read2st64_b32 v[32:33], v6 offset0:32 offset1:34\n"
      "v_fma_f32 v49, 0.5, v61, -v73\n"
      "v_mfma_f32_16x16x32_bf16 v[120:123], a[92:95], a[208:211], v[120:123]\n"
      "ds_read2st64_b32 v[34:35], v6 offset0:36 offset1:38\n"
      "v_fma_f32 v50, 0.5, v62, -v74\n"
      "v_mfma_f32_16x16x32_bf16 v[112:115], a[96:99], a[212:215], v[112:115]\n"
      "ds_read_b128 a[204:207], v0 offset:4160\n"
      "v_fma_f32 v51, 0.5, v63, -v75\n"
      "v_mfma_f32_16x16x32_bf16 v[116:119], a[100:103], a[212:215], v[116:119]\n"
      "ds_read2st64_b32 v[36:37], v6 offset0:40 offset1:42\n"
      "v_mfma_f32_16x16x32_bf16 v[120:123], a[104:107], a[212:215], v[120:123]\n"
      "s_waitcnt lgkmcnt(7)\n"
      "ds_read2st64_b32 v[38:39], v6 offset0:44 offset1:46\n"
      "v_sub_f32 v28, v28, v40\n"
      "v_sub_f32 v29, v29, v41\n"
      "v_sub_f32 v30, v30, v42\n"
      "v_mfma_f32_16x16x32_bf16 v[52:55], v[252:255], a[192:195], v[100:103]\n"
      "s_waitcnt lgkmcnt(5)\n"
      "ds_read_b128 a[208:211], v0 offset:128\n"
      "v_sub_f32 v31, v31, v43\n"
      "v_mfma_f32_4x4x4_16b_bf16 v[12:15], a[216:217], a[220:221], 0\n"
      "v_fmac_f32_e64 v28, s46, v88\n"
      "v_mfma_f32_4x4x4_16b_bf16 v[16:19], a[216:217], a[222:223], 0\n"
      "v_fmac_f32 v29, s46, v89\n"
      "v_fmac_f32 v30, s46, v90\n"
      "v_mfma_f32_16x16x32_bf16 v[56:59], v[244:247], a[192:195], v[104:107]\n"
      "v_fmac_f32 v31, s46, v91\n"
      "v_fmac_f32 v40, s45, v28\n"
      "v_mfma_f32_16x16x32_bf16 v[60:63], v[236:239], a[192:195], v[108:111]\n"
      "v_fmac_f32 v41, s45, v29\n"
      "v_cvt_pk_bf16_f32 v12, v12, v16\n"
      "v_fmac_f32 v42, s45, v30\n"
      "v_mfma_f32_16x16x32_bf16 v[64:67], v[248:251], a[196:199], v[100:103]\n"
      "ds_read_b128 a[212:215], v0 offset:4224\n"
      "ds_write_addtid_b32 v12 offset:41216\n"
      "v_mfma_f32_16x16x32_bf16 v[68:71], v[240:243], a[196:199], v[104:107]\n"
      "ds_read_b64_tr_b16 a[224:225], v7 offset:17408\n"
      "ds_read_b64_tr_b16 a[226:227], v7 offset:17416\n"
      "v_mfma_f32_16x16x32_bf16 v[72:75], v[232:235], a[196:199], v[108:111]\n"
      "v_fmac_f32_e64 v43, s45, v31\n"
      "v_cvt_pk_bf16_f32 v13, v13, v17\n"
      "v_mfma_f32_16x16x32_bf16 v[52:55], v[228:231], a[200:203], v[52:55]\n"
      "s_nop 0\n"
      "ds_read_b128 a[192:195], v1 offset:0\n"
      "ds_write_addtid_b32 v13 offset:45312\n"
      "v_fma_f32 v28, s47, v28, v112\n"
      "v_mfma_f32_16x16x32_bf16 v[56:59], v[220:223], a[200:203], v[56:59]\n"
      "s_nop 0\n"
      "v_fma_f32 v29, s47, v29, v113\n"
      "v_cvt_pk_bf16_f32 v14, v14, v18\n"
      "v_mfma_f32_16x16x32_bf16 v[60:63], v[212:215], a[200:203], v[60:63]\n"
      "s_waitcnt lgkmcnt(3)\n"
      "ds_write_addtid_b32 v14 offset:49408\n"
      "v_fma_f32 v30, s47, v30, v114\n"
      "v_mfma_f32_16x16x32_bf16 v[64:67], v[224:227], a[204:207], v[64:67]\n"
      "s_nop 0\n"
      "ds_read_b128 a[196:199], v1 offset:4096\n"
      "v_fma_f32 v31, s47, v31, v115\n"
      "v_mfma_f32_16x16x32_bf16 v[68:71], v[216:219], a[204:207], v[68:71]\n"
      "s_nop 0\n"
      "v_cvt_pk_bf16_f32 v40, v40, v41\n"
      "v_cvt_pk_bf16_f32 v15, v15, v19\n"
      "v_mfma_f32_16x16x32_bf16 v[72:75], v[208:211], a[204:207], v[72:75]\n"
      "s_waitcnt lgkmcnt(2)\n"
      "ds_write_addtid_b32 v40 offset:13312\n"
      "ds_write_addtid_b32 v15 offset:53504\n"
      "v_cvt_pk_bf16_f32 v42, v42, v43\n"
      "v_mfma_f32_4x4x4_16b_bf16 v[20:23], a[216:217], a[224:225], 0\n"
      "v_cvt_pk_bf16_f32 v28, v28, v29\n"
      "v_mfma_f32_4x4x4_16b_bf16 v[24:27], a[216:217], a[226:227], 0\n"
      "ds_write_addtid_b32 v42 offset:13824\n"
      "ds_write_addtid_b32 v28 offset:15360\n"
      "v_mfma_f32_16x16x32_bf16 v[52:55], v[204:207], a[208:211], v[52:55]\n"
      "ds_read_b128 a[200:203], v1 offset:64\n"
      "v_cvt_pk_bf16_f32 v30, v30, v31\n"
      "v_mfma_f32_16x16x32_bf16 v[56:59], v[196:199], a[208:211], v[56:59]\n"
      "ds_write_addtid_b32 v30 offset:15872\n"
      "v_sub_f32 v32, v32, v44\n"
      "v_cvt_pk_bf16_f32 v20, v20, v24\n"
      "v_mfma_f32_16x16x32_bf16 v[60:63], v[188:191], a[208:211], v[60:63]\n"
      "ds_write_addtid_b32 v20 offset:41728\n"
      "v_sub_f32 v33, v33, v45\n"
      "v_mfma_f32_16x16x32_bf16 v[64:67], v[200:203], a[212:215], v[64:67]\n"
      "ds_read_b128 a[204:207], v1 offset:4160\n"
      "ds_read_b64_tr_b16 a[228:229], v7 offset:17920\n"
      "v_mfma_f32_16x16x32_bf16 v[68:71], v[192:195], a[212:215], v[68:71]\n"
      "ds_read_b64_tr_b16 a[230:231], v7 offset:17928\n"
      "v_sub_f32 v34, v34, v46\n"
      "v_mfma_f32_16x16x32_bf16 v[72:75], v[184:187], a[212:215], v[72:75]\n"
      "v_sub_f32 v35, v35, v47\n"
      "v_cvt_pk_bf16_f32 v21, v21, v25\n"
      "v_mfma_f32_16x16x32_bf16 v[52:55], v[180:183], a[192:195], v[52:55]\n"
      "ds_read_b128 a[208:211], v1 offset:128\n"
      "ds_write_addtid_b32 v21 offset:45824\n"
      "v_fmac_f32 v32, s46, v92\n"
      "v_mfma_f32_16x16x32_bf16 v[56:59], v[172:175], a[192:195], v[56:59]\n"
      "v_fmac_f32 v33, s46, v93\n"
      "v_cvt_pk_bf16_f32 v22, v22, v26\n"
      "v_mfma_f32_16x16x32_bf16 v[60:63], v[164:167], a[192:195], v[60:63]\n"
      "s_waitcnt lgkmcnt(3)\n"
      "ds_write_addtid_b32 v22 offset:49920\n"
      "v_fmac_f32 v34, s46, v94\n"
      "v_mfma_f32_16x16x32_bf16 v[64:67], v[176:179], a[196:199], v[64:67]\n"
      "ds_read_b128 a[212:215], v1 offset:4224\n"
      "v_fmac_f32 v35, s46, v95\n"
      "v_mfma_f32_16x16x32_bf16 v[68:71], v[168:171], a[196:199], v[68:71]\n"
      "s_waitcnt lgkmcnt(4)\n"
      "v_fmac_f32 v44, s45, v32\n"
      "v_cvt_pk_bf16_f32 v23, v23, v27\n"
      "v_mfma_f32_4x4x4_16b_bf16 v[12:15], a[216:217], a[228:229], 0\n"
      "v_fmac_f32 v45, s45, v33\n"
      "v_mfma_f32_4x4x4_16b_bf16 v[16:19], a[216:217], a[230:231], 0\n"
      "ds_write_addtid_b32 v23 offset:54016\n"
      "v_fmac_f32_e64 v46, s45, v34\n"
      "v_mfma_f32_16x16x32_bf16 v[72:75], v[160:163], a[196:199], v[72:75]\n"
      "v_fmac_f32 v47, s45, v35\n"
      "v_fma_f32 v32, s47, v32, v116\n"
      "v_cvt_pk_bf16_f32 v12, v12, v16\n"
      "v_mfma_f32_16x16x32_bf16 v[52:55], v[156:159], a[200:203], v[52:55]\n"
      "ds_read_b128 a[192:195], v2 offset:0\n"
      "ds_write_addtid_b32 v12 offset:42240\n"
      "v_mfma_f32_16x16x32_bf16 v[56:59], v[148:151], a[200:203], v[56:59]\n"
      "s_nop 0\n"
      "v_fma_f32 v33, s47, v33, v117\n"
      "v_cvt_pk_bf16_f32 v13, v13, v17\n"
      "v_mfma_f32_16x16x32_bf16 v[60:63], v[140:143], a[200:203], v[60:63]\n"
      "ds_read_b64_tr_b16 a[232:233], v7 offset:18432\n"
      "ds_write_addtid_b32 v13 offset:46336\n"
      "v_fma_f32 v34, s47, v34, v118\n"
      "v_mfma_f32_16x16x32_bf16 v[64:67], v[152:155], a[204:207], v[64:67]\n"
      "ds_read_b128 a[196:199], v2 offset:4096\n"
      "ds_read_b64_tr_b16 a[234:235], v7 offset:18440\n"
      "v_mfma_f32_16x16x32_bf16 v[68:71], v[144:147], a[204:207], v[68:71]\n"
      "s_nop 0\n"
      "v_fma_f32 v35, s47, v35, v119\n"
      "v_cvt_pk_bf16_f32 v44, v44, v45\n"
      "v_mfma_f32_16x16x32_bf16 v[72:75], v[136:139], a[204:207], v[72:75]\n"
      "s_waitcnt lgkmcnt(4)\n"
      "ds_write_addtid_b32 v44 offset:21508\n"
      "v_cvt_pk_bf16_f32 v14, v14, v18\n"
      "v_mfma_f32_16x16x32_bf16 v[52:55], v[132:135], a[208:211], v[52:55]\n"
      "ds_read_b128 a[200:203], v2 offset:64\n"
      "ds_write_addtid_b32 v14 offset:50432\n"
      "v_cvt_pk_bf16_f32 v46, v46, v47\n"
      "v_mfma_f32_16x16x32_bf16 v[56:59], v[124:127], a[208:211], v[56:59]\n"
      "ds_write_addtid_b32 v46 offset:22020\n"
      "v_cvt_pk_bf16_f32 v32, v32, v33\n"
      "v_mfma_f32_16x16x32_bf16 v[60:63], a[184:187], a[208:211], v[60:63]\n"
      "ds_write_addtid_b32 v32 offset:23556\n"
      "v_cvt_pk_bf16_f32 v34, v34, v35\n"
      "v_mfma_f32_16x16x32_bf16 v[64:67], v[128:131], a[212:215], v[64:67]\n"
      "s_waitcnt lgkmcnt(5)\n"
      "ds_read_b128 a[204:207], v2 offset:4160\n"
      "ds_write_addtid_b32 v34 offset:24068\n"
      "v_sub_f32 v36, v36, v48\n"
      "v_mfma_f32_4x4x4_16b_bf16 v[20:23], a[216:217], a[232:233], 0\n"
      "v_sub_f32_e64 v37, v37, v49\n"
      "v_mfma_f32_4x4x4_16b_bf16 v[24:27], a[216:217], a[234:235], 0\n"
      "v_sub_f32 v38, v38, v50\n"
      "v_cvt_pk_bf16_f32 v15, v15, v19\n"
      "v_mfma_f32_16x16x32_bf16 v[68:71], a[188:191], a[212:215], v[68:71]\n"
      "ds_write_addtid_b32 v15 offset:54528\n"
      "v_sub_f32 v39, v39, v51\n"
      "v_cvt_pk_bf16_f32 v20, v20, v24\n"
      "v_mfma_f32_16x16x32_bf16 v[72:75], a[180:183], a[212:215], v[72:75]\n"
      "ds_write_addtid_b32 v20 offset:42752\n"
      "v_fmac_f32 v36, s46, v96\n"
      "v_mfma_f32_16x16x32_bf16 v[52:55], a[176:179], a[192:195], v[52:55]\n"
      "ds_read_b128 a[208:211], v2 offset:128\n"
      "v_fmac_f32 v37, s46, v97\n"
      "v_mfma_f32_16x16x32_bf16 v[56:59], a[168:171], a[192:195], v[56:59]\n"
      "ds_read_b64_tr_b16 a[220:221], v7 offset:18944\n"
      "ds_read_b64_tr_b16 a[222:223], v7 offset:18952\n"
      "v_mfma_f32_16x16x32_bf16 v[60:63], a[160:163], a[192:195], v[60:63]\n"
      "v_fmac_f32_e64 v38, s46, v98\n"
      "v_cvt_pk_bf16_f32 v21, v21, v25\n"
      "v_mfma_f32_16x16x32_bf16 v[64:67], a[172:175], a[196:199], v[64:67]\n"
      "ds_read_b128 a[212:215], v2 offset:4224\n"
      "ds_write_addtid_b32 v21 offset:46848\n"
      "v_fmac_f32 v39, s46, v99\n"
      "v_mfma_f32_16x16x32_bf16 v[68:71], a[164:167], a[196:199], v[68:71]\n"
      "s_nop 0\n"
      "v_fmac_f32_e64 v48, s45, v36\n"
      "v_cvt_pk_bf16_f32 v22, v22, v26\n"
      "v_mfma_f32_16x16x32_bf16 v[72:75], a[156:159], a[196:199], v[72:75]\n"
      "s_waitcnt lgkmcnt(4)\n"
      "ds_write_addtid_b32 v22 offset:50944\n"
      "v_fmac_f32 v49, s45, v37\n"
      "v_mfma_f32_16x16x32_bf16 v[52:55], a[152:155], a[200:203], v[52:55]\n"
      "s_nop 0\n"
      "ds_read_b128 a[192:195], v3 offset:33792\n"
      "v_fmac_f32_e64 v50, s45, v38\n"
      "v_mfma_f32_16x16x32_bf16 v[56:59], a[144:147], a[200:203], v[56:59]\n"
      "s_nop 0\n"
      "v_fmac_f32 v51, s45, v39\n"
      "v_cvt_pk_bf16_f32 v23, v23, v27\n"
      "v_mfma_f32_16x16x32_bf16 v[60:63], a[136:139], a[200:203], v[60:63]\n"
      "s_waitcnt lgkmcnt(2)\n"
      "ds_write_addtid_b32 v23 offset:55040\n"
      "v_fma_f32 v36, s47, v36, v120\n"
      "v_mfma_f32_4x4x4_16b_bf16 v[12:15], a[216:217], a[220:221], 0\n"
      "v_fma_f32 v37, s47, v37, v121\n"
      "v_mfma_f32_4x4x4_16b_bf16 v[16:19], a[216:217], a[222:223], 0\n"
      "v_fma_f32 v38, s47, v38, v122\n"
      "v_fma_f32 v39, s47, v39, v123\n"
      "v_mfma_f32_16x16x32_bf16 v[64:67], a[148:151], a[204:207], v[64:67]\n"
      "ds_read_b128 a[196:199], v3 offset:33856\n"
      "v_cvt_pk_bf16_f32 v48, v48, v49\n"
      "v_cvt_pk_bf16_f32 v12, v12, v16\n"
      "v_mfma_f32_16x16x32_bf16 v[68:71], a[140:143], a[204:207], v[68:71]\n"
      "ds_write_addtid_b32 v48 offset:17408\n"
      "ds_write_addtid_b32 v12 offset:43264\n"
      "v_mfma_f32_16x16x32_bf16 v[72:75], a[132:135], a[204:207], v[72:75]\n"
      "v_cvt_pk_bf16_f32 v13, v13, v17\n"
      "v_cvt_pk_bf16_f32 v14, v14, v18\n"
      "v_mfma_f32_16x16x32_bf16 v[52:55], a[128:131], a[208:211], v[52:55]\n"
      "ds_read_b128 a[200:203], v3 offset:33920\n"
      "ds_write_addtid_b32 v13 offset:47360\n"
      "v_mfma_f32_16x16x32_bf16 v[56:59], a[120:123], a[208:211], v[56:59]\n"
      "ds_read_b64_tr_b16 a[224:225], v7 offset:19456\n"
      "ds_write_addtid_b32 v14 offset:51456\n"
      "v_cvt_pk_bf16_f32 v50, v50, v51\n"
      "v_mfma_f32_16x16x32_bf16 v[60:63], a[112:115], a[208:211], v[60:63]\n"
      "ds_write_addtid_b32 v50 offset:17920\n"
      "ds_read_b64_tr_b16 a[226:227], v7 offset:19464\n"
      "v_mfma_f32_16x16x32_bf16 v[64:67], a[124:127], a[212:215], v[64:67]\n"
      "ds_read_b128 a[204:207], v4 offset:33792\n"
      "v_cvt_pk_bf16_f32 v36, v36, v37\n"
      "v_mfma_f32_16x16x32_bf16 v[68:71], a[116:119], a[212:215], v[68:71]\n"
      "ds_write_addtid_b32 v36 offset:19456\n"
      "v_cvt_pk_bf16_f32 v15, v15, v19\n"
      "v_mfma_f32_16x16x32_bf16 v[72:75], a[108:111], a[212:215], v[72:75]\n"
      "s_waitcnt lgkmcnt(6)\n"
      "ds_write_addtid_b32 v15 offset:55552\n"
      "v_sub_f32 v52, v64, v52\n"
      "v_sub_f32_e64 v53, v65, v53\n"
      "v_cvt_pk_bf16_f32 v38, v38, v39\n"
      "v_mfma_f32_16x16x32_bf16 v[76:79], a[0:3], a[192:195], 0\n"
      "s_nop 0\n"
      "ds_read_b128 a[208:211], v4 offset:33856\n"
      "ds_write_addtid_b32 v38 offset:19968\n"
      "v_mfma_f32_16x16x32_bf16 v[80:83], a[4:7], a[192:195], 0\n"
      "s_waitcnt lgkmcnt(5)\n"
      "ds_write_addtid_b32 v52 offset:0\n"
      "ds_write_addtid_b32 v53 offset:512\n"
      "v_mfma_f32_4x4x4_16b_bf16 v[20:23], a[216:217], a[224:225], 0\n"
      "v_sub_f32 v54, v66, v54\n"
      "v_mfma_f32_4x4x4_16b_bf16 v[24:27], a[216:217], a[226:227], 0\n"
      "ds_write_addtid_b32 v54 offset:1024\n"
      "v_sub_f32 v55, v67, v55\n"
      "v_mfma_f32_16x16x32_bf16 v[84:87], a[8:11], a[192:195], 0\n"
      "ds_write_addtid_b32 v55 offset:1536\n"
      "v_sub_f32 v56, v68, v56\n"
      "v_sub_f32 v57, v69, v57\n"
      "v_mfma_f32_16x16x32_bf16 v[76:79], a[12:15], a[196:199], v[76:79]\n"
      "ds_read_b128 a[212:215], v4 offset:33920\n"
      "ds_write_addtid_b32 v56 offset:2048\n"
      "v_mfma_f32_16x16x32_bf16 v[80:83], a[16:19], a[196:199], v[80:83]\n"
      "ds_write_addtid_b32 v57 offset:2560\n"
      "v_sub_f32 v58, v70, v58\n"
      "v_mfma_f32_16x16x32_bf16 v[84:87], a[20:23], a[196:199], v[84:87]\n"
      "s_nop 0\n"
      "ds_write_addtid_b32 v58 offset:3072\n"
      "v_cvt_pk_bf16_f32 v20, v20, v24\n"
      "v_mfma_f32_16x16x32_bf16 v[76:79], a[24:27], a[200:203], v[76:79]\n"
      "ds_read_b128 a[192:195], v5 offset:33792\n"
      "ds_write_addtid_b32 v20 offset:43776\n"
      "v_sub_f32 v59, v71, v59\n"
      "v_mfma_f32_16x16x32_bf16 v[80:83], a[28:31], a[200:203], v[80:83]\n"
      "ds_write_addtid_b32 v59 offset:3584\n"
      "v_sub_f32_e64 v60, v72, v60\n"
      "v_mfma_f32_16x16x32_bf16 v[84:87], a[32:35], a[200:203], v[84:87]\n"
      "s_waitcnt lgkmcnt(5)\n"
      "ds_write_addtid_b32 v60 offset:4096\n"
      "v_cvt_pk_bf16_f32 v21, v21, v25\n"
      "v_mfma_f32_16x16x32_bf16 v[76:79], a[36:39], a[204:207], v[76:79]\n"
      "ds_read_b128 a[196:199], v5 offset:33856\n"
      "ds_write_addtid_b32 v21 offset:47872\n"
      "v_mfma_f32_16x16x32_bf16 v[80:83], a[40:43], a[204:207], v[80:83]\n"
      "v_sub_f32 v61, v73, v61\n"
      "v_sub_f32 v62, v74, v62\n"
      "v_cvt_pk_bf16_f32 v22, v22, v26\n"
      "v_mfma_f32_16x16x32_bf16 v[84:87], a[44:47], a[204:207], v[84:87]\n"
      "ds_write_addtid_b32 v22 offset:51968\n"
      "ds_write_addtid_b32 v61 offset:4608\n"
      "v_sub_f32 v63, v75, v63\n"
      "v_mfma_f32_16x16x32_bf16 v[76:79], a[48:51], a[208:211], v[76:79]\n"
      "ds_read_b128 a[200:203], v5 offset:33920\n"
      "ds_write_addtid_b32 v62 offset:5120\n"
      "v_mfma_f32_16x16x32_bf16 v[80:83], a[52:55], a[208:211], v[80:83]\n"
      "s_nop 0\n"
      "ds_write_addtid_b32 v63 offset:5632\n"
      "v_cvt_pk_bf16_f32 v23, v23, v27\n"
      "v_mfma_f32_16x16x32_bf16 v[84:87], a[56:59], a[208:211], v[84:87]\n"
      "ds_write_addtid_b32 v23 offset:56064\n"
      "v_fma_f32 v40, 0.5, v52, -v64\n"
      "v_mfma_f32_16x16x32_bf16 v[76:79], a[60:63], a[212:215], v[76:79]\n"
      "v_fma_f32 v41, 0.5, v53, -v65\n"
      "v_fma_f32 v42, 0.5, v54, -v66\n"
      "v_mfma_f32_16x16x32_bf16 v[80:83], a[64:67], a[212:215], v[80:83]\n"
      "s_nop 0\n"
      "v_fma_f32 v43, 0.5, v55, -v67\n"
      "v_fma_f32 v44, 0.5, v56, -v68\n"
      "v_mfma_f32_16x16x32_bf16 v[84:87], a[68:71], a[212:215], v[84:87]\n"
      "s_waitcnt lgkmcnt(0)\n"
      "v_fma_f32 v45, 0.5, v57, -v69\n"
      "s_barrier\n"
      "s_add_u32 s55, s55, 2\n"
      "s_cmp_lt_u32 s55, 6\n"
      "s_cbranch_scc1 LSloop_%=\n"
      "LSlast_%=:\n"
      "v_mfma_f32_16x16x32_bf16 v[76:79], a[72:75], a[192:195], v[76:79]\n"
      "s_add_u32 s59, s62, 4918272\n"
      "ds_read_b128 a[204:207], v0 offset:16384\n"
      "ds_read2st64_b32 v[28:29], v6 offset0:0 offset1:2\n"
      "v_fma_f32 v46, 0.5, v58, -v70\n"
      "v_mfma_f32_16x16x32_bf16 v[80:83], a[76:79], a[192:195], v[80:83]\n"
      "ds_read_b128 a[208:211], v0 offset:20480\n"
      "ds_read2st64_b32 v[30:31], v6 offset0:4 offset1:6\n"
      "v_mfma_f32_16x16x32_bf16 v[84:87], a[80:83], a[192:195], v[84:87]\n"
      "ds_read_b64_tr_b16 a[220:221], v7 offset:33792\n"
      "v_fma_f32 v47, 0.5, v59, -v71\n"
      "v_mfma_f32_16x16x32_bf16 v[76:79], a[84:87], a[196:199], v[76:79]\n"
      "ds_read_b128 a[212:215], v0 offset:16448\n"
      "v_fma_f32 v48, 0.5, v60, -v72\n"
      "v_mfma_f32_16x16x32_bf16 v[80:83], a[88:91], a[196:199], v[80:83]\n"
      "ds_read_b64_tr_b16 a[222:223], v7 offset:33800\n"
      "ds_read2st64_b32 v[32:33], v6 offset0:8 offset1:10\n"
      "v_fma_f32 v49, 0.5, v61, -v73\n"
      "v_mfma_f32_16x16x32_bf16 v[84:87], a[92:95], a[196:199], v[84:87]\n"
      "ds_read2st64_b32 v[34:35], v6 offset0:12 offset1:14\n"
      "v_fma_f32 v50, 0.5, v62, -v74\n"
      "v_mfma_f32_16x16x32_bf16 v[76:79], a[96:99], a[200:203], v[76:79]\n"
      "ds_read_b128 a[192:195], v0 offset:20544\n"
      "v_fma_f32 v51, 0.5, v63, -v75\n"
      "v_mfma_f32_16x16x32_bf16 v[80:83], a[100:103], a[200:203], v[80:83]\n"
      "ds_read2st64_b32 v[36:37], v6 offset0:16 offset1:18\n"
      "ds_read2st64_b32 v[38:39], v6 offset0:20 offset1:22\n"
      "v_mfma_f32_16x16x32_bf16 v[84:87], a[104:107], a[200:203], v[84:87]\n"
      "s_waitcnt lgkmcnt(8)\n"
      "v_sub_f32 v28, v28, v40\n"
      "v_sub_f32 v29, v29, v41\n"
      "v_sub_f32_e64 v30, v30, v42\n"
      "v_mfma_f32_16x16x32_bf16 v[52:55], v[252:255], a[204:207], v[112:115]\n"
      "s_waitcnt lgkmcnt(5)\n"
      "ds_read_b128 a[196:199], v0 offset:16512\n"
      "ds_read2st64_b32 a[240:241], v8 offset0:4 offset1:6\n"
      "v_mfma_f32_4x4x4_16b_bf16 v[12:15], a[216:217], a[220:221], 0\n"
      "ds_read2st64_b32 a[242:243], v9 offset0:4 offset1:6\n"
      "v_mfma_f32_4x4x4_16b_bf16 v[16:19], a[216:217], a[222:223], 0\n"
      "v_sub_f32 v31, v31, v43\n"
      "v_fmac_f32 v28, s46, v100\n"
      "v_fmac_f32 v29, s46, v101\n"
      "v_mfma_f32_16x16x32_bf16 v[56:59], v[244:247], a[204:207], v[116:119]\n"
      "v_fmac_f32 v30, s46, v102\n"
      "v_fmac_f32 v31, s46, v103\n"
      "v_mfma_f32_16x16x32_bf16 v[60:63], v[236:239], a[204:207], v[120:123]\n"
      "v_fmac_f32 v40, s45, v28\n"
      "v_fmac_f32 v41, s45, v29\n"
      "v_mfma_f32_16x16x32_bf16 v[64:67], v[248:251], a[208:211], v[112:115]\n"
      "ds_read_b128 a[200:203], v0 offset:20608\n"
      "v_cvt_pk_bf16_f32 v12, v12, v16\n"
      "v_mfma_f32_16x16x32_bf16 v[68:71], v[240:243], a[208:211], v[116:119]\n"
      "s_nop 0\n"
      "ds_write_addtid_b32 v12 offset:24832\n"
      "ds_read_b64_tr_b16 a[224:225], v7 offset:34304\n"
      "v_fmac_f32 v42, s45, v30\n"
      "v_mfma_f32_16x16x32_bf16 v[72:75], v[232:235], a[208:211], v[120:123]\n"
      "ds_read_b64_tr_b16 a[226:227], v7 offset:34312\n"
      "v_fmac_f32 v43, s45, v31\n"
      "v_mfma_f32_16x16x32_bf16 v[52:55], v[228:231], a[212:215], v[52:55]\n"
      "ds_read_b128 a[204:207], v1 offset:16384\n"
      "v_cvt_pk_bf16_f32 v13, v13, v17\n"
      "v_mfma_f32_16x16x32_bf16 v[56:59], v[220:223], a[212:215], v[56:59]\n"
      "ds_write_addtid_b32 v13 offset:28928\n"
      "v_fma_f32 v28, s47, v28, v76\n"
      "v_mfma_f32_16x16x32_bf16 v[60:63], v[212:215], a[212:215], v[60:63]\n"
      "s_waitcnt lgkmcnt(4)\n"
      "v_fma_f32 v29, s47, v29, v77\n"
      "v_cvt_pk_bf16_f32 v14, v14, v18\n"
      "v_mfma_f32_16x16x32_bf16 v[64:67], v[224:227], a[192:195], v[64:67]\n"
      "s_nop 0\n"
      "ds_read_b128 a[208:211], v1 offset:20480\n"
      "ds_write_addtid_b32 v14 offset:33024\n"
      "v_fma_f32 v30, s47, v30, v78\n"
      "v_mfma_f32_16x16x32_bf16 v[68:71], v[216:219], a[192:195], v[68:71]\n"
      "s_nop 0\n"
      "v_fma_f32 v31, s47, v31, v79\n"
      "v_cvt_pk_bf16_f32 v15, v15, v19\n"
      "v_mfma_f32_16x16x32_bf16 v[72:75], v[208:211], a[192:195], v[72:75]\n"
      "s_waitcnt lgkmcnt(2)\n"
      "ds_write_addtid_b32 v15 offset:37120\n"
      "v_cvt_pk_bf16_f32 v40, v40, v41\n"
      "v_mfma_f32_4x4x4_16b_bf16 v[20:23], a[216:217], a[224:225], 0\n"
      "v_cvt_pk_bf16_f32 v42, v42, v43\n"
      "v_mfma_f32_4x4x4_16b_bf16 v[24:27], a[216:217], a[226:227], 0\n"
      "ds_write_addtid_b32 v40 offset:12288\n"
      "ds_write_addtid_b32 v42 offset:12800\n"
      "v_cvt_pk_bf16_f32 v28, v28, v29\n"
      "v_mfma_f32_16x16x32_bf16 v[52:55], v[204:207], a[196:199], v[52:55]\n"
      "ds_read_b128 a[212:215], v1 offset:16448\n"
      "ds_write_addtid_b32 v28 offset:14336\n"
      "v_cvt_pk_bf16_f32 v20, v20, v24\n"
      "v_mfma_f32_16x16x32_bf16 v[56:59], v[196:199], a[196:199], v[56:59]\n"
      "ds_write_addtid_b32 v20 offset:25344\n"
      "v_cvt_pk_bf16_f32 v30, v30, v31\n"
      "v_mfma_f32_16x16x32_bf16 v[60:63], v[188:191], a[196:199], v[60:63]\n"
      "buffer_store_dwordx4 a[240:243], v11, s[8:11], s59 offen offset:256 sc1\n"
      "v_mfma_f32_16x16x32_bf16 v[64:67], v[200:203], a[200:203], v[64:67]\n"
      "ds_read_b128 a[192:195], v1 offset:20544\n"
      "ds_write_addtid_b32 v30 offset:14848\n"
      "v_mfma_f32_16x16x32_bf16 v[68:71], v[192:195], a[200:203], v[68:71]\n"
      "ds_read_b64_tr_b16 a[228:229], v7 offset:34816\n"
      "ds_read_b64_tr_b16 a[230:231], v7 offset:34824\n"
      "v_sub_f32 v32, v32, v44\n"
      "v_mfma_f32_16x16x32_bf16 v[72:75], v[184:187], a[200:203], v[72:75]\n"
      "v_sub_f32 v33, v33, v45\n"
      "v_cvt_pk_bf16_f32 v21, v21, v25\n"
      "v_mfma_f32_16x16x32_bf16 v[52:55], v[180:183], a[204:207], v[52:55]\n"
      "ds_read_b128 a[196:199], v1 offset:16512\n"
      "ds_write_addtid_b32 v21 offset:29440\n"
      "v_mfma_f32_16x16x32_bf16 v[56:59], v[172:175], a[204:207], v[56:59]\n"
      "v_sub_f32 v34, v34, v46\n"
      "v_sub_f32 v35, v35, v47\n"
      "v_mfma_f32_16x16x32_bf16 v[60:63], v[164:167], a[204:207], v[60:63]\n"
      "s_waitcnt lgkmcnt(4)\n"
      "v_fmac_f32 v32, s46, v104\n"
      "v_cvt_pk_bf16_f32 v22, v22, v26\n"
      "v_mfma_f32_16x16x32_bf16 v[64:67], v[176:179], a[208:211], v[64:67]\n"
      "ds_read_b128 a[200:203], v1 offset:20608\n"
      "ds_write_addtid_b32 v22 offset:33536\n"
      "v_fmac_f32 v33, s46, v105\n"
      "v_mfma_f32_16x16x32_bf16 v[68:71], v[168:171], a[208:211], v[68:71]\n"
      "s_waitcnt lgkmcnt(4)\n"
      "v_fmac_f32_e64 v34, s46, v106\n"
      "v_cvt_pk_bf16_f32 v23, v23, v27\n"
      "v_mfma_f32_4x4x4_16b_bf16 v[12:15], a[216:217], a[228:229], 0\n"
      "v_fmac_f32 v35, s46, v107\n"
      "v_mfma_f32_4x4x4_16b_bf16 v[16:19], a[216:217], a[230:231], 0\n"
      "ds_write_addtid_b32 v23 offset:37632\n"
      "v_fmac_f32 v44, s45, v32\n"
      "v_mfma_f32_16x16x32_bf16 v[72:75], v[160:163], a[208:211], v[72:75]\n"
      "v_fmac_f32 v45, s45, v33\n"
      "v_fmac_f32 v46, s45, v34\n"
      "v_cvt_pk_bf16_f32 v12, v12, v16\n"
      "v_mfma_f32_16x16x32_bf16 v[52:55], v[156:159], a[212:215], v[52:55]\n"
      "ds_read_b128 a[204:207], v2 offset:16384\n"
      "ds_write_addtid_b32 v12 offset:25856\n"
      "v_mfma_f32_16x16x32_bf16 v[56:59], v[148:151], a[212:215], v[56:59]\n"
      "v_fmac_f32 v47, s45, v35\n"
      "v_cvt_pk_bf16_f32 v13, v13, v17\n"
      "v_mfma_f32_16x16x32_bf16 v[60:63], v[140:143], a[212:215], v[60:63]\n"
      "s_nop 0\n"
      "ds_read_b64_tr_b16 a[232:233], v7 offset:35328\n"
      "ds_write_addtid_b32 v13 offset:29952\n"
      "v_fma_f32 v32, s47, v32, v80\n"
      "v_mfma_f32_16x16x32_bf16 v[64:67], v[152:155], a[192:195], v[64:67]\n"
      "ds_read_b128 a[208:211], v2 offset:20480\n"
      "ds_read_b64_tr_b16 a[234:235], v7 offset:35336\n"
      "v_mfma_f32_16x16x32_bf16 v[68:71], v[144:147], a[192:195], v[68:71]\n"
      "v_fma_f32 v33, s47, v33, v81\n"
      "v_fma_f32 v34, s47, v34, v82\n"
      "v_mfma_f32_16x16x32_bf16 v[72:75], v[136:139], a[192:195], v[72:75]\n"
      "s_waitcnt lgkmcnt(4)\n"
      "v_fma_f32 v35, s47, v35, v83\n"
      "v_cvt_pk_bf16_f32 v14, v14, v18\n"
      "v_mfma_f32_16x16x32_bf16 v[52:55], v[132:135], a[196:199], v[52:55]\n"
      "ds_read_b128 a[212:215], v2 offset:16448\n"
      "ds_write_addtid_b32 v14 offset:34048\n"
      "v_mfma_f32_16x16x32_bf16 v[56:59], v[124:127], a[196:199], v[56:59]\n"
      "s_nop 0\n"
      "v_cvt_pk_bf16_f32 v44, v44, v45\n"
      "v_cvt_pk_bf16_f32 v46, v46, v47\n"
      "v_mfma_f32_16x16x32_bf16 v[60:63], a[184:187], a[196:199], v[60:63]\n"
      "s_nop 0\n"
      "ds_write_addtid_b32 v44 offset:20484\n"
      "ds_write_addtid_b32 v46 offset:20996\n"
      "v_cvt_pk_bf16_f32 v32, v32, v33\n"
      "v_mfma_f32_16x16x32_bf16 v[64:67], v[128:131], a[200:203], v[64:67]\n"
      "s_waitcnt lgkmcnt(4)\n"
      "ds_read_b128 a[192:195], v2 offset:20544\n"
      "ds_write_addtid_b32 v32 offset:22532\n"
      "v_mfma_f32_4x4x4_16b_bf16 v[20:23], a[216:217], a[232:233], 0\n"
      "v_cvt_pk_bf16_f32 v34, v34, v35\n"
      "v_cvt_pk_bf16_f32 v15, v15, v19\n"
      "v_mfma_f32_4x4x4_16b_bf16 v[24:27], a[216:217], a[234:235], 0\n"
      "ds_write_addtid_b32 v34 offset:23044\n"
      "ds_write_addtid_b32 v15 offset:38144\n"
      "v_sub_f32_e64 v36, v36, v48\n"
      "v_mfma_f32_16x16x32_bf16 v[68:71], a[188:191], a[200:203], v[68:71]\n"
      "v_sub_f32 v37, v37, v49\n"
      "v_cvt_pk_bf16_f32 v20, v20, v24\n"
      "v_mfma_f32_16x16x32_bf16 v[72:75], a[180:183], a[200:203], v[72:75]\n"
      "ds_write_addtid_b32 v20 offset:26368\n"
      "v_sub_f32 v38, v38, v50\n"
      "v_mfma_f32_16x16x32_bf16 v[52:55], a[176:179], a[204:207], v[52:55]\n"
      "ds_read_b128 a[196:199], v2 offset:16512\n"
      "v_sub_f32_e64 v39, v39, v51\n"
      "v_mfma_f32_16x16x32_bf16 v[56:59], a[168:171], a[204:207], v[56:59]\n"
      "ds_read_b64_tr_b16 a[220:221], v7 offset:35840\n"
      "ds_read_b64_tr_b16 a[222:223], v7 offset:35848\n"
      "v_fmac_f32 v36, s46, v108\n"
      "v_mfma_f32_16x16x32_bf16 v[60:63], a[160:163], a[204:207], v[60:63]\n"
      "v_fmac_f32 v37, s46, v109\n"
      "v_cvt_pk_bf16_f32 v21, v21, v25\n"
      "v_mfma_f32_16x16x32_bf16 v[64:67], a[172:175], a[208:211], v[64:67]\n"
      "ds_read_b128 a[200:203], v2 offset:20608\n"
      "ds_write_addtid_b32 v21 offset:30464\n"
      "v_mfma_f32_16x16x32_bf16 v[68:71], a[164:167], a[208:211], v[68:71]\n"
      "v_fmac_f32 v38, s46, v110\n"
      "v_fmac_f32 v39, s46, v111\n"
      "v_mfma_f32_16x16x32_bf16 v[72:75], a[156:159], a[208:211], v[72:75]\n"
      "s_waitcnt lgkmcnt(3)\n"
      "v_fmac_f32 v48, s45, v36\n"
      "v_cvt_pk_bf16_f32 v22, v22, v26\n"
      "v_mfma_f32_16x16x32_bf16 v[52:55], a[152:155], a[212:215], v[52:55]\n"
      "ds_read_b128 a[204:207], v3 offset:50688\n"
      "ds_write_addtid_b32 v22 offset:34560\n"
      "v_fmac_f32 v49, s45, v37\n"
      "v_mfma_f32_16x16x32_bf16 v[56:59], a[144:147], a[212:215], v[56:59]\n"
      "v_fmac_f32 v50, s45, v38\n"
      "v_cvt_pk_bf16_f32 v23, v23, v27\n"
      "v_mfma_f32_16x16x32_bf16 v[60:63], a[136:139], a[212:215], v[60:63]\n"
      "s_waitcnt lgkmcnt(2)\n"
      "ds_write_addtid_b32 v23 offset:38656\n"
      "v_fmac_f32 v51, s45, v39\n"
      "v_mfma_f32_4x4x4_16b_bf16 v[12:15], a[216:217], a[220:221], 0\n"
      "v_fma_f32 v36, s47, v36, v84\n"
      "v_mfma_f32_4x4x4_16b_bf16 v[16:19], a[216:217], a[222:223], 0\n"
      "v_fma_f32 v37, s47, v37, v85\n"
      "v_fma_f32 v38, s47, v38, v86\n"
      "v_mfma_f32_16x16x32_bf16 v[64:67], a[148:151], a[192:195], v[64:67]\n"
      "ds_read_b128 a[208:211], v3 offset:50752\n"
      "v_fma_f32 v39, s47, v39, v87\n"
      "v_cvt_pk_bf16_f32 v12, v12, v16\n"
      "v_mfma_f32_16x16x32_bf16 v[68:71], a[140:143], a[192:195], v[68:71]\n"
      "ds_write_addtid_b32 v12 offset:26880\n"
      "v_cvt_pk_bf16_f32 v13, v13, v17\n"
      "v_mfma_f32_16x16x32_bf16 v[72:75], a[132:135], a[192:195], v[72:75]\n"
      "ds_write_addtid_b32 v13 offset:30976\n"
      "v_cvt_pk_bf16_f32 v48, v48, v49\n"
      "v_mfma_f32_16x16x32_bf16 v[52:55], a[128:131], a[196:199], v[52:55]\n"
      "ds_read_b128 a[212:215], v3 offset:50816\n"
      "ds_write_addtid_b32 v48 offset:16384\n"
      "v_mfma_f32_16x16x32_bf16 v[56:59], a[120:123], a[196:199], v[56:59]\n"
      "ds_read_b64_tr_b16 a[224:225], v7 offset:36352\n"
      "ds_read_b64_tr_b16 a[226:227], v7 offset:36360\n"
      "v_cvt_pk_bf16_f32 v14, v14, v18\n"
      "v_mfma_f32_16x16x32_bf16 v[60:63], a[112:115], a[196:199], v[60:63]\n"
      "ds_write_addtid_b32 v14 offset:35072\n"
      "v_cvt_pk_bf16_f32 v50, v50, v51\n"
      "v_mfma_f32_16x16x32_bf16 v[64:67], a[124:127], a[200:203], v[64:67]\n"
      "ds_read_b128 a[192:195], v4 offset:50688\n"
      "ds_write_addtid_b32 v50 offset:16896\n"
      "v_mfma_f32_16x16x32_bf16 v[68:71], a[116:119], a[200:203], v[68:71]\n"
      "s_nop 0\n"
      "v_cvt_pk_bf16_f32 v36, v36, v37\n"
      "v_cvt_pk_bf16_f32 v15, v15, v19\n"
      "v_mfma_f32_16x16x32_bf16 v[72:75], a[108:111], a[200:203], v[72:75]\n"
      "s_waitcnt lgkmcnt(5)\n"
      "ds_write_addtid_b32 v36 offset:18432\n"
      "ds_write_addtid_b32 v15 offset:39168\n"
      "v_sub_f32 v52, v64, v52\n"
      "v_sub_f32 v53, v65, v53\n"
      "v_mfma_f32_16x16x32_bf16 v[88:91], a[0:3], a[204:207], 0\n"
      "s_nop 0\n"
      "ds_read_b128 a[196:199], v4 offset:50752\n"
      "ds_write_addtid_b32 v52 offset:6144\n"
      "v_mfma_f32_16x16x32_bf16 v[92:95], a[4:7], a[204:207], 0\n"
      "s_waitcnt lgkmcnt(5)\n"
      "ds_write_addtid_b32 v53 offset:6656\n"
      "v_sub_f32 v54, v66, v54\n"
      "v_mfma_f32_4x4x4_16b_bf16 v[20:23], a[216:217], a[224:225], 0\n"
      "v_sub_f32 v55, v67, v55\n"
      "v_cvt_pk_bf16_f32 v38, v38, v39\n"
      "v_mfma_f32_4x4x4_16b_bf16 v[24:27], a[216:217], a[226:227], 0\n"
      "ds_write_addtid_b32 v38 offset:18944\n"
      "ds_write_addtid_b32 v54 offset:7168\n"
      "v_mfma_f32_16x16x32_bf16 v[96:99], a[8:11], a[204:207], 0\n"
      "ds_write_addtid_b32 v55 offset:7680\n"
      "v_sub_f32 v56, v68, v56\n"
      "v_mfma_f32_16x16x32_bf16 v[88:91], a[12:15], a[208:211], v[88:91]\n"
      "ds_read_b128 a[200:203], v4 offset:50816\n"
      "ds_write_addtid_b32 v56 offset:8192\n"
      "v_sub_f32 v57, v69, v57\n"
      "v_mfma_f32_16x16x32_bf16 v[92:95], a[16:19], a[208:211], v[92:95]\n"
      "ds_write_addtid_b32 v57 offset:8704\n"
      "v_sub_f32_e64 v58, v70, v58\n"
      "v_mfma_f32_16x16x32_bf16 v[96:99], a[20:23], a[208:211], v[96:99]\n"
      "ds_write_addtid_b32 v58 offset:9216\n"
      "v_cvt_pk_bf16_f32 v20, v20, v24\n"
      "v_mfma_f32_16x16x32_bf16 v[88:91], a[24:27], a[212:215], v[88:91]\n"
      "ds_read_b128 a[204:207], v5 offset:50688\n"
      "ds_write_addtid_b32 v20 offset:27392\n"
      "v_mfma_f32_16x16x32_bf16 v[92:95], a[28:31], a[212:215], v[92:95]\n"
      "v_sub_f32 v59, v71, v59\n"
      "v_sub_f32 v60, v72, v60\n"
      "v_cvt_pk_bf16_f32 v21, v21, v25\n"
      "v_mfma_f32_16x16x32_bf16 v[96:99], a[32:35], a[212:215], v[96:99]\n"
      "ds_write_addtid_b32 v21 offset:31488\n"
      "ds_write_addtid_b32 v59 offset:9728\n"
      "v_mfma_f32_16x16x32_bf16 v[88:91], a[36:39], a[192:195], v[88:91]\n"
      "ds_read_b128 a[208:211], v5 offset:50752\n"
      "ds_write_addtid_b32 v60 offset:10240\n"
      "v_mfma_f32_16x16x32_bf16 v[92:95], a[40:43], a[192:195], v[92:95]\n"
      "v_sub_f32 v61, v73, v61\n"
      "v_sub_f32 v62, v74, v62\n"
      "v_cvt_pk_bf16_f32 v22, v22, v26\n"
      "v_mfma_f32_16x16x32_bf16 v[96:99], a[44:47], a[192:195], v[96:99]\n"
      "s_waitcnt lgkmcnt(4)\n"
      "ds_write_addtid_b32 v22 offset:35584\n"
      "ds_write_addtid_b32 v61 offset:10752\n"
      "v_sub_f32 v63, v75, v63\n"
      "v_mfma_f32_16x16x32_bf16 v[88:91], a[48:51], a[196:199], v[88:91]\n"
      "ds_read_b128 a[212:215], v5 offset:50816\n"
      "ds_write_addtid_b32 v62 offset:11264\n"
      "v_mfma_f32_16x16x32_bf16 v[92:95], a[52:55], a[196:199], v[92:95]\n"
      "ds_write_addtid_b32 v63 offset:11776\n"
      "v_cvt_pk_bf16_f32 v23, v23, v27\n"
      "v_mfma_f32_16x16x32_bf16 v[96:99], a[56:59], a[196:199], v[96:99]\n"
      "ds_write_addtid_b32 v23 offset:39680\n"
      "v_fma_f32 v40, 0.5, v52, -v64\n"
      "v_mfma_f32_16x16x32_bf16 v[88:91], a[60:63], a[200:203], v[88:91]\n"
      "v_fma_f32 v41, 0.5, v53, -v65\n"
      "v_fma_f32 v42, 0.5, v54, -v66\n"
      "v_mfma_f32_16x16x32_bf16 v[92:95], a[64:67], a[200:203], v[92:95]\n"
      "v_fma_f32 v43, 0.5, v55, -v67\n"
      "v_fma_f32 v44, 0.5, v56, -v68\n"
      "v_mfma_f32_16x16x32_bf16 v[96:99], a[68:71], a[200:203], v[96:99]\n"
      "s_waitcnt lgkmcnt(0)\n"
      "v_fma_f32 v45, 0.5, v57, -v69\n"
      "s_barrier\n"
      "v_mfma_f32_16x16x32_bf16 v[88:91], a[72:75], a[204:207], v[88:91]\n"
      "s_add_u32 s59, s62, 5701632\n"
      "ds_read_b128 a[192:195], v0 offset:0\n"
      "ds_read2st64_b32 v[28:29], v6 offset0:24 offset1:26\n"
      "v_fma_f32 v46, 0.5, v58, -v70\n"
      "v_mfma_f32_16x16x32_bf16 v[92:95], a[76:79], a[204:207], v[92:95]\n"
      "ds_read_b128 a[196:199], v0 offset:4096\n"
      "ds_read2st64_b32 v[30:31], v6 offset0:28 offset1:30\n"
      "v_mfma_f32_16x16x32_bf16 v[96:99], a[80:83], a[204:207], v[96:99]\n"
      "ds_read_b64_tr_b16 a[220:221], v7 offset:50688\n"
      "v_fma_f32 v47, 0.5, v59, -v71\n"
      "v_mfma_f32_16x16x32_bf16 v[88:91], a[84:87], a[208:211], v[88:91]\n"
      "ds_read_b128 a[200:203], v0 offset:64\n"
      "v_fma_f32 v48, 0.5, v60, -v72\n"
      "v_mfma_f32_16x16x32_bf16 v[92:95], a[88:91], a[208:211], v[92:95]\n"
      "ds_read_b64_tr_b16 a[222:223], v7 offset:50696\n"
      "ds_read2st64_b32 v[32:33], v6 offset0:32 offset1:34\n"
      "v_fma_f32 v49, 0.5, v61, -v73\n"
      "v_mfma_f32_16x16x32_bf16 v[96:99], a[92:95], a[208:211], v[96:99]\n"
      "ds_read2st64_b32 v[34:35], v6 offset0:36 offset1:38\n"
      "v_fma_f32 v50, 0.5, v62, -v74\n"
      "v_mfma_f32_16x16x32_bf16 v[88:91], a[96:99], a[212:215], v[88:91]\n"
      "ds_read_b128 a[204:207], v0 offset:4160\n"
      "v_fma_f32 v51, 0.5, v63, -v75\n"
      "v_mfma_f32_16x16x32_bf16 v[92:95], a[100:103], a[212:215], v[92:95]\n"
      "ds_read2st64_b32 v[36:37], v6 offset0:40 offset1:42\n"
      "ds_read2st64_b32 v[38:39], v6 offset0:44 offset1:46\n"
      "v_add_u32 v3, 67840, v3\n"
      "v_mfma_f32_16x16x32_bf16 v[96:99], a[104:107], a[212:215], v[96:99]\n"
      "s_waitcnt lgkmcnt(7)\n"
      "v_sub_f32 v28, v28, v40\n"
      "v_sub_f32 v29, v29, v41\n"
      "v_mfma_f32_16x16x32_bf16 v[52:55], v[252:255], a[192:195], v[76:79]\n"
      "s_waitcnt lgkmcnt(5)\n"
      "ds_read_b128 a[208:211], v0 offset:128\n"
      "ds_read2st64_b32 a[240:241], v8 offset0:0 offset1:2\n"
      "v_mfma_f32_4x4x4_16b_bf16 v[12:15], a[216:217], a[220:221], 0\n"
      "ds_read2st64_b32 a[242:243], v9 offset0:0 offset1:2\n"
      "v_sub_f32 v30, v30, v42\n"
      "v_mfma_f32_4x4x4_16b_bf16 v[16:19], a[216:217], a[222:223], 0\n"
      "v_sub_f32 v31, v31, v43\n"
      "v_fmac_f32 v28, s46, v112\n"
      "v_mfma_f32_16x16x32_bf16 v[56:59], v[244:247], a[192:195], v[80:83]\n"
      "v_fmac_f32 v29, s46, v113\n"
      "v_fmac_f32 v30, s46, v114\n"
      "v_mfma_f32_16x16x32_bf16 v[60:63], v[236:239], a[192:195], v[84:87]\n"
      "v_fmac_f32 v31, s46, v115\n"
      "v_add_u32 v4, 67840, v4\n"
      "v_cvt_pk_bf16_f32 v12, v12, v16\n"
      "v_mfma_f32_16x16x32_bf16 v[64:67], v[248:251], a[196:199], v[76:79]\n"
      "ds_read_b128 a[212:215], v0 offset:4224\n"
      "ds_write_addtid_b32 v12 offset:41216\n"
      "v_mfma_f32_16x16x32_bf16 v[68:71], v[240:243], a[196:199], v[80:83]\n"
      "ds_read_b64_tr_b16 a[224:225], v7 offset:51200\n"
      "ds_read_b64_tr_b16 a[226:227], v7 offset:51208\n"
      "v_mfma_f32_16x16x32_bf16 v[72:75], v[232:235], a[196:199], v[84:87]\n"
      "v_fmac_f32 v40, s45, v28\n"
      "v_fmac_f32 v41, s45, v29\n"
      "v_cvt_pk_bf16_f32 v13, v13, v17\n"
      "v_mfma_f32_16x16x32_bf16 v[52:55], v[228:231], a[200:203], v[52:55]\n"
      "ds_read_b128 a[192:195], v1 offset:0\n"
      "ds_write_addtid_b32 v13 offset:45312\n"
      "v_mfma_f32_16x16x32_bf16 v[56:59], v[220:223], a[200:203], v[56:59]\n"
      "v_fmac_f32 v42, s45, v30\n"
      "v_add_u32 v5, 67840, v5\n"
      "v_mfma_f32_16x16x32_bf16 v[60:63], v[212:215], a[200:203], v[60:63]\n"
      "s_waitcnt lgkmcnt(3)\n"
      "v_fmac_f32 v43, s45, v31\n"
      "v_fma_f32 v28, s47, v28, v88\n"
      "v_mfma_f32_16x16x32_bf16 v[64:67], v[224:227], a[204:207], v[64:67]\n"
      "s_nop 0\n"
      "ds_read_b128 a[196:199], v1 offset:4096\n"
      "v_cvt_pk_bf16_f32 v14, v14, v18\n"
      "v_mfma_f32_16x16x32_bf16 v[68:71], v[216:219], a[204:207], v[68:71]\n"
      "s_nop 0\n"
      "ds_write_addtid_b32 v14 offset:49408\n"
      "v_fma_f32 v29, s47, v29, v89\n"
      "v_mfma_f32_16x16x32_bf16 v[72:75], v[208:211], a[204:207], v[72:75]\n"
      "s_waitcnt lgkmcnt(2)\n"
      "v_fma_f32 v30, s47, v30, v90\n"
      "v_cvt_pk_bf16_f32 v15, v15, v19\n"
      "v_mfma_f32_4x4x4_16b_bf16 v[20:23], a[216:217], a[224:225], 0\n"
      "v_fma_f32 v31, s47, v31, v91\n"
      "v_mfma_f32_4x4x4_16b_bf16 v[24:27], a[216:217], a[226:227], 0\n"
      "ds_write_addtid_b32 v15 offset:53504\n"
      "v_cvt_pk_bf16_f32 v40, v40, v41\n"
      "v_mfma_f32_16x16x32_bf16 v[52:55], v[204:207], a[208:211], v[52:55]\n"
      "ds_read_b128 a[200:203], v1 offset:64\n"
      "ds_write_addtid_b32 v40 offset:13312\n"
      "v_cvt_pk_bf16_f32 v20, v20, v24\n"
      "v_mfma_f32_16x16x32_bf16 v[56:59], v[196:199], a[208:211], v[56:59]\n"
      "ds_write_addtid_b32 v20 offset:41728\n"
      "v_cvt_pk_bf16_f32 v42, v42, v43\n"
      "v_mfma_f32_16x16x32_bf16 v[60:63], v[188:191], a[208:211], v[60:63]\n"
      "buffer_store_dwordx4 a[240:243], v11, s[8:11], s59 offen offset:256 sc1\n"
      "ds_write_addtid_b32 v42 offset:13824\n"
      "v_mfma_f32_16x16x32_bf16 v[64:67], v[200:203], a[212:215], v[64:67]\n"
      "ds_read_b128 a[204:207], v1 offset:4160\n"
      "ds_read_b64_tr_b16 a[228:229], v7 offset:51712\n"
      "v_cvt_pk_bf16_f32 v28, v28, v29\n"
      "v_mfma_f32_16x16x32_bf16 v[68:71], v[192:195], a[212:215], v[68:71]\n"
      "ds_write_addtid_b32 v28 offset:15360\n"
      "ds_read_b64_tr_b16 a[230:231], v7 offset:51720\n"
      "v_cvt_pk_bf16_f32 v30, v30, v31\n"
      "v_mfma_f32_16x16x32_bf16 v[72:75], v[184:187], a[212:215], v[72:75]\n"
      "ds_write_addtid_b32 v30 offset:15872\n"
      "v_sub_f32 v32, v32, v44\n"
      "v_mfma_f32_16x16x32_bf16 v[52:55], v[180:183], a[192:195], v[52:55]\n"
      "ds_read_b128 a[208:211], v1 offset:128\n"
      "v_cvt_pk_bf16_f32 v21, v21, v25\n"
      "v_mfma_f32_16x16x32_bf16 v[56:59], v[172:175], a[192:195], v[56:59]\n"
      "ds_write_addtid_b32 v21 offset:45824\n"
      "v_sub_f32 v33, v33, v45\n"
      "v_sub_f32_e64 v34, v34, v46\n"
      "v_mfma_f32_16x16x32_bf16 v[60:63], v[164:167], a[192:195], v[60:63]\n"
      "s_waitcnt lgkmcnt(4)\n"
      "v_sub_f32 v35, v35, v47\n"
      "v_cvt_pk_bf16_f32 v22, v22, v26\n"
      "v_mfma_f32_16x16x32_bf16 v[64:67], v[176:179], a[196:199], v[64:67]\n"
      "ds_read_b128 a[212:215], v1 offset:4224\n"
      "ds_write_addtid_b32 v22 offset:49920\n"
      "v_mfma_f32_16x16x32_bf16 v[68:71], v[168:171], a[196:199], v[68:71]\n"
      "s_waitcnt lgkmcnt(4)\n"
      "v_fmac_f32 v32, s46, v116\n"
      "v_fmac_f32 v33, s46, v117\n"
      "v_mfma_f32_4x4x4_16b_bf16 v[12:15], a[216:217], a[228:229], 0\n"
      "v_cvt_pk_bf16_f32 v23, v23, v27\n"
      "v_mfma_f32_4x4x4_16b_bf16 v[16:19], a[216:217], a[230:231], 0\n"
      "ds_write_addtid_b32 v23 offset:54016\n"
      "v_fmac_f32 v34, s46, v118\n"
      "v_fmac_f32 v35, s46, v119\n"
      "v_mfma_f32_16x16x32_bf16 v[72:75], v[160:163], a[196:199], v[72:75]\n"
      "v_fmac_f32 v44, s45, v32\n"
      "v_cvt_pk_bf16_f32 v12, v12, v16\n"
      "v_mfma_f32_16x16x32_bf16 v[52:55], v[156:159], a[200:203], v[52:55]\n"
      "ds_read_b128 a[192:195], v2 offset:0\n"
      "ds_write_addtid_b32 v12 offset:42240\n"
      "v_mfma_f32_16x16x32_bf16 v[56:59], v[148:151], a[200:203], v[56:59]\n"
      "v_fmac_f32 v45, s45, v33\n"
      "v_fmac_f32 v46, s45, v34\n"
      "v_mfma_f32_16x16x32_bf16 v[60:63], v[140:143], a[200:203], v[60:63]\n"
      "ds_read_b64_tr_b16 a[232:233], v7 offset:52224\n"
      "ds_read_b64_tr_b16 a[234:235], v7 offset:52232\n"
      "v_fmac_f32 v47, s45, v35\n"
      "v_cvt_pk_bf16_f32 v13, v13, v17\n"
      "v_mfma_f32_16x16x32_bf16 v[64:67], v[152:155], a[204:207], v[64:67]\n"
      "ds_read_b128 a[196:199], v2 offset:4096\n"
      "ds_write_addtid_b32 v13 offset:46336\n"
      "v_mfma_f32_16x16x32_bf16 v[68:71], v[144:147], a[204:207], v[68:71]\n"
      "s_nop 0\n"
      "v_fma_f32 v32, s47, v32, v92\n"
      "v_fma_f32 v33, s47, v33, v93\n"
      "v_mfma_f32_16x16x32_bf16 v[72:75], v[136:139], a[204:207], v[72:75]\n"
      "s_waitcnt lgkmcnt(4)\n"
      "v_cvt_pk_bf16_f32 v14, v14, v18\n"
      "v_fma_f32 v34, s47, v34, v94\n"
      "v_mfma_f32_16x16x32_bf16 v[52:55], v[132:135], a[208:211], v[52:55]\n"
      "ds_read_b128 a[200:203], v2 offset:64\n"
      "ds_write_addtid_b32 v14 offset:50432\n"
      "v_mfma_f32_16x16x32_bf16 v[56:59], v[124:127], a[208:211], v[56:59]\n"
      "s_nop 0\n"
      "v_fma_f32 v35, s47, v35, v95\n"
      "v_cvt_pk_bf16_f32 v44, v44, v45\n"
      "v_mfma_f32_16x16x32_bf16 v[60:63], a[184:187], a[208:211], v[60:63]\n"
      "s_nop 0\n"
      "ds_write_addtid_b32 v44 offset:21508\n"
      "v_cvt_pk_bf16_f32 v46, v46, v47\n"
      "v_mfma_f32_16x16x32_bf16 v[64:67], v[128:131], a[212:215], v[64:67]\n"
      "s_waitcnt lgkmcnt(3)\n"
      "ds_read_b128 a[204:207], v2 offset:4160\n"
      "ds_write_addtid_b32 v46 offset:22020\n"
      "v_mfma_f32_4x4x4_16b_bf16 v[20:23], a[216:217], a[232:233], 0\n"
      "v_cvt_pk_bf16_f32 v32, v32, v33\n"
      "v_cvt_pk_bf16_f32 v15, v15, v19\n"
      "v_mfma_f32_4x4x4_16b_bf16 v[24:27], a[216:217], a[234:235], 0\n"
      "ds_write_addtid_b32 v32 offset:23556\n"
      "ds_write_addtid_b32 v15 offset:54528\n"
      "v_cvt_pk_bf16_f32 v34, v34, v35\n"
      "v_mfma_f32_16x16x32_bf16 v[68:71], a[188:191], a[212:215], v[68:71]\n"
      "ds_write_addtid_b32 v34 offset:24068\n"
      "v_sub_f32 v36, v36, v48\n"
      "v_cvt_pk_bf16_f32 v20, v20, v24\n"
      "v_mfma_f32_16x16x32_bf16 v[72:75], a[180:183], a[212:215], v[72:75]\n"
      "ds_write_addtid_b32 v20 offset:42752\n"
      "v_sub_f32 v37, v37, v49\n"
      "v_mfma_f32_16x16x32_bf16 v[52:55], a[176:179], a[192:195], v[52:55]\n"
      "ds_read_b128 a[208:211], v2 offset:128\n"
      "v_sub_f32 v38, v38, v50\n"
      "v_mfma_f32_16x16x32_bf16 v[56:59], a[168:171], a[192:195], v[56:59]\n"
      "ds_read_b64_tr_b16 a[220:221], v7 offset:52736\n"
      "ds_read_b64_tr_b16 a[222:223], v7 offset:52744\n"
      "v_sub_f32 v39, v39, v51\n"
      "v_mfma_f32_16x16x32_bf16 v[60:63], a[160:163], a[192:195], v[60:63]\n"
      "v_fmac_f32 v36, s46, v120\n"
      "v_cvt_pk_bf16_f32 v21, v21, v25\n"
      "v_mfma_f32_16x16x32_bf16 v[64:67], a[172:175], a[196:199], v[64:67]\n"
      "s_nop 0\n"
      "ds_read_b128 a[212:215], v2 offset:4224\n"
      "ds_write_addtid_b32 v21 offset:46848\n"
      "v_mfma_f32_16x16x32_bf16 v[68:71], a[164:167], a[196:199], v[68:71]\n"
      "v_fmac_f32 v37, s46, v121\n"
      "v_fmac_f32 v38, s46, v122\n"
      "v_mfma_f32_16x16x32_bf16 v[72:75], a[156:159], a[196:199], v[72:75]\n"
      "s_waitcnt lgkmcnt(4)\n"
      "v_fmac_f32 v39, s46, v123\n"
      "v_cvt_pk_bf16_f32 v22, v22, v26\n"
      "v_mfma_f32_16x16x32_bf16 v[52:55], a[152:155], a[200:203], v[52:55]\n"
      "ds_read_b128 a[192:195], v3 offset:0\n"
      "ds_write_addtid_b32 v22 offset:50944\n"
      "v_fmac_f32 v48, s45, v36\n"
      "v_mfma_f32_16x16x32_bf16 v[56:59], a[144:147], a[200:203], v[56:59]\n"
      "v_fmac_f32 v49, s45, v37\n"
      "v_cvt_pk_bf16_f32 v23, v23, v27\n"
      "v_mfma_f32_16x16x32_bf16 v[60:63], a[136:139], a[200:203], v[60:63]\n"
      "s_waitcnt lgkmcnt(2)\n"
      "ds_write_addtid_b32 v23 offset:55040\n"
      "v_fmac_f32 v50, s45, v38\n"
      "v_mfma_f32_4x4x4_16b_bf16 v[12:15], a[216:217], a[220:221], 0\n"
      "v_fmac_f32 v51, s45, v39\n"
      "v_mfma_f32_4x4x4_16b_bf16 v[16:19], a[216:217], a[222:223], 0\n"
      "v_fma_f32 v36, s47, v36, v96\n"
      "v_fma_f32 v37, s47, v37, v97\n"
      "v_mfma_f32_16x16x32_bf16 v[64:67], a[148:151], a[204:207], v[64:67]\n"
      "s_nop 0\n"
      "ds_read_b128 a[196:199], v3 offset:64\n"
      "v_fma_f32 v38, s47, v38, v98\n"
      "v_cvt_pk_bf16_f32 v12, v12, v16\n"
      "v_mfma_f32_16x16x32_bf16 v[68:71], a[140:143], a[204:207], v[68:71]\n"
      "ds_write_addtid_b32 v12 offset:43264\n"
      "v_cvt_pk_bf16_f32 v13, v13, v17\n"
      "v_mfma_f32_16x16x32_bf16 v[72:75], a[132:135], a[204:207], v[72:75]\n"
      "ds_write_addtid_b32 v13 offset:47360\n"
      "v_fma_f32 v39, s47, v39, v99\n"
      "v_mfma_f32_16x16x32_bf16 v[52:55], a[128:131], a[208:211], v[52:55]\n"
      "ds_read_b128 a[200:203], v3 offset:128\n"
      "ds_read_b64_tr_b16 a[224:225], v7 offset:53248\n"
      "v_mfma_f32_16x16x32_bf16 v[56:59], a[120:123], a[208:211], v[56:59]\n"
      "ds_read_b64_tr_b16 a[226:227], v7 offset:53256\n"
      "v_cvt_pk_bf16_f32 v48, v48, v49\n"
      "v_cvt_pk_bf16_f32 v14, v14, v18\n"
      "v_mfma_f32_16x16x32_bf16 v[60:63], a[112:115], a[208:211], v[60:63]\n"
      "ds_write_addtid_b32 v48 offset:17408\n"
      "ds_write_addtid_b32 v14 offset:51456\n"
      "v_mfma_f32_16x16x32_bf16 v[64:67], a[124:127], a[212:215], v[64:67]\n"
      "ds_read_b128 a[204:207], v4 offset:0\n"
      "v_cvt_pk_bf16_f32 v50, v50, v51\n"
      "v_mfma_f32_16x16x32_bf16 v[68:71], a[116:119], a[212:215], v[68:71]\n"
      "ds_write_addtid_b32 v50 offset:17920\n"
      "v_cvt_pk_bf16_f32 v15, v15, v19\n"
      "v_cvt_pk_bf16_f32 v36, v36, v37\n"
      "v_mfma_f32_16x16x32_bf16 v[72:75], a[108:111], a[212:215], v[72:75]\n"
      "s_waitcnt lgkmcnt(4)\n"
      "ds_write_addtid_b32 v36 offset:19456\n"
      "ds_write_addtid_b32 v15 offset:55552\n"
      "v_sub_f32 v52, v64, v52\n"
      "v_mfma_f32_16x16x32_bf16 v[100:103], a[0:3], a[192:195], 0\n"
      "ds_read_b128 a[208:211], v4 offset:64\n"
      "ds_write_addtid_b32 v52 offset:0\n"
      "v_sub_f32_e64 v53, v65, v53\n"
      "v_mfma_f32_16x16x32_bf16 v[104:107], a[4:7], a[192:195], 0\n"
      "ds_write_addtid_b32 v53 offset:512\n"
      "v_sub_f32 v54, v66, v54\n"
      "v_mfma_f32_4x4x4_16b_bf16 v[20:23], a[216:217], a[224:225], 0\n"
      "v_sub_f32 v55, v67, v55\n"
      "v_mfma_f32_4x4x4_16b_bf16 v[24:27], a[216:217], a[226:227], 0\n"
      "ds_write_addtid_b32 v54 offset:1024\n"
      "ds_write_addtid_b32 v55 offset:1536\n"
      "v_sub_f32_e64 v56, v68, v56\n"
      "v_mfma_f32_16x16x32_bf16 v[108:111], a[8:11], a[192:195], 0\n"
      "ds_write_addtid_b32 v56 offset:2048\n"
      "v_sub_f32 v57, v69, v57\n"
      "v_mfma_f32_16x16x32_bf16 v[100:103], a[12:15], a[196:199], v[100:103]\n"
      "ds_read_b128 a[212:215], v4 offset:128\n"
      "ds_write_addtid_b32 v57 offset:2560\n"
      "v_mfma_f32_16x16x32_bf16 v[104:107], a[16:19], a[196:199], v[104:107]\n"
      "v_cvt_pk_bf16_f32 v20, v20, v24\n"
      "v_sub_f32 v58, v70, v58\n"
      "v_mfma_f32_16x16x32_bf16 v[108:111], a[20:23], a[196:199], v[108:111]\n"
      "ds_write_addtid_b32 v20 offset:43776\n"
      "ds_write_addtid_b32 v58 offset:3072\n"
      "v_sub_f32 v59, v71, v59\n"
      "v_mfma_f32_16x16x32_bf16 v[100:103], a[24:27], a[200:203], v[100:103]\n"
      "ds_read_b128 a[192:195], v5 offset:0\n"
      "ds_write_addtid_b32 v59 offset:3584\n"
      "v_mfma_f32_16x16x32_bf16 v[104:107], a[28:31], a[200:203], v[104:107]\n"
      "v_sub_f32 v60, v72, v60\n"
      "v_cvt_pk_bf16_f32 v38, v38, v39\n"
      "v_mfma_f32_16x16x32_bf16 v[108:111], a[32:35], a[200:203], v[108:111]\n"
      "s_waitcnt lgkmcnt(4)\n"
      "ds_write_addtid_b32 v38 offset:19968\n"
      "ds_write_addtid_b32 v60 offset:4096\n"
      "v_cvt_pk_bf16_f32 v21, v21, v25\n"
      "v_mfma_f32_16x16x32_bf16 v[100:103], a[36:39], a[204:207], v[100:103]\n"
      "s_nop 0\n"
      "ds_read_b128 a[196:199], v5 offset:64\n"
      "ds_write_addtid_b32 v21 offset:47872\n"
      "v_mfma_f32_16x16x32_bf16 v[104:107], a[40:43], a[204:207], v[104:107]\n"
      "v_sub_f32 v61, v73, v61\n"
      "v_sub_f32 v62, v74, v62\n"
      "v_mfma_f32_16x16x32_bf16 v[108:111], a[44:47], a[204:207], v[108:111]\n"
      "ds_write_addtid_b32 v61 offset:4608\n"
      "ds_write_addtid_b32 v62 offset:5120\n"
      "v_sub_f32 v63, v75, v63\n"
      "v_mfma_f32_16x16x32_bf16 v[100:103], a[48:51], a[208:211], v[100:103]\n"
      "s_nop 0\n"
      "ds_read_b128 a[200:203], v5 offset:128\n"
      "ds_write_addtid_b32 v63 offset:5632\n"
      "v_mfma_f32_16x16x32_bf16 v[104:107], a[52:55], a[208:211], v[104:107]\n"
      "v_cvt_pk_bf16_f32 v22, v22, v26\n"
      "v_cvt_pk_bf16_f32 v23, v23, v27\n"
      "v_mfma_f32_16x16x32_bf16 v[108:111], a[56:59], a[208:211], v[108:111]\n"
      "ds_write_addtid_b32 v22 offset:51968\n"
      "ds_write_addtid_b32 v23 offset:56064\n"
      "v_fma_f32 v40, 0.5, v52, -v64\n"
      "v_mfma_f32_16x16x32_bf16 v[100:103], a[60:63], a[212:215], v[100:103]\n"
      "v_fma_f32 v41, 0.5, v53, -v65\n"
      "v_fma_f32 v42, 0.5, v54, -v66\n"
      "v_mfma_f32_16x16x32_bf16 v[104:107], a[64:67], a[212:215], v[104:107]\n"
      "v_fma_f32 v43, 0.5, v55, -v67\n"
      "v_fma_f32 v44, 0.5, v56, -v68\n"
      "v_mfma_f32_16x16x32_bf16 v[108:111], a[68:71], a[212:215], v[108:111]\n"
      "s_waitcnt lgkmcnt(0)\n"
      "v_fma_f32 v45, 0.5, v57, -v69\n"
      "s_barrier\n"
      "v_mfma_f32_16x16x32_bf16 v[100:103], a[72:75], a[192:195], v[100:103]\n"
      "s_add_u32 s59, s62, 5704704\n"
      "ds_read_b128 a[204:207], v0 offset:16384\n"
      "ds_read2st64_b32 v[28:29], v6 offset0:0 offset1:2\n"
      "v_fma_f32 v46, 0.5, v58, -v70\n"
      "v_mfma_f32_16x16x32_bf16 v[104:107], a[76:79], a[192:195], v[104:107]\n"
      "ds_read_b128 a[208:211], v0 offset:20480\n"
      "ds_read2st64_b32 v[30:31], v6 offset0:4 offset1:6\n"
      "v_mfma_f32_16x16x32_bf16 v[108:111], a[80:83], a[192:195], v[108:111]\n"
      "ds_read2st64_b32 v[32:33], v6 offset0:8 offset1:10\n"
      "v_fma_f32 v47, 0.5, v59, -v71\n"
      "v_mfma_f32_16x16x32_bf16 v[100:103], a[84:87], a[196:199], v[100:103]\n"
      "ds_read_b128 a[212:215], v0 offset:16448\n"
      "v_fma_f32 v48, 0.5, v60, -v72\n"
      "v_mfma_f32_16x16x32_bf16 v[104:107], a[88:91], a[196:199], v[104:107]\n"
      "ds_read2st64_b32 v[34:35], v6 offset0:12 offset1:14\n"
      "ds_read2st64_b32 v[36:37], v6 offset0:16 offset1:18\n"
      "v_mfma_f32_16x16x32_bf16 v[108:111], a[92:95], a[196:199], v[108:111]\n"
      "v_bfe_u32 v16, v11, 6, 1\n"
      "v_mul_i32_i24 v17, 12296, v16\n"
      "s_mov_b32 s50, -8200\n"
      "v_add3_u32 v12, v8, v17, s50\n"
      "v_add_u32 v14, 128, v12\n"
      "v_mul_i32_i24 v17, 8, v16\n"
      "s_mov_b32 s50, -4104\n"
      "v_add3_u32 v13, v8, v17, s50\n"
      "v_add_u32 v15, 128, v13\n"
      "v_mfma_f32_16x16x32_bf16 v[100:103], a[96:99], a[200:203], v[100:103]\n"
      "s_nop 0\n"
      "ds_read_b128 a[192:195], v0 offset:20544\n"
      "ds_read2st64_b32 a[232:233], v12 offset0:4 offset1:6\n"
      "v_mfma_f32_16x16x32_bf16 v[104:107], a[100:103], a[200:203], v[104:107]\n"
      "ds_read2st64_b32 a[234:235], v14 offset0:4 offset1:6\n"
      "ds_read2st64_b32 v[38:39], v6 offset0:20 offset1:22\n"
      "v_fma_f32 v49, 0.5, v61, -v73\n"
      "v_fma_f32 v50, 0.5, v62, -v74\n"
      "v_fma_f32 v51, 0.5, v63, -v75\n"
      "v_mfma_f32_16x16x32_bf16 v[108:111], a[104:107], a[200:203], v[108:111]\n"
      "s_waitcnt lgkmcnt(6)\n"
      "v_sub_f32 v28, v28, v40\n"
      "v_sub_f32 v29, v29, v41\n"
      "v_mfma_f32_16x16x32_bf16 v[52:55], v[252:255], a[204:207], v[88:91]\n"
      "ds_read_b128 a[196:199], v0 offset:16512\n"
      "v_sub_f32 v30, v30, v42\n"
      "v_mfma_f32_16x16x32_bf16 v[56:59], v[244:247], a[204:207], v[92:95]\n"
      "v_sub_f32 v31, v31, v43\n"
      "v_fmac_f32 v28, s46, v76\n"
      "v_mfma_f32_16x16x32_bf16 v[60:63], v[236:239], a[204:207], v[96:99]\n"
      "v_fmac_f32 v29, s46, v77\n"
      "v_fmac_f32 v30, s46, v78\n"
      "v_mfma_f32_16x16x32_bf16 v[64:67], v[248:251], a[208:211], v[88:91]\n"
      "ds_read_b128 a[200:203], v0 offset:20608\n"
      "v_fmac_f32 v31, s46, v79\n"
      "v_mfma_f32_16x16x32_bf16 v[68:71], v[240:243], a[208:211], v[92:95]\n"
      "v_fmac_f32 v40, s45, v28\n"
      "v_fmac_f32 v41, s45, v29\n"
      "v_mfma_f32_16x16x32_bf16 v[72:75], v[232:235], a[208:211], v[96:99]\n"
      "v_fmac_f32 v42, s45, v30\n"
      "v_fmac_f32 v43, s45, v31\n"
      "v_mfma_f32_16x16x32_bf16 v[52:55], v[228:231], a[212:215], v[52:55]\n"
      "s_nop 0\n"
      "ds_read_b128 a[204:207], v1 offset:16384\n"
      "v_fma_f32 v28, s47, v28, v100\n"
      "v_mfma_f32_16x16x32_bf16 v[56:59], v[220:223], a[212:215], v[56:59]\n"
      "v_fma_f32 v29, s47, v29, v101\n"
      "v_mfma_f32_16x16x32_bf16 v[60:63], v[212:215], a[212:215], v[60:63]\n"
      "s_waitcnt lgkmcnt(1)\n"
      "v_fma_f32 v30, s47, v30, v102\n"
      "v_mfma_f32_16x16x32_bf16 v[64:67], v[224:227], a[192:195], v[64:67]\n"
      "s_nop 0\n"
      "ds_read_b128 a[208:211], v1 offset:20480\n"
      "v_fma_f32 v31, s47, v31, v103\n"
      "v_mfma_f32_16x16x32_bf16 v[68:71], v[216:219], a[192:195], v[68:71]\n"
      "v_cvt_pk_bf16_f32 v40, v40, v41\n"
      "v_mfma_f32_16x16x32_bf16 v[72:75], v[208:211], a[192:195], v[72:75]\n"
      "buffer_store_dwordx4 a[232:235], v11, s[8:11], s59 offen offset:0 sc1\n"
      "v_mfma_f32_16x16x32_bf16 v[52:55], v[204:207], a[196:199], v[52:55]\n"
      "ds_read_b128 a[212:215], v1 offset:16448\n"
      "ds_write_addtid_b32 v40 offset:12288\n"
      "v_mfma_f32_16x16x32_bf16 v[56:59], v[196:199], a[196:199], v[56:59]\n"
      "ds_read2st64_b32 a[236:237], v13 offset0:4 offset1:6\n"
      "ds_read2st64_b32 a[238:239], v15 offset0:4 offset1:6\n"
      "v_mfma_f32_16x16x32_bf16 v[60:63], v[188:191], a[196:199], v[60:63]\n"
      "v_cvt_pk_bf16_f32 v42, v42, v43\n"
      "v_cvt_pk_bf16_f32 v28, v28, v29\n"
      "v_mfma_f32_16x16x32_bf16 v[64:67], v[200:203], a[200:203], v[64:67]\n"
      "ds_read_b128 a[192:195], v1 offset:20544\n"
      "ds_write_addtid_b32 v42 offset:12800\n"
      "v_mfma_f32_16x16x32_bf16 v[68:71], v[192:195], a[200:203], v[68:71]\n"
      "ds_write_addtid_b32 v28 offset:14336\n"
      "v_cvt_pk_bf16_f32 v30, v30, v31\n"
      "v_mfma_f32_16x16x32_bf16 v[72:75], v[184:187], a[200:203], v[72:75]\n"
      "s_waitcnt lgkmcnt(6)\n"
      "ds_write_addtid_b32 v30 offset:14848\n"
      "v_sub_f32 v32, v32, v44\n"
      "v_mfma_f32_16x16x32_bf16 v[52:55], v[180:183], a[204:207], v[52:55]\n"
      "ds_read_b128 a[196:199], v1 offset:16512\n"
      "v_sub_f32 v33, v33, v45\n"
      "v_mfma_f32_16x16x32_bf16 v[56:59], v[172:175], a[204:207], v[56:59]\n"
      "v_sub_f32 v34, v34, v46\n"
      "v_sub_f32 v35, v35, v47\n"
      "v_mfma_f32_16x16x32_bf16 v[60:63], v[164:167], a[204:207], v[60:63]\n"
      "v_fmac_f32 v32, s46, v80\n"
      "v_fmac_f32 v33, s46, v81\n"
      "v_mfma_f32_16x16x32_bf16 v[64:67], v[176:179], a[208:211], v[64:67]\n"
      "ds_read_b128 a[200:203], v1 offset:20608\n"
      "v_fmac_f32 v34, s46, v82\n"
      "v_mfma_f32_16x16x32_bf16 v[68:71], v[168:171], a[208:211], v[68:71]\n"
      "v_fmac_f32 v35, s46, v83\n"
      "v_fmac_f32 v44, s45, v32\n"
      "v_mfma_f32_16x16x32_bf16 v[72:75], v[160:163], a[208:211], v[72:75]\n"
      "v_fmac_f32 v45, s45, v33\n"
      "v_fmac_f32 v46, s45, v34\n"
      "v_mfma_f32_16x16x32_bf16 v[52:55], v[156:159], a[212:215], v[52:55]\n"
      "ds_read_b128 a[204:207], v2 offset:16384\n"
      "v_fmac_f32 v47, s45, v35\n"
      "v_mfma_f32_16x16x32_bf16 v[56:59], v[148:151], a[212:215], v[56:59]\n"
      "s_nop 0\n"
      "v_fma_f32 v32, s47, v32, v104\n"
      "v_fma_f32 v33, s47, v33, v105\n"
      "v_mfma_f32_16x16x32_bf16 v[60:63], v[140:143], a[212:215], v[60:63]\n"
      "s_waitcnt lgkmcnt(1)\n"
      "v_fma_f32 v34, s47, v34, v106\n"
      "v_fma_f32 v35, s47, v35, v107\n"
      "v_mfma_f32_16x16x32_bf16 v[64:67], v[152:155], a[192:195], v[64:67]\n"
      "s_nop 0\n"
      "buffer_store_dwordx4 a[236:239], v11, s[8:11], s59 offen offset:128 sc1\n"
      "ds_read_b128 a[208:211], v2 offset:20480\n"
      "v_mfma_f32_16x16x32_bf16 v[68:71], v[144:147], a[192:195], v[68:71]\n"
      "v_cvt_pk_bf16_f32 v44, v44, v45\n"
      "v_cvt_pk_bf16_f32 v46, v46, v47\n"
      "v_mfma_f32_16x16x32_bf16 v[72:75], v[136:139], a[192:195], v[72:75]\n"
      "ds_write_addtid_b32 v44 offset:20484\n"
      "ds_read2st64_b32 a[240:241], v8 offset0:4 offset1:6\n"
      "v_mfma_f32_16x16x32_bf16 v[52:55], v[132:135], a[196:199], v[52:55]\n"
      "ds_read_b128 a[212:215], v2 offset:16448\n"
      "ds_write_addtid_b32 v46 offset:20996\n"
      "v_mfma_f32_16x16x32_bf16 v[56:59], v[124:127], a[196:199], v[56:59]\n"
      "ds_read2st64_b32 a[242:243], v9 offset0:4 offset1:6\n"
      "v_cvt_pk_bf16_f32 v32, v32, v33\n"
      "v_mfma_f32_16x16x32_bf16 v[60:63], a[184:187], a[196:199], v[60:63]\n"
      "ds_write_addtid_b32 v32 offset:22532\n"
      "v_cvt_pk_bf16_f32 v34, v34, v35\n"
      "v_mfma_f32_16x16x32_bf16 v[64:67], v[128:131], a[200:203], v[64:67]\n"
      "ds_read_b128 a[192:195], v2 offset:20544\n"
      "ds_write_addtid_b32 v34 offset:23044\n"
      "v_mfma_f32_16x16x32_bf16 v[68:71], a[188:191], a[200:203], v[68:71]\n"
      "v_sub_f32 v36, v36, v48\n"
      "v_sub_f32 v37, v37, v49\n"
      "v_mfma_f32_16x16x32_bf16 v[72:75], a[180:183], a[200:203], v[72:75]\n"
      "s_waitcnt lgkmcnt(5)\n"
      "v_sub_f32 v38, v38, v50\n"
      "v_sub_f32_e64 v39, v39, v51\n"
      "v_mfma_f32_16x16x32_bf16 v[52:55], a[176:179], a[204:207], v[52:55]\n"
      "ds_read_b128 a[196:199], v2 offset:16512\n"
      "v_fmac_f32 v36, s46, v84\n"
      "v_mfma_f32_16x16x32_bf16 v[56:59], a[168:171], a[204:207], v[56:59]\n"
      "v_fmac_f32 v37, s46, v85\n"
      "v_fmac_f32 v38, s46, v86\n"
      "v_mfma_f32_16x16x32_bf16 v[60:63], a[160:163], a[204:207], v[60:63]\n"
      "v_fmac_f32 v39, s46, v87\n"
      "v_fmac_f32 v48, s45, v36\n"
      "v_mfma_f32_16x16x32_bf16 v[64:67], a[172:175], a[208:211], v[64:67]\n"
      "s_nop 0\n"
      "ds_read_b128 a[200:203], v2 offset:20608\n"
      "v_fmac_f32 v49, s45, v37\n"
      "v_mfma_f32_16x16x32_bf16 v[68:71], a[164:167], a[208:211], v[68:71]\n"
      "v_fmac_f32 v50, s45, v38\n"
      "v_fmac_f32 v51, s45, v39\n"
      "v_mfma_f32_16x16x32_bf16 v[72:75], a[156:159], a[208:211], v[72:75]\n"
      "v_fma_f32 v36, s47, v36, v108\n"
      "v_fma_f32 v37, s47, v37, v109\n"
      "v_mfma_f32_16x16x32_bf16 v[52:55], a[152:155], a[212:215], v[52:55]\n"
      "s_waitcnt lgkmcnt(1)\n"
      "ds_read_b128 a[204:207], v3 offset:8192\n"
      "v_fma_f32 v38, s47, v38, v110\n"
      "v_mfma_f32_16x16x32_bf16 v[56:59], a[144:147], a[212:215], v[56:59]\n"
      "buffer_store_dwordx4 a[240:243], v11, s[8:11], s59 offen offset:256 sc1\n"
      "v_mfma_f32_16x16x32_bf16 v[60:63], a[136:139], a[212:215], v[60:63]\n"
      "v_fma_f32 v39, s47, v39, v111\n"
      "v_cvt_pk_bf16_f32 v48, v48, v49\n"
      "v_mfma_f32_16x16x32_bf16 v[64:67], a[148:151], a[192:195], v[64:67]\n"
      "ds_read_b128 a[208:211], v3 offset:8256\n"
      "ds_write_addtid_b32 v48 offset:16384\n"
      "v_mfma_f32_16x16x32_bf16 v[68:71], a[140:143], a[192:195], v[68:71]\n"
      "v_cvt_pk_bf16_f32 v50, v50, v51\n"
      "v_cvt_pk_bf16_f32 v36, v36, v37\n"
      "v_mfma_f32_16x16x32_bf16 v[72:75], a[132:135], a[192:195], v[72:75]\n"
      "ds_write_addtid_b32 v50 offset:16896\n"
      "ds_write_addtid_b32 v36 offset:18432\n"
      "v_mfma_f32_16x16x32_bf16 v[52:55], a[128:131], a[196:199], v[52:55]\n"
      "ds_read_b128 a[212:215], v3 offset:8320\n"
      "v_cvt_pk_bf16_f32 v38, v38, v39\n"
      "v_mfma_f32_16x16x32_bf16 v[56:59], a[120:123], a[196:199], v[56:59]\n"
      "ds_write_addtid_b32 v38 offset:18944\n"
      "v_mfma_f32_16x16x32_bf16 v[60:63], a[112:115], a[196:199], v[60:63]\n"
      "s_waitcnt lgkmcnt(4)\n"
      "v_mfma_f32_16x16x32_bf16 v[64:67], a[124:127], a[200:203], v[64:67]\n"
      "s_nop 0\n"
      "ds_read_b128 a[192:195], v4 offset:8192\n"
      "v_mfma_f32_16x16x32_bf16 v[68:71], a[116:119], a[200:203], v[68:71]\n"
      "v_mfma_f32_16x16x32_bf16 v[72:75], a[108:111], a[200:203], v[72:75]\n"
      "v_mfma_f32_16x16x32_bf16 v[112:115], a[0:3], a[204:207], 0\n"
      "ds_read_b128 a[196:199], v4 offset:8256\n"
      "v_mfma_f32_16x16x32_bf16 v[116:119], a[4:7], a[204:207], 0\n"
      "v_mfma_f32_16x16x32_bf16 v[120:123], a[8:11], a[204:207], 0\n"
      "v_mfma_f32_16x16x32_bf16 v[112:115], a[12:15], a[208:211], v[112:115]\n"
      "ds_read_b128 a[200:203], v4 offset:8320\n"
      "v_sub_f32 v52, v64, v52\n"
      "v_sub_f32 v53, v65, v53\n"
      "v_sub_f32 v54, v66, v54\n"
      "v_sub_f32 v55, v67, v55\n"
      "v_sub_f32 v56, v68, v56\n"
      "v_sub_f32 v57, v69, v57\n"
      "v_mfma_f32_16x16x32_bf16 v[116:119], a[16:19], a[208:211], v[116:119]\n"
      "ds_write_addtid_b32 v52 offset:6144\n"
      "ds_write_addtid_b32 v53 offset:6656\n"
      "ds_write_addtid_b32 v54 offset:7168\n"
      "v_sub_f32 v58, v70, v58\n"
      "v_sub_f32 v59, v71, v59\n"
      "v_sub_f32 v60, v72, v60\n"
      "v_sub_f32 v61, v73, v61\n"
      "v_mfma_f32_16x16x32_bf16 v[120:123], a[20:23], a[208:211], v[120:123]\n"
      "s_waitcnt lgkmcnt(3)\n"
      "ds_write_addtid_b32 v55 offset:7680\n"
      "ds_write_addtid_b32 v56 offset:8192\n"
      "ds_write_addtid_b32 v57 offset:8704\n"
      "v_sub_f32 v62, v74, v62\n"
      "v_sub_f32_e64 v63, v75, v63\n"
      "v_mfma_f32_16x16x32_bf16 v[112:115], a[24:27], a[212:215], v[112:115]\n"
      "ds_write_addtid_b32 v58 offset:9216\n"
      "ds_write_addtid_b32 v59 offset:9728\n"
      "ds_write_addtid_b32 v60 offset:10240\n"
      "v_mfma_f32_16x16x32_bf16 v[116:119], a[28:31], a[212:215], v[116:119]\n"
      "ds_write_addtid_b32 v61 offset:10752\n"
      "ds_write_addtid_b32 v62 offset:11264\n"
      "ds_write_addtid_b32 v63 offset:11776\n"
      "v_mfma_f32_16x16x32_bf16 v[120:123], a[32:35], a[212:215], v[120:123]\n"
      "s_waitcnt lgkmcnt(0)\n"
      "s_barrier\n"
      "v_mfma_f32_16x16x32_bf16 v[112:115], a[36:39], a[192:195], v[112:115]\n"
      "s_add_u32 s59, s59, 783360\n"
      "ds_read_b128 a[204:207], v5 offset:8192\n"
      "ds_read2st64_b32 v[28:29], v6 offset0:24 offset1:26\n"
      "v_mfma_f32_16x16x32_bf16 v[116:119], a[40:43], a[192:195], v[116:119]\n"
      "ds_read2st64_b32 v[30:31], v6 offset0:28 offset1:30\n"
      "ds_read2st64_b32 v[32:33], v6 offset0:32 offset1:34\n"
      "v_mfma_f32_16x16x32_bf16 v[120:123], a[44:47], a[192:195], v[120:123]\n"
      "ds_read2st64_b32 v[34:35], v6 offset0:36 offset1:38\n"
      "ds_read2st64_b32 v[36:37], v6 offset0:40 offset1:42\n"
      "v_mfma_f32_16x16x32_bf16 v[112:115], a[48:51], a[196:199], v[112:115]\n"
      "ds_read_b128 a[208:211], v5 offset:8256\n"
      "ds_read2st64_b32 v[38:39], v6 offset0:44 offset1:46\n"
      "v_mfma_f32_16x16x32_bf16 v[116:119], a[52:55], a[196:199], v[116:119]\n"
      "v_fma_f32 v40, 0.5, v52, -v64\n"
      "v_fma_f32 v41, 0.5, v53, -v65\n"
      "v_mfma_f32_16x16x32_bf16 v[120:123], a[56:59], a[196:199], v[120:123]\n"
      "ds_read2st64_b32 a[232:233], v12 offset0:0 offset1:2\n"
      "ds_read2st64_b32 a[234:235], v14 offset0:0 offset1:2\n"
      "v_mfma_f32_16x16x32_bf16 v[112:115], a[60:63], a[200:203], v[112:115]\n"
      "ds_read_b128 a[212:215], v5 offset:8320\n"
      "v_fma_f32 v42, 0.5, v54, -v66\n"
      "v_mfma_f32_16x16x32_bf16 v[116:119], a[64:67], a[200:203], v[116:119]\n"
      "ds_read2st64_b32 a[236:237], v13 offset0:0 offset1:2\n"
      "ds_read2st64_b32 a[238:239], v15 offset0:0 offset1:2\n"
      "v_mfma_f32_16x16x32_bf16 v[120:123], a[68:71], a[200:203], v[120:123]\n"
      "s_waitcnt lgkmcnt(9)\n"
      "v_fma_f32 v43, 0.5, v55, -v67\n"
      "v_fma_f32 v44, 0.5, v56, -v68\n"
      "v_mfma_f32_16x16x32_bf16 v[112:115], a[72:75], a[204:207], v[112:115]\n"
      "s_waitcnt lgkmcnt(3)\n"
      "buffer_store_dwordx4 a[232:235], v11, s[8:11], s59 offen offset:0 sc1\n"
      "v_mfma_f32_16x16x32_bf16 v[116:119], a[76:79], a[204:207], v[116:119]\n"
      "v_fma_f32 v45, 0.5, v57, -v69\n"
      "v_fma_f32 v46, 0.5, v58, -v70\n"
      "v_mfma_f32_16x16x32_bf16 v[120:123], a[80:83], a[204:207], v[120:123]\n"
      "ds_read2st64_b32 a[240:241], v8 offset0:0 offset1:2\n"
      "ds_read2st64_b32 a[242:243], v9 offset0:0 offset1:2\n"
      "v_mfma_f32_16x16x32_bf16 v[112:115], a[84:87], a[208:211], v[112:115]\n"
      "s_waitcnt lgkmcnt(2)\n"
      "buffer_store_dwordx4 a[236:239], v11, s[8:11], s59 offen offset:128 sc1\n"
      "v_mfma_f32_16x16x32_bf16 v[116:119], a[88:91], a[208:211], v[116:119]\n"
      "v_fma_f32 v47, 0.5, v59, -v71\n"
      "v_fma_f32 v48, 0.5, v60, -v72\n"
      "v_mfma_f32_16x16x32_bf16 v[120:123], a[92:95], a[208:211], v[120:123]\n"
      "s_nop 0\n"
      "v_fma_f32 v49, 0.5, v61, -v73\n"
      "v_fma_f32 v50, 0.5, v62, -v74\n"
      "v_mfma_f32_16x16x32_bf16 v[112:115], a[96:99], a[212:215], v[112:115]\n"
      "s_waitcnt lgkmcnt(0)\n"
      "buffer_store_dwordx4 a[240:243], v11, s[8:11], s59 offen offset:256 sc1\n"
      "v_mfma_f32_16x16x32_bf16 v[116:119], a[100:103], a[212:215], v[116:119]\n"
      "v_fma_f32 v51, 0.5, v63, -v75\n"
      "v_sub_f32 v28, v28, v40\n"
      "v_mfma_f32_16x16x32_bf16 v[120:123], a[104:107], a[212:215], v[120:123]\n"
      "v_sub_f32 v29, v29, v41\n"
      "v_sub_f32 v30, v30, v42\n"
      "v_sub_f32 v31, v31, v43\n"
      "v_fmac_f32 v28, s46, v88\n"
      "v_fmac_f32 v29, s46, v89\n"
      "v_fmac_f32 v30, s46, v90\n"
      "v_fmac_f32 v31, s46, v91\n"
      "v_fmac_f32 v40, s45, v28\n"
      "v_fmac_f32_e64 v41, s45, v29\n"
      "v_pk_fma_f32 v[42:43], v[30:31], s[44:45], v[42:43] op_sel:[0,1,0] op_sel_hi:[1,1,1]\n"
      "v_cvt_pk_bf16_f32 v40, v40, v41\n"
      "v_cvt_pk_bf16_f32 v42, v42, v43\n"
      "v_pk_add_f32 v[32:33], v[32:33], v[44:45] neg_lo:[0,1] neg_hi:[0,1]\n"
      "v_pk_add_f32 v[34:35], v[34:35], v[46:47] neg_lo:[0,1] neg_hi:[0,1]\n"
      "v_pk_fma_f32 v[32:33], v[92:93], s[46:47], v[32:33] op_sel:[0,0,0] op_sel_hi:[1,0,1]\n"
      "v_pk_fma_f32 v[34:35], v[94:95], s[46:47], v[34:35] op_sel:[0,0,0] op_sel_hi:[1,0,1]\n"
      "v_pk_fma_f32 v[44:45], v[32:33], s[44:45], v[44:45] op_sel:[0,1,0] op_sel_hi:[1,1,1]\n"
      "v_pk_fma_f32 v[46:47], v[34:35], s[44:45], v[46:47] op_sel:[0,1,0] op_sel_hi:[1,1,1]\n"
      "v_cvt_pk_bf16_f32 v44, v44, v45\n"
      "v_cvt_pk_bf16_f32 v46, v46, v47\n"
      "v_pk_add_f32 v[36:37], v[36:37], v[48:49] neg_lo:[0,1] neg_hi:[0,1]\n"
      "v_pk_add_f32 v[38:39], v[38:39], v[50:51] neg_lo:[0,1] neg_hi:[0,1]\n"
      "v_pk_fma_f32 v[36:37], v[96:97], s[46:47], v[36:37] op_sel:[0,0,0] op_sel_hi:[1,0,1]\n"
      "v_pk_fma_f32 v[38:39], v[98:99], s[46:47], v[38:39] op_sel:[0,0,0] op_sel_hi:[1,0,1]\n"
      "v_pk_fma_f32 v[48:49], v[36:37], s[44:45], v[48:49] op_sel:[0,1,0] op_sel_hi:[1,1,1]\n"
      "v_pk_fma_f32 v[50:51], v[38:39], s[44:45], v[50:51] op_sel:[0,1,0] op_sel_hi:[1,1,1]\n"
      "v_cvt_pk_bf16_f32 v48, v48, v49\n"
      "v_cvt_pk_bf16_f32 v50, v50, v51\n"
      "ds_write_addtid_b32 v40 offset:13312\n"
      "ds_write_addtid_b32 v42 offset:13824\n"
      "ds_write_addtid_b32 v44 offset:21508\n"
      "ds_write_addtid_b32 v46 offset:22020\n"
      "ds_write_addtid_b32 v48 offset:17408\n"
      "ds_write_addtid_b32 v50 offset:17920\n"
      "v_pk_fma_f32 v[28:29], v[28:29], s[46:47], v[112:113] op_sel:[0,1,0] op_sel_hi:[1,1,1]\n"
      "v_pk_fma_f32 v[30:31], v[30:31], s[46:47], v[114:115] op_sel:[0,1,0] op_sel_hi:[1,1,1]\n"
      "v_cvt_pk_bf16_f32 v28, v28, v29\n"
      "v_cvt_pk_bf16_f32 v30, v30, v31\n"
      "v_pk_fma_f32 v[32:33], v[32:33], s[46:47], v[116:117] op_sel:[0,1,0] op_sel_hi:[1,1,1]\n"
      "v_pk_fma_f32 v[34:35], v[34:35], s[46:47], v[118:119] op_sel:[0,1,0] op_sel_hi:[1,1,1]\n"
      "v_cvt_pk_bf16_f32 v32, v32, v33\n"
      "v_cvt_pk_bf16_f32 v34, v34, v35\n"
      "v_pk_fma_f32 v[36:37], v[36:37], s[46:47], v[120:121] op_sel:[0,1,0] op_sel_hi:[1,1,1]\n"
      "v_pk_fma_f32 v[38:39], v[38:39], s[46:47], v[122:123] op_sel:[0,1,0] op_sel_hi:[1,1,1]\n"
      "v_cvt_pk_bf16_f32 v36, v36, v37\n"
      "v_cvt_pk_bf16_f32 v38, v38, v39\n"
      "ds_write_addtid_b32 v28 offset:15360\n"
      "ds_write_addtid_b32 v30 offset:15872\n"
      "ds_write_addtid_b32 v32 offset:23556\n"
      "ds_write_addtid_b32 v34 offset:24068\n"
      "ds_write_addtid_b32 v36 offset:19456\n"
      "ds_write_addtid_b32 v38 offset:19968\n"
      "s_waitcnt lgkmcnt(0)\n"
      "s_barrier\n"
      "ds_read2st64_b32 a[232:233], v12 offset0:4 offset1:6\n"
      "ds_read2st64_b32 a[234:235], v14 offset0:4 offset1:6\n"
      "ds_read2st64_b32 a[236:237], v13 offset0:4 offset1:6\n"
      "ds_read2st64_b32 a[238:239], v15 offset0:4 offset1:6\n"
      "ds_read2st64_b32 a[240:241], v8 offset0:4 offset1:6\n"
      "ds_read2st64_b32 a[242:243], v9 offset0:4 offset1:6\n"
      "s_add_u32 s59, s59, 3072\n"
      "s_waitcnt lgkmcnt(4)\n"
      "buffer_store_dwordx4 a[232:235], v11, s[8:11], s59 offen offset:0 sc1\n"
      "s_waitcnt lgkmcnt(2)\n"
      "buffer_store_dwordx4 a[236:239], v11, s[8:11], s59 offen offset:128 sc1\n"
      "s_waitcnt lgkmcnt(0)\n"
      "buffer_store_dwordx4 a[240:243], v11, s[8:11], s59 offen offset:256 sc1\n"
      "LSend_%=:\n"
      "Lfin_%=:\n"
      "s_waitcnt vmcnt(0) lgkmcnt(0)\n"
      "s_endpgm\n"
      ".rept 512\n"
      "s_nop 0\n"
      ".endr\n"
      ".Lwg_end:\n"
      : "+{v0}"(tid)
      : "{s[0:1]}"(__builtin_amdgcn_kernarg_segment_ptr()), "{s2}"(blockIdx.x)
      : "v1", "v2", "v3", "v4", "v5", "v6", "v7", "v8", "v9", "v10", "v11", "v12", "v13", "v14", "v15", "v16", "v17", "v18", "v19", "v20", "v21", "v22", "v23", "v24", "v25", "v26", "v27", "v28", "v29", "v30", "v31", "v32", "v33", "v34", "v35", "v36", "v37", "v38", "v39", "v40", "v41", "v42", "v43", "v44", "v45", "v46", "v47", "v48", "v49", "v50", "v51", "v52", "v53", "v54", "v55", "v56", "v57", "v58", "v59", "v60", "v61", "v62", "v63", "v64", "v65", "v66", "v67", "v68", "v69", "v70", "v71", "v72", "v73", "v74", "v75", "v76", "v77", "v78", "v79", "v80", "v81", "v82", "v83", "v84", "v85", "v86", "v87", "v88", "v89", "v90", "v91", "v92", "v93", "v94", "v95", "v96", "v97", "v98", "v99", "v100", "v101", "v102", "v103", "v104", "v105", "v106", "v107", "v108", "v109", "v110", "v111", "v112", "v113", "v114", "v115", "v116", "v117", "v118", "v119", "v120", "v121", "v122", "v123", "v124", "v125", "v126", "v127", "v128", "v129", "v130", "v131", "v132", "v133", "v134", "v135", "v136", "v137", "v138", "v139", "v140", "v141", "v142", "v143", "v144", "v145", "v146", "v147", "v148", "v149", "v150", "v151", "v152", "v153", "v154", "v155", "v156", "v157", "v158", "v159", "v160", "v161", "v162", "v163", "v164", "v165", "v166", "v167", "v168", "v169", "v170", "v171", "v172", "v173", "v174", "v175", "v176", "v177", "v178", "v179", "v180", "v181", "v182", "v183", "v184", "v185", "v186", "v187", "v188", "v189", "v190", "v191", "v192", "v193", "v194", "v195", "v196", "v197", "v198", "v199", "v200", "v201", "v202", "v203", "v204", "v205", "v206", "v207", "v208", "v209", "v210", "v211", "v212", "v213", "v214", "v215", "v216", "v217", "v218", "v219", "v220", "v221", "v222", "v223", "v224", "v225", "v226", "v227", "v228", "v229", "v230", "v231", "v232", "v233", "v234", "v235", "v236", "v237", "v238", "v239", "v240", "v241", "v242", "v243", "v244", "v245", "v246", "v247", "v248", "v249", "v250", "v251", "v252", "v253", "v254", "v255", "a0", "a1", "a2", "a3", "a4", "a5", "a6", "a7", "a8", "a9", "a10", "a11", "a12", "a13", "a14", "a15", "a16", "a17", "a18", "a19", "a20", "a21", "a22", "a23", "a24", "a25", "a26", "a27", "a28", "a29", "a30", "a31", "a32", "a33", "a34", "a35", "a36", "a37", "a38", "a39", "a40", "a41", "a42", "a43", "a44", "a45", "a46", "a47", "a48", "a49", "a50", "a51", "a52", "a53", "a54", "a55", "a56", "a57", "a58", "a59", "a60", "a61", "a62", "a63", "a64", "a65", "a66", "a67", "a68", "a69", "a70", "a71", "a72", "a73", "a74", "a75", "a76", "a77", "a78", "a79", "a80", "a81", "a82", "a83", "a84", "a85", "a86", "a87", "a88", "a89", "a90", "a91", "a92", "a93", "a94", "a95", "a96", "a97", "a98", "a99", "a100", "a101", "a102", "a103", "a104", "a105", "a106", "a107", "a108", "a109", "a110", "a111", "a112", "a113", "a114", "a115", "a116", "a117", "a118", "a119", "a120", "a121", "a122", "a123", "a124", "a125", "a126", "a127", "a128", "a129", "a130", "a131", "a132", "a133", "a134", "a135", "a136", "a137", "a138", "a139", "a140", "a141", "a142", "a143", "a144", "a145", "a146", "a147", "a148", "a149", "a150", "a151", "a152", "a153", "a154", "a155", "a156", "a157", "a158", "a159", "a160", "a161", "a162", "a163", "a164", "a165", "a166", "a167", "a168", "a169", "a170", "a171", "a172", "a173", "a174", "a175", "a176", "a177", "a178", "a179", "a180", "a181", "a182", "a183", "a184", "a185", "a186", "a187", "a188", "a189", "a190", "a191", "a192", "a193", "a194", "a195", "a196", "a197", "a198", "a199", "a200", "a201", "a202", "a203", "a204", "a205", "a206", "a207", "a208", "a209", "a210", "a211", "a212", "a213", "a214", "a215", "a216", "a217", "a218", "a219", "a220", "a221", "a222", "a223", "a224", "a225", "a226", "a227", "a228", "a229", "a230", "a231", "a232", "a233", "a234", "a235", "a236", "a237", "a238", "a239", "a240", "a241", "a242", "a243", "a244", "a245", "a246", "a247", "a248", "a249", "a250", "a251", "a252", "a253", "a254", "a255", "s3", "s4", "s5", "s6", "s7", "s8", "s9", "s10", "s11", "s12", "s13", "s14", "s15", "s16", "s17", "s18", "s19", "s20", "s21", "s22", "s23", "s24", "s25", "s26", "s27", "s28", "s29", "s30", "s31", "s33", "s34", "s35", "s36", "s37", "s38", "s39", "s40", "s41", "s42", "s43", "s44", "s45", "s46", "s47", "s48", "s49", "s50", "s51", "s52", "s53", "s54", "s55", "s56", "s57", "s58", "s59", "s60", "s61", "s62", "s63", "s64", "s65", "s66", "s67", "s68", "s69", "s70", "s71", "s72", "s73", "s74", "s75", "s76", "s77", "s78", "s79", "s80", "s81", "s82", "s83", "s84", "s85", "s86", "s87", "s88", "s89", "s90", "s91", "s92", "s93", "s94", "s95", "s96", "s97", "s98", "s99", "scc", "vcc", "memory");
  asm volatile("" :: "v"((unsigned)(uintptr_t)smem) : "memory");
}

extern "C" void kernel_launch(void* const* d_in, const int* in_sizes, int n_in, void* d_out,
                              int out_size, void* d_ws, size_t ws_size, hipStream_t stream) {
  conv_kernel<<<256, 256, 0, stream>>>((const char*)d_in[0], (const char*)d_in[1], (char*)d_out, (char*)d_ws);
}
```

```cpp
#include <hip/hip_runtime.h>
#include <hip/hip_bf16.h>
#include <stdint.h>

__global__ void __launch_bounds__(256, 1)
conv_kernel(const char* __restrict__ x, const char* __restrict__ w, char* __restrict__ out, char* __restrict__ dbg) {
  __shared__ __attribute__((aligned(1024))) char smem[163840];
  unsigned tid = threadIdx.x;
  asm volatile(
      "s_load_dwordx2 s[12:13], s[0:1], 8\n"
      "s_load_dwordx2 s[4:5], s[0:1], 0\n"
      "s_load_dwordx2 s[8:9], s[0:1], 16\n"
      "v_readfirstlane_b32 s16, v0\n"
      "s_getpc_b64 s[26:27]\n"
      ".Lwg_pc:\n"
      "v_and_b32 v37, 3, v0\n"
      "s_lshr_b32 s23, s2, 3\n"
      "s_lshl_b32 s28, s16, 3\n"
      "v_lshlrev_b32 v40, 7, v37\n"
      "s_lshl_b32 s29, s23, 11\n"
      "s_mov_b32 m0, 150016\n"
      "s_add_u32 s28, s28, s29\n"
      "v_and_b32 v36, 63, v0\n"
      "v_add_u32 v40, s28, v40\n"
      "s_lshr_b32 s16, s16, 6\n"
      "v_min_u32 v40, .Lwg_end-.Lwg_pc-4, v40\n"
      "s_and_b32 s18, s16, 1\n"
      "global_load_lds_dword v40, s[26:27]\n"
      "v_lshlrev_b32 v92, 4, v36\n"
      "s_lshr_b32 s17, s16, 1\n"
      "s_and_b32 s28, s2, 7\n"
      "s_mul_i32 s19, s28, 6291456\n"
      "s_and_b32 s28, s23, 1\n"
      "s_mul_i32 s20, s28, 98304\n"
      "s_lshr_b32 s28, s23, 1\n"
      "s_mul_i32 s29, s28, 6144\n"
      "s_add_u32 s21, s20, s29\n"
      "s_add_u32 s6, s19, 6492160\n"
      "s_min_u32 s6, s6, 50335744\n"
      "s_mov_b32 s7, 0x20000\n"
      "s_mov_b32 s10, 50331648\n"
      "s_mov_b32 s11, 0x20000\n"
      "s_mov_b32 s14, 165888\n"
      "s_mov_b32 s15, 0x20000\n"
      "s_mul_i32 s63, s18, 3072\n"
      "s_add_u32 s54, s19, s20\n"
      "s_sub_u32 s54, s54, 196608\n"
      "s_lshl_b32 s64, s18, 8\n"
      "s_mul_i32 s28, s17, 24832\n"
      "s_add_u32 s64, s64, s28\n"
      "s_mov_b32 s55, 0\n"
      "v_bitop3_b32 v93, v36, v92, 32 bitop3:0x6c\n"
      "v_xor_b32 v94, 32, v92\n"
      "v_bfe_u32 v38, v36, 2, 2\n"
      "v_lshrrev_b32 v39, 4, v36\n"
      "s_cmp_eq_u32 s17, 1\n"
      "s_cbranch_scc1 LBD_%=\n"
      "v_lshlrev_b32 v40, 3, v92\n"
      "s_mul_i32 s53, s18, 2688\n"
      "s_mul_i32 s34, s23, 5184\n"
      "v_min_u32 v40, 2560, v40\n"
      "s_add_u32 s53, s53, s34\n"
      "v_lshlrev_b32 v43, 4, v37\n"
      "s_mul_i32 s26, s17, 0x8080\n"
      "s_sub_u32 s26, 0xbf80, s26\n"
      "s_mov_b32 s27, 0\n"
      "s_lshl_b32 s34, s17, 7\n"
      "s_mov_b32 s29, 0\n"
      "s_sub_u32 s28, s26, s34\n"
      "s_mov_b32 s31, 0\n"
      "s_sub_u32 s30, s28, s34\n"
      "v_lshlrev_b64 v[44:45], v43, s[26:27]\n"
      "v_lshlrev_b64 v[46:47], v43, s[28:29]\n"
      "s_xor_b32 s28, s28, 0x8000\n"
      "v_lshlrev_b64 v[48:49], v43, s[30:31]\n"
      "v_lshlrev_b64 v[50:51], v43, s[28:29]\n"
      "v_accvgpr_write_b32 a244, v44\n"
      "v_accvgpr_write_b32 a245, v45\n"
      "v_accvgpr_write_b32 a246, v46\n"
      "v_accvgpr_write_b32 a247, v47\n"
      "v_accvgpr_write_b32 a248, v48\n"
      "v_accvgpr_write_b32 a249, v49\n"
      "v_accvgpr_write_b32 a250, v50\n"
      "v_accvgpr_write_b32 a251, v51\n"
      "s_waitcnt lgkmcnt(0)\n"
      "s_and_b32 s13, s13, 0xffff\n"
      "buffer_load_dword v40, s[12:15], s53 offen lds\n"
      "s_add_u32 m0, s63, 30976\n"
      "s_add_u32 s52, s63, 55296\n"
      "buffer_load_dwordx4 v92, s[12:15], s52 offen offset:0 lds\n"
      "buffer_load_dwordx4 v93, s[12:15], s52 offen offset:1024 lds\n"
      "buffer_load_dwordx4 v94, s[12:15], s52 offen offset:2048 lds\n"
      "s_add_u32 m0, s63, 0\n"
      "s_add_u32 s52, s63, 110592\n"
      "buffer_load_dwordx4 v92, s[12:15], s52 offen offset:0 lds\n"
      "buffer_load_dwordx4 v93, s[12:15], s52 offen offset:1024 lds\n"
      "buffer_load_dwordx4 v94, s[12:15], s52 offen offset:2048 lds\n"
      "s_branch LS_%=\n"
      "LBD_%=:\n"
      "s_sub_u32 s26, s21, s20\n"
      "s_lshl_b32 s37, s18, 11\n"
      "s_add_u32 s26, s26, s37\n"
      "s_add_u32 s27, s26, 3712\n"
      "v_add_u32 v44, s27, v92\n"
      "s_cmp_eq_u32 s18, 1\n"
      "s_cselect_b32 s28, 16, 48\n"
      "s_cselect_b32 s29, 0, 32\n"
      "s_cselect_b32 s27, 48, 64\n"
      "v_cmp_le_u32 vcc, s28, v36\n"
      "v_cmp_gt_u32 s[30:31], s29, v36\n"
      "v_xor_b32 v45, 32, v44\n"
      "s_cmp_eq_u32 s26, 0\n"
      "s_cselect_b32 s28, 24, 0\n"
      "v_cndmask_b32 v10, v44, v45, vcc\n"
      "v_cndmask_b32 v46, v44, v45, s[30:31]\n"
      "v_cmp_le_u32 vcc, s28, v36\n"
      "v_cmp_gt_u32 s[30:31], s27, v36\n"
      "s_cmp_eq_u32 s26, 94208\n"
      "s_cselect_b32 s27, 24, 64\n"
      "v_cmp_gt_u32 s[34:35], s27, v36\n"
      "v_subrev_u32 v45, 256, v44\n"
      "v_cndmask_b32 v47, 2.0, v10, vcc\n"
      "v_cndmask_b32 v48, v45, v46, s[30:31]\n"
      "v_add_u32 v49, 3072, v10\n"
      "v_add_u32 v50, 3072, v48\n"
      "s_bitcmp0_b32 s23, 1\n"
      "s_cselect_b64 vcc, -1, 0\n"
      "v_cndmask_b32 v50, 2.0, v50, s[34:35]\n"
      "v_cndmask_b32 v8, v47, v49, vcc\n"
      "v_cndmask_b32 v9, v48, v50, vcc\n"
      "v_cndmask_b32 v10, v49, v47, vcc\n"
      "v_cndmask_b32 v11, v50, v48, vcc\n"
      "s_add_u32 s34, s19, s21\n"
      "s_cmp_eq_u32 s19, 0\n"
      "s_cselect_b32 s36, 0x80000000, s54\n"
      "s_nop 0\n"
      "s_waitcnt lgkmcnt(0)\n"
      "s_load_dword s44, s[4:5], s34\n"
      "s_and_b32 s13, s13, 0xffff\n"
      "s_add_u32 m0, s63, 24832\n"
      "s_add_u32 s52, s63, 0\n"
      "buffer_load_dwordx4 v92, s[12:15], s52 offen offset:0 lds\n"
      "buffer_load_dwordx4 v93, s[12:15], s52 offen offset:1024 lds\n"
      "buffer_load_dwordx4 v94, s[12:15], s52 offen offset:2048 lds\n"
      "s_add_u32 s4, s4, -4096\n"
      "s_addc_u32 s5, s5, -1\n"
      "s_and_b32 s5, s5, 0xffff\n"
      "s_add_u32 s53, s54, 196608\n"
      "s_add_u32 m0, s37, 82432\n"
      "s_nop 0\n"
      "buffer_load_dwordx4 v8, s[4:7], s53 offen offset:0 nt lds\n"
      "buffer_load_dwordx4 v9, s[4:7], s53 offen offset:1024 nt lds\n"
      "s_add_u32 s53, s54, 393216\n"
      "s_add_u32 m0, s37, 86536\n"
      "s_nop 0\n"
      "buffer_load_dwordx4 v8, s[4:7], s53 offen offset:0 nt lds\n"
      "buffer_load_dwordx4 v9, s[4:7], s53 offen offset:1024 nt lds\n"
      "s_add_u32 s53, s54, 589824\n"
      "s_add_u32 m0, s37, 90760\n"
      "s_nop 0\n"
      "s_nop 0\n"
      "buffer_load_dwordx4 v8, s[4:7], s53 offen offset:0 nt lds\n"
      "buffer_load_dwordx4 v9, s[4:7], s53 offen offset:1024 nt lds\n"
      "s_add_u32 s53, s54, 786432\n"
      "s_add_u32 m0, s37, 95104\n"
      "s_nop 0\n"
      "s_nop 0\n"
      "buffer_load_dwordx4 v8, s[4:7], s53 offen offset:0 nt lds\n"
      "buffer_load_dwordx4 v9, s[4:7], s53 offen offset:1024 nt lds\n"
      "s_add_u32 s53, s36, 0\n"
      "s_add_u32 m0, s37, 150272\n"
      "s_nop 0\n"
      "buffer_load_dwordx4 v8, s[4:7], s53 offen offset:0 nt lds\n"
      "buffer_load_dwordx4 v9, s[4:7], s53 offen offset:1024 nt lds\n"
      "v_lshlrev_b32 v43, 4, v37\n"
      "v_lshlrev_b32 v44, 2, v37\n"
      "v_and_b32 v43, 16, v43\n"
      "v_and_or_b32 v43, v44, 8, v43\n"
      "v_mul_u32_u24 v44, 192, v38\n"
      "v_mul_u32_u24 v45, 1536, v39\n"
      "s_lshl_b32 s27, s18, 5\n"
      "v_lshlrev_b32 v46, 1, v36\n"
      "v_add3_u32 v43, v43, v44, v45\n"
      "v_bitop3_b32 v46, v46, s27, 32 bitop3:0x6c\n"
      "v_add_u32 v95, v43, v46\n"
      "v_add_u32 v96, 57344, v95\n"
      "v_add_u32 v97, 114688, v95\n"
      "v_lshlrev_b32 v43, 4, v37\n"
      "s_mul_i32 s26, s17, 0x8080\n"
      "s_sub_u32 s26, 0xbf80, s26\n"
      "s_mov_b32 s27, 0\n"
      "s_lshl_b32 s34, s17, 7\n"
      "s_mov_b32 s29, 0\n"
      "s_sub_u32 s28, s26, s34\n"
      "s_mov_b32 s31, 0\n"
      "s_sub_u32 s30, s28, s34\n"
      "v_lshlrev_b64 v[44:45], v43, s[26:27]\n"
      "v_lshlrev_b64 v[46:47], v43, s[28:29]\n"
      "s_xor_b32 s28, s28, 0x8000\n"
      "v_lshlrev_b64 v[48:49], v43, s[30:31]\n"
      "v_lshlrev_b64 v[50:51], v43, s[28:29]\n"
      "v_accvgpr_write_b32 a244, v44\n"
      "v_accvgpr_write_b32 a245, v45\n"
      "v_accvgpr_write_b32 a246, v46\n"
      "v_accvgpr_write_b32 a247, v47\n"
      "v_accvgpr_write_b32 a248, v48\n"
      "v_accvgpr_write_b32 a249, v49\n"
      "v_accvgpr_write_b32 a250, v50\n"
      "v_accvgpr_write_b32 a251, v51\n"
      "s_lshl_b32 s26, s18, 3\n"
      "v_and_b32 v42, 15, v36\n"
      "v_xor_b32 v42, s26, v42\n"
      "s_lshl_b32 s26, s17, 13\n"
      "s_add_u32 s26, s26, 24832\n"
      "s_mul_i32 s27, s17, 12672\n"
      "v_mul_u32_u24 v44, 192, v42\n"
      "v_lshlrev_b32 v43, 3, v42\n"
      "v_and_b32 v45, 48, v36\n"
      "s_add_u32 s27, s27, 82432\n"
      "v_bitop3_b32 v46, v43, v45, 32 bitop3:0x6c\n"
      "v_add3_u32 v0, v46, v44, s26\n"
      "v_add3_u32 v3, v46, v44, s27\n"
      "v_add_u32 v43, 16, v43\n"
      "v_add_u32 v44, 384, v44\n"
      "v_bitop3_b32 v46, v43, v45, 32 bitop3:0x6c\n"
      "v_add3_u32 v1, v46, v44, s26\n"
      "v_add3_u32 v4, v46, v44, s27\n"
      "v_add_u32 v43, 16, v43\n"
      "v_add_u32 v44, 384, v44\n"
      "v_bitop3_b32 v46, v43, v45, 32 bitop3:0x6c\n"
      "v_add3_u32 v2, v46, v44, s26\n"
      "v_add3_u32 v5, v46, v44, s27\n"
      "v_lshlrev_b32 v43, 31, v36\n"
      "v_and_b32 v44, 2, v36\n"
      "v_or_b32 v43, 0x3f803f80, v43\n"
      "v_mul_u32_u24 v45, 0xc00080, v44\n"
      "v_lshl_add_u32 v44, v44, 22, v43\n"
      "v_add_u32 v45, v45, v43\n"
      "v_accvgpr_write_b32 a216, v44\n"
      "v_accvgpr_write_b32 a217, v45\n"
      "v_lshlrev_b32 v43, 3, v38\n"
      "s_mov_b32 s26, 806420736\n"
      "v_bfe_u32 v43, s26, v43, 8\n"
      "v_lshlrev_b32 v44, 12, v38\n"
      "s_lshl_b32 s28, s18, 8\n"
      "s_add_u32 s28, s28, 82432\n"
      "v_lshl_add_u32 v43, v43, 3, v44\n"
      "v_lshl_add_u32 v44, v39, 2, v37\n"
      "v_lshl_add_u32 v43, v44, 4, v43\n"
      "v_add_u32 v7, s28, v43\n"
      "s_mul_i32 s26, s17, 70144\n"
      "s_lshl_b32 s27, s18, 8\n"
      "s_sub_u32 s26, 70144, s26\n"
      "s_add_u32 s26, s26, s27\n"
      "v_lshl_add_u32 v6, v36, 2, s26\n"
      "v_lshrrev_b32 v43, 3, v36\n"
      "v_and_b32 v44, 7, v36\n"
      "v_mul_u32_u24 v45, 384, v43\n"
      "v_lshl_add_u32 v18, v44, 4, v45\n"
      "v_and_b32 v44, 2, v36\n"
      "v_lshlrev_b32 v43, 3, v43\n"
      "v_lshlrev_b32 v45, 4, v44\n"
      "v_xor_b32 v43, v43, v45\n"
      "v_lshl_add_u32 v43, v44, 7, v43\n"
      "v_and_b32 v44, 1, v36\n"
      "s_lshl_b32 s26, s18, 11\n"
      "v_lshl_add_u32 v43, v44, 6, v43\n"
      "s_add_u32 s26, s26, 12288\n"
      "v_bfe_u32 v44, v36, 2, 1\n"
      "v_add_u32 v43, s26, v43\n"
      "v_mul_i32_i24 v45, 8196, v44\n"
      "s_mov_b32 s27, 0\n"
      "v_add3_u32 v12, v43, v45, s27\n"
      "v_add_u32 v15, 128, v12\n"
      "v_mul_i32_i24 v45, -4092, v44\n"
      "s_mov_b32 s27, 4096\n"
      "v_add3_u32 v13, v43, v45, s27\n"
      "v_add_u32 v16, 128, v13\n"
      "v_mul_i32_i24 v45, -4100, v44\n"
      "s_mov_b32 s27, 8200\n"
      "v_add3_u32 v14, v43, v45, s27\n"
      "v_add_u32 v17, 128, v14\n"
      "s_mul_i32 s62, s18, 393216\n"
      "s_add_u32 s62, s62, s19\n"
      "s_add_u32 s62, s62, s21\n"
      "s_sub_u32 s62, s62, 786432\n"
      "s_bitcmp0_b32 s23, 1\n"
      "s_cselect_b32 s26, 3072, 0\n"
      "s_movk_i32 s40, 3072\n"
      "s_cmovk_i32 s40, -3072\n"
      "s_add_u32 s62, s62, s26\n"
      "s_mov_b32 s58, 0x80000000\n"
      "s_mov_b32 s59, 0x80000000\n"
      "s_and_b32 s9, s9, 0xffff\n"
      "s_mov_b32 s45, 0xbf2aaaab\n"
      "s_mov_b32 s46, 0xbe800000\n"
      "s_mov_b32 s47, 0x3f2aaaab\n"
      "s_mov_b32 s48, 0xc0c00000\n"
      "s_mov_b32 s49, 0x3f000000\n"
      "s_mov_b32 s38, 4.0\n"
      "s_mov_b32 s39, 0xc0a00000\n"
      "s_add_u32 m0, s63, 6144\n"
      "s_add_u32 s52, s63, 6144\n"
      "buffer_load_dwordx4 v92, s[12:15], s52 offen offset:0 lds\n"
      "buffer_load_dwordx4 v93, s[12:15], s52 offen offset:1024 lds\n"
      "buffer_load_dwordx4 v94, s[12:15], s52 offen offset:2048 lds\n"
      "LDpro_%=:\n"
      "s_waitcnt vmcnt(13)\n"
      "s_barrier\n"
      "ds_read_b64_tr_b16 a[0:1], v95 offset:24832\n"
      "ds_read_b64_tr_b16 a[2:3], v95 offset:25600\n"
      "ds_read_b64_tr_b16 a[224:225], v95 offset:0\n"
      "ds_read_b64_tr_b16 a[226:227], v95 offset:768\n"
      "ds_read_b64_tr_b16 a[220:221], v95 offset:30976\n"
      "ds_read_b64_tr_b16 a[222:223], v95 offset:31744\n"
      "ds_read_b64_tr_b16 a[4:5], v95 offset:24896\n"
      "ds_read_b64_tr_b16 a[6:7], v95 offset:25664\n"
      "ds_read_b64_tr_b16 a[232:233], v95 offset:64\n"
      "ds_read_b64_tr_b16 a[234:235], v95 offset:832\n"
      "ds_read_b64_tr_b16 a[228:229], v95 offset:31040\n"
      "ds_read_b64_tr_b16 a[230:231], v95 offset:31808\n"
      "s_add_u32 m0, s63, 53504\n"
      "s_add_u32 s52, s63, 122880\n"
      "s_waitcnt lgkmcnt(10)\n"
      "v_mfma_f32_4x4x4_16b_bf16 v[28:31], a[244:245], a[0:1], 0\n"
      "v_mfma_f32_4x4x4_16b_bf16 v[32:35], a[244:245], a[2:3], 0\n"
      "buffer_load_dwordx4 v92, s[12:15], s52 offen offset:0 lds\n"
      "s_waitcnt lgkmcnt(8)\n"
      "v_mfma_f32_4x4x4_16b_bf16 v[28:31], a[248:249], a[224:225], v[28:31]\n"
      "ds_read_b64_tr_b16 a[8:9], v95 offset:24960\n"
      "v_mfma_f32_4x4x4_16b_bf16 v[32:35], a[248:249], a[226:227], v[32:35]\n"
      "buffer_load_dwordx4 v93, s[12:15], s52 offen offset:1024 lds\n"
      "s_nop 0\n"
      "s_waitcnt lgkmcnt(7)\n"
      "v_mfma_f32_4x4x4_16b_bf16 v[36:39], a[246:247], a[220:221], v[28:31]\n"
      "ds_read_b64_tr_b16 a[10:11], v95 offset:25728\n"
      "v_mfma_f32_4x4x4_16b_bf16 v[40:43], a[246:247], a[222:223], v[32:35]\n"
      "buffer_load_dwordx4 v94, s[12:15], s52 offen offset:2048 lds\n"
      "v_mfma_f32_4x4x4_16b_bf16 v[28:31], a[250:251], a[220:221], v[28:31]\n"
      "ds_read_b64_tr_b16 a[240:241], v95 offset:128\n"
      "v_mfma_f32_4x4x4_16b_bf16 v[32:35], a[250:251], a[222:223], v[32:35]\n"
      "ds_read_b64_tr_b16 a[242:243], v95 offset:896\n"
      "ds_read_b64_tr_b16 a[236:237], v95 offset:31104\n"
      "ds_read_b64_tr_b16 a[238:239], v95 offset:31872\n"
      "s_add_u32 m0, s63, 71936\n"
      "s_add_u32 s52, s63, 129024\n"
      "s_nop 0\n"
      "s_waitcnt lgkmcnt(5)\n"
      "v_mfma_f32_4x4x4_16b_bf16 v[76:79], a[244:245], a[4:5], 0\n"
      "v_mfma_f32_4x4x4_16b_bf16 v[80:83], a[244:245], a[6:7], 0\n"
      "buffer_load_dwordx4 v92, s[12:15], s52 offen offset:0 lds\n"
      "v_mfma_f32_4x4x4_16b_bf16 v[76:79], a[248:249], a[232:233], v[76:79]\n"
      "buffer_load_dwordx4 v93, s[12:15], s52 offen offset:1024 lds\n"
      "v_mfma_f32_4x4x4_16b_bf16 v[80:83], a[248:249], a[234:235], v[80:83]\n"
      "buffer_load_dwordx4 v94, s[12:15], s52 offen offset:2048 lds\n"
      "v_mfma_f32_4x4x4_16b_bf16 v[84:87], a[246:247], a[228:229], v[76:79]\n"
      "v_mfma_f32_4x4x4_16b_bf16 v[88:91], a[246:247], a[230:231], v[80:83]\n"
      "v_mfma_f32_4x4x4_16b_bf16 v[76:79], a[250:251], a[228:229], v[76:79]\n"
      "v_mfma_f32_4x4x4_16b_bf16 v[80:83], a[250:251], a[230:231], v[80:83]\n"
      "v_cvt_pk_bf16_f32 v252, v36, v40\n"
      "v_cvt_pk_bf16_f32 v253, v37, v41\n"
      "v_cvt_pk_bf16_f32 v254, v38, v42\n"
      "v_cvt_pk_bf16_f32 v255, v39, v43\n"
      "v_cvt_pk_bf16_f32 v248, v28, v32\n"
      "v_cvt_pk_bf16_f32 v249, v29, v33\n"
      "v_cvt_pk_bf16_f32 v250, v30, v34\n"
      "v_cvt_pk_bf16_f32 v251, v31, v35\n"
      "s_waitcnt lgkmcnt(0)\n"
      "v_mfma_f32_4x4x4_16b_bf16 v[28:31], a[244:245], a[8:9], 0\n"
      "v_mfma_f32_4x4x4_16b_bf16 v[32:35], a[244:245], a[10:11], 0\n"
      "s_nop 0\n"
      "v_mfma_f32_4x4x4_16b_bf16 v[28:31], a[248:249], a[240:241], v[28:31]\n"
      "v_mfma_f32_4x4x4_16b_bf16 v[32:35], a[248:249], a[242:243], v[32:35]\n"
      "s_nop 0\n"
      "s_nop 0\n"
      "v_mfma_f32_4x4x4_16b_bf16 v[36:39], a[246:247], a[236:237], v[28:31]\n"
      "v_mfma_f32_4x4x4_16b_bf16 v[40:43], a[246:247], a[238:239], v[32:35]\n"
      "v_mfma_f32_4x4x4_16b_bf16 v[28:31], a[250:251], a[236:237], v[28:31]\n"
      "v_mfma_f32_4x4x4_16b_bf16 v[32:35], a[250:251], a[238:239], v[32:35]\n"
      "v_cvt_pk_bf16_f32 v244, v84, v88\n"
      "v_cvt_pk_bf16_f32 v245, v85, v89\n"
      "v_cvt_pk_bf16_f32 v246, v86, v90\n"
      "v_cvt_pk_bf16_f32 v247, v87, v91\n"
      "v_cvt_pk_bf16_f32 v240, v76, v80\n"
      "v_cvt_pk_bf16_f32 v241, v77, v81\n"
      "v_cvt_pk_bf16_f32 v242, v78, v82\n"
      "v_cvt_pk_bf16_f32 v243, v79, v83\n"
      "s_waitcnt vmcnt(6)\n"
      "s_barrier\n"
      "ds_read_b64_tr_b16 a[12:13], v95 offset:6144\n"
      "ds_read_b64_tr_b16 a[14:15], v95 offset:6912\n"
      "ds_read_b64_tr_b16 a[224:225], v95 offset:18432\n"
      "ds_read_b64_tr_b16 a[226:227], v95 offset:19200\n"
      "ds_read_b64_tr_b16 a[220:221], v95 offset:12288\n"
      "ds_read_b64_tr_b16 a[222:223], v95 offset:13056\n"
      "s_add_u32 m0, s63, 128512\n"
      "s_add_u32 s52, s63, 135168\n"
      "s_nop 0\n"
      "s_waitcnt lgkmcnt(4)\n"
      "v_mfma_f32_4x4x4_16b_bf16 v[76:79], a[244:245], a[12:13], 0\n"
      "v_mfma_f32_4x4x4_16b_bf16 v[80:83], a[244:245], a[14:15], 0\n"
      "buffer_load_dwordx4 v92, s[12:15], s52 offen offset:0 lds\n"
      "s_waitcnt lgkmcnt(2)\n"
      "v_mfma_f32_4x4x4_16b_bf16 v[76:79], a[248:249], a[224:225], v[76:79]\n"
      "ds_read_b64_tr_b16 a[16:17], v95 offset:6208\n"
      "v_mfma_f32_4x4x4_16b_bf16 v[80:83], a[248:249], a[226:227], v[80:83]\n"
      "buffer_load_dwordx4 v93, s[12:15], s52 offen offset:1024 lds\n"
      "s_waitcnt lgkmcnt(1)\n"
      "v_mfma_f32_4x4x4_16b_bf16 v[84:87], a[246:247], a[220:221], v[76:79]\n"
      "ds_read_b64_tr_b16 a[18:19], v95 offset:6976\n"
      "v_mfma_f32_4x4x4_16b_bf16 v[88:91], a[246:247], a[222:223], v[80:83]\n"
      "buffer_load_dwordx4 v94, s[12:15], s52 offen offset:2048 lds\n"
      "v_mfma_f32_4x4x4_16b_bf16 v[76:79], a[250:251], a[220:221], v[76:79]\n"
      "ds_read_b64_tr_b16 a[232:233], v95 offset:18496\n"
      "v_mfma_f32_4x4x4_16b_bf16 v[80:83], a[250:251], a[222:223], v[80:83]\n"
      "ds_read_b64_tr_b16 a[234:235], v95 offset:19264\n"
      "ds_read_b64_tr_b16 a[228:229], v95 offset:12352\n"
      "ds_read_b64_tr_b16 a[230:231], v95 offset:13120\n"
      "ds_read_b64_tr_b16 a[20:21], v95 offset:6272\n"
      "ds_read_b64_tr_b16 a[22:23], v95 offset:7040\n"
      "ds_read_b64_tr_b16 a[240:241], v95 offset:18560\n"
      "ds_read_b64_tr_b16 a[242:243], v95 offset:19328\n"
      "ds_read_b64_tr_b16 a[236:237], v95 offset:12416\n"
      "ds_read_b64_tr_b16 a[238:239], v95 offset:13184\n"
      "ds_read_b64_tr_b16 a[188:189], v7 offset:2048\n"
      "ds_read_b64_tr_b16 a[190:191], v7 offset:2056\n"
      "ds_read_b128 a[192:195], v3 offset:55168\n"
      "v_cvt_pk_bf16_f32 v236, v36, v40\n"
      "v_cvt_pk_bf16_f32 v237, v37, v41\n"
      "v_cvt_pk_bf16_f32 v238, v38, v42\n"
      "v_cvt_pk_bf16_f32 v239, v39, v43\n"
      "v_cvt_pk_bf16_f32 v232, v28, v32\n"
      "v_cvt_pk_bf16_f32 v233, v29, v33\n"
      "v_cvt_pk_bf16_f32 v234, v30, v34\n"
      "v_cvt_pk_bf16_f32 v235, v31, v35\n"
      "s_waitcnt vmcnt(6)\n"
      "s_barrier\n"
      "s_nop 0\n"
      "s_waitcnt lgkmcnt(8)\n"
      "v_mfma_f32_4x4x4_16b_bf16 v[28:31], a[244:245], a[16:17], 0\n"
      "v_mfma_f32_4x4x4_16b_bf16 v[32:35], a[244:245], a[18:19], 0\n"
      "ds_read_b64_tr_b16 a[24:25], v95 offset:41216\n"
      "v_mfma_f32_4x4x4_16b_bf16 v[28:31], a[248:249], a[232:233], v[28:31]\n"
      "ds_read_b64_tr_b16 a[26:27], v95 offset:41984\n"
      "v_mfma_f32_4x4x4_16b_bf16 v[32:35], a[248:249], a[234:235], v[32:35]\n"
      "ds_read_b64_tr_b16 a[224:225], v95 offset:53504\n"
      "v_mfma_f32_4x4x4_16b_bf16 v[36:39], a[246:247], a[228:229], v[28:31]\n"
      "ds_read_b64_tr_b16 a[226:227], v95 offset:54272\n"
      "v_mfma_f32_4x4x4_16b_bf16 v[40:43], a[246:247], a[230:231], v[32:35]\n"
      "ds_read_b64_tr_b16 a[220:221], v95 offset:47360\n"
      "v_mfma_f32_4x4x4_16b_bf16 v[28:31], a[250:251], a[228:229], v[28:31]\n"
      "ds_read_b64_tr_b16 a[222:223], v95 offset:48128\n"
      "v_mfma_f32_4x4x4_16b_bf16 v[32:35], a[250:251], a[230:231], v[32:35]\n"
      "ds_read_b64_tr_b16 a[184:185], v7 offset:2560\n"
      "s_waitcnt lgkmcnt(7)\n"
      "v_mfma_f32_4x4x4_16b_bf16 v[44:47], a[216:217], a[188:189], 0\n"
      "ds_read_b64_tr_b16 a[186:187], v7 offset:2568\n"
      "v_mfma_f32_4x4x4_16b_bf16 v[48:51], a[216:217], a[190:191], 0\n"
      "ds_read_b128 a[196:199], v3 offset:55232\n"
      "v_cvt_pk_bf16_f32 v228, v84, v88\n"
      "v_cvt_pk_bf16_f32 v229, v85, v89\n"
      "v_cvt_pk_bf16_f32 v230, v86, v90\n"
      "v_cvt_pk_bf16_f32 v231, v87, v91\n"
      "v_cvt_pk_bf16_f32 v224, v76, v80\n"
      "v_cvt_pk_bf16_f32 v225, v77, v81\n"
      "v_cvt_pk_bf16_f32 v226, v78, v82\n"
      "v_cvt_pk_bf16_f32 v227, v79, v83\n"
      "s_add_u32 m0, s63, 0\n"
      "s_add_u32 s52, s63, 141312\n"
      "v_mfma_f32_4x4x4_16b_bf16 v[76:79], a[244:245], a[20:21], 0\n"
      "v_mfma_f32_4x4x4_16b_bf16 v[80:83], a[244:245], a[22:23], 0\n"
      "buffer_load_dwordx4 v92, s[12:15], s52 offen offset:0 lds\n"
      "v_mfma_f32_4x4x4_16b_bf16 v[76:79], a[248:249], a[240:241], v[76:79]\n"
      "ds_read_b64_tr_b16 a[28:29], v95 offset:41280\n"
      "v_mfma_f32_4x4x4_16b_bf16 v[80:83], a[248:249], a[242:243], v[80:83]\n"
      "buffer_load_dwordx4 v93, s[12:15], s52 offen offset:1024 lds\n"
      "v_mfma_f32_4x4x4_16b_bf16 v[84:87], a[246:247], a[236:237], v[76:79]\n"
      "ds_read_b64_tr_b16 a[30:31], v95 offset:42048\n"
      "v_mfma_f32_4x4x4_16b_bf16 v[88:91], a[246:247], a[238:239], v[80:83]\n"
      "buffer_load_dwordx4 v94, s[12:15], s52 offen offset:2048 lds\n"
      "v_mfma_f32_4x4x4_16b_bf16 v[76:79], a[250:251], a[236:237], v[76:79]\n"
      "ds_read_b64_tr_b16 a[232:233], v95 offset:53568\n"
      "v_mfma_f32_4x4x4_16b_bf16 v[80:83], a[250:251], a[238:239], v[80:83]\n"
      "s_add_u32 s53, s54, 196608\n"
      "s_nop 0\n"
      "s_waitcnt lgkmcnt(3)\n"
      "v_mfma_f32_4x4x4_16b_bf16 v[112:115], a[216:217], a[184:185], 0\n"
      "ds_read_b64_tr_b16 a[234:235], v95 offset:54336\n"
      "v_mfma_f32_4x4x4_16b_bf16 v[116:119], a[216:217], a[186:187], 0\n"
      "s_add_u32 m0, s37, 99328\n"
      "ds_read_b64_tr_b16 a[228:229], v95 offset:47424\n"
      "buffer_load_dwordx4 v10, s[4:7], s53 offen offset:0 nt lds\n"
      "buffer_load_dwordx4 v11, s[4:7], s53 offen offset:1024 nt lds\n"
      "ds_read_b64_tr_b16 a[230:231], v95 offset:48192\n"
      "s_add_u32 s53, s54, 393216\n"
      "ds_read_b64_tr_b16 a[180:181], v7 offset:3072\n"
      "s_add_u32 m0, s37, 103432\n"
      "ds_read_b64_tr_b16 a[182:183], v7 offset:3080\n"
      "buffer_load_dwordx4 v10, s[4:7], s53 offen offset:0 nt lds\n"
      "buffer_load_dwordx4 v11, s[4:7], s53 offen offset:1024 nt lds\n"
      "v_cvt_pk_bf16_f32 v220, v36, v40\n"
      "v_cvt_pk_bf16_f32 v221, v37, v41\n"
      "v_cvt_pk_bf16_f32 v222, v38, v42\n"
      "v_cvt_pk_bf16_f32 v223, v39, v43\n"
      "v_cvt_pk_bf16_f32 v216, v28, v32\n"
      "v_cvt_pk_bf16_f32 v217, v29, v33\n"
      "v_mfma_f32_16x16x32_bf16 v[100:103], a[0:3], a[192:195], 0\n"
      "v_cvt_pk_bf16_f32 v218, v30, v34\n"
      "v_cvt_pk_bf16_f32 v219, v31, v35\n"
      "v_mfma_f32_16x16x32_bf16 v[104:107], a[4:7], a[192:195], 0\n"
      "s_add_u32 m0, s37, 107656\n"
      "v_cvt_pk_bf16_f32 v44, v44, v48\n"
      "v_cvt_pk_bf16_f32 v45, v45, v49\n"
      "v_mfma_f32_16x16x32_bf16 v[108:111], a[8:11], a[192:195], 0\n"
      "s_add_u32 s53, s54, 589824\n"
      "v_cvt_pk_bf16_f32 v46, v46, v50\n"
      "v_cvt_pk_bf16_f32 v47, v47, v51\n"
      "v_mfma_f32_4x4x4_16b_bf16 v[28:31], a[244:245], a[24:25], 0\n"
      "v_mfma_f32_4x4x4_16b_bf16 v[32:35], a[244:245], a[26:27], 0\n"
      "buffer_load_dwordx4 v10, s[4:7], s53 offen offset:0 nt lds\n"
      "buffer_load_dwordx4 v11, s[4:7], s53 offen offset:1024 nt lds\n"
      "v_mfma_f32_4x4x4_16b_bf16 v[28:31], a[248:249], a[224:225], v[28:31]\n"
      "ds_read_b64_tr_b16 a[32:33], v95 offset:41344\n"
      "v_mfma_f32_4x4x4_16b_bf16 v[32:35], a[248:249], a[226:227], v[32:35]\n"
      "s_add_u32 s53, s54, 786432\n"
      "v_mfma_f32_4x4x4_16b_bf16 v[36:39], a[246:247], a[220:221], v[28:31]\n"
      "ds_read_b64_tr_b16 a[34:35], v95 offset:42112\n"
      "v_mfma_f32_4x4x4_16b_bf16 v[40:43], a[246:247], a[222:223], v[32:35]\n"
      "s_add_u32 m0, s37, 112000\n"
      "v_mfma_f32_4x4x4_16b_bf16 v[28:31], a[250:251], a[220:221], v[28:31]\n"
      "ds_read_b64_tr_b16 a[240:241], v95 offset:53632\n"
      "v_mfma_f32_4x4x4_16b_bf16 v[32:35], a[250:251], a[222:223], v[32:35]\n"
      "buffer_load_dwordx4 v10, s[4:7], s53 offen offset:0 nt lds\n"
      "buffer_load_dwordx4 v11, s[4:7], s53 offen offset:1024 nt lds\n"
      "s_waitcnt lgkmcnt(3)\n"
      "v_mfma_f32_4x4x4_16b_bf16 v[20:23], a[216:217], a[180:181], 0\n"
      "ds_read_b64_tr_b16 a[242:243], v95 offset:54400\n"
      "v_mfma_f32_4x4x4_16b_bf16 v[24:27], a[216:217], a[182:183], 0\n"
      "s_add_u32 s53, s36, 0\n"
      "ds_read_b64_tr_b16 a[236:237], v95 offset:47488\n"
      "s_add_u32 m0, s37, 154368\n"
      "ds_read_b64_tr_b16 a[238:239], v95 offset:48256\n"
      "buffer_load_dwordx4 v10, s[4:7], s53 offen offset:0 nt lds\n"
      "buffer_load_dwordx4 v11, s[4:7], s53 offen offset:1024 nt lds\n"
      "ds_read_b64_tr_b16 a[176:177], v7 offset:3584\n"
      "ds_read_b64_tr_b16 a[178:179], v7 offset:3592\n"
      "s_mov_b32 m0, s64\n"
      "s_nop 0\n"
      "ds_write_addtid_b32 v44 offset:2048\n"
      "ds_write_addtid_b32 v45 offset:6144\n"
      "ds_write_addtid_b32 v46 offset:10240\n"
      "ds_write_addtid_b32 v47 offset:14336\n"
      "v_cvt_pk_bf16_f32 v212, v84, v88\n"
      "v_cvt_pk_bf16_f32 v213, v85, v89\n"
      "v_cvt_pk_bf16_f32 v214, v86, v90\n"
      "v_cvt_pk_bf16_f32 v215, v87, v91\n"
      "v_cvt_pk_bf16_f32 v208, v76, v80\n"
      "v_cvt_pk_bf16_f32 v209, v77, v81\n"
      "v_mfma_f32_16x16x32_bf16 v[100:103], a[12:15], a[196:199], v[100:103]\n"
      "v_cvt_pk_bf16_f32 v210, v78, v82\n"
      "v_cvt_pk_bf16_f32 v211, v79, v83\n"
      "v_mfma_f32_16x16x32_bf16 v[104:107], a[16:19], a[196:199], v[104:107]\n"
      "v_cvt_pk_bf16_f32 v112, v112, v116\n"
      "v_cvt_pk_bf16_f32 v113, v113, v117\n"
      "v_mfma_f32_16x16x32_bf16 v[108:111], a[20:23], a[196:199], v[108:111]\n"
      "s_waitcnt vmcnt(16)\n"
      "v_cvt_pk_bf16_f32 v114, v114, v118\n"
      "v_cvt_pk_bf16_f32 v115, v115, v119\n"
      "s_barrier\n"
      "v_mfma_f32_4x4x4_16b_bf16 v[76:79], a[244:245], a[28:29], 0\n"
      "v_mfma_f32_4x4x4_16b_bf16 v[80:83], a[244:245], a[30:31], 0\n"
      "ds_read_b64_tr_b16 a[36:37], v96 offset:2304\n"
      "v_mfma_f32_4x4x4_16b_bf16 v[76:79], a[248:249], a[232:233], v[76:79]\n"
      "ds_read_b64_tr_b16 a[38:39], v96 offset:3072\n"
      "v_mfma_f32_4x4x4_16b_bf16 v[80:83], a[248:249], a[234:235], v[80:83]\n"
      "ds_read_b64_tr_b16 a[224:225], v96 offset:14592\n"
      "v_mfma_f32_4x4x4_16b_bf16 v[84:87], a[246:247], a[228:229], v[76:79]\n"
      "ds_read_b64_tr_b16 a[226:227], v96 offset:15360\n"
      "v_mfma_f32_4x4x4_16b_bf16 v[88:91], a[246:247], a[230:231], v[80:83]\n"
      "ds_read_b64_tr_b16 a[220:221], v96 offset:8448\n"
      "v_mfma_f32_4x4x4_16b_bf16 v[76:79], a[250:251], a[228:229], v[76:79]\n"
      "ds_read_b64_tr_b16 a[222:223], v96 offset:9216\n"
      "v_mfma_f32_4x4x4_16b_bf16 v[80:83], a[250:251], a[230:231], v[80:83]\n"
      "ds_write_addtid_b32 v112 offset:2560\n"
      "s_nop 0\n"
      "s_waitcnt lgkmcnt(7)\n"
      "v_mfma_f32_4x4x4_16b_bf16 v[124:127], a[216:217], a[176:177], 0\n"
      "ds_write_addtid_b32 v113 offset:6656\n"
      "v_mfma_f32_4x4x4_16b_bf16 v[128:131], a[216:217], a[178:179], 0\n"
      "ds_write_addtid_b32 v114 offset:10752\n"
      "ds_write_addtid_b32 v115 offset:14848\n"
      "ds_read_b128 a[200:203], v3 offset:55296\n"
      "v_cvt_pk_bf16_f32 v204, v36, v40\n"
      "v_cvt_pk_bf16_f32 v205, v37, v41\n"
      "v_cvt_pk_bf16_f32 v206, v38, v42\n"
      "v_cvt_pk_bf16_f32 v207, v39, v43\n"
      "v_cvt_pk_bf16_f32 v200, v28, v32\n"
      "v_cvt_pk_bf16_f32 v201, v29, v33\n"
      "v_cvt_pk_bf16_f32 v202, v30, v34\n"
      "v_cvt_pk_bf16_f32 v203, v31, v35\n"
      "v_cvt_pk_bf16_f32 v20, v20, v24\n"
      "v_cvt_pk_bf16_f32 v21, v21, v25\n"
      "v_cvt_pk_bf16_f32 v22, v22, v26\n"
      "v_cvt_pk_bf16_f32 v23, v23, v27\n"
      "v_mfma_f32_4x4x4_16b_bf16 v[28:31], a[244:245], a[32:33], 0\n"
      "v_mfma_f32_4x4x4_16b_bf16 v[32:35], a[244:245], a[34:35], 0\n"
      "ds_read_b64_tr_b16 a[40:41], v96 offset:2368\n"
      "v_mfma_f32_4x4x4_16b_bf16 v[28:31], a[248:249], a[240:241], v[28:31]\n"
      "ds_read_b64_tr_b16 a[42:43], v96 offset:3136\n"
      "v_mfma_f32_4x4x4_16b_bf16 v[32:35], a[248:249], a[242:243], v[32:35]\n"
      "ds_read_b64_tr_b16 a[232:233], v96 offset:14656\n"
      "v_mfma_f32_4x4x4_16b_bf16 v[36:39], a[246:247], a[236:237], v[28:31]\n"
      "ds_read_b64_tr_b16 a[234:235], v96 offset:15424\n"
      "v_mfma_f32_4x4x4_16b_bf16 v[40:43], a[246:247], a[238:239], v[32:35]\n"
      "ds_read_b64_tr_b16 a[228:229], v96 offset:8512\n"
      "v_mfma_f32_4x4x4_16b_bf16 v[28:31], a[250:251], a[236:237], v[28:31]\n"
      "ds_read_b64_tr_b16 a[230:231], v96 offset:9280\n"
      "v_mfma_f32_4x4x4_16b_bf16 v[32:35], a[250:251], a[238:239], v[32:35]\n"
      "ds_write_addtid_b32 v20 offset:3072\n"
      "ds_write_addtid_b32 v21 offset:7168\n"
      "ds_write_addtid_b32 v22 offset:11264\n"
      "ds_write_addtid_b32 v23 offset:15360\n"
      "v_cvt_pk_bf16_f32 v196, v84, v88\n"
      "v_cvt_pk_bf16_f32 v197, v85, v89\n"
      "v_cvt_pk_bf16_f32 v198, v86, v90\n"
      "v_cvt_pk_bf16_f32 v199, v87, v91\n"
      "v_cvt_pk_bf16_f32 v192, v76, v80\n"
      "v_cvt_pk_bf16_f32 v193, v77, v81\n"
      "v_cvt_pk_bf16_f32 v194, v78, v82\n"
      "v_cvt_pk_bf16_f32 v195, v79, v83\n"
      "v_cvt_pk_bf16_f32 v124, v124, v128\n"
      "v_cvt_pk_bf16_f32 v125, v125, v129\n"
      "v_cvt_pk_bf16_f32 v126, v126, v130\n"
      "v_cvt_pk_bf16_f32 v127, v127, v131\n"
      "s_add_u32 m0, s63, 18432\n"
      "s_add_u32 s52, s63, 147456\n"
      "s_nop 0\n"
      "s_waitcnt lgkmcnt(3)\n"
      "v_mfma_f32_4x4x4_16b_bf16 v[76:79], a[244:245], a[36:37], 0\n"
      "v_mfma_f32_4x4x4_16b_bf16 v[80:83], a[244:245], a[38:39], 0\n"
      "buffer_load_dwordx4 v92, s[12:15], s52 offen offset:0 lds\n"
      "v_mfma_f32_4x4x4_16b_bf16 v[76:79], a[248:249], a[224:225], v[76:79]\n"
      "ds_read_b64_tr_b16 a[44:45], v96 offset:2432\n"
      "v_mfma_f32_4x4x4_16b_bf16 v[80:83], a[248:249], a[226:227], v[80:83]\n"
      "buffer_load_dwordx4 v93, s[12:15], s52 offen offset:1024 lds\n"
      "v_mfma_f32_4x4x4_16b_bf16 v[84:87], a[246:247], a[220:221], v[76:79]\n"
      "ds_read_b64_tr_b16 a[46:47], v96 offset:3200\n"
      "v_mfma_f32_4x4x4_16b_bf16 v[88:91], a[246:247], a[222:223], v[80:83]\n"
      "buffer_load_dwordx4 v94, s[12:15], s52 offen offset:2048 lds\n"
      "v_mfma_f32_4x4x4_16b_bf16 v[76:79], a[250:251], a[220:221], v[76:79]\n"
      "ds_read_b64_tr_b16 a[240:241], v96 offset:14720\n"
      "v_mfma_f32_4x4x4_16b_bf16 v[80:83], a[250:251], a[222:223], v[80:83]\n"
      "ds_read_b64_tr_b16 a[242:243], v96 offset:15488\n"
      "ds_read_b64_tr_b16 a[236:237], v96 offset:8576\n"
      "ds_read_b64_tr_b16 a[238:239], v96 offset:9344\n"
      "s_mov_b32 m0, s64\n"
      "s_nop 0\n"
      "ds_write_addtid_b32 v124 offset:3584\n"
      "ds_write_addtid_b32 v125 offset:7680\n"
      "ds_write_addtid_b32 v126 offset:11776\n"
      "ds_write_addtid_b32 v127 offset:15872\n"
      "v_cvt_pk_bf16_f32 v188, v36, v40\n"
      "v_cvt_pk_bf16_f32 v189, v37, v41\n"
      "v_mfma_f32_16x16x32_bf16 v[100:103], a[24:27], a[200:203], v[100:103]\n"
      "s_nop 0\n"
      "v_cvt_pk_bf16_f32 v190, v38, v42\n"
      "v_cvt_pk_bf16_f32 v191, v39, v43\n"
      "v_mfma_f32_16x16x32_bf16 v[104:107], a[28:31], a[200:203], v[104:107]\n"
      "s_waitcnt vmcnt(16)\n"
      "v_cvt_pk_bf16_f32 v184, v28, v32\n"
      "v_cvt_pk_bf16_f32 v185, v29, v33\n"
      "v_mfma_f32_16x16x32_bf16 v[108:111], a[32:35], a[200:203], v[108:111]\n"
      "s_waitcnt lgkmcnt(0)\n"
      "v_cvt_pk_bf16_f32 v186, v30, v34\n"
      "v_cvt_pk_bf16_f32 v187, v31, v35\n"
      "s_barrier\n"
      "v_mfma_f32_4x4x4_16b_bf16 v[28:31], a[244:245], a[40:41], 0\n"
      "v_mfma_f32_4x4x4_16b_bf16 v[32:35], a[244:245], a[42:43], 0\n"
      "ds_read_b64_tr_b16 a[48:49], v97 offset:1536\n"
      "v_mfma_f32_4x4x4_16b_bf16 v[28:31], a[248:249], a[232:233], v[28:31]\n"
      "ds_read_b64_tr_b16 a[50:51], v97 offset:2304\n"
      "v_mfma_f32_4x4x4_16b_bf16 v[32:35], a[248:249], a[234:235], v[32:35]\n"
      "ds_read_b64_tr_b16 a[224:225], v97 offset:13824\n"
      "v_mfma_f32_4x4x4_16b_bf16 v[36:39], a[246:247], a[228:229], v[28:31]\n"
      "ds_read_b64_tr_b16 a[226:227], v97 offset:14592\n"
      "v_mfma_f32_4x4x4_16b_bf16 v[40:43], a[246:247], a[230:231], v[32:35]\n"
      "ds_read_b64_tr_b16 a[220:221], v97 offset:7680\n"
      "v_mfma_f32_4x4x4_16b_bf16 v[28:31], a[250:251], a[228:229], v[28:31]\n"
      "ds_read_b64_tr_b16 a[222:223], v97 offset:8448\n"
      "v_mfma_f32_4x4x4_16b_bf16 v[32:35], a[250:251], a[230:231], v[32:35]\n"
      "ds_read_b128 a[204:207], v4 offset:55168\n"
      "ds_read_b128 a[208:211], v0 offset:0\n"
      "ds_read_b128 a[212:215], v0 offset:4096\n"
      "v_cvt_pk_bf16_f32 v180, v84, v88\n"
      "v_cvt_pk_bf16_f32 v181, v85, v89\n"
      "v_cvt_pk_bf16_f32 v182, v86, v90\n"
      "v_cvt_pk_bf16_f32 v183, v87, v91\n"
      "v_cvt_pk_bf16_f32 v176, v76, v80\n"
      "v_cvt_pk_bf16_f32 v177, v77, v81\n"
      "v_cvt_pk_bf16_f32 v178, v78, v82\n"
      "v_cvt_pk_bf16_f32 v179, v79, v83\n"
      "v_mfma_f32_4x4x4_16b_bf16 v[76:79], a[244:245], a[44:45], 0\n"
      "v_mfma_f32_4x4x4_16b_bf16 v[80:83], a[244:245], a[46:47], 0\n"
      "ds_read_b64_tr_b16 a[52:53], v97 offset:1600\n"
      "v_mfma_f32_4x4x4_16b_bf16 v[76:79], a[248:249], a[240:241], v[76:79]\n"
      "ds_read_b64_tr_b16 a[54:55], v97 offset:2368\n"
      "v_mfma_f32_4x4x4_16b_bf16 v[80:83], a[248:249], a[242:243], v[80:83]\n"
      "ds_read_b64_tr_b16 a[232:233], v97 offset:13888\n"
      "v_mfma_f32_4x4x4_16b_bf16 v[84:87], a[246:247], a[236:237], v[76:79]\n"
      "ds_read_b64_tr_b16 a[234:235], v97 offset:14656\n"
      "v_mfma_f32_4x4x4_16b_bf16 v[88:91], a[246:247], a[238:239], v[80:83]\n"
      "ds_read_b64_tr_b16 a[228:229], v97 offset:7744\n"
      "v_mfma_f32_4x4x4_16b_bf16 v[76:79], a[250:251], a[236:237], v[76:79]\n"
      "ds_read_b64_tr_b16 a[230:231], v97 offset:8512\n"
      "v_mfma_f32_4x4x4_16b_bf16 v[80:83], a[250:251], a[238:239], v[80:83]\n"
      "ds_read_b128 a[192:195], v0 offset:64\n"
      "ds_read_b128 a[196:199], v0 offset:4160\n"
      "ds_read_b128 a[200:203], v0 offset:128\n"
      "v_cvt_pk_bf16_f32 v172, v36, v40\n"
      "v_cvt_pk_bf16_f32 v173, v37, v41\n"
      "v_cvt_pk_bf16_f32 v174, v38, v42\n"
      "v_cvt_pk_bf16_f32 v175, v39, v43\n"
      "v_cvt_pk_bf16_f32 v168, v28, v32\n"
      "v_cvt_pk_bf16_f32 v169, v29, v33\n"
      "v_cvt_pk_bf16_f32 v170, v30, v34\n"
      "v_cvt_pk_bf16_f32 v171, v31, v35\n"
      "s_add_u32 m0, s63, 53504\n"
      "s_add_u32 s52, s63, 153600\n"
      "s_nop 0\n"
      "s_waitcnt lgkmcnt(4)\n"
      "v_mfma_f32_4x4x4_16b_bf16 v[28:31], a[244:245], a[48:49], 0\n"
      "v_mfma_f32_4x4x4_16b_bf16 v[32:35], a[244:245], a[50:51], 0\n"
      "buffer_load_dwordx4 v92, s[12:15], s52 offen offset:0 lds\n"
      "v_mfma_f32_4x4x4_16b_bf16 v[28:31], a[248:249], a[224:225], v[28:31]\n"
      "ds_read_b64_tr_b16 a[56:57], v97 offset:1664\n"
      "v_mfma_f32_4x4x4_16b_bf16 v[32:35], a[248:249], a[226:227], v[32:35]\n"
      "buffer_load_dwordx4 v93, s[12:15], s52 offen offset:1024 lds\n"
      "v_mfma_f32_4x4x4_16b_bf16 v[36:39], a[246:247], a[220:221], v[28:31]\n"
      "ds_read_b64_tr_b16 a[58:59], v97 offset:2432\n"
      "v_mfma_f32_4x4x4_16b_bf16 v[40:43], a[246:247], a[222:223], v[32:35]\n"
      "buffer_load_dwordx4 v94, s[12:15], s52 offen offset:2048 lds\n"
      "v_mfma_f32_4x4x4_16b_bf16 v[28:31], a[250:251], a[220:221], v[28:31]\n"
      "ds_read_b64_tr_b16 a[240:241], v97 offset:13952\n"
      "v_mfma_f32_4x4x4_16b_bf16 v[32:35], a[250:251], a[222:223], v[32:35]\n"
      "ds_read_b64_tr_b16 a[242:243], v97 offset:14720\n"
      "ds_read_b64_tr_b16 a[236:237], v97 offset:7808\n"
      "ds_read_b64_tr_b16 a[238:239], v97 offset:8576\n"
      "v_mfma_f32_16x16x32_bf16 v[100:103], a[36:39], a[204:207], v[100:103]\n"
      "v_cvt_pk_bf16_f32 v164, v84, v88\n"
      "v_cvt_pk_bf16_f32 v165, v85, v89\n"
      "v_mfma_f32_16x16x32_bf16 v[104:107], a[40:43], a[204:207], v[104:107]\n"
      "v_cvt_pk_bf16_f32 v166, v86, v90\n"
      "v_cvt_pk_bf16_f32 v167, v87, v91\n"
      "v_mfma_f32_16x16x32_bf16 v[108:111], a[44:47], a[204:207], v[108:111]\n"
      "s_nop 0\n"
      "v_cvt_pk_bf16_f32 v160, v76, v80\n"
      "v_cvt_pk_bf16_f32 v161, v77, v81\n"
      "v_mfma_f32_16x16x32_bf16 v[52:55], v[252:255], a[208:211], 0\n"
      "s_waitcnt vmcnt(16)\n"
      "v_cvt_pk_bf16_f32 v162, v78, v82\n"
      "v_cvt_pk_bf16_f32 v163, v79, v83\n"
      "s_barrier\n"
      "s_add_u32 m0, s63, 71936\n"
      "s_add_u32 s52, s63, 159744\n"
      "v_mfma_f32_4x4x4_16b_bf16 v[76:79], a[244:245], a[52:53], 0\n"
      "v_mfma_f32_4x4x4_16b_bf16 v[80:83], a[244:245], a[54:55], 0\n"
      "buffer_load_dwordx4 v92, s[12:15], s52 offen offset:0 lds\n"
      "v_mfma_f32_4x4x4_16b_bf16 v[76:79], a[248:249], a[232:233], v[76:79]\n"
      "ds_read_b64_tr_b16 a[60:61], v97 offset:19968\n"
      "v_mfma_f32_4x4x4_16b_bf16 v[80:83], a[248:249], a[234:235], v[80:83]\n"
      "buffer_load_dwordx4 v93, s[12:15], s52 offen offset:1024 lds\n"
      "s_waitcnt lgkmcnt(1)\n"
      "v_mfma_f32_4x4x4_16b_bf16 v[84:87], a[246:247], a[228:229], v[76:79]\n"
      "ds_read_b64_tr_b16 a[62:63], v97 offset:20736\n"
      "v_mfma_f32_4x4x4_16b_bf16 v[88:91], a[246:247], a[230:231], v[80:83]\n"
      "buffer_load_dwordx4 v94, s[12:15], s52 offen offset:2048 lds\n"
      "v_mfma_f32_4x4x4_16b_bf16 v[76:79], a[250:251], a[228:229], v[76:79]\n"
      "ds_read_b64_tr_b16 a[224:225], v95 offset:0\n"
      "v_mfma_f32_4x4x4_16b_bf16 v[80:83], a[250:251], a[230:231], v[80:83]\n"
      "ds_read_b64_tr_b16 a[226:227], v95 offset:768\n"
      "ds_read_b64_tr_b16 a[220:221], v97 offset:26112\n"
      "ds_read_b64_tr_b16 a[222:223], v97 offset:26880\n"
      "ds_read_b128 a[204:207], v0 offset:4224\n"
      "v_mfma_f32_16x16x32_bf16 v[56:59], v[244:247], a[208:211], 0\n"
      "v_cvt_pk_bf16_f32 v156, v36, v40\n"
      "v_cvt_pk_bf16_f32 v157, v37, v41\n"
      "v_mfma_f32_16x16x32_bf16 v[60:63], v[236:239], a[208:211], 0\n"
      "v_cvt_pk_bf16_f32 v158, v38, v42\n"
      "v_cvt_pk_bf16_f32 v159, v39, v43\n"
      "v_mfma_f32_16x16x32_bf16 v[64:67], v[248:251], a[212:215], 0\n"
      "v_cvt_pk_bf16_f32 v152, v28, v32\n"
      "v_cvt_pk_bf16_f32 v153, v29, v33\n"
      "v_mfma_f32_16x16x32_bf16 v[68:71], v[240:243], a[212:215], 0\n"
      "v_cvt_pk_bf16_f32 v154, v30, v34\n"
      "v_cvt_pk_bf16_f32 v155, v31, v35\n"
      "v_mfma_f32_4x4x4_16b_bf16 v[28:31], a[244:245], a[56:57], 0\n"
      "v_mfma_f32_4x4x4_16b_bf16 v[32:35], a[244:245], a[58:59], 0\n"
      "ds_read_b64_tr_b16 a[64:65], v97 offset:20032\n"
      "v_mfma_f32_4x4x4_16b_bf16 v[28:31], a[248:249], a[240:241], v[28:31]\n"
      "ds_read_b64_tr_b16 a[66:67], v97 offset:20800\n"
      "v_mfma_f32_4x4x4_16b_bf16 v[32:35], a[248:249], a[242:243], v[32:35]\n"
      "ds_read_b64_tr_b16 a[232:233], v95 offset:64\n"
      "v_mfma_f32_4x4x4_16b_bf16 v[36:39], a[246:247], a[236:237], v[28:31]\n"
      "ds_read_b64_tr_b16 a[234:235], v95 offset:832\n"
      "v_mfma_f32_4x4x4_16b_bf16 v[40:43], a[246:247], a[238:239], v[32:35]\n"
      "ds_read_b64_tr_b16 a[228:229], v97 offset:26176\n"
      "v_mfma_f32_4x4x4_16b_bf16 v[28:31], a[250:251], a[236:237], v[28:31]\n"
      "ds_read_b64_tr_b16 a[230:231], v97 offset:26944\n"
      "v_mfma_f32_4x4x4_16b_bf16 v[32:35], a[250:251], a[238:239], v[32:35]\n"
      "ds_read_b128 a[208:211], v1 offset:0\n"
      "v_mfma_f32_16x16x32_bf16 v[72:75], v[232:235], a[212:215], 0\n"
      "v_cvt_pk_bf16_f32 v148, v84, v88\n"
      "v_cvt_pk_bf16_f32 v149, v85, v89\n"
      "v_mfma_f32_16x16x32_bf16 v[52:55], v[228:231], a[192:195], v[52:55]\n"
      "v_cvt_pk_bf16_f32 v150, v86, v90\n"
      "v_cvt_pk_bf16_f32 v151, v87, v91\n"
      "v_mfma_f32_16x16x32_bf16 v[56:59], v[220:223], a[192:195], v[56:59]\n"
      "s_nop 0\n"
      "v_cvt_pk_bf16_f32 v144, v76, v80\n"
      "v_cvt_pk_bf16_f32 v145, v77, v81\n"
      "v_mfma_f32_16x16x32_bf16 v[60:63], v[212:215], a[192:195], v[60:63]\n"
      "s_waitcnt lgkmcnt(1)\n"
      "v_cvt_pk_bf16_f32 v146, v78, v82\n"
      "v_cvt_pk_bf16_f32 v147, v79, v83\n"
      "v_mfma_f32_4x4x4_16b_bf16 v[76:79], a[244:245], a[60:61], 0\n"
      "v_mfma_f32_4x4x4_16b_bf16 v[80:83], a[244:245], a[62:63], 0\n"
      "ds_read_b64_tr_b16 a[68:69], v97 offset:20096\n"
      "v_mfma_f32_4x4x4_16b_bf16 v[76:79], a[248:249], a[224:225], v[76:79]\n"
      "ds_read_b64_tr_b16 a[70:71], v97 offset:20864\n"
      "v_mfma_f32_4x4x4_16b_bf16 v[80:83], a[248:249], a[226:227], v[80:83]\n"
      "ds_read_b64_tr_b16 a[240:241], v95 offset:128\n"
      "v_mfma_f32_4x4x4_16b_bf16 v[84:87], a[246:247], a[220:221], v[76:79]\n"
      "ds_read_b64_tr_b16 a[242:243], v95 offset:896\n"
      "v_mfma_f32_4x4x4_16b_bf16 v[88:91], a[246:247], a[222:223], v[80:83]\n"
      "ds_read_b64_tr_b16 a[236:237], v97 offset:26240\n"
      "v_mfma_f32_4x4x4_16b_bf16 v[76:79], a[250:251], a[220:221], v[76:79]\n"
      "ds_read_b64_tr_b16 a[238:239], v97 offset:27008\n"
      "v_mfma_f32_4x4x4_16b_bf16 v[80:83], a[250:251], a[222:223], v[80:83]\n"
      "ds_read_b128 a[212:215], v1 offset:4096\n"
      "ds_read_b128 a[192:195], v4 offset:55232\n"
      "v_mfma_f32_16x16x32_bf16 v[64:67], v[224:227], a[196:199], v[64:67]\n"
      "v_cvt_pk_bf16_f32 v140, v36, v40\n"
      "v_cvt_pk_bf16_f32 v141, v37, v41\n"
      "v_mfma_f32_16x16x32_bf16 v[68:71], v[216:219], a[196:199], v[68:71]\n"
      "v_cvt_pk_bf16_f32 v142, v38, v42\n"
      "v_cvt_pk_bf16_f32 v143, v39, v43\n"
      "v_mfma_f32_16x16x32_bf16 v[72:75], v[208:211], a[196:199], v[72:75]\n"
      "v_cvt_pk_bf16_f32 v136, v28, v32\n"
      "v_cvt_pk_bf16_f32 v137, v29, v33\n"
      "v_mfma_f32_16x16x32_bf16 v[52:55], v[204:207], a[200:203], v[52:55]\n"
      "s_waitcnt vmcnt(6)\n"
      "v_cvt_pk_bf16_f32 v138, v30, v34\n"
      "v_cvt_pk_bf16_f32 v139, v31, v35\n"
      "s_barrier\n"
      "s_add_u32 s53, s54, 983040\n"
      "s_add_u32 m0, s37, 116224\n"
      "v_mfma_f32_4x4x4_16b_bf16 v[28:31], a[244:245], a[64:65], 0\n"
      "v_mfma_f32_4x4x4_16b_bf16 v[32:35], a[244:245], a[66:67], 0\n"
      "buffer_load_dwordx4 v8, s[4:7], s53 offen offset:0 nt lds\n"
      "buffer_load_dwordx4 v9, s[4:7], s53 offen offset:1024 nt lds\n"
      "v_mfma_f32_4x4x4_16b_bf16 v[28:31], a[248:249], a[232:233], v[28:31]\n"
      "ds_read_b64_tr_b16 a[72:73], v95 offset:6144\n"
      "v_mfma_f32_4x4x4_16b_bf16 v[32:35], a[248:249], a[234:235], v[32:35]\n"
      "ds_read_b64_tr_b16 a[74:75], v95 offset:6912\n"
      "v_mfma_f32_4x4x4_16b_bf16 v[36:39], a[246:247], a[228:229], v[28:31]\n"
      "ds_read_b64_tr_b16 a[224:225], v95 offset:18432\n"
      "v_mfma_f32_4x4x4_16b_bf16 v[40:43], a[246:247], a[230:231], v[32:35]\n"
      "ds_read_b64_tr_b16 a[226:227], v95 offset:19200\n"
      "v_mfma_f32_4x4x4_16b_bf16 v[28:31], a[250:251], a[228:229], v[28:31]\n"
      "ds_read_b64_tr_b16 a[220:221], v95 offset:12288\n"
      "v_mfma_f32_4x4x4_16b_bf16 v[32:35], a[250:251], a[230:231], v[32:35]\n"
      "ds_read_b64_tr_b16 a[222:223], v95 offset:13056\n"
      "ds_read_b128 a[196:199], v1 offset:64\n"
      "v_mfma_f32_16x16x32_bf16 v[56:59], v[196:199], a[200:203], v[56:59]\n"
      "v_cvt_pk_bf16_f32 v132, v84, v88\n"
      "v_cvt_pk_bf16_f32 v133, v85, v89\n"
      "v_mfma_f32_16x16x32_bf16 v[60:63], v[188:191], a[200:203], v[60:63]\n"
      "v_cvt_pk_bf16_f32 v134, v86, v90\n"
      "v_cvt_pk_bf16_f32 v135, v87, v91\n"
      "v_mfma_f32_16x16x32_bf16 v[64:67], v[200:203], a[204:207], v[64:67]\n"
      "s_add_u32 m0, s37, 120328\n"
      "v_cvt_pk_bf16_f32 v128, v76, v80\n"
      "v_cvt_pk_bf16_f32 v129, v77, v81\n"
      "v_mfma_f32_16x16x32_bf16 v[68:71], v[192:195], a[204:207], v[68:71]\n"
      "s_add_u32 s53, s54, 1179648\n"
      "v_cvt_pk_bf16_f32 v130, v78, v82\n"
      "v_cvt_pk_bf16_f32 v131, v79, v83\n"
      "s_nop 0\n"
      "s_waitcnt lgkmcnt(1)\n"
      "v_mfma_f32_4x4x4_16b_bf16 v[76:79], a[244:245], a[68:69], 0\n"
      "v_mfma_f32_4x4x4_16b_bf16 v[80:83], a[244:245], a[70:71], 0\n"
      "buffer_load_dwordx4 v8, s[4:7], s53 offen offset:0 nt lds\n"
      "buffer_load_dwordx4 v9, s[4:7], s53 offen offset:1024 nt lds\n"
      "v_mfma_f32_4x4x4_16b_bf16 v[76:79], a[248:249], a[240:241], v[76:79]\n"
      "ds_read_b64_tr_b16 a[76:77], v95 offset:6208\n"
      "v_mfma_f32_4x4x4_16b_bf16 v[80:83], a[248:249], a[242:243], v[80:83]\n"
      "ds_read_b64_tr_b16 a[78:79], v95 offset:6976\n"
      "v_mfma_f32_4x4x4_16b_bf16 v[84:87], a[246:247], a[236:237], v[76:79]\n"
      "ds_read_b64_tr_b16 a[232:233], v95 offset:18496\n"
      "v_mfma_f32_4x4x4_16b_bf16 v[88:91], a[246:247], a[238:239], v[80:83]\n"
      "ds_read_b64_tr_b16 a[234:235], v95 offset:19264\n"
      "v_mfma_f32_4x4x4_16b_bf16 v[76:79], a[250:251], a[236:237], v[76:79]\n"
      "ds_read_b64_tr_b16 a[228:229], v95 offset:12352\n"
      "v_mfma_f32_4x4x4_16b_bf16 v[80:83], a[250:251], a[238:239], v[80:83]\n"
      "ds_read_b64_tr_b16 a[230:231], v95 offset:13120\n"
      "ds_read_b128 a[200:203], v1 offset:4160\n"
      "v_mfma_f32_16x16x32_bf16 v[72:75], v[184:187], a[204:207], v[72:75]\n"
      "v_cvt_pk_bf16_f32 v124, v36, v40\n"
      "v_cvt_pk_bf16_f32 v125, v37, v41\n"
      "v_mfma_f32_16x16x32_bf16 v[52:55], v[180:183], a[208:211], v[52:55]\n"
      "v_cvt_pk_bf16_f32 v126, v38, v42\n"
      "v_cvt_pk_bf16_f32 v127, v39, v43\n"
      "v_mfma_f32_16x16x32_bf16 v[56:59], v[172:175], a[208:211], v[56:59]\n"
      "v_cvt_pk_bf16_f32 v28, v28, v32\n"
      "v_cvt_pk_bf16_f32 v29, v29, v33\n"
      "v_mfma_f32_16x16x32_bf16 v[60:63], v[164:167], a[208:211], v[60:63]\n"
      "v_cvt_pk_bf16_f32 v30, v30, v34\n"
      "v_cvt_pk_bf16_f32 v31, v31, v35\n"
      "v_mfma_f32_16x16x32_bf16 v[64:67], v[176:179], a[212:215], v[64:67]\n"
      "s_add_u32 m0, s37, 124552\n"
      "v_accvgpr_write_b32 a188, v28\n"
      "v_accvgpr_write_b32 a189, v29\n"
      "v_mfma_f32_16x16x32_bf16 v[68:71], v[168:171], a[212:215], v[68:71]\n"
      "s_add_u32 s53, s54, 1376256\n"
      "v_accvgpr_write_b32 a190, v30\n"
      "v_accvgpr_write_b32 a191, v31\n"
      "v_mfma_f32_4x4x4_16b_bf16 v[28:31], a[244:245], a[72:73], 0\n"
      "v_mfma_f32_4x4x4_16b_bf16 v[32:35], a[244:245], a[74:75], 0\n"
      "buffer_load_dwordx4 v8, s[4:7], s53 offen offset:0 nt lds\n"
      "buffer_load_dwordx4 v9, s[4:7], s53 offen offset:1024 nt lds\n"
      "v_mfma_f32_4x4x4_16b_bf16 v[28:31], a[248:249], a[224:225], v[28:31]\n"
      "ds_read_b64_tr_b16 a[80:81], v95 offset:6272\n"
      "v_mfma_f32_4x4x4_16b_bf16 v[32:35], a[248:249], a[226:227], v[32:35]\n"
      "ds_read_b64_tr_b16 a[82:83], v95 offset:7040\n"
      "v_mfma_f32_4x4x4_16b_bf16 v[36:39], a[246:247], a[220:221], v[28:31]\n"
      "ds_read_b64_tr_b16 a[240:241], v95 offset:18560\n"
      "v_mfma_f32_4x4x4_16b_bf16 v[40:43], a[246:247], a[222:223], v[32:35]\n"
      "ds_read_b64_tr_b16 a[242:243], v95 offset:19328\n"
      "v_mfma_f32_4x4x4_16b_bf16 v[28:31], a[250:251], a[220:221], v[28:31]\n"
      "ds_read_b64_tr_b16 a[236:237], v95 offset:12416\n"
      "v_mfma_f32_4x4x4_16b_bf16 v[32:35], a[250:251], a[222:223], v[32:35]\n"
      "ds_read_b64_tr_b16 a[238:239], v95 offset:13184\n"
      "ds_read_b128 a[204:207], v4 offset:55296\n"
      "ds_read_b128 a[208:211], v1 offset:128\n"
      "v_cvt_pk_bf16_f32 v84, v84, v88\n"
      "v_cvt_pk_bf16_f32 v85, v85, v89\n"
      "v_cvt_pk_bf16_f32 v86, v86, v90\n"
      "v_cvt_pk_bf16_f32 v87, v87, v91\n"
      "v_mfma_f32_16x16x32_bf16 v[72:75], v[160:163], a[212:215], v[72:75]\n"
      "v_accvgpr_write_b32 a184, v84\n"
      "v_accvgpr_write_b32 a185, v85\n"
      "v_mfma_f32_16x16x32_bf16 v[100:103], a[48:51], a[192:195], v[100:103]\n"
      "v_accvgpr_write_b32 a186, v86\n"
      "v_accvgpr_write_b32 a187, v87\n"
      "v_mfma_f32_16x16x32_bf16 v[104:107], a[52:55], a[192:195], v[104:107]\n"
      "v_cvt_pk_bf16_f32 v76, v76, v80\n"
      "v_cvt_pk_bf16_f32 v77, v77, v81\n"
      "v_mfma_f32_16x16x32_bf16 v[108:111], a[56:59], a[192:195], v[108:111]\n"
      "s_waitcnt lgkmcnt(1)\n"
      "v_cvt_pk_bf16_f32 v78, v78, v82\n"
      "v_cvt_pk_bf16_f32 v79, v79, v83\n"
      "v_mfma_f32_16x16x32_bf16 v[52:55], v[156:159], a[196:199], v[52:55]\n"
      "s_nop 0\n"
      "v_accvgpr_write_b32 a180, v76\n"
      "v_accvgpr_write_b32 a181, v77\n"
      "v_mfma_f32_16x16x32_bf16 v[56:59], v[148:151], a[196:199], v[56:59]\n"
      "s_waitcnt vmcnt(9)\n"
      "v_accvgpr_write_b32 a182, v78\n"
      "v_accvgpr_write_b32 a183, v79\n"
      "s_barrier\n"
      "s_add_u32 s53, s54, 1572864\n"
      "s_add_u32 m0, s37, 128896\n"
      "v_mfma_f32_4x4x4_16b_bf16 v[76:79], a[244:245], a[76:77], 0\n"
      "v_mfma_f32_4x4x4_16b_bf16 v[80:83], a[244:245], a[78:79], 0\n"
      "buffer_load_dwordx4 v8, s[4:7], s53 offen offset:0 nt lds\n"
      "buffer_load_dwordx4 v9, s[4:7], s53 offen offset:1024 nt lds\n"
      "v_mfma_f32_4x4x4_16b_bf16 v[76:79], a[248:249], a[232:233], v[76:79]\n"
      "ds_read_b64_tr_b16 a[84:85], v95 offset:41216\n"
      "v_mfma_f32_4x4x4_16b_bf16 v[80:83], a[248:249], a[234:235], v[80:83]\n"
      "ds_read_b64_tr_b16 a[86:87], v95 offset:41984\n"
      "v_mfma_f32_4x4x4_16b_bf16 v[84:87], a[246:247], a[228:229], v[76:79]\n"
      "ds_read_b64_tr_b16 a[224:225], v95 offset:53504\n"
      "v_mfma_f32_4x4x4_16b_bf16 v[88:91], a[246:247], a[230:231], v[80:83]\n"
      "ds_read_b64_tr_b16 a[226:227], v95 offset:54272\n"
      "v_mfma_f32_4x4x4_16b_bf16 v[76:79], a[250:251], a[228:229], v[76:79]\n"
      "ds_read_b64_tr_b16 a[220:221], v95 offset:47360\n"
      "v_mfma_f32_4x4x4_16b_bf16 v[80:83], a[250:251], a[230:231], v[80:83]\n"
      "ds_read_b64_tr_b16 a[222:223], v95 offset:48128\n"
      "ds_read_b128 a[212:215], v1 offset:4224\n"
      "ds_read_b128 a[192:195], v5 offset:55168\n"
      "v_cvt_pk_bf16_f32 v36, v36, v40\n"
      "v_cvt_pk_bf16_f32 v37, v37, v41\n"
      "v_cvt_pk_bf16_f32 v38, v38, v42\n"
      "v_cvt_pk_bf16_f32 v39, v39, v43\n"
      "v_mfma_f32_16x16x32_bf16 v[60:63], v[140:143], a[196:199], v[60:63]\n"
      "v_accvgpr_write_b32 a176, v36\n"
      "v_accvgpr_write_b32 a177, v37\n"
      "v_mfma_f32_16x16x32_bf16 v[64:67], v[152:155], a[200:203], v[64:67]\n"
      "v_accvgpr_write_b32 a178, v38\n"
      "v_accvgpr_write_b32 a179, v39\n"
      "v_mfma_f32_16x16x32_bf16 v[68:71], v[144:147], a[200:203], v[68:71]\n"
      "s_nop 0\n"
      "v_cvt_pk_bf16_f32 v28, v28, v32\n"
      "v_cvt_pk_bf16_f32 v29, v29, v33\n"
      "v_mfma_f32_16x16x32_bf16 v[72:75], v[136:139], a[200:203], v[72:75]\n"
      "s_nop 0\n"
      "v_cvt_pk_bf16_f32 v30, v30, v34\n"
      "v_cvt_pk_bf16_f32 v31, v31, v35\n"
      "v_mfma_f32_16x16x32_bf16 v[100:103], a[60:63], a[204:207], v[100:103]\n"
      "s_add_u32 m0, s37, 133120\n"
      "v_accvgpr_write_b32 a172, v28\n"
      "v_accvgpr_write_b32 a173, v29\n"
      "v_mfma_f32_16x16x32_bf16 v[104:107], a[64:67], a[204:207], v[104:107]\n"
      "s_add_u32 s53, s54, 983040\n"
      "v_accvgpr_write_b32 a174, v30\n"
      "v_accvgpr_write_b32 a175, v31\n"
      "v_mfma_f32_4x4x4_16b_bf16 v[28:31], a[244:245], a[80:81], 0\n"
      "v_mfma_f32_4x4x4_16b_bf16 v[32:35], a[244:245], a[82:83], 0\n"
      "buffer_load_dwordx4 v10, s[4:7], s53 offen offset:0 nt lds\n"
      "buffer_load_dwordx4 v11, s[4:7], s53 offen offset:1024 nt lds\n"
      "v_mfma_f32_4x4x4_16b_bf16 v[28:31], a[248:249], a[240:241], v[28:31]\n"
      "ds_read_b64_tr_b16 a[88:89], v95 offset:41280\n"
      "v_mfma_f32_4x4x4_16b_bf16 v[32:35], a[248:249], a[242:243], v[32:35]\n"
      "ds_read_b64_tr_b16 a[90:91], v95 offset:42048\n"
      "v_mfma_f32_4x4x4_16b_bf16 v[36:39], a[246:247], a[236:237], v[28:31]\n"
      "ds_read_b64_tr_b16 a[232:233], v95 offset:53568\n"
      "v_mfma_f32_4x4x4_16b_bf16 v[40:43], a[246:247], a[238:239], v[32:35]\n"
      "ds_read_b64_tr_b16 a[234:235], v95 offset:54336\n"
      "v_mfma_f32_4x4x4_16b_bf16 v[28:31], a[250:251], a[236:237], v[28:31]\n"
      "ds_read_b64_tr_b16 a[228:229], v95 offset:47424\n"
      "v_mfma_f32_4x4x4_16b_bf16 v[32:35], a[250:251], a[238:239], v[32:35]\n"
      "ds_read_b64_tr_b16 a[230:231], v95 offset:48192\n"
      "ds_read_b128 a[196:199], v2 offset:0\n"
      "ds_read_b128 a[200:203], v2 offset:4096\n"
      "v_cvt_pk_bf16_f32 v84, v84, v88\n"
      "v_cvt_pk_bf16_f32 v85, v85, v89\n"
      "v_cvt_pk_bf16_f32 v86, v86, v90\n"
      "v_cvt_pk_bf16_f32 v87, v87, v91\n"
      "v_mfma_f32_16x16x32_bf16 v[108:111], a[68:71], a[204:207], v[108:111]\n"
      "s_waitcnt lgkmcnt(5)\n"
      "v_accvgpr_write_b32 a168, v84\n"
      "v_accvgpr_write_b32 a169, v85\n"
      "v_mfma_f32_16x16x32_bf16 v[52:55], v[132:135], a[208:211], v[52:55]\n"
      "v_accvgpr_write_b32 a170, v86\n"
      "v_accvgpr_write_b32 a171, v87\n"
      "v_mfma_f32_16x16x32_bf16 v[56:59], v[124:127], a[208:211], v[56:59]\n"
      "s_nop 0\n"
      "v_cvt_pk_bf16_f32 v76, v76, v80\n"
      "v_cvt_pk_bf16_f32 v77, v77, v81\n"
      "v_mfma_f32_16x16x32_bf16 v[60:63], a[184:187], a[208:211], v[60:63]\n"
      "v_cvt_pk_bf16_f32 v78, v78, v82\n"
      "v_cvt_pk_bf16_f32 v79, v79, v83\n"
      "v_mfma_f32_16x16x32_bf16 v[64:67], v[128:131], a[212:215], v[64:67]\n"
      "s_add_u32 m0, s37, 137224\n"
      "v_accvgpr_write_b32 a164, v76\n"
      "v_accvgpr_write_b32 a165, v77\n"
      "v_mfma_f32_16x16x32_bf16 v[68:71], a[188:191], a[212:215], v[68:71]\n"
      "s_add_u32 s53, s54, 1179648\n"
      "v_accvgpr_write_b32 a166, v78\n"
      "v_accvgpr_write_b32 a167, v79\n"
      "v_mfma_f32_4x4x4_16b_bf16 v[76:79], a[244:245], a[84:85], 0\n"
      "v_mfma_f32_4x4x4_16b_bf16 v[80:83], a[244:245], a[86:87], 0\n"
      "buffer_load_dwordx4 v10, s[4:7], s53 offen offset:0 nt lds\n"
      "buffer_load_dwordx4 v11, s[4:7], s53 offen offset:1024 nt lds\n"
      "v_mfma_f32_4x4x4_16b_bf16 v[76:79], a[248:249], a[224:225], v[76:79]\n"
      "ds_read_b64_tr_b16 a[92:93], v95 offset:41344\n"
      "v_mfma_f32_4x4x4_16b_bf16 v[80:83], a[248:249], a[226:227], v[80:83]\n"
      "ds_read_b64_tr_b16 a[94:95], v95 offset:42112\n"
      "v_mfma_f32_4x4x4_16b_bf16 v[84:87], a[246:247], a[220:221], v[76:79]\n"
      "ds_read_b64_tr_b16 a[240:241], v95 offset:53632\n"
      "v_mfma_f32_4x4x4_16b_bf16 v[88:91], a[246:247], a[222:223], v[80:83]\n"
      "ds_read_b64_tr_b16 a[242:243], v95 offset:54400\n"
      "v_mfma_f32_4x4x4_16b_bf16 v[76:79], a[250:251], a[220:221], v[76:79]\n"
      "ds_read_b64_tr_b16 a[236:237], v95 offset:47488\n"
      "v_mfma_f32_4x4x4_16b_bf16 v[80:83], a[250:251], a[222:223], v[80:83]\n"
      "ds_read_b64_tr_b16 a[238:239], v95 offset:48256\n"
      "v_cvt_pk_bf16_f32 v36, v36, v40\n"
      "v_cvt_pk_bf16_f32 v37, v37, v41\n"
      "v_cvt_pk_bf16_f32 v38, v38, v42\n"
      "v_cvt_pk_bf16_f32 v39, v39, v43\n"
      "v_accvgpr_write_b32 a160, v36\n"
      "v_accvgpr_write_b32 a161, v37\n"
      "v_accvgpr_write_b32 a162, v38\n"
      "v_accvgpr_write_b32 a163, v39\n"
      "v_mfma_f32_16x16x32_bf16 v[72:75], a[180:183], a[212:215], v[72:75]\n"
      "v_cvt_pk_bf16_f32 v28, v28, v32\n"
      "v_cvt_pk_bf16_f32 v29, v29, v33\n"
      "v_mfma_f32_16x16x32_bf16 v[100:103], a[72:75], a[192:195], v[100:103]\n"
      "v_cvt_pk_bf16_f32 v30, v30, v34\n"
      "v_cvt_pk_bf16_f32 v31, v31, v35\n"
      "v_mfma_f32_16x16x32_bf16 v[104:107], a[76:79], a[192:195], v[104:107]\n"
      "s_nop 0\n"
      "v_accvgpr_write_b32 a156, v28\n"
      "v_accvgpr_write_b32 a157, v29\n"
      "v_mfma_f32_16x16x32_bf16 v[108:111], a[80:83], a[192:195], v[108:111]\n"
      "s_waitcnt vmcnt(12)\n"
      "v_accvgpr_write_b32 a158, v30\n"
      "v_accvgpr_write_b32 a159, v31\n"
      "s_barrier\n"
      "s_add_u32 s53, s54, 1376256\n"
      "s_add_u32 m0, s37, 141448\n"
      "v_mfma_f32_4x4x4_16b_bf16 v[28:31], a[244:245], a[88:89], 0\n"
      "v_mfma_f32_4x4x4_16b_bf16 v[32:35], a[244:245], a[90:91], 0\n"
      "buffer_load_dwordx4 v10, s[4:7], s53 offen offset:0 nt lds\n"
      "buffer_load_dwordx4 v11, s[4:7], s53 offen offset:1024 nt lds\n"
      "s_waitcnt lgkmcnt(0)\n"
      "v_mfma_f32_4x4x4_16b_bf16 v[28:31], a[248:249], a[232:233], v[28:31]\n"
      "ds_read_b64_tr_b16 a[96:97], v96 offset:2304\n"
      "v_mfma_f32_4x4x4_16b_bf16 v[32:35], a[248:249], a[234:235], v[32:35]\n"
      "ds_read_b64_tr_b16 a[98:99], v96 offset:3072\n"
      "v_mfma_f32_4x4x4_16b_bf16 v[36:39], a[246:247], a[228:229], v[28:31]\n"
      "ds_read_b64_tr_b16 a[224:225], v96 offset:14592\n"
      "v_mfma_f32_4x4x4_16b_bf16 v[40:43], a[246:247], a[230:231], v[32:35]\n"
      "ds_read_b64_tr_b16 a[226:227], v96 offset:15360\n"
      "v_mfma_f32_4x4x4_16b_bf16 v[28:31], a[250:251], a[228:229], v[28:31]\n"
      "ds_read_b64_tr_b16 a[220:221], v96 offset:8448\n"
      "v_mfma_f32_4x4x4_16b_bf16 v[32:35], a[250:251], a[230:231], v[32:35]\n"
      "ds_read_b64_tr_b16 a[222:223], v96 offset:9216\n"
      "ds_read_b128 a[204:207], v5 offset:55232\n"
      "v_cvt_pk_bf16_f32 v84, v84, v88\n"
      "v_cvt_pk_bf16_f32 v85, v85, v89\n"
      "v_cvt_pk_bf16_f32 v86, v86, v90\n"
      "v_cvt_pk_bf16_f32 v87, v87, v91\n"
      "v_mfma_f32_16x16x32_bf16 v[52:55], a[176:179], a[196:199], v[52:55]\n"
      "v_accvgpr_write_b32 a152, v84\n"
      "v_accvgpr_write_b32 a153, v85\n"
      "v_mfma_f32_16x16x32_bf16 v[56:59], a[168:171], a[196:199], v[56:59]\n"
      "v_accvgpr_write_b32 a154, v86\n"
      "v_accvgpr_write_b32 a155, v87\n"
      "v_mfma_f32_16x16x32_bf16 v[60:63], a[160:163], a[196:199], v[60:63]\n"
      "v_cvt_pk_bf16_f32 v76, v76, v80\n"
      "v_cvt_pk_bf16_f32 v77, v77, v81\n"
      "v_mfma_f32_16x16x32_bf16 v[64:67], a[172:175], a[200:203], v[64:67]\n"
      "v_cvt_pk_bf16_f32 v78, v78, v82\n"
      "v_cvt_pk_bf16_f32 v79, v79, v83\n"
      "v_mfma_f32_16x16x32_bf16 v[68:71], a[164:167], a[200:203], v[68:71]\n"
      "s_add_u32 m0, s37, 145792\n"
      "v_accvgpr_write_b32 a148, v76\n"
      "v_accvgpr_write_b32 a149, v77\n"
      "v_mfma_f32_16x16x32_bf16 v[72:75], a[156:159], a[200:203], v[72:75]\n"
      "s_add_u32 s53, s54, 1572864\n"
      "v_accvgpr_write_b32 a150, v78\n"
      "v_accvgpr_write_b32 a151, v79\n"
      "v_mfma_f32_4x4x4_16b_bf16 v[76:79], a[244:245], a[92:93], 0\n"
      "v_mfma_f32_4x4x4_16b_bf16 v[80:83], a[244:245], a[94:95], 0\n"
      "buffer_load_dwordx4 v10, s[4:7], s53 offen offset:0 nt lds\n"
      "buffer_load_dwordx4 v11, s[4:7], s53 offen offset:1024 nt lds\n"
      "v_mfma_f32_4x4x4_16b_bf16 v[76:79], a[248:249], a[240:241], v[76:79]\n"
      "ds_read_b64_tr_b16 a[100:101], v96 offset:2368\n"
      "v_mfma_f32_4x4x4_16b_bf16 v[80:83], a[248:249], a[242:243], v[80:83]\n"
      "ds_read_b64_tr_b16 a[102:103], v96 offset:3136\n"
      "v_mfma_f32_4x4x4_16b_bf16 v[84:87], a[246:247], a[236:237], v[76:79]\n"
      "ds_read_b64_tr_b16 a[232:233], v96 offset:14656\n"
      "v_mfma_f32_4x4x4_16b_bf16 v[88:91], a[246:247], a[238:239], v[80:83]\n"
      "ds_read_b64_tr_b16 a[234:235], v96 offset:15424\n"
      "v_mfma_f32_4x4x4_16b_bf16 v[76:79], a[250:251], a[236:237], v[76:79]\n"
      "ds_read_b64_tr_b16 a[228:229], v96 offset:8512\n"
      "v_mfma_f32_4x4x4_16b_bf16 v[80:83], a[250:251], a[238:239], v[80:83]\n"
      "ds_read_b64_tr_b16 a[230:231], v96 offset:9280\n"
      "ds_read_b128 a[208:211], v2 offset:64\n"
      "ds_read_b128 a[212:215], v2 offset:4160\n"
      "v_cvt_pk_bf16_f32 v36, v36, v40\n"
      "v_cvt_pk_bf16_f32 v37, v37, v41\n"
      "v_cvt_pk_bf16_f32 v38, v38, v42\n"
      "v_cvt_pk_bf16_f32 v39, v39, v43\n"
      "v_accvgpr_write_b32 a144, v36\n"
      "v_accvgpr_write_b32 a145, v37\n"
      "v_accvgpr_write_b32 a146, v38\n"
      "v_accvgpr_write_b32 a147, v39\n"
      "v_cvt_pk_bf16_f32 v28, v28, v32\n"
      "v_cvt_pk_bf16_f32 v29, v29, v33\n"
      "v_cvt_pk_bf16_f32 v30, v30, v34\n"
      "v_cvt_pk_bf16_f32 v31, v31, v35\n"
      "v_accvgpr_write_b32 a140, v28\n"
      "v_accvgpr_write_b32 a141, v29\n"
      "v_accvgpr_write_b32 a142, v30\n"
      "v_accvgpr_write_b32 a143, v31\n"
      "s_nop 0\n"
      "s_waitcnt lgkmcnt(2)\n"
      "v_mfma_f32_4x4x4_16b_bf16 v[28:31], a[244:245], a[96:97], 0\n"
      "v_mfma_f32_4x4x4_16b_bf16 v[32:35], a[244:245], a[98:99], 0\n"
      "ds_read_b64_tr_b16 a[104:105], v96 offset:2432\n"
      "v_mfma_f32_4x4x4_16b_bf16 v[28:31], a[248:249], a[224:225], v[28:31]\n"
      "ds_read_b64_tr_b16 a[106:107], v96 offset:3200\n"
      "v_mfma_f32_4x4x4_16b_bf16 v[32:35], a[248:249], a[226:227], v[32:35]\n"
      "ds_read_b64_tr_b16 a[240:241], v96 offset:14720\n"
      "v_mfma_f32_4x4x4_16b_bf16 v[36:39], a[246:247], a[220:221], v[28:31]\n"
      "ds_read_b64_tr_b16 a[242:243], v96 offset:15488\n"
      "v_mfma_f32_4x4x4_16b_bf16 v[40:43], a[246:247], a[222:223], v[32:35]\n"
      "ds_read_b64_tr_b16 a[236:237], v96 offset:8576\n"
      "v_mfma_f32_4x4x4_16b_bf16 v[28:31], a[250:251], a[220:221], v[28:31]\n"
      "ds_read_b64_tr_b16 a[238:239], v96 offset:9344\n"
      "v_mfma_f32_4x4x4_16b_bf16 v[32:35], a[250:251], a[222:223], v[32:35]\n"
      "v_cvt_pk_bf16_f32 v84, v84, v88\n"
      "v_cvt_pk_bf16_f32 v85, v85, v89\n"
      "v_cvt_pk_bf16_f32 v86, v86, v90\n"
      "v_cvt_pk_bf16_f32 v87, v87, v91\n"
      "v_accvgpr_write_b32 a136, v84\n"
      "v_accvgpr_write_b32 a137, v85\n"
      "v_accvgpr_write_b32 a138, v86\n"
      "v_accvgpr_write_b32 a139, v87\n"
      "v_cvt_pk_bf16_f32 v76, v76, v80\n"
      "v_cvt_pk_bf16_f32 v77, v77, v81\n"
      "v_mfma_f32_16x16x32_bf16 v[100:103], a[84:87], a[204:207], v[100:103]\n"
      "s_nop 0\n"
      "v_cvt_pk_bf16_f32 v78, v78, v82\n"
      "v_cvt_pk_bf16_f32 v79, v79, v83\n"
      "v_mfma_f32_16x16x32_bf16 v[104:107], a[88:91], a[204:207], v[104:107]\n"
      "s_nop 0\n"
      "v_accvgpr_write_b32 a132, v76\n"
      "v_accvgpr_write_b32 a133, v77\n"
      "v_mfma_f32_16x16x32_bf16 v[108:111], a[92:95], a[204:207], v[108:111]\n"
      "s_nop 0\n"
      "v_accvgpr_write_b32 a134, v78\n"
      "v_accvgpr_write_b32 a135, v79\n"
      "v_mfma_f32_4x4x4_16b_bf16 v[76:79], a[244:245], a[100:101], 0\n"
      "v_mfma_f32_4x4x4_16b_bf16 v[80:83], a[244:245], a[102:103], 0\n"
      "ds_read_b128 a[192:195], v5 offset:55296\n"
      "v_mfma_f32_4x4x4_16b_bf16 v[76:79], a[248:249], a[232:233], v[76:79]\n"
      "v_mfma_f32_4x4x4_16b_bf16 v[80:83], a[248:249], a[234:235], v[80:83]\n"
      "s_nop 0\n"
      "v_mfma_f32_4x4x4_16b_bf16 v[84:87], a[246:247], a[228:229], v[76:79]\n"
      "v_mfma_f32_4x4x4_16b_bf16 v[88:91], a[246:247], a[230:231], v[80:83]\n"
      "v_mfma_f32_4x4x4_16b_bf16 v[76:79], a[250:251], a[228:229], v[76:79]\n"
      "v_mfma_f32_4x4x4_16b_bf16 v[80:83], a[250:251], a[230:231], v[80:83]\n"
      "v_cvt_pk_bf16_f32 v36, v36, v40\n"
      "v_cvt_pk_bf16_f32 v37, v37, v41\n"
      "v_cvt_pk_bf16_f32 v38, v38, v42\n"
      "v_cvt_pk_bf16_f32 v39, v39, v43\n"
      "s_waitcnt lgkmcnt(1)\n"
      "v_mfma_f32_16x16x32_bf16 v[52:55], a[152:155], a[208:211], v[52:55]\n"
      "v_accvgpr_write_b32 a128, v36\n"
      "v_accvgpr_write_b32 a129, v37\n"
      "v_mfma_f32_16x16x32_bf16 v[56:59], a[144:147], a[208:211], v[56:59]\n"
      "v_accvgpr_write_b32 a130, v38\n"
      "v_accvgpr_write_b32 a131, v39\n"
      "v_mfma_f32_16x16x32_bf16 v[60:63], a[136:139], a[208:211], v[60:63]\n"
      "s_nop 0\n"
      "v_cvt_pk_bf16_f32 v28, v28, v32\n"
      "v_cvt_pk_bf16_f32 v29, v29, v33\n"
      "v_mfma_f32_16x16x32_bf16 v[64:67], a[148:151], a[212:215], v[64:67]\n"
      "v_cvt_pk_bf16_f32 v30, v30, v34\n"
      "v_cvt_pk_bf16_f32 v31, v31, v35\n"
      "v_mfma_f32_16x16x32_bf16 v[68:71], a[140:143], a[212:215], v[68:71]\n"
      "v_accvgpr_write_b32 a124, v28\n"
      "v_accvgpr_write_b32 a125, v29\n"
      "v_mfma_f32_16x16x32_bf16 v[72:75], a[132:135], a[212:215], v[72:75]\n"
      "v_accvgpr_write_b32 a126, v30\n"
      "v_accvgpr_write_b32 a127, v31\n"
      "v_mfma_f32_4x4x4_16b_bf16 v[28:31], a[244:245], a[104:105], 0\n"
      "v_mfma_f32_4x4x4_16b_bf16 v[32:35], a[244:245], a[106:107], 0\n"
      "ds_read_b128 a[196:199], v2 offset:128\n"
      "v_mfma_f32_4x4x4_16b_bf16 v[28:31], a[248:249], a[240:241], v[28:31]\n"
      "ds_read_b128 a[200:203], v2 offset:4224\n"
      "v_mfma_f32_4x4x4_16b_bf16 v[32:35], a[248:249], a[242:243], v[32:35]\n"
      "v_mfma_f32_4x4x4_16b_bf16 v[36:39], a[246:247], a[236:237], v[28:31]\n"
      "s_nop 0\n"
      "s_nop 0\n"
      "v_mfma_f32_4x4x4_16b_bf16 v[40:43], a[246:247], a[238:239], v[32:35]\n"
      "v_mfma_f32_4x4x4_16b_bf16 v[28:31], a[250:251], a[236:237], v[28:31]\n"
      "v_mfma_f32_4x4x4_16b_bf16 v[32:35], a[250:251], a[238:239], v[32:35]\n"
      "v_cvt_pk_bf16_f32 v84, v84, v88\n"
      "v_cvt_pk_bf16_f32 v85, v85, v89\n"
      "v_cvt_pk_bf16_f32 v86, v86, v90\n"
      "v_cvt_pk_bf16_f32 v87, v87, v91\n"
      "v_accvgpr_write_b32 a120, v84\n"
      "v_accvgpr_write_b32 a121, v85\n"
      "v_accvgpr_write_b32 a122, v86\n"
      "v_accvgpr_write_b32 a123, v87\n"
      "v_cvt_pk_bf16_f32 v76, v76, v80\n"
      "v_cvt_pk_bf16_f32 v77, v77, v81\n"
      "v_cvt_pk_bf16_f32 v78, v78, v82\n"
      "v_cvt_pk_bf16_f32 v79, v79, v83\n"
      "v_accvgpr_write_b32 a116, v76\n"
      "v_accvgpr_write_b32 a117, v77\n"
      "v_accvgpr_write_b32 a118, v78\n"
      "v_accvgpr_write_b32 a119, v79\n"
      "s_waitcnt lgkmcnt(0)\n"
      "s_waitcnt vmcnt(14)\n"
      "s_barrier\n"
      "s_mov_b32 m0, s64\n"
      "s_nop 0\n"
      "s_nop 0\n"
      "v_mfma_f32_16x16x32_bf16 v[100:103], a[96:99], a[192:195], v[100:103]\n"
      "s_add_u32 s53, s54, 1769472\n"
      "ds_read_b128 a[204:207], v3 offset:59264\n"
      "v_cvt_pk_bf16_f32 v36, v36, v40\n"
      "v_cvt_pk_bf16_f32 v37, v37, v41\n"
      "v_cvt_pk_bf16_f32 v38, v38, v42\n"
      "v_cvt_pk_bf16_f32 v39, v39, v43\n"
      "v_accvgpr_write_b32 a112, v36\n"
      "v_mfma_f32_16x16x32_bf16 v[104:107], a[100:103], a[192:195], v[104:107]\n"
      "s_add_u32 m0, s37, 82432\n"
      "ds_read_b128 a[208:211], v3 offset:59328\n"
      "ds_read_b64_tr_b16 a[220:221], v7 offset:18944\n"
      "ds_read_b64_tr_b16 a[222:223], v7 offset:18952\n"
      "v_accvgpr_write_b32 a113, v37\n"
      "v_accvgpr_write_b32 a114, v38\n"
      "v_accvgpr_write_b32 a115, v39\n"
      "v_mfma_f32_16x16x32_bf16 v[108:111], a[104:107], a[192:195], v[108:111]\n"
      "s_nop 0\n"
      "buffer_load_dwordx4 v8, s[4:7], s53 offen offset:0 nt lds\n"
      "buffer_load_dwordx4 v9, s[4:7], s53 offen offset:1024 nt lds\n"
      "v_cvt_pk_bf16_f32 v28, v28, v32\n"
      "v_cvt_pk_bf16_f32 v29, v29, v33\n"
      "v_mfma_f32_16x16x32_bf16 v[52:55], a[128:131], a[196:199], v[52:55]\n"
      "s_mov_b32 m0, s64\n"
      "ds_read_b128 a[212:215], v3 offset:59392\n"
      "v_cvt_pk_bf16_f32 v30, v30, v34\n"
      "v_cvt_pk_bf16_f32 v31, v31, v35\n"
      "v_accvgpr_write_b32 a108, v28\n"
      "v_accvgpr_write_b32 a109, v29\n"
      "v_accvgpr_write_b32 a110, v30\n"
      "v_accvgpr_write_b32 a111, v31\n"
      "v_mfma_f32_16x16x32_bf16 v[56:59], a[120:123], a[196:199], v[56:59]\n"
      "s_nop 0\n"
      "v_mfma_f32_16x16x32_bf16 v[60:63], a[112:115], a[196:199], v[60:63]\n"
      "s_add_u32 s53, s54, 1966080\n"
      "v_mfma_f32_16x16x32_bf16 v[64:67], a[124:127], a[200:203], v[64:67]\n"
      "s_add_u32 m0, s37, 86536\n"
      "ds_read_b128 a[192:195], v4 offset:59264\n"
      "v_mfma_f32_16x16x32_bf16 v[68:71], a[116:119], a[200:203], v[68:71]\n"
      "s_waitcnt lgkmcnt(1)\n"
      "buffer_load_dwordx4 v8, s[4:7], s53 offen offset:0 nt lds\n"
      "buffer_load_dwordx4 v9, s[4:7], s53 offen offset:1024 nt lds\n"
      "v_mfma_f32_16x16x32_bf16 v[72:75], a[108:111], a[200:203], v[72:75]\n"
      "s_mov_b32 m0, s64\n"
      "v_add_f32 v52, v52, v64\n"
      "v_add_f32 v53, v53, v65\n"
      "v_mfma_f32_4x4x4_16b_bf16 v[20:23], a[216:217], a[220:221], 0\n"
      "v_add_f32 v54, v54, v66\n"
      "v_mfma_f32_4x4x4_16b_bf16 v[24:27], a[216:217], a[222:223], 0\n"
      "v_add_f32 v55, v55, v67\n"
      "v_add_f32 v56, v56, v68\n"
      "v_mfma_f32_16x16x32_bf16 v[112:115], a[0:3], a[204:207], 0\n"
      "ds_read_b128 a[196:199], v4 offset:59328\n"
      "ds_write_addtid_b32 v52 offset:45312\n"
      "v_mfma_f32_16x16x32_bf16 v[116:119], a[4:7], a[204:207], 0\n"
      "s_add_u32 s53, s54, 6488064\n"
      "ds_read_b64_tr_b16 a[224:225], v7 offset:19456\n"
      "ds_read_b64_tr_b16 a[226:227], v7 offset:19464\n"
      "v_mfma_f32_16x16x32_bf16 v[120:123], a[8:11], a[204:207], 0\n"
      "s_add_u32 m0, s37, 150272\n"
      "v_cvt_pk_bf16_f32 v20, v20, v24\n"
      "v_cvt_pk_bf16_f32 v21, v21, v25\n"
      "v_mfma_f32_16x16x32_bf16 v[112:115], a[12:15], a[208:211], v[112:115]\n"
      "buffer_load_dwordx4 v8, s[4:7], s53 offen offset:0 nt lds\n"
      "buffer_load_dwordx4 v9, s[4:7], s53 offen offset:1024 nt lds\n"
      "ds_read_b128 a[200:203], v4 offset:59392\n"
      "v_mfma_f32_16x16x32_bf16 v[116:119], a[16:19], a[208:211], v[116:119]\n"
      "s_mov_b32 m0, s64\n"
      "v_add_f32 v57, v57, v69\n"
      "v_cvt_pk_bf16_f32 v22, v22, v26\n"
      "v_mfma_f32_16x16x32_bf16 v[120:123], a[20:23], a[208:211], v[120:123]\n"
      "ds_write_addtid_b32 v20 offset:18432\n"
      "ds_write_addtid_b32 v21 offset:22528\n"
      "v_mfma_f32_16x16x32_bf16 v[112:115], a[24:27], a[212:215], v[112:115]\n"
      "s_add_u32 s53, s54, 6488064\n"
      "ds_read_b128 a[204:207], v5 offset:59264\n"
      "ds_write_addtid_b32 v53 offset:45824\n"
      "v_mfma_f32_16x16x32_bf16 v[116:119], a[28:31], a[212:215], v[116:119]\n"
      "s_add_u32 m0, s37, 158464\n"
      "v_cvt_pk_bf16_f32 v23, v23, v27\n"
      "v_add_f32 v58, v58, v70\n"
      "v_mfma_f32_16x16x32_bf16 v[120:123], a[32:35], a[212:215], v[120:123]\n"
      "s_waitcnt lgkmcnt(4)\n"
      "buffer_load_dwordx4 v10, s[4:7], s53 offen offset:0 nt lds\n"
      "buffer_load_dwordx4 v11, s[4:7], s53 offen offset:1024 nt lds\n"
      "v_mfma_f32_4x4x4_16b_bf16 v[28:31], a[216:217], a[224:225], 0\n"
      "v_add_f32 v59, v59, v71\n"
      "v_mfma_f32_4x4x4_16b_bf16 v[32:35], a[216:217], a[226:227], 0\n"
      "v_add_f32 v60, v60, v72\n"
      "v_add_f32 v61, v61, v73\n"
      "v_mfma_f32_16x16x32_bf16 v[112:115], a[36:39], a[192:195], v[112:115]\n"
      "s_mov_b32 m0, s64\n"
      "ds_read_b128 a[208:211], v5 offset:59328\n"
      "ds_write_addtid_b32 v22 offset:26624\n"
      "v_mfma_f32_16x16x32_bf16 v[116:119], a[40:43], a[192:195], v[116:119]\n"
      "ds_write_addtid_b32 v23 offset:30720\n"
      "ds_read_b64_tr_b16 a[228:229], v7 offset:19968\n"
      "v_mfma_f32_16x16x32_bf16 v[120:123], a[44:47], a[192:195], v[120:123]\n"
      "ds_write_addtid_b32 v54 offset:46336\n"
      "ds_read_b64_tr_b16 a[230:231], v7 offset:19976\n"
      "v_mfma_f32_16x16x32_bf16 v[112:115], a[48:51], a[196:199], v[112:115]\n"
      "ds_read_b128 a[212:215], v5 offset:59392\n"
      "ds_write_addtid_b32 v55 offset:46848\n"
      "v_mfma_f32_16x16x32_bf16 v[116:119], a[52:55], a[196:199], v[116:119]\n"
      "ds_write_addtid_b32 v56 offset:47360\n"
      "ds_write_addtid_b32 v57 offset:47872\n"
      "v_mfma_f32_16x16x32_bf16 v[120:123], a[56:59], a[196:199], v[120:123]\n"
      "ds_write_addtid_b32 v58 offset:48384\n"
      "ds_write_addtid_b32 v59 offset:48896\n"
      "v_mfma_f32_16x16x32_bf16 v[112:115], a[60:63], a[200:203], v[112:115]\n"
      "ds_read_b128 a[192:195], v3 offset:0\n"
      "ds_write_addtid_b32 v60 offset:49408\n"
      "v_mfma_f32_16x16x32_bf16 v[116:119], a[64:67], a[200:203], v[116:119]\n"
      "s_nop 0\n"
      "ds_write_addtid_b32 v61 offset:49920\n"
      "v_cvt_pk_bf16_f32 v28, v28, v32\n"
      "v_mfma_f32_16x16x32_bf16 v[120:123], a[68:71], a[200:203], v[120:123]\n"
      "s_waitcnt lgkmcnt(7)\n"
      "ds_write_addtid_b32 v28 offset:18944\n"
      "v_cvt_pk_bf16_f32 v29, v29, v33\n"
      "v_mfma_f32_4x4x4_16b_bf16 v[20:23], a[216:217], a[228:229], 0\n"
      "v_cvt_pk_bf16_f32 v30, v30, v34\n"
      "v_mfma_f32_4x4x4_16b_bf16 v[24:27], a[216:217], a[230:231], 0\n"
      "ds_write_addtid_b32 v29 offset:23040\n"
      "ds_write_addtid_b32 v30 offset:27136\n"
      "v_mfma_f32_16x16x32_bf16 v[112:115], a[72:75], a[204:207], v[112:115]\n"
      "ds_read_b128 a[196:199], v3 offset:64\n"
      "v_cvt_pk_bf16_f32 v31, v31, v35\n"
      "v_mfma_f32_16x16x32_bf16 v[116:119], a[76:79], a[204:207], v[116:119]\n"
      "ds_write_addtid_b32 v31 offset:31232\n"
      "ds_read_b64_tr_b16 a[232:233], v7 offset:20480\n"
      "v_mfma_f32_16x16x32_bf16 v[120:123], a[80:83], a[204:207], v[120:123]\n"
      "ds_read_b64_tr_b16 a[234:235], v7 offset:20488\n"
      "v_cvt_pk_bf16_f32 v20, v20, v24\n"
      "v_mfma_f32_16x16x32_bf16 v[112:115], a[84:87], a[208:211], v[112:115]\n"
      "ds_read_b128 a[200:203], v3 offset:128\n"
      "ds_write_addtid_b32 v20 offset:19456\n"
      "v_mfma_f32_16x16x32_bf16 v[116:119], a[88:91], a[208:211], v[116:119]\n"
      "v_cvt_pk_bf16_f32 v21, v21, v25\n"
      "v_cvt_pk_bf16_f32 v22, v22, v26\n"
      "v_mfma_f32_16x16x32_bf16 v[120:123], a[92:95], a[208:211], v[120:123]\n"
      "ds_write_addtid_b32 v21 offset:23552\n"
      "ds_write_addtid_b32 v22 offset:27648\n"
      "v_mfma_f32_16x16x32_bf16 v[112:115], a[96:99], a[212:215], v[112:115]\n"
      "ds_read_b128 a[204:207], v4 offset:0\n"
      "v_cvt_pk_bf16_f32 v23, v23, v27\n"
      "v_mfma_f32_16x16x32_bf16 v[116:119], a[100:103], a[212:215], v[116:119]\n"
      "ds_write_addtid_b32 v23 offset:31744\n"
      "v_add_f32 v62, v62, v74\n"
      "v_mfma_f32_16x16x32_bf16 v[120:123], a[104:107], a[212:215], v[120:123]\n"
      "s_waitcnt lgkmcnt(4)\n"
      "ds_write_addtid_b32 v62 offset:50432\n"
      "v_add_f32_e64 v63, v63, v75\n"
      "v_mfma_f32_4x4x4_16b_bf16 v[28:31], a[216:217], a[232:233], 0\n"
      "v_mfma_f32_4x4x4_16b_bf16 v[32:35], a[216:217], a[234:235], 0\n"
      "ds_write_addtid_b32 v63 offset:50944\n"
      "v_mfma_f32_16x16x32_bf16 v[76:79], a[0:3], a[192:195], 0\n"
      "ds_read_b128 a[208:211], v4 offset:64\n"
      "v_mfma_f32_16x16x32_bf16 v[80:83], a[4:7], a[192:195], 0\n"
      "v_mfma_f32_16x16x32_bf16 v[84:87], a[8:11], a[192:195], 0\n"
      "v_cvt_pk_bf16_f32 v28, v28, v32\n"
      "v_cvt_pk_bf16_f32 v29, v29, v33\n"
      "v_mfma_f32_16x16x32_bf16 v[76:79], a[12:15], a[196:199], v[76:79]\n"
      "ds_read_b128 a[212:215], v4 offset:128\n"
      "ds_write_addtid_b32 v28 offset:19968\n"
      "v_mfma_f32_16x16x32_bf16 v[80:83], a[16:19], a[196:199], v[80:83]\n"
      "ds_write_addtid_b32 v29 offset:24064\n"
      "v_cvt_pk_bf16_f32 v30, v30, v34\n"
      "v_mfma_f32_16x16x32_bf16 v[84:87], a[20:23], a[196:199], v[84:87]\n"
      "ds_write_addtid_b32 v30 offset:28160\n"
      "v_cvt_pk_bf16_f32 v31, v31, v35\n"
      "v_mfma_f32_16x16x32_bf16 v[76:79], a[24:27], a[200:203], v[76:79]\n"
      "ds_read_b128 a[192:195], v5 offset:0\n"
      "ds_write_addtid_b32 v31 offset:32256\n"
      "v_mfma_f32_16x16x32_bf16 v[80:83], a[28:31], a[200:203], v[80:83]\n"
      "v_mfma_f32_16x16x32_bf16 v[84:87], a[32:35], a[200:203], v[84:87]\n"
      "s_waitcnt lgkmcnt(4)\n"
      "v_mfma_f32_16x16x32_bf16 v[76:79], a[36:39], a[204:207], v[76:79]\n"
      "s_nop 0\n"
      "ds_read_b128 a[196:199], v5 offset:64\n"
      "v_mfma_f32_16x16x32_bf16 v[80:83], a[40:43], a[204:207], v[80:83]\n"
      "v_fma_f32 v40, -2.0, v64, v52\n"
      "v_fma_f32 v41, -2.0, v65, v53\n"
      "v_mfma_f32_16x16x32_bf16 v[84:87], a[44:47], a[204:207], v[84:87]\n"
      "v_fma_f32 v42, -2.0, v66, v54\n"
      "v_fma_f32 v43, -2.0, v67, v55\n"
      "v_mfma_f32_16x16x32_bf16 v[76:79], a[48:51], a[208:211], v[76:79]\n"
      "ds_read_b128 a[200:203], v5 offset:128\n"
      "v_fma_f32 v44, -2.0, v68, v56\n"
      "v_mfma_f32_16x16x32_bf16 v[80:83], a[52:55], a[208:211], v[80:83]\n"
      "v_fma_f32 v45, -2.0, v69, v57\n"
      "v_mfma_f32_16x16x32_bf16 v[84:87], a[56:59], a[208:211], v[84:87]\n"
      "s_nop 0\n"
      "v_mfma_f32_16x16x32_bf16 v[76:79], a[60:63], a[212:215], v[76:79]\n"
      "s_nop 0\n"
      "v_mfma_f32_16x16x32_bf16 v[80:83], a[64:67], a[212:215], v[80:83]\n"
      "s_waitcnt vmcnt(14)\n"
      "v_mfma_f32_16x16x32_bf16 v[84:87], a[68:71], a[212:215], v[84:87]\n"
      "s_waitcnt lgkmcnt(0)\n"
      "s_barrier\n"
      "s_add_u32 s54, s54, 1966080\n"
      "s_add_u32 s6, s19, 6295552\n"
      "LDloop_%=:\n"
      "v_mfma_f32_16x16x32_bf16 v[76:79], a[72:75], a[192:195], v[76:79]\n"
      "s_add_u32 s58, s58, s40\n"
      "ds_read_b128 a[204:207], v0 offset:16384\n"
      "ds_read2st64_b32 v[28:29], v6 offset0:0 offset1:2\n"
      "ds_read2st64_b32 v[30:31], v6 offset0:4 offset1:6\n"
      "v_mfma_f32_16x16x32_bf16 v[80:83], a[76:79], a[192:195], v[80:83]\n"
      "s_add_u32 s59, s58, 196608\n"
      "ds_read_b128 a[208:211], v0 offset:20480\n"
      "ds_read2st64_b32 v[32:33], v6 offset0:8 offset1:10\n"
      "v_mfma_f32_16x16x32_bf16 v[84:87], a[80:83], a[192:195], v[84:87]\n"
      "ds_read2st64_b32 v[34:35], v6 offset0:12 offset1:14\n"
      "v_fma_f32 v46, -2.0, v70, v58\n"
      "v_mfma_f32_16x16x32_bf16 v[76:79], a[84:87], a[196:199], v[76:79]\n"
      "ds_read_b128 a[212:215], v0 offset:16448\n"
      "v_fma_f32 v47, -2.0, v71, v59\n"
      "v_fma_f32 v48, -2.0, v72, v60\n"
      "v_mfma_f32_16x16x32_bf16 v[80:83], a[88:91], a[196:199], v[80:83]\n"
      "ds_read2st64_b32 a[220:221], v12 offset0:181 offset1:183\n"
      "v_fma_f32 v49, -2.0, v73, v61\n"
      "v_mfma_f32_16x16x32_bf16 v[84:87], a[92:95], a[196:199], v[84:87]\n"
      "ds_read2st64_b32 a[222:223], v15 offset0:181 offset1:183\n"
      "v_fma_f32 v50, -2.0, v74, v62\n"
      "v_fma_f32 v51, -2.0, v75, v63\n"
      "v_mfma_f32_16x16x32_bf16 v[76:79], a[96:99], a[200:203], v[76:79]\n"
      "ds_read_b128 a[192:195], v0 offset:20544\n"
      "ds_read2st64_b32 v[36:37], v6 offset0:16 offset1:18\n"
      "v_mfma_f32_16x16x32_bf16 v[80:83], a[100:103], a[200:203], v[80:83]\n"
      "v_mfma_f32_16x16x32_bf16 v[84:87], a[104:107], a[200:203], v[84:87]\n"
      "s_waitcnt lgkmcnt(6)\n"
      "ds_read2st64_b32 v[38:39], v6 offset0:20 offset1:22\n"
      "v_fmac_f32 v40, s46, v28\n"
      "v_fmac_f32 v41, s46, v29\n"
      "v_fmac_f32 v42, s46, v30\n"
      "v_fmac_f32 v43, s46, v31\n"
      "v_mfma_f32_16x16x32_bf16 v[52:55], v[252:255], a[204:207], 0\n"
      "s_nop 0\n"
      "ds_read_b128 a[196:199], v0 offset:16512\n"
      "v_fma_f32 v28, 0.5, v28, v100\n"
      "v_mfma_f32_16x16x32_bf16 v[56:59], v[244:247], a[204:207], 0\n"
      "s_add_u32 s54, s54, -196608\n"
      "ds_read_b64_tr_b16 a[244:245], v7 offset:36864\n"
      "ds_read_b64_tr_b16 a[246:247], v7 offset:36872\n"
      "v_fma_f32 v29, 0.5, v29, v101\n"
      "v_mfma_f32_16x16x32_bf16 v[60:63], v[236:239], a[204:207], 0\n"
      "s_add_u32 m0, s37, 99328\n"
      "v_fma_f32 v30, 0.5, v30, v102\n"
      "v_fma_f32 v31, 0.5, v31, v103\n"
      "v_mfma_f32_16x16x32_bf16 v[64:67], v[248:251], a[208:211], 0\n"
      "buffer_load_dwordx4 v10, s[4:7], s54 offen offset:0 nt lds\n"
      "buffer_load_dwordx4 v11, s[4:7], s54 offen offset:1024 nt lds\n"
      "ds_read_b128 a[200:203], v0 offset:20608\n"
      "v_mfma_f32_16x16x32_bf16 v[68:71], v[240:243], a[208:211], 0\n"
      "s_mov_b32 m0, s64\n"
      "v_fmac_f32 v40, s48, v76\n"
      "v_fmac_f32 v41, s48, v77\n"
      "v_fmac_f32 v42, s48, v78\n"
      "v_mfma_f32_16x16x32_bf16 v[72:75], v[232:235], a[208:211], 0\n"
      "s_waitcnt lgkmcnt(2)\n"
      "v_fmac_f32 v43, s48, v79\n"
      "v_fmac_f32 v28, s45, v40\n"
      "v_mfma_f32_16x16x32_bf16 v[52:55], v[228:231], a[212:215], v[52:55]\n"
      "ds_read_b128 a[204:207], v1 offset:16384\n"
      "v_fmac_f32 v29, s45, v41\n"
      "v_mfma_f32_16x16x32_bf16 v[56:59], v[220:223], a[212:215], v[56:59]\n"
      "v_fmac_f32 v30, s45, v42\n"
      "v_fmac_f32_e64 v31, s45, v43\n"
      "v_mfma_f32_16x16x32_bf16 v[60:63], v[212:215], a[212:215], v[60:63]\n"
      "s_waitcnt lgkmcnt(1)\n"
      "ds_read2st64_b32 a[224:225], v13 offset0:181 offset1:183\n"
      "ds_read2st64_b32 a[226:227], v16 offset0:181 offset1:183\n"
      "v_fma_f32 v40, s47, v40, -v76\n"
      "v_fma_f32 v41, s47, v41, -v77\n"
      "v_mfma_f32_4x4x4_16b_bf16 v[20:23], a[216:217], a[244:245], 0\n"
      "v_fma_f32 v42, s47, v42, -v78\n"
      "v_mfma_f32_4x4x4_16b_bf16 v[24:27], a[216:217], a[246:247], 0\n"
      "v_fma_f32 v43, s47, v43, -v79\n"
      "v_cvt_pk_bf16_f32 v28, v28, v29\n"
      "v_mfma_f32_16x16x32_bf16 v[64:67], v[224:227], a[192:195], v[64:67]\n"
      "buffer_store_dwordx4 a[220:223], v18, s[8:11], s58 offen offset:0 sc1\n"
      "ds_read_b128 a[208:211], v1 offset:20480\n"
      "v_mfma_f32_16x16x32_bf16 v[68:71], v[216:219], a[192:195], v[68:71]\n"
      "ds_write_addtid_b32 v28 offset:32768\n"
      "v_cvt_pk_bf16_f32 v20, v20, v24\n"
      "v_mfma_f32_16x16x32_bf16 v[72:75], v[208:211], a[192:195], v[72:75]\n"
      "ds_write_addtid_b32 v20 offset:3072\n"
      "v_cvt_pk_bf16_f32 v30, v30, v31\n"
      "v_mfma_f32_16x16x32_bf16 v[52:55], v[204:207], a[196:199], v[52:55]\n"
      "s_add_u32 s54, s54, 196608\n"
      "ds_read_b128 a[212:215], v1 offset:16448\n"
      "ds_write_addtid_b32 v30 offset:33280\n"
      "v_cvt_pk_bf16_f32 v40, v40, v41\n"
      "v_cvt_pk_bf16_f32 v21, v21, v25\n"
      "v_mfma_f32_16x16x32_bf16 v[56:59], v[196:199], a[196:199], v[56:59]\n"
      "s_add_u32 m0, s37, 103432\n"
      "v_cvt_pk_bf16_f32 v42, v42, v43\n"
      "v_cvt_pk_bf16_f32 v22, v22, v26\n"
      "v_mfma_f32_16x16x32_bf16 v[60:63], v[188:191], a[196:199], v[60:63]\n"
      "buffer_load_dwordx4 v10, s[4:7], s54 offen offset:0 nt lds\n"
      "buffer_load_dwordx4 v11, s[4:7], s54 offen offset:1024 nt lds\n"
      "v_mfma_f32_16x16x32_bf16 v[64:67], v[200:203], a[200:203], v[64:67]\n"
      "s_mov_b32 m0, s64\n"
      "ds_read_b128 a[192:195], v1 offset:20544\n"
      "ds_write_addtid_b32 v40 offset:34816\n"
      "ds_write_addtid_b32 v21 offset:7168\n"
      "v_mfma_f32_16x16x32_bf16 v[68:71], v[192:195], a[200:203], v[68:71]\n"
      "ds_write_addtid_b32 v42 offset:35328\n"
      "ds_write_addtid_b32 v22 offset:11264\n"
      "v_mfma_f32_16x16x32_bf16 v[72:75], v[184:187], a[200:203], v[72:75]\n"
      "s_waitcnt lgkmcnt(5)\n"
      "v_fmac_f32 v44, s46, v32\n"
      "v_fmac_f32 v45, s46, v33\n"
      "v_fmac_f32 v46, s46, v34\n"
      "v_mfma_f32_16x16x32_bf16 v[52:55], v[180:183], a[204:207], v[52:55]\n"
      "ds_read_b128 a[196:199], v1 offset:16512\n"
      "v_fmac_f32 v47, s46, v35\n"
      "v_mfma_f32_16x16x32_bf16 v[56:59], v[172:175], a[204:207], v[56:59]\n"
      "ds_read2st64_b32 a[228:229], v14 offset0:181 offset1:183\n"
      "ds_read2st64_b32 a[230:231], v17 offset0:181 offset1:183\n"
      "v_fma_f32 v32, 0.5, v32, v104\n"
      "v_mfma_f32_16x16x32_bf16 v[60:63], v[164:167], a[204:207], v[60:63]\n"
      "v_cvt_pk_bf16_f32 v23, v23, v27\n"
      "v_fma_f32 v33, 0.5, v33, v105\n"
      "v_mfma_f32_16x16x32_bf16 v[64:67], v[176:179], a[208:211], v[64:67]\n"
      "ds_read_b128 a[200:203], v1 offset:20608\n"
      "ds_write_addtid_b32 v23 offset:15360\n"
      "v_mfma_f32_16x16x32_bf16 v[68:71], v[168:171], a[208:211], v[68:71]\n"
      "buffer_store_dwordx4 a[224:227], v18, s[8:11], s58 offen offset:128 sc1\n"
      "v_fma_f32 v34, 0.5, v34, v106\n"
      "v_mfma_f32_16x16x32_bf16 v[72:75], v[160:163], a[208:211], v[72:75]\n"
      "v_fma_f32 v35, 0.5, v35, v107\n"
      "v_fmac_f32 v44, s48, v80\n"
      "v_mfma_f32_16x16x32_bf16 v[52:55], v[156:159], a[212:215], v[52:55]\n"
      "s_waitcnt lgkmcnt(2)\n"
      "ds_read_b128 a[204:207], v2 offset:16384\n"
      "v_fmac_f32_e64 v45, s48, v81\n"
      "v_mfma_f32_16x16x32_bf16 v[56:59], v[148:151], a[212:215], v[56:59]\n"
      "s_add_u32 s54, s54, 196608\n"
      "v_fmac_f32 v46, s48, v82\n"
      "v_fmac_f32 v47, s48, v83\n"
      "v_mfma_f32_16x16x32_bf16 v[60:63], v[140:143], a[212:215], v[60:63]\n"
      "s_add_u32 m0, s37, 90760\n"
      "v_fmac_f32 v32, s45, v44\n"
      "v_fmac_f32 v33, s45, v45\n"
      "v_mfma_f32_16x16x32_bf16 v[64:67], v[152:155], a[192:195], v[64:67]\n"
      "buffer_load_dwordx4 v8, s[4:7], s54 offen offset:0 nt lds\n"
      "buffer_load_dwordx4 v9, s[4:7], s54 offen offset:1024 nt lds\n"
      "ds_read_b128 a[208:211], v2 offset:20480\n"
      "v_mfma_f32_16x16x32_bf16 v[68:71], v[144:147], a[192:195], v[68:71]\n"
      "s_mov_b32 m0, s64\n"
      "v_fmac_f32 v34, s45, v46\n"
      "v_fmac_f32 v35, s45, v47\n"
      "v_mfma_f32_16x16x32_bf16 v[72:75], v[136:139], a[192:195], v[72:75]\n"
      "s_nop 0\n"
      "ds_read_b64_tr_b16 a[248:249], v7 offset:37376\n"
      "ds_read_b64_tr_b16 a[250:251], v7 offset:37384\n"
      "v_fma_f32 v44, s47, v44, -v80\n"
      "v_mfma_f32_16x16x32_bf16 v[52:55], v[132:135], a[196:199], v[52:55]\n"
      "ds_read_b128 a[212:215], v2 offset:16448\n"
      "v_fma_f32 v45, s47, v45, -v81\n"
      "v_mfma_f32_16x16x32_bf16 v[56:59], v[124:127], a[196:199], v[56:59]\n"
      "s_nop 0\n"
      "v_fma_f32 v46, s47, v46, -v82\n"
      "v_fma_f32 v47, s47, v47, -v83\n"
      "v_mfma_f32_16x16x32_bf16 v[60:63], a[184:187], a[196:199], v[60:63]\n"
      "s_waitcnt lgkmcnt(3)\n"
      "v_cvt_pk_bf16_f32 v32, v32, v33\n"
      "v_cvt_pk_bf16_f32 v34, v34, v35\n"
      "v_mfma_f32_16x16x32_bf16 v[64:67], v[128:131], a[200:203], v[64:67]\n"
      "buffer_store_dwordx4 a[228:231], v18, s[8:11], s58 offen offset:256 sc1\n"
      "ds_read_b128 a[192:195], v2 offset:20544\n"
      "v_mfma_f32_16x16x32_bf16 v[68:71], a[188:191], a[200:203], v[68:71]\n"
      "ds_write_addtid_b32 v32 offset:40964\n"
      "ds_write_addtid_b32 v34 offset:41476\n"
      "ds_read2st64_b32 a[232:233], v12 offset0:4 offset1:6\n"
      "v_cvt_pk_bf16_f32 v44, v44, v45\n"
      "v_cvt_pk_bf16_f32 v46, v46, v47\n"
      "v_mfma_f32_16x16x32_bf16 v[72:75], a[180:183], a[200:203], v[72:75]\n"
      "ds_write_addtid_b32 v44 offset:43012\n"
      "ds_write_addtid_b32 v46 offset:43524\n"
      "ds_read2st64_b32 a[234:235], v15 offset0:4 offset1:6\n"
      "v_mfma_f32_16x16x32_bf16 v[52:55], a[176:179], a[204:207], v[52:55]\n"
      "s_waitcnt lgkmcnt(7)\n"
      "ds_read_b128 a[196:199], v2 offset:16512\n"
      "v_fmac_f32 v48, s46, v36\n"
      "v_mfma_f32_4x4x4_16b_bf16 v[20:23], a[216:217], a[248:249], 0\n"
      "v_fmac_f32 v49, s46, v37\n"
      "v_fmac_f32 v50, s46, v38\n"
      "v_mfma_f32_4x4x4_16b_bf16 v[24:27], a[216:217], a[250:251], 0\n"
      "v_fmac_f32 v51, s46, v39\n"
      "v_fma_f32 v36, 0.5, v36, v108\n"
      "v_mfma_f32_16x16x32_bf16 v[56:59], a[168:171], a[204:207], v[56:59]\n"
      "s_nop 0\n"
      "v_fma_f32 v37, 0.5, v37, v109\n"
      "v_fma_f32 v38, 0.5, v38, v110\n"
      "v_cvt_pk_bf16_f32 v20, v20, v24\n"
      "v_mfma_f32_16x16x32_bf16 v[60:63], a[160:163], a[204:207], v[60:63]\n"
      "ds_write_addtid_b32 v20 offset:3584\n"
      "v_fma_f32 v39, 0.5, v39, v111\n"
      "v_mfma_f32_16x16x32_bf16 v[64:67], a[172:175], a[208:211], v[64:67]\n"
      "s_add_u32 s54, s54, 196608\n"
      "ds_read_b128 a[200:203], v2 offset:20608\n"
      "v_cvt_pk_bf16_f32 v21, v21, v25\n"
      "v_mfma_f32_16x16x32_bf16 v[68:71], a[164:167], a[208:211], v[68:71]\n"
      "s_add_u32 m0, s37, 95104\n"
      "v_fmac_f32 v48, s48, v84\n"
      "v_fmac_f32 v49, s48, v85\n"
      "v_mfma_f32_16x16x32_bf16 v[72:75], a[156:159], a[208:211], v[72:75]\n"
      "buffer_load_dwordx4 v8, s[4:7], s54 offen offset:0 nt lds\n"
      "buffer_load_dwordx4 v9, s[4:7], s54 offen offset:1024 nt lds\n"
      "ds_read2st64_b32 a[236:237], v13 offset0:4 offset1:6\n"
      "v_mfma_f32_16x16x32_bf16 v[52:55], a[152:155], a[212:215], v[52:55]\n"
      "s_mov_b32 m0, s64\n"
      "ds_read_b128 a[204:207], v3 offset:16896\n"
      "ds_write_addtid_b32 v21 offset:7680\n"
      "v_mfma_f32_16x16x32_bf16 v[56:59], a[144:147], a[212:215], v[56:59]\n"
      "ds_read2st64_b32 a[238:239], v16 offset0:4 offset1:6\n"
      "v_fmac_f32 v50, s48, v86\n"
      "v_cvt_pk_bf16_f32 v22, v22, v26\n"
      "v_mfma_f32_16x16x32_bf16 v[60:63], a[136:139], a[212:215], v[60:63]\n"
      "s_waitcnt lgkmcnt(4)\n"
      "ds_write_addtid_b32 v22 offset:11776\n"
      "v_fmac_f32 v51, s48, v87\n"
      "v_mfma_f32_16x16x32_bf16 v[64:67], a[148:151], a[192:195], v[64:67]\n"
      "ds_read_b128 a[208:211], v3 offset:16960\n"
      "v_fmac_f32 v36, s45, v48\n"
      "v_mfma_f32_16x16x32_bf16 v[68:71], a[140:143], a[192:195], v[68:71]\n"
      "v_cvt_pk_bf16_f32 v23, v23, v27\n"
      "v_fmac_f32 v37, s45, v49\n"
      "v_mfma_f32_16x16x32_bf16 v[72:75], a[132:135], a[192:195], v[72:75]\n"
      "buffer_store_dwordx4 a[232:235], v18, s[8:11], s59 offen offset:0 sc1\n"
      "ds_write_addtid_b32 v23 offset:15872\n"
      "v_mfma_f32_16x16x32_bf16 v[52:55], a[128:131], a[196:199], v[52:55]\n"
      "ds_read_b128 a[212:215], v3 offset:17024\n"
      "v_fmac_f32 v38, s45, v50\n"
      "v_mfma_f32_16x16x32_bf16 v[56:59], a[120:123], a[196:199], v[56:59]\n"
      "s_nop 0\n"
      "ds_read2st64_b32 a[240:241], v14 offset0:4 offset1:6\n"
      "ds_read2st64_b32 a[242:243], v17 offset0:4 offset1:6\n"
      "v_mfma_f32_16x16x32_bf16 v[60:63], a[112:115], a[196:199], v[60:63]\n"
      "v_fmac_f32 v39, s45, v51\n"
      "v_fma_f32 v48, s47, v48, -v84\n"
      "v_mfma_f32_16x16x32_bf16 v[64:67], a[124:127], a[200:203], v[64:67]\n"
      "ds_read_b128 a[192:195], v4 offset:16896\n"
      "v_fma_f32 v49, s47, v49, -v85\n"
      "v_mfma_f32_16x16x32_bf16 v[68:71], a[116:119], a[200:203], v[68:71]\n"
      "s_nop 0\n"
      "v_fma_f32 v50, s47, v50, -v86\n"
      "v_fma_f32 v51, s47, v51, -v87\n"
      "v_mfma_f32_16x16x32_bf16 v[72:75], a[108:111], a[200:203], v[72:75]\n"
      "s_waitcnt lgkmcnt(3)\n"
      "v_cvt_pk_bf16_f32 v36, v36, v37\n"
      "v_add_f32 v52, v52, v64\n"
      "v_add_f32 v53, v53, v65\n"
      "v_mfma_f32_16x16x32_bf16 v[88:91], a[0:3], a[204:207], 0\n"
      "s_nop 0\n"
      "ds_read_b128 a[196:199], v4 offset:16960\n"
      "ds_write_addtid_b32 v36 offset:36864\n"
      "ds_write_addtid_b32 v52 offset:51456\n"
      "v_mfma_f32_16x16x32_bf16 v[92:95], a[4:7], a[204:207], 0\n"
      "buffer_store_dwordx4 a[236:239], v18, s[8:11], s59 offen offset:128 sc1\n"
      "ds_write_addtid_b32 v53 offset:51968\n"
      "v_mfma_f32_16x16x32_bf16 v[96:99], a[8:11], a[204:207], 0\n"
      "v_add_f32 v54, v54, v66\n"
      "v_add_f32 v55, v55, v67\n"
      "v_mfma_f32_16x16x32_bf16 v[88:91], a[12:15], a[208:211], v[88:91]\n"
      "ds_read_b128 a[200:203], v4 offset:17024\n"
      "ds_write_addtid_b32 v54 offset:52480\n"
      "ds_write_addtid_b32 v55 offset:52992\n"
      "v_mfma_f32_16x16x32_bf16 v[92:95], a[16:19], a[208:211], v[92:95]\n"
      "v_add_f32 v56, v56, v68\n"
      "v_cvt_pk_bf16_f32 v38, v38, v39\n"
      "v_mfma_f32_16x16x32_bf16 v[96:99], a[20:23], a[208:211], v[96:99]\n"
      "s_nop 0\n"
      "ds_write_addtid_b32 v38 offset:37376\n"
      "ds_write_addtid_b32 v56 offset:53504\n"
      "v_add_f32 v57, v57, v69\n"
      "v_mfma_f32_16x16x32_bf16 v[88:91], a[24:27], a[212:215], v[88:91]\n"
      "ds_read_b128 a[204:207], v5 offset:16896\n"
      "ds_write_addtid_b32 v57 offset:54016\n"
      "v_mfma_f32_16x16x32_bf16 v[92:95], a[28:31], a[212:215], v[92:95]\n"
      "v_add_f32 v58, v58, v70\n"
      "v_cvt_pk_bf16_f32 v48, v48, v49\n"
      "v_add_f32_e64 v59, v59, v71\n"
      "v_mfma_f32_16x16x32_bf16 v[96:99], a[32:35], a[212:215], v[96:99]\n"
      "s_waitcnt lgkmcnt(4)\n"
      "ds_write_addtid_b32 v48 offset:38912\n"
      "ds_write_addtid_b32 v58 offset:54528\n"
      "v_mfma_f32_16x16x32_bf16 v[88:91], a[36:39], a[192:195], v[88:91]\n"
      "s_nop 0\n"
      "buffer_store_dwordx4 a[240:243], v18, s[8:11], s59 offen offset:256 sc1\n"
      "ds_read_b128 a[208:211], v5 offset:16960\n"
      "v_cvt_pk_bf16_f32 v50, v50, v51\n"
      "v_mfma_f32_16x16x32_bf16 v[92:95], a[40:43], a[192:195], v[92:95]\n"
      "ds_write_addtid_b32 v50 offset:39424\n"
      "ds_write_addtid_b32 v59 offset:55040\n"
      "v_mfma_f32_16x16x32_bf16 v[96:99], a[44:47], a[192:195], v[96:99]\n"
      "v_add_f32 v60, v60, v72\n"
      "v_add_f32 v61, v61, v73\n"
      "v_add_f32 v62, v62, v74\n"
      "v_add_f32 v63, v63, v75\n"
      "v_mfma_f32_16x16x32_bf16 v[88:91], a[48:51], a[196:199], v[88:91]\n"
      "ds_read_b128 a[212:215], v5 offset:17024\n"
      "ds_write_addtid_b32 v60 offset:55552\n"
      "v_mfma_f32_16x16x32_bf16 v[92:95], a[52:55], a[196:199], v[92:95]\n"
      "ds_write_addtid_b32 v61 offset:56064\n"
      "ds_write_addtid_b32 v62 offset:56576\n"
      "v_mfma_f32_16x16x32_bf16 v[96:99], a[56:59], a[196:199], v[96:99]\n"
      "ds_write_addtid_b32 v63 offset:57088\n"
      "v_fma_f32 v40, -2.0, v64, v52\n"
      "v_mfma_f32_16x16x32_bf16 v[88:91], a[60:63], a[200:203], v[88:91]\n"
      "v_fma_f32 v41, -2.0, v65, v53\n"
      "v_fma_f32 v42, -2.0, v66, v54\n"
      "v_mfma_f32_16x16x32_bf16 v[92:95], a[64:67], a[200:203], v[92:95]\n"
      "s_waitcnt vmcnt(17)\n"
      "v_fma_f32 v43, -2.0, v67, v55\n"
      "v_fma_f32 v44, -2.0, v68, v56\n"
      "v_mfma_f32_16x16x32_bf16 v[96:99], a[68:71], a[200:203], v[96:99]\n"
      "s_waitcnt lgkmcnt(0)\n"
      "v_fma_f32 v45, -2.0, v69, v57\n"
      "s_barrier\n"
      "LDmid_%=:\n"
      "v_mfma_f32_16x16x32_bf16 v[88:91], a[72:75], a[204:207], v[88:91]\n"
      "s_add_u32 s62, s62, 786432\n"
      "ds_read_b128 a[192:195], v0 offset:0\n"
      "ds_read2st64_b32 v[28:29], v6 offset0:24 offset1:26\n"
      "ds_read2st64_b32 v[30:31], v6 offset0:28 offset1:30\n"
      "v_mfma_f32_16x16x32_bf16 v[92:95], a[76:79], a[204:207], v[92:95]\n"
      "s_add_u32 s58, s62, 0\n"
      "ds_read_b128 a[196:199], v0 offset:4096\n"
      "ds_read2st64_b32 v[32:33], v6 offset0:32 offset1:34\n"
      "v_mfma_f32_16x16x32_bf16 v[96:99], a[80:83], a[204:207], v[96:99]\n"
      "s_add_u32 s59, s58, 196608\n"
      "ds_read2st64_b32 v[34:35], v6 offset0:36 offset1:38\n"
      "v_fma_f32 v46, -2.0, v70, v58\n"
      "v_mfma_f32_16x16x32_bf16 v[88:91], a[84:87], a[208:211], v[88:91]\n"
      "ds_read_b128 a[200:203], v0 offset:64\n"
      "v_fma_f32 v47, -2.0, v71, v59\n"
      "v_fma_f32 v48, -2.0, v72, v60\n"
      "v_mfma_f32_16x16x32_bf16 v[92:95], a[88:91], a[208:211], v[92:95]\n"
      "ds_read2st64_b32 a[220:221], v12 offset0:177 offset1:179\n"
      "v_fma_f32 v49, -2.0, v73, v61\n"
      "v_mfma_f32_16x16x32_bf16 v[96:99], a[92:95], a[208:211], v[96:99]\n"
      "ds_read2st64_b32 a[222:223], v15 offset0:177 offset1:179\n"
      "v_fma_f32 v50, -2.0, v74, v62\n"
      "v_fma_f32 v51, -2.0, v75, v63\n"
      "v_mfma_f32_16x16x32_bf16 v[88:91], a[96:99], a[212:215], v[88:91]\n"
      "ds_read_b128 a[204:207], v0 offset:4160\n"
      "ds_read2st64_b32 v[36:37], v6 offset0:40 offset1:42\n"
      "v_mfma_f32_16x16x32_bf16 v[92:95], a[100:103], a[212:215], v[92:95]\n"
      "v_mfma_f32_16x16x32_bf16 v[96:99], a[104:107], a[212:215], v[96:99]\n"
      "s_waitcnt lgkmcnt(6)\n"
      "ds_read2st64_b32 v[38:39], v6 offset0:44 offset1:46\n"
      "v_fmac_f32 v40, s46, v28\n"
      "v_fmac_f32 v41, s46, v29\n"
      "v_fmac_f32 v42, s46, v30\n"
      "v_fmac_f32 v43, s46, v31\n"
      "v_mfma_f32_16x16x32_bf16 v[52:55], v[252:255], a[192:195], 0\n"
      "s_nop 0\n"
      "ds_read_b128 a[208:211], v0 offset:128\n"
      "v_fma_f32 v28, 0.5, v28, v112\n"
      "v_mfma_f32_16x16x32_bf16 v[56:59], v[244:247], a[192:195], 0\n"
      "s_add_u32 s54, s54, -196608\n"
      "ds_read_b64_tr_b16 a[244:245], v7 offset:53760\n"
      "ds_read_b64_tr_b16 a[246:247], v7 offset:53768\n"
      "v_fma_f32 v29, 0.5, v29, v113\n"
      "v_mfma_f32_16x16x32_bf16 v[60:63], v[236:239], a[192:195], 0\n"
      "s_add_u32 m0, s37, 107656\n"
      "v_fma_f32 v30, 0.5, v30, v114\n"
      "v_fma_f32 v31, 0.5, v31, v115\n"
      "v_mfma_f32_16x16x32_bf16 v[64:67], v[248:251], a[196:199], 0\n"
      "buffer_load_dwordx4 v10, s[4:7], s54 offen offset:0 nt lds\n"
      "buffer_load_dwordx4 v11, s[4:7], s54 offen offset:1024 nt lds\n"
      "ds_read_b128 a[212:215], v0 offset:4224\n"
      "v_mfma_f32_16x16x32_bf16 v[68:71], v[240:243], a[196:199], 0\n"
      "s_mov_b32 m0, s64\n"
      "v_fmac_f32 v40, s48, v88\n"
      "v_fmac_f32 v41, s48, v89\n"
      "v_fmac_f32 v42, s48, v90\n"
      "v_mfma_f32_16x16x32_bf16 v[72:75], v[232:235], a[196:199], 0\n"
      "s_waitcnt lgkmcnt(2)\n"
      "v_fmac_f32 v43, s48, v91\n"
      "v_fmac_f32 v28, s45, v40\n"
      "v_mfma_f32_16x16x32_bf16 v[52:55], v[228:231], a[200:203], v[52:55]\n"
      "s_nop 0\n"
      "ds_read_b128 a[192:195], v1 offset:0\n"
      "v_fmac_f32 v29, s45, v41\n"
      "v_mfma_f32_16x16x32_bf16 v[56:59], v[220:223], a[200:203], v[56:59]\n"
      "v_fmac_f32 v30, s45, v42\n"
      "v_fmac_f32 v31, s45, v43\n"
      "v_mfma_f32_16x16x32_bf16 v[60:63], v[212:215], a[200:203], v[60:63]\n"
      "s_waitcnt lgkmcnt(1)\n"
      "ds_read2st64_b32 a[224:225], v13 offset0:177 offset1:179\n"
      "ds_read2st64_b32 a[226:227], v16 offset0:177 offset1:179\n"
      "v_fma_f32 v40, s47, v40, -v88\n"
      "v_fma_f32 v41, s47, v41, -v89\n"
      "v_mfma_f32_4x4x4_16b_bf16 v[20:23], a[216:217], a[244:245], 0\n"
      "v_fma_f32 v42, s47, v42, -v90\n"
      "v_mfma_f32_4x4x4_16b_bf16 v[24:27], a[216:217], a[246:247], 0\n"
      "v_fma_f32 v43, s47, v43, -v91\n"
      "v_cvt_pk_bf16_f32 v28, v28, v29\n"
      "v_mfma_f32_16x16x32_bf16 v[64:67], v[224:227], a[204:207], v[64:67]\n"
      "buffer_store_dwordx4 a[220:223], v18, s[8:11], s58 offen offset:0 sc1\n"
      "ds_read_b128 a[196:199], v1 offset:4096\n"
      "v_mfma_f32_16x16x32_bf16 v[68:71], v[216:219], a[204:207], v[68:71]\n"
      "ds_write_addtid_b32 v28 offset:33792\n"
      "v_cvt_pk_bf16_f32 v20, v20, v24\n"
      "v_mfma_f32_16x16x32_bf16 v[72:75], v[208:211], a[204:207], v[72:75]\n"
      "ds_write_addtid_b32 v20 offset:19456\n"
      "v_cvt_pk_bf16_f32 v30, v30, v31\n"
      "v_mfma_f32_16x16x32_bf16 v[52:55], v[204:207], a[208:211], v[52:55]\n"
      "s_add_u32 s54, s54, 196608\n"
      "ds_read_b128 a[200:203], v1 offset:64\n"
      "ds_write_addtid_b32 v30 offset:34304\n"
      "v_cvt_pk_bf16_f32 v40, v40, v41\n"
      "v_cvt_pk_bf16_f32 v21, v21, v25\n"
      "v_mfma_f32_16x16x32_bf16 v[56:59], v[196:199], a[208:211], v[56:59]\n"
      "s_add_u32 m0, s37, 112000\n"
      "v_cvt_pk_bf16_f32 v42, v42, v43\n"
      "v_cvt_pk_bf16_f32 v22, v22, v26\n"
      "v_mfma_f32_16x16x32_bf16 v[60:63], v[188:191], a[208:211], v[60:63]\n"
      "s_nop 0\n"
      "buffer_load_dwordx4 v10, s[4:7], s54 offen offset:0 nt lds\n"
      "buffer_load_dwordx4 v11, s[4:7], s54 offen offset:1024 nt lds\n"
      "v_mfma_f32_16x16x32_bf16 v[64:67], v[200:203], a[212:215], v[64:67]\n"
      "s_mov_b32 m0, s64\n"
      "ds_read_b128 a[204:207], v1 offset:4160\n"
      "ds_write_addtid_b32 v40 offset:35840\n"
      "ds_write_addtid_b32 v21 offset:23552\n"
      "v_mfma_f32_16x16x32_bf16 v[68:71], v[192:195], a[212:215], v[68:71]\n"
      "ds_write_addtid_b32 v42 offset:36352\n"
      "ds_write_addtid_b32 v22 offset:27648\n"
      "v_mfma_f32_16x16x32_bf16 v[72:75], v[184:187], a[212:215], v[72:75]\n"
      "s_waitcnt lgkmcnt(5)\n"
      "v_fmac_f32 v44, s46, v32\n"
      "v_fmac_f32 v45, s46, v33\n"
      "v_fmac_f32 v46, s46, v34\n"
      "v_mfma_f32_16x16x32_bf16 v[52:55], v[180:183], a[192:195], v[52:55]\n"
      "ds_read_b128 a[208:211], v1 offset:128\n"
      "v_fmac_f32 v47, s46, v35\n"
      "v_mfma_f32_16x16x32_bf16 v[56:59], v[172:175], a[192:195], v[56:59]\n"
      "ds_read2st64_b32 a[228:229], v14 offset0:177 offset1:179\n"
      "ds_read2st64_b32 a[230:231], v17 offset0:177 offset1:179\n"
      "v_fma_f32 v32, 0.5, v32, v116\n"
      "v_mfma_f32_16x16x32_bf16 v[60:63], v[164:167], a[192:195], v[60:63]\n"
      "s_nop 0\n"
      "v_cvt_pk_bf16_f32 v23, v23, v27\n"
      "v_fma_f32 v33, 0.5, v33, v117\n"
      "v_mfma_f32_16x16x32_bf16 v[64:67], v[176:179], a[196:199], v[64:67]\n"
      "ds_read_b128 a[212:215], v1 offset:4224\n"
      "ds_write_addtid_b32 v23 offset:31744\n"
      "v_mfma_f32_16x16x32_bf16 v[68:71], v[168:171], a[196:199], v[68:71]\n"
      "buffer_store_dwordx4 a[224:227], v18, s[8:11], s58 offen offset:128 sc1\n"
      "v_fma_f32 v34, 0.5, v34, v118\n"
      "v_mfma_f32_16x16x32_bf16 v[72:75], v[160:163], a[196:199], v[72:75]\n"
      "v_fma_f32 v35, 0.5, v35, v119\n"
      "v_fmac_f32 v44, s48, v92\n"
      "v_mfma_f32_16x16x32_bf16 v[52:55], v[156:159], a[200:203], v[52:55]\n"
      "s_waitcnt lgkmcnt(2)\n"
      "ds_read_b128 a[192:195], v2 offset:0\n"
      "v_fmac_f32 v45, s48, v93\n"
      "v_mfma_f32_16x16x32_bf16 v[56:59], v[148:151], a[200:203], v[56:59]\n"
      "s_add_u32 s54, s54, 196608\n"
      "v_fmac_f32 v46, s48, v94\n"
      "v_fmac_f32_e64 v47, s48, v95\n"
      "v_mfma_f32_16x16x32_bf16 v[60:63], v[140:143], a[200:203], v[60:63]\n"
      "s_add_u32 m0, s37, 116224\n"
      "v_fmac_f32 v32, s45, v44\n"
      "v_fmac_f32 v33, s45, v45\n"
      "v_mfma_f32_16x16x32_bf16 v[64:67], v[152:155], a[204:207], v[64:67]\n"
      "buffer_load_dwordx4 v8, s[4:7], s54 offen offset:0 nt lds\n"
      "buffer_load_dwordx4 v9, s[4:7], s54 offen offset:1024 nt lds\n"
      "ds_read_b128 a[196:199], v2 offset:4096\n"
      "v_mfma_f32_16x16x32_bf16 v[68:71], v[144:147], a[204:207], v[68:71]\n"
      "s_mov_b32 m0, s64\n"
      "v_fmac_f32 v34, s45, v46\n"
      "v_fmac_f32 v35, s45, v47\n"
      "v_mfma_f32_16x16x32_bf16 v[72:75], v[136:139], a[204:207], v[72:75]\n"
      "ds_read_b64_tr_b16 a[248:249], v7 offset:54272\n"
      "ds_read_b64_tr_b16 a[250:251], v7 offset:54280\n"
      "v_fma_f32 v44, s47, v44, -v92\n"
      "v_mfma_f32_16x16x32_bf16 v[52:55], v[132:135], a[208:211], v[52:55]\n"
      "s_nop 0\n"
      "ds_read_b128 a[200:203], v2 offset:64\n"
      "v_fma_f32 v45, s47, v45, -v93\n"
      "v_mfma_f32_16x16x32_bf16 v[56:59], v[124:127], a[208:211], v[56:59]\n"
      "s_nop 0\n"
      "v_fma_f32 v46, s47, v46, -v94\n"
      "v_fma_f32 v47, s47, v47, -v95\n"
      "v_mfma_f32_16x16x32_bf16 v[60:63], a[184:187], a[208:211], v[60:63]\n"
      "s_waitcnt lgkmcnt(3)\n"
      "v_cvt_pk_bf16_f32 v32, v32, v33\n"
      "v_cvt_pk_bf16_f32 v34, v34, v35\n"
      "v_mfma_f32_16x16x32_bf16 v[64:67], v[128:131], a[212:215], v[64:67]\n"
      "buffer_store_dwordx4 a[228:231], v18, s[8:11], s58 offen offset:256 sc1\n"
      "ds_read_b128 a[204:207], v2 offset:4160\n"
      "v_mfma_f32_16x16x32_bf16 v[68:71], a[188:191], a[212:215], v[68:71]\n"
      "ds_write_addtid_b32 v32 offset:41988\n"
      "ds_write_addtid_b32 v34 offset:42500\n"
      "ds_read2st64_b32 a[232:233], v12 offset0:0 offset1:2\n"
      "v_cvt_pk_bf16_f32 v44, v44, v45\n"
      "v_cvt_pk_bf16_f32 v46, v46, v47\n"
      "v_mfma_f32_16x16x32_bf16 v[72:75], a[180:183], a[212:215], v[72:75]\n"
      "ds_write_addtid_b32 v44 offset:44036\n"
      "ds_write_addtid_b32 v46 offset:44548\n"
      "ds_read2st64_b32 a[234:235], v15 offset0:0 offset1:2\n"
      "v_mfma_f32_16x16x32_bf16 v[52:55], a[176:179], a[192:195], v[52:55]\n"
      "s_waitcnt lgkmcnt(7)\n"
      "ds_read_b128 a[208:211], v2 offset:128\n"
      "v_fmac_f32 v48, s46, v36\n"
      "v_mfma_f32_4x4x4_16b_bf16 v[20:23], a[216:217], a[248:249], 0\n"
      "v_fmac_f32 v49, s46, v37\n"
      "v_fmac_f32 v50, s46, v38\n"
      "v_mfma_f32_4x4x4_16b_bf16 v[24:27], a[216:217], a[250:251], 0\n"
      "v_fmac_f32 v51, s46, v39\n"
      "v_fma_f32 v36, 0.5, v36, v120\n"
      "v_mfma_f32_16x16x32_bf16 v[56:59], a[168:171], a[192:195], v[56:59]\n"
      "s_nop 0\n"
      "v_fma_f32 v37, 0.5, v37, v121\n"
      "v_fma_f32 v38, 0.5, v38, v122\n"
      "v_cvt_pk_bf16_f32 v20, v20, v24\n"
      "v_mfma_f32_16x16x32_bf16 v[60:63], a[160:163], a[192:195], v[60:63]\n"
      "ds_write_addtid_b32 v20 offset:19968\n"
      "v_fma_f32 v39, 0.5, v39, v123\n"
      "v_mfma_f32_16x16x32_bf16 v[64:67], a[172:175], a[196:199], v[64:67]\n"
      "s_add_u32 s54, s54, 196608\n"
      "ds_read_b128 a[212:215], v2 offset:4224\n"
      "v_cvt_pk_bf16_f32 v21, v21, v25\n"
      "v_mfma_f32_16x16x32_bf16 v[68:71], a[164:167], a[196:199], v[68:71]\n"
      "s_add_u32 m0, s37, 120328\n"
      "v_fmac_f32 v48, s48, v96\n"
      "v_fmac_f32 v49, s48, v97\n"
      "v_mfma_f32_16x16x32_bf16 v[72:75], a[156:159], a[196:199], v[72:75]\n"
      "buffer_load_dwordx4 v8, s[4:7], s54 offen offset:0 nt lds\n"
      "buffer_load_dwordx4 v9, s[4:7], s54 offen offset:1024 nt lds\n"
      "ds_read2st64_b32 a[236:237], v13 offset0:0 offset1:2\n"
      "v_mfma_f32_16x16x32_bf16 v[52:55], a[152:155], a[200:203], v[52:55]\n"
      "s_mov_b32 m0, s64\n"
      "ds_read_b128 a[192:195], v3 offset:33792\n"
      "ds_write_addtid_b32 v21 offset:24064\n"
      "v_mfma_f32_16x16x32_bf16 v[56:59], a[144:147], a[200:203], v[56:59]\n"
      "ds_read2st64_b32 a[238:239], v16 offset0:0 offset1:2\n"
      "v_fmac_f32_e64 v50, s48, v98\n"
      "v_cvt_pk_bf16_f32 v22, v22, v26\n"
      "v_mfma_f32_16x16x32_bf16 v[60:63], a[136:139], a[200:203], v[60:63]\n"
      "s_waitcnt lgkmcnt(4)\n"
      "ds_write_addtid_b32 v22 offset:28160\n"
      "v_fmac_f32 v51, s48, v99\n"
      "v_mfma_f32_16x16x32_bf16 v[64:67], a[148:151], a[204:207], v[64:67]\n"
      "ds_read_b128 a[196:199], v3 offset:33856\n"
      "v_fmac_f32 v36, s45, v48\n"
      "v_mfma_f32_16x16x32_bf16 v[68:71], a[140:143], a[204:207], v[68:71]\n"
      "v_cvt_pk_bf16_f32 v23, v23, v27\n"
      "v_fmac_f32 v37, s45, v49\n"
      "v_mfma_f32_16x16x32_bf16 v[72:75], a[132:135], a[204:207], v[72:75]\n"
      "s_nop 0\n"
      "buffer_store_dwordx4 a[232:235], v18, s[8:11], s59 offen offset:0 sc1\n"
      "ds_write_addtid_b32 v23 offset:32256\n"
      "v_mfma_f32_16x16x32_bf16 v[52:55], a[128:131], a[208:211], v[52:55]\n"
      "ds_read_b128 a[200:203], v3 offset:33920\n"
      "v_fmac_f32 v38, s45, v50\n"
      "v_mfma_f32_16x16x32_bf16 v[56:59], a[120:123], a[208:211], v[56:59]\n"
      "s_nop 0\n"
      "ds_read2st64_b32 a[240:241], v14 offset0:0 offset1:2\n"
      "ds_read2st64_b32 a[242:243], v17 offset0:0 offset1:2\n"
      "v_mfma_f32_16x16x32_bf16 v[60:63], a[112:115], a[208:211], v[60:63]\n"
      "v_fmac_f32 v39, s45, v51\n"
      "v_fma_f32 v48, s47, v48, -v96\n"
      "v_mfma_f32_16x16x32_bf16 v[64:67], a[124:127], a[212:215], v[64:67]\n"
      "ds_read_b128 a[204:207], v4 offset:33792\n"
      "v_fma_f32 v49, s47, v49, -v97\n"
      "v_mfma_f32_16x16x32_bf16 v[68:71], a[116:119], a[212:215], v[68:71]\n"
      "s_nop 0\n"
      "v_fma_f32 v50, s47, v50, -v98\n"
      "v_fma_f32 v51, s47, v51, -v99\n"
      "v_mfma_f32_16x16x32_bf16 v[72:75], a[108:111], a[212:215], v[72:75]\n"
      "s_waitcnt lgkmcnt(3)\n"
      "v_cvt_pk_bf16_f32 v36, v36, v37\n"
      "v_add_f32 v52, v52, v64\n"
      "v_add_f32 v53, v53, v65\n"
      "v_mfma_f32_16x16x32_bf16 v[100:103], a[0:3], a[192:195], 0\n"
      "s_nop 0\n"
      "ds_read_b128 a[208:211], v4 offset:33856\n"
      "ds_write_addtid_b32 v36 offset:37888\n"
      "ds_write_addtid_b32 v52 offset:45312\n"
      "v_mfma_f32_16x16x32_bf16 v[104:107], a[4:7], a[192:195], 0\n"
      "buffer_store_dwordx4 a[236:239], v18, s[8:11], s59 offen offset:128 sc1\n"
      "ds_write_addtid_b32 v53 offset:45824\n"
      "v_mfma_f32_16x16x32_bf16 v[108:111], a[8:11], a[192:195], 0\n"
      "v_add_f32 v54, v54, v66\n"
      "v_add_f32 v55, v55, v67\n"
      "v_mfma_f32_16x16x32_bf16 v[100:103], a[12:15], a[196:199], v[100:103]\n"
      "ds_read_b128 a[212:215], v4 offset:33920\n"
      "ds_write_addtid_b32 v54 offset:46336\n"
      "ds_write_addtid_b32 v55 offset:46848\n"
      "v_mfma_f32_16x16x32_bf16 v[104:107], a[16:19], a[196:199], v[104:107]\n"
      "v_add_f32 v56, v56, v68\n"
      "v_cvt_pk_bf16_f32 v38, v38, v39\n"
      "v_mfma_f32_16x16x32_bf16 v[108:111], a[20:23], a[196:199], v[108:111]\n"
      "ds_write_addtid_b32 v38 offset:38400\n"
      "ds_write_addtid_b32 v56 offset:47360\n"
      "v_add_f32 v57, v57, v69\n"
      "v_mfma_f32_16x16x32_bf16 v[100:103], a[24:27], a[200:203], v[100:103]\n"
      "ds_read_b128 a[192:195], v5 offset:33792\n"
      "ds_write_addtid_b32 v57 offset:47872\n"
      "v_mfma_f32_16x16x32_bf16 v[104:107], a[28:31], a[200:203], v[104:107]\n"
      "v_add_f32 v58, v58, v70\n"
      "v_cvt_pk_bf16_f32 v48, v48, v49\n"
      "v_add_f32_e64 v59, v59, v71\n"
      "v_mfma_f32_16x16x32_bf16 v[108:111], a[32:35], a[200:203], v[108:111]\n"
      "s_waitcnt lgkmcnt(4)\n"
      "ds_write_addtid_b32 v48 offset:39936\n"
      "ds_write_addtid_b32 v58 offset:48384\n"
      "v_mfma_f32_16x16x32_bf16 v[100:103], a[36:39], a[204:207], v[100:103]\n"
      "buffer_store_dwordx4 a[240:243], v18, s[8:11], s59 offen offset:256 sc1\n"
      "ds_read_b128 a[196:199], v5 offset:33856\n"
      "v_cvt_pk_bf16_f32 v50, v50, v51\n"
      "v_mfma_f32_16x16x32_bf16 v[104:107], a[40:43], a[204:207], v[104:107]\n"
      "ds_write_addtid_b32 v50 offset:40448\n"
      "ds_write_addtid_b32 v59 offset:48896\n"
      "v_mfma_f32_16x16x32_bf16 v[108:111], a[44:47], a[204:207], v[108:111]\n"
      "v_add_f32 v60, v60, v72\n"
      "v_add_f32 v61, v61, v73\n"
      "v_add_f32 v62, v62, v74\n"
      "v_add_f32 v63, v63, v75\n"
      "v_mfma_f32_16x16x32_bf16 v[100:103], a[48:51], a[208:211], v[100:103]\n"
      "ds_read_b128 a[200:203], v5 offset:33920\n"
      "ds_write_addtid_b32 v60 offset:49408\n"
      "v_mfma_f32_16x16x32_bf16 v[104:107], a[52:55], a[208:211], v[104:107]\n"
      "ds_write_addtid_b32 v61 offset:49920\n"
      "ds_write_addtid_b32 v62 offset:50432\n"
      "v_mfma_f32_16x16x32_bf16 v[108:111], a[56:59], a[208:211], v[108:111]\n"
      "ds_write_addtid_b32 v63 offset:50944\n"
      "v_fma_f32 v40, -2.0, v64, v52\n"
      "v_mfma_f32_16x16x32_bf16 v[100:103], a[60:63], a[212:215], v[100:103]\n"
      "v_fma_f32 v41, -2.0, v65, v53\n"
      "v_fma_f32 v42, -2.0, v66, v54\n"
      "v_mfma_f32_16x16x32_bf16 v[104:107], a[64:67], a[212:215], v[104:107]\n"
      "s_waitcnt vmcnt(17)\n"
      "v_fma_f32 v43, -2.0, v67, v55\n"
      "v_fma_f32 v44, -2.0, v68, v56\n"
      "v_mfma_f32_16x16x32_bf16 v[108:111], a[68:71], a[212:215], v[108:111]\n"
      "s_waitcnt lgkmcnt(0)\n"
      "v_fma_f32 v45, -2.0, v69, v57\n"
      "s_barrier\n"
      "v_mfma_f32_16x16x32_bf16 v[100:103], a[72:75], a[192:195], v[100:103]\n"
      "s_add_u32 s58, s58, s40\n"
      "ds_read_b128 a[204:207], v0 offset:16384\n"
      "ds_read2st64_b32 v[28:29], v6 offset0:0 offset1:2\n"
      "ds_read2st64_b32 v[30:31], v6 offset0:4 offset1:6\n"
      "v_mfma_f32_16x16x32_bf16 v[104:107], a[76:79], a[192:195], v[104:107]\n"
      "s_add_u32 s59, s58, 196608\n"
      "ds_read_b128 a[208:211], v0 offset:20480\n"
      "ds_read2st64_b32 v[32:33], v6 offset0:8 offset1:10\n"
      "v_mfma_f32_16x16x32_bf16 v[108:111], a[80:83], a[192:195], v[108:111]\n"
      "ds_read2st64_b32 v[34:35], v6 offset0:12 offset1:14\n"
      "v_fma_f32 v46, -2.0, v70, v58\n"
      "v_mfma_f32_16x16x32_bf16 v[100:103], a[84:87], a[196:199], v[100:103]\n"
      "ds_read_b128 a[212:215], v0 offset:16448\n"
      "v_fma_f32 v47, -2.0, v71, v59\n"
      "v_fma_f32 v48, -2.0, v72, v60\n"
      "v_mfma_f32_16x16x32_bf16 v[104:107], a[88:91], a[196:199], v[104:107]\n"
      "ds_read2st64_b32 a[220:221], v12 offset0:181 offset1:183\n"
      "v_fma_f32 v49, -2.0, v73, v61\n"
      "v_mfma_f32_16x16x32_bf16 v[108:111], a[92:95], a[196:199], v[108:111]\n"
      "ds_read2st64_b32 a[222:223], v15 offset0:181 offset1:183\n"
      "v_fma_f32 v50, -2.0, v74, v62\n"
      "v_fma_f32 v51, -2.0, v75, v63\n"
      "v_mfma_f32_16x16x32_bf16 v[100:103], a[96:99], a[200:203], v[100:103]\n"
      "ds_read_b128 a[192:195], v0 offset:20544\n"
      "ds_read2st64_b32 v[36:37], v6 offset0:16 offset1:18\n"
      "v_mfma_f32_16x16x32_bf16 v[104:107], a[100:103], a[200:203], v[104:107]\n"
      "v_mfma_f32_16x16x32_bf16 v[108:111], a[104:107], a[200:203], v[108:111]\n"
      "s_waitcnt lgkmcnt(6)\n"
      "ds_read2st64_b32 v[38:39], v6 offset0:20 offset1:22\n"
      "v_fmac_f32 v40, s46, v28\n"
      "v_fmac_f32 v41, s46, v29\n"
      "v_fmac_f32 v42, s46, v30\n"
      "v_fmac_f32 v43, s46, v31\n"
      "v_mfma_f32_16x16x32_bf16 v[52:55], v[252:255], a[204:207], 0\n"
      "s_nop 0\n"
      "ds_read_b128 a[196:199], v0 offset:16512\n"
      "v_fma_f32 v28, 0.5, v28, v76\n"
      "v_mfma_f32_16x16x32_bf16 v[56:59], v[244:247], a[204:207], 0\n"
      "s_add_u32 s54, s54, -196608\n"
      "ds_read_b64_tr_b16 a[244:245], v7 offset:3072\n"
      "ds_read_b64_tr_b16 a[246:247], v7 offset:3080\n"
      "v_fma_f32 v29, 0.5, v29, v77\n"
      "v_mfma_f32_16x16x32_bf16 v[60:63], v[236:239], a[204:207], 0\n"
      "s_add_u32 m0, s37, 133120\n"
      "v_fma_f32 v30, 0.5, v30, v78\n"
      "v_fma_f32 v31, 0.5, v31, v79\n"
      "v_mfma_f32_16x16x32_bf16 v[64:67], v[248:251], a[208:211], 0\n"
      "buffer_load_dwordx4 v10, s[4:7], s54 offen offset:0 nt lds\n"
      "buffer_load_dwordx4 v11, s[4:7], s54 offen offset:1024 nt lds\n"
      "ds_read_b128 a[200:203], v0 offset:20608\n"
      "v_mfma_f32_16x16x32_bf16 v[68:71], v[240:243], a[208:211], 0\n"
      "s_mov_b32 m0, s64\n"
      "v_fmac_f32 v40, s48, v100\n"
      "v_fmac_f32 v41, s48, v101\n"
      "v_fmac_f32 v42, s48, v102\n"
      "v_mfma_f32_16x16x32_bf16 v[72:75], v[232:235], a[208:211], 0\n"
      "s_waitcnt lgkmcnt(2)\n"
      "v_fmac_f32 v43, s48, v103\n"
      "v_fmac_f32 v28, s45, v40\n"
      "v_mfma_f32_16x16x32_bf16 v[52:55], v[228:231], a[212:215], v[52:55]\n"
      "ds_read_b128 a[204:207], v1 offset:16384\n"
      "v_fmac_f32 v29, s45, v41\n"
      "v_mfma_f32_16x16x32_bf16 v[56:59], v[220:223], a[212:215], v[56:59]\n"
      "v_fmac_f32_e64 v30, s45, v42\n"
      "v_fmac_f32 v31, s45, v43\n"
      "v_mfma_f32_16x16x32_bf16 v[60:63], v[212:215], a[212:215], v[60:63]\n"
      "s_waitcnt lgkmcnt(1)\n"
      "ds_read2st64_b32 a[224:225], v13 offset0:181 offset1:183\n"
      "ds_read2st64_b32 a[226:227], v16 offset0:181 offset1:183\n"
      "v_fma_f32 v40, s47, v40, -v100\n"
      "v_fma_f32 v41, s47, v41, -v101\n"
      "v_mfma_f32_4x4x4_16b_bf16 v[20:23], a[216:217], a[244:245], 0\n"
      "v_fma_f32 v42, s47, v42, -v102\n"
      "v_mfma_f32_4x4x4_16b_bf16 v[24:27], a[216:217], a[246:247], 0\n"
      "v_fma_f32 v43, s47, v43, -v103\n"
      "v_cvt_pk_bf16_f32 v28, v28, v29\n"
      "v_mfma_f32_16x16x32_bf16 v[64:67], v[224:227], a[192:195], v[64:67]\n"
      "buffer_store_dwordx4 a[220:223], v18, s[8:11], s58 offen offset:0 sc1\n"
      "ds_read_b128 a[208:211], v1 offset:20480\n"
      "v_mfma_f32_16x16x32_bf16 v[68:71], v[216:219], a[192:195], v[68:71]\n"
      "ds_write_addtid_b32 v28 offset:32768\n"
      "v_cvt_pk_bf16_f32 v20, v20, v24\n"
      "v_mfma_f32_16x16x32_bf16 v[72:75], v[208:211], a[192:195], v[72:75]\n"
      "ds_write_addtid_b32 v20 offset:3072\n"
      "v_cvt_pk_bf16_f32 v30, v30, v31\n"
      "v_mfma_f32_16x16x32_bf16 v[52:55], v[204:207], a[196:199], v[52:55]\n"
      "s_add_u32 s54, s54, 196608\n"
      "ds_read_b128 a[212:215], v1 offset:16448\n"
      "ds_write_addtid_b32 v30 offset:33280\n"
      "v_cvt_pk_bf16_f32 v40, v40, v41\n"
      "v_cvt_pk_bf16_f32 v21, v21, v25\n"
      "v_mfma_f32_16x16x32_bf16 v[56:59], v[196:199], a[196:199], v[56:59]\n"
      "s_add_u32 m0, s37, 137224\n"
      "v_cvt_pk_bf16_f32 v42, v42, v43\n"
      "v_cvt_pk_bf16_f32 v22, v22, v26\n"
      "v_mfma_f32_16x16x32_bf16 v[60:63], v[188:191], a[196:199], v[60:63]\n"
      "buffer_load_dwordx4 v10, s[4:7], s54 offen offset:0 nt lds\n"
      "buffer_load_dwordx4 v11, s[4:7], s54 offen offset:1024 nt lds\n"
      "v_mfma_f32_16x16x32_bf16 v[64:67], v[200:203], a[200:203], v[64:67]\n"
      "s_mov_b32 m0, s64\n"
      "ds_read_b128 a[192:195], v1 offset:20544\n"
      "ds_write_addtid_b32 v40 offset:34816\n"
      "ds_write_addtid_b32 v21 offset:7168\n"
      "v_mfma_f32_16x16x32_bf16 v[68:71], v[192:195], a[200:203], v[68:71]\n"
      "s_nop 0\n"
      "ds_write_addtid_b32 v42 offset:35328\n"
      "ds_write_addtid_b32 v22 offset:11264\n"
      "v_mfma_f32_16x16x32_bf16 v[72:75], v[184:187], a[200:203], v[72:75]\n"
      "s_waitcnt lgkmcnt(5)\n"
      "v_fmac_f32 v44, s46, v32\n"
      "v_fmac_f32 v45, s46, v33\n"
      "v_fmac_f32 v46, s46, v34\n"
      "v_mfma_f32_16x16x32_bf16 v[52:55], v[180:183], a[204:207], v[52:55]\n"
      "ds_read_b128 a[196:199], v1 offset:16512\n"
      "v_fmac_f32 v47, s46, v35\n"
      "v_mfma_f32_16x16x32_bf16 v[56:59], v[172:175], a[204:207], v[56:59]\n"
      "s_nop 0\n"
      "ds_read2st64_b32 a[228:229], v14 offset0:181 offset1:183\n"
      "ds_read2st64_b32 a[230:231], v17 offset0:181 offset1:183\n"
      "v_fma_f32 v32, 0.5, v32, v80\n"
      "v_mfma_f32_16x16x32_bf16 v[60:63], v[164:167], a[204:207], v[60:63]\n"
      "v_cvt_pk_bf16_f32 v23, v23, v27\n"
      "v_fma_f32 v33, 0.5, v33, v81\n"
      "v_mfma_f32_16x16x32_bf16 v[64:67], v[176:179], a[208:211], v[64:67]\n"
      "ds_read_b128 a[200:203], v1 offset:20608\n"
      "ds_write_addtid_b32 v23 offset:15360\n"
      "v_mfma_f32_16x16x32_bf16 v[68:71], v[168:171], a[208:211], v[68:71]\n"
      "buffer_store_dwordx4 a[224:227], v18, s[8:11], s58 offen offset:128 sc1\n"
      "v_fma_f32 v34, 0.5, v34, v82\n"
      "v_mfma_f32_16x16x32_bf16 v[72:75], v[160:163], a[208:211], v[72:75]\n"
      "v_fma_f32 v35, 0.5, v35, v83\n"
      "v_fmac_f32 v44, s48, v104\n"
      "v_mfma_f32_16x16x32_bf16 v[52:55], v[156:159], a[212:215], v[52:55]\n"
      "s_waitcnt lgkmcnt(2)\n"
      "ds_read_b128 a[204:207], v2 offset:16384\n"
      "v_fmac_f32 v45, s48, v105\n"
      "v_mfma_f32_16x16x32_bf16 v[56:59], v[148:151], a[212:215], v[56:59]\n"
      "s_add_u32 s54, s54, 196608\n"
      "v_fmac_f32 v46, s48, v106\n"
      "v_fmac_f32 v47, s48, v107\n"
      "v_mfma_f32_16x16x32_bf16 v[60:63], v[140:143], a[212:215], v[60:63]\n"
      "s_add_u32 m0, s37, 124552\n"
      "v_fmac_f32 v32, s45, v44\n"
      "v_fmac_f32_e64 v33, s45, v45\n"
      "v_mfma_f32_16x16x32_bf16 v[64:67], v[152:155], a[192:195], v[64:67]\n"
      "buffer_load_dwordx4 v8, s[4:7], s54 offen offset:0 nt lds\n"
      "buffer_load_dwordx4 v9, s[4:7], s54 offen offset:1024 nt lds\n"
      "ds_read_b128 a[208:211], v2 offset:20480\n"
      "v_mfma_f32_16x16x32_bf16 v[68:71], v[144:147], a[192:195], v[68:71]\n"
      "s_mov_b32 m0, s64\n"
      "v_fmac_f32 v34, s45, v46\n"
      "v_fmac_f32 v35, s45, v47\n"
      "v_mfma_f32_16x16x32_bf16 v[72:75], v[136:139], a[192:195], v[72:75]\n"
      "s_nop 0\n"
      "ds_read_b64_tr_b16 a[248:249], v7 offset:3584\n"
      "ds_read_b64_tr_b16 a[250:251], v7 offset:3592\n"
      "v_fma_f32 v44, s47, v44, -v104\n"
      "v_mfma_f32_16x16x32_bf16 v[52:55], v[132:135], a[196:199], v[52:55]\n"
      "ds_read_b128 a[212:215], v2 offset:16448\n"
      "v_fma_f32 v45, s47, v45, -v105\n"
      "v_mfma_f32_16x16x32_bf16 v[56:59], v[124:127], a[196:199], v[56:59]\n"
      "v_fma_f32 v46, s47, v46, -v106\n"
      "v_fma_f32 v47, s47, v47, -v107\n"
      "v_mfma_f32_16x16x32_bf16 v[60:63], a[184:187], a[196:199], v[60:63]\n"
      "s_waitcnt lgkmcnt(3)\n"
      "v_cvt_pk_bf16_f32 v32, v32, v33\n"
      "v_cvt_pk_bf16_f32 v34, v34, v35\n"
      "v_mfma_f32_16x16x32_bf16 v[64:67], v[128:131], a[200:203], v[64:67]\n"
      "s_nop 0\n"
      "buffer_store_dwordx4 a[228:231], v18, s[8:11], s58 offen offset:256 sc1\n"
      "ds_read_b128 a[192:195], v2 offset:20544\n"
      "v_mfma_f32_16x16x32_bf16 v[68:71], a[188:191], a[200:203], v[68:71]\n"
      "ds_write_addtid_b32 v32 offset:40964\n"
      "ds_write_addtid_b32 v34 offset:41476\n"
      "ds_read2st64_b32 a[232:233], v12 offset0:4 offset1:6\n"
      "v_cvt_pk_bf16_f32 v44, v44, v45\n"
      "v_cvt_pk_bf16_f32 v46, v46, v47\n"
      "v_mfma_f32_16x16x32_bf16 v[72:75], a[180:183], a[200:203], v[72:75]\n"
      "ds_write_addtid_b32 v44 offset:43012\n"
      "ds_write_addtid_b32 v46 offset:43524\n"
      "ds_read2st64_b32 a[234:235], v15 offset0:4 offset1:6\n"
      "v_mfma_f32_16x16x32_bf16 v[52:55], a[176:179], a[204:207], v[52:55]\n"
      "s_waitcnt lgkmcnt(7)\n"
      "ds_read_b128 a[196:199], v2 offset:16512\n"
      "v_fmac_f32 v48, s46, v36\n"
      "v_mfma_f32_4x4x4_16b_bf16 v[20:23], a[216:217], a[248:249], 0\n"
      "v_fmac_f32 v49, s46, v37\n"
      "v_fmac_f32 v50, s46, v38\n"
      "v_mfma_f32_4x4x4_16b_bf16 v[24:27], a[216:217], a[250:251], 0\n"
      "v_fmac_f32 v51, s46, v39\n"
      "v_fma_f32 v36, 0.5, v36, v84\n"
      "v_mfma_f32_16x16x32_bf16 v[56:59], a[168:171], a[204:207], v[56:59]\n"
      "v_fma_f32 v37, 0.5, v37, v85\n"
      "v_fma_f32 v38, 0.5, v38, v86\n"
      "v_cvt_pk_bf16_f32 v20, v20, v24\n"
      "v_mfma_f32_16x16x32_bf16 v[60:63], a[160:163], a[204:207], v[60:63]\n"
      "s_nop 0\n"
      "ds_write_addtid_b32 v20 offset:3584\n"
      "v_fma_f32 v39, 0.5, v39, v87\n"
      "v_mfma_f32_16x16x32_bf16 v[64:67], a[172:175], a[208:211], v[64:67]\n"
      "s_add_u32 s54, s54, 196608\n"
      "ds_read_b128 a[200:203], v2 offset:20608\n"
      "v_cvt_pk_bf16_f32 v21, v21, v25\n"
      "v_mfma_f32_16x16x32_bf16 v[68:71], a[164:167], a[208:211], v[68:71]\n"
      "s_add_u32 m0, s37, 128896\n"
      "v_fmac_f32 v48, s48, v108\n"
      "v_fmac_f32 v49, s48, v109\n"
      "v_mfma_f32_16x16x32_bf16 v[72:75], a[156:159], a[208:211], v[72:75]\n"
      "s_nop 0\n"
      "buffer_load_dwordx4 v8, s[4:7], s54 offen offset:0 nt lds\n"
      "buffer_load_dwordx4 v9, s[4:7], s54 offen offset:1024 nt lds\n"
      "ds_read2st64_b32 a[236:237], v13 offset0:4 offset1:6\n"
      "v_mfma_f32_16x16x32_bf16 v[52:55], a[152:155], a[212:215], v[52:55]\n"
      "s_mov_b32 m0, s64\n"
      "ds_read_b128 a[204:207], v3 offset:50688\n"
      "ds_write_addtid_b32 v21 offset:7680\n"
      "v_mfma_f32_16x16x32_bf16 v[56:59], a[144:147], a[212:215], v[56:59]\n"
      "ds_read2st64_b32 a[238:239], v16 offset0:4 offset1:6\n"
      "v_fmac_f32 v50, s48, v110\n"
      "v_cvt_pk_bf16_f32 v22, v22, v26\n"
      "v_mfma_f32_16x16x32_bf16 v[60:63], a[136:139], a[212:215], v[60:63]\n"
      "s_waitcnt lgkmcnt(4)\n"
      "ds_write_addtid_b32 v22 offset:11776\n"
      "v_fmac_f32 v51, s48, v111\n"
      "v_mfma_f32_16x16x32_bf16 v[64:67], a[148:151], a[192:195], v[64:67]\n"
      "s_nop 0\n"
      "ds_read_b128 a[208:211], v3 offset:50752\n"
      "v_fmac_f32 v36, s45, v48\n"
      "v_mfma_f32_16x16x32_bf16 v[68:71], a[140:143], a[192:195], v[68:71]\n"
      "v_cvt_pk_bf16_f32 v23, v23, v27\n"
      "v_fmac_f32 v37, s45, v49\n"
      "v_mfma_f32_16x16x32_bf16 v[72:75], a[132:135], a[192:195], v[72:75]\n"
      "buffer_store_dwordx4 a[232:235], v18, s[8:11], s59 offen offset:0 sc1\n"
      "ds_write_addtid_b32 v23 offset:15872\n"
      "v_mfma_f32_16x16x32_bf16 v[52:55], a[128:131], a[196:199], v[52:55]\n"
      "ds_read_b128 a[212:215], v3 offset:50816\n"
      "v_fmac_f32 v38, s45, v50\n"
      "v_mfma_f32_16x16x32_bf16 v[56:59], a[120:123], a[196:199], v[56:59]\n"
      "ds_read2st64_b32 a[240:241], v14 offset0:4 offset1:6\n"
      "ds_read2st64_b32 a[242:243], v17 offset0:4 offset1:6\n"
      "v_mfma_f32_16x16x32_bf16 v[60:63], a[112:115], a[196:199], v[60:63]\n"
      "v_fmac_f32 v39, s45, v51\n"
      "v_fma_f32 v48, s47, v48, -v108\n"
      "v_mfma_f32_16x16x32_bf16 v[64:67], a[124:127], a[200:203], v[64:67]\n"
      "ds_read_b128 a[192:195], v4 offset:50688\n"
      "v_fma_f32 v49, s47, v49, -v109\n"
      "v_mfma_f32_16x16x32_bf16 v[68:71], a[116:119], a[200:203], v[68:71]\n"
      "v_fma_f32 v50, s47, v50, -v110\n"
      "v_fma_f32 v51, s47, v51, -v111\n"
      "v_mfma_f32_16x16x32_bf16 v[72:75], a[108:111], a[200:203], v[72:75]\n"
      "s_waitcnt lgkmcnt(3)\n"
      "v_cvt_pk_bf16_f32 v36, v36, v37\n"
      "v_add_f32 v52, v52, v64\n"
      "v_add_f32 v53, v53, v65\n"
      "v_mfma_f32_16x16x32_bf16 v[112:115], a[0:3], a[204:207], 0\n"
      "ds_read_b128 a[196:199], v4 offset:50752\n"
      "ds_write_addtid_b32 v36 offset:36864\n"
      "ds_write_addtid_b32 v52 offset:51456\n"
      "v_mfma_f32_16x16x32_bf16 v[116:119], a[4:7], a[204:207], 0\n"
      "s_nop 0\n"
      "buffer_store_dwordx4 a[236:239], v18, s[8:11], s59 offen offset:128 sc1\n"
      "ds_write_addtid_b32 v53 offset:51968\n"
      "v_mfma_f32_16x16x32_bf16 v[120:123], a[8:11], a[204:207], 0\n"
      "v_add_f32 v54, v54, v66\n"
      "v_add_f32 v55, v55, v67\n"
      "v_mfma_f32_16x16x32_bf16 v[112:115], a[12:15], a[208:211], v[112:115]\n"
      "ds_read_b128 a[200:203], v4 offset:50816\n"
      "ds_write_addtid_b32 v54 offset:52480\n"
      "ds_write_addtid_b32 v55 offset:52992\n"
      "v_mfma_f32_16x16x32_bf16 v[116:119], a[16:19], a[208:211], v[116:119]\n"
      "v_add_f32_e64 v56, v56, v68\n"
      "v_cvt_pk_bf16_f32 v38, v38, v39\n"
      "v_mfma_f32_16x16x32_bf16 v[120:123], a[20:23], a[208:211], v[120:123]\n"
      "ds_write_addtid_b32 v38 offset:37376\n"
      "ds_write_addtid_b32 v56 offset:53504\n"
      "v_add_f32 v57, v57, v69\n"
      "v_mfma_f32_16x16x32_bf16 v[112:115], a[24:27], a[212:215], v[112:115]\n"
      "s_nop 0\n"
      "ds_read_b128 a[204:207], v5 offset:50688\n"
      "ds_write_addtid_b32 v57 offset:54016\n"
      "v_mfma_f32_16x16x32_bf16 v[116:119], a[28:31], a[212:215], v[116:119]\n"
      "v_add_f32 v58, v58, v70\n"
      "v_cvt_pk_bf16_f32 v48, v48, v49\n"
      "v_add_f32_e64 v59, v59, v71\n"
      "v_mfma_f32_16x16x32_bf16 v[120:123], a[32:35], a[212:215], v[120:123]\n"
      "s_waitcnt lgkmcnt(4)\n"
      "ds_write_addtid_b32 v48 offset:38912\n"
      "ds_write_addtid_b32 v58 offset:54528\n"
      "v_mfma_f32_16x16x32_bf16 v[112:115], a[36:39], a[192:195], v[112:115]\n"
      "s_nop 0\n"
      "buffer_store_dwordx4 a[240:243], v18, s[8:11], s59 offen offset:256 sc1\n"
      "ds_read_b128 a[208:211], v5 offset:50752\n"
      "v_cvt_pk_bf16_f32 v50, v50, v51\n"
      "v_mfma_f32_16x16x32_bf16 v[116:119], a[40:43], a[192:195], v[116:119]\n"
      "s_nop 0\n"
      "ds_write_addtid_b32 v50 offset:39424\n"
      "ds_write_addtid_b32 v59 offset:55040\n"
      "v_mfma_f32_16x16x32_bf16 v[120:123], a[44:47], a[192:195], v[120:123]\n"
      "s_nop 0\n"
      "v_add_f32_e64 v60, v60, v72\n"
      "v_add_f32_e64 v61, v61, v73\n"
      "v_add_f32_e64 v62, v62, v74\n"
      "v_add_f32_e64 v63, v63, v75\n"
      "v_mfma_f32_16x16x32_bf16 v[112:115], a[48:51], a[196:199], v[112:115]\n"
      "s_nop 0\n"
      "ds_read_b128 a[212:215], v5 offset:50816\n"
      "ds_write_addtid_b32 v60 offset:55552\n"
      "v_mfma_f32_16x16x32_bf16 v[116:119], a[52:55], a[196:199], v[116:119]\n"
      "ds_write_addtid_b32 v61 offset:56064\n"
      "ds_write_addtid_b32 v62 offset:56576\n"
      "v_mfma_f32_16x16x32_bf16 v[120:123], a[56:59], a[196:199], v[120:123]\n"
      "s_nop 0\n"
      "ds_write_addtid_b32 v63 offset:57088\n"
      "v_fma_f32 v40, -2.0, v64, v52\n"
      "v_mfma_f32_16x16x32_bf16 v[112:115], a[60:63], a[200:203], v[112:115]\n"
      "s_nop 0\n"
      "v_fma_f32 v41, -2.0, v65, v53\n"
      "v_fma_f32 v42, -2.0, v66, v54\n"
      "v_mfma_f32_16x16x32_bf16 v[116:119], a[64:67], a[200:203], v[116:119]\n"
      "s_waitcnt vmcnt(17)\n"
      "v_fma_f32 v43, -2.0, v67, v55\n"
      "v_fma_f32 v44, -2.0, v68, v56\n"
      "v_mfma_f32_16x16x32_bf16 v[120:123], a[68:71], a[200:203], v[120:123]\n"
      "s_waitcnt lgkmcnt(0)\n"
      "v_fma_f32 v45, -2.0, v69, v57\n"
      "s_barrier\n"
      "v_mfma_f32_16x16x32_bf16 v[112:115], a[72:75], a[204:207], v[112:115]\n"
      "s_add_u32 s62, s62, 786432\n"
      "ds_read_b128 a[192:195], v0 offset:0\n"
      "ds_read2st64_b32 v[28:29], v6 offset0:24 offset1:26\n"
      "ds_read2st64_b32 v[30:31], v6 offset0:28 offset1:30\n"
      "v_mfma_f32_16x16x32_bf16 v[116:119], a[76:79], a[204:207], v[116:119]\n"
      "s_add_u32 s58, s62, 0\n"
      "ds_read_b128 a[196:199], v0 offset:4096\n"
      "ds_read2st64_b32 v[32:33], v6 offset0:32 offset1:34\n"
      "v_mfma_f32_16x16x32_bf16 v[120:123], a[80:83], a[204:207], v[120:123]\n"
      "s_add_u32 s59, s58, 196608\n"
      "ds_read2st64_b32 v[34:35], v6 offset0:36 offset1:38\n"
      "v_fma_f32 v46, -2.0, v70, v58\n"
      "v_mfma_f32_16x16x32_bf16 v[112:115], a[84:87], a[208:211], v[112:115]\n"
      "ds_read_b128 a[200:203], v0 offset:64\n"
      "v_fma_f32 v47, -2.0, v71, v59\n"
      "v_fma_f32 v48, -2.0, v72, v60\n"
      "v_mfma_f32_16x16x32_bf16 v[116:119], a[88:91], a[208:211], v[116:119]\n"
      "ds_read2st64_b32 a[220:221], v12 offset0:177 offset1:179\n"
      "v_fma_f32 v49, -2.0, v73, v61\n"
      "v_mfma_f32_16x16x32_bf16 v[120:123], a[92:95], a[208:211], v[120:123]\n"
      "ds_read2st64_b32 a[222:223], v15 offset0:177 offset1:179\n"
      "v_fma_f32 v50, -2.0, v74, v62\n"
      "v_fma_f32 v51, -2.0, v75, v63\n"
      "v_mfma_f32_16x16x32_bf16 v[112:115], a[96:99], a[212:215], v[112:115]\n"
      "ds_read_b128 a[204:207], v0 offset:4160\n"
      "ds_read2st64_b32 v[36:37], v6 offset0:40 offset1:42\n"
      "v_mfma_f32_16x16x32_bf16 v[116:119], a[100:103], a[212:215], v[116:119]\n"
      "v_mfma_f32_16x16x32_bf16 v[120:123], a[104:107], a[212:215], v[120:123]\n"
      "s_waitcnt lgkmcnt(6)\n"
      "ds_read2st64_b32 v[38:39], v6 offset0:44 offset1:46\n"
      "v_fmac_f32 v40, s46, v28\n"
      "v_fmac_f32 v41, s46, v29\n"
      "v_fmac_f32 v42, s46, v30\n"
      "v_fmac_f32 v43, s46, v31\n"
      "v_mfma_f32_16x16x32_bf16 v[52:55], v[252:255], a[192:195], 0\n"
      "s_nop 0\n"
      "ds_read_b128 a[208:211], v0 offset:128\n"
      "v_fma_f32 v28, 0.5, v28, v88\n"
      "v_mfma_f32_16x16x32_bf16 v[56:59], v[244:247], a[192:195], 0\n"
      "s_add_u32 s54, s54, -196608\n"
      "ds_read_b64_tr_b16 a[244:245], v7 offset:19968\n"
      "ds_read_b64_tr_b16 a[246:247], v7 offset:19976\n"
      "v_fma_f32 v29, 0.5, v29, v89\n"
      "v_mfma_f32_16x16x32_bf16 v[60:63], v[236:239], a[192:195], 0\n"
      "s_add_u32 m0, s37, 141448\n"
      "v_fma_f32 v30, 0.5, v30, v90\n"
      "v_fma_f32 v31, 0.5, v31, v91\n"
      "v_mfma_f32_16x16x32_bf16 v[64:67], v[248:251], a[196:199], 0\n"
      "buffer_load_dwordx4 v10, s[4:7], s54 offen offset:0 nt lds\n"
      "buffer_load_dwordx4 v11, s[4:7], s54 offen offset:1024 nt lds\n"
      "ds_read_b128 a[212:215], v0 offset:4224\n"
      "v_mfma_f32_16x16x32_bf16 v[68:71], v[240:243], a[196:199], 0\n"
      "s_mov_b32 m0, s64\n"
      "v_fmac_f32 v40, s48, v112\n"
      "v_fmac_f32 v41, s48, v113\n"
      "v_fmac_f32 v42, s48, v114\n"
      "v_mfma_f32_16x16x32_bf16 v[72:75], v[232:235], a[196:199], 0\n"
      "s_waitcnt lgkmcnt(2)\n"
      "v_fmac_f32 v43, s48, v115\n"
      "v_fmac_f32 v28, s45, v40\n"
      "v_mfma_f32_16x16x32_bf16 v[52:55], v[228:231], a[200:203], v[52:55]\n"
      "s_nop 0\n"
      "ds_read_b128 a[192:195], v1 offset:0\n"
      "v_fmac_f32 v29, s45, v41\n"
      "v_mfma_f32_16x16x32_bf16 v[56:59], v[220:223], a[200:203], v[56:59]\n"
      "v_fmac_f32 v30, s45, v42\n"
      "v_fmac_f32 v31, s45, v43\n"
      "v_mfma_f32_16x16x32_bf16 v[60:63], v[212:215], a[200:203], v[60:63]\n"
      "s_waitcnt lgkmcnt(1)\n"
      "ds_read2st64_b32 a[224:225], v13 offset0:177 offset1:179\n"
      "ds_read2st64_b32 a[226:227], v16 offset0:177 offset1:179\n"
      "v_fma_f32 v40, s47, v40, -v112\n"
      "v_fma_f32 v41, s47, v41, -v113\n"
      "v_mfma_f32_4x4x4_16b_bf16 v[20:23], a[216:217], a[244:245], 0\n"
      "v_fma_f32 v42, s47, v42, -v114\n"
      "v_mfma_f32_4x4x4_16b_bf16 v[24:27], a[216:217], a[246:247], 0\n"
      "v_fma_f32 v43, s47, v43, -v115\n"
      "v_cvt_pk_bf16_f32 v28, v28, v29\n"
      "v_mfma_f32_16x16x32_bf16 v[64:67], v[224:227], a[204:207], v[64:67]\n"
      "buffer_store_dwordx4 a[220:223], v18, s[8:11], s58 offen offset:0 sc1\n"
      "ds_read_b128 a[196:199], v1 offset:4096\n"
      "v_mfma_f32_16x16x32_bf16 v[68:71], v[216:219], a[204:207], v[68:71]\n"
      "ds_write_addtid_b32 v28 offset:33792\n"
      "v_cvt_pk_bf16_f32 v20, v20, v24\n"
      "v_mfma_f32_16x16x32_bf16 v[72:75], v[208:211], a[204:207], v[72:75]\n"
      "ds_write_addtid_b32 v20 offset:19456\n"
      "v_cvt_pk_bf16_f32 v30, v30, v31\n"
      "v_mfma_f32_16x16x32_bf16 v[52:55], v[204:207], a[208:211], v[52:55]\n"
      "s_add_u32 s54, s54, 196608\n"
      "ds_read_b128 a[200:203], v1 offset:64\n"
      "ds_write_addtid_b32 v30 offset:34304\n"
      "v_cvt_pk_bf16_f32 v40, v40, v41\n"
      "v_cvt_pk_bf16_f32 v21, v21, v25\n"
      "v_mfma_f32_16x16x32_bf16 v[56:59], v[196:199], a[208:211], v[56:59]\n"
      "s_add_u32 m0, s37, 145792\n"
      "v_cvt_pk_bf16_f32 v42, v42, v43\n"
      "v_cvt_pk_bf16_f32 v22, v22, v26\n"
      "v_mfma_f32_16x16x32_bf16 v[60:63], v[188:191], a[208:211], v[60:63]\n"
      "s_nop 0\n"
      "buffer_load_dwordx4 v10, s[4:7], s54 offen offset:0 nt lds\n"
      "buffer_load_dwordx4 v11, s[4:7], s54 offen offset:1024 nt lds\n"
      "v_mfma_f32_16x16x32_bf16 v[64:67], v[200:203], a[212:215], v[64:67]\n"
      "s_mov_b32 m0, s64\n"
      "ds_read_b128 a[204:207], v1 offset:4160\n"
      "ds_write_addtid_b32 v40 offset:35840\n"
      "ds_write_addtid_b32 v21 offset:23552\n"
      "v_mfma_f32_16x16x32_bf16 v[68:71], v[192:195], a[212:215], v[68:71]\n"
      "ds_write_addtid_b32 v42 offset:36352\n"
      "ds_write_addtid_b32 v22 offset:27648\n"
      "v_mfma_f32_16x16x32_bf16 v[72:75], v[184:187], a[212:215], v[72:75]\n"
      "s_waitcnt lgkmcnt(5)\n"
      "v_fmac_f32 v44, s46, v32\n"
      "v_fmac_f32 v45, s46, v33\n"
      "v_fmac_f32 v46, s46, v34\n"
      "v_mfma_f32_16x16x32_bf16 v[52:55], v[180:183], a[192:195], v[52:55]\n"
      "ds_read_b128 a[208:211], v1 offset:128\n"
      "v_fmac_f32 v47, s46, v35\n"
      "v_mfma_f32_16x16x32_bf16 v[56:59], v[172:175], a[192:195], v[56:59]\n"
      "ds_read2st64_b32 a[228:229], v14 offset0:177 offset1:179\n"
      "ds_read2st64_b32 a[230:231], v17 offset0:177 offset1:179\n"
      "v_fma_f32 v32, 0.5, v32, v92\n"
      "v_mfma_f32_16x16x32_bf16 v[60:63], v[164:167], a[192:195], v[60:63]\n"
      "s_nop 0\n"
      "v_cvt_pk_bf16_f32 v23, v23, v27\n"
      "v_fma_f32 v33, 0.5, v33, v93\n"
      "v_mfma_f32_16x16x32_bf16 v[64:67], v[176:179], a[196:199], v[64:67]\n"
      "ds_read_b128 a[212:215], v1 offset:4224\n"
      "ds_write_addtid_b32 v23 offset:31744\n"
      "v_mfma_f32_16x16x32_bf16 v[68:71], v[168:171], a[196:199], v[68:71]\n"
      "buffer_store_dwordx4 a[224:227], v18, s[8:11], s58 offen offset:128 sc1\n"
      "v_fma_f32 v34, 0.5, v34, v94\n"
      "v_mfma_f32_16x16x32_bf16 v[72:75], v[160:163], a[196:199], v[72:75]\n"
      "v_fma_f32 v35, 0.5, v35, v95\n"
      "v_fmac_f32 v44, s48, v116\n"
      "v_mfma_f32_16x16x32_bf16 v[52:55], v[156:159], a[200:203], v[52:55]\n"
      "s_waitcnt lgkmcnt(2)\n"
      "ds_read_b128 a[192:195], v2 offset:0\n"
      "v_fmac_f32 v45, s48, v117\n"
      "v_mfma_f32_16x16x32_bf16 v[56:59], v[148:151], a[200:203], v[56:59]\n"
      "s_add_u32 s54, s54, 196608\n"
      "v_fmac_f32 v46, s48, v118\n"
      "v_fmac_f32_e64 v47, s48, v119\n"
      "v_mfma_f32_16x16x32_bf16 v[60:63], v[140:143], a[200:203], v[60:63]\n"
      "s_add_u32 m0, s37, 82432\n"
      "v_fmac_f32 v32, s45, v44\n"
      "v_fmac_f32 v33, s45, v45\n"
      "v_mfma_f32_16x16x32_bf16 v[64:67], v[152:155], a[204:207], v[64:67]\n"
      "buffer_load_dwordx4 v8, s[4:7], s54 offen offset:0 nt lds\n"
      "buffer_load_dwordx4 v9, s[4:7], s54 offen offset:1024 nt lds\n"
      "ds_read_b128 a[196:199], v2 offset:4096\n"
      "v_mfma_f32_16x16x32_bf16 v[68:71], v[144:147], a[204:207], v[68:71]\n"
      "s_mov_b32 m0, s64\n"
      "v_fmac_f32 v34, s45, v46\n"
      "v_fmac_f32 v35, s45, v47\n"
      "v_mfma_f32_16x16x32_bf16 v[72:75], v[136:139], a[204:207], v[72:75]\n"
      "ds_read_b64_tr_b16 a[248:249], v7 offset:20480\n"
      "ds_read_b64_tr_b16 a[250:251], v7 offset:20488\n"
      "v_fma_f32 v44, s47, v44, -v116\n"
      "v_mfma_f32_16x16x32_bf16 v[52:55], v[132:135], a[208:211], v[52:55]\n"
      "s_nop 0\n"
      "ds_read_b128 a[200:203], v2 offset:64\n"
      "v_fma_f32 v45, s47, v45, -v117\n"
      "v_mfma_f32_16x16x32_bf16 v[56:59], v[124:127], a[208:211], v[56:59]\n"
      "s_nop 0\n"
      "v_fma_f32 v46, s47, v46, -v118\n"
      "v_fma_f32 v47, s47, v47, -v119\n"
      "v_mfma_f32_16x16x32_bf16 v[60:63], a[184:187], a[208:211], v[60:63]\n"
      "s_waitcnt lgkmcnt(3)\n"
      "v_cvt_pk_bf16_f32 v32, v32, v33\n"
      "v_cvt_pk_bf16_f32 v34, v34, v35\n"
      "v_mfma_f32_16x16x32_bf16 v[64:67], v[128:131], a[212:215], v[64:67]\n"
      "buffer_store_dwordx4 a[228:231], v18, s[8:11], s58 offen offset:256 sc1\n"
      "ds_read_b128 a[204:207], v2 offset:4160\n"
      "v_mfma_f32_16x16x32_bf16 v[68:71], a[188:191], a[212:215], v[68:71]\n"
      "ds_write_addtid_b32 v32 offset:41988\n"
      "ds_write_addtid_b32 v34 offset:42500\n"
      "ds_read2st64_b32 a[232:233], v12 offset0:0 offset1:2\n"
      "v_cvt_pk_bf16_f32 v44, v44, v45\n"
      "v_cvt_pk_bf16_f32 v46, v46, v47\n"
      "v_mfma_f32_16x16x32_bf16 v[72:75], a[180:183], a[212:215], v[72:75]\n"
      "ds_write_addtid_b32 v44 offset:44036\n"
      "ds_write_addtid_b32 v46 offset:44548\n"
      "ds_read2st64_b32 a[234:235], v15 offset0:0 offset1:2\n"
      "v_mfma_f32_16x16x32_bf16 v[52:55], a[176:179], a[192:195], v[52:55]\n"
      "s_waitcnt lgkmcnt(7)\n"
      "ds_read_b128 a[208:211], v2 offset:128\n"
      "v_fmac_f32 v48, s46, v36\n"
      "v_mfma_f32_4x4x4_16b_bf16 v[20:23], a[216:217], a[248:249], 0\n"
      "v_fmac_f32 v49, s46, v37\n"
      "v_fmac_f32 v50, s46, v38\n"
      "v_mfma_f32_4x4x4_16b_bf16 v[24:27], a[216:217], a[250:251], 0\n"
      "v_fmac_f32 v51, s46, v39\n"
      "v_fma_f32 v36, 0.5, v36, v96\n"
      "v_mfma_f32_16x16x32_bf16 v[56:59], a[168:171], a[192:195], v[56:59]\n"
      "s_nop 0\n"
      "v_fma_f32 v37, 0.5, v37, v97\n"
      "v_fma_f32 v38, 0.5, v38, v98\n"
      "v_cvt_pk_bf16_f32 v20, v20, v24\n"
      "v_mfma_f32_16x16x32_bf16 v[60:63], a[160:163], a[192:195], v[60:63]\n"
      "ds_write_addtid_b32 v20 offset:19968\n"
      "v_fma_f32 v39, 0.5, v39, v99\n"
      "v_mfma_f32_16x16x32_bf16 v[64:67], a[172:175], a[196:199], v[64:67]\n"
      "s_add_u32 s54, s54, 196608\n"
      "ds_read_b128 a[212:215], v2 offset:4224\n"
      "v_cvt_pk_bf16_f32 v21, v21, v25\n"
      "v_mfma_f32_16x16x32_bf16 v[68:71], a[164:167], a[196:199], v[68:71]\n"
      "s_add_u32 m0, s37, 86536\n"
      "v_fmac_f32 v48, s48, v120\n"
      "v_fmac_f32 v49, s48, v121\n"
      "v_mfma_f32_16x16x32_bf16 v[72:75], a[156:159], a[196:199], v[72:75]\n"
      "buffer_load_dwordx4 v8, s[4:7], s54 offen offset:0 nt lds\n"
      "buffer_load_dwordx4 v9, s[4:7], s54 offen offset:1024 nt lds\n"
      "ds_read2st64_b32 a[236:237], v13 offset0:0 offset1:2\n"
      "v_mfma_f32_16x16x32_bf16 v[52:55], a[152:155], a[200:203], v[52:55]\n"
      "s_mov_b32 m0, s64\n"
      "ds_read_b128 a[192:195], v3 offset:0\n"
      "ds_write_addtid_b32 v21 offset:24064\n"
      "v_mfma_f32_16x16x32_bf16 v[56:59], a[144:147], a[200:203], v[56:59]\n"
      "ds_read2st64_b32 a[238:239], v16 offset0:0 offset1:2\n"
      "v_fmac_f32_e64 v50, s48, v122\n"
      "v_cvt_pk_bf16_f32 v22, v22, v26\n"
      "v_mfma_f32_16x16x32_bf16 v[60:63], a[136:139], a[200:203], v[60:63]\n"
      "s_waitcnt lgkmcnt(4)\n"
      "ds_write_addtid_b32 v22 offset:28160\n"
      "v_fmac_f32 v51, s48, v123\n"
      "v_mfma_f32_16x16x32_bf16 v[64:67], a[148:151], a[204:207], v[64:67]\n"
      "ds_read_b128 a[196:199], v3 offset:64\n"
      "v_fmac_f32 v36, s45, v48\n"
      "v_mfma_f32_16x16x32_bf16 v[68:71], a[140:143], a[204:207], v[68:71]\n"
      "v_cvt_pk_bf16_f32 v23, v23, v27\n"
      "v_fmac_f32 v37, s45, v49\n"
      "v_mfma_f32_16x16x32_bf16 v[72:75], a[132:135], a[204:207], v[72:75]\n"
      "s_nop 0\n"
      "buffer_store_dwordx4 a[232:235], v18, s[8:11], s59 offen offset:0 sc1\n"
      "ds_write_addtid_b32 v23 offset:32256\n"
      "v_mfma_f32_16x16x32_bf16 v[52:55], a[128:131], a[208:211], v[52:55]\n"
      "ds_read_b128 a[200:203], v3 offset:128\n"
      "v_fmac_f32 v38, s45, v50\n"
      "v_mfma_f32_16x16x32_bf16 v[56:59], a[120:123], a[208:211], v[56:59]\n"
      "s_nop 0\n"
      "ds_read2st64_b32 a[240:241], v14 offset0:0 offset1:2\n"
      "ds_read2st64_b32 a[242:243], v17 offset0:0 offset1:2\n"
      "v_mfma_f32_16x16x32_bf16 v[60:63], a[112:115], a[208:211], v[60:63]\n"
      "v_fmac_f32 v39, s45, v51\n"
      "v_fma_f32 v48, s47, v48, -v120\n"
      "v_mfma_f32_16x16x32_bf16 v[64:67], a[124:127], a[212:215], v[64:67]\n"
      "ds_read_b128 a[204:207], v4 offset:0\n"
      "v_fma_f32 v49, s47, v49, -v121\n"
      "v_mfma_f32_16x16x32_bf16 v[68:71], a[116:119], a[212:215], v[68:71]\n"
      "s_nop 0\n"
      "v_fma_f32 v50, s47, v50, -v122\n"
      "v_fma_f32 v51, s47, v51, -v123\n"
      "v_mfma_f32_16x16x32_bf16 v[72:75], a[108:111], a[212:215], v[72:75]\n"
      "s_waitcnt lgkmcnt(3)\n"
      "v_cvt_pk_bf16_f32 v36, v36, v37\n"
      "v_add_f32 v52, v52, v64\n"
      "v_add_f32 v53, v53, v65\n"
      "v_mfma_f32_16x16x32_bf16 v[76:79], a[0:3], a[192:195], 0\n"
      "s_nop 0\n"
      "ds_read_b128 a[208:211], v4 offset:64\n"
      "ds_write_addtid_b32 v36 offset:37888\n"
      "ds_write_addtid_b32 v52 offset:45312\n"
      "v_mfma_f32_16x16x32_bf16 v[80:83], a[4:7], a[192:195], 0\n"
      "buffer_store_dwordx4 a[236:239], v18, s[8:11], s59 offen offset:128 sc1\n"
      "ds_write_addtid_b32 v53 offset:45824\n"
      "v_mfma_f32_16x16x32_bf16 v[84:87], a[8:11], a[192:195], 0\n"
      "v_add_f32 v54, v54, v66\n"
      "v_add_f32 v55, v55, v67\n"
      "v_mfma_f32_16x16x32_bf16 v[76:79], a[12:15], a[196:199], v[76:79]\n"
      "ds_read_b128 a[212:215], v4 offset:128\n"
      "ds_write_addtid_b32 v54 offset:46336\n"
      "ds_write_addtid_b32 v55 offset:46848\n"
      "v_mfma_f32_16x16x32_bf16 v[80:83], a[16:19], a[196:199], v[80:83]\n"
      "v_add_f32 v56, v56, v68\n"
      "v_cvt_pk_bf16_f32 v38, v38, v39\n"
      "v_mfma_f32_16x16x32_bf16 v[84:87], a[20:23], a[196:199], v[84:87]\n"
      "ds_write_addtid_b32 v38 offset:38400\n"
      "ds_write_addtid_b32 v56 offset:47360\n"
      "v_add_f32 v57, v57, v69\n"
      "v_mfma_f32_16x16x32_bf16 v[76:79], a[24:27], a[200:203], v[76:79]\n"
      "ds_read_b128 a[192:195], v5 offset:0\n"
      "ds_write_addtid_b32 v57 offset:47872\n"
      "v_mfma_f32_16x16x32_bf16 v[80:83], a[28:31], a[200:203], v[80:83]\n"
      "v_add_f32 v58, v58, v70\n"
      "v_cvt_pk_bf16_f32 v48, v48, v49\n"
      "v_add_f32_e64 v59, v59, v71\n"
      "v_mfma_f32_16x16x32_bf16 v[84:87], a[32:35], a[200:203], v[84:87]\n"
      "s_waitcnt lgkmcnt(4)\n"
      "ds_write_addtid_b32 v48 offset:39936\n"
      "ds_write_addtid_b32 v58 offset:48384\n"
      "v_mfma_f32_16x16x32_bf16 v[76:79], a[36:39], a[204:207], v[76:79]\n"
      "buffer_store_dwordx4 a[240:243], v18, s[8:11], s59 offen offset:256 sc1\n"
      "ds_read_b128 a[196:199], v5 offset:64\n"
      "v_cvt_pk_bf16_f32 v50, v50, v51\n"
      "v_mfma_f32_16x16x32_bf16 v[80:83], a[40:43], a[204:207], v[80:83]\n"
      "ds_write_addtid_b32 v50 offset:40448\n"
      "ds_write_addtid_b32 v59 offset:48896\n"
      "v_mfma_f32_16x16x32_bf16 v[84:87], a[44:47], a[204:207], v[84:87]\n"
      "v_add_f32 v60, v60, v72\n"
      "v_add_f32_e64 v61, v61, v73\n"
      "v_add_f32_e64 v62, v62, v74\n"
      "v_add_f32_e64 v63, v63, v75\n"
      "v_mfma_f32_16x16x32_bf16 v[76:79], a[48:51], a[208:211], v[76:79]\n"
      "s_nop 0\n"
      "ds_read_b128 a[200:203], v5 offset:128\n"
      "ds_write_addtid_b32 v60 offset:49408\n"
      "v_mfma_f32_16x16x32_bf16 v[80:83], a[52:55], a[208:211], v[80:83]\n"
      "s_nop 0\n"
      "ds_write_addtid_b32 v61 offset:49920\n"
      "ds_write_addtid_b32 v62 offset:50432\n"
      "v_mfma_f32_16x16x32_bf16 v[84:87], a[56:59], a[208:211], v[84:87]\n"
      "s_nop 0\n"
      "ds_write_addtid_b32 v63 offset:50944\n"
      "v_fma_f32 v40, -2.0, v64, v52\n"
      "v_mfma_f32_16x16x32_bf16 v[76:79], a[60:63], a[212:215], v[76:79]\n"
      "s_nop 0\n"
      "v_fma_f32 v41, -2.0, v65, v53\n"
      "v_fma_f32 v42, -2.0, v66, v54\n"
      "v_mfma_f32_16x16x32_bf16 v[80:83], a[64:67], a[212:215], v[80:83]\n"
      "s_waitcnt vmcnt(17)\n"
      "v_fma_f32 v43, -2.0, v67, v55\n"
      "v_fma_f32 v44, -2.0, v68, v56\n"
      "v_mfma_f32_16x16x32_bf16 v[84:87], a[68:71], a[212:215], v[84:87]\n"
      "s_waitcnt lgkmcnt(0)\n"
      "v_fma_f32 v45, -2.0, v69, v57\n"
      "s_barrier\n"
      "s_add_u32 s55, s55, 2\n"
      "s_cmp_lt_u32 s55, 6\n"
      "s_cbranch_scc1 LDloop_%=\n"
      "LDlast_%=:\n"
      "v_mfma_f32_16x16x32_bf16 v[76:79], a[72:75], a[192:195], v[76:79]\n"
      "s_add_u32 s58, s58, s40\n"
      "ds_read_b128 a[204:207], v0 offset:16384\n"
      "ds_read2st64_b32 v[28:29], v6 offset0:0 offset1:2\n"
      "v_fma_f32 v46, -2.0, v70, v58\n"
      "v_mfma_f32_16x16x32_bf16 v[80:83], a[76:79], a[192:195], v[80:83]\n"
      "s_add_u32 s59, s58, 196608\n"
      "ds_read_b128 a[208:211], v0 offset:20480\n"
      "ds_read2st64_b32 v[30:31], v6 offset0:4 offset1:6\n"
      "v_mfma_f32_16x16x32_bf16 v[84:87], a[80:83], a[192:195], v[84:87]\n"
      "ds_read2st64_b32 v[32:33], v6 offset0:8 offset1:10\n"
      "ds_read2st64_b32 v[34:35], v6 offset0:12 offset1:14\n"
      "v_mfma_f32_16x16x32_bf16 v[76:79], a[84:87], a[196:199], v[76:79]\n"
      "ds_read_b128 a[212:215], v0 offset:16448\n"
      "v_fma_f32 v47, -2.0, v71, v59\n"
      "v_mfma_f32_16x16x32_bf16 v[80:83], a[88:91], a[196:199], v[80:83]\n"
      "ds_read2st64_b32 v[36:37], v6 offset0:16 offset1:18\n"
      "v_fma_f32 v48, -2.0, v72, v60\n"
      "v_fma_f32 v49, -2.0, v73, v61\n"
      "v_mfma_f32_16x16x32_bf16 v[84:87], a[92:95], a[196:199], v[84:87]\n"
      "ds_read2st64_b32 v[38:39], v6 offset0:20 offset1:22\n"
      "v_fma_f32 v50, -2.0, v74, v62\n"
      "v_mfma_f32_16x16x32_bf16 v[76:79], a[96:99], a[200:203], v[76:79]\n"
      "ds_read_b128 a[192:195], v0 offset:20544\n"
      "v_fma_f32 v51, -2.0, v75, v63\n"
      "v_mfma_f32_16x16x32_bf16 v[80:83], a[100:103], a[200:203], v[80:83]\n"
      "s_nop 0\n"
      "v_mfma_f32_16x16x32_bf16 v[84:87], a[104:107], a[200:203], v[84:87]\n"
      "s_waitcnt lgkmcnt(6)\n"
      "ds_read_b64_tr_b16 a[244:245], v7 offset:36864\n"
      "ds_read_b64_tr_b16 a[246:247], v7 offset:36872\n"
      "v_fmac_f32 v40, s46, v28\n"
      "v_fmac_f32 v41, s46, v29\n"
      "v_fmac_f32 v42, s46, v30\n"
      "v_mfma_f32_16x16x32_bf16 v[52:55], v[252:255], a[204:207], 0\n"
      "ds_read_b128 a[196:199], v0 offset:16512\n"
      "v_fmac_f32 v43, s46, v31\n"
      "v_mfma_f32_16x16x32_bf16 v[56:59], v[244:247], a[204:207], 0\n"
      "v_fma_f32 v28, 0.5, v28, v100\n"
      "v_fma_f32 v29, 0.5, v29, v101\n"
      "v_mfma_f32_16x16x32_bf16 v[60:63], v[236:239], a[204:207], 0\n"
      "v_fma_f32 v30, 0.5, v30, v102\n"
      "v_fma_f32 v31, 0.5, v31, v103\n"
      "v_fmac_f32 v40, s48, v76\n"
      "v_mfma_f32_16x16x32_bf16 v[64:67], v[248:251], a[208:211], 0\n"
      "ds_read_b128 a[200:203], v0 offset:20608\n"
      "v_fmac_f32 v41, s48, v77\n"
      "v_mfma_f32_16x16x32_bf16 v[68:71], v[240:243], a[208:211], 0\n"
      "v_fmac_f32 v42, s48, v78\n"
      "v_fmac_f32 v43, s48, v79\n"
      "v_mfma_f32_16x16x32_bf16 v[72:75], v[232:235], a[208:211], 0\n"
      "s_waitcnt lgkmcnt(2)\n"
      "v_fmac_f32 v28, s45, v40\n"
      "v_fmac_f32 v29, s45, v41\n"
      "v_mfma_f32_16x16x32_bf16 v[52:55], v[228:231], a[212:215], v[52:55]\n"
      "ds_read_b128 a[204:207], v1 offset:16384\n"
      "v_fmac_f32 v30, s45, v42\n"
      "v_mfma_f32_4x4x4_16b_bf16 v[20:23], a[216:217], a[244:245], 0\n"
      "v_fmac_f32_e64 v31, s45, v43\n"
      "v_mfma_f32_4x4x4_16b_bf16 v[24:27], a[216:217], a[246:247], 0\n"
      "v_fma_f32 v40, s47, v40, -v76\n"
      "v_fma_f32 v41, s47, v41, -v77\n"
      "v_mfma_f32_16x16x32_bf16 v[56:59], v[220:223], a[212:215], v[56:59]\n"
      "v_fma_f32 v42, s47, v42, -v78\n"
      "v_fma_f32 v43, s47, v43, -v79\n"
      "v_cvt_pk_bf16_f32 v20, v20, v24\n"
      "v_mfma_f32_16x16x32_bf16 v[60:63], v[212:215], a[212:215], v[60:63]\n"
      "ds_write_addtid_b32 v20 offset:3072\n"
      "ds_read2st64_b32 a[220:221], v12 offset0:181 offset1:183\n"
      "v_mfma_f32_16x16x32_bf16 v[64:67], v[224:227], a[192:195], v[64:67]\n"
      "ds_read_b128 a[208:211], v1 offset:20480\n"
      "ds_read2st64_b32 a[222:223], v15 offset0:181 offset1:183\n"
      "v_mfma_f32_16x16x32_bf16 v[68:71], v[216:219], a[192:195], v[68:71]\n"
      "v_cvt_pk_bf16_f32 v28, v28, v29\n"
      "v_cvt_pk_bf16_f32 v21, v21, v25\n"
      "v_mfma_f32_16x16x32_bf16 v[72:75], v[208:211], a[192:195], v[72:75]\n"
      "s_waitcnt lgkmcnt(4)\n"
      "ds_write_addtid_b32 v28 offset:32768\n"
      "ds_write_addtid_b32 v21 offset:7168\n"
      "v_cvt_pk_bf16_f32 v30, v30, v31\n"
      "v_mfma_f32_16x16x32_bf16 v[52:55], v[204:207], a[196:199], v[52:55]\n"
      "s_nop 0\n"
      "ds_read_b128 a[212:215], v1 offset:16448\n"
      "ds_write_addtid_b32 v30 offset:33280\n"
      "v_cvt_pk_bf16_f32 v40, v40, v41\n"
      "v_mfma_f32_16x16x32_bf16 v[56:59], v[196:199], a[196:199], v[56:59]\n"
      "ds_write_addtid_b32 v40 offset:34816\n"
      "v_cvt_pk_bf16_f32 v42, v42, v43\n"
      "v_mfma_f32_16x16x32_bf16 v[60:63], v[188:191], a[196:199], v[60:63]\n"
      "ds_write_addtid_b32 v42 offset:35328\n"
      "v_fmac_f32 v44, s46, v32\n"
      "v_cvt_pk_bf16_f32 v22, v22, v26\n"
      "v_mfma_f32_16x16x32_bf16 v[64:67], v[200:203], a[200:203], v[64:67]\n"
      "s_nop 0\n"
      "ds_read_b128 a[192:195], v1 offset:20544\n"
      "ds_write_addtid_b32 v22 offset:11264\n"
      "v_mfma_f32_16x16x32_bf16 v[68:71], v[192:195], a[200:203], v[68:71]\n"
      "v_fmac_f32 v45, s46, v33\n"
      "v_fmac_f32 v46, s46, v34\n"
      "v_mfma_f32_16x16x32_bf16 v[72:75], v[184:187], a[200:203], v[72:75]\n"
      "v_fmac_f32 v47, s46, v35\n"
      "v_cvt_pk_bf16_f32 v23, v23, v27\n"
      "v_mfma_f32_16x16x32_bf16 v[52:55], v[180:183], a[204:207], v[52:55]\n"
      "s_nop 0\n"
      "ds_read_b128 a[196:199], v1 offset:16512\n"
      "ds_write_addtid_b32 v23 offset:15360\n"
      "v_fma_f32 v32, 0.5, v32, v104\n"
      "v_mfma_f32_16x16x32_bf16 v[56:59], v[172:175], a[204:207], v[56:59]\n"
      "s_nop 0\n"
      "ds_read2st64_b32 a[224:225], v13 offset0:181 offset1:183\n"
      "ds_read2st64_b32 a[226:227], v16 offset0:181 offset1:183\n"
      "v_mfma_f32_16x16x32_bf16 v[60:63], v[164:167], a[204:207], v[60:63]\n"
      "s_waitcnt lgkmcnt(6)\n"
      "v_fma_f32 v33, 0.5, v33, v105\n"
      "v_fma_f32 v34, 0.5, v34, v106\n"
      "v_mfma_f32_16x16x32_bf16 v[64:67], v[176:179], a[208:211], v[64:67]\n"
      "ds_read_b128 a[200:203], v1 offset:20608\n"
      "v_fma_f32 v35, 0.5, v35, v107\n"
      "v_fmac_f32 v44, s48, v80\n"
      "v_mfma_f32_16x16x32_bf16 v[68:71], v[168:171], a[208:211], v[68:71]\n"
      "s_nop 0\n"
      "buffer_store_dwordx4 a[220:223], v18, s[8:11], s58 offen offset:0 sc1\n"
      "v_mfma_f32_16x16x32_bf16 v[72:75], v[160:163], a[208:211], v[72:75]\n"
      "v_fmac_f32 v45, s48, v81\n"
      "v_fmac_f32 v46, s48, v82\n"
      "v_mfma_f32_16x16x32_bf16 v[52:55], v[156:159], a[212:215], v[52:55]\n"
      "ds_read_b128 a[204:207], v2 offset:16384\n"
      "v_fmac_f32 v47, s48, v83\n"
      "v_mfma_f32_16x16x32_bf16 v[56:59], v[148:151], a[212:215], v[56:59]\n"
      "v_fmac_f32 v32, s45, v44\n"
      "v_fmac_f32 v33, s45, v45\n"
      "v_mfma_f32_16x16x32_bf16 v[60:63], v[140:143], a[212:215], v[60:63]\n"
      "s_waitcnt lgkmcnt(1)\n"
      "v_fmac_f32 v34, s45, v46\n"
      "v_fmac_f32 v35, s45, v47\n"
      "v_mfma_f32_16x16x32_bf16 v[64:67], v[152:155], a[192:195], v[64:67]\n"
      "ds_read_b128 a[208:211], v2 offset:20480\n"
      "ds_read_b64_tr_b16 a[248:249], v7 offset:37376\n"
      "v_mfma_f32_16x16x32_bf16 v[68:71], v[144:147], a[192:195], v[68:71]\n"
      "ds_read_b64_tr_b16 a[250:251], v7 offset:37384\n"
      "v_fma_f32 v44, s47, v44, -v80\n"
      "v_mfma_f32_16x16x32_bf16 v[72:75], v[136:139], a[192:195], v[72:75]\n"
      "v_fma_f32 v45, s47, v45, -v81\n"
      "v_fma_f32 v46, s47, v46, -v82\n"
      "v_mfma_f32_16x16x32_bf16 v[52:55], v[132:135], a[196:199], v[52:55]\n"
      "ds_read_b128 a[212:215], v2 offset:16448\n"
      "v_fma_f32 v47, s47, v47, -v83\n"
      "v_mfma_f32_16x16x32_bf16 v[56:59], v[124:127], a[196:199], v[56:59]\n"
      "v_cvt_pk_bf16_f32 v32, v32, v33\n"
      "v_cvt_pk_bf16_f32 v34, v34, v35\n"
      "v_mfma_f32_16x16x32_bf16 v[60:63], a[184:187], a[196:199], v[60:63]\n"
      "ds_write_addtid_b32 v32 offset:40964\n"
      "ds_write_addtid_b32 v34 offset:41476\n"
      "v_cvt_pk_bf16_f32 v44, v44, v45\n"
      "v_mfma_f32_16x16x32_bf16 v[64:67], v[128:131], a[200:203], v[64:67]\n"
      "buffer_store_dwordx4 a[224:227], v18, s[8:11], s58 offen offset:128 sc1\n"
      "ds_read_b128 a[192:195], v2 offset:20544\n"
      "v_mfma_f32_16x16x32_bf16 v[68:71], a[188:191], a[200:203], v[68:71]\n"
      "s_waitcnt lgkmcnt(3)\n"
      "ds_write_addtid_b32 v44 offset:43012\n"
      "ds_read2st64_b32 a[228:229], v14 offset0:181 offset1:183\n"
      "v_cvt_pk_bf16_f32 v46, v46, v47\n"
      "v_fmac_f32 v48, s46, v36\n"
      "v_mfma_f32_4x4x4_16b_bf16 v[20:23], a[216:217], a[248:249], 0\n"
      "v_fmac_f32_e64 v49, s46, v37\n"
      "v_mfma_f32_4x4x4_16b_bf16 v[24:27], a[216:217], a[250:251], 0\n"
      "ds_write_addtid_b32 v46 offset:43524\n"
      "ds_read2st64_b32 a[230:231], v17 offset0:181 offset1:183\n"
      "v_fmac_f32 v50, s46, v38\n"
      "v_mfma_f32_16x16x32_bf16 v[72:75], a[180:183], a[200:203], v[72:75]\n"
      "v_fmac_f32 v51, s46, v39\n"
      "v_cvt_pk_bf16_f32 v20, v20, v24\n"
      "v_mfma_f32_16x16x32_bf16 v[52:55], a[176:179], a[204:207], v[52:55]\n"
      "ds_read_b128 a[196:199], v2 offset:16512\n"
      "ds_write_addtid_b32 v20 offset:3584\n"
      "v_mfma_f32_16x16x32_bf16 v[56:59], a[168:171], a[204:207], v[56:59]\n"
      "v_fma_f32 v36, 0.5, v36, v108\n"
      "v_fma_f32 v37, 0.5, v37, v109\n"
      "v_cvt_pk_bf16_f32 v21, v21, v25\n"
      "v_mfma_f32_16x16x32_bf16 v[60:63], a[160:163], a[204:207], v[60:63]\n"
      "ds_write_addtid_b32 v21 offset:7680\n"
      "v_fma_f32 v38, 0.5, v38, v110\n"
      "v_mfma_f32_16x16x32_bf16 v[64:67], a[172:175], a[208:211], v[64:67]\n"
      "ds_read_b128 a[200:203], v2 offset:20608\n"
      "v_fma_f32 v39, 0.5, v39, v111\n"
      "v_mfma_f32_16x16x32_bf16 v[68:71], a[164:167], a[208:211], v[68:71]\n"
      "v_fmac_f32 v48, s48, v84\n"
      "v_cvt_pk_bf16_f32 v22, v22, v26\n"
      "v_mfma_f32_16x16x32_bf16 v[72:75], a[156:159], a[208:211], v[72:75]\n"
      "ds_write_addtid_b32 v22 offset:11776\n"
      "ds_read2st64_b32 a[232:233], v12 offset0:4 offset1:6\n"
      "v_fmac_f32 v49, s48, v85\n"
      "v_mfma_f32_16x16x32_bf16 v[52:55], a[152:155], a[212:215], v[52:55]\n"
      "ds_read_b128 a[204:207], v3 offset:16896\n"
      "ds_read2st64_b32 a[234:235], v15 offset0:4 offset1:6\n"
      "v_mfma_f32_16x16x32_bf16 v[56:59], a[144:147], a[212:215], v[56:59]\n"
      "v_fmac_f32 v50, s48, v86\n"
      "v_fmac_f32 v51, s48, v87\n"
      "v_mfma_f32_16x16x32_bf16 v[60:63], a[136:139], a[212:215], v[60:63]\n"
      "s_waitcnt lgkmcnt(4)\n"
      "v_fmac_f32 v36, s45, v48\n"
      "v_cvt_pk_bf16_f32 v23, v23, v27\n"
      "v_mfma_f32_16x16x32_bf16 v[64:67], a[148:151], a[192:195], v[64:67]\n"
      "ds_read_b128 a[208:211], v3 offset:16960\n"
      "ds_write_addtid_b32 v23 offset:15872\n"
      "v_mfma_f32_16x16x32_bf16 v[68:71], a[140:143], a[192:195], v[68:71]\n"
      "v_fmac_f32 v37, s45, v49\n"
      "v_fmac_f32 v38, s45, v50\n"
      "v_mfma_f32_16x16x32_bf16 v[72:75], a[132:135], a[192:195], v[72:75]\n"
      "buffer_store_dwordx4 a[228:231], v18, s[8:11], s58 offen offset:256 sc1\n"
      "v_mfma_f32_16x16x32_bf16 v[52:55], a[128:131], a[196:199], v[52:55]\n"
      "ds_read_b128 a[212:215], v3 offset:17024\n"
      "v_fmac_f32 v39, s45, v51\n"
      "v_mfma_f32_16x16x32_bf16 v[56:59], a[120:123], a[196:199], v[56:59]\n"
      "ds_read2st64_b32 a[236:237], v13 offset0:4 offset1:6\n"
      "ds_read2st64_b32 a[238:239], v16 offset0:4 offset1:6\n"
      "v_fma_f32 v48, s47, v48, -v84\n"
      "v_mfma_f32_16x16x32_bf16 v[60:63], a[112:115], a[196:199], v[60:63]\n"
      "s_nop 0\n"
      "v_fma_f32 v49, s47, v49, -v85\n"
      "v_fma_f32 v50, s47, v50, -v86\n"
      "v_mfma_f32_16x16x32_bf16 v[64:67], a[124:127], a[200:203], v[64:67]\n"
      "ds_read_b128 a[192:195], v4 offset:16896\n"
      "v_fma_f32 v51, s47, v51, -v87\n"
      "v_mfma_f32_16x16x32_bf16 v[68:71], a[116:119], a[200:203], v[68:71]\n"
      "s_nop 0\n"
      "v_cvt_pk_bf16_f32 v36, v36, v37\n"
      "v_cvt_pk_bf16_f32 v38, v38, v39\n"
      "v_mfma_f32_16x16x32_bf16 v[72:75], a[108:111], a[200:203], v[72:75]\n"
      "s_waitcnt lgkmcnt(3)\n"
      "ds_write_addtid_b32 v36 offset:36864\n"
      "ds_write_addtid_b32 v38 offset:37376\n"
      "v_add_f32 v52, v52, v64\n"
      "v_add_f32 v53, v53, v65\n"
      "v_mfma_f32_16x16x32_bf16 v[88:91], a[0:3], a[204:207], 0\n"
      "ds_read_b128 a[196:199], v4 offset:16960\n"
      "ds_write_addtid_b32 v52 offset:51456\n"
      "v_add_f32 v54, v54, v66\n"
      "v_mfma_f32_16x16x32_bf16 v[92:95], a[4:7], a[204:207], 0\n"
      "s_nop 0\n"
      "buffer_store_dwordx4 a[232:235], v18, s[8:11], s59 offen offset:0 sc1\n"
      "v_mfma_f32_16x16x32_bf16 v[96:99], a[8:11], a[204:207], 0\n"
      "ds_write_addtid_b32 v53 offset:51968\n"
      "ds_write_addtid_b32 v54 offset:52480\n"
      "v_mfma_f32_16x16x32_bf16 v[88:91], a[12:15], a[208:211], v[88:91]\n"
      "ds_read_b128 a[200:203], v4 offset:17024\n"
      "v_add_f32 v55, v55, v67\n"
      "v_mfma_f32_16x16x32_bf16 v[92:95], a[16:19], a[208:211], v[92:95]\n"
      "s_nop 0\n"
      "ds_write_addtid_b32 v55 offset:52992\n"
      "v_add_f32 v56, v56, v68\n"
      "v_cvt_pk_bf16_f32 v48, v48, v49\n"
      "v_mfma_f32_16x16x32_bf16 v[96:99], a[20:23], a[208:211], v[96:99]\n"
      "ds_write_addtid_b32 v48 offset:38912\n"
      "ds_write_addtid_b32 v56 offset:53504\n"
      "v_mfma_f32_16x16x32_bf16 v[88:91], a[24:27], a[212:215], v[88:91]\n"
      "ds_read_b128 a[204:207], v5 offset:16896\n"
      "v_add_f32 v57, v57, v69\n"
      "v_mfma_f32_16x16x32_bf16 v[92:95], a[28:31], a[212:215], v[92:95]\n"
      "s_nop 0\n"
      "ds_write_addtid_b32 v57 offset:54016\n"
      "v_add_f32_e64 v58, v58, v70\n"
      "v_add_f32_e64 v59, v59, v71\n"
      "v_mfma_f32_16x16x32_bf16 v[96:99], a[32:35], a[212:215], v[96:99]\n"
      "s_waitcnt lgkmcnt(4)\n"
      "ds_write_addtid_b32 v58 offset:54528\n"
      "ds_write_addtid_b32 v59 offset:55040\n"
      "v_mfma_f32_16x16x32_bf16 v[88:91], a[36:39], a[192:195], v[88:91]\n"
      "s_nop 0\n"
      "buffer_store_dwordx4 a[236:239], v18, s[8:11], s59 offen offset:128 sc1\n"
      "ds_read_b128 a[208:211], v5 offset:16960\n"
      "v_cvt_pk_bf16_f32 v50, v50, v51\n"
      "v_mfma_f32_16x16x32_bf16 v[92:95], a[40:43], a[192:195], v[92:95]\n"
      "s_nop 0\n"
      "ds_write_addtid_b32 v50 offset:39424\n"
      "v_add_f32_e64 v60, v60, v72\n"
      "v_mfma_f32_16x16x32_bf16 v[96:99], a[44:47], a[192:195], v[96:99]\n"
      "s_nop 0\n"
      "ds_write_addtid_b32 v60 offset:55552\n"
      "v_add_f32_e64 v61, v61, v73\n"
      "v_add_f32_e64 v62, v62, v74\n"
      "v_add_f32_e64 v63, v63, v75\n"
      "v_mfma_f32_16x16x32_bf16 v[88:91], a[48:51], a[196:199], v[88:91]\n"
      "s_nop 0\n"
      "ds_read_b128 a[212:215], v5 offset:17024\n"
      "ds_write_addtid_b32 v61 offset:56064\n"
      "v_mfma_f32_16x16x32_bf16 v[92:95], a[52:55], a[196:199], v[92:95]\n"
      "s_nop 0\n"
      "ds_write_addtid_b32 v62 offset:56576\n"
      "ds_write_addtid_b32 v63 offset:57088\n"
      "v_mfma_f32_16x16x32_bf16 v[96:99], a[56:59], a[196:199], v[96:99]\n"
      "s_nop 0\n"
      "v_fma_f32 v40, -2.0, v64, v52\n"
      "v_fma_f32 v41, -2.0, v65, v53\n"
      "v_mfma_f32_16x16x32_bf16 v[88:91], a[60:63], a[200:203], v[88:91]\n"
      "v_fma_f32 v42, -2.0, v66, v54\n"
      "v_fma_f32 v43, -2.0, v67, v55\n"
      "v_mfma_f32_16x16x32_bf16 v[92:95], a[64:67], a[200:203], v[92:95]\n"
      "s_waitcnt vmcnt(14)\n"
      "v_fma_f32 v44, -2.0, v68, v56\n"
      "v_fma_f32 v45, -2.0, v69, v57\n"
      "v_mfma_f32_16x16x32_bf16 v[96:99], a[68:71], a[200:203], v[96:99]\n"
      "s_waitcnt lgkmcnt(0)\n"
      "s_barrier\n"
      "v_mfma_f32_16x16x32_bf16 v[88:91], a[72:75], a[204:207], v[88:91]\n"
      "s_add_u32 s62, s62, 786432\n"
      "ds_read_b128 a[192:195], v0 offset:0\n"
      "ds_read2st64_b32 v[28:29], v6 offset0:24 offset1:26\n"
      "v_fma_f32 v46, -2.0, v70, v58\n"
      "v_mfma_f32_16x16x32_bf16 v[92:95], a[76:79], a[204:207], v[92:95]\n"
      "s_add_u32 s58, s62, 0\n"
      "ds_read_b128 a[196:199], v0 offset:4096\n"
      "ds_read2st64_b32 v[30:31], v6 offset0:28 offset1:30\n"
      "v_mfma_f32_16x16x32_bf16 v[96:99], a[80:83], a[204:207], v[96:99]\n"
      "s_add_u32 s59, s58, 196608\n"
      "ds_read2st64_b32 v[32:33], v6 offset0:32 offset1:34\n"
      "ds_read2st64_b32 v[34:35], v6 offset0:36 offset1:38\n"
      "v_mfma_f32_16x16x32_bf16 v[88:91], a[84:87], a[208:211], v[88:91]\n"
      "ds_read_b128 a[200:203], v0 offset:64\n"
      "v_fma_f32 v47, -2.0, v71, v59\n"
      "v_mfma_f32_16x16x32_bf16 v[92:95], a[88:91], a[208:211], v[92:95]\n"
      "ds_read2st64_b32 v[36:37], v6 offset0:40 offset1:42\n"
      "v_fma_f32 v48, -2.0, v72, v60\n"
      "v_fma_f32 v49, -2.0, v73, v61\n"
      "v_mfma_f32_16x16x32_bf16 v[96:99], a[92:95], a[208:211], v[96:99]\n"
      "ds_read2st64_b32 v[38:39], v6 offset0:44 offset1:46\n"
      "v_fma_f32 v50, -2.0, v74, v62\n"
      "v_mfma_f32_16x16x32_bf16 v[88:91], a[96:99], a[212:215], v[88:91]\n"
      "ds_read_b128 a[204:207], v0 offset:4160\n"
      "v_fma_f32 v51, -2.0, v75, v63\n"
      "v_mfma_f32_16x16x32_bf16 v[92:95], a[100:103], a[212:215], v[92:95]\n"
      "v_mfma_f32_16x16x32_bf16 v[96:99], a[104:107], a[212:215], v[96:99]\n"
      "s_waitcnt lgkmcnt(6)\n"
      "ds_read_b64_tr_b16 a[244:245], v7 offset:53760\n"
      "ds_read_b64_tr_b16 a[246:247], v7 offset:53768\n"
      "v_fmac_f32 v40, s46, v28\n"
      "v_fmac_f32 v41, s46, v29\n"
      "v_fmac_f32 v42, s46, v30\n"
      "v_mfma_f32_16x16x32_bf16 v[52:55], v[252:255], a[192:195], 0\n"
      "ds_read_b128 a[208:211], v0 offset:128\n"
      "v_fmac_f32 v43, s46, v31\n"
      "v_mfma_f32_16x16x32_bf16 v[56:59], v[244:247], a[192:195], 0\n"
      "v_fma_f32 v28, 0.5, v28, v112\n"
      "v_fma_f32 v29, 0.5, v29, v113\n"
      "v_mfma_f32_16x16x32_bf16 v[60:63], v[236:239], a[192:195], 0\n"
      "s_nop 0\n"
      "v_fma_f32 v30, 0.5, v30, v114\n"
      "v_fma_f32 v31, 0.5, v31, v115\n"
      "v_fmac_f32 v40, s48, v88\n"
      "v_mfma_f32_16x16x32_bf16 v[64:67], v[248:251], a[196:199], 0\n"
      "ds_read_b128 a[212:215], v0 offset:4224\n"
      "v_fmac_f32 v41, s48, v89\n"
      "v_mfma_f32_16x16x32_bf16 v[68:71], v[240:243], a[196:199], 0\n"
      "v_fmac_f32 v42, s48, v90\n"
      "v_fmac_f32 v43, s48, v91\n"
      "v_mfma_f32_16x16x32_bf16 v[72:75], v[232:235], a[196:199], 0\n"
      "s_waitcnt lgkmcnt(2)\n"
      "v_fmac_f32 v28, s45, v40\n"
      "v_fmac_f32 v29, s45, v41\n"
      "v_mfma_f32_16x16x32_bf16 v[52:55], v[228:231], a[200:203], v[52:55]\n"
      "ds_read_b128 a[192:195], v1 offset:0\n"
      "v_fmac_f32 v30, s45, v42\n"
      "v_mfma_f32_4x4x4_16b_bf16 v[20:23], a[216:217], a[244:245], 0\n"
      "v_fmac_f32_e64 v31, s45, v43\n"
      "v_mfma_f32_4x4x4_16b_bf16 v[24:27], a[216:217], a[246:247], 0\n"
      "v_fma_f32 v40, s47, v40, -v88\n"
      "v_fma_f32 v41, s47, v41, -v89\n"
      "v_mfma_f32_16x16x32_bf16 v[56:59], v[220:223], a[200:203], v[56:59]\n"
      "v_fma_f32 v42, s47, v42, -v90\n"
      "v_fma_f32 v43, s47, v43, -v91\n"
      "v_cvt_pk_bf16_f32 v20, v20, v24\n"
      "v_mfma_f32_16x16x32_bf16 v[60:63], v[212:215], a[200:203], v[60:63]\n"
      "ds_write_addtid_b32 v20 offset:19456\n"
      "ds_read2st64_b32 a[220:221], v12 offset0:177 offset1:179\n"
      "v_mfma_f32_16x16x32_bf16 v[64:67], v[224:227], a[204:207], v[64:67]\n"
      "ds_read_b128 a[196:199], v1 offset:4096\n"
      "ds_read2st64_b32 a[222:223], v15 offset0:177 offset1:179\n"
      "v_mfma_f32_16x16x32_bf16 v[68:71], v[216:219], a[204:207], v[68:71]\n"
      "s_nop 0\n"
      "v_cvt_pk_bf16_f32 v28, v28, v29\n"
      "v_cvt_pk_bf16_f32 v21, v21, v25\n"
      "v_mfma_f32_16x16x32_bf16 v[72:75], v[208:211], a[204:207], v[72:75]\n"
      "s_waitcnt lgkmcnt(4)\n"
      "ds_write_addtid_b32 v28 offset:33792\n"
      "ds_write_addtid_b32 v21 offset:23552\n"
      "v_cvt_pk_bf16_f32 v30, v30, v31\n"
      "v_mfma_f32_16x16x32_bf16 v[52:55], v[204:207], a[208:211], v[52:55]\n"
      "ds_read_b128 a[200:203], v1 offset:64\n"
      "ds_write_addtid_b32 v30 offset:34304\n"
      "v_cvt_pk_bf16_f32 v40, v40, v41\n"
      "v_mfma_f32_16x16x32_bf16 v[56:59], v[196:199], a[208:211], v[56:59]\n"
      "ds_write_addtid_b32 v40 offset:35840\n"
      "v_cvt_pk_bf16_f32 v42, v42, v43\n"
      "v_mfma_f32_16x16x32_bf16 v[60:63], v[188:191], a[208:211], v[60:63]\n"
      "ds_write_addtid_b32 v42 offset:36352\n"
      "v_fmac_f32 v44, s46, v32\n"
      "v_cvt_pk_bf16_f32 v22, v22, v26\n"
      "v_mfma_f32_16x16x32_bf16 v[64:67], v[200:203], a[212:215], v[64:67]\n"
      "s_nop 0\n"
      "ds_read_b128 a[204:207], v1 offset:4160\n"
      "ds_write_addtid_b32 v22 offset:27648\n"
      "v_mfma_f32_16x16x32_bf16 v[68:71], v[192:195], a[212:215], v[68:71]\n"
      "v_fmac_f32 v45, s46, v33\n"
      "v_fmac_f32 v46, s46, v34\n"
      "v_mfma_f32_16x16x32_bf16 v[72:75], v[184:187], a[212:215], v[72:75]\n"
      "v_fmac_f32 v47, s46, v35\n"
      "v_cvt_pk_bf16_f32 v23, v23, v27\n"
      "v_mfma_f32_16x16x32_bf16 v[52:55], v[180:183], a[192:195], v[52:55]\n"
      "s_nop 0\n"
      "ds_read_b128 a[208:211], v1 offset:128\n"
      "ds_write_addtid_b32 v23 offset:31744\n"
      "v_fma_f32 v32, 0.5, v32, v116\n"
      "v_mfma_f32_16x16x32_bf16 v[56:59], v[172:175], a[192:195], v[56:59]\n"
      "s_nop 0\n"
      "ds_read2st64_b32 a[224:225], v13 offset0:177 offset1:179\n"
      "ds_read2st64_b32 a[226:227], v16 offset0:177 offset1:179\n"
      "v_mfma_f32_16x16x32_bf16 v[60:63], v[164:167], a[192:195], v[60:63]\n"
      "s_waitcnt lgkmcnt(6)\n"
      "v_fma_f32 v33, 0.5, v33, v117\n"
      "v_fma_f32 v34, 0.5, v34, v118\n"
      "v_mfma_f32_16x16x32_bf16 v[64:67], v[176:179], a[196:199], v[64:67]\n"
      "ds_read_b128 a[212:215], v1 offset:4224\n"
      "v_fma_f32 v35, 0.5, v35, v119\n"
      "v_fmac_f32 v44, s48, v92\n"
      "v_mfma_f32_16x16x32_bf16 v[68:71], v[168:171], a[196:199], v[68:71]\n"
      "buffer_store_dwordx4 a[220:223], v18, s[8:11], s58 offen offset:0 sc1\n"
      "v_mfma_f32_16x16x32_bf16 v[72:75], v[160:163], a[196:199], v[72:75]\n"
      "v_fmac_f32 v45, s48, v93\n"
      "v_fmac_f32_e64 v46, s48, v94\n"
      "v_mfma_f32_16x16x32_bf16 v[52:55], v[156:159], a[200:203], v[52:55]\n"
      "ds_read_b128 a[192:195], v2 offset:0\n"
      "v_fmac_f32 v47, s48, v95\n"
      "v_mfma_f32_16x16x32_bf16 v[56:59], v[148:151], a[200:203], v[56:59]\n"
      "v_fmac_f32 v32, s45, v44\n"
      "v_fmac_f32 v33, s45, v45\n"
      "v_mfma_f32_16x16x32_bf16 v[60:63], v[140:143], a[200:203], v[60:63]\n"
      "s_waitcnt lgkmcnt(1)\n"
      "v_fmac_f32 v34, s45, v46\n"
      "v_fmac_f32 v35, s45, v47\n"
      "v_mfma_f32_16x16x32_bf16 v[64:67], v[152:155], a[204:207], v[64:67]\n"
      "ds_read_b128 a[196:199], v2 offset:4096\n"
      "ds_read_b64_tr_b16 a[248:249], v7 offset:54272\n"
      "v_mfma_f32_16x16x32_bf16 v[68:71], v[144:147], a[204:207], v[68:71]\n"
      "ds_read_b64_tr_b16 a[250:251], v7 offset:54280\n"
      "v_fma_f32 v44, s47, v44, -v92\n"
      "v_mfma_f32_16x16x32_bf16 v[72:75], v[136:139], a[204:207], v[72:75]\n"
      "v_fma_f32 v45, s47, v45, -v93\n"
      "v_fma_f32 v46, s47, v46, -v94\n"
      "v_mfma_f32_16x16x32_bf16 v[52:55], v[132:135], a[208:211], v[52:55]\n"
      "ds_read_b128 a[200:203], v2 offset:64\n"
      "v_fma_f32 v47, s47, v47, -v95\n"
      "v_mfma_f32_16x16x32_bf16 v[56:59], v[124:127], a[208:211], v[56:59]\n"
      "v_cvt_pk_bf16_f32 v32, v32, v33\n"
      "v_cvt_pk_bf16_f32 v34, v34, v35\n"
      "v_mfma_f32_16x16x32_bf16 v[60:63], a[184:187], a[208:211], v[60:63]\n"
      "ds_write_addtid_b32 v32 offset:41988\n"
      "ds_write_addtid_b32 v34 offset:42500\n"
      "v_cvt_pk_bf16_f32 v44, v44, v45\n"
      "v_mfma_f32_16x16x32_bf16 v[64:67], v[128:131], a[212:215], v[64:67]\n"
      "s_nop 0\n"
      "buffer_store_dwordx4 a[224:227], v18, s[8:11], s58 offen offset:128 sc1\n"
      "ds_read_b128 a[204:207], v2 offset:4160\n"
      "v_mfma_f32_16x16x32_bf16 v[68:71], a[188:191], a[212:215], v[68:71]\n"
      "s_waitcnt lgkmcnt(3)\n"
      "ds_write_addtid_b32 v44 offset:44036\n"
      "ds_read2st64_b32 a[228:229], v14 offset0:177 offset1:179\n"
      "v_cvt_pk_bf16_f32 v46, v46, v47\n"
      "v_fmac_f32 v48, s46, v36\n"
      "v_mfma_f32_4x4x4_16b_bf16 v[20:23], a[216:217], a[248:249], 0\n"
      "v_fmac_f32 v49, s46, v37\n"
      "v_mfma_f32_4x4x4_16b_bf16 v[24:27], a[216:217], a[250:251], 0\n"
      "ds_write_addtid_b32 v46 offset:44548\n"
      "ds_read2st64_b32 a[230:231], v17 offset0:177 offset1:179\n"
      "v_fmac_f32_e64 v50, s46, v38\n"
      "v_mfma_f32_16x16x32_bf16 v[72:75], a[180:183], a[212:215], v[72:75]\n"
      "v_fmac_f32 v51, s46, v39\n"
      "v_cvt_pk_bf16_f32 v20, v20, v24\n"
      "v_mfma_f32_16x16x32_bf16 v[52:55], a[176:179], a[192:195], v[52:55]\n"
      "ds_read_b128 a[208:211], v2 offset:128\n"
      "ds_write_addtid_b32 v20 offset:19968\n"
      "v_mfma_f32_16x16x32_bf16 v[56:59], a[168:171], a[192:195], v[56:59]\n"
      "s_nop 0\n"
      "v_fma_f32 v36, 0.5, v36, v120\n"
      "v_fma_f32 v37, 0.5, v37, v121\n"
      "v_cvt_pk_bf16_f32 v21, v21, v25\n"
      "v_mfma_f32_16x16x32_bf16 v[60:63], a[160:163], a[192:195], v[60:63]\n"
      "ds_write_addtid_b32 v21 offset:24064\n"
      "v_fma_f32 v38, 0.5, v38, v122\n"
      "v_mfma_f32_16x16x32_bf16 v[64:67], a[172:175], a[196:199], v[64:67]\n"
      "ds_read_b128 a[212:215], v2 offset:4224\n"
      "v_fma_f32 v39, 0.5, v39, v123\n"
      "v_mfma_f32_16x16x32_bf16 v[68:71], a[164:167], a[196:199], v[68:71]\n"
      "v_fmac_f32 v48, s48, v96\n"
      "v_cvt_pk_bf16_f32 v22, v22, v26\n"
      "v_mfma_f32_16x16x32_bf16 v[72:75], a[156:159], a[196:199], v[72:75]\n"
      "ds_write_addtid_b32 v22 offset:28160\n"
      "ds_read2st64_b32 a[232:233], v12 offset0:0 offset1:2\n"
      "v_fmac_f32 v49, s48, v97\n"
      "v_mfma_f32_16x16x32_bf16 v[52:55], a[152:155], a[200:203], v[52:55]\n"
      "ds_read_b128 a[192:195], v3 offset:33792\n"
      "ds_read2st64_b32 a[234:235], v15 offset0:0 offset1:2\n"
      "v_mfma_f32_16x16x32_bf16 v[56:59], a[144:147], a[200:203], v[56:59]\n"
      "v_fmac_f32 v50, s48, v98\n"
      "v_fmac_f32 v51, s48, v99\n"
      "v_mfma_f32_16x16x32_bf16 v[60:63], a[136:139], a[200:203], v[60:63]\n"
      "s_waitcnt lgkmcnt(4)\n"
      "v_fmac_f32 v36, s45, v48\n"
      "v_cvt_pk_bf16_f32 v23, v23, v27\n"
      "v_mfma_f32_16x16x32_bf16 v[64:67], a[148:151], a[204:207], v[64:67]\n"
      "ds_read_b128 a[196:199], v3 offset:33856\n"
      "ds_write_addtid_b32 v23 offset:32256\n"
      "v_mfma_f32_16x16x32_bf16 v[68:71], a[140:143], a[204:207], v[68:71]\n"
      "v_fmac_f32 v37, s45, v49\n"
      "v_fmac_f32 v38, s45, v50\n"
      "v_mfma_f32_16x16x32_bf16 v[72:75], a[132:135], a[204:207], v[72:75]\n"
      "buffer_store_dwordx4 a[228:231], v18, s[8:11], s58 offen offset:256 sc1\n"
      "v_mfma_f32_16x16x32_bf16 v[52:55], a[128:131], a[208:211], v[52:55]\n"
      "ds_read_b128 a[200:203], v3 offset:33920\n"
      "v_fmac_f32_e64 v39, s45, v51\n"
      "v_mfma_f32_16x16x32_bf16 v[56:59], a[120:123], a[208:211], v[56:59]\n"
      "ds_read2st64_b32 a[236:237], v13 offset0:0 offset1:2\n"
      "ds_read2st64_b32 a[238:239], v16 offset0:0 offset1:2\n"
      "v_fma_f32 v48, s47, v48, -v96\n"
      "v_mfma_f32_16x16x32_bf16 v[60:63], a[112:115], a[208:211], v[60:63]\n"
      "v_fma_f32 v49, s47, v49, -v97\n"
      "v_fma_f32 v50, s47, v50, -v98\n"
      "v_mfma_f32_16x16x32_bf16 v[64:67], a[124:127], a[212:215], v[64:67]\n"
      "ds_read_b128 a[204:207], v4 offset:33792\n"
      "v_fma_f32 v51, s47, v51, -v99\n"
      "v_mfma_f32_16x16x32_bf16 v[68:71], a[116:119], a[212:215], v[68:71]\n"
      "s_nop 0\n"
      "v_cvt_pk_bf16_f32 v36, v36, v37\n"
      "v_cvt_pk_bf16_f32 v38, v38, v39\n"
      "v_mfma_f32_16x16x32_bf16 v[72:75], a[108:111], a[212:215], v[72:75]\n"
      "s_waitcnt lgkmcnt(3)\n"
      "ds_write_addtid_b32 v36 offset:37888\n"
      "ds_write_addtid_b32 v38 offset:38400\n"
      "v_add_f32 v52, v52, v64\n"
      "v_add_f32 v53, v53, v65\n"
      "v_mfma_f32_16x16x32_bf16 v[100:103], a[0:3], a[192:195], 0\n"
      "ds_read_b128 a[208:211], v4 offset:33856\n"
      "ds_write_addtid_b32 v52 offset:45312\n"
      "v_add_f32 v54, v54, v66\n"
      "v_mfma_f32_16x16x32_bf16 v[104:107], a[4:7], a[192:195], 0\n"
      "s_nop 0\n"
      "buffer_store_dwordx4 a[232:235], v18, s[8:11], s59 offen offset:0 sc1\n"
      "v_mfma_f32_16x16x32_bf16 v[108:111], a[8:11], a[192:195], 0\n"
      "ds_write_addtid_b32 v53 offset:45824\n"
      "ds_write_addtid_b32 v54 offset:46336\n"
      "v_mfma_f32_16x16x32_bf16 v[100:103], a[12:15], a[196:199], v[100:103]\n"
      "ds_read_b128 a[212:215], v4 offset:33920\n"
      "v_add_f32 v55, v55, v67\n"
      "v_mfma_f32_16x16x32_bf16 v[104:107], a[16:19], a[196:199], v[104:107]\n"
      "ds_write_addtid_b32 v55 offset:46848\n"
      "v_add_f32 v56, v56, v68\n"
      "v_cvt_pk_bf16_f32 v48, v48, v49\n"
      "v_mfma_f32_16x16x32_bf16 v[108:111], a[20:23], a[196:199], v[108:111]\n"
      "ds_write_addtid_b32 v48 offset:39936\n"
      "ds_write_addtid_b32 v56 offset:47360\n"
      "v_mfma_f32_16x16x32_bf16 v[100:103], a[24:27], a[200:203], v[100:103]\n"
      "ds_read_b128 a[192:195], v5 offset:33792\n"
      "v_add_f32 v57, v57, v69\n"
      "v_mfma_f32_16x16x32_bf16 v[104:107], a[28:31], a[200:203], v[104:107]\n"
      "s_nop 0\n"
      "ds_write_addtid_b32 v57 offset:47872\n"
      "v_add_f32 v58, v58, v70\n"
      "v_add_f32 v59, v59, v71\n"
      "v_mfma_f32_16x16x32_bf16 v[108:111], a[32:35], a[200:203], v[108:111]\n"
      "s_waitcnt lgkmcnt(4)\n"
      "ds_write_addtid_b32 v58 offset:48384\n"
      "ds_write_addtid_b32 v59 offset:48896\n"
      "v_mfma_f32_16x16x32_bf16 v[100:103], a[36:39], a[204:207], v[100:103]\n"
      "s_nop 0\n"
      "buffer_store_dwordx4 a[236:239], v18, s[8:11], s59 offen offset:128 sc1\n"
      "ds_read_b128 a[196:199], v5 offset:33856\n"
      "v_cvt_pk_bf16_f32 v50, v50, v51\n"
      "v_mfma_f32_16x16x32_bf16 v[104:107], a[40:43], a[204:207], v[104:107]\n"
      "ds_write_addtid_b32 v50 offset:40448\n"
      "v_add_f32 v60, v60, v72\n"
      "v_mfma_f32_16x16x32_bf16 v[108:111], a[44:47], a[204:207], v[108:111]\n"
      "ds_write_addtid_b32 v60 offset:49408\n"
      "v_add_f32 v61, v61, v73\n"
      "v_add_f32 v62, v62, v74\n"
      "v_add_f32 v63, v63, v75\n"
      "v_mfma_f32_16x16x32_bf16 v[100:103], a[48:51], a[208:211], v[100:103]\n"
      "ds_read_b128 a[200:203], v5 offset:33920\n"
      "ds_write_addtid_b32 v61 offset:49920\n"
      "v_mfma_f32_16x16x32_bf16 v[104:107], a[52:55], a[208:211], v[104:107]\n"
      "ds_write_addtid_b32 v62 offset:50432\n"
      "ds_write_addtid_b32 v63 offset:50944\n"
      "v_mfma_f32_16x16x32_bf16 v[108:111], a[56:59], a[208:211], v[108:111]\n"
      "v_fma_f32 v40, -2.0, v64, v52\n"
      "v_fma_f32 v41, -2.0, v65, v53\n"
      "v_mfma_f32_16x16x32_bf16 v[100:103], a[60:63], a[212:215], v[100:103]\n"
      "v_fma_f32 v42, -2.0, v66, v54\n"
      "v_fma_f32 v43, -2.0, v67, v55\n"
      "v_mfma_f32_16x16x32_bf16 v[104:107], a[64:67], a[212:215], v[104:107]\n"
      "v_fma_f32 v44, -2.0, v68, v56\n"
      "v_fma_f32 v45, -2.0, v69, v57\n"
      "v_mfma_f32_16x16x32_bf16 v[108:111], a[68:71], a[212:215], v[108:111]\n"
      "s_waitcnt lgkmcnt(0)\n"
      "s_barrier\n"
      "v_mfma_f32_16x16x32_bf16 v[100:103], a[72:75], a[192:195], v[100:103]\n"
      "s_add_u32 s58, s58, s40\n"
      "ds_read_b128 a[204:207], v0 offset:16384\n"
      "ds_read2st64_b32 v[28:29], v6 offset0:0 offset1:2\n"
      "v_fma_f32 v46, -2.0, v70, v58\n"
      "v_mfma_f32_16x16x32_bf16 v[104:107], a[76:79], a[192:195], v[104:107]\n"
      "s_nop 0\n"
      "ds_read_b128 a[208:211], v0 offset:20480\n"
      "ds_read2st64_b32 v[30:31], v6 offset0:4 offset1:6\n"
      "v_mfma_f32_16x16x32_bf16 v[108:111], a[80:83], a[192:195], v[108:111]\n"
      "ds_read2st64_b32 v[32:33], v6 offset0:8 offset1:10\n"
      "ds_read2st64_b32 v[34:35], v6 offset0:12 offset1:14\n"
      "v_mfma_f32_16x16x32_bf16 v[100:103], a[84:87], a[196:199], v[100:103]\n"
      "ds_read_b128 a[212:215], v0 offset:16448\n"
      "v_fma_f32 v47, -2.0, v71, v59\n"
      "v_mfma_f32_16x16x32_bf16 v[104:107], a[88:91], a[196:199], v[104:107]\n"
      "v_fma_f32 v48, -2.0, v72, v60\n"
      "v_fma_f32 v49, -2.0, v73, v61\n"
      "v_mfma_f32_16x16x32_bf16 v[108:111], a[92:95], a[196:199], v[108:111]\n"
      "ds_read2st64_b32 v[36:37], v6 offset0:16 offset1:18\n"
      "ds_read2st64_b32 v[38:39], v6 offset0:20 offset1:22\n"
      "v_mfma_f32_16x16x32_bf16 v[100:103], a[96:99], a[200:203], v[100:103]\n"
      "ds_read_b128 a[192:195], v0 offset:20544\n"
      "ds_read2st64_b32 a[220:221], v12 offset0:181 offset1:183\n"
      "v_mfma_f32_16x16x32_bf16 v[104:107], a[100:103], a[200:203], v[104:107]\n"
      "ds_read2st64_b32 a[222:223], v15 offset0:181 offset1:183\n"
      "v_fma_f32 v50, -2.0, v74, v62\n"
      "v_fma_f32 v51, -2.0, v75, v63\n"
      "v_mfma_f32_16x16x32_bf16 v[108:111], a[104:107], a[200:203], v[108:111]\n"
      "s_waitcnt lgkmcnt(8)\n"
      "v_fmac_f32 v40, s46, v28\n"
      "v_fmac_f32 v41, s46, v29\n"
      "v_mfma_f32_16x16x32_bf16 v[52:55], v[252:255], a[204:207], 0\n"
      "s_nop 0\n"
      "ds_read_b128 a[196:199], v0 offset:16512\n"
      "v_fmac_f32 v42, s46, v30\n"
      "v_mfma_f32_16x16x32_bf16 v[56:59], v[244:247], a[204:207], 0\n"
      "v_fmac_f32 v43, s46, v31\n"
      "v_fma_f32 v28, 0.5, v28, v76\n"
      "v_mfma_f32_16x16x32_bf16 v[60:63], v[236:239], a[204:207], 0\n"
      "v_fma_f32 v29, 0.5, v29, v77\n"
      "v_fma_f32 v30, 0.5, v30, v78\n"
      "v_mfma_f32_16x16x32_bf16 v[64:67], v[248:251], a[208:211], 0\n"
      "ds_read_b128 a[200:203], v0 offset:20608\n"
      "v_fma_f32 v31, 0.5, v31, v79\n"
      "v_mfma_f32_16x16x32_bf16 v[68:71], v[240:243], a[208:211], 0\n"
      "v_fmac_f32 v40, s48, v100\n"
      "v_fmac_f32 v41, s48, v101\n"
      "v_mfma_f32_16x16x32_bf16 v[72:75], v[232:235], a[208:211], 0\n"
      "s_waitcnt lgkmcnt(2)\n"
      "v_fmac_f32 v42, s48, v102\n"
      "v_fmac_f32 v43, s48, v103\n"
      "v_mfma_f32_16x16x32_bf16 v[52:55], v[228:231], a[212:215], v[52:55]\n"
      "s_nop 0\n"
      "ds_read_b128 a[204:207], v1 offset:16384\n"
      "v_fmac_f32 v28, s45, v40\n"
      "v_mfma_f32_16x16x32_bf16 v[56:59], v[220:223], a[212:215], v[56:59]\n"
      "v_fmac_f32 v29, s45, v41\n"
      "v_fmac_f32 v30, s45, v42\n"
      "v_mfma_f32_16x16x32_bf16 v[60:63], v[212:215], a[212:215], v[60:63]\n"
      "v_fmac_f32 v31, s45, v43\n"
      "v_fma_f32 v40, s47, v40, -v100\n"
      "v_mfma_f32_16x16x32_bf16 v[64:67], v[224:227], a[192:195], v[64:67]\n"
      "ds_read_b128 a[208:211], v1 offset:20480\n"
      "v_fma_f32 v41, s47, v41, -v101\n"
      "v_mfma_f32_16x16x32_bf16 v[68:71], v[216:219], a[192:195], v[68:71]\n"
      "v_fma_f32 v42, s47, v42, -v102\n"
      "v_fma_f32 v43, s47, v43, -v103\n"
      "v_mfma_f32_16x16x32_bf16 v[72:75], v[208:211], a[192:195], v[72:75]\n"
      "s_waitcnt lgkmcnt(1)\n"
      "buffer_store_dwordx4 a[220:223], v18, s[8:11], s58 offen offset:0 sc1\n"
      "v_mfma_f32_16x16x32_bf16 v[52:55], v[204:207], a[196:199], v[52:55]\n"
      "s_nop 0\n"
      "ds_read_b128 a[212:215], v1 offset:16448\n"
      "v_cvt_pk_bf16_f32 v28, v28, v29\n"
      "v_mfma_f32_16x16x32_bf16 v[56:59], v[196:199], a[196:199], v[56:59]\n"
      "ds_write_addtid_b32 v28 offset:32768\n"
      "ds_read2st64_b32 a[224:225], v13 offset0:181 offset1:183\n"
      "v_cvt_pk_bf16_f32 v30, v30, v31\n"
      "v_mfma_f32_16x16x32_bf16 v[60:63], v[188:191], a[196:199], v[60:63]\n"
      "ds_write_addtid_b32 v30 offset:33280\n"
      "ds_read2st64_b32 a[226:227], v16 offset0:181 offset1:183\n"
      "v_cvt_pk_bf16_f32 v40, v40, v41\n"
      "v_mfma_f32_16x16x32_bf16 v[64:67], v[200:203], a[200:203], v[64:67]\n"
      "ds_read_b128 a[192:195], v1 offset:20544\n"
      "ds_write_addtid_b32 v40 offset:34816\n"
      "v_mfma_f32_16x16x32_bf16 v[68:71], v[192:195], a[200:203], v[68:71]\n"
      "v_cvt_pk_bf16_f32 v42, v42, v43\n"
      "v_fmac_f32_e64 v44, s46, v32\n"
      "v_mfma_f32_16x16x32_bf16 v[72:75], v[184:187], a[200:203], v[72:75]\n"
      "ds_write_addtid_b32 v42 offset:35328\n"
      "v_fmac_f32 v45, s46, v33\n"
      "v_mfma_f32_16x16x32_bf16 v[52:55], v[180:183], a[204:207], v[52:55]\n"
      "ds_read_b128 a[196:199], v1 offset:16512\n"
      "v_fmac_f32 v46, s46, v34\n"
      "v_mfma_f32_16x16x32_bf16 v[56:59], v[172:175], a[204:207], v[56:59]\n"
      "v_fmac_f32_e64 v47, s46, v35\n"
      "v_fma_f32 v32, 0.5, v32, v80\n"
      "v_mfma_f32_16x16x32_bf16 v[60:63], v[164:167], a[204:207], v[60:63]\n"
      "s_waitcnt lgkmcnt(4)\n"
      "v_fma_f32 v33, 0.5, v33, v81\n"
      "v_fma_f32 v34, 0.5, v34, v82\n"
      "v_mfma_f32_16x16x32_bf16 v[64:67], v[176:179], a[208:211], v[64:67]\n"
      "ds_read_b128 a[200:203], v1 offset:20608\n"
      "v_fma_f32 v35, 0.5, v35, v83\n"
      "v_fmac_f32 v44, s48, v104\n"
      "v_mfma_f32_16x16x32_bf16 v[68:71], v[168:171], a[208:211], v[68:71]\n"
      "v_fmac_f32 v45, s48, v105\n"
      "v_fmac_f32 v46, s48, v106\n"
      "v_mfma_f32_16x16x32_bf16 v[72:75], v[160:163], a[208:211], v[72:75]\n"
      "v_fmac_f32 v47, s48, v107\n"
      "v_fmac_f32 v32, s45, v44\n"
      "v_mfma_f32_16x16x32_bf16 v[52:55], v[156:159], a[212:215], v[52:55]\n"
      "ds_read_b128 a[204:207], v2 offset:16384\n"
      "v_fmac_f32 v33, s45, v45\n"
      "v_mfma_f32_16x16x32_bf16 v[56:59], v[148:151], a[212:215], v[56:59]\n"
      "v_fmac_f32 v34, s45, v46\n"
      "v_fmac_f32_e64 v35, s45, v47\n"
      "v_mfma_f32_16x16x32_bf16 v[60:63], v[140:143], a[212:215], v[60:63]\n"
      "s_waitcnt lgkmcnt(1)\n"
      "v_fma_f32 v44, s47, v44, -v104\n"
      "v_fma_f32 v45, s47, v45, -v105\n"
      "v_mfma_f32_16x16x32_bf16 v[64:67], v[152:155], a[192:195], v[64:67]\n"
      "buffer_store_dwordx4 a[224:227], v18, s[8:11], s58 offen offset:128 sc1\n"
      "ds_read_b128 a[208:211], v2 offset:20480\n"
      "v_mfma_f32_16x16x32_bf16 v[68:71], v[144:147], a[192:195], v[68:71]\n"
      "s_nop 0\n"
      "v_fma_f32 v46, s47, v46, -v106\n"
      "v_fma_f32 v47, s47, v47, -v107\n"
      "v_mfma_f32_16x16x32_bf16 v[72:75], v[136:139], a[192:195], v[72:75]\n"
      "ds_read2st64_b32 a[228:229], v14 offset0:181 offset1:183\n"
      "ds_read2st64_b32 a[230:231], v17 offset0:181 offset1:183\n"
      "v_cvt_pk_bf16_f32 v32, v32, v33\n"
      "v_mfma_f32_16x16x32_bf16 v[52:55], v[132:135], a[196:199], v[52:55]\n"
      "ds_read_b128 a[212:215], v2 offset:16448\n"
      "ds_write_addtid_b32 v32 offset:40964\n"
      "v_mfma_f32_16x16x32_bf16 v[56:59], v[124:127], a[196:199], v[56:59]\n"
      "v_cvt_pk_bf16_f32 v34, v34, v35\n"
      "v_cvt_pk_bf16_f32 v44, v44, v45\n"
      "v_mfma_f32_16x16x32_bf16 v[60:63], a[184:187], a[196:199], v[60:63]\n"
      "ds_write_addtid_b32 v34 offset:41476\n"
      "ds_write_addtid_b32 v44 offset:43012\n"
      "v_mfma_f32_16x16x32_bf16 v[64:67], v[128:131], a[200:203], v[64:67]\n"
      "ds_read_b128 a[192:195], v2 offset:20544\n"
      "v_cvt_pk_bf16_f32 v46, v46, v47\n"
      "v_mfma_f32_16x16x32_bf16 v[68:71], a[188:191], a[200:203], v[68:71]\n"
      "ds_write_addtid_b32 v46 offset:43524\n"
      "v_fmac_f32 v48, s46, v36\n"
      "v_fmac_f32 v49, s46, v37\n"
      "v_mfma_f32_16x16x32_bf16 v[72:75], a[180:183], a[200:203], v[72:75]\n"
      "s_waitcnt lgkmcnt(4)\n"
      "v_fmac_f32 v50, s46, v38\n"
      "v_fmac_f32_e64 v51, s46, v39\n"
      "v_mfma_f32_16x16x32_bf16 v[52:55], a[176:179], a[204:207], v[52:55]\n"
      "ds_read_b128 a[196:199], v2 offset:16512\n"
      "v_fma_f32 v36, 0.5, v36, v84\n"
      "v_mfma_f32_16x16x32_bf16 v[56:59], a[168:171], a[204:207], v[56:59]\n"
      "v_fma_f32 v37, 0.5, v37, v85\n"
      "v_fma_f32 v38, 0.5, v38, v86\n"
      "v_mfma_f32_16x16x32_bf16 v[60:63], a[160:163], a[204:207], v[60:63]\n"
      "v_fma_f32 v39, 0.5, v39, v87\n"
      "v_fmac_f32 v48, s48, v108\n"
      "v_mfma_f32_16x16x32_bf16 v[64:67], a[172:175], a[208:211], v[64:67]\n"
      "ds_read_b128 a[200:203], v2 offset:20608\n"
      "v_fmac_f32 v49, s48, v109\n"
      "v_mfma_f32_16x16x32_bf16 v[68:71], a[164:167], a[208:211], v[68:71]\n"
      "v_fmac_f32 v50, s48, v110\n"
      "v_fmac_f32 v51, s48, v111\n"
      "v_mfma_f32_16x16x32_bf16 v[72:75], a[156:159], a[208:211], v[72:75]\n"
      "v_fmac_f32 v36, s45, v48\n"
      "v_fmac_f32 v37, s45, v49\n"
      "v_mfma_f32_16x16x32_bf16 v[52:55], a[152:155], a[212:215], v[52:55]\n"
      "ds_read_b128 a[204:207], v3 offset:50688\n"
      "v_fmac_f32 v38, s45, v50\n"
      "v_fmac_f32 v39, s45, v51\n"
      "v_mfma_f32_16x16x32_bf16 v[56:59], a[144:147], a[212:215], v[56:59]\n"
      "s_nop 0\n"
      "buffer_store_dwordx4 a[228:231], v18, s[8:11], s58 offen offset:256 sc1\n"
      "v_mfma_f32_16x16x32_bf16 v[60:63], a[136:139], a[212:215], v[60:63]\n"
      "s_waitcnt lgkmcnt(1)\n"
      "v_fma_f32 v48, s47, v48, -v108\n"
      "v_fma_f32 v49, s47, v49, -v109\n"
      "v_mfma_f32_16x16x32_bf16 v[64:67], a[148:151], a[192:195], v[64:67]\n"
      "ds_read_b128 a[208:211], v3 offset:50752\n"
      "v_fma_f32 v50, s47, v50, -v110\n"
      "v_mfma_f32_16x16x32_bf16 v[68:71], a[140:143], a[192:195], v[68:71]\n"
      "v_fma_f32 v51, s47, v51, -v111\n"
      "v_cvt_pk_bf16_f32 v36, v36, v37\n"
      "v_mfma_f32_16x16x32_bf16 v[72:75], a[132:135], a[192:195], v[72:75]\n"
      "ds_write_addtid_b32 v36 offset:36864\n"
      "v_cvt_pk_bf16_f32 v38, v38, v39\n"
      "v_mfma_f32_16x16x32_bf16 v[52:55], a[128:131], a[196:199], v[52:55]\n"
      "ds_read_b128 a[212:215], v3 offset:50816\n"
      "ds_write_addtid_b32 v38 offset:37376\n"
      "v_cvt_pk_bf16_f32 v48, v48, v49\n"
      "v_cvt_pk_bf16_f32 v50, v50, v51\n"
      "v_mfma_f32_16x16x32_bf16 v[56:59], a[120:123], a[196:199], v[56:59]\n"
      "ds_write_addtid_b32 v48 offset:38912\n"
      "ds_write_addtid_b32 v50 offset:39424\n"
      "v_mfma_f32_16x16x32_bf16 v[60:63], a[112:115], a[196:199], v[60:63]\n"
      "v_mfma_f32_16x16x32_bf16 v[64:67], a[124:127], a[200:203], v[64:67]\n"
      "ds_read_b128 a[192:195], v4 offset:50688\n"
      "v_mfma_f32_16x16x32_bf16 v[68:71], a[116:119], a[200:203], v[68:71]\n"
      "v_mfma_f32_16x16x32_bf16 v[72:75], a[108:111], a[200:203], v[72:75]\n"
      "s_waitcnt lgkmcnt(3)\n"
      "v_mfma_f32_16x16x32_bf16 v[112:115], a[0:3], a[204:207], 0\n"
      "s_nop 0\n"
      "ds_read_b128 a[196:199], v4 offset:50752\n"
      "v_mfma_f32_16x16x32_bf16 v[116:119], a[4:7], a[204:207], 0\n"
      "v_mfma_f32_16x16x32_bf16 v[120:123], a[8:11], a[204:207], 0\n"
      "v_add_f32 v52, v52, v64\n"
      "v_add_f32 v53, v53, v65\n"
      "v_add_f32 v54, v54, v66\n"
      "v_add_f32 v55, v55, v67\n"
      "v_add_f32 v56, v56, v68\n"
      "v_add_f32 v57, v57, v69\n"
      "v_add_f32 v58, v58, v70\n"
      "v_mfma_f32_16x16x32_bf16 v[112:115], a[12:15], a[208:211], v[112:115]\n"
      "s_nop 0\n"
      "ds_read_b128 a[200:203], v4 offset:50816\n"
      "ds_write_addtid_b32 v52 offset:51456\n"
      "ds_write_addtid_b32 v53 offset:51968\n"
      "v_add_f32 v59, v59, v71\n"
      "v_add_f32 v60, v60, v72\n"
      "v_add_f32 v61, v61, v73\n"
      "v_add_f32 v62, v62, v74\n"
      "v_mfma_f32_16x16x32_bf16 v[116:119], a[16:19], a[208:211], v[116:119]\n"
      "ds_write_addtid_b32 v54 offset:52480\n"
      "ds_write_addtid_b32 v55 offset:52992\n"
      "ds_write_addtid_b32 v56 offset:53504\n"
      "v_mfma_f32_16x16x32_bf16 v[120:123], a[20:23], a[208:211], v[120:123]\n"
      "ds_write_addtid_b32 v57 offset:54016\n"
      "ds_write_addtid_b32 v58 offset:54528\n"
      "ds_write_addtid_b32 v59 offset:55040\n"
      "v_add_f32 v63, v63, v75\n"
      "v_mfma_f32_16x16x32_bf16 v[112:115], a[24:27], a[212:215], v[112:115]\n"
      "s_nop 0\n"
      "ds_write_addtid_b32 v60 offset:55552\n"
      "ds_write_addtid_b32 v61 offset:56064\n"
      "v_mfma_f32_16x16x32_bf16 v[116:119], a[28:31], a[212:215], v[116:119]\n"
      "s_waitcnt vmcnt(16)\n"
      "ds_write_addtid_b32 v62 offset:56576\n"
      "ds_write_addtid_b32 v63 offset:57088\n"
      "v_mfma_f32_16x16x32_bf16 v[120:123], a[32:35], a[212:215], v[120:123]\n"
      "s_waitcnt lgkmcnt(0)\n"
      "s_barrier\n"
      "v_mfma_f32_16x16x32_bf16 v[112:115], a[36:39], a[192:195], v[112:115]\n"
      "s_sub_u32 s58, s58, s40\n"
      "ds_read_b128 a[204:207], v5 offset:50688\n"
      "ds_read2st64_b32 v[28:29], v6 offset0:24 offset1:26\n"
      "v_mfma_f32_16x16x32_bf16 v[116:119], a[40:43], a[192:195], v[116:119]\n"
      "s_add_u32 s58, s58, 786432\n"
      "ds_read2st64_b32 v[30:31], v6 offset0:28 offset1:30\n"
      "ds_read2st64_b32 v[32:33], v6 offset0:32 offset1:34\n"
      "v_mfma_f32_16x16x32_bf16 v[120:123], a[44:47], a[192:195], v[120:123]\n"
      "ds_read2st64_b32 v[34:35], v6 offset0:36 offset1:38\n"
      "ds_read2st64_b32 v[36:37], v6 offset0:40 offset1:42\n"
      "v_mfma_f32_16x16x32_bf16 v[112:115], a[48:51], a[196:199], v[112:115]\n"
      "ds_read_b128 a[208:211], v5 offset:50752\n"
      "ds_read2st64_b32 v[38:39], v6 offset0:44 offset1:46\n"
      "v_mfma_f32_16x16x32_bf16 v[116:119], a[52:55], a[196:199], v[116:119]\n"
      "v_fma_f32 v40, -2.0, v64, v52\n"
      "v_fma_f32 v41, -2.0, v65, v53\n"
      "v_mfma_f32_16x16x32_bf16 v[120:123], a[56:59], a[196:199], v[120:123]\n"
      "ds_read2st64_b32 a[220:221], v12 offset0:177 offset1:179\n"
      "ds_read2st64_b32 a[222:223], v15 offset0:177 offset1:179\n"
      "v_mfma_f32_16x16x32_bf16 v[112:115], a[60:63], a[200:203], v[112:115]\n"
      "ds_read_b128 a[212:215], v5 offset:50816\n"
      "v_fma_f32 v42, -2.0, v66, v54\n"
      "v_mfma_f32_16x16x32_bf16 v[116:119], a[64:67], a[200:203], v[116:119]\n"
      "ds_read2st64_b32 a[224:225], v13 offset0:177 offset1:179\n"
      "ds_read2st64_b32 a[226:227], v16 offset0:177 offset1:179\n"
      "v_mfma_f32_16x16x32_bf16 v[120:123], a[68:71], a[200:203], v[120:123]\n"
      "s_waitcnt lgkmcnt(9)\n"
      "v_fma_f32 v43, -2.0, v67, v55\n"
      "v_fma_f32 v44, -2.0, v68, v56\n"
      "v_mfma_f32_16x16x32_bf16 v[112:115], a[72:75], a[204:207], v[112:115]\n"
      "s_waitcnt lgkmcnt(3)\n"
      "buffer_store_dwordx4 a[220:223], v18, s[8:11], s58 offen offset:0 sc1\n"
      "v_mfma_f32_16x16x32_bf16 v[116:119], a[76:79], a[204:207], v[116:119]\n"
      "v_fma_f32 v45, -2.0, v69, v57\n"
      "v_fma_f32 v46, -2.0, v70, v58\n"
      "v_mfma_f32_16x16x32_bf16 v[120:123], a[80:83], a[204:207], v[120:123]\n"
      "ds_read2st64_b32 a[228:229], v14 offset0:177 offset1:179\n"
      "ds_read2st64_b32 a[230:231], v17 offset0:177 offset1:179\n"
      "v_mfma_f32_16x16x32_bf16 v[112:115], a[84:87], a[208:211], v[112:115]\n"
      "s_waitcnt lgkmcnt(2)\n"
      "buffer_store_dwordx4 a[224:227], v18, s[8:11], s58 offen offset:128 sc1\n"
      "v_mfma_f32_16x16x32_bf16 v[116:119], a[88:91], a[208:211], v[116:119]\n"
      "s_nop 0\n"
      "v_fma_f32 v47, -2.0, v71, v59\n"
      "v_fma_f32 v48, -2.0, v72, v60\n"
      "v_mfma_f32_16x16x32_bf16 v[120:123], a[92:95], a[208:211], v[120:123]\n"
      "s_nop 0\n"
      "v_fma_f32 v49, -2.0, v73, v61\n"
      "v_fma_f32 v50, -2.0, v74, v62\n"
      "v_mfma_f32_16x16x32_bf16 v[112:115], a[96:99], a[212:215], v[112:115]\n"
      "s_waitcnt lgkmcnt(0)\n"
      "buffer_store_dwordx4 a[228:231], v18, s[8:11], s58 offen offset:256 sc1\n"
      "v_mfma_f32_16x16x32_bf16 v[116:119], a[100:103], a[212:215], v[116:119]\n"
      "v_fma_f32 v51, -2.0, v75, v63\n"
      "v_fmac_f32 v40, s46, v28\n"
      "v_mfma_f32_16x16x32_bf16 v[120:123], a[104:107], a[212:215], v[120:123]\n"
      "v_fmac_f32 v41, s46, v29\n"
      "v_fmac_f32 v42, s46, v30\n"
      "v_fmac_f32 v43, s46, v31\n"
      "v_fma_f32 v28, 0.5, v28, v88\n"
      "v_fma_f32 v29, 0.5, v29, v89\n"
      "v_fma_f32 v30, 0.5, v30, v90\n"
      "v_fma_f32 v31, 0.5, v31, v91\n"
      "v_fmac_f32 v44, s46, v32\n"
      "v_fmac_f32 v45, s46, v33\n"
      "v_pk_fma_f32 v[46:47], v[34:35], s[46:47], v[46:47] op_sel:[0,0,0] op_sel_hi:[1,0,1]\n"
      "v_pk_fma_f32 v[32:33], v[32:33], s[48:49], v[92:93] op_sel:[0,1,0] op_sel_hi:[1,1,1]\n"
      "v_pk_fma_f32 v[34:35], v[34:35], s[48:49], v[94:95] op_sel:[0,1,0] op_sel_hi:[1,1,1]\n"
      "v_pk_fma_f32 v[48:49], v[36:37], s[46:47], v[48:49] op_sel:[0,0,0] op_sel_hi:[1,0,1]\n"
      "v_pk_fma_f32 v[50:51], v[38:39], s[46:47], v[50:51] op_sel:[0,0,0] op_sel_hi:[1,0,1]\n"
      "v_pk_fma_f32 v[36:37], v[36:37], s[48:49], v[96:97] op_sel:[0,1,0] op_sel_hi:[1,1,1]\n"
      "v_pk_fma_f32 v[38:39], v[38:39], s[48:49], v[98:99] op_sel:[0,1,0] op_sel_hi:[1,1,1]\n"
      "v_pk_fma_f32 v[40:41], v[112:113], s[48:49], v[40:41] op_sel:[0,0,0] op_sel_hi:[1,0,1]\n"
      "v_pk_fma_f32 v[42:43], v[114:115], s[48:49], v[42:43] op_sel:[0,0,0] op_sel_hi:[1,0,1]\n"
      "v_pk_fma_f32 v[28:29], v[40:41], s[44:45], v[28:29] op_sel:[0,1,0] op_sel_hi:[1,1,1]\n"
      "v_pk_fma_f32 v[30:31], v[42:43], s[44:45], v[30:31] op_sel:[0,1,0] op_sel_hi:[1,1,1]\n"
      "v_pk_fma_f32 v[40:41], v[40:41], s[46:47], v[112:113] op_sel:[0,1,0] op_sel_hi:[1,1,1] neg_lo:[0,0,1] neg_hi:[0,0,1]\n"
      "v_pk_fma_f32 v[42:43], v[42:43], s[46:47], v[114:115] op_sel:[0,1,0] op_sel_hi:[1,1,1] neg_lo:[0,0,1] neg_hi:[0,0,1]\n"
      "v_cvt_pk_bf16_f32 v28, v28, v29\n"
      "v_cvt_pk_bf16_f32 v30, v30, v31\n"
      "v_cvt_pk_bf16_f32 v40, v40, v41\n"
      "v_cvt_pk_bf16_f32 v42, v42, v43\n"
      "v_pk_fma_f32 v[44:45], v[116:117], s[48:49], v[44:45] op_sel:[0,0,0] op_sel_hi:[1,0,1]\n"
      "v_pk_fma_f32 v[46:47], v[118:119], s[48:49], v[46:47] op_sel:[0,0,0] op_sel_hi:[1,0,1]\n"
      "v_pk_fma_f32 v[32:33], v[44:45], s[44:45], v[32:33] op_sel:[0,1,0] op_sel_hi:[1,1,1]\n"
      "v_pk_fma_f32 v[34:35], v[46:47], s[44:45], v[34:35] op_sel:[0,1,0] op_sel_hi:[1,1,1]\n"
      "v_pk_fma_f32 v[44:45], v[44:45], s[46:47], v[116:117] op_sel:[0,1,0] op_sel_hi:[1,1,1] neg_lo:[0,0,1] neg_hi:[0,0,1]\n"
      "v_pk_fma_f32 v[46:47], v[46:47], s[46:47], v[118:119] op_sel:[0,1,0] op_sel_hi:[1,1,1] neg_lo:[0,0,1] neg_hi:[0,0,1]\n"
      "v_cvt_pk_bf16_f32 v32, v32, v33\n"
      "v_cvt_pk_bf16_f32 v34, v34, v35\n"
      "ds_write_addtid_b32 v28 offset:33792\n"
      "ds_write_addtid_b32 v30 offset:34304\n"
      "ds_write_addtid_b32 v40 offset:35840\n"
      "ds_write_addtid_b32 v42 offset:36352\n"
      "ds_write_addtid_b32 v32 offset:41988\n"
      "ds_write_addtid_b32 v34 offset:42500\n"
      "v_cvt_pk_bf16_f32 v44, v44, v45\n"
      "v_cvt_pk_bf16_f32 v46, v46, v47\n"
      "v_pk_fma_f32 v[48:49], v[120:121], s[48:49], v[48:49] op_sel:[0,0,0] op_sel_hi:[1,0,1]\n"
      "v_pk_fma_f32 v[50:51], v[122:123], s[48:49], v[50:51] op_sel:[0,0,0] op_sel_hi:[1,0,1]\n"
      "v_pk_fma_f32 v[36:37], v[48:49], s[44:45], v[36:37] op_sel:[0,1,0] op_sel_hi:[1,1,1]\n"
      "v_pk_fma_f32 v[38:39], v[50:51], s[44:45], v[38:39] op_sel:[0,1,0] op_sel_hi:[1,1,1]\n"
      "v_pk_fma_f32 v[48:49], v[48:49], s[46:47], v[120:121] op_sel:[0,1,0] op_sel_hi:[1,1,1] neg_lo:[0,0,1] neg_hi:[0,0,1]\n"
      "v_pk_fma_f32 v[50:51], v[50:51], s[46:47], v[122:123] op_sel:[0,1,0] op_sel_hi:[1,1,1] neg_lo:[0,0,1] neg_hi:[0,0,1]\n"
      "v_cvt_pk_bf16_f32 v36, v36, v37\n"
      "v_cvt_pk_bf16_f32 v38, v38, v39\n"
      "v_cvt_pk_bf16_f32 v48, v48, v49\n"
      "v_cvt_pk_bf16_f32 v50, v50, v51\n"
      "ds_write_addtid_b32 v44 offset:44036\n"
      "ds_write_addtid_b32 v46 offset:44548\n"
      "ds_write_addtid_b32 v36 offset:37888\n"
      "ds_write_addtid_b32 v38 offset:38400\n"
      "ds_write_addtid_b32 v48 offset:39936\n"
      "ds_write_addtid_b32 v50 offset:40448\n"
      "s_waitcnt lgkmcnt(0)\n"
      "s_barrier\n"
      "ds_read2st64_b32 a[220:221], v12 offset0:181 offset1:183\n"
      "ds_read2st64_b32 a[222:223], v15 offset0:181 offset1:183\n"
      "ds_read2st64_b32 a[224:225], v13 offset0:181 offset1:183\n"
      "ds_read2st64_b32 a[226:227], v16 offset0:181 offset1:183\n"
      "ds_read2st64_b32 a[228:229], v14 offset0:181 offset1:183\n"
      "ds_read2st64_b32 a[230:231], v17 offset0:181 offset1:183\n"
      "s_add_u32 s58, s58, s40\n"
      "s_waitcnt lgkmcnt(4)\n"
      "buffer_store_dwordx4 a[220:223], v18, s[8:11], s58 offen offset:0 sc1\n"
      "s_waitcnt lgkmcnt(2)\n"
      "buffer_store_dwordx4 a[224:227], v18, s[8:11], s58 offen offset:128 sc1\n"
      "s_waitcnt lgkmcnt(0)\n"
      "buffer_store_dwordx4 a[228:231], v18, s[8:11], s58 offen offset:256 sc1\n"
      "LDend_%=:\n"
      "s_branch Lfin_%=\n"
      ".p2align 6\n"
      "LS_%=:\n"
      "v_lshlrev_b32 v43, 4, v37\n"
      "v_lshlrev_b32 v44, 2, v37\n"
      "v_and_b32 v43, 16, v43\n"
      "v_and_or_b32 v43, v44, 8, v43\n"
      "v_mul_u32_u24 v44, 192, v38\n"
      "v_mul_u32_u24 v45, 1536, v39\n"
      "s_lshl_b32 s27, s18, 5\n"
      "v_lshlrev_b32 v46, 1, v36\n"
      "v_add3_u32 v43, v43, v44, v45\n"
      "v_bitop3_b32 v46, v46, s27, 32 bitop3:0x6c\n"
      "v_add_u32 v95, v43, v46\n"
      "v_add_u32 v96, 57344, v95\n"
      "v_add_u32 v97, 114688, v95\n"
      "s_lshl_b32 s26, s18, 3\n"
      "v_and_b32 v42, 15, v36\n"
      "v_xor_b32 v42, s26, v42\n"
      "s_lshl_b32 s26, s17, 13\n"
      "s_add_u32 s26, s26, 24832\n"
      "s_mul_i32 s27, s17, 12672\n"
      "v_mul_u32_u24 v44, 192, v42\n"
      "v_lshlrev_b32 v43, 3, v42\n"
      "v_and_b32 v45, 48, v36\n"
      "s_add_u32 s27, s27, 82432\n"
      "v_bitop3_b32 v46, v43, v45, 32 bitop3:0x6c\n"
      "v_add3_u32 v0, v46, v44, s26\n"
      "v_add3_u32 v3, v46, v44, s27\n"
      "v_add_u32 v43, 16, v43\n"
      "v_add_u32 v44, 384, v44\n"
      "v_bitop3_b32 v46, v43, v45, 32 bitop3:0x6c\n"
      "v_add3_u32 v1, v46, v44, s26\n"
      "v_add3_u32 v4, v46, v44, s27\n"
      "v_add_u32 v43, 16, v43\n"
      "v_add_u32 v44, 384, v44\n"
      "v_bitop3_b32 v46, v43, v45, 32 bitop3:0x6c\n"
      "v_add3_u32 v2, v46, v44, s26\n"
      "v_add3_u32 v5, v46, v44, s27\n"
      "v_lshlrev_b32 v43, 31, v36\n"
      "v_and_b32 v44, 2, v36\n"
      "v_or_b32 v43, 0x3f803f80, v43\n"
      "v_mul_u32_u24 v45, 0xc00080, v44\n"
      "v_lshl_add_u32 v44, v44, 22, v43\n"
      "v_add_u32 v45, v45, v43\n"
      "v_accvgpr_write_b32 a216, v44\n"
      "v_accvgpr_write_b32 a217, v45\n"
      "v_lshlrev_b32 v43, 3, v38\n"
      "s_mov_b32 s26, 806420736\n"
      "v_bfe_u32 v43, s26, v43, 8\n"
      "v_lshlrev_b32 v44, 12, v38\n"
      "s_lshl_b32 s28, s18, 8\n"
      "s_add_u32 s28, s28, 82432\n"
      "v_lshl_add_u32 v43, v43, 3, v44\n"
      "v_lshl_add_u32 v44, v39, 2, v37\n"
      "v_lshl_add_u32 v43, v44, 4, v43\n"
      "v_add_u32 v7, s28, v43\n"
      "s_mul_i32 s26, s17, 70144\n"
      "s_lshl_b32 s27, s18, 8\n"
      "s_sub_u32 s26, 70144, s26\n"
      "s_add_u32 s26, s26, s27\n"
      "v_lshl_add_u32 v6, v36, 2, s26\n"
      "v_lshrrev_b32 v43, 3, v36\n"
      "v_and_b32 v44, 7, v36\n"
      "v_mul_u32_u24 v45, 384, v43\n"
      "v_lshl_add_u32 v11, v44, 4, v45\n"
      "v_and_b32 v44, 2, v36\n"
      "v_lshlrev_b32 v43, 3, v43\n"
      "v_lshlrev_b32 v45, 4, v44\n"
      "v_xor_b32 v43, v43, v45\n"
      "v_lshl_add_u32 v43, v44, 7, v43\n"
      "v_and_b32 v44, 1, v36\n"
      "s_lshl_b32 s26, s18, 11\n"
      "v_lshl_add_u32 v43, v44, 6, v43\n"
      "s_add_u32 s26, s26, 12288\n"
      "v_bfe_u32 v44, v36, 2, 1\n"
      "v_add_u32 v43, s26, v43\n"
      "v_mul_i32_i24 v45, -4100, v44\n"
      "s_mov_b32 s27, 8200\n"
      "v_add3_u32 v8, v43, v45, s27\n"
      "v_add_u32 v9, 128, v8\n"
      "s_mul_i32 s62, s18, 393216\n"
      "s_add_u32 s62, s62, s19\n"
      "s_add_u32 s62, s62, s21\n"
      "s_sub_u32 s62, s62, 786432\n"
      "s_bitcmp0_b32 s23, 1\n"
      "s_cselect_b32 s26, 3072, 0\n"
      "s_movk_i32 s40, 3072\n"
      "s_cmovk_i32 s40, -3072\n"
      "s_add_u32 s62, s62, s26\n"
      "s_mov_b32 s58, 0x80000000\n"
      "s_mov_b32 s59, 0x80000000\n"
      "s_and_b32 s9, s9, 0xffff\n"
      "s_mov_b32 s45, 0xbeaaaaab\n"
      "s_mov_b32 s46, 0xbe800000\n"
      "s_mov_b32 s47, 0x3eaaaaab\n"
      "s_add_u32 m0, s63, 12288\n"
      "s_add_u32 s52, s63, 61440\n"
      "buffer_load_dwordx4 v92, s[12:15], s52 offen offset:0 lds\n"
      "buffer_load_dwordx4 v93, s[12:15], s52 offen offset:1024 lds\n"
      "buffer_load_dwordx4 v94, s[12:15], s52 offen offset:2048 lds\n"
      "s_add_u32 m0, s63, 18432\n"
      "s_add_u32 s52, s63, 116736\n"
      "buffer_load_dwordx4 v92, s[12:15], s52 offen offset:0 lds\n"
      "buffer_load_dwordx4 v93, s[12:15], s52 offen offset:1024 lds\n"
      "buffer_load_dwordx4 v94, s[12:15], s52 offen offset:2048 lds\n"
      "LSpro_%=:\n"
      "s_waitcnt vmcnt(6)\n"
      "s_barrier\n"
      "ds_read_b64_tr_b16 a[224:225], v95 offset:24832\n"
      "ds_read_b64_tr_b16 a[226:227], v95 offset:25600\n"
      "ds_read_b64_tr_b16 a[0:1], v95 offset:0\n"
      "ds_read_b64_tr_b16 a[2:3], v95 offset:768\n"
      "ds_read_b64_tr_b16 a[220:221], v95 offset:30976\n"
      "ds_read_b64_tr_b16 a[222:223], v95 offset:31744\n"
      "ds_read_b64_tr_b16 a[232:233], v95 offset:24896\n"
      "ds_read_b64_tr_b16 a[234:235], v95 offset:25664\n"
      "ds_read_b64_tr_b16 a[4:5], v95 offset:64\n"
      "ds_read_b64_tr_b16 a[6:7], v95 offset:832\n"
      "ds_read_b64_tr_b16 a[228:229], v95 offset:31040\n"
      "ds_read_b64_tr_b16 a[230:231], v95 offset:31808\n"
      "s_add_u32 m0, s63, 41216\n"
      "s_add_u32 s52, s63, 12288\n"
      "s_waitcnt lgkmcnt(10)\n"
      "v_mfma_f32_4x4x4_16b_bf16 v[28:31], a[244:245], a[224:225], 0\n"
      "v_mfma_f32_4x4x4_16b_bf16 v[32:35], a[244:245], a[226:227], 0\n"
      "buffer_load_dwordx4 v92, s[12:15], s52 offen offset:0 lds\n"
      "s_waitcnt lgkmcnt(8)\n"
      "v_mfma_f32_4x4x4_16b_bf16 v[28:31], a[248:249], a[0:1], v[28:31]\n"
      "ds_read_b64_tr_b16 a[240:241], v95 offset:24960\n"
      "v_mfma_f32_4x4x4_16b_bf16 v[32:35], a[248:249], a[2:3], v[32:35]\n"
      "buffer_load_dwordx4 v93, s[12:15], s52 offen offset:1024 lds\n"
      "s_waitcnt lgkmcnt(7)\n"
      "v_mfma_f32_4x4x4_16b_bf16 v[36:39], a[246:247], a[220:221], v[28:31]\n"
      "ds_read_b64_tr_b16 a[242:243], v95 offset:25728\n"
      "v_mfma_f32_4x4x4_16b_bf16 v[40:43], a[246:247], a[222:223], v[32:35]\n"
      "buffer_load_dwordx4 v94, s[12:15], s52 offen offset:2048 lds\n"
      "v_mfma_f32_4x4x4_16b_bf16 v[28:31], a[250:251], a[220:221], v[28:31]\n"
      "ds_read_b64_tr_b16 a[8:9], v95 offset:128\n"
      "v_mfma_f32_4x4x4_16b_bf16 v[32:35], a[250:251], a[222:223], v[32:35]\n"
      "ds_read_b64_tr_b16 a[10:11], v95 offset:896\n"
      "ds_read_b64_tr_b16 a[236:237], v95 offset:31104\n"
      "ds_read_b64_tr_b16 a[238:239], v95 offset:31872\n"
      "s_add_u32 m0, s63, 47360\n"
      "s_add_u32 s52, s63, 67584\n"
      "s_waitcnt lgkmcnt(5)\n"
      "v_mfma_f32_4x4x4_16b_bf16 v[76:79], a[244:245], a[232:233], 0\n"
      "v_mfma_f32_4x4x4_16b_bf16 v[80:83], a[244:245], a[234:235], 0\n"
      "buffer_load_dwordx4 v92, s[12:15], s52 offen offset:0 lds\n"
      "v_mfma_f32_4x4x4_16b_bf16 v[76:79], a[248:249], a[4:5], v[76:79]\n"
      "buffer_load_dwordx4 v93, s[12:15], s52 offen offset:1024 lds\n"
      "v_mfma_f32_4x4x4_16b_bf16 v[80:83], a[248:249], a[6:7], v[80:83]\n"
      "buffer_load_dwordx4 v94, s[12:15], s52 offen offset:2048 lds\n"
      "v_mfma_f32_4x4x4_16b_bf16 v[84:87], a[246:247], a[228:229], v[76:79]\n"
      "v_mfma_f32_4x4x4_16b_bf16 v[88:91], a[246:247], a[230:231], v[80:83]\n"
      "v_mfma_f32_4x4x4_16b_bf16 v[76:79], a[250:251], a[228:229], v[76:79]\n"
      "v_mfma_f32_4x4x4_16b_bf16 v[80:83], a[250:251], a[230:231], v[80:83]\n"
      "v_cvt_pk_bf16_f32 v252, v36, v40\n"
      "v_cvt_pk_bf16_f32 v253, v37, v41\n"
      "v_cvt_pk_bf16_f32 v254, v38, v42\n"
      "v_cvt_pk_bf16_f32 v255, v39, v43\n"
      "v_cvt_pk_bf16_f32 v248, v28, v32\n"
      "v_cvt_pk_bf16_f32 v249, v29, v33\n"
      "v_cvt_pk_bf16_f32 v250, v30, v34\n"
      "v_cvt_pk_bf16_f32 v251, v31, v35\n"
      "s_add_u32 m0, s63, 59648\n"
      "s_add_u32 s52, s63, 18432\n"
      "s_nop 0\n"
      "s_waitcnt lgkmcnt(0)\n"
      "v_mfma_f32_4x4x4_16b_bf16 v[28:31], a[244:245], a[240:241], 0\n"
      "v_mfma_f32_4x4x4_16b_bf16 v[32:35], a[244:245], a[242:243], 0\n"
      "buffer_load_dwordx4 v92, s[12:15], s52 offen offset:0 lds\n"
      "v_mfma_f32_4x4x4_16b_bf16 v[28:31], a[248:249], a[8:9], v[28:31]\n"
      "buffer_load_dwordx4 v93, s[12:15], s52 offen offset:1024 lds\n"
      "v_mfma_f32_4x4x4_16b_bf16 v[32:35], a[248:249], a[10:11], v[32:35]\n"
      "buffer_load_dwordx4 v94, s[12:15], s52 offen offset:2048 lds\n"
      "v_mfma_f32_4x4x4_16b_bf16 v[36:39], a[246:247], a[236:237], v[28:31]\n"
      "v_mfma_f32_4x4x4_16b_bf16 v[40:43], a[246:247], a[238:239], v[32:35]\n"
      "v_mfma_f32_4x4x4_16b_bf16 v[28:31], a[250:251], a[236:237], v[28:31]\n"
      "v_mfma_f32_4x4x4_16b_bf16 v[32:35], a[250:251], a[238:239], v[32:35]\n"
      "v_cvt_pk_bf16_f32 v244, v84, v88\n"
      "v_cvt_pk_bf16_f32 v245, v85, v89\n"
      "v_cvt_pk_bf16_f32 v246, v86, v90\n"
      "v_cvt_pk_bf16_f32 v247, v87, v91\n"
      "v_cvt_pk_bf16_f32 v240, v76, v80\n"
      "v_cvt_pk_bf16_f32 v241, v77, v81\n"
      "v_cvt_pk_bf16_f32 v242, v78, v82\n"
      "v_cvt_pk_bf16_f32 v243, v79, v83\n"
      "s_waitcnt vmcnt(9)\n"
      "s_barrier\n"
      "ds_read_b64_tr_b16 a[224:225], v95 offset:6144\n"
      "ds_read_b64_tr_b16 a[226:227], v95 offset:6912\n"
      "ds_read_b64_tr_b16 a[12:13], v95 offset:18432\n"
      "ds_read_b64_tr_b16 a[14:15], v95 offset:19200\n"
      "ds_read_b64_tr_b16 a[220:221], v95 offset:12288\n"
      "ds_read_b64_tr_b16 a[222:223], v95 offset:13056\n"
      "s_add_u32 m0, s63, 65792\n"
      "s_add_u32 s52, s63, 73728\n"
      "s_waitcnt lgkmcnt(4)\n"
      "v_mfma_f32_4x4x4_16b_bf16 v[76:79], a[244:245], a[224:225], 0\n"
      "v_mfma_f32_4x4x4_16b_bf16 v[80:83], a[244:245], a[226:227], 0\n"
      "buffer_load_dwordx4 v92, s[12:15], s52 offen offset:0 lds\n"
      "s_waitcnt lgkmcnt(2)\n"
      "v_mfma_f32_4x4x4_16b_bf16 v[76:79], a[248:249], a[12:13], v[76:79]\n"
      "ds_read_b64_tr_b16 a[232:233], v95 offset:6208\n"
      "v_mfma_f32_4x4x4_16b_bf16 v[80:83], a[248:249], a[14:15], v[80:83]\n"
      "buffer_load_dwordx4 v93, s[12:15], s52 offen offset:1024 lds\n"
      "s_nop 0\n"
      "s_waitcnt lgkmcnt(1)\n"
      "v_mfma_f32_4x4x4_16b_bf16 v[84:87], a[246:247], a[220:221], v[76:79]\n"
      "ds_read_b64_tr_b16 a[234:235], v95 offset:6976\n"
      "v_mfma_f32_4x4x4_16b_bf16 v[88:91], a[246:247], a[222:223], v[80:83]\n"
      "buffer_load_dwordx4 v94, s[12:15], s52 offen offset:2048 lds\n"
      "v_mfma_f32_4x4x4_16b_bf16 v[76:79], a[250:251], a[220:221], v[76:79]\n"
      "ds_read_b64_tr_b16 a[16:17], v95 offset:18496\n"
      "v_mfma_f32_4x4x4_16b_bf16 v[80:83], a[250:251], a[222:223], v[80:83]\n"
      "ds_read_b64_tr_b16 a[18:19], v95 offset:19264\n"
      "ds_read_b64_tr_b16 a[228:229], v95 offset:12352\n"
      "ds_read_b64_tr_b16 a[230:231], v95 offset:13120\n"
      "ds_read_b64_tr_b16 a[240:241], v95 offset:6272\n"
      "ds_read_b64_tr_b16 a[242:243], v95 offset:7040\n"
      "ds_read_b64_tr_b16 a[20:21], v95 offset:18560\n"
      "ds_read_b64_tr_b16 a[22:23], v95 offset:19328\n"
      "ds_read_b64_tr_b16 a[236:237], v95 offset:12416\n"
      "ds_read_b64_tr_b16 a[238:239], v95 offset:13184\n"
      "ds_read_b64_tr_b16 a[188:189], v7 offset:0\n"
      "ds_read_b64_tr_b16 a[190:191], v7 offset:8\n"
      "ds_read_b128 a[192:195], v3 offset:0\n"
      "v_cvt_pk_bf16_f32 v236, v36, v40\n"
      "v_cvt_pk_bf16_f32 v237, v37, v41\n"
      "v_cvt_pk_bf16_f32 v238, v38, v42\n"
      "v_cvt_pk_bf16_f32 v239, v39, v43\n"
      "v_cvt_pk_bf16_f32 v232, v28, v32\n"
      "v_cvt_pk_bf16_f32 v233, v29, v33\n"
      "v_cvt_pk_bf16_f32 v234, v30, v34\n"
      "v_cvt_pk_bf16_f32 v235, v31, v35\n"
      "s_waitcnt vmcnt(6)\n"
      "s_barrier\n"
      "s_add_u32 m0, s63, 116224\n"
      "s_add_u32 s52, s63, 24576\n"
      "s_nop 0\n"
      "s_waitcnt lgkmcnt(7)\n"
      "v_mfma_f32_4x4x4_16b_bf16 v[28:31], a[244:245], a[232:233], 0\n"
      "v_mfma_f32_4x4x4_16b_bf16 v[32:35], a[244:245], a[234:235], 0\n"
      "buffer_load_dwordx4 v92, s[12:15], s52 offen offset:0 lds\n"
      "v_mfma_f32_4x4x4_16b_bf16 v[28:31], a[248:249], a[16:17], v[28:31]\n"
      "ds_read_b64_tr_b16 a[224:225], v95 offset:41216\n"
      "v_mfma_f32_4x4x4_16b_bf16 v[32:35], a[248:249], a[18:19], v[32:35]\n"
      "buffer_load_dwordx4 v93, s[12:15], s52 offen offset:1024 lds\n"
      "v_mfma_f32_4x4x4_16b_bf16 v[36:39], a[246:247], a[228:229], v[28:31]\n"
      "ds_read_b64_tr_b16 a[226:227], v95 offset:41984\n"
      "v_mfma_f32_4x4x4_16b_bf16 v[40:43], a[246:247], a[230:231], v[32:35]\n"
      "buffer_load_dwordx4 v94, s[12:15], s52 offen offset:2048 lds\n"
      "v_mfma_f32_4x4x4_16b_bf16 v[28:31], a[250:251], a[228:229], v[28:31]\n"
      "ds_read_b64_tr_b16 a[24:25], v95 offset:53504\n"
      "v_mfma_f32_4x4x4_16b_bf16 v[32:35], a[250:251], a[230:231], v[32:35]\n"
      "ds_read_b64_tr_b16 a[26:27], v95 offset:54272\n"
      "s_nop 0\n"
      "s_waitcnt lgkmcnt(4)\n"
      "v_mfma_f32_4x4x4_16b_bf16 v[44:47], a[216:217], a[188:189], 0\n"
      "ds_read_b64_tr_b16 a[220:221], v95 offset:47360\n"
      "v_mfma_f32_4x4x4_16b_bf16 v[48:51], a[216:217], a[190:191], 0\n"
      "ds_read_b64_tr_b16 a[222:223], v95 offset:48128\n"
      "ds_read_b64_tr_b16 a[184:185], v7 offset:512\n"
      "ds_read_b64_tr_b16 a[186:187], v7 offset:520\n"
      "ds_read_b128 a[196:199], v3 offset:64\n"
      "v_cvt_pk_bf16_f32 v228, v84, v88\n"
      "v_cvt_pk_bf16_f32 v229, v85, v89\n"
      "v_cvt_pk_bf16_f32 v230, v86, v90\n"
      "v_cvt_pk_bf16_f32 v231, v87, v91\n"
      "v_cvt_pk_bf16_f32 v224, v76, v80\n"
      "v_cvt_pk_bf16_f32 v225, v77, v81\n"
      "v_cvt_pk_bf16_f32 v226, v78, v82\n"
      "v_cvt_pk_bf16_f32 v227, v79, v83\n"
      "s_add_u32 m0, s63, 122368\n"
      "s_add_u32 s52, s63, 79872\n"
      "v_mfma_f32_4x4x4_16b_bf16 v[76:79], a[244:245], a[240:241], 0\n"
      "v_mfma_f32_4x4x4_16b_bf16 v[80:83], a[244:245], a[242:243], 0\n"
      "buffer_load_dwordx4 v92, s[12:15], s52 offen offset:0 lds\n"
      "v_mfma_f32_4x4x4_16b_bf16 v[76:79], a[248:249], a[20:21], v[76:79]\n"
      "ds_read_b64_tr_b16 a[232:233], v95 offset:41280\n"
      "v_mfma_f32_4x4x4_16b_bf16 v[80:83], a[248:249], a[22:23], v[80:83]\n"
      "buffer_load_dwordx4 v93, s[12:15], s52 offen offset:1024 lds\n"
      "v_mfma_f32_4x4x4_16b_bf16 v[84:87], a[246:247], a[236:237], v[76:79]\n"
      "ds_read_b64_tr_b16 a[234:235], v95 offset:42048\n"
      "v_mfma_f32_4x4x4_16b_bf16 v[88:91], a[246:247], a[238:239], v[80:83]\n"
      "buffer_load_dwordx4 v94, s[12:15], s52 offen offset:2048 lds\n"
      "v_mfma_f32_4x4x4_16b_bf16 v[76:79], a[250:251], a[236:237], v[76:79]\n"
      "ds_read_b64_tr_b16 a[28:29], v95 offset:53568\n"
      "v_mfma_f32_4x4x4_16b_bf16 v[80:83], a[250:251], a[238:239], v[80:83]\n"
      "ds_read_b64_tr_b16 a[30:31], v95 offset:54336\n"
      "s_nop 0\n"
      "s_waitcnt lgkmcnt(4)\n"
      "v_mfma_f32_4x4x4_16b_bf16 v[112:115], a[216:217], a[184:185], 0\n"
      "ds_read_b64_tr_b16 a[228:229], v95 offset:47424\n"
      "v_mfma_f32_4x4x4_16b_bf16 v[116:119], a[216:217], a[186:187], 0\n"
      "ds_read_b64_tr_b16 a[230:231], v95 offset:48192\n"
      "ds_read_b64_tr_b16 a[180:181], v7 offset:1024\n"
      "ds_read_b64_tr_b16 a[182:183], v7 offset:1032\n"
      "v_cvt_pk_bf16_f32 v220, v36, v40\n"
      "v_cvt_pk_bf16_f32 v221, v37, v41\n"
      "v_cvt_pk_bf16_f32 v222, v38, v42\n"
      "v_cvt_pk_bf16_f32 v223, v39, v43\n"
      "v_cvt_pk_bf16_f32 v216, v28, v32\n"
      "v_cvt_pk_bf16_f32 v217, v29, v33\n"
      "v_mfma_f32_16x16x32_bf16 v[100:103], a[0:3], a[192:195], 0\n"
      "v_cvt_pk_bf16_f32 v218, v30, v34\n"
      "v_cvt_pk_bf16_f32 v219, v31, v35\n"
      "v_mfma_f32_16x16x32_bf16 v[104:107], a[4:7], a[192:195], 0\n"
      "v_cvt_pk_bf16_f32 v44, v44, v48\n"
      "v_cvt_pk_bf16_f32 v45, v45, v49\n"
      "v_mfma_f32_16x16x32_bf16 v[108:111], a[8:11], a[192:195], 0\n"
      "s_add_u32 m0, s63, 134656\n"
      "v_cvt_pk_bf16_f32 v46, v46, v50\n"
      "v_cvt_pk_bf16_f32 v47, v47, v51\n"
      "s_add_u32 s52, s63, 30720\n"
      "v_mfma_f32_4x4x4_16b_bf16 v[28:31], a[244:245], a[224:225], 0\n"
      "v_mfma_f32_4x4x4_16b_bf16 v[32:35], a[244:245], a[226:227], 0\n"
      "buffer_load_dwordx4 v92, s[12:15], s52 offen offset:0 lds\n"
      "v_mfma_f32_4x4x4_16b_bf16 v[28:31], a[248:249], a[24:25], v[28:31]\n"
      "ds_read_b64_tr_b16 a[240:241], v95 offset:41344\n"
      "v_mfma_f32_4x4x4_16b_bf16 v[32:35], a[248:249], a[26:27], v[32:35]\n"
      "buffer_load_dwordx4 v93, s[12:15], s52 offen offset:1024 lds\n"
      "v_mfma_f32_4x4x4_16b_bf16 v[36:39], a[246:247], a[220:221], v[28:31]\n"
      "ds_read_b64_tr_b16 a[242:243], v95 offset:42112\n"
      "v_mfma_f32_4x4x4_16b_bf16 v[40:43], a[246:247], a[222:223], v[32:35]\n"
      "buffer_load_dwordx4 v94, s[12:15], s52 offen offset:2048 lds\n"
      "v_mfma_f32_4x4x4_16b_bf16 v[28:31], a[250:251], a[220:221], v[28:31]\n"
      "ds_read_b64_tr_b16 a[32:33], v95 offset:53632\n"
      "v_mfma_f32_4x4x4_16b_bf16 v[32:35], a[250:251], a[222:223], v[32:35]\n"
      "ds_read_b64_tr_b16 a[34:35], v95 offset:54400\n"
      "s_waitcnt lgkmcnt(4)\n"
      "v_mfma_f32_4x4x4_16b_bf16 v[20:23], a[216:217], a[180:181], 0\n"
      "ds_read_b64_tr_b16 a[236:237], v95 offset:47488\n"
      "v_mfma_f32_4x4x4_16b_bf16 v[24:27], a[216:217], a[182:183], 0\n"
      "ds_read_b64_tr_b16 a[238:239], v95 offset:48256\n"
      "ds_read_b64_tr_b16 a[176:177], v7 offset:1536\n"
      "ds_read_b64_tr_b16 a[178:179], v7 offset:1544\n"
      "s_mov_b32 m0, s64\n"
      "s_nop 0\n"
      "s_nop 0\n"
      "ds_write_addtid_b32 v44 offset:24832\n"
      "ds_write_addtid_b32 v45 offset:28928\n"
      "ds_write_addtid_b32 v46 offset:33024\n"
      "ds_write_addtid_b32 v47 offset:37120\n"
      "v_cvt_pk_bf16_f32 v212, v84, v88\n"
      "v_cvt_pk_bf16_f32 v213, v85, v89\n"
      "v_cvt_pk_bf16_f32 v214, v86, v90\n"
      "v_cvt_pk_bf16_f32 v215, v87, v91\n"
      "v_cvt_pk_bf16_f32 v208, v76, v80\n"
      "v_cvt_pk_bf16_f32 v209, v77, v81\n"
      "v_mfma_f32_16x16x32_bf16 v[100:103], a[12:15], a[196:199], v[100:103]\n"
      "v_cvt_pk_bf16_f32 v210, v78, v82\n"
      "v_cvt_pk_bf16_f32 v211, v79, v83\n"
      "v_mfma_f32_16x16x32_bf16 v[104:107], a[16:19], a[196:199], v[104:107]\n"
      "v_cvt_pk_bf16_f32 v112, v112, v116\n"
      "v_cvt_pk_bf16_f32 v113, v113, v117\n"
      "v_mfma_f32_16x16x32_bf16 v[108:111], a[20:23], a[196:199], v[108:111]\n"
      "s_waitcnt vmcnt(9)\n"
      "v_cvt_pk_bf16_f32 v114, v114, v118\n"
      "v_cvt_pk_bf16_f32 v115, v115, v119\n"
      "s_barrier\n"
      "s_add_u32 m0, s63, 140800\n"
      "s_add_u32 s52, s63, 86016\n"
      "v_mfma_f32_4x4x4_16b_bf16 v[76:79], a[244:245], a[232:233], 0\n"
      "v_mfma_f32_4x4x4_16b_bf16 v[80:83], a[244:245], a[234:235], 0\n"
      "buffer_load_dwordx4 v92, s[12:15], s52 offen offset:0 lds\n"
      "v_mfma_f32_4x4x4_16b_bf16 v[76:79], a[248:249], a[28:29], v[76:79]\n"
      "ds_read_b64_tr_b16 a[224:225], v96 offset:2304\n"
      "v_mfma_f32_4x4x4_16b_bf16 v[80:83], a[248:249], a[30:31], v[80:83]\n"
      "buffer_load_dwordx4 v93, s[12:15], s52 offen offset:1024 lds\n"
      "v_mfma_f32_4x4x4_16b_bf16 v[84:87], a[246:247], a[228:229], v[76:79]\n"
      "ds_read_b64_tr_b16 a[226:227], v96 offset:3072\n"
      "v_mfma_f32_4x4x4_16b_bf16 v[88:91], a[246:247], a[230:231], v[80:83]\n"
      "buffer_load_dwordx4 v94, s[12:15], s52 offen offset:2048 lds\n"
      "v_mfma_f32_4x4x4_16b_bf16 v[76:79], a[250:251], a[228:229], v[76:79]\n"
      "ds_read_b64_tr_b16 a[36:37], v96 offset:14592\n"
      "v_mfma_f32_4x4x4_16b_bf16 v[80:83], a[250:251], a[230:231], v[80:83]\n"
      "ds_read_b64_tr_b16 a[38:39], v96 offset:15360\n"
      "s_nop 0\n"
      "s_waitcnt lgkmcnt(4)\n"
      "v_mfma_f32_4x4x4_16b_bf16 v[12:15], a[216:217], a[176:177], 0\n"
      "ds_read_b64_tr_b16 a[220:221], v96 offset:8448\n"
      "v_mfma_f32_4x4x4_16b_bf16 v[16:19], a[216:217], a[178:179], 0\n"
      "ds_read_b64_tr_b16 a[222:223], v96 offset:9216\n"
      "s_mov_b32 m0, s64\n"
      "s_nop 0\n"
      "ds_write_addtid_b32 v112 offset:25344\n"
      "ds_write_addtid_b32 v113 offset:29440\n"
      "ds_write_addtid_b32 v114 offset:33536\n"
      "ds_write_addtid_b32 v115 offset:37632\n"
      "ds_read_b128 a[200:203], v3 offset:128\n"
      "v_cvt_pk_bf16_f32 v204, v36, v40\n"
      "v_cvt_pk_bf16_f32 v205, v37, v41\n"
      "v_cvt_pk_bf16_f32 v206, v38, v42\n"
      "v_cvt_pk_bf16_f32 v207, v39, v43\n"
      "v_cvt_pk_bf16_f32 v200, v28, v32\n"
      "v_cvt_pk_bf16_f32 v201, v29, v33\n"
      "v_cvt_pk_bf16_f32 v202, v30, v34\n"
      "v_cvt_pk_bf16_f32 v203, v31, v35\n"
      "v_cvt_pk_bf16_f32 v20, v20, v24\n"
      "v_cvt_pk_bf16_f32 v21, v21, v25\n"
      "v_cvt_pk_bf16_f32 v22, v22, v26\n"
      "v_cvt_pk_bf16_f32 v23, v23, v27\n"
      "s_add_u32 m0, s63, 6144\n"
      "s_add_u32 s52, s63, 36864\n"
      "v_mfma_f32_4x4x4_16b_bf16 v[28:31], a[244:245], a[240:241], 0\n"
      "v_mfma_f32_4x4x4_16b_bf16 v[32:35], a[244:245], a[242:243], 0\n"
      "buffer_load_dwordx4 v92, s[12:15], s52 offen offset:0 lds\n"
      "v_mfma_f32_4x4x4_16b_bf16 v[28:31], a[248:249], a[32:33], v[28:31]\n"
      "ds_read_b64_tr_b16 a[232:233], v96 offset:2368\n"
      "v_mfma_f32_4x4x4_16b_bf16 v[32:35], a[248:249], a[34:35], v[32:35]\n"
      "buffer_load_dwordx4 v93, s[12:15], s52 offen offset:1024 lds\n"
      "v_mfma_f32_4x4x4_16b_bf16 v[36:39], a[246:247], a[236:237], v[28:31]\n"
      "ds_read_b64_tr_b16 a[234:235], v96 offset:3136\n"
      "v_mfma_f32_4x4x4_16b_bf16 v[40:43], a[246:247], a[238:239], v[32:35]\n"
      "ds_read_b64_tr_b16 a[40:41], v96 offset:14656\n"
      "v_mfma_f32_4x4x4_16b_bf16 v[28:31], a[250:251], a[236:237], v[28:31]\n"
      "ds_read_b64_tr_b16 a[42:43], v96 offset:15424\n"
      "v_mfma_f32_4x4x4_16b_bf16 v[32:35], a[250:251], a[238:239], v[32:35]\n"
      "ds_read_b64_tr_b16 a[228:229], v96 offset:8512\n"
      "ds_read_b64_tr_b16 a[230:231], v96 offset:9280\n"
      "s_mov_b32 m0, s64\n"
      "s_nop 0\n"
      "ds_write_addtid_b32 v20 offset:25856\n"
      "ds_write_addtid_b32 v21 offset:29952\n"
      "ds_write_addtid_b32 v22 offset:34048\n"
      "ds_write_addtid_b32 v23 offset:38144\n"
      "v_cvt_pk_bf16_f32 v196, v84, v88\n"
      "v_cvt_pk_bf16_f32 v197, v85, v89\n"
      "v_cvt_pk_bf16_f32 v198, v86, v90\n"
      "v_cvt_pk_bf16_f32 v199, v87, v91\n"
      "v_cvt_pk_bf16_f32 v192, v76, v80\n"
      "v_cvt_pk_bf16_f32 v193, v77, v81\n"
      "v_cvt_pk_bf16_f32 v194, v78, v82\n"
      "v_cvt_pk_bf16_f32 v195, v79, v83\n"
      "v_cvt_pk_bf16_f32 v12, v12, v16\n"
      "v_cvt_pk_bf16_f32 v13, v13, v17\n"
      "v_cvt_pk_bf16_f32 v14, v14, v18\n"
      "v_cvt_pk_bf16_f32 v15, v15, v19\n"
      "s_waitcnt lgkmcnt(4)\n"
      "v_mfma_f32_4x4x4_16b_bf16 v[76:79], a[244:245], a[224:225], 0\n"
      "v_mfma_f32_4x4x4_16b_bf16 v[80:83], a[244:245], a[226:227], 0\n"
      "s_add_u32 m0, s63, 6144\n"
      "s_nop 0\n"
      "buffer_load_dwordx4 v94, s[12:15], s52 offen offset:2048 lds\n"
      "v_mfma_f32_4x4x4_16b_bf16 v[76:79], a[248:249], a[36:37], v[76:79]\n"
      "ds_read_b64_tr_b16 a[240:241], v96 offset:2432\n"
      "v_mfma_f32_4x4x4_16b_bf16 v[80:83], a[248:249], a[38:39], v[80:83]\n"
      "s_add_u32 m0, s63, 12288\n"
      "v_mfma_f32_4x4x4_16b_bf16 v[84:87], a[246:247], a[220:221], v[76:79]\n"
      "ds_read_b64_tr_b16 a[242:243], v96 offset:3200\n"
      "v_mfma_f32_4x4x4_16b_bf16 v[88:91], a[246:247], a[222:223], v[80:83]\n"
      "s_add_u32 s52, s63, 92160\n"
      "v_mfma_f32_4x4x4_16b_bf16 v[76:79], a[250:251], a[220:221], v[76:79]\n"
      "ds_read_b64_tr_b16 a[44:45], v96 offset:14720\n"
      "v_mfma_f32_4x4x4_16b_bf16 v[80:83], a[250:251], a[222:223], v[80:83]\n"
      "buffer_load_dwordx4 v92, s[12:15], s52 offen offset:0 lds\n"
      "ds_read_b64_tr_b16 a[46:47], v96 offset:15488\n"
      "ds_read_b64_tr_b16 a[236:237], v96 offset:8576\n"
      "ds_read_b64_tr_b16 a[238:239], v96 offset:9344\n"
      "s_mov_b32 m0, s64\n"
      "s_nop 0\n"
      "ds_write_addtid_b32 v12 offset:26368\n"
      "ds_write_addtid_b32 v13 offset:30464\n"
      "ds_write_addtid_b32 v14 offset:34560\n"
      "ds_write_addtid_b32 v15 offset:38656\n"
      "v_cvt_pk_bf16_f32 v188, v36, v40\n"
      "v_cvt_pk_bf16_f32 v189, v37, v41\n"
      "v_mfma_f32_16x16x32_bf16 v[100:103], a[24:27], a[200:203], v[100:103]\n"
      "s_nop 0\n"
      "v_cvt_pk_bf16_f32 v190, v38, v42\n"
      "v_cvt_pk_bf16_f32 v191, v39, v43\n"
      "v_mfma_f32_16x16x32_bf16 v[104:107], a[28:31], a[200:203], v[104:107]\n"
      "s_waitcnt vmcnt(10)\n"
      "v_cvt_pk_bf16_f32 v184, v28, v32\n"
      "v_cvt_pk_bf16_f32 v185, v29, v33\n"
      "v_mfma_f32_16x16x32_bf16 v[108:111], a[32:35], a[200:203], v[108:111]\n"
      "s_waitcnt lgkmcnt(0)\n"
      "v_cvt_pk_bf16_f32 v186, v30, v34\n"
      "v_cvt_pk_bf16_f32 v187, v31, v35\n"
      "s_barrier\n"
      "v_mfma_f32_4x4x4_16b_bf16 v[28:31], a[244:245], a[232:233], 0\n"
      "v_mfma_f32_4x4x4_16b_bf16 v[32:35], a[244:245], a[234:235], 0\n"
      "s_add_u32 m0, s63, 12288\n"
      "s_nop 0\n"
      "s_nop 0\n"
      "buffer_load_dwordx4 v93, s[12:15], s52 offen offset:1024 lds\n"
      "v_mfma_f32_4x4x4_16b_bf16 v[28:31], a[248:249], a[40:41], v[28:31]\n"
      "ds_read_b64_tr_b16 a[224:225], v97 offset:1536\n"
      "v_mfma_f32_4x4x4_16b_bf16 v[32:35], a[248:249], a[42:43], v[32:35]\n"
      "buffer_load_dwordx4 v94, s[12:15], s52 offen offset:2048 lds\n"
      "v_mfma_f32_4x4x4_16b_bf16 v[36:39], a[246:247], a[228:229], v[28:31]\n"
      "ds_read_b64_tr_b16 a[226:227], v97 offset:2304\n"
      "v_mfma_f32_4x4x4_16b_bf16 v[40:43], a[246:247], a[230:231], v[32:35]\n"
      "ds_read_b64_tr_b16 a[48:49], v97 offset:13824\n"
      "v_mfma_f32_4x4x4_16b_bf16 v[28:31], a[250:251], a[228:229], v[28:31]\n"
      "ds_read_b64_tr_b16 a[50:51], v97 offset:14592\n"
      "v_mfma_f32_4x4x4_16b_bf16 v[32:35], a[250:251], a[230:231], v[32:35]\n"
      "ds_read_b64_tr_b16 a[220:221], v97 offset:7680\n"
      "ds_read_b64_tr_b16 a[222:223], v97 offset:8448\n"
      "ds_read_b128 a[204:207], v4 offset:0\n"
      "ds_read_b128 a[208:211], v0 offset:0\n"
      "ds_read_b128 a[212:215], v0 offset:4096\n"
      "v_cvt_pk_bf16_f32 v180, v84, v88\n"
      "v_cvt_pk_bf16_f32 v181, v85, v89\n"
      "v_cvt_pk_bf16_f32 v182, v86, v90\n"
      "v_cvt_pk_bf16_f32 v183, v87, v91\n"
      "v_cvt_pk_bf16_f32 v176, v76, v80\n"
      "v_cvt_pk_bf16_f32 v177, v77, v81\n"
      "v_cvt_pk_bf16_f32 v178, v78, v82\n"
      "v_cvt_pk_bf16_f32 v179, v79, v83\n"
      "s_add_u32 m0, s63, 41216\n"
      "s_add_u32 s52, s63, 43008\n"
      "v_mfma_f32_4x4x4_16b_bf16 v[76:79], a[244:245], a[240:241], 0\n"
      "v_mfma_f32_4x4x4_16b_bf16 v[80:83], a[244:245], a[242:243], 0\n"
      "buffer_load_dwordx4 v92, s[12:15], s52 offen offset:0 lds\n"
      "v_mfma_f32_4x4x4_16b_bf16 v[76:79], a[248:249], a[44:45], v[76:79]\n"
      "ds_read_b64_tr_b16 a[232:233], v97 offset:1600\n"
      "v_mfma_f32_4x4x4_16b_bf16 v[80:83], a[248:249], a[46:47], v[80:83]\n"
      "buffer_load_dwordx4 v93, s[12:15], s52 offen offset:1024 lds\n"
      "v_mfma_f32_4x4x4_16b_bf16 v[84:87], a[246:247], a[236:237], v[76:79]\n"
      "ds_read_b64_tr_b16 a[234:235], v97 offset:2368\n"
      "v_mfma_f32_4x4x4_16b_bf16 v[88:91], a[246:247], a[238:239], v[80:83]\n"
      "ds_read_b64_tr_b16 a[52:53], v97 offset:13888\n"
      "v_mfma_f32_4x4x4_16b_bf16 v[76:79], a[250:251], a[236:237], v[76:79]\n"
      "ds_read_b64_tr_b16 a[54:55], v97 offset:14656\n"
      "v_mfma_f32_4x4x4_16b_bf16 v[80:83], a[250:251], a[238:239], v[80:83]\n"
      "ds_read_b64_tr_b16 a[228:229], v97 offset:7744\n"
      "ds_read_b64_tr_b16 a[230:231], v97 offset:8512\n"
      "ds_read_b128 a[192:195], v0 offset:64\n"
      "ds_read_b128 a[196:199], v0 offset:4160\n"
      "ds_read_b128 a[200:203], v0 offset:128\n"
      "v_cvt_pk_bf16_f32 v172, v36, v40\n"
      "v_cvt_pk_bf16_f32 v173, v37, v41\n"
      "v_cvt_pk_bf16_f32 v174, v38, v42\n"
      "v_cvt_pk_bf16_f32 v175, v39, v43\n"
      "v_cvt_pk_bf16_f32 v168, v28, v32\n"
      "v_cvt_pk_bf16_f32 v169, v29, v33\n"
      "v_cvt_pk_bf16_f32 v170, v30, v34\n"
      "v_cvt_pk_bf16_f32 v171, v31, v35\n"
      "s_nop 0\n"
      "s_waitcnt lgkmcnt(6)\n"
      "v_mfma_f32_4x4x4_16b_bf16 v[28:31], a[244:245], a[224:225], 0\n"
      "v_mfma_f32_4x4x4_16b_bf16 v[32:35], a[244:245], a[226:227], 0\n"
      "buffer_load_dwordx4 v94, s[12:15], s52 offen offset:2048 lds\n"
      "v_mfma_f32_4x4x4_16b_bf16 v[28:31], a[248:249], a[48:49], v[28:31]\n"
      "ds_read_b64_tr_b16 a[240:241], v97 offset:1664\n"
      "v_mfma_f32_4x4x4_16b_bf16 v[32:35], a[248:249], a[50:51], v[32:35]\n"
      "s_add_u32 m0, s63, 47360\n"
      "v_mfma_f32_4x4x4_16b_bf16 v[36:39], a[246:247], a[220:221], v[28:31]\n"
      "ds_read_b64_tr_b16 a[242:243], v97 offset:2432\n"
      "v_mfma_f32_4x4x4_16b_bf16 v[40:43], a[246:247], a[222:223], v[32:35]\n"
      "s_add_u32 s52, s63, 98304\n"
      "v_mfma_f32_4x4x4_16b_bf16 v[28:31], a[250:251], a[220:221], v[28:31]\n"
      "ds_read_b64_tr_b16 a[56:57], v97 offset:13952\n"
      "v_mfma_f32_4x4x4_16b_bf16 v[32:35], a[250:251], a[222:223], v[32:35]\n"
      "buffer_load_dwordx4 v92, s[12:15], s52 offen offset:0 lds\n"
      "ds_read_b64_tr_b16 a[58:59], v97 offset:14720\n"
      "ds_read_b64_tr_b16 a[236:237], v97 offset:7808\n"
      "ds_read_b64_tr_b16 a[238:239], v97 offset:8576\n"
      "v_mfma_f32_16x16x32_bf16 v[100:103], a[36:39], a[204:207], v[100:103]\n"
      "v_cvt_pk_bf16_f32 v164, v84, v88\n"
      "v_cvt_pk_bf16_f32 v165, v85, v89\n"
      "v_mfma_f32_16x16x32_bf16 v[104:107], a[40:43], a[204:207], v[104:107]\n"
      "v_cvt_pk_bf16_f32 v166, v86, v90\n"
      "v_cvt_pk_bf16_f32 v167, v87, v91\n"
      "v_mfma_f32_16x16x32_bf16 v[108:111], a[44:47], a[204:207], v[108:111]\n"
      "s_nop 0\n"
      "v_cvt_pk_bf16_f32 v160, v76, v80\n"
      "v_cvt_pk_bf16_f32 v161, v77, v81\n"
      "v_mfma_f32_16x16x32_bf16 v[52:55], v[252:255], a[208:211], 0\n"
      "s_waitcnt vmcnt(10)\n"
      "v_cvt_pk_bf16_f32 v162, v78, v82\n"
      "v_cvt_pk_bf16_f32 v163, v79, v83\n"
      "s_barrier\n"
      "v_mfma_f32_4x4x4_16b_bf16 v[76:79], a[244:245], a[232:233], 0\n"
      "v_mfma_f32_4x4x4_16b_bf16 v[80:83], a[244:245], a[234:235], 0\n"
      "buffer_load_dwordx4 v93, s[12:15], s52 offen offset:1024 lds\n"
      "s_waitcnt lgkmcnt(0)\n"
      "v_mfma_f32_4x4x4_16b_bf16 v[76:79], a[248:249], a[52:53], v[76:79]\n"
      "ds_read_b64_tr_b16 a[224:225], v97 offset:19968\n"
      "v_mfma_f32_4x4x4_16b_bf16 v[80:83], a[248:249], a[54:55], v[80:83]\n"
      "buffer_load_dwordx4 v94, s[12:15], s52 offen offset:2048 lds\n"
      "v_mfma_f32_4x4x4_16b_bf16 v[84:87], a[246:247], a[228:229], v[76:79]\n"
      "ds_read_b64_tr_b16 a[226:227], v97 offset:20736\n"
      "v_mfma_f32_4x4x4_16b_bf16 v[88:91], a[246:247], a[230:231], v[80:83]\n"
      "ds_read_b64_tr_b16 a[60:61], v95 offset:0\n"
      "v_mfma_f32_4x4x4_16b_bf16 v[76:79], a[250:251], a[228:229], v[76:79]\n"
      "ds_read_b64_tr_b16 a[62:63], v95 offset:768\n"
      "v_mfma_f32_4x4x4_16b_bf16 v[80:83], a[250:251], a[230:231], v[80:83]\n"
      "ds_read_b64_tr_b16 a[220:221], v97 offset:26112\n"
      "ds_read_b64_tr_b16 a[222:223], v97 offset:26880\n"
      "ds_read_b128 a[204:207], v0 offset:4224\n"
      "v_mfma_f32_16x16x32_bf16 v[56:59], v[244:247], a[208:211], 0\n"
      "v_cvt_pk_bf16_f32 v156, v36, v40\n"
      "v_cvt_pk_bf16_f32 v157, v37, v41\n"
      "v_mfma_f32_16x16x32_bf16 v[60:63], v[236:239], a[208:211], 0\n"
      "v_cvt_pk_bf16_f32 v158, v38, v42\n"
      "v_cvt_pk_bf16_f32 v159, v39, v43\n"
      "v_mfma_f32_16x16x32_bf16 v[64:67], v[248:251], a[212:215], 0\n"
      "v_cvt_pk_bf16_f32 v152, v28, v32\n"
      "v_cvt_pk_bf16_f32 v153, v29, v33\n"
      "v_mfma_f32_16x16x32_bf16 v[68:71], v[240:243], a[212:215], 0\n"
      "s_add_u32 m0, s63, 59648\n"
      "v_cvt_pk_bf16_f32 v154, v30, v34\n"
      "v_cvt_pk_bf16_f32 v155, v31, v35\n"
      "s_add_u32 s52, s63, 49152\n"
      "v_mfma_f32_4x4x4_16b_bf16 v[28:31], a[244:245], a[240:241], 0\n"
      "v_mfma_f32_4x4x4_16b_bf16 v[32:35], a[244:245], a[242:243], 0\n"
      "buffer_load_dwordx4 v92, s[12:15], s52 offen offset:0 lds\n"
      "v_mfma_f32_4x4x4_16b_bf16 v[28:31], a[248:249], a[56:57], v[28:31]\n"
      "ds_read_b64_tr_b16 a[232:233], v97 offset:20032\n"
      "v_mfma_f32_4x4x4_16b_bf16 v[32:35], a[248:249], a[58:59], v[32:35]\n"
      "buffer_load_dwordx4 v93, s[12:15], s52 offen offset:1024 lds\n"
      "v_mfma_f32_4x4x4_16b_bf16 v[36:39], a[246:247], a[236:237], v[28:31]\n"
      "ds_read_b64_tr_b16 a[234:235], v97 offset:20800\n"
      "v_mfma_f32_4x4x4_16b_bf16 v[40:43], a[246:247], a[238:239], v[32:35]\n"
      "ds_read_b64_tr_b16 a[64:65], v95 offset:64\n"
      "v_mfma_f32_4x4x4_16b_bf16 v[28:31], a[250:251], a[236:237], v[28:31]\n"
      "ds_read_b64_tr_b16 a[66:67], v95 offset:832\n"
      "v_mfma_f32_4x4x4_16b_bf16 v[32:35], a[250:251], a[238:239], v[32:35]\n"
      "ds_read_b64_tr_b16 a[228:229], v97 offset:26176\n"
      "ds_read_b64_tr_b16 a[230:231], v97 offset:26944\n"
      "ds_read_b128 a[208:211], v1 offset:0\n"
      "v_mfma_f32_16x16x32_bf16 v[72:75], v[232:235], a[212:215], 0\n"
      "v_cvt_pk_bf16_f32 v148, v84, v88\n"
      "v_cvt_pk_bf16_f32 v149, v85, v89\n"
      "v_mfma_f32_16x16x32_bf16 v[52:55], v[228:231], a[192:195], v[52:55]\n"
      "v_cvt_pk_bf16_f32 v150, v86, v90\n"
      "v_cvt_pk_bf16_f32 v151, v87, v91\n"
      "v_mfma_f32_16x16x32_bf16 v[56:59], v[220:223], a[192:195], v[56:59]\n"
      "s_nop 0\n"
      "v_cvt_pk_bf16_f32 v144, v76, v80\n"
      "v_cvt_pk_bf16_f32 v145, v77, v81\n"
      "v_mfma_f32_16x16x32_bf16 v[60:63], v[212:215], a[192:195], v[60:63]\n"
      "s_waitcnt lgkmcnt(2)\n"
      "v_cvt_pk_bf16_f32 v146, v78, v82\n"
      "v_cvt_pk_bf16_f32 v147, v79, v83\n"
      "v_mfma_f32_4x4x4_16b_bf16 v[76:79], a[244:245], a[224:225], 0\n"
      "v_mfma_f32_4x4x4_16b_bf16 v[80:83], a[244:245], a[226:227], 0\n"
      "buffer_load_dwordx4 v94, s[12:15], s52 offen offset:2048 lds\n"
      "v_mfma_f32_4x4x4_16b_bf16 v[76:79], a[248:249], a[60:61], v[76:79]\n"
      "ds_read_b64_tr_b16 a[240:241], v97 offset:20096\n"
      "v_mfma_f32_4x4x4_16b_bf16 v[80:83], a[248:249], a[62:63], v[80:83]\n"
      "s_add_u32 m0, s63, 65792\n"
      "v_mfma_f32_4x4x4_16b_bf16 v[84:87], a[246:247], a[220:221], v[76:79]\n"
      "ds_read_b64_tr_b16 a[242:243], v97 offset:20864\n"
      "v_mfma_f32_4x4x4_16b_bf16 v[88:91], a[246:247], a[222:223], v[80:83]\n"
      "s_add_u32 s52, s63, 104448\n"
      "v_mfma_f32_4x4x4_16b_bf16 v[76:79], a[250:251], a[220:221], v[76:79]\n"
      "ds_read_b64_tr_b16 a[68:69], v95 offset:128\n"
      "v_mfma_f32_4x4x4_16b_bf16 v[80:83], a[250:251], a[222:223], v[80:83]\n"
      "buffer_load_dwordx4 v92, s[12:15], s52 offen offset:0 lds\n"
      "ds_read_b64_tr_b16 a[70:71], v95 offset:896\n"
      "ds_read_b64_tr_b16 a[236:237], v97 offset:26240\n"
      "ds_read_b64_tr_b16 a[238:239], v97 offset:27008\n"
      "ds_read_b128 a[212:215], v1 offset:4096\n"
      "ds_read_b128 a[192:195], v4 offset:64\n"
      "v_mfma_f32_16x16x32_bf16 v[64:67], v[224:227], a[196:199], v[64:67]\n"
      "s_nop 0\n"
      "v_cvt_pk_bf16_f32 v140, v36, v40\n"
      "v_cvt_pk_bf16_f32 v141, v37, v41\n"
      "v_mfma_f32_16x16x32_bf16 v[68:71], v[216:219], a[196:199], v[68:71]\n"
      "s_nop 0\n"
      "v_cvt_pk_bf16_f32 v142, v38, v42\n"
      "v_cvt_pk_bf16_f32 v143, v39, v43\n"
      "v_mfma_f32_16x16x32_bf16 v[72:75], v[208:211], a[196:199], v[72:75]\n"
      "s_nop 0\n"
      "v_cvt_pk_bf16_f32 v136, v28, v32\n"
      "v_cvt_pk_bf16_f32 v137, v29, v33\n"
      "v_mfma_f32_16x16x32_bf16 v[52:55], v[204:207], a[200:203], v[52:55]\n"
      "s_waitcnt vmcnt(10)\n"
      "v_cvt_pk_bf16_f32 v138, v30, v34\n"
      "v_cvt_pk_bf16_f32 v139, v31, v35\n"
      "s_barrier\n"
      "v_mfma_f32_4x4x4_16b_bf16 v[28:31], a[244:245], a[232:233], 0\n"
      "v_mfma_f32_4x4x4_16b_bf16 v[32:35], a[244:245], a[234:235], 0\n"
      "buffer_load_dwordx4 v93, s[12:15], s52 offen offset:1024 lds\n"
      "v_mfma_f32_4x4x4_16b_bf16 v[28:31], a[248:249], a[64:65], v[28:31]\n"
      "ds_read_b64_tr_b16 a[224:225], v95 offset:6144\n"
      "v_mfma_f32_4x4x4_16b_bf16 v[32:35], a[248:249], a[66:67], v[32:35]\n"
      "buffer_load_dwordx4 v94, s[12:15], s52 offen offset:2048 lds\n"
      "s_waitcnt lgkmcnt(1)\n"
      "v_mfma_f32_4x4x4_16b_bf16 v[36:39], a[246:247], a[228:229], v[28:31]\n"
      "ds_read_b64_tr_b16 a[226:227], v95 offset:6912\n"
      "v_mfma_f32_4x4x4_16b_bf16 v[40:43], a[246:247], a[230:231], v[32:35]\n"
      "ds_read_b64_tr_b16 a[72:73], v95 offset:18432\n"
      "v_mfma_f32_4x4x4_16b_bf16 v[28:31], a[250:251], a[228:229], v[28:31]\n"
      "ds_read_b64_tr_b16 a[74:75], v95 offset:19200\n"
      "v_mfma_f32_4x4x4_16b_bf16 v[32:35], a[250:251], a[230:231], v[32:35]\n"
      "ds_read_b64_tr_b16 a[220:221], v95 offset:12288\n"
      "ds_read_b64_tr_b16 a[222:223], v95 offset:13056\n"
      "ds_read_b128 a[196:199], v1 offset:64\n"
      "v_mfma_f32_16x16x32_bf16 v[56:59], v[196:199], a[200:203], v[56:59]\n"
      "v_cvt_pk_bf16_f32 v132, v84, v88\n"
      "v_cvt_pk_bf16_f32 v133, v85, v89\n"
      "v_mfma_f32_16x16x32_bf16 v[60:63], v[188:191], a[200:203], v[60:63]\n"
      "v_cvt_pk_bf16_f32 v134, v86, v90\n"
      "v_cvt_pk_bf16_f32 v135, v87, v91\n"
      "v_mfma_f32_16x16x32_bf16 v[64:67], v[200:203], a[204:207], v[64:67]\n"
      "v_cvt_pk_bf16_f32 v128, v76, v80\n"
      "v_cvt_pk_bf16_f32 v129, v77, v81\n"
      "v_mfma_f32_16x16x32_bf16 v[68:71], v[192:195], a[204:207], v[68:71]\n"
      "v_cvt_pk_bf16_f32 v130, v78, v82\n"
      "v_cvt_pk_bf16_f32 v131, v79, v83\n"
      "v_mfma_f32_4x4x4_16b_bf16 v[76:79], a[244:245], a[240:241], 0\n"
      "v_mfma_f32_4x4x4_16b_bf16 v[80:83], a[244:245], a[242:243], 0\n"
      "ds_read_b64_tr_b16 a[232:233], v95 offset:6208\n"
      "v_mfma_f32_4x4x4_16b_bf16 v[76:79], a[248:249], a[68:69], v[76:79]\n"
      "ds_read_b64_tr_b16 a[234:235], v95 offset:6976\n"
      "v_mfma_f32_4x4x4_16b_bf16 v[80:83], a[248:249], a[70:71], v[80:83]\n"
      "ds_read_b64_tr_b16 a[76:77], v95 offset:18496\n"
      "v_mfma_f32_4x4x4_16b_bf16 v[84:87], a[246:247], a[236:237], v[76:79]\n"
      "ds_read_b64_tr_b16 a[78:79], v95 offset:19264\n"
      "v_mfma_f32_4x4x4_16b_bf16 v[88:91], a[246:247], a[238:239], v[80:83]\n"
      "ds_read_b64_tr_b16 a[228:229], v95 offset:12352\n"
      "v_mfma_f32_4x4x4_16b_bf16 v[76:79], a[250:251], a[236:237], v[76:79]\n"
      "ds_read_b64_tr_b16 a[230:231], v95 offset:13120\n"
      "v_mfma_f32_4x4x4_16b_bf16 v[80:83], a[250:251], a[238:239], v[80:83]\n"
      "ds_read_b128 a[200:203], v1 offset:4160\n"
      "v_mfma_f32_16x16x32_bf16 v[72:75], v[184:187], a[204:207], v[72:75]\n"
      "v_cvt_pk_bf16_f32 v124, v36, v40\n"
      "v_cvt_pk_bf16_f32 v125, v37, v41\n"
      "v_mfma_f32_16x16x32_bf16 v[52:55], v[180:183], a[208:211], v[52:55]\n"
      "v_cvt_pk_bf16_f32 v126, v38, v42\n"
      "v_cvt_pk_bf16_f32 v127, v39, v43\n"
      "v_mfma_f32_16x16x32_bf16 v[56:59], v[172:175], a[208:211], v[56:59]\n"
      "v_cvt_pk_bf16_f32 v28, v28, v32\n"
      "v_cvt_pk_bf16_f32 v29, v29, v33\n"
      "v_mfma_f32_16x16x32_bf16 v[60:63], v[164:167], a[208:211], v[60:63]\n"
      "v_cvt_pk_bf16_f32 v30, v30, v34\n"
      "v_cvt_pk_bf16_f32 v31, v31, v35\n"
      "v_mfma_f32_16x16x32_bf16 v[64:67], v[176:179], a[212:215], v[64:67]\n"
      "s_nop 0\n"
      "v_accvgpr_write_b32 a188, v28\n"
      "v_accvgpr_write_b32 a189, v29\n"
      "v_mfma_f32_16x16x32_bf16 v[68:71], v[168:171], a[212:215], v[68:71]\n"
      "s_waitcnt lgkmcnt(0)\n"
      "v_accvgpr_write_b32 a190, v30\n"
      "v_accvgpr_write_b32 a191, v31\n"
      "v_mfma_f32_4x4x4_16b_bf16 v[28:31], a[244:245], a[224:225], 0\n"
      "v_mfma_f32_4x4x4_16b_bf16 v[32:35], a[244:245], a[226:227], 0\n"
      "ds_read_b64_tr_b16 a[240:241], v95 offset:6272\n"
      "v_mfma_f32_4x4x4_16b_bf16 v[28:31], a[248:249], a[72:73], v[28:31]\n"
      "ds_read_b64_tr_b16 a[242:243], v95 offset:7040\n"
      "v_mfma_f32_4x4x4_16b_bf16 v[32:35], a[248:249], a[74:75], v[32:35]\n"
      "ds_read_b64_tr_b16 a[80:81], v95 offset:18560\n"
      "v_mfma_f32_4x4x4_16b_bf16 v[36:39], a[246:247], a[220:221], v[28:31]\n"
      "ds_read_b64_tr_b16 a[82:83], v95 offset:19328\n"
      "v_mfma_f32_4x4x4_16b_bf16 v[40:43], a[246:247], a[222:223], v[32:35]\n"
      "ds_read_b64_tr_b16 a[236:237], v95 offset:12416\n"
      "v_mfma_f32_4x4x4_16b_bf16 v[28:31], a[250:251], a[220:221], v[28:31]\n"
      "ds_read_b64_tr_b16 a[238:239], v95 offset:13184\n"
      "v_mfma_f32_4x4x4_16b_bf16 v[32:35], a[250:251], a[222:223], v[32:35]\n"
      "ds_read_b128 a[204:207], v4 offset:128\n"
      "ds_read_b128 a[208:211], v1 offset:128\n"
      "v_cvt_pk_bf16_f32 v84, v84, v88\n"
      "v_cvt_pk_bf16_f32 v85, v85, v89\n"
      "v_cvt_pk_bf16_f32 v86, v86, v90\n"
      "v_cvt_pk_bf16_f32 v87, v87, v91\n"
      "v_mfma_f32_16x16x32_bf16 v[72:75], v[160:163], a[212:215], v[72:75]\n"
      "v_accvgpr_write_b32 a184, v84\n"
      "v_accvgpr_write_b32 a185, v85\n"
      "v_mfma_f32_16x16x32_bf16 v[100:103], a[48:51], a[192:195], v[100:103]\n"
      "v_accvgpr_write_b32 a186, v86\n"
      "v_accvgpr_write_b32 a187, v87\n"
      "v_mfma_f32_16x16x32_bf16 v[104:107], a[52:55], a[192:195], v[104:107]\n"
      "v_cvt_pk_bf16_f32 v76, v76, v80\n"
      "v_cvt_pk_bf16_f32 v77, v77, v81\n"
      "v_mfma_f32_16x16x32_bf16 v[108:111], a[56:59], a[192:195], v[108:111]\n"
      "v_cvt_pk_bf16_f32 v78, v78, v82\n"
      "v_cvt_pk_bf16_f32 v79, v79, v83\n"
      "v_mfma_f32_16x16x32_bf16 v[52:55], v[156:159], a[196:199], v[52:55]\n"
      "v_accvgpr_write_b32 a180, v76\n"
      "v_accvgpr_write_b32 a181, v77\n"
      "v_mfma_f32_16x16x32_bf16 v[56:59], v[148:151], a[196:199], v[56:59]\n"
      "s_waitcnt vmcnt(6)\n"
      "v_accvgpr_write_b32 a182, v78\n"
      "v_accvgpr_write_b32 a183, v79\n"
      "s_barrier\n"
      "v_mfma_f32_4x4x4_16b_bf16 v[76:79], a[244:245], a[232:233], 0\n"
      "v_mfma_f32_4x4x4_16b_bf16 v[80:83], a[244:245], a[234:235], 0\n"
      "ds_read_b64_tr_b16 a[224:225], v95 offset:41216\n"
      "v_mfma_f32_4x4x4_16b_bf16 v[76:79], a[248:249], a[76:77], v[76:79]\n"
      "ds_read_b64_tr_b16 a[226:227], v95 offset:41984\n"
      "v_mfma_f32_4x4x4_16b_bf16 v[80:83], a[248:249], a[78:79], v[80:83]\n"
      "ds_read_b64_tr_b16 a[84:85], v95 offset:53504\n"
      "v_mfma_f32_4x4x4_16b_bf16 v[84:87], a[246:247], a[228:229], v[76:79]\n"
      "ds_read_b64_tr_b16 a[86:87], v95 offset:54272\n"
      "v_mfma_f32_4x4x4_16b_bf16 v[88:91], a[246:247], a[230:231], v[80:83]\n"
      "ds_read_b64_tr_b16 a[220:221], v95 offset:47360\n"
      "v_mfma_f32_4x4x4_16b_bf16 v[76:79], a[250:251], a[228:229], v[76:79]\n"
      "ds_read_b64_tr_b16 a[222:223], v95 offset:48128\n"
      "v_mfma_f32_4x4x4_16b_bf16 v[80:83], a[250:251], a[230:231], v[80:83]\n"
      "ds_read_b128 a[212:215], v1 offset:4224\n"
      "ds_read_b128 a[192:195], v5 offset:0\n"
      "v_cvt_pk_bf16_f32 v36, v36, v40\n"
      "v_cvt_pk_bf16_f32 v37, v37, v41\n"
      "v_cvt_pk_bf16_f32 v38, v38, v42\n"
      "v_cvt_pk_bf16_f32 v39, v39, v43\n"
      "v_mfma_f32_16x16x32_bf16 v[60:63], v[140:143], a[196:199], v[60:63]\n"
      "v_accvgpr_write_b32 a176, v36\n"
      "v_accvgpr_write_b32 a177, v37\n"
      "v_mfma_f32_16x16x32_bf16 v[64:67], v[152:155], a[200:203], v[64:67]\n"
      "v_accvgpr_write_b32 a178, v38\n"
      "v_accvgpr_write_b32 a179, v39\n"
      "v_mfma_f32_16x16x32_bf16 v[68:71], v[144:147], a[200:203], v[68:71]\n"
      "s_nop 0\n"
      "v_cvt_pk_bf16_f32 v28, v28, v32\n"
      "v_cvt_pk_bf16_f32 v29, v29, v33\n"
      "v_mfma_f32_16x16x32_bf16 v[72:75], v[136:139], a[200:203], v[72:75]\n"
      "s_waitcnt lgkmcnt(1)\n"
      "v_cvt_pk_bf16_f32 v30, v30, v34\n"
      "v_cvt_pk_bf16_f32 v31, v31, v35\n"
      "v_mfma_f32_16x16x32_bf16 v[100:103], a[60:63], a[204:207], v[100:103]\n"
      "v_accvgpr_write_b32 a172, v28\n"
      "v_accvgpr_write_b32 a173, v29\n"
      "v_mfma_f32_16x16x32_bf16 v[104:107], a[64:67], a[204:207], v[104:107]\n"
      "v_accvgpr_write_b32 a174, v30\n"
      "v_accvgpr_write_b32 a175, v31\n"
      "v_mfma_f32_4x4x4_16b_bf16 v[28:31], a[244:245], a[240:241], 0\n"
      "v_mfma_f32_4x4x4_16b_bf16 v[32:35], a[244:245], a[242:243], 0\n"
      "ds_read_b64_tr_b16 a[232:233], v95 offset:41280\n"
      "v_mfma_f32_4x4x4_16b_bf16 v[28:31], a[248:249], a[80:81], v[28:31]\n"
      "ds_read_b64_tr_b16 a[234:235], v95 offset:42048\n"
      "v_mfma_f32_4x4x4_16b_bf16 v[32:35], a[248:249], a[82:83], v[32:35]\n"
      "ds_read_b64_tr_b16 a[88:89], v95 offset:53568\n"
      "v_mfma_f32_4x4x4_16b_bf16 v[36:39], a[246:247], a[236:237], v[28:31]\n"
      "ds_read_b64_tr_b16 a[90:91], v95 offset:54336\n"
      "v_mfma_f32_4x4x4_16b_bf16 v[40:43], a[246:247], a[238:239], v[32:35]\n"
      "ds_read_b64_tr_b16 a[228:229], v95 offset:47424\n"
      "v_mfma_f32_4x4x4_16b_bf16 v[28:31], a[250:251], a[236:237], v[28:31]\n"
      "ds_read_b64_tr_b16 a[230:231], v95 offset:48192\n"
      "v_mfma_f32_4x4x4_16b_bf16 v[32:35], a[250:251], a[238:239], v[32:35]\n"
      "ds_read_b128 a[196:199], v2 offset:0\n"
      "ds_read_b128 a[200:203], v2 offset:4096\n"
      "v_cvt_pk_bf16_f32 v84, v84, v88\n"
      "v_cvt_pk_bf16_f32 v85, v85, v89\n"
      "v_cvt_pk_bf16_f32 v86, v86, v90\n"
      "v_cvt_pk_bf16_f32 v87, v87, v91\n"
      "v_mfma_f32_16x16x32_bf16 v[108:111], a[68:71], a[204:207], v[108:111]\n"
      "v_accvgpr_write_b32 a168, v84\n"
      "v_accvgpr_write_b32 a169, v85\n"
      "v_mfma_f32_16x16x32_bf16 v[52:55], v[132:135], a[208:211], v[52:55]\n"
      "v_accvgpr_write_b32 a170, v86\n"
      "v_accvgpr_write_b32 a171, v87\n"
      "v_mfma_f32_16x16x32_bf16 v[56:59], v[124:127], a[208:211], v[56:59]\n"
      "v_cvt_pk_bf16_f32 v76, v76, v80\n"
      "v_cvt_pk_bf16_f32 v77, v77, v81\n"
      "v_mfma_f32_16x16x32_bf16 v[60:63], a[184:187], a[208:211], v[60:63]\n"
      "v_cvt_pk_bf16_f32 v78, v78, v82\n"
      "v_cvt_pk_bf16_f32 v79, v79, v83\n"
      "v_mfma_f32_16x16x32_bf16 v[64:67], v[128:131], a[212:215], v[64:67]\n"
      "v_accvgpr_write_b32 a164, v76\n"
      "v_accvgpr_write_b32 a165, v77\n"
      "v_mfma_f32_16x16x32_bf16 v[68:71], a[188:191], a[212:215], v[68:71]\n"
      "v_accvgpr_write_b32 a166, v78\n"
      "v_accvgpr_write_b32 a167, v79\n"
      "v_mfma_f32_4x4x4_16b_bf16 v[76:79], a[244:245], a[224:225], 0\n"
      "v_mfma_f32_4x4x4_16b_bf16 v[80:83], a[244:245], a[226:227], 0\n"
      "ds_read_b64_tr_b16 a[240:241], v95 offset:41344\n"
      "v_mfma_f32_4x4x4_16b_bf16 v[76:79], a[248:249], a[84:85], v[76:79]\n"
      "ds_read_b64_tr_b16 a[242:243], v95 offset:42112\n"
      "v_mfma_f32_4x4x4_16b_bf16 v[80:83], a[248:249], a[86:87], v[80:83]\n"
      "ds_read_b64_tr_b16 a[92:93], v95 offset:53632\n"
      "v_mfma_f32_4x4x4_16b_bf16 v[84:87], a[246:247], a[220:221], v[76:79]\n"
      "ds_read_b64_tr_b16 a[94:95], v95 offset:54400\n"
      "v_mfma_f32_4x4x4_16b_bf16 v[88:91], a[246:247], a[222:223], v[80:83]\n"
      "ds_read_b64_tr_b16 a[236:237], v95 offset:47488\n"
      "v_mfma_f32_4x4x4_16b_bf16 v[76:79], a[250:251], a[220:221], v[76:79]\n"
      "ds_read_b64_tr_b16 a[238:239], v95 offset:48256\n"
      "v_mfma_f32_4x4x4_16b_bf16 v[80:83], a[250:251], a[222:223], v[80:83]\n"
      "v_cvt_pk_bf16_f32 v36, v36, v40\n"
      "v_cvt_pk_bf16_f32 v37, v37, v41\n"
      "v_cvt_pk_bf16_f32 v38, v38, v42\n"
      "v_cvt_pk_bf16_f32 v39, v39, v43\n"
      "v_accvgpr_write_b32 a160, v36\n"
      "v_accvgpr_write_b32 a161, v37\n"
      "v_accvgpr_write_b32 a162, v38\n"
      "v_accvgpr_write_b32 a163, v39\n"
      "v_mfma_f32_16x16x32_bf16 v[72:75], a[180:183], a[212:215], v[72:75]\n"
      "s_waitcnt lgkmcnt(2)\n"
      "v_cvt_pk_bf16_f32 v28, v28, v32\n"
      "v_cvt_pk_bf16_f32 v29, v29, v33\n"
      "v_mfma_f32_16x16x32_bf16 v[100:103], a[72:75], a[192:195], v[100:103]\n"
      "s_nop 0\n"
      "v_cvt_pk_bf16_f32 v30, v30, v34\n"
      "v_cvt_pk_bf16_f32 v31, v31, v35\n"
      "v_mfma_f32_16x16x32_bf16 v[104:107], a[76:79], a[192:195], v[104:107]\n"
      "v_accvgpr_write_b32 a156, v28\n"
      "v_accvgpr_write_b32 a157, v29\n"
      "v_mfma_f32_16x16x32_bf16 v[108:111], a[80:83], a[192:195], v[108:111]\n"
      "s_waitcnt vmcnt(0)\n"
      "v_accvgpr_write_b32 a158, v30\n"
      "v_accvgpr_write_b32 a159, v31\n"
      "s_barrier\n"
      "v_mfma_f32_4x4x4_16b_bf16 v[28:31], a[244:245], a[232:233], 0\n"
      "v_mfma_f32_4x4x4_16b_bf16 v[32:35], a[244:245], a[234:235], 0\n"
      "ds_read_b64_tr_b16 a[224:225], v96 offset:2304\n"
      "v_mfma_f32_4x4x4_16b_bf16 v[28:31], a[248:249], a[88:89], v[28:31]\n"
      "ds_read_b64_tr_b16 a[226:227], v96 offset:3072\n"
      "v_mfma_f32_4x4x4_16b_bf16 v[32:35], a[248:249], a[90:91], v[32:35]\n"
      "ds_read_b64_tr_b16 a[96:97], v96 offset:14592\n"
      "v_mfma_f32_4x4x4_16b_bf16 v[36:39], a[246:247], a[228:229], v[28:31]\n"
      "ds_read_b64_tr_b16 a[98:99], v96 offset:15360\n"
      "v_mfma_f32_4x4x4_16b_bf16 v[40:43], a[246:247], a[230:231], v[32:35]\n"
      "ds_read_b64_tr_b16 a[220:221], v96 offset:8448\n"
      "v_mfma_f32_4x4x4_16b_bf16 v[28:31], a[250:251], a[228:229], v[28:31]\n"
      "ds_read_b64_tr_b16 a[222:223], v96 offset:9216\n"
      "v_mfma_f32_4x4x4_16b_bf16 v[32:35], a[250:251], a[230:231], v[32:35]\n"
      "ds_read_b128 a[204:207], v5 offset:64\n"
      "v_cvt_pk_bf16_f32 v84, v84, v88\n"
      "v_cvt_pk_bf16_f32 v85, v85, v89\n"
      "v_cvt_pk_bf16_f32 v86, v86, v90\n"
      "v_cvt_pk_bf16_f32 v87, v87, v91\n"
      "v_mfma_f32_16x16x32_bf16 v[52:55], a[176:179], a[196:199], v[52:55]\n"
      "v_accvgpr_write_b32 a152, v84\n"
      "v_accvgpr_write_b32 a153, v85\n"
      "v_mfma_f32_16x16x32_bf16 v[56:59], a[168:171], a[196:199], v[56:59]\n"
      "v_accvgpr_write_b32 a154, v86\n"
      "v_accvgpr_write_b32 a155, v87\n"
      "v_mfma_f32_16x16x32_bf16 v[60:63], a[160:163], a[196:199], v[60:63]\n"
      "s_nop 0\n"
      "v_cvt_pk_bf16_f32 v76, v76, v80\n"
      "v_cvt_pk_bf16_f32 v77, v77, v81\n"
      "v_mfma_f32_16x16x32_bf16 v[64:67], a[172:175], a[200:203], v[64:67]\n"
      "s_nop 0\n"
      "v_cvt_pk_bf16_f32 v78, v78, v82\n"
      "v_cvt_pk_bf16_f32 v79, v79, v83\n"
      "v_mfma_f32_16x16x32_bf16 v[68:71], a[164:167], a[200:203], v[68:71]\n"
      "s_nop 0\n"
      "v_accvgpr_write_b32 a148, v76\n"
      "v_accvgpr_write_b32 a149, v77\n"
      "v_mfma_f32_16x16x32_bf16 v[72:75], a[156:159], a[200:203], v[72:75]\n"
      "s_nop 0\n"
      "v_accvgpr_write_b32 a150, v78\n"
      "v_accvgpr_write_b32 a151, v79\n"
      "v_mfma_f32_4x4x4_16b_bf16 v[76:79], a[244:245], a[240:241], 0\n"
      "v_mfma_f32_4x4x4_16b_bf16 v[80:83], a[244:245], a[242:243], 0\n"
      "ds_read_b64_tr_b16 a[232:233], v96 offset:2368\n"
      "v_mfma_f32_4x4x4_16b_bf16 v[76:79], a[248:249], a[92:93], v[76:79]\n"
      "ds_read_b64_tr_b16 a[234:235], v96 offset:3136\n"
      "v_mfma_f32_4x4x4_16b_bf16 v[80:83], a[248:249], a[94:95], v[80:83]\n"
      "ds_read_b64_tr_b16 a[100:101], v96 offset:14656\n"
      "s_nop 0\n"
      "s_waitcnt lgkmcnt(3)\n"
      "v_mfma_f32_4x4x4_16b_bf16 v[84:87], a[246:247], a[236:237], v[76:79]\n"
      "ds_read_b64_tr_b16 a[102:103], v96 offset:15424\n"
      "v_mfma_f32_4x4x4_16b_bf16 v[88:91], a[246:247], a[238:239], v[80:83]\n"
      "ds_read_b64_tr_b16 a[228:229], v96 offset:8512\n"
      "v_mfma_f32_4x4x4_16b_bf16 v[76:79], a[250:251], a[236:237], v[76:79]\n"
      "ds_read_b64_tr_b16 a[230:231], v96 offset:9280\n"
      "v_mfma_f32_4x4x4_16b_bf16 v[80:83], a[250:251], a[238:239], v[80:83]\n"
      "ds_read_b128 a[208:211], v2 offset:64\n"
      "ds_read_b128 a[212:215], v2 offset:4160\n"
      "v_cvt_pk_bf16_f32 v36, v36, v40\n"
      "v_cvt_pk_bf16_f32 v37, v37, v41\n"
      "v_cvt_pk_bf16_f32 v38, v38, v42\n"
      "v_cvt_pk_bf16_f32 v39, v39, v43\n"
      "v_accvgpr_write_b32 a144, v36\n"
      "v_accvgpr_write_b32 a145, v37\n"
      "v_accvgpr_write_b32 a146, v38\n"
      "v_accvgpr_write_b32 a147, v39\n"
      "v_cvt_pk_bf16_f32 v28, v28, v32\n"
      "v_cvt_pk_bf16_f32 v29, v29, v33\n"
      "v_cvt_pk_bf16_f32 v30, v30, v34\n"
      "v_cvt_pk_bf16_f32 v31, v31, v35\n"
      "v_accvgpr_write_b32 a140, v28\n"
      "v_accvgpr_write_b32 a141, v29\n"
      "v_accvgpr_write_b32 a142, v30\n"
      "v_accvgpr_write_b32 a143, v31\n"
      "v_mfma_f32_4x4x4_16b_bf16 v[28:31], a[244:245], a[224:225], 0\n"
      "v_mfma_f32_4x4x4_16b_bf16 v[32:35], a[244:245], a[226:227], 0\n"
      "ds_read_b64_tr_b16 a[240:241], v96 offset:2432\n"
      "v_mfma_f32_4x4x4_16b_bf16 v[28:31], a[248:249], a[96:97], v[28:31]\n"
      "ds_read_b64_tr_b16 a[242:243], v96 offset:3200\n"
      "v_mfma_f32_4x4x4_16b_bf16 v[32:35], a[248:249], a[98:99], v[32:35]\n"
      "ds_read_b64_tr_b16 a[104:105], v96 offset:14720\n"
      "v_mfma_f32_4x4x4_16b_bf16 v[36:39], a[246:247], a[220:221], v[28:31]\n"
      "ds_read_b64_tr_b16 a[106:107], v96 offset:15488\n"
      "v_mfma_f32_4x4x4_16b_bf16 v[40:43], a[246:247], a[222:223], v[32:35]\n"
      "ds_read_b64_tr_b16 a[236:237], v96 offset:8576\n"
      "v_mfma_f32_4x4x4_16b_bf16 v[28:31], a[250:251], a[220:221], v[28:31]\n"
      "ds_read_b64_tr_b16 a[238:239], v96 offset:9344\n"
      "v_mfma_f32_4x4x4_16b_bf16 v[32:35], a[250:251], a[222:223], v[32:35]\n"
      "v_cvt_pk_bf16_f32 v84, v84, v88\n"
      "v_cvt_pk_bf16_f32 v85, v85, v89\n"
      "v_cvt_pk_bf16_f32 v86, v86, v90\n"
      "v_cvt_pk_bf16_f32 v87, v87, v91\n"
      "v_accvgpr_write_b32 a136, v84\n"
      "v_accvgpr_write_b32 a137, v85\n"
      "v_accvgpr_write_b32 a138, v86\n"
      "v_accvgpr_write_b32 a139, v87\n"
      "v_cvt_pk_bf16_f32 v76, v76, v80\n"
      "v_cvt_pk_bf16_f32 v77, v77, v81\n"
      "v_mfma_f32_16x16x32_bf16 v[100:103], a[84:87], a[204:207], v[100:103]\n"
      "s_nop 0\n"
      "v_cvt_pk_bf16_f32 v78, v78, v82\n"
      "v_cvt_pk_bf16_f32 v79, v79, v83\n"
      "v_mfma_f32_16x16x32_bf16 v[104:107], a[88:91], a[204:207], v[104:107]\n"
      "s_nop 0\n"
      "v_accvgpr_write_b32 a132, v76\n"
      "v_accvgpr_write_b32 a133, v77\n"
      "v_mfma_f32_16x16x32_bf16 v[108:111], a[92:95], a[204:207], v[108:111]\n"
      "s_waitcnt lgkmcnt(0)\n"
      "v_accvgpr_write_b32 a134, v78\n"
      "v_accvgpr_write_b32 a135, v79\n"
      "v_mfma_f32_4x4x4_16b_bf16 v[76:79], a[244:245], a[232:233], 0\n"
      "v_mfma_f32_4x4x4_16b_bf16 v[80:83], a[244:245], a[234:235], 0\n"
      "ds_read_b128 a[192:195], v5 offset:128\n"
      "v_mfma_f32_4x4x4_16b_bf16 v[76:79], a[248:249], a[100:101], v[76:79]\n"
      "v_mfma_f32_4x4x4_16b_bf16 v[80:83], a[248:249], a[102:103], v[80:83]\n"
      "s_nop 0\n"
      "v_mfma_f32_4x4x4_16b_bf16 v[84:87], a[246:247], a[228:229], v[76:79]\n"
      "v_mfma_f32_4x4x4_16b_bf16 v[88:91], a[246:247], a[230:231], v[80:83]\n"
      "v_mfma_f32_4x4x4_16b_bf16 v[76:79], a[250:251], a[228:229], v[76:79]\n"
      "v_mfma_f32_4x4x4_16b_bf16 v[80:83], a[250:251], a[230:231], v[80:83]\n"
      "v_cvt_pk_bf16_f32 v36, v36, v40\n"
      "v_cvt_pk_bf16_f32 v37, v37, v41\n"
      "v_cvt_pk_bf16_f32 v38, v38, v42\n"
      "v_cvt_pk_bf16_f32 v39, v39, v43\n"
      "v_mfma_f32_16x16x32_bf16 v[52:55], a[152:155], a[208:211], v[52:55]\n"
      "v_accvgpr_write_b32 a128, v36\n"
      "v_accvgpr_write_b32 a129, v37\n"
      "v_mfma_f32_16x16x32_bf16 v[56:59], a[144:147], a[208:211], v[56:59]\n"
      "v_accvgpr_write_b32 a130, v38\n"
      "v_accvgpr_write_b32 a131, v39\n"
      "v_mfma_f32_16x16x32_bf16 v[60:63], a[136:139], a[208:211], v[60:63]\n"
      "v_cvt_pk_bf16_f32 v28, v28, v32\n"
      "v_cvt_pk_bf16_f32 v29, v29, v33\n"
      "v_mfma_f32_16x16x32_bf16 v[64:67], a[148:151], a[212:215], v[64:67]\n"
      "v_cvt_pk_bf16_f32 v30, v30, v34\n"
      "v_cvt_pk_bf16_f32 v31, v31, v35\n"
      "v_mfma_f32_16x16x32_bf16 v[68:71], a[140:143], a[212:215], v[68:71]\n"
      "v_accvgpr_write_b32 a124, v28\n"
      "v_accvgpr_write_b32 a125, v29\n"
      "v_mfma_f32_16x16x32_bf16 v[72:75], a[132:135], a[212:215], v[72:75]\n"
      "v_accvgpr_write_b32 a126, v30\n"
      "v_accvgpr_write_b32 a127, v31\n"
      "v_mfma_f32_4x4x4_16b_bf16 v[28:31], a[244:245], a[240:241], 0\n"
      "v_mfma_f32_4x4x4_16b_bf16 v[32:35], a[244:245], a[242:243], 0\n"
      "ds_read_b128 a[196:199], v2 offset:128\n"
      "v_mfma_f32_4x4x4_16b_bf16 v[28:31], a[248:249], a[104:105], v[28:31]\n"
      "ds_read_b128 a[200:203], v2 offset:4224\n"
      "v_mfma_f32_4x4x4_16b_bf16 v[32:35], a[248:249], a[106:107], v[32:35]\n"
      "v_mfma_f32_4x4x4_16b_bf16 v[36:39], a[246:247], a[236:237], v[28:31]\n"
      "s_nop 0\n"
      "s_nop 0\n"
      "v_mfma_f32_4x4x4_16b_bf16 v[40:43], a[246:247], a[238:239], v[32:35]\n"
      "v_mfma_f32_4x4x4_16b_bf16 v[28:31], a[250:251], a[236:237], v[28:31]\n"
      "v_mfma_f32_4x4x4_16b_bf16 v[32:35], a[250:251], a[238:239], v[32:35]\n"
      "v_cvt_pk_bf16_f32 v84, v84, v88\n"
      "v_cvt_pk_bf16_f32 v85, v85, v89\n"
      "v_cvt_pk_bf16_f32 v86, v86, v90\n"
      "v_cvt_pk_bf16_f32 v87, v87, v91\n"
      "v_accvgpr_write_b32 a120, v84\n"
      "v_accvgpr_write_b32 a121, v85\n"
      "v_accvgpr_write_b32 a122, v86\n"
      "v_accvgpr_write_b32 a123, v87\n"
      "v_cvt_pk_bf16_f32 v76, v76, v80\n"
      "v_cvt_pk_bf16_f32 v77, v77, v81\n"
      "v_cvt_pk_bf16_f32 v78, v78, v82\n"
      "v_cvt_pk_bf16_f32 v79, v79, v83\n"
      "v_accvgpr_write_b32 a116, v76\n"
      "v_accvgpr_write_b32 a117, v77\n"
      "v_accvgpr_write_b32 a118, v78\n"
      "v_accvgpr_write_b32 a119, v79\n"
      "s_waitcnt lgkmcnt(0)\n"
      "s_barrier\n"
      "s_mov_b32 m0, s64\n"
      "s_nop 0\n"
      "v_mfma_f32_16x16x32_bf16 v[100:103], a[96:99], a[192:195], v[100:103]\n"
      "ds_read_b128 a[204:207], v3 offset:16896\n"
      "v_cvt_pk_bf16_f32 v36, v36, v40\n"
      "v_cvt_pk_bf16_f32 v37, v37, v41\n"
      "v_cvt_pk_bf16_f32 v38, v38, v42\n"
      "v_cvt_pk_bf16_f32 v39, v39, v43\n"
      "v_mfma_f32_16x16x32_bf16 v[104:107], a[100:103], a[192:195], v[104:107]\n"
      "ds_read_b128 a[208:211], v3 offset:16960\n"
      "ds_read_b64_tr_b16 a[220:221], v7 offset:16896\n"
      "ds_read_b64_tr_b16 a[222:223], v7 offset:16904\n"
      "v_accvgpr_write_b32 a112, v36\n"
      "v_mfma_f32_16x16x32_bf16 v[108:111], a[104:107], a[192:195], v[108:111]\n"
      "v_accvgpr_write_b32 a113, v37\n"
      "v_accvgpr_write_b32 a114, v38\n"
      "v_accvgpr_write_b32 a115, v39\n"
      "v_cvt_pk_bf16_f32 v28, v28, v32\n"
      "v_cvt_pk_bf16_f32 v29, v29, v33\n"
      "v_cvt_pk_bf16_f32 v30, v30, v34\n"
      "v_mfma_f32_16x16x32_bf16 v[52:55], a[128:131], a[196:199], v[52:55]\n"
      "ds_read_b128 a[212:215], v3 offset:17024\n"
      "v_cvt_pk_bf16_f32 v31, v31, v35\n"
      "v_accvgpr_write_b32 a108, v28\n"
      "v_accvgpr_write_b32 a109, v29\n"
      "v_accvgpr_write_b32 a110, v30\n"
      "v_accvgpr_write_b32 a111, v31\n"
      "v_mfma_f32_16x16x32_bf16 v[56:59], a[120:123], a[196:199], v[56:59]\n"
      "v_mfma_f32_16x16x32_bf16 v[60:63], a[112:115], a[196:199], v[60:63]\n"
      "v_mfma_f32_16x16x32_bf16 v[64:67], a[124:127], a[200:203], v[64:67]\n"
      "ds_read_b128 a[192:195], v4 offset:16896\n"
      "v_mfma_f32_16x16x32_bf16 v[68:71], a[116:119], a[200:203], v[68:71]\n"
      "s_nop 0\n"
      "v_mfma_f32_16x16x32_bf16 v[72:75], a[108:111], a[200:203], v[72:75]\n"
      "s_waitcnt lgkmcnt(1)\n"
      "v_mfma_f32_4x4x4_16b_bf16 v[20:23], a[216:217], a[220:221], 0\n"
      "v_mfma_f32_4x4x4_16b_bf16 v[24:27], a[216:217], a[222:223], 0\n"
      "v_mfma_f32_16x16x32_bf16 v[112:115], a[0:3], a[204:207], 0\n"
      "ds_read_b128 a[196:199], v4 offset:16960\n"
      "v_sub_f32 v52, v64, v52\n"
      "v_sub_f32 v53, v65, v53\n"
      "v_sub_f32 v54, v66, v54\n"
      "v_sub_f32 v55, v67, v55\n"
      "v_sub_f32 v56, v68, v56\n"
      "v_mfma_f32_16x16x32_bf16 v[116:119], a[4:7], a[204:207], 0\n"
      "s_nop 0\n"
      "ds_read_b64_tr_b16 a[224:225], v7 offset:17408\n"
      "ds_write_addtid_b32 v52 offset:0\n"
      "v_cvt_pk_bf16_f32 v20, v20, v24\n"
      "v_mfma_f32_16x16x32_bf16 v[120:123], a[8:11], a[204:207], 0\n"
      "ds_write_addtid_b32 v20 offset:41216\n"
      "ds_read_b64_tr_b16 a[226:227], v7 offset:17416\n"
      "v_mfma_f32_16x16x32_bf16 v[112:115], a[12:15], a[208:211], v[112:115]\n"
      "ds_read_b128 a[200:203], v4 offset:17024\n"
      "ds_write_addtid_b32 v53 offset:512\n"
      "v_mfma_f32_16x16x32_bf16 v[116:119], a[16:19], a[208:211], v[116:119]\n"
      "ds_write_addtid_b32 v54 offset:1024\n"
      "ds_write_addtid_b32 v55 offset:1536\n"
      "v_mfma_f32_16x16x32_bf16 v[120:123], a[20:23], a[208:211], v[120:123]\n"
      "ds_write_addtid_b32 v56 offset:2048\n"
      "v_add_f32 v64, v64, v100\n"
      "v_mfma_f32_16x16x32_bf16 v[112:115], a[24:27], a[212:215], v[112:115]\n"
      "s_nop 0\n"
      "ds_read_b128 a[204:207], v5 offset:16896\n"
      "v_cvt_pk_bf16_f32 v21, v21, v25\n"
      "v_mfma_f32_16x16x32_bf16 v[116:119], a[28:31], a[212:215], v[116:119]\n"
      "ds_write_addtid_b32 v21 offset:45312\n"
      "v_sub_f32 v57, v69, v57\n"
      "v_mfma_f32_16x16x32_bf16 v[120:123], a[32:35], a[212:215], v[120:123]\n"
      "s_waitcnt lgkmcnt(5)\n"
      "ds_write_addtid_b32 v57 offset:2560\n"
      "v_add_f32_e64 v65, v65, v101\n"
      "v_mfma_f32_4x4x4_16b_bf16 v[28:31], a[216:217], a[224:225], 0\n"
      "v_cvt_pk_bf16_f32 v22, v22, v26\n"
      "v_mfma_f32_4x4x4_16b_bf16 v[32:35], a[216:217], a[226:227], 0\n"
      "ds_write_addtid_b32 v22 offset:49408\n"
      "v_add_f32 v66, v66, v102\n"
      "v_mfma_f32_16x16x32_bf16 v[112:115], a[36:39], a[192:195], v[112:115]\n"
      "ds_read_b128 a[208:211], v5 offset:16960\n"
      "v_cvt_pk_bf16_f32 v23, v23, v27\n"
      "v_mfma_f32_16x16x32_bf16 v[116:119], a[40:43], a[192:195], v[116:119]\n"
      "s_nop 0\n"
      "ds_write_addtid_b32 v23 offset:53504\n"
      "ds_read_b64_tr_b16 a[228:229], v7 offset:17920\n"
      "v_mfma_f32_16x16x32_bf16 v[120:123], a[44:47], a[192:195], v[120:123]\n"
      "ds_read_b64_tr_b16 a[230:231], v7 offset:17928\n"
      "v_sub_f32 v58, v70, v58\n"
      "v_mfma_f32_16x16x32_bf16 v[112:115], a[48:51], a[196:199], v[112:115]\n"
      "s_nop 0\n"
      "ds_read_b128 a[212:215], v5 offset:17024\n"
      "ds_write_addtid_b32 v58 offset:3072\n"
      "v_mfma_f32_16x16x32_bf16 v[116:119], a[52:55], a[196:199], v[116:119]\n"
      "v_cvt_pk_bf16_f32 v28, v28, v32\n"
      "v_add_f32 v67, v67, v103\n"
      "v_mfma_f32_16x16x32_bf16 v[120:123], a[56:59], a[196:199], v[120:123]\n"
      "ds_write_addtid_b32 v28 offset:41728\n"
      "v_cvt_pk_bf16_f32 v29, v29, v33\n"
      "v_mfma_f32_16x16x32_bf16 v[112:115], a[60:63], a[200:203], v[112:115]\n"
      "s_nop 0\n"
      "ds_read_b128 a[192:195], v3 offset:33792\n"
      "ds_write_addtid_b32 v29 offset:45824\n"
      "v_mfma_f32_16x16x32_bf16 v[116:119], a[64:67], a[200:203], v[116:119]\n"
      "v_sub_f32 v59, v71, v59\n"
      "v_cvt_pk_bf16_f32 v30, v30, v34\n"
      "v_mfma_f32_16x16x32_bf16 v[120:123], a[68:71], a[200:203], v[120:123]\n"
      "s_waitcnt lgkmcnt(3)\n"
      "ds_write_addtid_b32 v30 offset:49920\n"
      "ds_write_addtid_b32 v59 offset:3584\n"
      "v_mfma_f32_4x4x4_16b_bf16 v[20:23], a[216:217], a[228:229], 0\n"
      "v_add_f32 v68, v68, v104\n"
      "v_mfma_f32_4x4x4_16b_bf16 v[24:27], a[216:217], a[230:231], 0\n"
      "v_cvt_pk_bf16_f32 v31, v31, v35\n"
      "v_sub_f32 v60, v72, v60\n"
      "v_mfma_f32_16x16x32_bf16 v[112:115], a[72:75], a[204:207], v[112:115]\n"
      "ds_read_b128 a[196:199], v3 offset:33856\n"
      "ds_write_addtid_b32 v31 offset:54016\n"
      "v_mfma_f32_16x16x32_bf16 v[116:119], a[76:79], a[204:207], v[116:119]\n"
      "ds_read_b64_tr_b16 a[232:233], v7 offset:18432\n"
      "ds_read_b64_tr_b16 a[234:235], v7 offset:18440\n"
      "v_mfma_f32_16x16x32_bf16 v[120:123], a[80:83], a[204:207], v[120:123]\n"
      "ds_write_addtid_b32 v60 offset:4096\n"
      "v_cvt_pk_bf16_f32 v20, v20, v24\n"
      "v_mfma_f32_16x16x32_bf16 v[112:115], a[84:87], a[208:211], v[112:115]\n"
      "ds_read_b128 a[200:203], v3 offset:33920\n"
      "ds_write_addtid_b32 v20 offset:42240\n"
      "v_mfma_f32_16x16x32_bf16 v[116:119], a[88:91], a[208:211], v[116:119]\n"
      "v_add_f32 v69, v69, v105\n"
      "v_cvt_pk_bf16_f32 v21, v21, v25\n"
      "v_mfma_f32_16x16x32_bf16 v[120:123], a[92:95], a[208:211], v[120:123]\n"
      "ds_write_addtid_b32 v21 offset:46336\n"
      "v_sub_f32 v61, v73, v61\n"
      "v_mfma_f32_16x16x32_bf16 v[112:115], a[96:99], a[212:215], v[112:115]\n"
      "ds_read_b128 a[204:207], v4 offset:33792\n"
      "ds_write_addtid_b32 v61 offset:4608\n"
      "v_mfma_f32_16x16x32_bf16 v[116:119], a[100:103], a[212:215], v[116:119]\n"
      "v_add_f32 v70, v70, v106\n"
      "v_cvt_pk_bf16_f32 v22, v22, v26\n"
      "v_mfma_f32_16x16x32_bf16 v[120:123], a[104:107], a[212:215], v[120:123]\n"
      "s_waitcnt lgkmcnt(3)\n"
      "ds_write_addtid_b32 v22 offset:50432\n"
      "v_sub_f32_e64 v62, v74, v62\n"
      "v_mfma_f32_4x4x4_16b_bf16 v[28:31], a[216:217], a[232:233], 0\n"
      "v_cvt_pk_bf16_f32 v23, v23, v27\n"
      "v_mfma_f32_4x4x4_16b_bf16 v[32:35], a[216:217], a[234:235], 0\n"
      "ds_write_addtid_b32 v23 offset:54528\n"
      "ds_write_addtid_b32 v62 offset:5120\n"
      "v_mfma_f32_16x16x32_bf16 v[76:79], a[0:3], a[192:195], 0\n"
      "ds_read_b128 a[208:211], v4 offset:33856\n"
      "v_add_f32 v71, v71, v107\n"
      "v_mfma_f32_16x16x32_bf16 v[80:83], a[4:7], a[192:195], 0\n"
      "s_nop 0\n"
      "v_cvt_pk_bf16_f32 v28, v28, v32\n"
      "v_cvt_pk_bf16_f32 v29, v29, v33\n"
      "v_mfma_f32_16x16x32_bf16 v[84:87], a[8:11], a[192:195], 0\n"
      "ds_write_addtid_b32 v28 offset:42752\n"
      "ds_write_addtid_b32 v29 offset:46848\n"
      "v_mfma_f32_16x16x32_bf16 v[76:79], a[12:15], a[196:199], v[76:79]\n"
      "ds_read_b128 a[212:215], v4 offset:33920\n"
      "v_add_f32 v72, v72, v108\n"
      "v_mfma_f32_16x16x32_bf16 v[80:83], a[16:19], a[196:199], v[80:83]\n"
      "v_sub_f32 v63, v75, v63\n"
      "v_cvt_pk_bf16_f32 v30, v30, v34\n"
      "v_mfma_f32_16x16x32_bf16 v[84:87], a[20:23], a[196:199], v[84:87]\n"
      "ds_write_addtid_b32 v30 offset:50944\n"
      "ds_write_addtid_b32 v63 offset:5632\n"
      "v_mfma_f32_16x16x32_bf16 v[76:79], a[24:27], a[200:203], v[76:79]\n"
      "ds_read_b128 a[192:195], v5 offset:33792\n"
      "v_add_f32 v73, v73, v109\n"
      "v_mfma_f32_16x16x32_bf16 v[80:83], a[28:31], a[200:203], v[80:83]\n"
      "v_cvt_pk_bf16_f32 v31, v31, v35\n"
      "v_add_f32 v74, v74, v110\n"
      "v_mfma_f32_16x16x32_bf16 v[84:87], a[32:35], a[200:203], v[84:87]\n"
      "s_waitcnt lgkmcnt(3)\n"
      "ds_write_addtid_b32 v31 offset:55040\n"
      "v_add_f32 v75, v75, v111\n"
      "v_mfma_f32_16x16x32_bf16 v[76:79], a[36:39], a[204:207], v[76:79]\n"
      "ds_read_b128 a[196:199], v5 offset:33856\n"
      "v_mfma_f32_16x16x32_bf16 v[80:83], a[40:43], a[204:207], v[80:83]\n"
      "v_fma_f32 v40, 0.5, v52, -v64\n"
      "v_fma_f32 v41, 0.5, v53, -v65\n"
      "v_mfma_f32_16x16x32_bf16 v[84:87], a[44:47], a[204:207], v[84:87]\n"
      "v_fma_f32 v42, 0.5, v54, -v66\n"
      "v_fma_f32 v43, 0.5, v55, -v67\n"
      "v_mfma_f32_16x16x32_bf16 v[76:79], a[48:51], a[208:211], v[76:79]\n"
      "ds_read_b128 a[200:203], v5 offset:33920\n"
      "v_fma_f32 v44, 0.5, v56, -v68\n"
      "v_mfma_f32_16x16x32_bf16 v[80:83], a[52:55], a[208:211], v[80:83]\n"
      "v_fma_f32 v45, 0.5, v57, -v69\n"
      "v_mfma_f32_16x16x32_bf16 v[84:87], a[56:59], a[208:211], v[84:87]\n"
      "v_mfma_f32_16x16x32_bf16 v[76:79], a[60:63], a[212:215], v[76:79]\n"
      "v_mfma_f32_16x16x32_bf16 v[80:83], a[64:67], a[212:215], v[80:83]\n"
      "v_mfma_f32_16x16x32_bf16 v[84:87], a[68:71], a[212:215], v[84:87]\n"
      "s_waitcnt lgkmcnt(0)\n"
      "s_barrier\n"
      "LSloop_%=:\n"
      "v_mfma_f32_16x16x32_bf16 v[76:79], a[72:75], a[192:195], v[76:79]\n"
      "ds_read_b128 a[204:207], v0 offset:16384\n"
      "ds_read2st64_b32 v[28:29], v6 offset0:0 offset1:2\n"
      "v_fma_f32 v46, 0.5, v58, -v70\n"
      "v_mfma_f32_16x16x32_bf16 v[80:83], a[76:79], a[192:195], v[80:83]\n"
      "ds_read_b128 a[208:211], v0 offset:20480\n"
      "ds_read2st64_b32 v[30:31], v6 offset0:4 offset1:6\n"
      "v_mfma_f32_16x16x32_bf16 v[84:87], a[80:83], a[192:195], v[84:87]\n"
      "v_fma_f32 v47, 0.5, v59, -v71\n"
      "v_fma_f32 v48, 0.5, v60, -v72\n"
      "v_mfma_f32_16x16x32_bf16 v[76:79], a[84:87], a[196:199], v[76:79]\n"
      "ds_read_b128 a[212:215], v0 offset:16448\n"
      "ds_read_b64_tr_b16 a[220:221], v7 offset:33792\n"
      "v_mfma_f32_16x16x32_bf16 v[80:83], a[88:91], a[196:199], v[80:83]\n"
      "ds_read_b64_tr_b16 a[222:223], v7 offset:33800\n"
      "ds_read2st64_b32 v[32:33], v6 offset0:8 offset1:10\n"
      "v_fma_f32 v49, 0.5, v61, -v73\n"
      "v_mfma_f32_16x16x32_bf16 v[84:87], a[92:95], a[196:199], v[84:87]\n"
      "ds_read2st64_b32 v[34:35], v6 offset0:12 offset1:14\n"
      "v_fma_f32 v50, 0.5, v62, -v74\n"
      "v_mfma_f32_16x16x32_bf16 v[76:79], a[96:99], a[200:203], v[76:79]\n"
      "ds_read_b128 a[192:195], v0 offset:20544\n"
      "v_fma_f32 v51, 0.5, v63, -v75\n"
      "v_mfma_f32_16x16x32_bf16 v[80:83], a[100:103], a[200:203], v[80:83]\n"
      "s_nop 0\n"
      "ds_read2st64_b32 v[36:37], v6 offset0:16 offset1:18\n"
      "v_mfma_f32_16x16x32_bf16 v[84:87], a[104:107], a[200:203], v[84:87]\n"
      "s_waitcnt lgkmcnt(7)\n"
      "ds_read2st64_b32 v[38:39], v6 offset0:20 offset1:22\n"
      "v_sub_f32 v28, v28, v40\n"
      "v_sub_f32 v29, v29, v41\n"
      "v_sub_f32 v30, v30, v42\n"
      "v_mfma_f32_16x16x32_bf16 v[52:55], v[252:255], a[204:207], v[112:115]\n"
      "s_waitcnt lgkmcnt(5)\n"
      "ds_read_b128 a[196:199], v0 offset:16512\n"
      "v_sub_f32 v31, v31, v43\n"
      "v_mfma_f32_4x4x4_16b_bf16 v[12:15], a[216:217], a[220:221], 0\n"
      "v_fmac_f32 v28, s46, v100\n"
      "v_mfma_f32_4x4x4_16b_bf16 v[16:19], a[216:217], a[222:223], 0\n"
      "v_fmac_f32 v29, s46, v101\n"
      "v_fmac_f32 v30, s46, v102\n"
      "v_mfma_f32_16x16x32_bf16 v[56:59], v[244:247], a[204:207], v[116:119]\n"
      "v_fmac_f32 v31, s46, v103\n"
      "v_fmac_f32 v40, s45, v28\n"
      "v_mfma_f32_16x16x32_bf16 v[60:63], v[236:239], a[204:207], v[120:123]\n"
      "v_fmac_f32 v41, s45, v29\n"
      "v_cvt_pk_bf16_f32 v12, v12, v16\n"
      "v_fmac_f32 v42, s45, v30\n"
      "v_mfma_f32_16x16x32_bf16 v[64:67], v[248:251], a[208:211], v[112:115]\n"
      "ds_read_b128 a[200:203], v0 offset:20608\n"
      "ds_write_addtid_b32 v12 offset:24832\n"
      "v_mfma_f32_16x16x32_bf16 v[68:71], v[240:243], a[208:211], v[116:119]\n"
      "s_nop 0\n"
      "ds_read_b64_tr_b16 a[224:225], v7 offset:34304\n"
      "ds_read_b64_tr_b16 a[226:227], v7 offset:34312\n"
      "v_mfma_f32_16x16x32_bf16 v[72:75], v[232:235], a[208:211], v[120:123]\n"
      "s_nop 0\n"
      "v_fmac_f32_e64 v43, s45, v31\n"
      "v_cvt_pk_bf16_f32 v13, v13, v17\n"
      "v_mfma_f32_16x16x32_bf16 v[52:55], v[228:231], a[212:215], v[52:55]\n"
      "s_nop 0\n"
      "ds_read_b128 a[204:207], v1 offset:16384\n"
      "ds_write_addtid_b32 v13 offset:28928\n"
      "v_fma_f32 v28, s47, v28, v76\n"
      "v_mfma_f32_16x16x32_bf16 v[56:59], v[220:223], a[212:215], v[56:59]\n"
      "s_nop 0\n"
      "v_fma_f32 v29, s47, v29, v77\n"
      "v_cvt_pk_bf16_f32 v14, v14, v18\n"
      "v_mfma_f32_16x16x32_bf16 v[60:63], v[212:215], a[212:215], v[60:63]\n"
      "s_waitcnt lgkmcnt(4)\n"
      "ds_write_addtid_b32 v14 offset:33024\n"
      "v_fma_f32 v30, s47, v30, v78\n"
      "v_mfma_f32_16x16x32_bf16 v[64:67], v[224:227], a[192:195], v[64:67]\n"
      "s_nop 0\n"
      "ds_read_b128 a[208:211], v1 offset:20480\n"
      "v_fma_f32 v31, s47, v31, v79\n"
      "v_mfma_f32_16x16x32_bf16 v[68:71], v[216:219], a[192:195], v[68:71]\n"
      "s_nop 0\n"
      "v_cvt_pk_bf16_f32 v40, v40, v41\n"
      "v_cvt_pk_bf16_f32 v15, v15, v19\n"
      "v_mfma_f32_16x16x32_bf16 v[72:75], v[208:211], a[192:195], v[72:75]\n"
      "s_waitcnt lgkmcnt(2)\n"
      "ds_write_addtid_b32 v40 offset:12288\n"
      "ds_write_addtid_b32 v15 offset:37120\n"
      "v_cvt_pk_bf16_f32 v42, v42, v43\n"
      "v_mfma_f32_4x4x4_16b_bf16 v[20:23], a[216:217], a[224:225], 0\n"
      "v_cvt_pk_bf16_f32 v28, v28, v29\n"
      "v_mfma_f32_4x4x4_16b_bf16 v[24:27], a[216:217], a[226:227], 0\n"
      "ds_write_addtid_b32 v42 offset:12800\n"
      "ds_write_addtid_b32 v28 offset:14336\n"
      "v_mfma_f32_16x16x32_bf16 v[52:55], v[204:207], a[196:199], v[52:55]\n"
      "ds_read_b128 a[212:215], v1 offset:16448\n"
      "v_cvt_pk_bf16_f32 v30, v30, v31\n"
      "v_mfma_f32_16x16x32_bf16 v[56:59], v[196:199], a[196:199], v[56:59]\n"
      "ds_write_addtid_b32 v30 offset:14848\n"
      "v_sub_f32 v32, v32, v44\n"
      "v_cvt_pk_bf16_f32 v20, v20, v24\n"
      "v_mfma_f32_16x16x32_bf16 v[60:63], v[188:191], a[196:199], v[60:63]\n"
      "ds_write_addtid_b32 v20 offset:25344\n"
      "v_sub_f32 v33, v33, v45\n"
      "v_mfma_f32_16x16x32_bf16 v[64:67], v[200:203], a[200:203], v[64:67]\n"
      "ds_read_b128 a[192:195], v1 offset:20544\n"
      "ds_read_b64_tr_b16 a[228:229], v7 offset:34816\n"
      "v_mfma_f32_16x16x32_bf16 v[68:71], v[192:195], a[200:203], v[68:71]\n"
      "ds_read_b64_tr_b16 a[230:231], v7 offset:34824\n"
      "v_sub_f32 v34, v34, v46\n"
      "v_mfma_f32_16x16x32_bf16 v[72:75], v[184:187], a[200:203], v[72:75]\n"
      "v_sub_f32 v35, v35, v47\n"
      "v_cvt_pk_bf16_f32 v21, v21, v25\n"
      "v_mfma_f32_16x16x32_bf16 v[52:55], v[180:183], a[204:207], v[52:55]\n"
      "ds_read_b128 a[196:199], v1 offset:16512\n"
      "ds_write_addtid_b32 v21 offset:29440\n"
      "v_fmac_f32 v32, s46, v104\n"
      "v_mfma_f32_16x16x32_bf16 v[56:59], v[172:175], a[204:207], v[56:59]\n"
      "v_fmac_f32 v33, s46, v105\n"
      "v_cvt_pk_bf16_f32 v22, v22, v26\n"
      "v_mfma_f32_16x16x32_bf16 v[60:63], v[164:167], a[204:207], v[60:63]\n"
      "s_waitcnt lgkmcnt(2)\n"
      "ds_write_addtid_b32 v22 offset:33536\n"
      "v_fmac_f32 v34, s46, v106\n"
      "v_mfma_f32_16x16x32_bf16 v[64:67], v[176:179], a[208:211], v[64:67]\n"
      "ds_read_b128 a[200:203], v1 offset:20608\n"
      "v_fmac_f32 v35, s46, v107\n"
      "v_mfma_f32_16x16x32_bf16 v[68:71], v[168:171], a[208:211], v[68:71]\n"
      "v_fmac_f32 v44, s45, v32\n"
      "v_cvt_pk_bf16_f32 v23, v23, v27\n"
      "v_mfma_f32_4x4x4_16b_bf16 v[12:15], a[216:217], a[228:229], 0\n"
      "v_fmac_f32 v45, s45, v33\n"
      "v_mfma_f32_4x4x4_16b_bf16 v[16:19], a[216:217], a[230:231], 0\n"
      "ds_write_addtid_b32 v23 offset:37632\n"
      "v_fmac_f32 v46, s45, v34\n"
      "v_mfma_f32_16x16x32_bf16 v[72:75], v[160:163], a[208:211], v[72:75]\n"
      "v_fmac_f32 v47, s45, v35\n"
      "v_fma_f32 v32, s47, v32, v80\n"
      "v_cvt_pk_bf16_f32 v12, v12, v16\n"
      "v_mfma_f32_16x16x32_bf16 v[52:55], v[156:159], a[212:215], v[52:55]\n"
      "ds_read_b128 a[204:207], v2 offset:16384\n"
      "ds_write_addtid_b32 v12 offset:25856\n"
      "v_mfma_f32_16x16x32_bf16 v[56:59], v[148:151], a[212:215], v[56:59]\n"
      "s_nop 0\n"
      "v_fma_f32 v33, s47, v33, v81\n"
      "v_cvt_pk_bf16_f32 v13, v13, v17\n"
      "v_mfma_f32_16x16x32_bf16 v[60:63], v[140:143], a[212:215], v[60:63]\n"
      "ds_read_b64_tr_b16 a[232:233], v7 offset:35328\n"
      "ds_write_addtid_b32 v13 offset:29952\n"
      "v_fma_f32 v34, s47, v34, v82\n"
      "v_mfma_f32_16x16x32_bf16 v[64:67], v[152:155], a[192:195], v[64:67]\n"
      "ds_read_b128 a[208:211], v2 offset:20480\n"
      "ds_read_b64_tr_b16 a[234:235], v7 offset:35336\n"
      "v_mfma_f32_16x16x32_bf16 v[68:71], v[144:147], a[192:195], v[68:71]\n"
      "v_fma_f32 v35, s47, v35, v83\n"
      "v_cvt_pk_bf16_f32 v44, v44, v45\n"
      "v_mfma_f32_16x16x32_bf16 v[72:75], v[136:139], a[192:195], v[72:75]\n"
      "s_waitcnt lgkmcnt(4)\n"
      "ds_write_addtid_b32 v44 offset:20484\n"
      "v_cvt_pk_bf16_f32 v14, v14, v18\n"
      "v_mfma_f32_16x16x32_bf16 v[52:55], v[132:135], a[196:199], v[52:55]\n"
      "s_nop 0\n"
      "ds_read_b128 a[212:215], v2 offset:16448\n"
      "ds_write_addtid_b32 v14 offset:34048\n"
      "v_cvt_pk_bf16_f32 v46, v46, v47\n"
      "v_mfma_f32_16x16x32_bf16 v[56:59], v[124:127], a[196:199], v[56:59]\n"
      "ds_write_addtid_b32 v46 offset:20996\n"
      "v_cvt_pk_bf16_f32 v32, v32, v33\n"
      "v_mfma_f32_16x16x32_bf16 v[60:63], a[184:187], a[196:199], v[60:63]\n"
      "ds_write_addtid_b32 v32 offset:22532\n"
      "v_cvt_pk_bf16_f32 v34, v34, v35\n"
      "v_mfma_f32_16x16x32_bf16 v[64:67], v[128:131], a[200:203], v[64:67]\n"
      "s_waitcnt lgkmcnt(4)\n"
      "ds_read_b128 a[192:195], v2 offset:20544\n"
      "ds_write_addtid_b32 v34 offset:23044\n"
      "v_sub_f32 v36, v36, v48\n"
      "v_mfma_f32_4x4x4_16b_bf16 v[20:23], a[216:217], a[232:233], 0\n"
      "v_sub_f32 v37, v37, v49\n"
      "v_mfma_f32_4x4x4_16b_bf16 v[24:27], a[216:217], a[234:235], 0\n"
      "v_sub_f32 v38, v38, v50\n"
      "v_cvt_pk_bf16_f32 v15, v15, v19\n"
      "v_mfma_f32_16x16x32_bf16 v[68:71], a[188:191], a[200:203], v[68:71]\n"
      "ds_write_addtid_b32 v15 offset:38144\n"
      "v_sub_f32 v39, v39, v51\n"
      "v_cvt_pk_bf16_f32 v20, v20, v24\n"
      "v_mfma_f32_16x16x32_bf16 v[72:75], a[180:183], a[200:203], v[72:75]\n"
      "ds_write_addtid_b32 v20 offset:26368\n"
      "v_fmac_f32 v36, s46, v108\n"
      "v_mfma_f32_16x16x32_bf16 v[52:55], a[176:179], a[204:207], v[52:55]\n"
      "ds_read_b128 a[196:199], v2 offset:16512\n"
      "v_fmac_f32 v37, s46, v109\n"
      "v_mfma_f32_16x16x32_bf16 v[56:59], a[168:171], a[204:207], v[56:59]\n"
      "ds_read_b64_tr_b16 a[220:221], v7 offset:35840\n"
      "ds_read_b64_tr_b16 a[222:223], v7 offset:35848\n"
      "v_mfma_f32_16x16x32_bf16 v[60:63], a[160:163], a[204:207], v[60:63]\n"
      "v_fmac_f32 v38, s46, v110\n"
      "v_cvt_pk_bf16_f32 v21, v21, v25\n"
      "v_mfma_f32_16x16x32_bf16 v[64:67], a[172:175], a[208:211], v[64:67]\n"
      "ds_read_b128 a[200:203], v2 offset:20608\n"
      "ds_write_addtid_b32 v21 offset:30464\n"
      "v_fmac_f32 v39, s46, v111\n"
      "v_mfma_f32_16x16x32_bf16 v[68:71], a[164:167], a[208:211], v[68:71]\n"
      "v_fmac_f32 v48, s45, v36\n"
      "v_cvt_pk_bf16_f32 v22, v22, v26\n"
      "v_mfma_f32_16x16x32_bf16 v[72:75], a[156:159], a[208:211], v[72:75]\n"
      "s_waitcnt lgkmcnt(4)\n"
      "ds_write_addtid_b32 v22 offset:34560\n"
      "v_fmac_f32 v49, s45, v37\n"
      "v_mfma_f32_16x16x32_bf16 v[52:55], a[152:155], a[212:215], v[52:55]\n"
      "ds_read_b128 a[204:207], v3 offset:50688\n"
      "v_fmac_f32 v50, s45, v38\n"
      "v_mfma_f32_16x16x32_bf16 v[56:59], a[144:147], a[212:215], v[56:59]\n"
      "v_fmac_f32_e64 v51, s45, v39\n"
      "v_cvt_pk_bf16_f32 v23, v23, v27\n"
      "v_mfma_f32_16x16x32_bf16 v[60:63], a[136:139], a[212:215], v[60:63]\n"
      "s_waitcnt lgkmcnt(2)\n"
      "ds_write_addtid_b32 v23 offset:38656\n"
      "v_fma_f32 v36, s47, v36, v84\n"
      "v_mfma_f32_4x4x4_16b_bf16 v[12:15], a[216:217], a[220:221], 0\n"
      "v_fma_f32 v37, s47, v37, v85\n"
      "v_mfma_f32_4x4x4_16b_bf16 v[16:19], a[216:217], a[222:223], 0\n"
      "v_fma_f32 v38, s47, v38, v86\n"
      "v_fma_f32 v39, s47, v39, v87\n"
      "v_mfma_f32_16x16x32_bf16 v[64:67], a[148:151], a[192:195], v[64:67]\n"
      "ds_read_b128 a[208:211], v3 offset:50752\n"
      "v_cvt_pk_bf16_f32 v48, v48, v49\n"
      "v_cvt_pk_bf16_f32 v12, v12, v16\n"
      "v_mfma_f32_16x16x32_bf16 v[68:71], a[140:143], a[192:195], v[68:71]\n"
      "ds_write_addtid_b32 v48 offset:16384\n"
      "ds_write_addtid_b32 v12 offset:26880\n"
      "v_mfma_f32_16x16x32_bf16 v[72:75], a[132:135], a[192:195], v[72:75]\n"
      "v_cvt_pk_bf16_f32 v13, v13, v17\n"
      "v_cvt_pk_bf16_f32 v14, v14, v18\n"
      "v_mfma_f32_16x16x32_bf16 v[52:55], a[128:131], a[196:199], v[52:55]\n"
      "ds_read_b128 a[212:215], v3 offset:50816\n"
      "ds_write_addtid_b32 v13 offset:30976\n"
      "v_mfma_f32_16x16x32_bf16 v[56:59], a[120:123], a[196:199], v[56:59]\n"
      "ds_read_b64_tr_b16 a[224:225], v7 offset:36352\n"
      "ds_write_addtid_b32 v14 offset:35072\n"
      "v_cvt_pk_bf16_f32 v50, v50, v51\n"
      "v_mfma_f32_16x16x32_bf16 v[60:63], a[112:115], a[196:199], v[60:63]\n"
      "ds_write_addtid_b32 v50 offset:16896\n"
      "ds_read_b64_tr_b16 a[226:227], v7 offset:36360\n"
      "v_mfma_f32_16x16x32_bf16 v[64:67], a[124:127], a[200:203], v[64:67]\n"
      "ds_read_b128 a[192:195], v4 offset:50688\n"
      "v_cvt_pk_bf16_f32 v36, v36, v37\n"
      "v_mfma_f32_16x16x32_bf16 v[68:71], a[116:119], a[200:203], v[68:71]\n"
      "ds_write_addtid_b32 v36 offset:18432\n"
      "v_cvt_pk_bf16_f32 v15, v15, v19\n"
      "v_mfma_f32_16x16x32_bf16 v[72:75], a[108:111], a[200:203], v[72:75]\n"
      "s_waitcnt lgkmcnt(6)\n"
      "ds_write_addtid_b32 v15 offset:39168\n"
      "v_sub_f32 v52, v64, v52\n"
      "v_sub_f32 v53, v65, v53\n"
      "v_cvt_pk_bf16_f32 v38, v38, v39\n"
      "v_mfma_f32_16x16x32_bf16 v[88:91], a[0:3], a[204:207], 0\n"
      "s_nop 0\n"
      "ds_read_b128 a[196:199], v4 offset:50752\n"
      "ds_write_addtid_b32 v38 offset:18944\n"
      "v_mfma_f32_16x16x32_bf16 v[92:95], a[4:7], a[204:207], 0\n"
      "s_waitcnt lgkmcnt(5)\n"
      "ds_write_addtid_b32 v52 offset:6144\n"
      "ds_write_addtid_b32 v53 offset:6656\n"
      "v_mfma_f32_4x4x4_16b_bf16 v[20:23], a[216:217], a[224:225], 0\n"
      "v_sub_f32 v54, v66, v54\n"
      "v_mfma_f32_4x4x4_16b_bf16 v[24:27], a[216:217], a[226:227], 0\n"
      "ds_write_addtid_b32 v54 offset:7168\n"
      "v_sub_f32_e64 v55, v67, v55\n"
      "v_mfma_f32_16x16x32_bf16 v[96:99], a[8:11], a[204:207], 0\n"
      "ds_write_addtid_b32 v55 offset:7680\n"
      "v_sub_f32 v56, v68, v56\n"
      "v_sub_f32 v57, v69, v57\n"
      "v_mfma_f32_16x16x32_bf16 v[88:91], a[12:15], a[208:211], v[88:91]\n"
      "ds_read_b128 a[200:203], v4 offset:50816\n"
      "ds_write_addtid_b32 v56 offset:8192\n"
      "v_mfma_f32_16x16x32_bf16 v[92:95], a[16:19], a[208:211], v[92:95]\n"
      "ds_write_addtid_b32 v57 offset:8704\n"
      "v_sub_f32 v58, v70, v58\n"
      "v_mfma_f32_16x16x32_bf16 v[96:99], a[20:23], a[208:211], v[96:99]\n"
      "ds_write_addtid_b32 v58 offset:9216\n"
      "v_cvt_pk_bf16_f32 v20, v20, v24\n"
      "v_mfma_f32_16x16x32_bf16 v[88:91], a[24:27], a[212:215], v[88:91]\n"
      "ds_read_b128 a[204:207], v5 offset:50688\n"
      "ds_write_addtid_b32 v20 offset:27392\n"
      "v_sub_f32 v59, v71, v59\n"
      "v_mfma_f32_16x16x32_bf16 v[92:95], a[28:31], a[212:215], v[92:95]\n"
      "ds_write_addtid_b32 v59 offset:9728\n"
      "v_sub_f32 v60, v72, v60\n"
      "v_mfma_f32_16x16x32_bf16 v[96:99], a[32:35], a[212:215], v[96:99]\n"
      "s_waitcnt lgkmcnt(7)\n"
      "ds_write_addtid_b32 v60 offset:10240\n"
      "v_cvt_pk_bf16_f32 v21, v21, v25\n"
      "v_mfma_f32_16x16x32_bf16 v[88:91], a[36:39], a[192:195], v[88:91]\n"
      "ds_read_b128 a[208:211], v5 offset:50752\n"
      "ds_write_addtid_b32 v21 offset:31488\n"
      "v_mfma_f32_16x16x32_bf16 v[92:95], a[40:43], a[192:195], v[92:95]\n"
      "v_sub_f32 v61, v73, v61\n"
      "v_sub_f32 v62, v74, v62\n"
      "v_cvt_pk_bf16_f32 v22, v22, v26\n"
      "v_mfma_f32_16x16x32_bf16 v[96:99], a[44:47], a[192:195], v[96:99]\n"
      "ds_write_addtid_b32 v22 offset:35584\n"
      "ds_write_addtid_b32 v61 offset:10752\n"
      "v_sub_f32 v63, v75, v63\n"
      "v_mfma_f32_16x16x32_bf16 v[88:91], a[48:51], a[196:199], v[88:91]\n"
      "s_nop 0\n"
      "ds_read_b128 a[212:215], v5 offset:50816\n"
      "ds_write_addtid_b32 v62 offset:11264\n"
      "v_mfma_f32_16x16x32_bf16 v[92:95], a[52:55], a[196:199], v[92:95]\n"
      "s_nop 0\n"
      "ds_write_addtid_b32 v63 offset:11776\n"
      "v_cvt_pk_bf16_f32 v23, v23, v27\n"
      "v_mfma_f32_16x16x32_bf16 v[96:99], a[56:59], a[196:199], v[96:99]\n"
      "s_waitcnt lgkmcnt(5)\n"
      "ds_write_addtid_b32 v23 offset:39680\n"
      "v_fma_f32 v40, 0.5, v52, -v64\n"
      "v_mfma_f32_16x16x32_bf16 v[88:91], a[60:63], a[200:203], v[88:91]\n"
      "v_fma_f32 v41, 0.5, v53, -v65\n"
      "v_fma_f32 v42, 0.5, v54, -v66\n"
      "v_mfma_f32_16x16x32_bf16 v[92:95], a[64:67], a[200:203], v[92:95]\n"
      "v_fma_f32 v43, 0.5, v55, -v67\n"
      "v_fma_f32 v44, 0.5, v56, -v68\n"
      "v_mfma_f32_16x16x32_bf16 v[96:99], a[68:71], a[200:203], v[96:99]\n"
      "s_waitcnt lgkmcnt(0)\n"
      "v_fma_f32 v45, 0.5, v57, -v69\n"
      "s_barrier\n"
      "LSmid_%=:\n"
      "v_mfma_f32_16x16x32_bf16 v[88:91], a[72:75], a[204:207], v[88:91]\n"
      "ds_read_b128 a[192:195], v0 offset:0\n"
      "ds_read2st64_b32 v[28:29], v6 offset0:24 offset1:26\n"
      "v_fma_f32 v46, 0.5, v58, -v70\n"
      "v_mfma_f32_16x16x32_bf16 v[92:95], a[76:79], a[204:207], v[92:95]\n"
      "ds_read_b128 a[196:199], v0 offset:4096\n"
      "ds_read2st64_b32 v[30:31], v6 offset0:28 offset1:30\n"
      "v_mfma_f32_16x16x32_bf16 v[96:99], a[80:83], a[204:207], v[96:99]\n"
      "v_fma_f32 v47, 0.5, v59, -v71\n"
      "v_fma_f32 v48, 0.5, v60, -v72\n"
      "v_mfma_f32_16x16x32_bf16 v[88:91], a[84:87], a[208:211], v[88:91]\n"
      "ds_read_b128 a[200:203], v0 offset:64\n"
      "ds_read_b64_tr_b16 a[220:221], v7 offset:50688\n"
      "v_mfma_f32_16x16x32_bf16 v[92:95], a[88:91], a[208:211], v[92:95]\n"
      "ds_read_b64_tr_b16 a[222:223], v7 offset:50696\n"
      "ds_read2st64_b32 v[32:33], v6 offset0:32 offset1:34\n"
      "v_fma_f32 v49, 0.5, v61, -v73\n"
      "v_mfma_f32_16x16x32_bf16 v[96:99], a[92:95], a[208:211], v[96:99]\n"
      "ds_read2st64_b32 v[34:35], v6 offset0:36 offset1:38\n"
      "v_fma_f32 v50, 0.5, v62, -v74\n"
      "v_mfma_f32_16x16x32_bf16 v[88:91], a[96:99], a[212:215], v[88:91]\n"
      "ds_read_b128 a[204:207], v0 offset:4160\n"
      "v_fma_f32 v51, 0.5, v63, -v75\n"
      "v_mfma_f32_16x16x32_bf16 v[92:95], a[100:103], a[212:215], v[92:95]\n"
      "ds_read2st64_b32 v[36:37], v6 offset0:40 offset1:42\n"
      "v_mfma_f32_16x16x32_bf16 v[96:99], a[104:107], a[212:215], v[96:99]\n"
      "s_waitcnt lgkmcnt(7)\n"
      "ds_read2st64_b32 v[38:39], v6 offset0:44 offset1:46\n"
      "v_sub_f32 v28, v28, v40\n"
      "v_sub_f32 v29, v29, v41\n"
      "v_sub_f32 v30, v30, v42\n"
      "v_mfma_f32_16x16x32_bf16 v[52:55], v[252:255], a[192:195], v[76:79]\n"
      "s_waitcnt lgkmcnt(5)\n"
      "ds_read_b128 a[208:211], v0 offset:128\n"
      "v_sub_f32 v31, v31, v43\n"
      "v_mfma_f32_4x4x4_16b_bf16 v[12:15], a[216:217], a[220:221], 0\n"
      "v_fmac_f32 v28, s46, v112\n"
      "v_mfma_f32_4x4x4_16b_bf16 v[16:19], a[216:217], a[222:223], 0\n"
      "v_fmac_f32 v29, s46, v113\n"
      "v_fmac_f32 v30, s46, v114\n"
      "v_mfma_f32_16x16x32_bf16 v[56:59], v[244:247], a[192:195], v[80:83]\n"
      "v_fmac_f32 v31, s46, v115\n"
      "v_fmac_f32_e64 v40, s45, v28\n"
      "v_mfma_f32_16x16x32_bf16 v[60:63], v[236:239], a[192:195], v[84:87]\n"
      "v_fmac_f32 v41, s45, v29\n"
      "v_cvt_pk_bf16_f32 v12, v12, v16\n"
      "v_fmac_f32 v42, s45, v30\n"
      "v_mfma_f32_16x16x32_bf16 v[64:67], v[248:251], a[196:199], v[76:79]\n"
      "ds_read_b128 a[212:215], v0 offset:4224\n"
      "ds_write_addtid_b32 v12 offset:41216\n"
      "v_mfma_f32_16x16x32_bf16 v[68:71], v[240:243], a[196:199], v[80:83]\n"
      "ds_read_b64_tr_b16 a[224:225], v7 offset:51200\n"
      "ds_read_b64_tr_b16 a[226:227], v7 offset:51208\n"
      "v_mfma_f32_16x16x32_bf16 v[72:75], v[232:235], a[196:199], v[84:87]\n"
      "v_fmac_f32 v43, s45, v31\n"
      "v_cvt_pk_bf16_f32 v13, v13, v17\n"
      "v_mfma_f32_16x16x32_bf16 v[52:55], v[228:231], a[200:203], v[52:55]\n"
      "s_nop 0\n"
      "ds_read_b128 a[192:195], v1 offset:0\n"
      "ds_write_addtid_b32 v13 offset:45312\n"
      "v_fma_f32 v28, s47, v28, v88\n"
      "v_mfma_f32_16x16x32_bf16 v[56:59], v[220:223], a[200:203], v[56:59]\n"
      "v_fma_f32 v29, s47, v29, v89\n"
      "v_cvt_pk_bf16_f32 v14, v14, v18\n"
      "v_mfma_f32_16x16x32_bf16 v[60:63], v[212:215], a[200:203], v[60:63]\n"
      "s_waitcnt lgkmcnt(4)\n"
      "ds_write_addtid_b32 v14 offset:49408\n"
      "v_fma_f32 v30, s47, v30, v90\n"
      "v_mfma_f32_16x16x32_bf16 v[64:67], v[224:227], a[204:207], v[64:67]\n"
      "s_nop 0\n"
      "ds_read_b128 a[196:199], v1 offset:4096\n"
      "v_fma_f32 v31, s47, v31, v91\n"
      "v_mfma_f32_16x16x32_bf16 v[68:71], v[216:219], a[204:207], v[68:71]\n"
      "s_nop 0\n"
      "v_cvt_pk_bf16_f32 v40, v40, v41\n"
      "v_cvt_pk_bf16_f32 v15, v15, v19\n"
      "v_mfma_f32_16x16x32_bf16 v[72:75], v[208:211], a[204:207], v[72:75]\n"
      "s_waitcnt lgkmcnt(2)\n"
      "ds_write_addtid_b32 v40 offset:13312\n"
      "ds_write_addtid_b32 v15 offset:53504\n"
      "v_cvt_pk_bf16_f32 v42, v42, v43\n"
      "v_mfma_f32_4x4x4_16b_bf16 v[20:23], a[216:217], a[224:225], 0\n"
      "v_cvt_pk_bf16_f32 v28, v28, v29\n"
      "v_mfma_f32_4x4x4_16b_bf16 v[24:27], a[216:217], a[226:227], 0\n"
      "ds_write_addtid_b32 v42 offset:13824\n"
      "ds_write_addtid_b32 v28 offset:15360\n"
      "v_mfma_f32_16x16x32_bf16 v[52:55], v[204:207], a[208:211], v[52:55]\n"
      "ds_read_b128 a[200:203], v1 offset:64\n"
      "v_cvt_pk_bf16_f32 v30, v30, v31\n"
      "v_mfma_f32_16x16x32_bf16 v[56:59], v[196:199], a[208:211], v[56:59]\n"
      "ds_write_addtid_b32 v30 offset:15872\n"
      "v_sub_f32 v32, v32, v44\n"
      "v_cvt_pk_bf16_f32 v20, v20, v24\n"
      "v_mfma_f32_16x16x32_bf16 v[60:63], v[188:191], a[208:211], v[60:63]\n"
      "ds_write_addtid_b32 v20 offset:41728\n"
      "v_sub_f32 v33, v33, v45\n"
      "v_mfma_f32_16x16x32_bf16 v[64:67], v[200:203], a[212:215], v[64:67]\n"
      "ds_read_b128 a[204:207], v1 offset:4160\n"
      "ds_read_b64_tr_b16 a[228:229], v7 offset:51712\n"
      "v_mfma_f32_16x16x32_bf16 v[68:71], v[192:195], a[212:215], v[68:71]\n"
      "ds_read_b64_tr_b16 a[230:231], v7 offset:51720\n"
      "v_sub_f32 v34, v34, v46\n"
      "v_mfma_f32_16x16x32_bf16 v[72:75], v[184:187], a[212:215], v[72:75]\n"
      "v_sub_f32 v35, v35, v47\n"
      "v_cvt_pk_bf16_f32 v21, v21, v25\n"
      "v_mfma_f32_16x16x32_bf16 v[52:55], v[180:183], a[192:195], v[52:55]\n"
      "ds_read_b128 a[208:211], v1 offset:128\n"
      "ds_write_addtid_b32 v21 offset:45824\n"
      "v_fmac_f32 v32, s46, v116\n"
      "v_mfma_f32_16x16x32_bf16 v[56:59], v[172:175], a[192:195], v[56:59]\n"
      "v_fmac_f32_e64 v33, s46, v117\n"
      "v_cvt_pk_bf16_f32 v22, v22, v26\n"
      "v_mfma_f32_16x16x32_bf16 v[60:63], v[164:167], a[192:195], v[60:63]\n"
      "s_waitcnt lgkmcnt(2)\n"
      "ds_write_addtid_b32 v22 offset:49920\n"
      "v_fmac_f32 v34, s46, v118\n"
      "v_mfma_f32_16x16x32_bf16 v[64:67], v[176:179], a[196:199], v[64:67]\n"
      "ds_read_b128 a[212:215], v1 offset:4224\n"
      "v_fmac_f32 v35, s46, v119\n"
      "v_mfma_f32_16x16x32_bf16 v[68:71], v[168:171], a[196:199], v[68:71]\n"
      "v_fmac_f32 v44, s45, v32\n"
      "v_cvt_pk_bf16_f32 v23, v23, v27\n"
      "v_mfma_f32_4x4x4_16b_bf16 v[12:15], a[216:217], a[228:229], 0\n"
      "v_fmac_f32 v45, s45, v33\n"
      "v_mfma_f32_4x4x4_16b_bf16 v[16:19], a[216:217], a[230:231], 0\n"
      "ds_write_addtid_b32 v23 offset:54016\n"
      "v_fmac_f32 v46, s45, v34\n"
      "v_mfma_f32_16x16x32_bf16 v[72:75], v[160:163], a[196:199], v[72:75]\n"
      "v_fmac_f32 v47, s45, v35\n"
      "v_fma_f32 v32, s47, v32, v92\n"
      "v_cvt_pk_bf16_f32 v12, v12, v16\n"
      "v_mfma_f32_16x16x32_bf16 v[52:55], v[156:159], a[200:203], v[52:55]\n"
      "ds_read_b128 a[192:195], v2 offset:0\n"
      "ds_write_addtid_b32 v12 offset:42240\n"
      "v_mfma_f32_16x16x32_bf16 v[56:59], v[148:151], a[200:203], v[56:59]\n"
      "v_fma_f32 v33, s47, v33, v93\n"
      "v_cvt_pk_bf16_f32 v13, v13, v17\n"
      "v_mfma_f32_16x16x32_bf16 v[60:63], v[140:143], a[200:203], v[60:63]\n"
      "ds_read_b64_tr_b16 a[232:233], v7 offset:52224\n"
      "ds_write_addtid_b32 v13 offset:46336\n"
      "v_fma_f32 v34, s47, v34, v94\n"
      "v_mfma_f32_16x16x32_bf16 v[64:67], v[152:155], a[204:207], v[64:67]\n"
      "ds_read_b128 a[196:199], v2 offset:4096\n"
      "ds_read_b64_tr_b16 a[234:235], v7 offset:52232\n"
      "v_mfma_f32_16x16x32_bf16 v[68:71], v[144:147], a[204:207], v[68:71]\n"
      "s_nop 0\n"
      "v_fma_f32 v35, s47, v35, v95\n"
      "v_cvt_pk_bf16_f32 v44, v44, v45\n"
      "v_mfma_f32_16x16x32_bf16 v[72:75], v[136:139], a[204:207], v[72:75]\n"
      "s_waitcnt lgkmcnt(4)\n"
      "ds_write_addtid_b32 v44 offset:21508\n"
      "v_cvt_pk_bf16_f32 v14, v14, v18\n"
      "v_mfma_f32_16x16x32_bf16 v[52:55], v[132:135], a[208:211], v[52:55]\n"
      "ds_read_b128 a[200:203], v2 offset:64\n"
      "ds_write_addtid_b32 v14 offset:50432\n"
      "v_cvt_pk_bf16_f32 v46, v46, v47\n"
      "v_mfma_f32_16x16x32_bf16 v[56:59], v[124:127], a[208:211], v[56:59]\n"
      "ds_write_addtid_b32 v46 offset:22020\n"
      "v_cvt_pk_bf16_f32 v32, v32, v33\n"
      "v_mfma_f32_16x16x32_bf16 v[60:63], a[184:187], a[208:211], v[60:63]\n"
      "ds_write_addtid_b32 v32 offset:23556\n"
      "v_cvt_pk_bf16_f32 v34, v34, v35\n"
      "v_mfma_f32_16x16x32_bf16 v[64:67], v[128:131], a[212:215], v[64:67]\n"
      "s_waitcnt lgkmcnt(4)\n"
      "ds_read_b128 a[204:207], v2 offset:4160\n"
      "ds_write_addtid_b32 v34 offset:24068\n"
      "v_sub_f32 v36, v36, v48\n"
      "v_mfma_f32_4x4x4_16b_bf16 v[20:23], a[216:217], a[232:233], 0\n"
      "v_sub_f32 v37, v37, v49\n"
      "v_mfma_f32_4x4x4_16b_bf16 v[24:27], a[216:217], a[234:235], 0\n"
      "v_sub_f32 v38, v38, v50\n"
      "v_cvt_pk_bf16_f32 v15, v15, v19\n"
      "v_mfma_f32_16x16x32_bf16 v[68:71], a[188:191], a[212:215], v[68:71]\n"
      "ds_write_addtid_b32 v15 offset:54528\n"
      "v_sub_f32_e64 v39, v39, v51\n"
      "v_cvt_pk_bf16_f32 v20, v20, v24\n"
      "v_mfma_f32_16x16x32_bf16 v[72:75], a[180:183], a[212:215], v[72:75]\n"
      "ds_write_addtid_b32 v20 offset:42752\n"
      "v_fmac_f32 v36, s46, v120\n"
      "v_mfma_f32_16x16x32_bf16 v[52:55], a[176:179], a[192:195], v[52:55]\n"
      "ds_read_b128 a[208:211], v2 offset:128\n"
      "v_fmac_f32 v37, s46, v121\n"
      "v_mfma_f32_16x16x32_bf16 v[56:59], a[168:171], a[192:195], v[56:59]\n"
      "ds_read_b64_tr_b16 a[220:221], v7 offset:52736\n"
      "ds_read_b64_tr_b16 a[222:223], v7 offset:52744\n"
      "v_mfma_f32_16x16x32_bf16 v[60:63], a[160:163], a[192:195], v[60:63]\n"
      "v_fmac_f32 v38, s46, v122\n"
      "v_cvt_pk_bf16_f32 v21, v21, v25\n"
      "v_mfma_f32_16x16x32_bf16 v[64:67], a[172:175], a[196:199], v[64:67]\n"
      "ds_read_b128 a[212:215], v2 offset:4224\n"
      "ds_write_addtid_b32 v21 offset:46848\n"
      "v_fmac_f32 v39, s46, v123\n"
      "v_mfma_f32_16x16x32_bf16 v[68:71], a[164:167], a[196:199], v[68:71]\n"
      "v_fmac_f32_e64 v48, s45, v36\n"
      "v_cvt_pk_bf16_f32 v22, v22, v26\n"
      "v_mfma_f32_16x16x32_bf16 v[72:75], a[156:159], a[196:199], v[72:75]\n"
      "s_waitcnt lgkmcnt(4)\n"
      "ds_write_addtid_b32 v22 offset:50944\n"
      "v_fmac_f32 v49, s45, v37\n"
      "v_mfma_f32_16x16x32_bf16 v[52:55], a[152:155], a[200:203], v[52:55]\n"
      "ds_read_b128 a[192:195], v3 offset:0\n"
      "v_fmac_f32 v50, s45, v38\n"
      "v_mfma_f32_16x16x32_bf16 v[56:59], a[144:147], a[200:203], v[56:59]\n"
      "s_nop 0\n"
      "v_fmac_f32 v51, s45, v39\n"
      "v_cvt_pk_bf16_f32 v23, v23, v27\n"
      "v_mfma_f32_16x16x32_bf16 v[60:63], a[136:139], a[200:203], v[60:63]\n"
      "s_waitcnt lgkmcnt(2)\n"
      "ds_write_addtid_b32 v23 offset:55040\n"
      "v_fma_f32 v36, s47, v36, v96\n"
      "v_mfma_f32_4x4x4_16b_bf16 v[12:15], a[216:217], a[220:221], 0\n"
      "v_fma_f32 v37, s47, v37, v97\n"
      "v_mfma_f32_4x4x4_16b_bf16 v[16:19], a[216:217], a[222:223], 0\n"
      "v_fma_f32 v38, s47, v38, v98\n"
      "v_fma_f32 v39, s47, v39, v99\n"
      "v_mfma_f32_16x16x32_bf16 v[64:67], a[148:151], a[204:207], v[64:67]\n"
      "ds_read_b128 a[196:199], v3 offset:64\n"
      "v_cvt_pk_bf16_f32 v48, v48, v49\n"
      "v_cvt_pk_bf16_f32 v12, v12, v16\n"
      "v_mfma_f32_16x16x32_bf16 v[68:71], a[140:143], a[204:207], v[68:71]\n"
      "ds_write_addtid_b32 v48 offset:17408\n"
      "ds_write_addtid_b32 v12 offset:43264\n"
      "v_mfma_f32_16x16x32_bf16 v[72:75], a[132:135], a[204:207], v[72:75]\n"
      "v_cvt_pk_bf16_f32 v13, v13, v17\n"
      "v_cvt_pk_bf16_f32 v14, v14, v18\n"
      "v_mfma_f32_16x16x32_bf16 v[52:55], a[128:131], a[208:211], v[52:55]\n"
      "ds_read_b128 a[200:203], v3 offset:128\n"
      "ds_write_addtid_b32 v13 offset:47360\n"
      "v_mfma_f32_16x16x32_bf16 v[56:59], a[120:123], a[208:211], v[56:59]\n"
      "ds_read_b64_tr_b16 a[224:225], v7 offset:53248\n"
      "ds_write_addtid_b32 v14 offset:51456\n"
      "v_cvt_pk_bf16_f32 v50, v50, v51\n"
      "v_mfma_f32_16x16x32_bf16 v[60:63], a[112:115], a[208:211], v[60:63]\n"
      "ds_write_addtid_b32 v50 offset:17920\n"
      "ds_read_b64_tr_b16 a[226:227], v7 offset:53256\n"
      "v_mfma_f32_16x16x32_bf16 v[64:67], a[124:127], a[212:215], v[64:67]\n"
      "ds_read_b128 a[204:207], v4 offset:0\n"
      "v_cvt_pk_bf16_f32 v36, v36, v37\n"
      "v_mfma_f32_16x16x32_bf16 v[68:71], a[116:119], a[212:215], v[68:71]\n"
      "ds_write_addtid_b32 v36 offset:19456\n"
      "v_cvt_pk_bf16_f32 v15, v15, v19\n"
      "v_mfma_f32_16x16x32_bf16 v[72:75], a[108:111], a[212:215], v[72:75]\n"
      "s_waitcnt lgkmcnt(6)\n"
      "ds_write_addtid_b32 v15 offset:55552\n"
      "v_sub_f32 v52, v64, v52\n"
      "v_sub_f32_e64 v53, v65, v53\n"
      "v_cvt_pk_bf16_f32 v38, v38, v39\n"
      "v_mfma_f32_16x16x32_bf16 v[100:103], a[0:3], a[192:195], 0\n"
      "s_nop 0\n"
      "ds_read_b128 a[208:211], v4 offset:64\n"
      "ds_write_addtid_b32 v38 offset:19968\n"
      "v_mfma_f32_16x16x32_bf16 v[104:107], a[4:7], a[192:195], 0\n"
      "s_waitcnt lgkmcnt(5)\n"
      "ds_write_addtid_b32 v52 offset:0\n"
      "ds_write_addtid_b32 v53 offset:512\n"
      "v_mfma_f32_4x4x4_16b_bf16 v[20:23], a[216:217], a[224:225], 0\n"
      "v_sub_f32 v54, v66, v54\n"
      "v_mfma_f32_4x4x4_16b_bf16 v[24:27], a[216:217], a[226:227], 0\n"
      "ds_write_addtid_b32 v54 offset:1024\n"
      "v_sub_f32 v55, v67, v55\n"
      "v_mfma_f32_16x16x32_bf16 v[108:111], a[8:11], a[192:195], 0\n"
      "ds_write_addtid_b32 v55 offset:1536\n"
      "v_sub_f32 v56, v68, v56\n"
      "v_sub_f32 v57, v69, v57\n"
      "v_mfma_f32_16x16x32_bf16 v[100:103], a[12:15], a[196:199], v[100:103]\n"
      "ds_read_b128 a[212:215], v4 offset:128\n"
      "ds_write_addtid_b32 v56 offset:2048\n"
      "v_mfma_f32_16x16x32_bf16 v[104:107], a[16:19], a[196:199], v[104:107]\n"
      "ds_write_addtid_b32 v57 offset:2560\n"
      "v_sub_f32 v58, v70, v58\n"
      "v_mfma_f32_16x16x32_bf16 v[108:111], a[20:23], a[196:199], v[108:111]\n"
      "s_nop 0\n"
      "ds_write_addtid_b32 v58 offset:3072\n"
      "v_cvt_pk_bf16_f32 v20, v20, v24\n"
      "v_mfma_f32_16x16x32_bf16 v[100:103], a[24:27], a[200:203], v[100:103]\n"
      "ds_read_b128 a[192:195], v5 offset:0\n"
      "ds_write_addtid_b32 v20 offset:43776\n"
      "v_sub_f32 v59, v71, v59\n"
      "v_mfma_f32_16x16x32_bf16 v[104:107], a[28:31], a[200:203], v[104:107]\n"
      "ds_write_addtid_b32 v59 offset:3584\n"
      "v_sub_f32_e64 v60, v72, v60\n"
      "v_mfma_f32_16x16x32_bf16 v[108:111], a[32:35], a[200:203], v[108:111]\n"
      "s_waitcnt lgkmcnt(7)\n"
      "ds_write_addtid_b32 v60 offset:4096\n"
      "v_cvt_pk_bf16_f32 v21, v21, v25\n"
      "v_mfma_f32_16x16x32_bf16 v[100:103], a[36:39], a[204:207], v[100:103]\n"
      "ds_read_b128 a[196:199], v5 offset:64\n"
      "ds_write_addtid_b32 v21 offset:47872\n"
      "v_mfma_f32_16x16x32_bf16 v[104:107], a[40:43], a[204:207], v[104:107]\n"
      "v_sub_f32 v61, v73, v61\n"
      "v_sub_f32 v62, v74, v62\n"
      "v_cvt_pk_bf16_f32 v22, v22, v26\n"
      "v_mfma_f32_16x16x32_bf16 v[108:111], a[44:47], a[204:207], v[108:111]\n"
      "ds_write_addtid_b32 v22 offset:51968\n"
      "ds_write_addtid_b32 v61 offset:4608\n"
      "v_sub_f32 v63, v75, v63\n"
      "v_mfma_f32_16x16x32_bf16 v[100:103], a[48:51], a[208:211], v[100:103]\n"
      "ds_read_b128 a[200:203], v5 offset:128\n"
      "ds_write_addtid_b32 v62 offset:5120\n"
      "v_mfma_f32_16x16x32_bf16 v[104:107], a[52:55], a[208:211], v[104:107]\n"
      "s_nop 0\n"
      "ds_write_addtid_b32 v63 offset:5632\n"
      "v_cvt_pk_bf16_f32 v23, v23, v27\n"
      "v_mfma_f32_16x16x32_bf16 v[108:111], a[56:59], a[208:211], v[108:111]\n"
      "s_waitcnt lgkmcnt(5)\n"
      "ds_write_addtid_b32 v23 offset:56064\n"
      "v_fma_f32 v40, 0.5, v52, -v64\n"
      "v_mfma_f32_16x16x32_bf16 v[100:103], a[60:63], a[212:215], v[100:103]\n"
      "s_nop 0\n"
      "v_fma_f32 v41, 0.5, v53, -v65\n"
      "v_fma_f32 v42, 0.5, v54, -v66\n"
      "v_mfma_f32_16x16x32_bf16 v[104:107], a[64:67], a[212:215], v[104:107]\n"
      "v_fma_f32 v43, 0.5, v55, -v67\n"
      "v_fma_f32 v44, 0.5, v56, -v68\n"
      "v_mfma_f32_16x16x32_bf16 v[108:111], a[68:71], a[212:215], v[108:111]\n"
      "s_waitcnt lgkmcnt(0)\n"
      "v_fma_f32 v45, 0.5, v57, -v69\n"
      "s_barrier\n"
      "v_mfma_f32_16x16x32_bf16 v[100:103], a[72:75], a[192:195], v[100:103]\n"
      "ds_read_b128 a[204:207], v0 offset:16384\n"
      "ds_read2st64_b32 v[28:29], v6 offset0:0 offset1:2\n"
      "v_fma_f32 v46, 0.5, v58, -v70\n"
      "v_mfma_f32_16x16x32_bf16 v[104:107], a[76:79], a[192:195], v[104:107]\n"
      "ds_read_b128 a[208:211], v0 offset:20480\n"
      "ds_read2st64_b32 v[30:31], v6 offset0:4 offset1:6\n"
      "v_mfma_f32_16x16x32_bf16 v[108:111], a[80:83], a[192:195], v[108:111]\n"
      "v_fma_f32 v47, 0.5, v59, -v71\n"
      "v_fma_f32 v48, 0.5, v60, -v72\n"
      "v_mfma_f32_16x16x32_bf16 v[100:103], a[84:87], a[196:199], v[100:103]\n"
      "ds_read_b128 a[212:215], v0 offset:16448\n"
      "ds_read_b64_tr_b16 a[220:221], v7 offset:0\n"
      "v_mfma_f32_16x16x32_bf16 v[104:107], a[88:91], a[196:199], v[104:107]\n"
      "ds_read_b64_tr_b16 a[222:223], v7 offset:8\n"
      "ds_read2st64_b32 v[32:33], v6 offset0:8 offset1:10\n"
      "v_fma_f32 v49, 0.5, v61, -v73\n"
      "v_mfma_f32_16x16x32_bf16 v[108:111], a[92:95], a[196:199], v[108:111]\n"
      "ds_read2st64_b32 v[34:35], v6 offset0:12 offset1:14\n"
      "v_fma_f32 v50, 0.5, v62, -v74\n"
      "v_mfma_f32_16x16x32_bf16 v[100:103], a[96:99], a[200:203], v[100:103]\n"
      "ds_read_b128 a[192:195], v0 offset:20544\n"
      "v_fma_f32 v51, 0.5, v63, -v75\n"
      "v_mfma_f32_16x16x32_bf16 v[104:107], a[100:103], a[200:203], v[104:107]\n"
      "ds_read2st64_b32 v[36:37], v6 offset0:16 offset1:18\n"
      "v_mfma_f32_16x16x32_bf16 v[108:111], a[104:107], a[200:203], v[108:111]\n"
      "s_waitcnt lgkmcnt(7)\n"
      "ds_read2st64_b32 v[38:39], v6 offset0:20 offset1:22\n"
      "v_sub_f32 v28, v28, v40\n"
      "v_sub_f32 v29, v29, v41\n"
      "v_sub_f32 v30, v30, v42\n"
      "v_mfma_f32_16x16x32_bf16 v[52:55], v[252:255], a[204:207], v[88:91]\n"
      "s_waitcnt lgkmcnt(5)\n"
      "ds_read_b128 a[196:199], v0 offset:16512\n"
      "v_sub_f32 v31, v31, v43\n"
      "v_mfma_f32_4x4x4_16b_bf16 v[12:15], a[216:217], a[220:221], 0\n"
      "v_fmac_f32 v28, s46, v76\n"
      "v_mfma_f32_4x4x4_16b_bf16 v[16:19], a[216:217], a[222:223], 0\n"
      "v_fmac_f32 v29, s46, v77\n"
      "v_fmac_f32 v30, s46, v78\n"
      "v_mfma_f32_16x16x32_bf16 v[56:59], v[244:247], a[204:207], v[92:95]\n"
      "v_fmac_f32 v31, s46, v79\n"
      "v_fmac_f32 v40, s45, v28\n"
      "v_mfma_f32_16x16x32_bf16 v[60:63], v[236:239], a[204:207], v[96:99]\n"
      "v_fmac_f32 v41, s45, v29\n"
      "v_cvt_pk_bf16_f32 v12, v12, v16\n"
      "v_fmac_f32_e64 v42, s45, v30\n"
      "v_mfma_f32_16x16x32_bf16 v[64:67], v[248:251], a[208:211], v[88:91]\n"
      "ds_read_b128 a[200:203], v0 offset:20608\n"
      "ds_write_addtid_b32 v12 offset:24832\n"
      "v_mfma_f32_16x16x32_bf16 v[68:71], v[240:243], a[208:211], v[92:95]\n"
      "ds_read_b64_tr_b16 a[224:225], v7 offset:512\n"
      "ds_read_b64_tr_b16 a[226:227], v7 offset:520\n"
      "v_mfma_f32_16x16x32_bf16 v[72:75], v[232:235], a[208:211], v[96:99]\n"
      "v_fmac_f32_e64 v43, s45, v31\n"
      "v_cvt_pk_bf16_f32 v13, v13, v17\n"
      "v_mfma_f32_16x16x32_bf16 v[52:55], v[228:231], a[212:215], v[52:55]\n"
      "ds_read_b128 a[204:207], v1 offset:16384\n"
      "ds_write_addtid_b32 v13 offset:28928\n"
      "v_fma_f32 v28, s47, v28, v100\n"
      "v_mfma_f32_16x16x32_bf16 v[56:59], v[220:223], a[212:215], v[56:59]\n"
      "v_fma_f32 v29, s47, v29, v101\n"
      "v_cvt_pk_bf16_f32 v14, v14, v18\n"
      "v_mfma_f32_16x16x32_bf16 v[60:63], v[212:215], a[212:215], v[60:63]\n"
      "s_waitcnt lgkmcnt(4)\n"
      "ds_write_addtid_b32 v14 offset:33024\n"
      "v_fma_f32 v30, s47, v30, v102\n"
      "v_mfma_f32_16x16x32_bf16 v[64:67], v[224:227], a[192:195], v[64:67]\n"
      "s_nop 0\n"
      "ds_read_b128 a[208:211], v1 offset:20480\n"
      "v_fma_f32 v31, s47, v31, v103\n"
      "v_mfma_f32_16x16x32_bf16 v[68:71], v[216:219], a[192:195], v[68:71]\n"
      "s_nop 0\n"
      "v_cvt_pk_bf16_f32 v40, v40, v41\n"
      "v_cvt_pk_bf16_f32 v15, v15, v19\n"
      "v_mfma_f32_16x16x32_bf16 v[72:75], v[208:211], a[192:195], v[72:75]\n"
      "s_waitcnt lgkmcnt(2)\n"
      "ds_write_addtid_b32 v40 offset:12288\n"
      "ds_write_addtid_b32 v15 offset:37120\n"
      "v_cvt_pk_bf16_f32 v42, v42, v43\n"
      "v_mfma_f32_4x4x4_16b_bf16 v[20:23], a[216:217], a[224:225], 0\n"
      "v_cvt_pk_bf16_f32 v28, v28, v29\n"
      "v_mfma_f32_4x4x4_16b_bf16 v[24:27], a[216:217], a[226:227], 0\n"
      "ds_write_addtid_b32 v42 offset:12800\n"
      "ds_write_addtid_b32 v28 offset:14336\n"
      "v_mfma_f32_16x16x32_bf16 v[52:55], v[204:207], a[196:199], v[52:55]\n"
      "ds_read_b128 a[212:215], v1 offset:16448\n"
      "v_cvt_pk_bf16_f32 v30, v30, v31\n"
      "v_mfma_f32_16x16x32_bf16 v[56:59], v[196:199], a[196:199], v[56:59]\n"
      "ds_write_addtid_b32 v30 offset:14848\n"
      "v_sub_f32 v32, v32, v44\n"
      "v_cvt_pk_bf16_f32 v20, v20, v24\n"
      "v_mfma_f32_16x16x32_bf16 v[60:63], v[188:191], a[196:199], v[60:63]\n"
      "ds_write_addtid_b32 v20 offset:25344\n"
      "v_sub_f32 v33, v33, v45\n"
      "v_mfma_f32_16x16x32_bf16 v[64:67], v[200:203], a[200:203], v[64:67]\n"
      "ds_read_b128 a[192:195], v1 offset:20544\n"
      "ds_read_b64_tr_b16 a[228:229], v7 offset:1024\n"
      "v_mfma_f32_16x16x32_bf16 v[68:71], v[192:195], a[200:203], v[68:71]\n"
      "ds_read_b64_tr_b16 a[230:231], v7 offset:1032\n"
      "v_sub_f32 v34, v34, v46\n"
      "v_mfma_f32_16x16x32_bf16 v[72:75], v[184:187], a[200:203], v[72:75]\n"
      "v_sub_f32 v35, v35, v47\n"
      "v_cvt_pk_bf16_f32 v21, v21, v25\n"
      "v_mfma_f32_16x16x32_bf16 v[52:55], v[180:183], a[204:207], v[52:55]\n"
      "ds_read_b128 a[196:199], v1 offset:16512\n"
      "ds_write_addtid_b32 v21 offset:29440\n"
      "v_fmac_f32 v32, s46, v80\n"
      "v_mfma_f32_16x16x32_bf16 v[56:59], v[172:175], a[204:207], v[56:59]\n"
      "v_fmac_f32 v33, s46, v81\n"
      "v_cvt_pk_bf16_f32 v22, v22, v26\n"
      "v_mfma_f32_16x16x32_bf16 v[60:63], v[164:167], a[204:207], v[60:63]\n"
      "s_waitcnt lgkmcnt(2)\n"
      "ds_write_addtid_b32 v22 offset:33536\n"
      "v_fmac_f32 v34, s46, v82\n"
      "v_mfma_f32_16x16x32_bf16 v[64:67], v[176:179], a[208:211], v[64:67]\n"
      "ds_read_b128 a[200:203], v1 offset:20608\n"
      "v_fmac_f32 v35, s46, v83\n"
      "v_mfma_f32_16x16x32_bf16 v[68:71], v[168:171], a[208:211], v[68:71]\n"
      "v_fmac_f32 v44, s45, v32\n"
      "v_cvt_pk_bf16_f32 v23, v23, v27\n"
      "v_mfma_f32_4x4x4_16b_bf16 v[12:15], a[216:217], a[228:229], 0\n"
      "v_fmac_f32 v45, s45, v33\n"
      "v_mfma_f32_4x4x4_16b_bf16 v[16:19], a[216:217], a[230:231], 0\n"
      "ds_write_addtid_b32 v23 offset:37632\n"
      "v_fmac_f32 v46, s45, v34\n"
      "v_mfma_f32_16x16x32_bf16 v[72:75], v[160:163], a[208:211], v[72:75]\n"
      "v_fmac_f32 v47, s45, v35\n"
      "v_fma_f32 v32, s47, v32, v104\n"
      "v_cvt_pk_bf16_f32 v12, v12, v16\n"
      "v_mfma_f32_16x16x32_bf16 v[52:55], v[156:159], a[212:215], v[52:55]\n"
      "ds_read_b128 a[204:207], v2 offset:16384\n"
      "ds_write_addtid_b32 v12 offset:25856\n"
      "v_mfma_f32_16x16x32_bf16 v[56:59], v[148:151], a[212:215], v[56:59]\n"
      "s_nop 0\n"
      "v_fma_f32 v33, s47, v33, v105\n"
      "v_cvt_pk_bf16_f32 v13, v13, v17\n"
      "v_mfma_f32_16x16x32_bf16 v[60:63], v[140:143], a[212:215], v[60:63]\n"
      "ds_read_b64_tr_b16 a[232:233], v7 offset:1536\n"
      "ds_write_addtid_b32 v13 offset:29952\n"
      "v_fma_f32 v34, s47, v34, v106\n"
      "v_mfma_f32_16x16x32_bf16 v[64:67], v[152:155], a[192:195], v[64:67]\n"
      "ds_read_b128 a[208:211], v2 offset:20480\n"
      "ds_read_b64_tr_b16 a[234:235], v7 offset:1544\n"
      "v_mfma_f32_16x16x32_bf16 v[68:71], v[144:147], a[192:195], v[68:71]\n"
      "v_fma_f32 v35, s47, v35, v107\n"
      "v_cvt_pk_bf16_f32 v44, v44, v45\n"
      "v_mfma_f32_16x16x32_bf16 v[72:75], v[136:139], a[192:195], v[72:75]\n"
      "s_waitcnt lgkmcnt(4)\n"
      "ds_write_addtid_b32 v44 offset:20484\n"
      "v_cvt_pk_bf16_f32 v14, v14, v18\n"
      "v_mfma_f32_16x16x32_bf16 v[52:55], v[132:135], a[196:199], v[52:55]\n"
      "s_nop 0\n"
      "ds_read_b128 a[212:215], v2 offset:16448\n"
      "ds_write_addtid_b32 v14 offset:34048\n"
      "v_cvt_pk_bf16_f32 v46, v46, v47\n"
      "v_mfma_f32_16x16x32_bf16 v[56:59], v[124:127], a[196:199], v[56:59]\n"
      "ds_write_addtid_b32 v46 offset:20996\n"
      "v_cvt_pk_bf16_f32 v32, v32, v33\n"
      "v_mfma_f32_16x16x32_bf16 v[60:63], a[184:187], a[196:199], v[60:63]\n"
      "ds_write_addtid_b32 v32 offset:22532\n"
      "v_cvt_pk_bf16_f32 v34, v34, v35\n"
      "v_mfma_f32_16x16x32_bf16 v[64:67], v[128:131], a[200:203], v[64:67]\n"
      "s_waitcnt lgkmcnt(4)\n"
      "ds_read_b128 a[192:195], v2 offset:20544\n"
      "ds_write_addtid_b32 v34 offset:23044\n"
      "v_sub_f32 v36, v36, v48\n"
      "v_mfma_f32_4x4x4_16b_bf16 v[20:23], a[216:217], a[232:233], 0\n"
      "v_sub_f32 v37, v37, v49\n"
      "v_mfma_f32_4x4x4_16b_bf16 v[24:27], a[216:217], a[234:235], 0\n"
      "v_sub_f32 v38, v38, v50\n"
      "v_cvt_pk_bf16_f32 v15, v15, v19\n"
      "v_mfma_f32_16x16x32_bf16 v[68:71], a[188:191], a[200:203], v[68:71]\n"
      "ds_write_addtid_b32 v15 offset:38144\n"
      "v_sub_f32 v39, v39, v51\n"
      "v_cvt_pk_bf16_f32 v20, v20, v24\n"
      "v_mfma_f32_16x16x32_bf16 v[72:75], a[180:183], a[200:203], v[72:75]\n"
      "ds_write_addtid_b32 v20 offset:26368\n"
      "v_fmac_f32 v36, s46, v84\n"
      "v_mfma_f32_16x16x32_bf16 v[52:55], a[176:179], a[204:207], v[52:55]\n"
      "ds_read_b128 a[196:199], v2 offset:16512\n"
      "v_fmac_f32 v37, s46, v85\n"
      "v_mfma_f32_16x16x32_bf16 v[56:59], a[168:171], a[204:207], v[56:59]\n"
      "ds_read_b64_tr_b16 a[220:221], v7 offset:2048\n"
      "ds_read_b64_tr_b16 a[222:223], v7 offset:2056\n"
      "v_mfma_f32_16x16x32_bf16 v[60:63], a[160:163], a[204:207], v[60:63]\n"
      "v_fmac_f32 v38, s46, v86\n"
      "v_cvt_pk_bf16_f32 v21, v21, v25\n"
      "v_mfma_f32_16x16x32_bf16 v[64:67], a[172:175], a[208:211], v[64:67]\n"
      "ds_read_b128 a[200:203], v2 offset:20608\n"
      "ds_write_addtid_b32 v21 offset:30464\n"
      "v_fmac_f32 v39, s46, v87\n"
      "v_mfma_f32_16x16x32_bf16 v[68:71], a[164:167], a[208:211], v[68:71]\n"
      "v_fmac_f32 v48, s45, v36\n"
      "v_cvt_pk_bf16_f32 v22, v22, v26\n"
      "v_mfma_f32_16x16x32_bf16 v[72:75], a[156:159], a[208:211], v[72:75]\n"
      "s_waitcnt lgkmcnt(4)\n"
      "ds_write_addtid_b32 v22 offset:34560\n"
      "v_fmac_f32 v49, s45, v37\n"
      "v_mfma_f32_16x16x32_bf16 v[52:55], a[152:155], a[212:215], v[52:55]\n"
      "ds_read_b128 a[204:207], v3 offset:16896\n"
      "v_fmac_f32 v50, s45, v38\n"
      "v_mfma_f32_16x16x32_bf16 v[56:59], a[144:147], a[212:215], v[56:59]\n"
      "v_fmac_f32_e64 v51, s45, v39\n"
      "v_cvt_pk_bf16_f32 v23, v23, v27\n"
      "v_mfma_f32_16x16x32_bf16 v[60:63], a[136:139], a[212:215], v[60:63]\n"
      "s_waitcnt lgkmcnt(2)\n"
      "ds_write_addtid_b32 v23 offset:38656\n"
      "v_fma_f32 v36, s47, v36, v108\n"
      "v_mfma_f32_4x4x4_16b_bf16 v[12:15], a[216:217], a[220:221], 0\n"
      "v_fma_f32 v37, s47, v37, v109\n"
      "v_mfma_f32_4x4x4_16b_bf16 v[16:19], a[216:217], a[222:223], 0\n"
      "v_fma_f32 v38, s47, v38, v110\n"
      "v_fma_f32 v39, s47, v39, v111\n"
      "v_mfma_f32_16x16x32_bf16 v[64:67], a[148:151], a[192:195], v[64:67]\n"
      "ds_read_b128 a[208:211], v3 offset:16960\n"
      "v_cvt_pk_bf16_f32 v48, v48, v49\n"
      "v_cvt_pk_bf16_f32 v12, v12, v16\n"
      "v_mfma_f32_16x16x32_bf16 v[68:71], a[140:143], a[192:195], v[68:71]\n"
      "ds_write_addtid_b32 v48 offset:16384\n"
      "ds_write_addtid_b32 v12 offset:26880\n"
      "v_mfma_f32_16x16x32_bf16 v[72:75], a[132:135], a[192:195], v[72:75]\n"
      "v_cvt_pk_bf16_f32 v13, v13, v17\n"
      "v_cvt_pk_bf16_f32 v14, v14, v18\n"
      "v_mfma_f32_16x16x32_bf16 v[52:55], a[128:131], a[196:199], v[52:55]\n"
      "ds_read_b128 a[212:215], v3 offset:17024\n"
      "ds_write_addtid_b32 v13 offset:30976\n"
      "v_mfma_f32_16x16x32_bf16 v[56:59], a[120:123], a[196:199], v[56:59]\n"
      "ds_read_b64_tr_b16 a[224:225], v7 offset:2560\n"
      "ds_write_addtid_b32 v14 offset:35072\n"
      "v_cvt_pk_bf16_f32 v50, v50, v51\n"
      "v_mfma_f32_16x16x32_bf16 v[60:63], a[112:115], a[196:199], v[60:63]\n"
      "ds_write_addtid_b32 v50 offset:16896\n"
      "ds_read_b64_tr_b16 a[226:227], v7 offset:2568\n"
      "v_mfma_f32_16x16x32_bf16 v[64:67], a[124:127], a[200:203], v[64:67]\n"
      "ds_read_b128 a[192:195], v4 offset:16896\n"
      "v_cvt_pk_bf16_f32 v36, v36, v37\n"
      "v_mfma_f32_16x16x32_bf16 v[68:71], a[116:119], a[200:203], v[68:71]\n"
      "ds_write_addtid_b32 v36 offset:18432\n"
      "v_cvt_pk_bf16_f32 v15, v15, v19\n"
      "v_mfma_f32_16x16x32_bf16 v[72:75], a[108:111], a[200:203], v[72:75]\n"
      "s_waitcnt lgkmcnt(6)\n"
      "ds_write_addtid_b32 v15 offset:39168\n"
      "v_sub_f32 v52, v64, v52\n"
      "v_sub_f32 v53, v65, v53\n"
      "v_cvt_pk_bf16_f32 v38, v38, v39\n"
      "v_mfma_f32_16x16x32_bf16 v[112:115], a[0:3], a[204:207], 0\n"
      "s_nop 0\n"
      "ds_read_b128 a[196:199], v4 offset:16960\n"
      "ds_write_addtid_b32 v38 offset:18944\n"
      "v_mfma_f32_16x16x32_bf16 v[116:119], a[4:7], a[204:207], 0\n"
      "s_waitcnt lgkmcnt(5)\n"
      "ds_write_addtid_b32 v52 offset:6144\n"
      "ds_write_addtid_b32 v53 offset:6656\n"
      "v_mfma_f32_4x4x4_16b_bf16 v[20:23], a[216:217], a[224:225], 0\n"
      "v_sub_f32 v54, v66, v54\n"
      "v_mfma_f32_4x4x4_16b_bf16 v[24:27], a[216:217], a[226:227], 0\n"
      "ds_write_addtid_b32 v54 offset:7168\n"
      "v_sub_f32_e64 v55, v67, v55\n"
      "v_mfma_f32_16x16x32_bf16 v[120:123], a[8:11], a[204:207], 0\n"
      "ds_write_addtid_b32 v55 offset:7680\n"
      "v_sub_f32 v56, v68, v56\n"
      "v_sub_f32 v57, v69, v57\n"
      "v_mfma_f32_16x16x32_bf16 v[112:115], a[12:15], a[208:211], v[112:115]\n"
      "ds_read_b128 a[200:203], v4 offset:17024\n"
      "ds_write_addtid_b32 v56 offset:8192\n"
      "v_mfma_f32_16x16x32_bf16 v[116:119], a[16:19], a[208:211], v[116:119]\n"
      "ds_write_addtid_b32 v57 offset:8704\n"
      "v_sub_f32 v58, v70, v58\n"
      "v_mfma_f32_16x16x32_bf16 v[120:123], a[20:23], a[208:211], v[120:123]\n"
      "ds_write_addtid_b32 v58 offset:9216\n"
      "v_cvt_pk_bf16_f32 v20, v20, v24\n"
      "v_mfma_f32_16x16x32_bf16 v[112:115], a[24:27], a[212:215], v[112:115]\n"
      "ds_read_b128 a[204:207], v5 offset:16896\n"
      "ds_write_addtid_b32 v20 offset:27392\n"
      "v_sub_f32 v59, v71, v59\n"
      "v_mfma_f32_16x16x32_bf16 v[116:119], a[28:31], a[212:215], v[116:119]\n"
      "ds_write_addtid_b32 v59 offset:9728\n"
      "v_sub_f32 v60, v72, v60\n"
      "v_mfma_f32_16x16x32_bf16 v[120:123], a[32:35], a[212:215], v[120:123]\n"
      "s_waitcnt lgkmcnt(7)\n"
      "ds_write_addtid_b32 v60 offset:10240\n"
      "v_cvt_pk_bf16_f32 v21, v21, v25\n"
      "v_mfma_f32_16x16x32_bf16 v[112:115], a[36:39], a[192:195], v[112:115]\n"
      "ds_read_b128 a[208:211], v5 offset:16960\n"
      "ds_write_addtid_b32 v21 offset:31488\n"
      "v_mfma_f32_16x16x32_bf16 v[116:119], a[40:43], a[192:195], v[116:119]\n"
      "v_sub_f32 v61, v73, v61\n"
      "v_sub_f32 v62, v74, v62\n"
      "v_cvt_pk_bf16_f32 v22, v22, v26\n"
      "v_mfma_f32_16x16x32_bf16 v[120:123], a[44:47], a[192:195], v[120:123]\n"
      "ds_write_addtid_b32 v22 offset:35584\n"
      "ds_write_addtid_b32 v61 offset:10752\n"
      "v_sub_f32 v63, v75, v63\n"
      "v_mfma_f32_16x16x32_bf16 v[112:115], a[48:51], a[196:199], v[112:115]\n"
      "s_nop 0\n"
      "ds_read_b128 a[212:215], v5 offset:17024\n"
      "ds_write_addtid_b32 v62 offset:11264\n"
      "v_mfma_f32_16x16x32_bf16 v[116:119], a[52:55], a[196:199], v[116:119]\n"
      "s_nop 0\n"
      "ds_write_addtid_b32 v63 offset:11776\n"
      "v_cvt_pk_bf16_f32 v23, v23, v27\n"
      "v_mfma_f32_16x16x32_bf16 v[120:123], a[56:59], a[196:199], v[120:123]\n"
      "s_waitcnt lgkmcnt(5)\n"
      "ds_write_addtid_b32 v23 offset:39680\n"
      "v_fma_f32 v40, 0.5, v52, -v64\n"
      "v_mfma_f32_16x16x32_bf16 v[112:115], a[60:63], a[200:203], v[112:115]\n"
      "v_fma_f32 v41, 0.5, v53, -v65\n"
      "v_fma_f32 v42, 0.5, v54, -v66\n"
      "v_mfma_f32_16x16x32_bf16 v[116:119], a[64:67], a[200:203], v[116:119]\n"
      "v_fma_f32 v43, 0.5, v55, -v67\n"
      "v_fma_f32 v44, 0.5, v56, -v68\n"
      "v_mfma_f32_16x16x32_bf16 v[120:123], a[68:71], a[200:203], v[120:123]\n"
      "s_waitcnt lgkmcnt(0)\n"
      "v_fma_f32 v45, 0.5, v57, -v69\n"
      "s_barrier\n"
      "v_mfma_f32_16x16x32_bf16 v[112:115], a[72:75], a[204:207], v[112:115]\n"
      "ds_read_b128 a[192:195], v0 offset:0\n"
      "ds_read2st64_b32 v[28:29], v6 offset0:24 offset1:26\n"
      "v_fma_f32 v46, 0.5, v58, -v70\n"
      "v_mfma_f32_16x16x32_bf16 v[116:119], a[76:79], a[204:207], v[116:119]\n"
      "ds_read_b128 a[196:199], v0 offset:4096\n"
      "ds_read2st64_b32 v[30:31], v6 offset0:28 offset1:30\n"
      "v_mfma_f32_16x16x32_bf16 v[120:123], a[80:83], a[204:207], v[120:123]\n"
      "v_fma_f32 v47, 0.5, v59, -v71\n"
      "v_fma_f32 v48, 0.5, v60, -v72\n"
      "v_mfma_f32_16x16x32_bf16 v[112:115], a[84:87], a[208:211], v[112:115]\n"
      "ds_read_b128 a[200:203], v0 offset:64\n"
      "ds_read_b64_tr_b16 a[220:221], v7 offset:16896\n"
      "v_mfma_f32_16x16x32_bf16 v[116:119], a[88:91], a[208:211], v[116:119]\n"
      "ds_read_b64_tr_b16 a[222:223], v7 offset:16904\n"
      "ds_read2st64_b32 v[32:33], v6 offset0:32 offset1:34\n"
      "v_fma_f32 v49, 0.5, v61, -v73\n"
      "v_mfma_f32_16x16x32_bf16 v[120:123], a[92:95], a[208:211], v[120:123]\n"
      "ds_read2st64_b32 v[34:35], v6 offset0:36 offset1:38\n"
      "v_fma_f32 v50, 0.5, v62, -v74\n"
      "v_mfma_f32_16x16x32_bf16 v[112:115], a[96:99], a[212:215], v[112:115]\n"
      "ds_read_b128 a[204:207], v0 offset:4160\n"
      "v_fma_f32 v51, 0.5, v63, -v75\n"
      "v_mfma_f32_16x16x32_bf16 v[116:119], a[100:103], a[212:215], v[116:119]\n"
      "ds_read2st64_b32 v[36:37], v6 offset0:40 offset1:42\n"
      "v_mfma_f32_16x16x32_bf16 v[120:123], a[104:107], a[212:215], v[120:123]\n"
      "s_waitcnt lgkmcnt(7)\n"
      "ds_read2st64_b32 v[38:39], v6 offset0:44 offset1:46\n"
      "v_sub_f32 v28, v28, v40\n"
      "v_sub_f32 v29, v29, v41\n"
      "v_sub_f32 v30, v30, v42\n"
      "v_mfma_f32_16x16x32_bf16 v[52:55], v[252:255], a[192:195], v[100:103]\n"
      "s_waitcnt lgkmcnt(5)\n"
      "ds_read_b128 a[208:211], v0 offset:128\n"
      "v_sub_f32 v31, v31, v43\n"
      "v_mfma_f32_4x4x4_16b_bf16 v[12:15], a[216:217], a[220:221], 0\n"
      "v_fmac_f32 v28, s46, v88\n"
      "v_mfma_f32_4x4x4_16b_bf16 v[16:19], a[216:217], a[222:223], 0\n"
      "v_fmac_f32 v29, s46, v89\n"
      "v_fmac_f32 v30, s46, v90\n"
      "v_mfma_f32_16x16x32_bf16 v[56:59], v[244:247], a[192:195], v[104:107]\n"
      "v_fmac_f32 v31, s46, v91\n"
      "v_fmac_f32_e64 v40, s45, v28\n"
      "v_mfma_f32_16x16x32_bf16 v[60:63], v[236:239], a[192:195], v[108:111]\n"
      "v_fmac_f32 v41, s45, v29\n"
      "v_cvt_pk_bf16_f32 v12, v12, v16\n"
      "v_fmac_f32 v42, s45, v30\n"
      "v_mfma_f32_16x16x32_bf16 v[64:67], v[248:251], a[196:199], v[100:103]\n"
      "ds_read_b128 a[212:215], v0 offset:4224\n"
      "ds_write_addtid_b32 v12 offset:41216\n"
      "v_mfma_f32_16x16x32_bf16 v[68:71], v[240:243], a[196:199], v[104:107]\n"
      "ds_read_b64_tr_b16 a[224:225], v7 offset:17408\n"
      "ds_read_b64_tr_b16 a[226:227], v7 offset:17416\n"
      "v_mfma_f32_16x16x32_bf16 v[72:75], v[232:235], a[196:199], v[108:111]\n"
      "v_fmac_f32 v43, s45, v31\n"
      "v_cvt_pk_bf16_f32 v13, v13, v17\n"
      "v_mfma_f32_16x16x32_bf16 v[52:55], v[228:231], a[200:203], v[52:55]\n"
      "s_nop 0\n"
      "ds_read_b128 a[192:195], v1 offset:0\n"
      "ds_write_addtid_b32 v13 offset:45312\n"
      "v_fma_f32 v28, s47, v28, v112\n"
      "v_mfma_f32_16x16x32_bf16 v[56:59], v[220:223], a[200:203], v[56:59]\n"
      "v_fma_f32 v29, s47, v29, v113\n"
      "v_cvt_pk_bf16_f32 v14, v14, v18\n"
      "v_mfma_f32_16x16x32_bf16 v[60:63], v[212:215], a[200:203], v[60:63]\n"
      "s_waitcnt lgkmcnt(4)\n"
      "ds_write_addtid_b32 v14 offset:49408\n"
      "v_fma_f32 v30, s47, v30, v114\n"
      "v_mfma_f32_16x16x32_bf16 v[64:67], v[224:227], a[204:207], v[64:67]\n"
      "s_nop 0\n"
      "ds_read_b128 a[196:199], v1 offset:4096\n"
      "v_fma_f32 v31, s47, v31, v115\n"
      "v_mfma_f32_16x16x32_bf16 v[68:71], v[216:219], a[204:207], v[68:71]\n"
      "s_nop 0\n"
      "v_cvt_pk_bf16_f32 v40, v40, v41\n"
      "v_cvt_pk_bf16_f32 v15, v15, v19\n"
      "v_mfma_f32_16x16x32_bf16 v[72:75], v[208:211], a[204:207], v[72:75]\n"
      "s_waitcnt lgkmcnt(2)\n"
      "ds_write_addtid_b32 v40 offset:13312\n"
      "ds_write_addtid_b32 v15 offset:53504\n"
      "v_cvt_pk_bf16_f32 v42, v42, v43\n"
      "v_mfma_f32_4x4x4_16b_bf16 v[20:23], a[216:217], a[224:225], 0\n"
      "v_cvt_pk_bf16_f32 v28, v28, v29\n"
      "v_mfma_f32_4x4x4_16b_bf16 v[24:27], a[216:217], a[226:227], 0\n"
      "ds_write_addtid_b32 v42 offset:13824\n"
      "ds_write_addtid_b32 v28 offset:15360\n"
      "v_mfma_f32_16x16x32_bf16 v[52:55], v[204:207], a[208:211], v[52:55]\n"
      "ds_read_b128 a[200:203], v1 offset:64\n"
      "v_cvt_pk_bf16_f32 v30, v30, v31\n"
      "v_mfma_f32_16x16x32_bf16 v[56:59], v[196:199], a[208:211], v[56:59]\n"
      "ds_write_addtid_b32 v30 offset:15872\n"
      "v_sub_f32 v32, v32, v44\n"
      "v_cvt_pk_bf16_f32 v20, v20, v24\n"
      "v_mfma_f32_16x16x32_bf16 v[60:63], v[188:191], a[208:211], v[60:63]\n"
      "ds_write_addtid_b32 v20 offset:41728\n"
      "v_sub_f32 v33, v33, v45\n"
      "v_mfma_f32_16x16x32_bf16 v[64:67], v[200:203], a[212:215], v[64:67]\n"
      "ds_read_b128 a[204:207], v1 offset:4160\n"
      "ds_read_b64_tr_b16 a[228:229], v7 offset:17920\n"
      "v_mfma_f32_16x16x32_bf16 v[68:71], v[192:195], a[212:215], v[68:71]\n"
      "ds_read_b64_tr_b16 a[230:231], v7 offset:17928\n"
      "v_sub_f32 v34, v34, v46\n"
      "v_mfma_f32_16x16x32_bf16 v[72:75], v[184:187], a[212:215], v[72:75]\n"
      "v_sub_f32 v35, v35, v47\n"
      "v_cvt_pk_bf16_f32 v21, v21, v25\n"
      "v_mfma_f32_16x16x32_bf16 v[52:55], v[180:183], a[192:195], v[52:55]\n"
      "ds_read_b128 a[208:211], v1 offset:128\n"
      "ds_write_addtid_b32 v21 offset:45824\n"
      "v_fmac_f32 v32, s46, v92\n"
      "v_mfma_f32_16x16x32_bf16 v[56:59], v[172:175], a[192:195], v[56:59]\n"
      "v_fmac_f32_e64 v33, s46, v93\n"
      "v_cvt_pk_bf16_f32 v22, v22, v26\n"
      "v_mfma_f32_16x16x32_bf16 v[60:63], v[164:167], a[192:195], v[60:63]\n"
      "s_waitcnt lgkmcnt(2)\n"
      "ds_write_addtid_b32 v22 offset:49920\n"
      "v_fmac_f32 v34, s46, v94\n"
      "v_mfma_f32_16x16x32_bf16 v[64:67], v[176:179], a[196:199], v[64:67]\n"
      "ds_read_b128 a[212:215], v1 offset:4224\n"
      "v_fmac_f32 v35, s46, v95\n"
      "v_mfma_f32_16x16x32_bf16 v[68:71], v[168:171], a[196:199], v[68:71]\n"
      "v_fmac_f32 v44, s45, v32\n"
      "v_cvt_pk_bf16_f32 v23, v23, v27\n"
      "v_mfma_f32_4x4x4_16b_bf16 v[12:15], a[216:217], a[228:229], 0\n"
      "v_fmac_f32 v45, s45, v33\n"
      "v_mfma_f32_4x4x4_16b_bf16 v[16:19], a[216:217], a[230:231], 0\n"
      "ds_write_addtid_b32 v23 offset:54016\n"
      "v_fmac_f32 v46, s45, v34\n"
      "v_mfma_f32_16x16x32_bf16 v[72:75], v[160:163], a[196:199], v[72:75]\n"
      "v_fmac_f32 v47, s45, v35\n"
      "v_fma_f32 v32, s47, v32, v116\n"
      "v_cvt_pk_bf16_f32 v12, v12, v16\n"
      "v_mfma_f32_16x16x32_bf16 v[52:55], v[156:159], a[200:203], v[52:55]\n"
      "ds_read_b128 a[192:195], v2 offset:0\n"
      "ds_write_addtid_b32 v12 offset:42240\n"
      "v_mfma_f32_16x16x32_bf16 v[56:59], v[148:151], a[200:203], v[56:59]\n"
      "v_fma_f32 v33, s47, v33, v117\n"
      "v_cvt_pk_bf16_f32 v13, v13, v17\n"
      "v_mfma_f32_16x16x32_bf16 v[60:63], v[140:143], a[200:203], v[60:63]\n"
      "ds_read_b64_tr_b16 a[232:233], v7 offset:18432\n"
      "ds_write_addtid_b32 v13 offset:46336\n"
      "v_fma_f32 v34, s47, v34, v118\n"
      "v_mfma_f32_16x16x32_bf16 v[64:67], v[152:155], a[204:207], v[64:67]\n"
      "ds_read_b128 a[196:199], v2 offset:4096\n"
      "ds_read_b64_tr_b16 a[234:235], v7 offset:18440\n"
      "v_mfma_f32_16x16x32_bf16 v[68:71], v[144:147], a[204:207], v[68:71]\n"
      "s_nop 0\n"
      "v_fma_f32 v35, s47, v35, v119\n"
      "v_cvt_pk_bf16_f32 v44, v44, v45\n"
      "v_mfma_f32_16x16x32_bf16 v[72:75], v[136:139], a[204:207], v[72:75]\n"
      "s_waitcnt lgkmcnt(4)\n"
      "ds_write_addtid_b32 v44 offset:21508\n"
      "v_cvt_pk_bf16_f32 v14, v14, v18\n"
      "v_mfma_f32_16x16x32_bf16 v[52:55], v[132:135], a[208:211], v[52:55]\n"
      "ds_read_b128 a[200:203], v2 offset:64\n"
      "ds_write_addtid_b32 v14 offset:50432\n"
      "v_cvt_pk_bf16_f32 v46, v46, v47\n"
      "v_mfma_f32_16x16x32_bf16 v[56:59], v[124:127], a[208:211], v[56:59]\n"
      "ds_write_addtid_b32 v46 offset:22020\n"
      "v_cvt_pk_bf16_f32 v32, v32, v33\n"
      "v_mfma_f32_16x16x32_bf16 v[60:63], a[184:187], a[208:211], v[60:63]\n"
      "ds_write_addtid_b32 v32 offset:23556\n"
      "v_cvt_pk_bf16_f32 v34, v34, v35\n"
      "v_mfma_f32_16x16x32_bf16 v[64:67], v[128:131], a[212:215], v[64:67]\n"
      "s_waitcnt lgkmcnt(4)\n"
      "ds_read_b128 a[204:207], v2 offset:4160\n"
      "ds_write_addtid_b32 v34 offset:24068\n"
      "v_sub_f32 v36, v36, v48\n"
      "v_mfma_f32_4x4x4_16b_bf16 v[20:23], a[216:217], a[232:233], 0\n"
      "v_sub_f32 v37, v37, v49\n"
      "v_mfma_f32_4x4x4_16b_bf16 v[24:27], a[216:217], a[234:235], 0\n"
      "v_sub_f32 v38, v38, v50\n"
      "v_cvt_pk_bf16_f32 v15, v15, v19\n"
      "v_mfma_f32_16x16x32_bf16 v[68:71], a[188:191], a[212:215], v[68:71]\n"
      "ds_write_addtid_b32 v15 offset:54528\n"
      "v_sub_f32_e64 v39, v39, v51\n"
      "v_cvt_pk_bf16_f32 v20, v20, v24\n"
      "v_mfma_f32_16x16x32_bf16 v[72:75], a[180:183], a[212:215], v[72:75]\n"
      "ds_write_addtid_b32 v20 offset:42752\n"
      "v_fmac_f32 v36, s46, v96\n"
      "v_mfma_f32_16x16x32_bf16 v[52:55], a[176:179], a[192:195], v[52:55]\n"
      "ds_read_b128 a[208:211], v2 offset:128\n"
      "v_fmac_f32 v37, s46, v97\n"
      "v_mfma_f32_16x16x32_bf16 v[56:59], a[168:171], a[192:195], v[56:59]\n"
      "ds_read_b64_tr_b16 a[220:221], v7 offset:18944\n"
      "ds_read_b64_tr_b16 a[222:223], v7 offset:18952\n"
      "v_mfma_f32_16x16x32_bf16 v[60:63], a[160:163], a[192:195], v[60:63]\n"
      "v_fmac_f32 v38, s46, v98\n"
      "v_cvt_pk_bf16_f32 v21, v21, v25\n"
      "v_mfma_f32_16x16x32_bf16 v[64:67], a[172:175], a[196:199], v[64:67]\n"
      "ds_read_b128 a[212:215], v2 offset:4224\n"
      "ds_write_addtid_b32 v21 offset:46848\n"
      "v_fmac_f32 v39, s46, v99\n"
      "v_mfma_f32_16x16x32_bf16 v[68:71], a[164:167], a[196:199], v[68:71]\n"
      "v_fmac_f32_e64 v48, s45, v36\n"
      "v_cvt_pk_bf16_f32 v22, v22, v26\n"
      "v_mfma_f32_16x16x32_bf16 v[72:75], a[156:159], a[196:199], v[72:75]\n"
      "s_waitcnt lgkmcnt(4)\n"
      "ds_write_addtid_b32 v22 offset:50944\n"
      "v_fmac_f32 v49, s45, v37\n"
      "v_mfma_f32_16x16x32_bf16 v[52:55], a[152:155], a[200:203], v[52:55]\n"
      "ds_read_b128 a[192:195], v3 offset:33792\n"
      "v_fmac_f32 v50, s45, v38\n"
      "v_mfma_f32_16x16x32_bf16 v[56:59], a[144:147], a[200:203], v[56:59]\n"
      "s_nop 0\n"
      "v_fmac_f32 v51, s45, v39\n"
      "v_cvt_pk_bf16_f32 v23, v23, v27\n"
      "v_mfma_f32_16x16x32_bf16 v[60:63], a[136:139], a[200:203], v[60:63]\n"
      "s_waitcnt lgkmcnt(2)\n"
      "ds_write_addtid_b32 v23 offset:55040\n"
      "v_fma_f32 v36, s47, v36, v120\n"
      "v_mfma_f32_4x4x4_16b_bf16 v[12:15], a[216:217], a[220:221], 0\n"
      "v_fma_f32 v37, s47, v37, v121\n"
      "v_mfma_f32_4x4x4_16b_bf16 v[16:19], a[216:217], a[222:223], 0\n"
      "v_fma_f32 v38, s47, v38, v122\n"
      "v_fma_f32 v39, s47, v39, v123\n"
      "v_mfma_f32_16x16x32_bf16 v[64:67], a[148:151], a[204:207], v[64:67]\n"
      "ds_read_b128 a[196:199], v3 offset:33856\n"
      "v_cvt_pk_bf16_f32 v48, v48, v49\n"
      "v_cvt_pk_bf16_f32 v12, v12, v16\n"
      "v_mfma_f32_16x16x32_bf16 v[68:71], a[140:143], a[204:207], v[68:71]\n"
      "ds_write_addtid_b32 v48 offset:17408\n"
      "ds_write_addtid_b32 v12 offset:43264\n"
      "v_mfma_f32_16x16x32_bf16 v[72:75], a[132:135], a[204:207], v[72:75]\n"
      "v_cvt_pk_bf16_f32 v13, v13, v17\n"
      "v_cvt_pk_bf16_f32 v14, v14, v18\n"
      "v_mfma_f32_16x16x32_bf16 v[52:55], a[128:131], a[208:211], v[52:55]\n"
      "ds_read_b128 a[200:203], v3 offset:33920\n"
      "ds_write_addtid_b32 v13 offset:47360\n"
      "v_mfma_f32_16x16x32_bf16 v[56:59], a[120:123], a[208:211], v[56:59]\n"
      "ds_read_b64_tr_b16 a[224:225], v7 offset:19456\n"
      "ds_write_addtid_b32 v14 offset:51456\n"
      "v_cvt_pk_bf16_f32 v50, v50, v51\n"
      "v_mfma_f32_16x16x32_bf16 v[60:63], a[112:115], a[208:211], v[60:63]\n"
      "ds_write_addtid_b32 v50 offset:17920\n"
      "ds_read_b64_tr_b16 a[226:227], v7 offset:19464\n"
      "v_mfma_f32_16x16x32_bf16 v[64:67], a[124:127], a[212:215], v[64:67]\n"
      "ds_read_b128 a[204:207], v4 offset:33792\n"
      "v_cvt_pk_bf16_f32 v36, v36, v37\n"
      "v_mfma_f32_16x16x32_bf16 v[68:71], a[116:119], a[212:215], v[68:71]\n"
      "ds_write_addtid_b32 v36 offset:19456\n"
      "v_cvt_pk_bf16_f32 v15, v15, v19\n"
      "v_mfma_f32_16x16x32_bf16 v[72:75], a[108:111], a[212:215], v[72:75]\n"
      "s_waitcnt lgkmcnt(6)\n"
      "ds_write_addtid_b32 v15 offset:55552\n"
      "v_sub_f32 v52, v64, v52\n"
      "v_sub_f32_e64 v53, v65, v53\n"
      "v_cvt_pk_bf16_f32 v38, v38, v39\n"
      "v_mfma_f32_16x16x32_bf16 v[76:79], a[0:3], a[192:195], 0\n"
      "s_nop 0\n"
      "ds_read_b128 a[208:211], v4 offset:33856\n"
      "ds_write_addtid_b32 v38 offset:19968\n"
      "v_mfma_f32_16x16x32_bf16 v[80:83], a[4:7], a[192:195], 0\n"
      "s_waitcnt lgkmcnt(5)\n"
      "ds_write_addtid_b32 v52 offset:0\n"
      "ds_write_addtid_b32 v53 offset:512\n"
      "v_mfma_f32_4x4x4_16b_bf16 v[20:23], a[216:217], a[224:225], 0\n"
      "v_sub_f32 v54, v66, v54\n"
      "v_mfma_f32_4x4x4_16b_bf16 v[24:27], a[216:217], a[226:227], 0\n"
      "ds_write_addtid_b32 v54 offset:1024\n"
      "v_sub_f32 v55, v67, v55\n"
      "v_mfma_f32_16x16x32_bf16 v[84:87], a[8:11], a[192:195], 0\n"
      "ds_write_addtid_b32 v55 offset:1536\n"
      "v_sub_f32 v56, v68, v56\n"
      "v_sub_f32 v57, v69, v57\n"
      "v_mfma_f32_16x16x32_bf16 v[76:79], a[12:15], a[196:199], v[76:79]\n"
      "ds_read_b128 a[212:215], v4 offset:33920\n"
      "ds_write_addtid_b32 v56 offset:2048\n"
      "v_mfma_f32_16x16x32_bf16 v[80:83], a[16:19], a[196:199], v[80:83]\n"
      "ds_write_addtid_b32 v57 offset:2560\n"
      "v_sub_f32 v58, v70, v58\n"
      "v_mfma_f32_16x16x32_bf16 v[84:87], a[20:23], a[196:199], v[84:87]\n"
      "s_nop 0\n"
      "ds_write_addtid_b32 v58 offset:3072\n"
      "v_cvt_pk_bf16_f32 v20, v20, v24\n"
      "v_mfma_f32_16x16x32_bf16 v[76:79], a[24:27], a[200:203], v[76:79]\n"
      "ds_read_b128 a[192:195], v5 offset:33792\n"
      "ds_write_addtid_b32 v20 offset:43776\n"
      "v_sub_f32 v59, v71, v59\n"
      "v_mfma_f32_16x16x32_bf16 v[80:83], a[28:31], a[200:203], v[80:83]\n"
      "ds_write_addtid_b32 v59 offset:3584\n"
      "v_sub_f32_e64 v60, v72, v60\n"
      "v_mfma_f32_16x16x32_bf16 v[84:87], a[32:35], a[200:203], v[84:87]\n"
      "s_waitcnt lgkmcnt(7)\n"
      "ds_write_addtid_b32 v60 offset:4096\n"
      "v_cvt_pk_bf16_f32 v21, v21, v25\n"
      "v_mfma_f32_16x16x32_bf16 v[76:79], a[36:39], a[204:207], v[76:79]\n"
      "ds_read_b128 a[196:199], v5 offset:33856\n"
      "ds_write_addtid_b32 v21 offset:47872\n"
      "v_mfma_f32_16x16x32_bf16 v[80:83], a[40:43], a[204:207], v[80:83]\n"
      "v_sub_f32 v61, v73, v61\n"
      "v_sub_f32 v62, v74, v62\n"
      "v_cvt_pk_bf16_f32 v22, v22, v26\n"
      "v_mfma_f32_16x16x32_bf16 v[84:87], a[44:47], a[204:207], v[84:87]\n"
      "ds_write_addtid_b32 v22 offset:51968\n"
      "ds_write_addtid_b32 v61 offset:4608\n"
      "v_sub_f32 v63, v75, v63\n"
      "v_mfma_f32_16x16x32_bf16 v[76:79], a[48:51], a[208:211], v[76:79]\n"
      "ds_read_b128 a[200:203], v5 offset:33920\n"
      "ds_write_addtid_b32 v62 offset:5120\n"
      "v_mfma_f32_16x16x32_bf16 v[80:83], a[52:55], a[208:211], v[80:83]\n"
      "s_nop 0\n"
      "ds_write_addtid_b32 v63 offset:5632\n"
      "v_cvt_pk_bf16_f32 v23, v23, v27\n"
      "v_mfma_f32_16x16x32_bf16 v[84:87], a[56:59], a[208:211], v[84:87]\n"
      "s_waitcnt lgkmcnt(5)\n"
      "ds_write_addtid_b32 v23 offset:56064\n"
      "v_fma_f32 v40, 0.5, v52, -v64\n"
      "v_mfma_f32_16x16x32_bf16 v[76:79], a[60:63], a[212:215], v[76:79]\n"
      "s_nop 0\n"
      "v_fma_f32 v41, 0.5, v53, -v65\n"
      "v_fma_f32 v42, 0.5, v54, -v66\n"
      "v_mfma_f32_16x16x32_bf16 v[80:83], a[64:67], a[212:215], v[80:83]\n"
      "v_fma_f32 v43, 0.5, v55, -v67\n"
      "v_fma_f32 v44, 0.5, v56, -v68\n"
      "v_mfma_f32_16x16x32_bf16 v[84:87], a[68:71], a[212:215], v[84:87]\n"
      "s_waitcnt lgkmcnt(0)\n"
      "v_fma_f32 v45, 0.5, v57, -v69\n"
      "s_barrier\n"
      "s_add_u32 s55, s55, 2\n"
      "s_cmp_lt_u32 s55, 6\n"
      "s_cbranch_scc1 LSloop_%=\n"
      "LSlast_%=:\n"
      "v_mfma_f32_16x16x32_bf16 v[76:79], a[72:75], a[192:195], v[76:79]\n"
      "s_add_u32 s59, s62, 4915200\n"
      "ds_read_b128 a[204:207], v0 offset:16384\n"
      "ds_read2st64_b32 v[28:29], v6 offset0:0 offset1:2\n"
      "v_fma_f32 v46, 0.5, v58, -v70\n"
      "v_mfma_f32_16x16x32_bf16 v[80:83], a[76:79], a[192:195], v[80:83]\n"
      "s_add_u32 s59, s59, s40\n"
      "ds_read_b128 a[208:211], v0 offset:20480\n"
      "ds_read2st64_b32 v[30:31], v6 offset0:4 offset1:6\n"
      "v_mfma_f32_16x16x32_bf16 v[84:87], a[80:83], a[192:195], v[84:87]\n"
      "ds_read_b64_tr_b16 a[220:221], v7 offset:33792\n"
      "v_fma_f32 v47, 0.5, v59, -v71\n"
      "v_mfma_f32_16x16x32_bf16 v[76:79], a[84:87], a[196:199], v[76:79]\n"
      "ds_read_b128 a[212:215], v0 offset:16448\n"
      "v_fma_f32 v48, 0.5, v60, -v72\n"
      "v_mfma_f32_16x16x32_bf16 v[80:83], a[88:91], a[196:199], v[80:83]\n"
      "ds_read_b64_tr_b16 a[222:223], v7 offset:33800\n"
      "ds_read2st64_b32 v[32:33], v6 offset0:8 offset1:10\n"
      "v_fma_f32 v49, 0.5, v61, -v73\n"
      "v_mfma_f32_16x16x32_bf16 v[84:87], a[92:95], a[196:199], v[84:87]\n"
      "ds_read2st64_b32 v[34:35], v6 offset0:12 offset1:14\n"
      "v_fma_f32 v50, 0.5, v62, -v74\n"
      "v_mfma_f32_16x16x32_bf16 v[76:79], a[96:99], a[200:203], v[76:79]\n"
      "ds_read_b128 a[192:195], v0 offset:20544\n"
      "v_fma_f32 v51, 0.5, v63, -v75\n"
      "v_mfma_f32_16x16x32_bf16 v[80:83], a[100:103], a[200:203], v[80:83]\n"
      "ds_read2st64_b32 v[36:37], v6 offset0:16 offset1:18\n"
      "ds_read2st64_b32 v[38:39], v6 offset0:20 offset1:22\n"
      "v_mfma_f32_16x16x32_bf16 v[84:87], a[104:107], a[200:203], v[84:87]\n"
      "s_waitcnt lgkmcnt(8)\n"
      "v_sub_f32 v28, v28, v40\n"
      "v_sub_f32 v29, v29, v41\n"
      "v_sub_f32 v30, v30, v42\n"
      "v_mfma_f32_16x16x32_bf16 v[52:55], v[252:255], a[204:207], v[112:115]\n"
      "s_waitcnt lgkmcnt(5)\n"
      "ds_read_b128 a[196:199], v0 offset:16512\n"
      "ds_read2st64_b32 a[240:241], v8 offset0:4 offset1:6\n"
      "v_mfma_f32_4x4x4_16b_bf16 v[12:15], a[216:217], a[220:221], 0\n"
      "ds_read2st64_b32 a[242:243], v9 offset0:4 offset1:6\n"
      "v_mfma_f32_4x4x4_16b_bf16 v[16:19], a[216:217], a[222:223], 0\n"
      "v_sub_f32 v31, v31, v43\n"
      "v_fmac_f32 v28, s46, v100\n"
      "v_fmac_f32 v29, s46, v101\n"
      "v_mfma_f32_16x16x32_bf16 v[56:59], v[244:247], a[204:207], v[116:119]\n"
      "v_fmac_f32 v30, s46, v102\n"
      "v_fmac_f32 v31, s46, v103\n"
      "v_mfma_f32_16x16x32_bf16 v[60:63], v[236:239], a[204:207], v[120:123]\n"
      "v_fmac_f32 v40, s45, v28\n"
      "v_fmac_f32 v41, s45, v29\n"
      "v_mfma_f32_16x16x32_bf16 v[64:67], v[248:251], a[208:211], v[112:115]\n"
      "ds_read_b128 a[200:203], v0 offset:20608\n"
      "v_cvt_pk_bf16_f32 v12, v12, v16\n"
      "v_mfma_f32_16x16x32_bf16 v[68:71], v[240:243], a[208:211], v[116:119]\n"
      "ds_write_addtid_b32 v12 offset:24832\n"
      "ds_read_b64_tr_b16 a[224:225], v7 offset:34304\n"
      "v_fmac_f32 v42, s45, v30\n"
      "v_mfma_f32_16x16x32_bf16 v[72:75], v[232:235], a[208:211], v[120:123]\n"
      "ds_read_b64_tr_b16 a[226:227], v7 offset:34312\n"
      "v_fmac_f32 v43, s45, v31\n"
      "v_mfma_f32_16x16x32_bf16 v[52:55], v[228:231], a[212:215], v[52:55]\n"
      "ds_read_b128 a[204:207], v1 offset:16384\n"
      "v_cvt_pk_bf16_f32 v13, v13, v17\n"
      "v_mfma_f32_16x16x32_bf16 v[56:59], v[220:223], a[212:215], v[56:59]\n"
      "ds_write_addtid_b32 v13 offset:28928\n"
      "v_fma_f32 v28, s47, v28, v76\n"
      "v_mfma_f32_16x16x32_bf16 v[60:63], v[212:215], a[212:215], v[60:63]\n"
      "s_waitcnt lgkmcnt(4)\n"
      "v_fma_f32 v29, s47, v29, v77\n"
      "v_cvt_pk_bf16_f32 v14, v14, v18\n"
      "v_mfma_f32_16x16x32_bf16 v[64:67], v[224:227], a[192:195], v[64:67]\n"
      "s_nop 0\n"
      "ds_read_b128 a[208:211], v1 offset:20480\n"
      "ds_write_addtid_b32 v14 offset:33024\n"
      "v_fma_f32 v30, s47, v30, v78\n"
      "v_mfma_f32_16x16x32_bf16 v[68:71], v[216:219], a[192:195], v[68:71]\n"
      "s_nop 0\n"
      "v_fma_f32 v31, s47, v31, v79\n"
      "v_cvt_pk_bf16_f32 v15, v15, v19\n"
      "v_mfma_f32_16x16x32_bf16 v[72:75], v[208:211], a[192:195], v[72:75]\n"
      "s_waitcnt lgkmcnt(2)\n"
      "ds_write_addtid_b32 v15 offset:37120\n"
      "v_cvt_pk_bf16_f32 v40, v40, v41\n"
      "v_mfma_f32_4x4x4_16b_bf16 v[20:23], a[216:217], a[224:225], 0\n"
      "v_cvt_pk_bf16_f32 v42, v42, v43\n"
      "v_mfma_f32_4x4x4_16b_bf16 v[24:27], a[216:217], a[226:227], 0\n"
      "ds_write_addtid_b32 v40 offset:12288\n"
      "ds_write_addtid_b32 v42 offset:12800\n"
      "v_cvt_pk_bf16_f32 v28, v28, v29\n"
      "v_mfma_f32_16x16x32_bf16 v[52:55], v[204:207], a[196:199], v[52:55]\n"
      "ds_read_b128 a[212:215], v1 offset:16448\n"
      "ds_write_addtid_b32 v28 offset:14336\n"
      "v_cvt_pk_bf16_f32 v20, v20, v24\n"
      "v_mfma_f32_16x16x32_bf16 v[56:59], v[196:199], a[196:199], v[56:59]\n"
      "ds_write_addtid_b32 v20 offset:25344\n"
      "v_cvt_pk_bf16_f32 v30, v30, v31\n"
      "v_mfma_f32_16x16x32_bf16 v[60:63], v[188:191], a[196:199], v[60:63]\n"
      "buffer_store_dwordx4 a[240:243], v11, s[8:11], s59 offen offset:256 sc1\n"
      "v_mfma_f32_16x16x32_bf16 v[64:67], v[200:203], a[200:203], v[64:67]\n"
      "ds_read_b128 a[192:195], v1 offset:20544\n"
      "ds_write_addtid_b32 v30 offset:14848\n"
      "v_mfma_f32_16x16x32_bf16 v[68:71], v[192:195], a[200:203], v[68:71]\n"
      "ds_read_b64_tr_b16 a[228:229], v7 offset:34816\n"
      "ds_read_b64_tr_b16 a[230:231], v7 offset:34824\n"
      "v_sub_f32 v32, v32, v44\n"
      "v_mfma_f32_16x16x32_bf16 v[72:75], v[184:187], a[200:203], v[72:75]\n"
      "v_sub_f32 v33, v33, v45\n"
      "v_cvt_pk_bf16_f32 v21, v21, v25\n"
      "v_mfma_f32_16x16x32_bf16 v[52:55], v[180:183], a[204:207], v[52:55]\n"
      "ds_read_b128 a[196:199], v1 offset:16512\n"
      "ds_write_addtid_b32 v21 offset:29440\n"
      "v_mfma_f32_16x16x32_bf16 v[56:59], v[172:175], a[204:207], v[56:59]\n"
      "v_sub_f32 v34, v34, v46\n"
      "v_sub_f32 v35, v35, v47\n"
      "v_mfma_f32_16x16x32_bf16 v[60:63], v[164:167], a[204:207], v[60:63]\n"
      "s_waitcnt lgkmcnt(4)\n"
      "v_fmac_f32 v32, s46, v104\n"
      "v_cvt_pk_bf16_f32 v22, v22, v26\n"
      "v_mfma_f32_16x16x32_bf16 v[64:67], v[176:179], a[208:211], v[64:67]\n"
      "ds_read_b128 a[200:203], v1 offset:20608\n"
      "ds_write_addtid_b32 v22 offset:33536\n"
      "v_fmac_f32 v33, s46, v105\n"
      "v_mfma_f32_16x16x32_bf16 v[68:71], v[168:171], a[208:211], v[68:71]\n"
      "s_waitcnt lgkmcnt(4)\n"
      "v_fmac_f32_e64 v34, s46, v106\n"
      "v_cvt_pk_bf16_f32 v23, v23, v27\n"
      "v_mfma_f32_4x4x4_16b_bf16 v[12:15], a[216:217], a[228:229], 0\n"
      "v_fmac_f32 v35, s46, v107\n"
      "v_mfma_f32_4x4x4_16b_bf16 v[16:19], a[216:217], a[230:231], 0\n"
      "ds_write_addtid_b32 v23 offset:37632\n"
      "v_fmac_f32 v44, s45, v32\n"
      "v_mfma_f32_16x16x32_bf16 v[72:75], v[160:163], a[208:211], v[72:75]\n"
      "v_fmac_f32 v45, s45, v33\n"
      "v_fmac_f32 v46, s45, v34\n"
      "v_cvt_pk_bf16_f32 v12, v12, v16\n"
      "v_mfma_f32_16x16x32_bf16 v[52:55], v[156:159], a[212:215], v[52:55]\n"
      "ds_read_b128 a[204:207], v2 offset:16384\n"
      "ds_write_addtid_b32 v12 offset:25856\n"
      "v_mfma_f32_16x16x32_bf16 v[56:59], v[148:151], a[212:215], v[56:59]\n"
      "v_fmac_f32 v47, s45, v35\n"
      "v_cvt_pk_bf16_f32 v13, v13, v17\n"
      "v_mfma_f32_16x16x32_bf16 v[60:63], v[140:143], a[212:215], v[60:63]\n"
      "s_nop 0\n"
      "ds_read_b64_tr_b16 a[232:233], v7 offset:35328\n"
      "ds_write_addtid_b32 v13 offset:29952\n"
      "v_fma_f32 v32, s47, v32, v80\n"
      "v_mfma_f32_16x16x32_bf16 v[64:67], v[152:155], a[192:195], v[64:67]\n"
      "ds_read_b128 a[208:211], v2 offset:20480\n"
      "ds_read_b64_tr_b16 a[234:235], v7 offset:35336\n"
      "v_mfma_f32_16x16x32_bf16 v[68:71], v[144:147], a[192:195], v[68:71]\n"
      "v_fma_f32 v33, s47, v33, v81\n"
      "v_fma_f32 v34, s47, v34, v82\n"
      "v_mfma_f32_16x16x32_bf16 v[72:75], v[136:139], a[192:195], v[72:75]\n"
      "s_waitcnt lgkmcnt(4)\n"
      "v_fma_f32 v35, s47, v35, v83\n"
      "v_cvt_pk_bf16_f32 v14, v14, v18\n"
      "v_mfma_f32_16x16x32_bf16 v[52:55], v[132:135], a[196:199], v[52:55]\n"
      "ds_read_b128 a[212:215], v2 offset:16448\n"
      "ds_write_addtid_b32 v14 offset:34048\n"
      "v_mfma_f32_16x16x32_bf16 v[56:59], v[124:127], a[196:199], v[56:59]\n"
      "s_nop 0\n"
      "v_cvt_pk_bf16_f32 v44, v44, v45\n"
      "v_cvt_pk_bf16_f32 v46, v46, v47\n"
      "v_mfma_f32_16x16x32_bf16 v[60:63], a[184:187], a[196:199], v[60:63]\n"
      "s_nop 0\n"
      "ds_write_addtid_b32 v44 offset:20484\n"
      "ds_write_addtid_b32 v46 offset:20996\n"
      "v_cvt_pk_bf16_f32 v32, v32, v33\n"
      "v_mfma_f32_16x16x32_bf16 v[64:67], v[128:131], a[200:203], v[64:67]\n"
      "s_waitcnt lgkmcnt(4)\n"
      "ds_read_b128 a[192:195], v2 offset:20544\n"
      "ds_write_addtid_b32 v32 offset:22532\n"
      "v_mfma_f32_4x4x4_16b_bf16 v[20:23], a[216:217], a[232:233], 0\n"
      "v_cvt_pk_bf16_f32 v34, v34, v35\n"
      "v_cvt_pk_bf16_f32 v15, v15, v19\n"
      "v_mfma_f32_4x4x4_16b_bf16 v[24:27], a[216:217], a[234:235], 0\n"
      "ds_write_addtid_b32 v34 offset:23044\n"
      "ds_write_addtid_b32 v15 offset:38144\n"
      "v_sub_f32_e64 v36, v36, v48\n"
      "v_mfma_f32_16x16x32_bf16 v[68:71], a[188:191], a[200:203], v[68:71]\n"
      "v_sub_f32 v37, v37, v49\n"
      "v_cvt_pk_bf16_f32 v20, v20, v24\n"
      "v_mfma_f32_16x16x32_bf16 v[72:75], a[180:183], a[200:203], v[72:75]\n"
      "ds_write_addtid_b32 v20 offset:26368\n"
      "v_sub_f32 v38, v38, v50\n"
      "v_mfma_f32_16x16x32_bf16 v[52:55], a[176:179], a[204:207], v[52:55]\n"
      "ds_read_b128 a[196:199], v2 offset:16512\n"
      "v_sub_f32_e64 v39, v39, v51\n"
      "v_mfma_f32_16x16x32_bf16 v[56:59], a[168:171], a[204:207], v[56:59]\n"
      "ds_read_b64_tr_b16 a[220:221], v7 offset:35840\n"
      "ds_read_b64_tr_b16 a[222:223], v7 offset:35848\n"
      "v_fmac_f32 v36, s46, v108\n"
      "v_mfma_f32_16x16x32_bf16 v[60:63], a[160:163], a[204:207], v[60:63]\n"
      "v_fmac_f32 v37, s46, v109\n"
      "v_cvt_pk_bf16_f32 v21, v21, v25\n"
      "v_mfma_f32_16x16x32_bf16 v[64:67], a[172:175], a[208:211], v[64:67]\n"
      "ds_read_b128 a[200:203], v2 offset:20608\n"
      "ds_write_addtid_b32 v21 offset:30464\n"
      "v_mfma_f32_16x16x32_bf16 v[68:71], a[164:167], a[208:211], v[68:71]\n"
      "v_fmac_f32 v38, s46, v110\n"
      "v_fmac_f32 v39, s46, v111\n"
      "v_mfma_f32_16x16x32_bf16 v[72:75], a[156:159], a[208:211], v[72:75]\n"
      "s_waitcnt lgkmcnt(3)\n"
      "v_fmac_f32 v48, s45, v36\n"
      "v_cvt_pk_bf16_f32 v22, v22, v26\n"
      "v_mfma_f32_16x16x32_bf16 v[52:55], a[152:155], a[212:215], v[52:55]\n"
      "ds_read_b128 a[204:207], v3 offset:50688\n"
      "ds_write_addtid_b32 v22 offset:34560\n"
      "v_fmac_f32 v49, s45, v37\n"
      "v_mfma_f32_16x16x32_bf16 v[56:59], a[144:147], a[212:215], v[56:59]\n"
      "v_fmac_f32 v50, s45, v38\n"
      "v_cvt_pk_bf16_f32 v23, v23, v27\n"
      "v_mfma_f32_16x16x32_bf16 v[60:63], a[136:139], a[212:215], v[60:63]\n"
      "s_waitcnt lgkmcnt(2)\n"
      "ds_write_addtid_b32 v23 offset:38656\n"
      "v_fmac_f32 v51, s45, v39\n"
      "v_mfma_f32_4x4x4_16b_bf16 v[12:15], a[216:217], a[220:221], 0\n"
      "v_fma_f32 v36, s47, v36, v84\n"
      "v_mfma_f32_4x4x4_16b_bf16 v[16:19], a[216:217], a[222:223], 0\n"
      "v_fma_f32 v37, s47, v37, v85\n"
      "v_fma_f32 v38, s47, v38, v86\n"
      "v_mfma_f32_16x16x32_bf16 v[64:67], a[148:151], a[192:195], v[64:67]\n"
      "ds_read_b128 a[208:211], v3 offset:50752\n"
      "v_fma_f32 v39, s47, v39, v87\n"
      "v_cvt_pk_bf16_f32 v12, v12, v16\n"
      "v_mfma_f32_16x16x32_bf16 v[68:71], a[140:143], a[192:195], v[68:71]\n"
      "ds_write_addtid_b32 v12 offset:26880\n"
      "v_cvt_pk_bf16_f32 v13, v13, v17\n"
      "v_mfma_f32_16x16x32_bf16 v[72:75], a[132:135], a[192:195], v[72:75]\n"
      "ds_write_addtid_b32 v13 offset:30976\n"
      "v_cvt_pk_bf16_f32 v48, v48, v49\n"
      "v_mfma_f32_16x16x32_bf16 v[52:55], a[128:131], a[196:199], v[52:55]\n"
      "ds_read_b128 a[212:215], v3 offset:50816\n"
      "ds_write_addtid_b32 v48 offset:16384\n"
      "v_mfma_f32_16x16x32_bf16 v[56:59], a[120:123], a[196:199], v[56:59]\n"
      "ds_read_b64_tr_b16 a[224:225], v7 offset:36352\n"
      "ds_read_b64_tr_b16 a[226:227], v7 offset:36360\n"
      "v_cvt_pk_bf16_f32 v14, v14, v18\n"
      "v_mfma_f32_16x16x32_bf16 v[60:63], a[112:115], a[196:199], v[60:63]\n"
      "ds_write_addtid_b32 v14 offset:35072\n"
      "v_cvt_pk_bf16_f32 v50, v50, v51\n"
      "v_mfma_f32_16x16x32_bf16 v[64:67], a[124:127], a[200:203], v[64:67]\n"
      "ds_read_b128 a[192:195], v4 offset:50688\n"
      "ds_write_addtid_b32 v50 offset:16896\n"
      "v_mfma_f32_16x16x32_bf16 v[68:71], a[116:119], a[200:203], v[68:71]\n"
      "s_nop 0\n"
      "v_cvt_pk_bf16_f32 v36, v36, v37\n"
      "v_cvt_pk_bf16_f32 v15, v15, v19\n"
      "v_mfma_f32_16x16x32_bf16 v[72:75], a[108:111], a[200:203], v[72:75]\n"
      "s_waitcnt lgkmcnt(5)\n"
      "ds_write_addtid_b32 v36 offset:18432\n"
      "ds_write_addtid_b32 v15 offset:39168\n"
      "v_sub_f32 v52, v64, v52\n"
      "v_sub_f32 v53, v65, v53\n"
      "v_mfma_f32_16x16x32_bf16 v[88:91], a[0:3], a[204:207], 0\n"
      "s_nop 0\n"
      "ds_read_b128 a[196:199], v4 offset:50752\n"
      "ds_write_addtid_b32 v52 offset:6144\n"
      "v_mfma_f32_16x16x32_bf16 v[92:95], a[4:7], a[204:207], 0\n"
      "s_waitcnt lgkmcnt(5)\n"
      "ds_write_addtid_b32 v53 offset:6656\n"
      "v_sub_f32 v54, v66, v54\n"
      "v_mfma_f32_4x4x4_16b_bf16 v[20:23], a[216:217], a[224:225], 0\n"
      "v_sub_f32 v55, v67, v55\n"
      "v_cvt_pk_bf16_f32 v38, v38, v39\n"
      "v_mfma_f32_4x4x4_16b_bf16 v[24:27], a[216:217], a[226:227], 0\n"
      "ds_write_addtid_b32 v38 offset:18944\n"
      "ds_write_addtid_b32 v54 offset:7168\n"
      "v_mfma_f32_16x16x32_bf16 v[96:99], a[8:11], a[204:207], 0\n"
      "ds_write_addtid_b32 v55 offset:7680\n"
      "v_sub_f32 v56, v68, v56\n"
      "v_mfma_f32_16x16x32_bf16 v[88:91], a[12:15], a[208:211], v[88:91]\n"
      "ds_read_b128 a[200:203], v4 offset:50816\n"
      "ds_write_addtid_b32 v56 offset:8192\n"
      "v_sub_f32 v57, v69, v57\n"
      "v_mfma_f32_16x16x32_bf16 v[92:95], a[16:19], a[208:211], v[92:95]\n"
      "ds_write_addtid_b32 v57 offset:8704\n"
      "v_sub_f32_e64 v58, v70, v58\n"
      "v_mfma_f32_16x16x32_bf16 v[96:99], a[20:23], a[208:211], v[96:99]\n"
      "ds_write_addtid_b32 v58 offset:9216\n"
      "v_cvt_pk_bf16_f32 v20, v20, v24\n"
      "v_mfma_f32_16x16x32_bf16 v[88:91], a[24:27], a[212:215], v[88:91]\n"
      "ds_read_b128 a[204:207], v5 offset:50688\n"
      "ds_write_addtid_b32 v20 offset:27392\n"
      "v_mfma_f32_16x16x32_bf16 v[92:95], a[28:31], a[212:215], v[92:95]\n"
      "v_sub_f32 v59, v71, v59\n"
      "v_sub_f32 v60, v72, v60\n"
      "v_cvt_pk_bf16_f32 v21, v21, v25\n"
      "v_mfma_f32_16x16x32_bf16 v[96:99], a[32:35], a[212:215], v[96:99]\n"
      "ds_write_addtid_b32 v21 offset:31488\n"
      "ds_write_addtid_b32 v59 offset:9728\n"
      "v_mfma_f32_16x16x32_bf16 v[88:91], a[36:39], a[192:195], v[88:91]\n"
      "ds_read_b128 a[208:211], v5 offset:50752\n"
      "ds_write_addtid_b32 v60 offset:10240\n"
      "v_mfma_f32_16x16x32_bf16 v[92:95], a[40:43], a[192:195], v[92:95]\n"
      "v_sub_f32 v61, v73, v61\n"
      "v_sub_f32 v62, v74, v62\n"
      "v_cvt_pk_bf16_f32 v22, v22, v26\n"
      "v_mfma_f32_16x16x32_bf16 v[96:99], a[44:47], a[192:195], v[96:99]\n"
      "s_waitcnt lgkmcnt(4)\n"
      "ds_write_addtid_b32 v22 offset:35584\n"
      "ds_write_addtid_b32 v61 offset:10752\n"
      "v_sub_f32 v63, v75, v63\n"
      "v_mfma_f32_16x16x32_bf16 v[88:91], a[48:51], a[196:199], v[88:91]\n"
      "ds_read_b128 a[212:215], v5 offset:50816\n"
      "ds_write_addtid_b32 v62 offset:11264\n"
      "v_mfma_f32_16x16x32_bf16 v[92:95], a[52:55], a[196:199], v[92:95]\n"
      "ds_write_addtid_b32 v63 offset:11776\n"
      "v_cvt_pk_bf16_f32 v23, v23, v27\n"
      "v_mfma_f32_16x16x32_bf16 v[96:99], a[56:59], a[196:199], v[96:99]\n"
      "ds_write_addtid_b32 v23 offset:39680\n"
      "v_fma_f32 v40, 0.5, v52, -v64\n"
      "v_mfma_f32_16x16x32_bf16 v[88:91], a[60:63], a[200:203], v[88:91]\n"
      "v_fma_f32 v41, 0.5, v53, -v65\n"
      "v_fma_f32 v42, 0.5, v54, -v66\n"
      "v_mfma_f32_16x16x32_bf16 v[92:95], a[64:67], a[200:203], v[92:95]\n"
      "v_fma_f32 v43, 0.5, v55, -v67\n"
      "v_fma_f32 v44, 0.5, v56, -v68\n"
      "v_mfma_f32_16x16x32_bf16 v[96:99], a[68:71], a[200:203], v[96:99]\n"
      "s_waitcnt lgkmcnt(0)\n"
      "v_fma_f32 v45, 0.5, v57, -v69\n"
      "s_barrier\n"
      "v_mfma_f32_16x16x32_bf16 v[88:91], a[72:75], a[204:207], v[88:91]\n"
      "s_add_u32 s59, s62, 5701632\n"
      "ds_read_b128 a[192:195], v0 offset:0\n"
      "ds_read2st64_b32 v[28:29], v6 offset0:24 offset1:26\n"
      "v_fma_f32 v46, 0.5, v58, -v70\n"
      "v_mfma_f32_16x16x32_bf16 v[92:95], a[76:79], a[204:207], v[92:95]\n"
      "ds_read_b128 a[196:199], v0 offset:4096\n"
      "ds_read2st64_b32 v[30:31], v6 offset0:28 offset1:30\n"
      "v_mfma_f32_16x16x32_bf16 v[96:99], a[80:83], a[204:207], v[96:99]\n"
      "ds_read_b64_tr_b16 a[220:221], v7 offset:50688\n"
      "v_fma_f32 v47, 0.5, v59, -v71\n"
      "v_mfma_f32_16x16x32_bf16 v[88:91], a[84:87], a[208:211], v[88:91]\n"
      "ds_read_b128 a[200:203], v0 offset:64\n"
      "v_fma_f32 v48, 0.5, v60, -v72\n"
      "v_mfma_f32_16x16x32_bf16 v[92:95], a[88:91], a[208:211], v[92:95]\n"
      "ds_read_b64_tr_b16 a[222:223], v7 offset:50696\n"
      "ds_read2st64_b32 v[32:33], v6 offset0:32 offset1:34\n"
      "v_fma_f32 v49, 0.5, v61, -v73\n"
      "v_mfma_f32_16x16x32_bf16 v[96:99], a[92:95], a[208:211], v[96:99]\n"
      "ds_read2st64_b32 v[34:35], v6 offset0:36 offset1:38\n"
      "v_fma_f32 v50, 0.5, v62, -v74\n"
      "v_mfma_f32_16x16x32_bf16 v[88:91], a[96:99], a[212:215], v[88:91]\n"
      "ds_read_b128 a[204:207], v0 offset:4160\n"
      "v_fma_f32 v51, 0.5, v63, -v75\n"
      "v_mfma_f32_16x16x32_bf16 v[92:95], a[100:103], a[212:215], v[92:95]\n"
      "ds_read2st64_b32 v[36:37], v6 offset0:40 offset1:42\n"
      "ds_read2st64_b32 v[38:39], v6 offset0:44 offset1:46\n"
      "v_add_u32 v3, 67840, v3\n"
      "v_mfma_f32_16x16x32_bf16 v[96:99], a[104:107], a[212:215], v[96:99]\n"
      "s_waitcnt lgkmcnt(7)\n"
      "v_sub_f32 v28, v28, v40\n"
      "v_sub_f32 v29, v29, v41\n"
      "v_mfma_f32_16x16x32_bf16 v[52:55], v[252:255], a[192:195], v[76:79]\n"
      "s_waitcnt lgkmcnt(5)\n"
      "ds_read_b128 a[208:211], v0 offset:128\n"
      "ds_read2st64_b32 a[240:241], v8 offset0:0 offset1:2\n"
      "v_mfma_f32_4x4x4_16b_bf16 v[12:15], a[216:217], a[220:221], 0\n"
      "ds_read2st64_b32 a[242:243], v9 offset0:0 offset1:2\n"
      "v_sub_f32 v30, v30, v42\n"
      "v_mfma_f32_4x4x4_16b_bf16 v[16:19], a[216:217], a[222:223], 0\n"
      "v_sub_f32 v31, v31, v43\n"
      "v_fmac_f32 v28, s46, v112\n"
      "v_mfma_f32_16x16x32_bf16 v[56:59], v[244:247], a[192:195], v[80:83]\n"
      "v_fmac_f32 v29, s46, v113\n"
      "v_fmac_f32 v30, s46, v114\n"
      "v_mfma_f32_16x16x32_bf16 v[60:63], v[236:239], a[192:195], v[84:87]\n"
      "v_fmac_f32 v31, s46, v115\n"
      "v_add_u32 v4, 67840, v4\n"
      "v_cvt_pk_bf16_f32 v12, v12, v16\n"
      "v_mfma_f32_16x16x32_bf16 v[64:67], v[248:251], a[196:199], v[76:79]\n"
      "ds_read_b128 a[212:215], v0 offset:4224\n"
      "ds_write_addtid_b32 v12 offset:41216\n"
      "v_mfma_f32_16x16x32_bf16 v[68:71], v[240:243], a[196:199], v[80:83]\n"
      "ds_read_b64_tr_b16 a[224:225], v7 offset:51200\n"
      "ds_read_b64_tr_b16 a[226:227], v7 offset:51208\n"
      "v_mfma_f32_16x16x32_bf16 v[72:75], v[232:235], a[196:199], v[84:87]\n"
      "v_fmac_f32 v40, s45, v28\n"
      "v_fmac_f32 v41, s45, v29\n"
      "v_cvt_pk_bf16_f32 v13, v13, v17\n"
      "v_mfma_f32_16x16x32_bf16 v[52:55], v[228:231], a[200:203], v[52:55]\n"
      "ds_read_b128 a[192:195], v1 offset:0\n"
      "ds_write_addtid_b32 v13 offset:45312\n"
      "v_mfma_f32_16x16x32_bf16 v[56:59], v[220:223], a[200:203], v[56:59]\n"
      "v_fmac_f32 v42, s45, v30\n"
      "v_add_u32 v5, 67840, v5\n"
      "v_mfma_f32_16x16x32_bf16 v[60:63], v[212:215], a[200:203], v[60:63]\n"
      "s_waitcnt lgkmcnt(3)\n"
      "v_fmac_f32 v43, s45, v31\n"
      "v_fma_f32 v28, s47, v28, v88\n"
      "v_mfma_f32_16x16x32_bf16 v[64:67], v[224:227], a[204:207], v[64:67]\n"
      "s_nop 0\n"
      "ds_read_b128 a[196:199], v1 offset:4096\n"
      "v_cvt_pk_bf16_f32 v14, v14, v18\n"
      "v_mfma_f32_16x16x32_bf16 v[68:71], v[216:219], a[204:207], v[68:71]\n"
      "s_nop 0\n"
      "ds_write_addtid_b32 v14 offset:49408\n"
      "v_fma_f32 v29, s47, v29, v89\n"
      "v_mfma_f32_16x16x32_bf16 v[72:75], v[208:211], a[204:207], v[72:75]\n"
      "s_waitcnt lgkmcnt(2)\n"
      "v_fma_f32 v30, s47, v30, v90\n"
      "v_cvt_pk_bf16_f32 v15, v15, v19\n"
      "v_mfma_f32_4x4x4_16b_bf16 v[20:23], a[216:217], a[224:225], 0\n"
      "v_fma_f32 v31, s47, v31, v91\n"
      "v_mfma_f32_4x4x4_16b_bf16 v[24:27], a[216:217], a[226:227], 0\n"
      "ds_write_addtid_b32 v15 offset:53504\n"
      "v_cvt_pk_bf16_f32 v40, v40, v41\n"
      "v_mfma_f32_16x16x32_bf16 v[52:55], v[204:207], a[208:211], v[52:55]\n"
      "ds_read_b128 a[200:203], v1 offset:64\n"
      "ds_write_addtid_b32 v40 offset:13312\n"
      "v_cvt_pk_bf16_f32 v20, v20, v24\n"
      "v_mfma_f32_16x16x32_bf16 v[56:59], v[196:199], a[208:211], v[56:59]\n"
      "ds_write_addtid_b32 v20 offset:41728\n"
      "v_cvt_pk_bf16_f32 v42, v42, v43\n"
      "v_mfma_f32_16x16x32_bf16 v[60:63], v[188:191], a[208:211], v[60:63]\n"
      "buffer_store_dwordx4 a[240:243], v11, s[8:11], s59 offen offset:256 sc1\n"
      "ds_write_addtid_b32 v42 offset:13824\n"
      "v_mfma_f32_16x16x32_bf16 v[64:67], v[200:203], a[212:215], v[64:67]\n"
      "ds_read_b128 a[204:207], v1 offset:4160\n"
      "ds_read_b64_tr_b16 a[228:229], v7 offset:51712\n"
      "v_cvt_pk_bf16_f32 v28, v28, v29\n"
      "v_mfma_f32_16x16x32_bf16 v[68:71], v[192:195], a[212:215], v[68:71]\n"
      "ds_write_addtid_b32 v28 offset:15360\n"
      "ds_read_b64_tr_b16 a[230:231], v7 offset:51720\n"
      "v_cvt_pk_bf16_f32 v30, v30, v31\n"
      "v_mfma_f32_16x16x32_bf16 v[72:75], v[184:187], a[212:215], v[72:75]\n"
      "ds_write_addtid_b32 v30 offset:15872\n"
      "v_sub_f32 v32, v32, v44\n"
      "v_mfma_f32_16x16x32_bf16 v[52:55], v[180:183], a[192:195], v[52:55]\n"
      "ds_read_b128 a[208:211], v1 offset:128\n"
      "v_cvt_pk_bf16_f32 v21, v21, v25\n"
      "v_mfma_f32_16x16x32_bf16 v[56:59], v[172:175], a[192:195], v[56:59]\n"
      "ds_write_addtid_b32 v21 offset:45824\n"
      "v_sub_f32 v33, v33, v45\n"
      "v_sub_f32_e64 v34, v34, v46\n"
      "v_mfma_f32_16x16x32_bf16 v[60:63], v[164:167], a[192:195], v[60:63]\n"
      "s_waitcnt lgkmcnt(4)\n"
      "v_sub_f32 v35, v35, v47\n"
      "v_cvt_pk_bf16_f32 v22, v22, v26\n"
      "v_mfma_f32_16x16x32_bf16 v[64:67], v[176:179], a[196:199], v[64:67]\n"
      "ds_read_b128 a[212:215], v1 offset:4224\n"
      "ds_write_addtid_b32 v22 offset:49920\n"
      "v_mfma_f32_16x16x32_bf16 v[68:71], v[168:171], a[196:199], v[68:71]\n"
      "s_waitcnt lgkmcnt(4)\n"
      "v_fmac_f32 v32, s46, v116\n"
      "v_fmac_f32 v33, s46, v117\n"
      "v_mfma_f32_4x4x4_16b_bf16 v[12:15], a[216:217], a[228:229], 0\n"
      "v_cvt_pk_bf16_f32 v23, v23, v27\n"
      "v_mfma_f32_4x4x4_16b_bf16 v[16:19], a[216:217], a[230:231], 0\n"
      "ds_write_addtid_b32 v23 offset:54016\n"
      "v_fmac_f32 v34, s46, v118\n"
      "v_fmac_f32 v35, s46, v119\n"
      "v_mfma_f32_16x16x32_bf16 v[72:75], v[160:163], a[196:199], v[72:75]\n"
      "v_fmac_f32 v44, s45, v32\n"
      "v_cvt_pk_bf16_f32 v12, v12, v16\n"
      "v_mfma_f32_16x16x32_bf16 v[52:55], v[156:159], a[200:203], v[52:55]\n"
      "ds_read_b128 a[192:195], v2 offset:0\n"
      "ds_write_addtid_b32 v12 offset:42240\n"
      "v_mfma_f32_16x16x32_bf16 v[56:59], v[148:151], a[200:203], v[56:59]\n"
      "v_fmac_f32 v45, s45, v33\n"
      "v_fmac_f32 v46, s45, v34\n"
      "v_mfma_f32_16x16x32_bf16 v[60:63], v[140:143], a[200:203], v[60:63]\n"
      "ds_read_b64_tr_b16 a[232:233], v7 offset:52224\n"
      "ds_read_b64_tr_b16 a[234:235], v7 offset:52232\n"
      "v_fmac_f32 v47, s45, v35\n"
      "v_cvt_pk_bf16_f32 v13, v13, v17\n"
      "v_mfma_f32_16x16x32_bf16 v[64:67], v[152:155], a[204:207], v[64:67]\n"
      "ds_read_b128 a[196:199], v2 offset:4096\n"
      "ds_write_addtid_b32 v13 offset:46336\n"
      "v_mfma_f32_16x16x32_bf16 v[68:71], v[144:147], a[204:207], v[68:71]\n"
      "s_nop 0\n"
      "v_fma_f32 v32, s47, v32, v92\n"
      "v_fma_f32 v33, s47, v33, v93\n"
      "v_mfma_f32_16x16x32_bf16 v[72:75], v[136:139], a[204:207], v[72:75]\n"
      "s_waitcnt lgkmcnt(4)\n"
      "v_cvt_pk_bf16_f32 v14, v14, v18\n"
      "v_fma_f32 v34, s47, v34, v94\n"
      "v_mfma_f32_16x16x32_bf16 v[52:55], v[132:135], a[208:211], v[52:55]\n"
      "ds_read_b128 a[200:203], v2 offset:64\n"
      "ds_write_addtid_b32 v14 offset:50432\n"
      "v_mfma_f32_16x16x32_bf16 v[56:59], v[124:127], a[208:211], v[56:59]\n"
      "s_nop 0\n"
      "v_fma_f32 v35, s47, v35, v95\n"
      "v_cvt_pk_bf16_f32 v44, v44, v45\n"
      "v_mfma_f32_16x16x32_bf16 v[60:63], a[184:187], a[208:211], v[60:63]\n"
      "s_nop 0\n"
      "ds_write_addtid_b32 v44 offset:21508\n"
      "v_cvt_pk_bf16_f32 v46, v46, v47\n"
      "v_mfma_f32_16x16x32_bf16 v[64:67], v[128:131], a[212:215], v[64:67]\n"
      "s_waitcnt lgkmcnt(3)\n"
      "ds_read_b128 a[204:207], v2 offset:4160\n"
      "ds_write_addtid_b32 v46 offset:22020\n"
      "v_mfma_f32_4x4x4_16b_bf16 v[20:23], a[216:217], a[232:233], 0\n"
      "v_cvt_pk_bf16_f32 v32, v32, v33\n"
      "v_cvt_pk_bf16_f32 v15, v15, v19\n"
      "v_mfma_f32_4x4x4_16b_bf16 v[24:27], a[216:217], a[234:235], 0\n"
      "ds_write_addtid_b32 v32 offset:23556\n"
      "ds_write_addtid_b32 v15 offset:54528\n"
      "v_cvt_pk_bf16_f32 v34, v34, v35\n"
      "v_mfma_f32_16x16x32_bf16 v[68:71], a[188:191], a[212:215], v[68:71]\n"
      "ds_write_addtid_b32 v34 offset:24068\n"
      "v_sub_f32 v36, v36, v48\n"
      "v_cvt_pk_bf16_f32 v20, v20, v24\n"
      "v_mfma_f32_16x16x32_bf16 v[72:75], a[180:183], a[212:215], v[72:75]\n"
      "ds_write_addtid_b32 v20 offset:42752\n"
      "v_sub_f32 v37, v37, v49\n"
      "v_mfma_f32_16x16x32_bf16 v[52:55], a[176:179], a[192:195], v[52:55]\n"
      "ds_read_b128 a[208:211], v2 offset:128\n"
      "v_sub_f32 v38, v38, v50\n"
      "v_mfma_f32_16x16x32_bf16 v[56:59], a[168:171], a[192:195], v[56:59]\n"
      "ds_read_b64_tr_b16 a[220:221], v7 offset:52736\n"
      "ds_read_b64_tr_b16 a[222:223], v7 offset:52744\n"
      "v_sub_f32 v39, v39, v51\n"
      "v_mfma_f32_16x16x32_bf16 v[60:63], a[160:163], a[192:195], v[60:63]\n"
      "v_fmac_f32 v36, s46, v120\n"
      "v_cvt_pk_bf16_f32 v21, v21, v25\n"
      "v_mfma_f32_16x16x32_bf16 v[64:67], a[172:175], a[196:199], v[64:67]\n"
      "s_nop 0\n"
      "ds_read_b128 a[212:215], v2 offset:4224\n"
      "ds_write_addtid_b32 v21 offset:46848\n"
      "v_mfma_f32_16x16x32_bf16 v[68:71], a[164:167], a[196:199], v[68:71]\n"
      "v_fmac_f32 v37, s46, v121\n"
      "v_fmac_f32 v38, s46, v122\n"
      "v_mfma_f32_16x16x32_bf16 v[72:75], a[156:159], a[196:199], v[72:75]\n"
      "s_waitcnt lgkmcnt(3)\n"
      "v_fmac_f32 v39, s46, v123\n"
      "v_cvt_pk_bf16_f32 v22, v22, v26\n"
      "v_mfma_f32_16x16x32_bf16 v[52:55], a[152:155], a[200:203], v[52:55]\n"
      "ds_read_b128 a[192:195], v3 offset:0\n"
      "ds_write_addtid_b32 v22 offset:50944\n"
      "v_fmac_f32 v48, s45, v36\n"
      "v_mfma_f32_16x16x32_bf16 v[56:59], a[144:147], a[200:203], v[56:59]\n"
      "v_fmac_f32 v49, s45, v37\n"
      "v_cvt_pk_bf16_f32 v23, v23, v27\n"
      "v_mfma_f32_16x16x32_bf16 v[60:63], a[136:139], a[200:203], v[60:63]\n"
      "s_waitcnt lgkmcnt(2)\n"
      "ds_write_addtid_b32 v23 offset:55040\n"
      "v_fmac_f32 v50, s45, v38\n"
      "v_mfma_f32_4x4x4_16b_bf16 v[12:15], a[216:217], a[220:221], 0\n"
      "v_fmac_f32 v51, s45, v39\n"
      "v_mfma_f32_4x4x4_16b_bf16 v[16:19], a[216:217], a[222:223], 0\n"
      "v_fma_f32 v36, s47, v36, v96\n"
      "v_fma_f32 v37, s47, v37, v97\n"
      "v_mfma_f32_16x16x32_bf16 v[64:67], a[148:151], a[204:207], v[64:67]\n"
      "s_nop 0\n"
      "ds_read_b128 a[196:199], v3 offset:64\n"
      "v_fma_f32 v38, s47, v38, v98\n"
      "v_cvt_pk_bf16_f32 v12, v12, v16\n"
      "v_mfma_f32_16x16x32_bf16 v[68:71], a[140:143], a[204:207], v[68:71]\n"
      "ds_write_addtid_b32 v12 offset:43264\n"
      "v_cvt_pk_bf16_f32 v13, v13, v17\n"
      "v_mfma_f32_16x16x32_bf16 v[72:75], a[132:135], a[204:207], v[72:75]\n"
      "ds_write_addtid_b32 v13 offset:47360\n"
      "v_fma_f32 v39, s47, v39, v99\n"
      "v_mfma_f32_16x16x32_bf16 v[52:55], a[128:131], a[208:211], v[52:55]\n"
      "ds_read_b128 a[200:203], v3 offset:128\n"
      "ds_read_b64_tr_b16 a[224:225], v7 offset:53248\n"
      "v_mfma_f32_16x16x32_bf16 v[56:59], a[120:123], a[208:211], v[56:59]\n"
      "ds_read_b64_tr_b16 a[226:227], v7 offset:53256\n"
      "v_cvt_pk_bf16_f32 v48, v48, v49\n"
      "v_cvt_pk_bf16_f32 v14, v14, v18\n"
      "v_mfma_f32_16x16x32_bf16 v[60:63], a[112:115], a[208:211], v[60:63]\n"
      "ds_write_addtid_b32 v48 offset:17408\n"
      "ds_write_addtid_b32 v14 offset:51456\n"
      "v_mfma_f32_16x16x32_bf16 v[64:67], a[124:127], a[212:215], v[64:67]\n"
      "ds_read_b128 a[204:207], v4 offset:0\n"
      "v_cvt_pk_bf16_f32 v50, v50, v51\n"
      "v_mfma_f32_16x16x32_bf16 v[68:71], a[116:119], a[212:215], v[68:71]\n"
      "ds_write_addtid_b32 v50 offset:17920\n"
      "v_cvt_pk_bf16_f32 v15, v15, v19\n"
      "v_cvt_pk_bf16_f32 v36, v36, v37\n"
      "v_mfma_f32_16x16x32_bf16 v[72:75], a[108:111], a[212:215], v[72:75]\n"
      "s_waitcnt lgkmcnt(4)\n"
      "ds_write_addtid_b32 v36 offset:19456\n"
      "ds_write_addtid_b32 v15 offset:55552\n"
      "v_sub_f32 v52, v64, v52\n"
      "v_mfma_f32_16x16x32_bf16 v[100:103], a[0:3], a[192:195], 0\n"
      "ds_read_b128 a[208:211], v4 offset:64\n"
      "ds_write_addtid_b32 v52 offset:0\n"
      "v_sub_f32_e64 v53, v65, v53\n"
      "v_mfma_f32_16x16x32_bf16 v[104:107], a[4:7], a[192:195], 0\n"
      "ds_write_addtid_b32 v53 offset:512\n"
      "v_sub_f32 v54, v66, v54\n"
      "v_mfma_f32_4x4x4_16b_bf16 v[20:23], a[216:217], a[224:225], 0\n"
      "v_sub_f32 v55, v67, v55\n"
      "v_mfma_f32_4x4x4_16b_bf16 v[24:27], a[216:217], a[226:227], 0\n"
      "ds_write_addtid_b32 v54 offset:1024\n"
      "ds_write_addtid_b32 v55 offset:1536\n"
      "v_sub_f32_e64 v56, v68, v56\n"
      "v_mfma_f32_16x16x32_bf16 v[108:111], a[8:11], a[192:195], 0\n"
      "ds_write_addtid_b32 v56 offset:2048\n"
      "v_sub_f32 v57, v69, v57\n"
      "v_mfma_f32_16x16x32_bf16 v[100:103], a[12:15], a[196:199], v[100:103]\n"
      "ds_read_b128 a[212:215], v4 offset:128\n"
      "ds_write_addtid_b32 v57 offset:2560\n"
      "v_mfma_f32_16x16x32_bf16 v[104:107], a[16:19], a[196:199], v[104:107]\n"
      "v_cvt_pk_bf16_f32 v20, v20, v24\n"
      "v_sub_f32 v58, v70, v58\n"
      "v_mfma_f32_16x16x32_bf16 v[108:111], a[20:23], a[196:199], v[108:111]\n"
      "ds_write_addtid_b32 v20 offset:43776\n"
      "ds_write_addtid_b32 v58 offset:3072\n"
      "v_sub_f32 v59, v71, v59\n"
      "v_mfma_f32_16x16x32_bf16 v[100:103], a[24:27], a[200:203], v[100:103]\n"
      "ds_read_b128 a[192:195], v5 offset:0\n"
      "ds_write_addtid_b32 v59 offset:3584\n"
      "v_mfma_f32_16x16x32_bf16 v[104:107], a[28:31], a[200:203], v[104:107]\n"
      "v_sub_f32 v60, v72, v60\n"
      "v_cvt_pk_bf16_f32 v38, v38, v39\n"
      "v_mfma_f32_16x16x32_bf16 v[108:111], a[32:35], a[200:203], v[108:111]\n"
      "s_waitcnt lgkmcnt(4)\n"
      "ds_write_addtid_b32 v38 offset:19968\n"
      "ds_write_addtid_b32 v60 offset:4096\n"
      "v_cvt_pk_bf16_f32 v21, v21, v25\n"
      "v_mfma_f32_16x16x32_bf16 v[100:103], a[36:39], a[204:207], v[100:103]\n"
      "s_nop 0\n"
      "ds_read_b128 a[196:199], v5 offset:64\n"
      "ds_write_addtid_b32 v21 offset:47872\n"
      "v_mfma_f32_16x16x32_bf16 v[104:107], a[40:43], a[204:207], v[104:107]\n"
      "v_sub_f32 v61, v73, v61\n"
      "v_sub_f32 v62, v74, v62\n"
      "v_mfma_f32_16x16x32_bf16 v[108:111], a[44:47], a[204:207], v[108:111]\n"
      "ds_write_addtid_b32 v61 offset:4608\n"
      "ds_write_addtid_b32 v62 offset:5120\n"
      "v_sub_f32 v63, v75, v63\n"
      "v_mfma_f32_16x16x32_bf16 v[100:103], a[48:51], a[208:211], v[100:103]\n"
      "s_nop 0\n"
      "ds_read_b128 a[200:203], v5 offset:128\n"
      "ds_write_addtid_b32 v63 offset:5632\n"
      "v_mfma_f32_16x16x32_bf16 v[104:107], a[52:55], a[208:211], v[104:107]\n"
      "v_cvt_pk_bf16_f32 v22, v22, v26\n"
      "v_cvt_pk_bf16_f32 v23, v23, v27\n"
      "v_mfma_f32_16x16x32_bf16 v[108:111], a[56:59], a[208:211], v[108:111]\n"
      "ds_write_addtid_b32 v22 offset:51968\n"
      "ds_write_addtid_b32 v23 offset:56064\n"
      "v_fma_f32 v40, 0.5, v52, -v64\n"
      "v_mfma_f32_16x16x32_bf16 v[100:103], a[60:63], a[212:215], v[100:103]\n"
      "v_fma_f32 v41, 0.5, v53, -v65\n"
      "v_fma_f32 v42, 0.5, v54, -v66\n"
      "v_mfma_f32_16x16x32_bf16 v[104:107], a[64:67], a[212:215], v[104:107]\n"
      "v_fma_f32 v43, 0.5, v55, -v67\n"
      "v_fma_f32 v44, 0.5, v56, -v68\n"
      "v_mfma_f32_16x16x32_bf16 v[108:111], a[68:71], a[212:215], v[108:111]\n"
      "s_waitcnt lgkmcnt(0)\n"
      "v_fma_f32 v45, 0.5, v57, -v69\n"
      "s_barrier\n"
      "v_mfma_f32_16x16x32_bf16 v[100:103], a[72:75], a[192:195], v[100:103]\n"
      "s_add_u32 s59, s62, 5701632\n"
      "ds_read_b128 a[204:207], v0 offset:16384\n"
      "ds_read2st64_b32 v[28:29], v6 offset0:0 offset1:2\n"
      "v_fma_f32 v46, 0.5, v58, -v70\n"
      "v_mfma_f32_16x16x32_bf16 v[104:107], a[76:79], a[192:195], v[104:107]\n"
      "s_add_u32 s59, s59, s40\n"
      "ds_read_b128 a[208:211], v0 offset:20480\n"
      "ds_read2st64_b32 v[30:31], v6 offset0:4 offset1:6\n"
      "v_mfma_f32_16x16x32_bf16 v[108:111], a[80:83], a[192:195], v[108:111]\n"
      "ds_read2st64_b32 v[32:33], v6 offset0:8 offset1:10\n"
      "v_fma_f32 v47, 0.5, v59, -v71\n"
      "v_mfma_f32_16x16x32_bf16 v[100:103], a[84:87], a[196:199], v[100:103]\n"
      "s_nop 0\n"
      "ds_read_b128 a[212:215], v0 offset:16448\n"
      "v_fma_f32 v48, 0.5, v60, -v72\n"
      "v_mfma_f32_16x16x32_bf16 v[104:107], a[88:91], a[196:199], v[104:107]\n"
      "ds_read2st64_b32 v[34:35], v6 offset0:12 offset1:14\n"
      "ds_read2st64_b32 v[36:37], v6 offset0:16 offset1:18\n"
      "v_mfma_f32_16x16x32_bf16 v[108:111], a[92:95], a[196:199], v[108:111]\n"
      "v_bfe_u32 v16, v11, 6, 1\n"
      "v_mul_i32_i24 v17, 12296, v16\n"
      "s_mov_b32 s50, -8200\n"
      "v_add3_u32 v12, v8, v17, s50\n"
      "v_add_u32 v14, 128, v12\n"
      "v_mul_i32_i24 v17, 8, v16\n"
      "s_mov_b32 s50, -4104\n"
      "v_add3_u32 v13, v8, v17, s50\n"
      "v_add_u32 v15, 128, v13\n"
      "v_mfma_f32_16x16x32_bf16 v[100:103], a[96:99], a[200:203], v[100:103]\n"
      "s_nop 0\n"
      "ds_read_b128 a[192:195], v0 offset:20544\n"
      "ds_read2st64_b32 a[232:233], v12 offset0:4 offset1:6\n"
      "v_mfma_f32_16x16x32_bf16 v[104:107], a[100:103], a[200:203], v[104:107]\n"
      "ds_read2st64_b32 a[234:235], v14 offset0:4 offset1:6\n"
      "ds_read2st64_b32 v[38:39], v6 offset0:20 offset1:22\n"
      "v_fma_f32 v49, 0.5, v61, -v73\n"
      "v_fma_f32 v50, 0.5, v62, -v74\n"
      "v_fma_f32 v51, 0.5, v63, -v75\n"
      "v_mfma_f32_16x16x32_bf16 v[108:111], a[104:107], a[200:203], v[108:111]\n"
      "s_waitcnt lgkmcnt(6)\n"
      "v_sub_f32 v28, v28, v40\n"
      "v_sub_f32 v29, v29, v41\n"
      "v_mfma_f32_16x16x32_bf16 v[52:55], v[252:255], a[204:207], v[88:91]\n"
      "ds_read_b128 a[196:199], v0 offset:16512\n"
      "v_sub_f32 v30, v30, v42\n"
      "v_mfma_f32_16x16x32_bf16 v[56:59], v[244:247], a[204:207], v[92:95]\n"
      "v_sub_f32 v31, v31, v43\n"
      "v_fmac_f32 v28, s46, v76\n"
      "v_mfma_f32_16x16x32_bf16 v[60:63], v[236:239], a[204:207], v[96:99]\n"
      "v_fmac_f32 v29, s46, v77\n"
      "v_fmac_f32 v30, s46, v78\n"
      "v_mfma_f32_16x16x32_bf16 v[64:67], v[248:251], a[208:211], v[88:91]\n"
      "ds_read_b128 a[200:203], v0 offset:20608\n"
      "v_fmac_f32 v31, s46, v79\n"
      "v_mfma_f32_16x16x32_bf16 v[68:71], v[240:243], a[208:211], v[92:95]\n"
      "v_fmac_f32 v40, s45, v28\n"
      "v_fmac_f32 v41, s45, v29\n"
      "v_mfma_f32_16x16x32_bf16 v[72:75], v[232:235], a[208:211], v[96:99]\n"
      "v_fmac_f32 v42, s45, v30\n"
      "v_fmac_f32 v43, s45, v31\n"
      "v_mfma_f32_16x16x32_bf16 v[52:55], v[228:231], a[212:215], v[52:55]\n"
      "s_nop 0\n"
      "ds_read_b128 a[204:207], v1 offset:16384\n"
      "v_fma_f32 v28, s47, v28, v100\n"
      "v_mfma_f32_16x16x32_bf16 v[56:59], v[220:223], a[212:215], v[56:59]\n"
      "v_fma_f32 v29, s47, v29, v101\n"
      "v_mfma_f32_16x16x32_bf16 v[60:63], v[212:215], a[212:215], v[60:63]\n"
      "s_waitcnt lgkmcnt(1)\n"
      "v_fma_f32 v30, s47, v30, v102\n"
      "v_mfma_f32_16x16x32_bf16 v[64:67], v[224:227], a[192:195], v[64:67]\n"
      "s_nop 0\n"
      "ds_read_b128 a[208:211], v1 offset:20480\n"
      "v_fma_f32 v31, s47, v31, v103\n"
      "v_mfma_f32_16x16x32_bf16 v[68:71], v[216:219], a[192:195], v[68:71]\n"
      "v_cvt_pk_bf16_f32 v40, v40, v41\n"
      "v_mfma_f32_16x16x32_bf16 v[72:75], v[208:211], a[192:195], v[72:75]\n"
      "buffer_store_dwordx4 a[232:235], v11, s[8:11], s59 offen offset:0 sc1\n"
      "v_mfma_f32_16x16x32_bf16 v[52:55], v[204:207], a[196:199], v[52:55]\n"
      "ds_read_b128 a[212:215], v1 offset:16448\n"
      "ds_write_addtid_b32 v40 offset:12288\n"
      "v_mfma_f32_16x16x32_bf16 v[56:59], v[196:199], a[196:199], v[56:59]\n"
      "ds_read2st64_b32 a[236:237], v13 offset0:4 offset1:6\n"
      "ds_read2st64_b32 a[238:239], v15 offset0:4 offset1:6\n"
      "v_mfma_f32_16x16x32_bf16 v[60:63], v[188:191], a[196:199], v[60:63]\n"
      "v_cvt_pk_bf16_f32 v42, v42, v43\n"
      "v_cvt_pk_bf16_f32 v28, v28, v29\n"
      "v_mfma_f32_16x16x32_bf16 v[64:67], v[200:203], a[200:203], v[64:67]\n"
      "ds_read_b128 a[192:195], v1 offset:20544\n"
      "ds_write_addtid_b32 v42 offset:12800\n"
      "v_mfma_f32_16x16x32_bf16 v[68:71], v[192:195], a[200:203], v[68:71]\n"
      "ds_write_addtid_b32 v28 offset:14336\n"
      "v_cvt_pk_bf16_f32 v30, v30, v31\n"
      "v_mfma_f32_16x16x32_bf16 v[72:75], v[184:187], a[200:203], v[72:75]\n"
      "s_waitcnt lgkmcnt(6)\n"
      "ds_write_addtid_b32 v30 offset:14848\n"
      "v_sub_f32 v32, v32, v44\n"
      "v_mfma_f32_16x16x32_bf16 v[52:55], v[180:183], a[204:207], v[52:55]\n"
      "ds_read_b128 a[196:199], v1 offset:16512\n"
      "v_sub_f32 v33, v33, v45\n"
      "v_mfma_f32_16x16x32_bf16 v[56:59], v[172:175], a[204:207], v[56:59]\n"
      "v_sub_f32 v34, v34, v46\n"
      "v_sub_f32 v35, v35, v47\n"
      "v_mfma_f32_16x16x32_bf16 v[60:63], v[164:167], a[204:207], v[60:63]\n"
      "v_fmac_f32 v32, s46, v80\n"
      "v_fmac_f32 v33, s46, v81\n"
      "v_mfma_f32_16x16x32_bf16 v[64:67], v[176:179], a[208:211], v[64:67]\n"
      "s_nop 0\n"
      "ds_read_b128 a[200:203], v1 offset:20608\n"
      "v_fmac_f32 v34, s46, v82\n"
      "v_mfma_f32_16x16x32_bf16 v[68:71], v[168:171], a[208:211], v[68:71]\n"
      "v_fmac_f32 v35, s46, v83\n"
      "v_fmac_f32 v44, s45, v32\n"
      "v_mfma_f32_16x16x32_bf16 v[72:75], v[160:163], a[208:211], v[72:75]\n"
      "v_fmac_f32 v45, s45, v33\n"
      "v_fmac_f32 v46, s45, v34\n"
      "v_mfma_f32_16x16x32_bf16 v[52:55], v[156:159], a[212:215], v[52:55]\n"
      "ds_read_b128 a[204:207], v2 offset:16384\n"
      "v_fmac_f32 v47, s45, v35\n"
      "v_mfma_f32_16x16x32_bf16 v[56:59], v[148:151], a[212:215], v[56:59]\n"
      "v_fma_f32 v32, s47, v32, v104\n"
      "v_fma_f32 v33, s47, v33, v105\n"
      "v_mfma_f32_16x16x32_bf16 v[60:63], v[140:143], a[212:215], v[60:63]\n"
      "s_waitcnt lgkmcnt(1)\n"
      "v_fma_f32 v34, s47, v34, v106\n"
      "v_fma_f32 v35, s47, v35, v107\n"
      "v_mfma_f32_16x16x32_bf16 v[64:67], v[152:155], a[192:195], v[64:67]\n"
      "s_nop 0\n"
      "buffer_store_dwordx4 a[236:239], v11, s[8:11], s59 offen offset:128 sc1\n"
      "ds_read_b128 a[208:211], v2 offset:20480\n"
      "v_mfma_f32_16x16x32_bf16 v[68:71], v[144:147], a[192:195], v[68:71]\n"
      "v_cvt_pk_bf16_f32 v44, v44, v45\n"
      "v_cvt_pk_bf16_f32 v46, v46, v47\n"
      "v_mfma_f32_16x16x32_bf16 v[72:75], v[136:139], a[192:195], v[72:75]\n"
      "ds_write_addtid_b32 v44 offset:20484\n"
      "ds_read2st64_b32 a[240:241], v8 offset0:4 offset1:6\n"
      "v_mfma_f32_16x16x32_bf16 v[52:55], v[132:135], a[196:199], v[52:55]\n"
      "ds_read_b128 a[212:215], v2 offset:16448\n"
      "ds_write_addtid_b32 v46 offset:20996\n"
      "v_mfma_f32_16x16x32_bf16 v[56:59], v[124:127], a[196:199], v[56:59]\n"
      "ds_read2st64_b32 a[242:243], v9 offset0:4 offset1:6\n"
      "v_cvt_pk_bf16_f32 v32, v32, v33\n"
      "v_mfma_f32_16x16x32_bf16 v[60:63], a[184:187], a[196:199], v[60:63]\n"
      "ds_write_addtid_b32 v32 offset:22532\n"
      "v_cvt_pk_bf16_f32 v34, v34, v35\n"
      "v_mfma_f32_16x16x32_bf16 v[64:67], v[128:131], a[200:203], v[64:67]\n"
      "ds_read_b128 a[192:195], v2 offset:20544\n"
      "ds_write_addtid_b32 v34 offset:23044\n"
      "v_mfma_f32_16x16x32_bf16 v[68:71], a[188:191], a[200:203], v[68:71]\n"
      "v_sub_f32 v36, v36, v48\n"
      "v_sub_f32 v37, v37, v49\n"
      "v_mfma_f32_16x16x32_bf16 v[72:75], a[180:183], a[200:203], v[72:75]\n"
      "s_waitcnt lgkmcnt(5)\n"
      "v_sub_f32 v38, v38, v50\n"
      "v_sub_f32 v39, v39, v51\n"
      "v_mfma_f32_16x16x32_bf16 v[52:55], a[176:179], a[204:207], v[52:55]\n"
      "ds_read_b128 a[196:199], v2 offset:16512\n"
      "v_fmac_f32 v36, s46, v84\n"
      "v_mfma_f32_16x16x32_bf16 v[56:59], a[168:171], a[204:207], v[56:59]\n"
      "v_fmac_f32 v37, s46, v85\n"
      "v_fmac_f32 v38, s46, v86\n"
      "v_mfma_f32_16x16x32_bf16 v[60:63], a[160:163], a[204:207], v[60:63]\n"
      "v_fmac_f32 v39, s46, v87\n"
      "v_fmac_f32 v48, s45, v36\n"
      "v_mfma_f32_16x16x32_bf16 v[64:67], a[172:175], a[208:211], v[64:67]\n"
      "ds_read_b128 a[200:203], v2 offset:20608\n"
      "v_fmac_f32 v49, s45, v37\n"
      "v_mfma_f32_16x16x32_bf16 v[68:71], a[164:167], a[208:211], v[68:71]\n"
      "v_fmac_f32 v50, s45, v38\n"
      "v_fmac_f32 v51, s45, v39\n"
      "v_mfma_f32_16x16x32_bf16 v[72:75], a[156:159], a[208:211], v[72:75]\n"
      "v_fma_f32 v36, s47, v36, v108\n"
      "v_fma_f32 v37, s47, v37, v109\n"
      "v_mfma_f32_16x16x32_bf16 v[52:55], a[152:155], a[212:215], v[52:55]\n"
      "s_waitcnt lgkmcnt(1)\n"
      "ds_read_b128 a[204:207], v3 offset:8192\n"
      "v_fma_f32 v38, s47, v38, v110\n"
      "v_mfma_f32_16x16x32_bf16 v[56:59], a[144:147], a[212:215], v[56:59]\n"
      "buffer_store_dwordx4 a[240:243], v11, s[8:11], s59 offen offset:256 sc1\n"
      "v_mfma_f32_16x16x32_bf16 v[60:63], a[136:139], a[212:215], v[60:63]\n"
      "v_fma_f32 v39, s47, v39, v111\n"
      "v_cvt_pk_bf16_f32 v48, v48, v49\n"
      "v_mfma_f32_16x16x32_bf16 v[64:67], a[148:151], a[192:195], v[64:67]\n"
      "ds_read_b128 a[208:211], v3 offset:8256\n"
      "ds_write_addtid_b32 v48 offset:16384\n"
      "v_mfma_f32_16x16x32_bf16 v[68:71], a[140:143], a[192:195], v[68:71]\n"
      "v_cvt_pk_bf16_f32 v50, v50, v51\n"
      "v_cvt_pk_bf16_f32 v36, v36, v37\n"
      "v_mfma_f32_16x16x32_bf16 v[72:75], a[132:135], a[192:195], v[72:75]\n"
      "ds_write_addtid_b32 v50 offset:16896\n"
      "ds_write_addtid_b32 v36 offset:18432\n"
      "v_mfma_f32_16x16x32_bf16 v[52:55], a[128:131], a[196:199], v[52:55]\n"
      "ds_read_b128 a[212:215], v3 offset:8320\n"
      "v_cvt_pk_bf16_f32 v38, v38, v39\n"
      "v_mfma_f32_16x16x32_bf16 v[56:59], a[120:123], a[196:199], v[56:59]\n"
      "ds_write_addtid_b32 v38 offset:18944\n"
      "v_mfma_f32_16x16x32_bf16 v[60:63], a[112:115], a[196:199], v[60:63]\n"
      "s_waitcnt lgkmcnt(4)\n"
      "v_mfma_f32_16x16x32_bf16 v[64:67], a[124:127], a[200:203], v[64:67]\n"
      "s_nop 0\n"
      "ds_read_b128 a[192:195], v4 offset:8192\n"
      "v_mfma_f32_16x16x32_bf16 v[68:71], a[116:119], a[200:203], v[68:71]\n"
      "v_mfma_f32_16x16x32_bf16 v[72:75], a[108:111], a[200:203], v[72:75]\n"
      "v_mfma_f32_16x16x32_bf16 v[112:115], a[0:3], a[204:207], 0\n"
      "ds_read_b128 a[196:199], v4 offset:8256\n"
      "v_mfma_f32_16x16x32_bf16 v[116:119], a[4:7], a[204:207], 0\n"
      "v_mfma_f32_16x16x32_bf16 v[120:123], a[8:11], a[204:207], 0\n"
      "v_mfma_f32_16x16x32_bf16 v[112:115], a[12:15], a[208:211], v[112:115]\n"
      "ds_read_b128 a[200:203], v4 offset:8320\n"
      "v_sub_f32 v52, v64, v52\n"
      "v_sub_f32 v53, v65, v53\n"
      "v_sub_f32 v54, v66, v54\n"
      "v_sub_f32 v55, v67, v55\n"
      "v_sub_f32 v56, v68, v56\n"
      "v_sub_f32_e64 v57, v69, v57\n"
      "v_mfma_f32_16x16x32_bf16 v[116:119], a[16:19], a[208:211], v[116:119]\n"
      "s_nop 0\n"
      "ds_write_addtid_b32 v52 offset:6144\n"
      "ds_write_addtid_b32 v53 offset:6656\n"
      "ds_write_addtid_b32 v54 offset:7168\n"
      "v_sub_f32 v58, v70, v58\n"
      "v_sub_f32_e64 v59, v71, v59\n"
      "v_sub_f32_e64 v60, v72, v60\n"
      "v_sub_f32_e64 v61, v73, v61\n"
      "v_mfma_f32_16x16x32_bf16 v[120:123], a[20:23], a[208:211], v[120:123]\n"
      "s_waitcnt lgkmcnt(3)\n"
      "ds_write_addtid_b32 v55 offset:7680\n"
      "ds_write_addtid_b32 v56 offset:8192\n"
      "ds_write_addtid_b32 v57 offset:8704\n"
      "v_sub_f32 v62, v74, v62\n"
      "v_sub_f32_e64 v63, v75, v63\n"
      "v_mfma_f32_16x16x32_bf16 v[112:115], a[24:27], a[212:215], v[112:115]\n"
      "s_nop 0\n"
      "ds_write_addtid_b32 v58 offset:9216\n"
      "ds_write_addtid_b32 v59 offset:9728\n"
      "ds_write_addtid_b32 v60 offset:10240\n"
      "v_mfma_f32_16x16x32_bf16 v[116:119], a[28:31], a[212:215], v[116:119]\n"
      "s_nop 0\n"
      "ds_write_addtid_b32 v61 offset:10752\n"
      "ds_write_addtid_b32 v62 offset:11264\n"
      "ds_write_addtid_b32 v63 offset:11776\n"
      "v_mfma_f32_16x16x32_bf16 v[120:123], a[32:35], a[212:215], v[120:123]\n"
      "s_waitcnt lgkmcnt(0)\n"
      "s_barrier\n"
      "v_mfma_f32_16x16x32_bf16 v[112:115], a[36:39], a[192:195], v[112:115]\n"
      "s_sub_u32 s59, s59, s40\n"
      "ds_read_b128 a[204:207], v5 offset:8192\n"
      "ds_read2st64_b32 v[28:29], v6 offset0:24 offset1:26\n"
      "v_mfma_f32_16x16x32_bf16 v[116:119], a[40:43], a[192:195], v[116:119]\n"
      "s_add_u32 s59, s59, 786432\n"
      "ds_read2st64_b32 v[30:31], v6 offset0:28 offset1:30\n"
      "ds_read2st64_b32 v[32:33], v6 offset0:32 offset1:34\n"
      "v_mfma_f32_16x16x32_bf16 v[120:123], a[44:47], a[192:195], v[120:123]\n"
      "ds_read2st64_b32 v[34:35], v6 offset0:36 offset1:38\n"
      "ds_read2st64_b32 v[36:37], v6 offset0:40 offset1:42\n"
      "v_mfma_f32_16x16x32_bf16 v[112:115], a[48:51], a[196:199], v[112:115]\n"
      "ds_read_b128 a[208:211], v5 offset:8256\n"
      "ds_read2st64_b32 v[38:39], v6 offset0:44 offset1:46\n"
      "v_mfma_f32_16x16x32_bf16 v[116:119], a[52:55], a[196:199], v[116:119]\n"
      "v_fma_f32 v40, 0.5, v52, -v64\n"
      "v_fma_f32 v41, 0.5, v53, -v65\n"
      "v_mfma_f32_16x16x32_bf16 v[120:123], a[56:59], a[196:199], v[120:123]\n"
      "ds_read2st64_b32 a[232:233], v12 offset0:0 offset1:2\n"
      "ds_read2st64_b32 a[234:235], v14 offset0:0 offset1:2\n"
      "v_mfma_f32_16x16x32_bf16 v[112:115], a[60:63], a[200:203], v[112:115]\n"
      "ds_read_b128 a[212:215], v5 offset:8320\n"
      "v_fma_f32 v42, 0.5, v54, -v66\n"
      "v_mfma_f32_16x16x32_bf16 v[116:119], a[64:67], a[200:203], v[116:119]\n"
      "ds_read2st64_b32 a[236:237], v13 offset0:0 offset1:2\n"
      "ds_read2st64_b32 a[238:239], v15 offset0:0 offset1:2\n"
      "v_mfma_f32_16x16x32_bf16 v[120:123], a[68:71], a[200:203], v[120:123]\n"
      "s_waitcnt lgkmcnt(9)\n"
      "v_fma_f32 v43, 0.5, v55, -v67\n"
      "v_fma_f32 v44, 0.5, v56, -v68\n"
      "v_mfma_f32_16x16x32_bf16 v[112:115], a[72:75], a[204:207], v[112:115]\n"
      "s_waitcnt lgkmcnt(3)\n"
      "buffer_store_dwordx4 a[232:235], v11, s[8:11], s59 offen offset:0 sc1\n"
      "v_mfma_f32_16x16x32_bf16 v[116:119], a[76:79], a[204:207], v[116:119]\n"
      "v_fma_f32 v45, 0.5, v57, -v69\n"
      "v_fma_f32 v46, 0.5, v58, -v70\n"
      "v_mfma_f32_16x16x32_bf16 v[120:123], a[80:83], a[204:207], v[120:123]\n"
      "ds_read2st64_b32 a[240:241], v8 offset0:0 offset1:2\n"
      "ds_read2st64_b32 a[242:243], v9 offset0:0 offset1:2\n"
      "v_mfma_f32_16x16x32_bf16 v[112:115], a[84:87], a[208:211], v[112:115]\n"
      "s_waitcnt lgkmcnt(2)\n"
      "buffer_store_dwordx4 a[236:239], v11, s[8:11], s59 offen offset:128 sc1\n"
      "v_mfma_f32_16x16x32_bf16 v[116:119], a[88:91], a[208:211], v[116:119]\n"
      "s_nop 0\n"
      "v_fma_f32 v47, 0.5, v59, -v71\n"
      "v_fma_f32 v48, 0.5, v60, -v72\n"
      "v_mfma_f32_16x16x32_bf16 v[120:123], a[92:95], a[208:211], v[120:123]\n"
      "v_fma_f32 v49, 0.5, v61, -v73\n"
      "v_fma_f32 v50, 0.5, v62, -v74\n"
      "v_mfma_f32_16x16x32_bf16 v[112:115], a[96:99], a[212:215], v[112:115]\n"
      "s_waitcnt lgkmcnt(0)\n"
      "buffer_store_dwordx4 a[240:243], v11, s[8:11], s59 offen offset:256 sc1\n"
      "v_mfma_f32_16x16x32_bf16 v[116:119], a[100:103], a[212:215], v[116:119]\n"
      "s_nop 0\n"
      "v_fma_f32 v51, 0.5, v63, -v75\n"
      "v_sub_f32 v28, v28, v40\n"
      "v_mfma_f32_16x16x32_bf16 v[120:123], a[104:107], a[212:215], v[120:123]\n"
      "v_sub_f32 v29, v29, v41\n"
      "v_sub_f32 v30, v30, v42\n"
      "v_sub_f32 v31, v31, v43\n"
      "v_fmac_f32 v28, s46, v88\n"
      "v_fmac_f32 v29, s46, v89\n"
      "v_fmac_f32 v30, s46, v90\n"
      "v_fmac_f32 v31, s46, v91\n"
      "v_fmac_f32 v40, s45, v28\n"
      "v_fmac_f32 v41, s45, v29\n"
      "v_pk_fma_f32 v[42:43], v[30:31], s[44:45], v[42:43] op_sel:[0,1,0] op_sel_hi:[1,1,1]\n"
      "v_cvt_pk_bf16_f32 v40, v40, v41\n"
      "v_cvt_pk_bf16_f32 v42, v42, v43\n"
      "v_pk_add_f32 v[32:33], v[32:33], v[44:45] neg_lo:[0,1] neg_hi:[0,1]\n"
      "v_pk_add_f32 v[34:35], v[34:35], v[46:47] neg_lo:[0,1] neg_hi:[0,1]\n"
      "v_pk_fma_f32 v[32:33], v[92:93], s[46:47], v[32:33] op_sel:[0,0,0] op_sel_hi:[1,0,1]\n"
      "v_pk_fma_f32 v[34:35], v[94:95], s[46:47], v[34:35] op_sel:[0,0,0] op_sel_hi:[1,0,1]\n"
      "v_pk_fma_f32 v[44:45], v[32:33], s[44:45], v[44:45] op_sel:[0,1,0] op_sel_hi:[1,1,1]\n"
      "v_pk_fma_f32 v[46:47], v[34:35], s[44:45], v[46:47] op_sel:[0,1,0] op_sel_hi:[1,1,1]\n"
      "v_cvt_pk_bf16_f32 v44, v44, v45\n"
      "v_cvt_pk_bf16_f32 v46, v46, v47\n"
      "v_pk_add_f32 v[36:37], v[36:37], v[48:49] neg_lo:[0,1] neg_hi:[0,1]\n"
      "v_pk_add_f32 v[38:39], v[38:39], v[50:51] neg_lo:[0,1] neg_hi:[0,1]\n"
      "v_pk_fma_f32 v[36:37], v[96:97], s[46:47], v[36:37] op_sel:[0,0,0] op_sel_hi:[1,0,1]\n"
      "v_pk_fma_f32 v[38:39], v[98:99], s[46:47], v[38:39] op_sel:[0,0,0] op_sel_hi:[1,0,1]\n"
      "v_pk_fma_f32 v[48:49], v[36:37], s[44:45], v[48:49] op_sel:[0,1,0] op_sel_hi:[1,1,1]\n"
      "v_pk_fma_f32 v[50:51], v[38:39], s[44:45], v[50:51] op_sel:[0,1,0] op_sel_hi:[1,1,1]\n"
      "v_cvt_pk_bf16_f32 v48, v48, v49\n"
      "v_cvt_pk_bf16_f32 v50, v50, v51\n"
      "ds_write_addtid_b32 v40 offset:13312\n"
      "ds_write_addtid_b32 v42 offset:13824\n"
      "ds_write_addtid_b32 v44 offset:21508\n"
      "ds_write_addtid_b32 v46 offset:22020\n"
      "ds_write_addtid_b32 v48 offset:17408\n"
      "ds_write_addtid_b32 v50 offset:17920\n"
      "v_pk_fma_f32 v[28:29], v[28:29], s[46:47], v[112:113] op_sel:[0,1,0] op_sel_hi:[1,1,1]\n"
      "v_pk_fma_f32 v[30:31], v[30:31], s[46:47], v[114:115] op_sel:[0,1,0] op_sel_hi:[1,1,1]\n"
      "v_cvt_pk_bf16_f32 v28, v28, v29\n"
      "v_cvt_pk_bf16_f32 v30, v30, v31\n"
      "v_pk_fma_f32 v[32:33], v[32:33], s[46:47], v[116:117] op_sel:[0,1,0] op_sel_hi:[1,1,1]\n"
      "v_pk_fma_f32 v[34:35], v[34:35], s[46:47], v[118:119] op_sel:[0,1,0] op_sel_hi:[1,1,1]\n"
      "v_cvt_pk_bf16_f32 v32, v32, v33\n"
      "v_cvt_pk_bf16_f32 v34, v34, v35\n"
      "v_pk_fma_f32 v[36:37], v[36:37], s[46:47], v[120:121] op_sel:[0,1,0] op_sel_hi:[1,1,1]\n"
      "v_pk_fma_f32 v[38:39], v[38:39], s[46:47], v[122:123] op_sel:[0,1,0] op_sel_hi:[1,1,1]\n"
      "v_cvt_pk_bf16_f32 v36, v36, v37\n"
      "v_cvt_pk_bf16_f32 v38, v38, v39\n"
      "ds_write_addtid_b32 v28 offset:15360\n"
      "ds_write_addtid_b32 v30 offset:15872\n"
      "ds_write_addtid_b32 v32 offset:23556\n"
      "ds_write_addtid_b32 v34 offset:24068\n"
      "ds_write_addtid_b32 v36 offset:19456\n"
      "ds_write_addtid_b32 v38 offset:19968\n"
      "s_waitcnt lgkmcnt(0)\n"
      "s_barrier\n"
      "ds_read2st64_b32 a[232:233], v12 offset0:4 offset1:6\n"
      "ds_read2st64_b32 a[234:235], v14 offset0:4 offset1:6\n"
      "ds_read2st64_b32 a[236:237], v13 offset0:4 offset1:6\n"
      "ds_read2st64_b32 a[238:239], v15 offset0:4 offset1:6\n"
      "ds_read2st64_b32 a[240:241], v8 offset0:4 offset1:6\n"
      "ds_read2st64_b32 a[242:243], v9 offset0:4 offset1:6\n"
      "s_add_u32 s59, s59, s40\n"
      "s_waitcnt lgkmcnt(4)\n"
      "buffer_store_dwordx4 a[232:235], v11, s[8:11], s59 offen offset:0 sc1\n"
      "s_waitcnt lgkmcnt(2)\n"
      "buffer_store_dwordx4 a[236:239], v11, s[8:11], s59 offen offset:128 sc1\n"
      "s_waitcnt lgkmcnt(0)\n"
      "buffer_store_dwordx4 a[240:243], v11, s[8:11], s59 offen offset:256 sc1\n"
      "LSend_%=:\n"
      "Lfin_%=:\n"
      "s_waitcnt vmcnt(0) lgkmcnt(0)\n"
      "s_endpgm\n"
      ".rept 512\n"
      "s_nop 0\n"
      ".endr\n"
      ".Lwg_end:\n"
      : "+{v0}"(tid)
      : "{s[0:1]}"(__builtin_amdgcn_kernarg_segment_ptr()), "{s2}"(blockIdx.x)
      : "v1", "v2", "v3", "v4", "v5", "v6", "v7", "v8", "v9", "v10", "v11", "v12", "v13", "v14", "v15", "v16", "v17", "v18", "v19", "v20", "v21", "v22", "v23", "v24", "v25", "v26", "v27", "v28", "v29", "v30", "v31", "v32", "v33", "v34", "v35", "v36", "v37", "v38", "v39", "v40", "v41", "v42", "v43", "v44", "v45", "v46", "v47", "v48", "v49", "v50", "v51", "v52", "v53", "v54", "v55", "v56", "v57", "v58", "v59", "v60", "v61", "v62", "v63", "v64", "v65", "v66", "v67", "v68", "v69", "v70", "v71", "v72", "v73", "v74", "v75", "v76", "v77", "v78", "v79", "v80", "v81", "v82", "v83", "v84", "v85", "v86", "v87", "v88", "v89", "v90", "v91", "v92", "v93", "v94", "v95", "v96", "v97", "v98", "v99", "v100", "v101", "v102", "v103", "v104", "v105", "v106", "v107", "v108", "v109", "v110", "v111", "v112", "v113", "v114", "v115", "v116", "v117", "v118", "v119", "v120", "v121", "v122", "v123", "v124", "v125", "v126", "v127", "v128", "v129", "v130", "v131", "v132", "v133", "v134", "v135", "v136", "v137", "v138", "v139", "v140", "v141", "v142", "v143", "v144", "v145", "v146", "v147", "v148", "v149", "v150", "v151", "v152", "v153", "v154", "v155", "v156", "v157", "v158", "v159", "v160", "v161", "v162", "v163", "v164", "v165", "v166", "v167", "v168", "v169", "v170", "v171", "v172", "v173", "v174", "v175", "v176", "v177", "v178", "v179", "v180", "v181", "v182", "v183", "v184", "v185", "v186", "v187", "v188", "v189", "v190", "v191", "v192", "v193", "v194", "v195", "v196", "v197", "v198", "v199", "v200", "v201", "v202", "v203", "v204", "v205", "v206", "v207", "v208", "v209", "v210", "v211", "v212", "v213", "v214", "v215", "v216", "v217", "v218", "v219", "v220", "v221", "v222", "v223", "v224", "v225", "v226", "v227", "v228", "v229", "v230", "v231", "v232", "v233", "v234", "v235", "v236", "v237", "v238", "v239", "v240", "v241", "v242", "v243", "v244", "v245", "v246", "v247", "v248", "v249", "v250", "v251", "v252", "v253", "v254", "v255", "a0", "a1", "a2", "a3", "a4", "a5", "a6", "a7", "a8", "a9", "a10", "a11", "a12", "a13", "a14", "a15", "a16", "a17", "a18", "a19", "a20", "a21", "a22", "a23", "a24", "a25", "a26", "a27", "a28", "a29", "a30", "a31", "a32", "a33", "a34", "a35", "a36", "a37", "a38", "a39", "a40", "a41", "a42", "a43", "a44", "a45", "a46", "a47", "a48", "a49", "a50", "a51", "a52", "a53", "a54", "a55", "a56", "a57", "a58", "a59", "a60", "a61", "a62", "a63", "a64", "a65", "a66", "a67", "a68", "a69", "a70", "a71", "a72", "a73", "a74", "a75", "a76", "a77", "a78", "a79", "a80", "a81", "a82", "a83", "a84", "a85", "a86", "a87", "a88", "a89", "a90", "a91", "a92", "a93", "a94", "a95", "a96", "a97", "a98", "a99", "a100", "a101", "a102", "a103", "a104", "a105", "a106", "a107", "a108", "a109", "a110", "a111", "a112", "a113", "a114", "a115", "a116", "a117", "a118", "a119", "a120", "a121", "a122", "a123", "a124", "a125", "a126", "a127", "a128", "a129", "a130", "a131", "a132", "a133", "a134", "a135", "a136", "a137", "a138", "a139", "a140", "a141", "a142", "a143", "a144", "a145", "a146", "a147", "a148", "a149", "a150", "a151", "a152", "a153", "a154", "a155", "a156", "a157", "a158", "a159", "a160", "a161", "a162", "a163", "a164", "a165", "a166", "a167", "a168", "a169", "a170", "a171", "a172", "a173", "a174", "a175", "a176", "a177", "a178", "a179", "a180", "a181", "a182", "a183", "a184", "a185", "a186", "a187", "a188", "a189", "a190", "a191", "a192", "a193", "a194", "a195", "a196", "a197", "a198", "a199", "a200", "a201", "a202", "a203", "a204", "a205", "a206", "a207", "a208", "a209", "a210", "a211", "a212", "a213", "a214", "a215", "a216", "a217", "a218", "a219", "a220", "a221", "a222", "a223", "a224", "a225", "a226", "a227", "a228", "a229", "a230", "a231", "a232", "a233", "a234", "a235", "a236", "a237", "a238", "a239", "a240", "a241", "a242", "a243", "a244", "a245", "a246", "a247", "a248", "a249", "a250", "a251", "a252", "a253", "a254", "a255", "s3", "s4", "s5", "s6", "s7", "s8", "s9", "s10", "s11", "s12", "s13", "s14", "s15", "s16", "s17", "s18", "s19", "s20", "s21", "s22", "s23", "s24", "s25", "s26", "s27", "s28", "s29", "s30", "s31", "s33", "s34", "s35", "s36", "s37", "s38", "s39", "s40", "s41", "s42", "s43", "s44", "s45", "s46", "s47", "s48", "s49", "s50", "s51", "s52", "s53", "s54", "s55", "s56", "s57", "s58", "s59", "s60", "s61", "s62", "s63", "s64", "s65", "s66", "s67", "s68", "s69", "s70", "s71", "s72", "s73", "s74", "s75", "s76", "s77", "s78", "s79", "s80", "s81", "s82", "s83", "s84", "s85", "s86", "s87", "s88", "s89", "s90", "s91", "s92", "s93", "s94", "s95", "s96", "s97", "s98", "s99", "scc", "vcc", "memory");
  asm volatile("" :: "v"((unsigned)(uintptr_t)smem) : "memory");
}

extern "C" void kernel_launch(void* const* d_in, const int* in_sizes, int n_in, void* d_out,
                              int out_size, void* d_ws, size_t ws_size, hipStream_t stream) {
  conv_kernel<<<256, 256, 0, stream>>>((const char*)d_in[0], (const char*)d_in[1], (char*)d_out, (char*)d_ws);
}
```

```cpp
#include <hip/hip_runtime.h>
#include <hip/hip_bf16.h>
#include <stdint.h>

__global__ void __launch_bounds__(256, 1)
conv_kernel(const char* __restrict__ x, const char* __restrict__ w, char* __restrict__ out, char* __restrict__ dbg) {
  __shared__ __attribute__((aligned(1024))) char smem[163840];
  unsigned tid = threadIdx.x;
  asm volatile(
      "s_load_dwordx2 s[12:13], s[0:1], 8\n"
      "s_load_dwordx2 s[4:5], s[0:1], 0\n"
      "s_load_dwordx2 s[8:9], s[0:1], 16\n"
      "v_readfirstlane_b32 s16, v0\n"
      "s_getpc_b64 s[26:27]\n"
      ".Lwg_pc:\n"
      "v_and_b32 v37, 3, v0\n"
      "s_lshr_b32 s23, s2, 3\n"
      "s_lshl_b32 s28, s16, 3\n"
      "v_lshlrev_b32 v40, 7, v37\n"
      "s_lshl_b32 s29, s23, 11\n"
      "s_mov_b32 m0, 150016\n"
      "s_add_u32 s28, s28, s29\n"
      "v_and_b32 v36, 63, v0\n"
      "v_add_u32 v40, s28, v40\n"
      "s_lshr_b32 s16, s16, 6\n"
      "v_min_u32 v40, .Lwg_end-.Lwg_pc-4, v40\n"
      "s_and_b32 s18, s16, 1\n"
      "global_load_lds_dword v40, s[26:27]\n"
      "v_lshlrev_b32 v17, 4, v36\n"
      "s_lshr_b32 s17, s16, 1\n"
      "s_bfe_u32 s28, s2, 0x20001\n"
      "s_mul_i32 s19, s28, 12582912\n"
      "s_and_b32 s28, s23, 1\n"
      "s_mul_i32 s20, s28, 98304\n"
      "s_lshr_b32 s28, s23, 1\n"
      "s_mul_i32 s29, s28, 3072\n"
      "s_bitcmp1_b32 s2, 0\n"
      "s_cselect_b32 s28, 49152, 0\n"
      "s_add_u32 s21, s20, s29\n"
      "s_add_u32 s21, s21, s28\n"
      "s_add_u32 s6, s19, 12783616\n"
      "s_min_u32 s6, s6, 50335744\n"
      "s_mov_b32 s7, 0x20000\n"
      "s_mov_b32 s10, 50331648\n"
      "s_mov_b32 s11, 0x20000\n"
      "s_mov_b32 s14, 165888\n"
      "s_mov_b32 s15, 0x20000\n"
      "s_mul_i32 s63, s18, 3072\n"
      "s_add_u32 s54, s19, s20\n"
      "s_sub_u32 s54, s54, 196608\n"
      "s_lshl_b32 s64, s18, 8\n"
      "s_mul_i32 s28, s17, 24832\n"
      "s_add_u32 s64, s64, s28\n"
      "s_mov_b32 s55, 0\n"
      "v_bitop3_b32 v18, v36, v17, 32 bitop3:0x6c\n"
      "v_xor_b32 v19, 32, v17\n"
      "v_bfe_u32 v38, v36, 2, 2\n"
      "v_lshrrev_b32 v39, 4, v36\n"
      "s_cmp_eq_u32 s17, 1\n"
      "s_cbranch_scc1 LBD_%=\n"
      "v_lshlrev_b32 v40, 3, v17\n"
      "s_mul_i32 s53, s18, 2688\n"
      "s_mul_i32 s34, s23, 5184\n"
      "v_min_u32 v40, 2560, v40\n"
      "s_add_u32 s53, s53, s34\n"
      "v_lshlrev_b32 v43, 4, v37\n"
      "s_mul_i32 s26, s17, 0x8080\n"
      "s_sub_u32 s26, 0xbf80, s26\n"
      "s_mov_b32 s27, 0\n"
      "s_lshl_b32 s34, s17, 7\n"
      "s_mov_b32 s29, 0\n"
      "s_sub_u32 s28, s26, s34\n"
      "s_mov_b32 s31, 0\n"
      "s_sub_u32 s30, s28, s34\n"
      "v_lshlrev_b64 v[44:45], v43, s[26:27]\n"
      "v_lshlrev_b64 v[46:47], v43, s[28:29]\n"
      "s_xor_b32 s28, s28, 0x8000\n"
      "v_lshlrev_b64 v[48:49], v43, s[30:31]\n"
      "v_lshlrev_b64 v[50:51], v43, s[28:29]\n"
      "v_accvgpr_write_b32 a244, v44\n"
      "v_accvgpr_write_b32 a245, v45\n"
      "v_accvgpr_write_b32 a246, v46\n"
      "v_accvgpr_write_b32 a247, v47\n"
      "v_accvgpr_write_b32 a248, v48\n"
      "v_accvgpr_write_b32 a249, v49\n"
      "v_accvgpr_write_b32 a250, v50\n"
      "v_accvgpr_write_b32 a251, v51\n"
      "s_nop 0\n"
      "s_waitcnt lgkmcnt(0)\n"
      "s_and_b32 s13, s13, 0xffff\n"
      "buffer_load_dword v40, s[12:15], s53 offen lds\n"
      "s_add_u32 m0, s63, 30976\n"
      "s_add_u32 s52, s63, 55296\n"
      "buffer_load_dwordx4 v17, s[12:15], s52 offen offset:0 lds\n"
      "buffer_load_dwordx4 v18, s[12:15], s52 offen offset:1024 lds\n"
      "buffer_load_dwordx4 v19, s[12:15], s52 offen offset:2048 lds\n"
      "s_add_u32 m0, s63, 0\n"
      "s_add_u32 s52, s63, 110592\n"
      "buffer_load_dwordx4 v17, s[12:15], s52 offen offset:0 lds\n"
      "buffer_load_dwordx4 v18, s[12:15], s52 offen offset:1024 lds\n"
      "buffer_load_dwordx4 v19, s[12:15], s52 offen offset:2048 lds\n"
      "s_branch LS_%=\n"
      "LBD_%=:\n"
      "s_sub_u32 s26, s21, s20\n"
      "s_lshl_b32 s37, s18, 11\n"
      "s_add_u32 s26, s26, s37\n"
      "s_add_u32 s27, s26, 3712\n"
      "v_add_u32 v44, s27, v17\n"
      "s_cmp_eq_u32 s18, 1\n"
      "s_cselect_b32 s28, 16, 48\n"
      "s_cselect_b32 s29, 0, 32\n"
      "s_cselect_b32 s27, 48, 64\n"
      "v_cmp_le_u32 vcc, s28, v36\n"
      "v_cmp_gt_u32 s[30:31], s29, v36\n"
      "v_xor_b32 v45, 32, v44\n"
      "s_cmp_eq_u32 s26, 0\n"
      "s_cselect_b32 s28, 24, 0\n"
      "v_cndmask_b32 v47, v44, v45, vcc\n"
      "v_cndmask_b32 v46, v44, v45, s[30:31]\n"
      "v_cmp_le_u32 vcc, s28, v36\n"
      "v_cmp_gt_u32 s[30:31], s27, v36\n"
      "s_cmp_eq_u32 s26, 97280\n"
      "s_cselect_b32 s27, 24, 64\n"
      "v_cmp_gt_u32 s[34:35], s27, v36\n"
      "v_subrev_u32 v45, 256, v44\n"
      "v_cndmask_b32 v8, 2.0, v47, vcc\n"
      "v_cndmask_b32 v48, v45, v46, s[30:31]\n"
      "v_cndmask_b32 v9, 2.0, v48, s[34:35]\n"
      "s_add_u32 s34, s19, s21\n"
      "s_cmp_eq_u32 s19, 0\n"
      "s_cselect_b32 s36, 0x80000000, s54\n"
      "s_waitcnt lgkmcnt(0)\n"
      "s_load_dword s44, s[4:5], s34\n"
      "s_and_b32 s13, s13, 0xffff\n"
      "s_add_u32 m0, s63, 24832\n"
      "s_add_u32 s52, s63, 0\n"
      "buffer_load_dwordx4 v17, s[12:15], s52 offen offset:0 lds\n"
      "buffer_load_dwordx4 v18, s[12:15], s52 offen offset:1024 lds\n"
      "buffer_load_dwordx4 v19, s[12:15], s52 offen offset:2048 lds\n"
      "s_add_u32 s4, s4, -4096\n"
      "s_addc_u32 s5, s5, -1\n"
      "s_and_b32 s5, s5, 0xffff\n"
      "s_add_u32 s53, s54, 196608\n"
      "s_add_u32 m0, s37, 82432\n"
      "s_nop 0\n"
      "buffer_load_dwordx4 v8, s[4:7], s53 offen offset:0 nt lds\n"
      "buffer_load_dwordx4 v9, s[4:7], s53 offen offset:1024 nt lds\n"
      "s_add_u32 s53, s54, 393216\n"
      "s_add_u32 m0, s37, 86536\n"
      "s_nop 0\n"
      "buffer_load_dwordx4 v8, s[4:7], s53 offen offset:0 nt lds\n"
      "buffer_load_dwordx4 v9, s[4:7], s53 offen offset:1024 nt lds\n"
      "s_add_u32 s53, s54, 589824\n"
      "s_add_u32 m0, s37, 90760\n"
      "s_nop 0\n"
      "buffer_load_dwordx4 v8, s[4:7], s53 offen offset:0 nt lds\n"
      "buffer_load_dwordx4 v9, s[4:7], s53 offen offset:1024 nt lds\n"
      "s_add_u32 s53, s54, 786432\n"
      "s_add_u32 m0, s37, 95104\n"
      "s_nop 0\n"
      "buffer_load_dwordx4 v8, s[4:7], s53 offen offset:0 nt lds\n"
      "buffer_load_dwordx4 v9, s[4:7], s53 offen offset:1024 nt lds\n"
      "s_add_u32 s53, s36, 0\n"
      "s_add_u32 m0, s37, 150272\n"
      "s_nop 0\n"
      "buffer_load_dwordx4 v8, s[4:7], s53 offen offset:0 nt lds\n"
      "buffer_load_dwordx4 v9, s[4:7], s53 offen offset:1024 nt lds\n"
      "v_lshlrev_b32 v43, 4, v37\n"
      "v_lshlrev_b32 v44, 2, v37\n"
      "v_and_b32 v43, 16, v43\n"
      "v_and_or_b32 v43, v44, 8, v43\n"
      "v_mul_u32_u24 v44, 192, v38\n"
      "v_mul_u32_u24 v45, 1536, v39\n"
      "s_lshl_b32 s27, s18, 5\n"
      "v_lshlrev_b32 v46, 1, v36\n"
      "v_add3_u32 v43, v43, v44, v45\n"
      "v_bitop3_b32 v46, v46, s27, 32 bitop3:0x6c\n"
      "v_add_u32 v60, v43, v46\n"
      "v_add_u32 v61, 57344, v60\n"
      "v_add_u32 v62, 114688, v60\n"
      "v_lshlrev_b32 v43, 4, v37\n"
      "s_mul_i32 s26, s17, 0x8080\n"
      "s_sub_u32 s26, 0xbf80, s26\n"
      "s_mov_b32 s27, 0\n"
      "s_lshl_b32 s34, s17, 7\n"
      "s_mov_b32 s29, 0\n"
      "s_sub_u32 s28, s26, s34\n"
      "s_mov_b32 s31, 0\n"
      "s_sub_u32 s30, s28, s34\n"
      "v_lshlrev_b64 v[44:45], v43, s[26:27]\n"
      "v_lshlrev_b64 v[46:47], v43, s[28:29]\n"
      "s_xor_b32 s28, s28, 0x8000\n"
      "v_lshlrev_b64 v[48:49], v43, s[30:31]\n"
      "v_lshlrev_b64 v[50:51], v43, s[28:29]\n"
      "v_accvgpr_write_b32 a244, v44\n"
      "v_accvgpr_write_b32 a245, v45\n"
      "v_accvgpr_write_b32 a246, v46\n"
      "v_accvgpr_write_b32 a247, v47\n"
      "v_accvgpr_write_b32 a248, v48\n"
      "v_accvgpr_write_b32 a249, v49\n"
      "v_accvgpr_write_b32 a250, v50\n"
      "v_accvgpr_write_b32 a251, v51\n"
      "s_lshl_b32 s26, s18, 3\n"
      "v_and_b32 v42, 15, v36\n"
      "v_xor_b32 v42, s26, v42\n"
      "s_lshl_b32 s26, s17, 13\n"
      "s_add_u32 s26, s26, 24832\n"
      "s_mul_i32 s27, s17, 12672\n"
      "v_mul_u32_u24 v44, 192, v42\n"
      "v_lshlrev_b32 v43, 3, v42\n"
      "v_and_b32 v45, 48, v36\n"
      "s_add_u32 s27, s27, 82432\n"
      "v_bitop3_b32 v46, v43, v45, 32 bitop3:0x6c\n"
      "v_add3_u32 v0, v46, v44, s26\n"
      "v_add3_u32 v3, v46, v44, s27\n"
      "v_add_u32 v43, 16, v43\n"
      "v_add_u32 v44, 384, v44\n"
      "v_bitop3_b32 v46, v43, v45, 32 bitop3:0x6c\n"
      "v_add3_u32 v1, v46, v44, s26\n"
      "v_add3_u32 v4, v46, v44, s27\n"
      "v_add_u32 v43, 16, v43\n"
      "v_add_u32 v44, 384, v44\n"
      "v_bitop3_b32 v46, v43, v45, 32 bitop3:0x6c\n"
      "v_add3_u32 v2, v46, v44, s26\n"
      "v_add3_u32 v5, v46, v44, s27\n"
      "v_lshlrev_b32 v43, 31, v36\n"
      "v_and_b32 v44, 2, v36\n"
      "v_or_b32 v43, 0x3f803f80, v43\n"
      "v_mul_u32_u24 v45, 0xc00080, v44\n"
      "v_lshl_add_u32 v44, v44, 22, v43\n"
      "v_add_u32 v45, v45, v43\n"
      "v_accvgpr_write_b32 a216, v44\n"
      "v_accvgpr_write_b32 a217, v45\n"
      "v_lshlrev_b32 v43, 3, v38\n"
      "s_mov_b32 s26, 806420736\n"
      "v_bfe_u32 v43, s26, v43, 8\n"
      "v_lshlrev_b32 v44, 12, v38\n"
      "s_lshl_b32 s28, s18, 8\n"
      "s_add_u32 s28, s28, 82432\n"
      "v_lshl_add_u32 v43, v43, 3, v44\n"
      "v_lshl_add_u32 v44, v39, 2, v37\n"
      "v_lshl_add_u32 v43, v44, 4, v43\n"
      "v_add_u32 v7, s28, v43\n"
      "s_mul_i32 s26, s17, 70144\n"
      "s_lshl_b32 s27, s18, 8\n"
      "s_sub_u32 s26, 70144, s26\n"
      "s_add_u32 s26, s26, s27\n"
      "v_lshl_add_u32 v6, v36, 2, s26\n"
      "v_lshrrev_b32 v43, 3, v36\n"
      "v_and_b32 v44, 7, v36\n"
      "v_mul_u32_u24 v45, 384, v43\n"
      "v_lshl_add_u32 v16, v44, 4, v45\n"
      "v_and_b32 v44, 2, v36\n"
      "v_lshlrev_b32 v43, 3, v43\n"
      "v_lshlrev_b32 v45, 4, v44\n"
      "v_xor_b32 v43, v43, v45\n"
      "v_lshl_add_u32 v43, v44, 7, v43\n"
      "v_and_b32 v44, 1, v36\n"
      "s_lshl_b32 s26, s18, 11\n"
      "v_lshl_add_u32 v43, v44, 6, v43\n"
      "s_add_u32 s26, s26, 12288\n"
      "v_bfe_u32 v44, v36, 2, 1\n"
      "v_add_u32 v43, s26, v43\n"
      "v_mul_i32_i24 v45, 8196, v44\n"
      "s_mov_b32 s27, 0\n"
      "v_add3_u32 v10, v43, v45, s27\n"
      "v_add_u32 v13, 128, v10\n"
      "v_mul_i32_i24 v45, -4092, v44\n"
      "s_mov_b32 s27, 4096\n"
      "v_add3_u32 v11, v43, v45, s27\n"
      "v_add_u32 v14, 128, v11\n"
      "v_mul_i32_i24 v45, -4100, v44\n"
      "s_mov_b32 s27, 8200\n"
      "v_add3_u32 v12, v43, v45, s27\n"
      "v_add_u32 v15, 128, v12\n"
      "s_mul_i32 s62, s18, 393216\n"
      "s_add_u32 s62, s62, s19\n"
      "s_add_u32 s62, s62, s21\n"
      "s_mov_b32 s40, 0x80000000\n"
      "s_and_b32 s9, s9, 0xffff\n"
      "s_mov_b32 s45, 0xbf2aaaab\n"
      "s_mov_b32 s46, 0xbe800000\n"
      "s_mov_b32 s47, 0x3f2aaaab\n"
      "s_mov_b32 s48, 0xc0c00000\n"
      "s_mov_b32 s49, 0x3f000000\n"
      "s_add_u32 m0, s63, 6144\n"
      "s_add_u32 s52, s63, 6144\n"
      "buffer_load_dwordx4 v17, s[12:15], s52 offen offset:0 lds\n"
      "buffer_load_dwordx4 v18, s[12:15], s52 offen offset:1024 lds\n"
      "buffer_load_dwordx4 v19, s[12:15], s52 offen offset:2048 lds\n"
      "LDpro_%=:\n"
      "s_waitcnt vmcnt(13)\n"
      "s_barrier\n"
      "ds_read_b64_tr_b16 a[0:1], v60 offset:24832\n"
      "ds_read_b64_tr_b16 a[2:3], v60 offset:25600\n"
      "ds_read_b64_tr_b16 a[224:225], v60 offset:0\n"
      "ds_read_b64_tr_b16 a[226:227], v60 offset:768\n"
      "ds_read_b64_tr_b16 a[220:221], v60 offset:30976\n"
      "ds_read_b64_tr_b16 a[222:223], v60 offset:31744\n"
      "ds_read_b64_tr_b16 a[4:5], v60 offset:24896\n"
      "ds_read_b64_tr_b16 a[6:7], v60 offset:25664\n"
      "ds_read_b64_tr_b16 a[232:233], v60 offset:64\n"
      "ds_read_b64_tr_b16 a[234:235], v60 offset:832\n"
      "ds_read_b64_tr_b16 a[228:229], v60 offset:31040\n"
      "ds_read_b64_tr_b16 a[230:231], v60 offset:31808\n"
      "s_add_u32 m0, s63, 53504\n"
      "s_add_u32 s52, s63, 122880\n"
      "s_nop 0\n"
      "s_waitcnt lgkmcnt(10)\n"
      "v_mfma_f32_4x4x4_16b_bf16 v[28:31], a[244:245], a[0:1], 0\n"
      "v_mfma_f32_4x4x4_16b_bf16 v[32:35], a[244:245], a[2:3], 0\n"
      "buffer_load_dwordx4 v17, s[12:15], s52 offen offset:0 lds\n"
      "s_waitcnt lgkmcnt(8)\n"
      "v_mfma_f32_4x4x4_16b_bf16 v[28:31], a[248:249], a[224:225], v[28:31]\n"
      "ds_read_b64_tr_b16 a[8:9], v60 offset:24960\n"
      "v_mfma_f32_4x4x4_16b_bf16 v[32:35], a[248:249], a[226:227], v[32:35]\n"
      "buffer_load_dwordx4 v18, s[12:15], s52 offen offset:1024 lds\n"
      "s_waitcnt lgkmcnt(7)\n"
      "v_mfma_f32_4x4x4_16b_bf16 v[36:39], a[246:247], a[220:221], v[28:31]\n"
      "ds_read_b64_tr_b16 a[10:11], v60 offset:25728\n"
      "v_mfma_f32_4x4x4_16b_bf16 v[40:43], a[246:247], a[222:223], v[32:35]\n"
      "buffer_load_dwordx4 v19, s[12:15], s52 offen offset:2048 lds\n"
      "v_mfma_f32_4x4x4_16b_bf16 v[28:31], a[250:251], a[220:221], v[28:31]\n"
      "ds_read_b64_tr_b16 a[240:241], v60 offset:128\n"
      "v_mfma_f32_4x4x4_16b_bf16 v[32:35], a[250:251], a[222:223], v[32:35]\n"
      "ds_read_b64_tr_b16 a[242:243], v60 offset:896\n"
      "ds_read_b64_tr_b16 a[236:237], v60 offset:31104\n"
      "ds_read_b64_tr_b16 a[238:239], v60 offset:31872\n"
      "s_add_u32 m0, s63, 71936\n"
      "s_add_u32 s52, s63, 129024\n"
      "s_nop 0\n"
      "s_waitcnt lgkmcnt(5)\n"
      "v_mfma_f32_4x4x4_16b_bf16 v[44:47], a[244:245], a[4:5], 0\n"
      "v_mfma_f32_4x4x4_16b_bf16 v[48:51], a[244:245], a[6:7], 0\n"
      "buffer_load_dwordx4 v17, s[12:15], s52 offen offset:0 lds\n"
      "v_mfma_f32_4x4x4_16b_bf16 v[44:47], a[248:249], a[232:233], v[44:47]\n"
      "buffer_load_dwordx4 v18, s[12:15], s52 offen offset:1024 lds\n"
      "v_mfma_f32_4x4x4_16b_bf16 v[48:51], a[248:249], a[234:235], v[48:51]\n"
      "buffer_load_dwordx4 v19, s[12:15], s52 offen offset:2048 lds\n"
      "v_mfma_f32_4x4x4_16b_bf16 v[52:55], a[246:247], a[228:229], v[44:47]\n"
      "v_mfma_f32_4x4x4_16b_bf16 v[56:59], a[246:247], a[230:231], v[48:51]\n"
      "v_mfma_f32_4x4x4_16b_bf16 v[44:47], a[250:251], a[228:229], v[44:47]\n"
      "v_mfma_f32_4x4x4_16b_bf16 v[48:51], a[250:251], a[230:231], v[48:51]\n"
      "v_cvt_pk_bf16_f32 v252, v36, v40\n"
      "v_cvt_pk_bf16_f32 v253, v37, v41\n"
      "v_cvt_pk_bf16_f32 v254, v38, v42\n"
      "v_cvt_pk_bf16_f32 v255, v39, v43\n"
      "v_cvt_pk_bf16_f32 v248, v28, v32\n"
      "v_cvt_pk_bf16_f32 v249, v29, v33\n"
      "v_cvt_pk_bf16_f32 v250, v30, v34\n"
      "v_cvt_pk_bf16_f32 v251, v31, v35\n"
      "s_waitcnt lgkmcnt(0)\n"
      "v_mfma_f32_4x4x4_16b_bf16 v[28:31], a[244:245], a[8:9], 0\n"
      "v_mfma_f32_4x4x4_16b_bf16 v[32:35], a[244:245], a[10:11], 0\n"
      "s_nop 0\n"
      "v_mfma_f32_4x4x4_16b_bf16 v[28:31], a[248:249], a[240:241], v[28:31]\n"
      "v_mfma_f32_4x4x4_16b_bf16 v[32:35], a[248:249], a[242:243], v[32:35]\n"
      "s_nop 0\n"
      "s_nop 0\n"
      "v_mfma_f32_4x4x4_16b_bf16 v[36:39], a[246:247], a[236:237], v[28:31]\n"
      "v_mfma_f32_4x4x4_16b_bf16 v[40:43], a[246:247], a[238:239], v[32:35]\n"
      "v_mfma_f32_4x4x4_16b_bf16 v[28:31], a[250:251], a[236:237], v[28:31]\n"
      "v_mfma_f32_4x4x4_16b_bf16 v[32:35], a[250:251], a[238:239], v[32:35]\n"
      "v_cvt_pk_bf16_f32 v244, v52, v56\n"
      "v_cvt_pk_bf16_f32 v245, v53, v57\n"
      "v_cvt_pk_bf16_f32 v246, v54, v58\n"
      "v_cvt_pk_bf16_f32 v247, v55, v59\n"
      "v_cvt_pk_bf16_f32 v240, v44, v48\n"
      "v_cvt_pk_bf16_f32 v241, v45, v49\n"
      "v_cvt_pk_bf16_f32 v242, v46, v50\n"
      "v_cvt_pk_bf16_f32 v243, v47, v51\n"
      "s_waitcnt vmcnt(6)\n"
      "s_barrier\n"
      "ds_read_b64_tr_b16 a[12:13], v60 offset:6144\n"
      "ds_read_b64_tr_b16 a[14:15], v60 offset:6912\n"
      "ds_read_b64_tr_b16 a[224:225], v60 offset:18432\n"
      "ds_read_b64_tr_b16 a[226:227], v60 offset:19200\n"
      "ds_read_b64_tr_b16 a[220:221], v60 offset:12288\n"
      "ds_read_b64_tr_b16 a[222:223], v60 offset:13056\n"
      "s_add_u32 m0, s63, 128512\n"
      "s_add_u32 s52, s63, 135168\n"
      "s_waitcnt lgkmcnt(4)\n"
      "v_mfma_f32_4x4x4_16b_bf16 v[44:47], a[244:245], a[12:13], 0\n"
      "v_mfma_f32_4x4x4_16b_bf16 v[48:51], a[244:245], a[14:15], 0\n"
      "buffer_load_dwordx4 v17, s[12:15], s52 offen offset:0 lds\n"
      "s_waitcnt lgkmcnt(2)\n"
      "v_mfma_f32_4x4x4_16b_bf16 v[44:47], a[248:249], a[224:225], v[44:47]\n"
      "ds_read_b64_tr_b16 a[16:17], v60 offset:6208\n"
      "v_mfma_f32_4x4x4_16b_bf16 v[48:51], a[248:249], a[226:227], v[48:51]\n"
      "buffer_load_dwordx4 v18, s[12:15], s52 offen offset:1024 lds\n"
      "s_nop 0\n"
      "s_waitcnt lgkmcnt(1)\n"
      "v_mfma_f32_4x4x4_16b_bf16 v[52:55], a[246:247], a[220:221], v[44:47]\n"
      "ds_read_b64_tr_b16 a[18:19], v60 offset:6976\n"
      "v_mfma_f32_4x4x4_16b_bf16 v[56:59], a[246:247], a[222:223], v[48:51]\n"
      "buffer_load_dwordx4 v19, s[12:15], s52 offen offset:2048 lds\n"
      "v_mfma_f32_4x4x4_16b_bf16 v[44:47], a[250:251], a[220:221], v[44:47]\n"
      "ds_read_b64_tr_b16 a[232:233], v60 offset:18496\n"
      "v_mfma_f32_4x4x4_16b_bf16 v[48:51], a[250:251], a[222:223], v[48:51]\n"
      "ds_read_b64_tr_b16 a[234:235], v60 offset:19264\n"
      "ds_read_b64_tr_b16 a[228:229], v60 offset:12352\n"
      "ds_read_b64_tr_b16 a[230:231], v60 offset:13120\n"
      "ds_read_b64_tr_b16 a[20:21], v60 offset:6272\n"
      "ds_read_b64_tr_b16 a[22:23], v60 offset:7040\n"
      "ds_read_b64_tr_b16 a[240:241], v60 offset:18560\n"
      "ds_read_b64_tr_b16 a[242:243], v60 offset:19328\n"
      "ds_read_b64_tr_b16 a[236:237], v60 offset:12416\n"
      "ds_read_b64_tr_b16 a[238:239], v60 offset:13184\n"
      "ds_read_b64_tr_b16 a[164:165], v7 offset:2048\n"
      "ds_read_b64_tr_b16 a[166:167], v7 offset:2056\n"
      "ds_read_b128 a[204:207], v3 offset:55168\n"
      "v_cvt_pk_bf16_f32 v236, v36, v40\n"
      "v_cvt_pk_bf16_f32 v237, v37, v41\n"
      "v_cvt_pk_bf16_f32 v238, v38, v42\n"
      "v_cvt_pk_bf16_f32 v239, v39, v43\n"
      "v_cvt_pk_bf16_f32 v232, v28, v32\n"
      "v_cvt_pk_bf16_f32 v233, v29, v33\n"
      "v_cvt_pk_bf16_f32 v234, v30, v34\n"
      "v_cvt_pk_bf16_f32 v235, v31, v35\n"
      "s_waitcnt vmcnt(6)\n"
      "s_barrier\n"
      "s_nop 0\n"
      "s_waitcnt lgkmcnt(8)\n"
      "v_mfma_f32_4x4x4_16b_bf16 v[28:31], a[244:245], a[16:17], 0\n"
      "v_mfma_f32_4x4x4_16b_bf16 v[32:35], a[244:245], a[18:19], 0\n"
      "ds_read_b64_tr_b16 a[24:25], v60 offset:41216\n"
      "v_mfma_f32_4x4x4_16b_bf16 v[28:31], a[248:249], a[232:233], v[28:31]\n"
      "ds_read_b64_tr_b16 a[26:27], v60 offset:41984\n"
      "v_mfma_f32_4x4x4_16b_bf16 v[32:35], a[248:249], a[234:235], v[32:35]\n"
      "ds_read_b64_tr_b16 a[224:225], v60 offset:53504\n"
      "v_mfma_f32_4x4x4_16b_bf16 v[36:39], a[246:247], a[228:229], v[28:31]\n"
      "ds_read_b64_tr_b16 a[226:227], v60 offset:54272\n"
      "v_mfma_f32_4x4x4_16b_bf16 v[40:43], a[246:247], a[230:231], v[32:35]\n"
      "ds_read_b64_tr_b16 a[220:221], v60 offset:47360\n"
      "v_mfma_f32_4x4x4_16b_bf16 v[28:31], a[250:251], a[228:229], v[28:31]\n"
      "ds_read_b64_tr_b16 a[222:223], v60 offset:48128\n"
      "v_mfma_f32_4x4x4_16b_bf16 v[32:35], a[250:251], a[230:231], v[32:35]\n"
      "ds_read_b64_tr_b16 a[160:161], v7 offset:2560\n"
      "s_waitcnt lgkmcnt(7)\n"
      "v_mfma_f32_4x4x4_16b_bf16 v[20:23], a[216:217], a[164:165], 0\n"
      "ds_read_b64_tr_b16 a[162:163], v7 offset:2568\n"
      "v_mfma_f32_4x4x4_16b_bf16 v[24:27], a[216:217], a[166:167], 0\n"
      "ds_read_b128 a[208:211], v3 offset:55232\n"
      "v_cvt_pk_bf16_f32 v228, v52, v56\n"
      "v_cvt_pk_bf16_f32 v229, v53, v57\n"
      "v_cvt_pk_bf16_f32 v230, v54, v58\n"
      "v_cvt_pk_bf16_f32 v231, v55, v59\n"
      "v_cvt_pk_bf16_f32 v224, v44, v48\n"
      "v_cvt_pk_bf16_f32 v225, v45, v49\n"
      "v_cvt_pk_bf16_f32 v226, v46, v50\n"
      "v_cvt_pk_bf16_f32 v227, v47, v51\n"
      "s_add_u32 m0, s63, 0\n"
      "s_add_u32 s52, s63, 141312\n"
      "v_mfma_f32_4x4x4_16b_bf16 v[44:47], a[244:245], a[20:21], 0\n"
      "v_mfma_f32_4x4x4_16b_bf16 v[48:51], a[244:245], a[22:23], 0\n"
      "buffer_load_dwordx4 v17, s[12:15], s52 offen offset:0 lds\n"
      "v_mfma_f32_4x4x4_16b_bf16 v[44:47], a[248:249], a[240:241], v[44:47]\n"
      "ds_read_b64_tr_b16 a[28:29], v60 offset:41280\n"
      "v_mfma_f32_4x4x4_16b_bf16 v[48:51], a[248:249], a[242:243], v[48:51]\n"
      "buffer_load_dwordx4 v18, s[12:15], s52 offen offset:1024 lds\n"
      "v_mfma_f32_4x4x4_16b_bf16 v[52:55], a[246:247], a[236:237], v[44:47]\n"
      "ds_read_b64_tr_b16 a[30:31], v60 offset:42048\n"
      "v_mfma_f32_4x4x4_16b_bf16 v[56:59], a[246:247], a[238:239], v[48:51]\n"
      "buffer_load_dwordx4 v19, s[12:15], s52 offen offset:2048 lds\n"
      "v_mfma_f32_4x4x4_16b_bf16 v[44:47], a[250:251], a[236:237], v[44:47]\n"
      "ds_read_b64_tr_b16 a[232:233], v60 offset:53568\n"
      "v_mfma_f32_4x4x4_16b_bf16 v[48:51], a[250:251], a[238:239], v[48:51]\n"
      "s_add_u32 s53, s54, 983040\n"
      "s_nop 0\n"
      "s_waitcnt lgkmcnt(3)\n"
      "v_mfma_f32_4x4x4_16b_bf16 v[100:103], a[216:217], a[160:161], 0\n"
      "ds_read_b64_tr_b16 a[234:235], v60 offset:54336\n"
      "v_mfma_f32_4x4x4_16b_bf16 v[104:107], a[216:217], a[162:163], 0\n"
      "s_add_u32 m0, s37, 99328\n"
      "ds_read_b64_tr_b16 a[228:229], v60 offset:47424\n"
      "buffer_load_dwordx4 v8, s[4:7], s53 offen offset:0 nt lds\n"
      "buffer_load_dwordx4 v9, s[4:7], s53 offen offset:1024 nt lds\n"
      "ds_read_b64_tr_b16 a[230:231], v60 offset:48192\n"
      "s_add_u32 s53, s54, 1179648\n"
      "ds_read_b64_tr_b16 a[156:157], v7 offset:3072\n"
      "s_add_u32 m0, s37, 103432\n"
      "ds_read_b64_tr_b16 a[158:159], v7 offset:3080\n"
      "buffer_load_dwordx4 v8, s[4:7], s53 offen offset:0 nt lds\n"
      "buffer_load_dwordx4 v9, s[4:7], s53 offen offset:1024 nt lds\n"
      "v_cvt_pk_bf16_f32 v220, v36, v40\n"
      "v_cvt_pk_bf16_f32 v221, v37, v41\n"
      "v_cvt_pk_bf16_f32 v222, v38, v42\n"
      "v_cvt_pk_bf16_f32 v223, v39, v43\n"
      "v_cvt_pk_bf16_f32 v216, v28, v32\n"
      "v_cvt_pk_bf16_f32 v217, v29, v33\n"
      "v_mfma_f32_16x16x32_bf16 v[64:67], a[0:3], a[204:207], 0\n"
      "v_cvt_pk_bf16_f32 v218, v30, v34\n"
      "v_cvt_pk_bf16_f32 v219, v31, v35\n"
      "v_mfma_f32_16x16x32_bf16 v[68:71], a[4:7], a[204:207], 0\n"
      "s_add_u32 m0, s37, 107656\n"
      "v_cvt_pk_bf16_f32 v20, v20, v24\n"
      "v_cvt_pk_bf16_f32 v21, v21, v25\n"
      "v_mfma_f32_16x16x32_bf16 v[72:75], a[8:11], a[204:207], 0\n"
      "s_add_u32 s53, s54, 1376256\n"
      "v_cvt_pk_bf16_f32 v22, v22, v26\n"
      "v_cvt_pk_bf16_f32 v23, v23, v27\n"
      "v_mfma_f32_4x4x4_16b_bf16 v[28:31], a[244:245], a[24:25], 0\n"
      "v_mfma_f32_4x4x4_16b_bf16 v[32:35], a[244:245], a[26:27], 0\n"
      "buffer_load_dwordx4 v8, s[4:7], s53 offen offset:0 nt lds\n"
      "buffer_load_dwordx4 v9, s[4:7], s53 offen offset:1024 nt lds\n"
      "v_mfma_f32_4x4x4_16b_bf16 v[28:31], a[248:249], a[224:225], v[28:31]\n"
      "ds_read_b64_tr_b16 a[32:33], v60 offset:41344\n"
      "v_mfma_f32_4x4x4_16b_bf16 v[32:35], a[248:249], a[226:227], v[32:35]\n"
      "s_add_u32 s53, s54, 1572864\n"
      "v_mfma_f32_4x4x4_16b_bf16 v[36:39], a[246:247], a[220:221], v[28:31]\n"
      "ds_read_b64_tr_b16 a[34:35], v60 offset:42112\n"
      "v_mfma_f32_4x4x4_16b_bf16 v[40:43], a[246:247], a[222:223], v[32:35]\n"
      "s_add_u32 m0, s37, 112000\n"
      "v_mfma_f32_4x4x4_16b_bf16 v[28:31], a[250:251], a[220:221], v[28:31]\n"
      "ds_read_b64_tr_b16 a[240:241], v60 offset:53632\n"
      "v_mfma_f32_4x4x4_16b_bf16 v[32:35], a[250:251], a[222:223], v[32:35]\n"
      "buffer_load_dwordx4 v8, s[4:7], s53 offen offset:0 nt lds\n"
      "buffer_load_dwordx4 v9, s[4:7], s53 offen offset:1024 nt lds\n"
      "s_nop 0\n"
      "s_waitcnt lgkmcnt(3)\n"
      "v_mfma_f32_4x4x4_16b_bf16 v[108:111], a[216:217], a[156:157], 0\n"
      "ds_read_b64_tr_b16 a[242:243], v60 offset:54400\n"
      "v_mfma_f32_4x4x4_16b_bf16 v[112:115], a[216:217], a[158:159], 0\n"
      "ds_read_b64_tr_b16 a[236:237], v60 offset:47488\n"
      "ds_read_b64_tr_b16 a[238:239], v60 offset:48256\n"
      "ds_read_b64_tr_b16 a[152:153], v7 offset:3584\n"
      "ds_read_b64_tr_b16 a[154:155], v7 offset:3592\n"
      "s_mov_b32 m0, s64\n"
      "s_nop 0\n"
      "ds_write_addtid_b32 v20 offset:2048\n"
      "ds_write_addtid_b32 v21 offset:6144\n"
      "ds_write_addtid_b32 v22 offset:10240\n"
      "ds_write_addtid_b32 v23 offset:14336\n"
      "v_cvt_pk_bf16_f32 v212, v52, v56\n"
      "v_cvt_pk_bf16_f32 v213, v53, v57\n"
      "v_cvt_pk_bf16_f32 v214, v54, v58\n"
      "v_cvt_pk_bf16_f32 v215, v55, v59\n"
      "v_cvt_pk_bf16_f32 v208, v44, v48\n"
      "v_cvt_pk_bf16_f32 v209, v45, v49\n"
      "v_mfma_f32_16x16x32_bf16 v[64:67], a[12:15], a[208:211], v[64:67]\n"
      "v_cvt_pk_bf16_f32 v210, v46, v50\n"
      "v_cvt_pk_bf16_f32 v211, v47, v51\n"
      "v_mfma_f32_16x16x32_bf16 v[68:71], a[16:19], a[208:211], v[68:71]\n"
      "v_cvt_pk_bf16_f32 v100, v100, v104\n"
      "v_cvt_pk_bf16_f32 v101, v101, v105\n"
      "v_mfma_f32_16x16x32_bf16 v[72:75], a[20:23], a[208:211], v[72:75]\n"
      "s_waitcnt vmcnt(14)\n"
      "v_cvt_pk_bf16_f32 v102, v102, v106\n"
      "v_cvt_pk_bf16_f32 v103, v103, v107\n"
      "s_barrier\n"
      "v_mfma_f32_4x4x4_16b_bf16 v[44:47], a[244:245], a[28:29], 0\n"
      "v_mfma_f32_4x4x4_16b_bf16 v[48:51], a[244:245], a[30:31], 0\n"
      "ds_read_b64_tr_b16 a[36:37], v61 offset:2304\n"
      "v_mfma_f32_4x4x4_16b_bf16 v[44:47], a[248:249], a[232:233], v[44:47]\n"
      "ds_read_b64_tr_b16 a[38:39], v61 offset:3072\n"
      "v_mfma_f32_4x4x4_16b_bf16 v[48:51], a[248:249], a[234:235], v[48:51]\n"
      "ds_read_b64_tr_b16 a[224:225], v61 offset:14592\n"
      "v_mfma_f32_4x4x4_16b_bf16 v[52:55], a[246:247], a[228:229], v[44:47]\n"
      "ds_read_b64_tr_b16 a[226:227], v61 offset:15360\n"
      "v_mfma_f32_4x4x4_16b_bf16 v[56:59], a[246:247], a[230:231], v[48:51]\n"
      "ds_read_b64_tr_b16 a[220:221], v61 offset:8448\n"
      "v_mfma_f32_4x4x4_16b_bf16 v[44:47], a[250:251], a[228:229], v[44:47]\n"
      "ds_read_b64_tr_b16 a[222:223], v61 offset:9216\n"
      "v_mfma_f32_4x4x4_16b_bf16 v[48:51], a[250:251], a[230:231], v[48:51]\n"
      "ds_write_addtid_b32 v100 offset:2560\n"
      "s_nop 0\n"
      "s_waitcnt lgkmcnt(7)\n"
      "v_mfma_f32_4x4x4_16b_bf16 v[116:119], a[216:217], a[152:153], 0\n"
      "ds_write_addtid_b32 v101 offset:6656\n"
      "v_mfma_f32_4x4x4_16b_bf16 v[120:123], a[216:217], a[154:155], 0\n"
      "ds_write_addtid_b32 v102 offset:10752\n"
      "ds_write_addtid_b32 v103 offset:14848\n"
      "ds_read_b128 a[212:215], v3 offset:55296\n"
      "v_cvt_pk_bf16_f32 v204, v36, v40\n"
      "v_cvt_pk_bf16_f32 v205, v37, v41\n"
      "v_cvt_pk_bf16_f32 v206, v38, v42\n"
      "v_cvt_pk_bf16_f32 v207, v39, v43\n"
      "v_cvt_pk_bf16_f32 v200, v28, v32\n"
      "v_cvt_pk_bf16_f32 v201, v29, v33\n"
      "v_cvt_pk_bf16_f32 v202, v30, v34\n"
      "v_cvt_pk_bf16_f32 v203, v31, v35\n"
      "v_cvt_pk_bf16_f32 v108, v108, v112\n"
      "v_cvt_pk_bf16_f32 v109, v109, v113\n"
      "v_cvt_pk_bf16_f32 v110, v110, v114\n"
      "v_cvt_pk_bf16_f32 v111, v111, v115\n"
      "v_mfma_f32_4x4x4_16b_bf16 v[28:31], a[244:245], a[32:33], 0\n"
      "v_mfma_f32_4x4x4_16b_bf16 v[32:35], a[244:245], a[34:35], 0\n"
      "ds_read_b64_tr_b16 a[40:41], v61 offset:2368\n"
      "v_mfma_f32_4x4x4_16b_bf16 v[28:31], a[248:249], a[240:241], v[28:31]\n"
      "ds_read_b64_tr_b16 a[42:43], v61 offset:3136\n"
      "v_mfma_f32_4x4x4_16b_bf16 v[32:35], a[248:249], a[242:243], v[32:35]\n"
      "ds_read_b64_tr_b16 a[232:233], v61 offset:14656\n"
      "v_mfma_f32_4x4x4_16b_bf16 v[36:39], a[246:247], a[236:237], v[28:31]\n"
      "ds_read_b64_tr_b16 a[234:235], v61 offset:15424\n"
      "v_mfma_f32_4x4x4_16b_bf16 v[40:43], a[246:247], a[238:239], v[32:35]\n"
      "ds_read_b64_tr_b16 a[228:229], v61 offset:8512\n"
      "v_mfma_f32_4x4x4_16b_bf16 v[28:31], a[250:251], a[236:237], v[28:31]\n"
      "ds_read_b64_tr_b16 a[230:231], v61 offset:9280\n"
      "v_mfma_f32_4x4x4_16b_bf16 v[32:35], a[250:251], a[238:239], v[32:35]\n"
      "ds_write_addtid_b32 v108 offset:3072\n"
      "ds_write_addtid_b32 v109 offset:7168\n"
      "ds_write_addtid_b32 v110 offset:11264\n"
      "ds_write_addtid_b32 v111 offset:15360\n"
      "v_cvt_pk_bf16_f32 v196, v52, v56\n"
      "v_cvt_pk_bf16_f32 v197, v53, v57\n"
      "v_cvt_pk_bf16_f32 v198, v54, v58\n"
      "v_cvt_pk_bf16_f32 v199, v55, v59\n"
      "v_cvt_pk_bf16_f32 v192, v44, v48\n"
      "v_cvt_pk_bf16_f32 v193, v45, v49\n"
      "v_cvt_pk_bf16_f32 v194, v46, v50\n"
      "v_cvt_pk_bf16_f32 v195, v47, v51\n"
      "v_cvt_pk_bf16_f32 v116, v116, v120\n"
      "v_cvt_pk_bf16_f32 v117, v117, v121\n"
      "v_cvt_pk_bf16_f32 v118, v118, v122\n"
      "v_cvt_pk_bf16_f32 v119, v119, v123\n"
      "s_add_u32 m0, s63, 18432\n"
      "s_add_u32 s52, s63, 147456\n"
      "s_nop 0\n"
      "s_waitcnt lgkmcnt(3)\n"
      "v_mfma_f32_4x4x4_16b_bf16 v[44:47], a[244:245], a[36:37], 0\n"
      "v_mfma_f32_4x4x4_16b_bf16 v[48:51], a[244:245], a[38:39], 0\n"
      "buffer_load_dwordx4 v17, s[12:15], s52 offen offset:0 lds\n"
      "v_mfma_f32_4x4x4_16b_bf16 v[44:47], a[248:249], a[224:225], v[44:47]\n"
      "ds_read_b64_tr_b16 a[44:45], v61 offset:2432\n"
      "v_mfma_f32_4x4x4_16b_bf16 v[48:51], a[248:249], a[226:227], v[48:51]\n"
      "buffer_load_dwordx4 v18, s[12:15], s52 offen offset:1024 lds\n"
      "v_mfma_f32_4x4x4_16b_bf16 v[52:55], a[246:247], a[220:221], v[44:47]\n"
      "ds_read_b64_tr_b16 a[46:47], v61 offset:3200\n"
      "v_mfma_f32_4x4x4_16b_bf16 v[56:59], a[246:247], a[222:223], v[48:51]\n"
      "buffer_load_dwordx4 v19, s[12:15], s52 offen offset:2048 lds\n"
      "v_mfma_f32_4x4x4_16b_bf16 v[44:47], a[250:251], a[220:221], v[44:47]\n"
      "ds_read_b64_tr_b16 a[240:241], v61 offset:14720\n"
      "v_mfma_f32_4x4x4_16b_bf16 v[48:51], a[250:251], a[222:223], v[48:51]\n"
      "ds_read_b64_tr_b16 a[242:243], v61 offset:15488\n"
      "ds_read_b64_tr_b16 a[236:237], v61 offset:8576\n"
      "ds_read_b64_tr_b16 a[238:239], v61 offset:9344\n"
      "s_mov_b32 m0, s64\n"
      "s_nop 0\n"
      "ds_write_addtid_b32 v116 offset:3584\n"
      "ds_write_addtid_b32 v117 offset:7680\n"
      "ds_write_addtid_b32 v118 offset:11776\n"
      "ds_write_addtid_b32 v119 offset:15872\n"
      "v_cvt_pk_bf16_f32 v188, v36, v40\n"
      "v_cvt_pk_bf16_f32 v189, v37, v41\n"
      "v_mfma_f32_16x16x32_bf16 v[64:67], a[24:27], a[212:215], v[64:67]\n"
      "v_cvt_pk_bf16_f32 v190, v38, v42\n"
      "v_cvt_pk_bf16_f32 v191, v39, v43\n"
      "v_mfma_f32_16x16x32_bf16 v[68:71], a[28:31], a[212:215], v[68:71]\n"
      "s_waitcnt vmcnt(14)\n"
      "v_cvt_pk_bf16_f32 v184, v28, v32\n"
      "v_cvt_pk_bf16_f32 v185, v29, v33\n"
      "v_mfma_f32_16x16x32_bf16 v[72:75], a[32:35], a[212:215], v[72:75]\n"
      "s_waitcnt lgkmcnt(0)\n"
      "v_cvt_pk_bf16_f32 v186, v30, v34\n"
      "v_cvt_pk_bf16_f32 v187, v31, v35\n"
      "s_nop 0\n"
      "s_barrier\n"
      "v_mfma_f32_4x4x4_16b_bf16 v[28:31], a[244:245], a[40:41], 0\n"
      "v_mfma_f32_4x4x4_16b_bf16 v[32:35], a[244:245], a[42:43], 0\n"
      "ds_read_b64_tr_b16 a[48:49], v62 offset:1536\n"
      "v_mfma_f32_4x4x4_16b_bf16 v[28:31], a[248:249], a[232:233], v[28:31]\n"
      "ds_read_b64_tr_b16 a[50:51], v62 offset:2304\n"
      "v_mfma_f32_4x4x4_16b_bf16 v[32:35], a[248:249], a[234:235], v[32:35]\n"
      "ds_read_b64_tr_b16 a[224:225], v62 offset:13824\n"
      "v_mfma_f32_4x4x4_16b_bf16 v[36:39], a[246:247], a[228:229], v[28:31]\n"
      "ds_read_b64_tr_b16 a[226:227], v62 offset:14592\n"
      "v_mfma_f32_4x4x4_16b_bf16 v[40:43], a[246:247], a[230:231], v[32:35]\n"
      "ds_read_b64_tr_b16 a[220:221], v62 offset:7680\n"
      "v_mfma_f32_4x4x4_16b_bf16 v[28:31], a[250:251], a[228:229], v[28:31]\n"
      "ds_read_b64_tr_b16 a[222:223], v62 offset:8448\n"
      "v_mfma_f32_4x4x4_16b_bf16 v[32:35], a[250:251], a[230:231], v[32:35]\n"
      "ds_read_b128 a[192:195], v4 offset:55168\n"
      "ds_read_b128 a[196:199], v0 offset:0\n"
      "ds_read_b128 a[200:203], v0 offset:4096\n"
      "v_cvt_pk_bf16_f32 v180, v52, v56\n"
      "v_cvt_pk_bf16_f32 v181, v53, v57\n"
      "v_cvt_pk_bf16_f32 v182, v54, v58\n"
      "v_cvt_pk_bf16_f32 v183, v55, v59\n"
      "v_cvt_pk_bf16_f32 v176, v44, v48\n"
      "v_cvt_pk_bf16_f32 v177, v45, v49\n"
      "v_cvt_pk_bf16_f32 v178, v46, v50\n"
      "v_cvt_pk_bf16_f32 v179, v47, v51\n"
      "v_mfma_f32_4x4x4_16b_bf16 v[44:47], a[244:245], a[44:45], 0\n"
      "v_mfma_f32_4x4x4_16b_bf16 v[48:51], a[244:245], a[46:47], 0\n"
      "ds_read_b64_tr_b16 a[52:53], v62 offset:1600\n"
      "v_mfma_f32_4x4x4_16b_bf16 v[44:47], a[248:249], a[240:241], v[44:47]\n"
      "ds_read_b64_tr_b16 a[54:55], v62 offset:2368\n"
      "v_mfma_f32_4x4x4_16b_bf16 v[48:51], a[248:249], a[242:243], v[48:51]\n"
      "ds_read_b64_tr_b16 a[232:233], v62 offset:13888\n"
      "v_mfma_f32_4x4x4_16b_bf16 v[52:55], a[246:247], a[236:237], v[44:47]\n"
      "ds_read_b64_tr_b16 a[234:235], v62 offset:14656\n"
      "v_mfma_f32_4x4x4_16b_bf16 v[56:59], a[246:247], a[238:239], v[48:51]\n"
      "ds_read_b64_tr_b16 a[228:229], v62 offset:7744\n"
      "v_mfma_f32_4x4x4_16b_bf16 v[44:47], a[250:251], a[236:237], v[44:47]\n"
      "ds_read_b64_tr_b16 a[230:231], v62 offset:8512\n"
      "v_mfma_f32_4x4x4_16b_bf16 v[48:51], a[250:251], a[238:239], v[48:51]\n"
      "ds_read_b128 a[204:207], v0 offset:64\n"
      "ds_read_b128 a[208:211], v0 offset:4160\n"
      "ds_read_b128 a[212:215], v0 offset:128\n"
      "v_cvt_pk_bf16_f32 v172, v36, v40\n"
      "v_cvt_pk_bf16_f32 v173, v37, v41\n"
      "v_cvt_pk_bf16_f32 v174, v38, v42\n"
      "v_cvt_pk_bf16_f32 v175, v39, v43\n"
      "v_cvt_pk_bf16_f32 v168, v28, v32\n"
      "v_cvt_pk_bf16_f32 v169, v29, v33\n"
      "v_cvt_pk_bf16_f32 v170, v30, v34\n"
      "v_cvt_pk_bf16_f32 v171, v31, v35\n"
      "s_add_u32 m0, s63, 53504\n"
      "s_add_u32 s52, s63, 153600\n"
      "s_nop 0\n"
      "s_waitcnt lgkmcnt(4)\n"
      "v_mfma_f32_4x4x4_16b_bf16 v[28:31], a[244:245], a[48:49], 0\n"
      "v_mfma_f32_4x4x4_16b_bf16 v[32:35], a[244:245], a[50:51], 0\n"
      "buffer_load_dwordx4 v17, s[12:15], s52 offen offset:0 lds\n"
      "v_mfma_f32_4x4x4_16b_bf16 v[28:31], a[248:249], a[224:225], v[28:31]\n"
      "ds_read_b64_tr_b16 a[56:57], v62 offset:1664\n"
      "v_mfma_f32_4x4x4_16b_bf16 v[32:35], a[248:249], a[226:227], v[32:35]\n"
      "buffer_load_dwordx4 v18, s[12:15], s52 offen offset:1024 lds\n"
      "v_mfma_f32_4x4x4_16b_bf16 v[36:39], a[246:247], a[220:221], v[28:31]\n"
      "ds_read_b64_tr_b16 a[58:59], v62 offset:2432\n"
      "v_mfma_f32_4x4x4_16b_bf16 v[40:43], a[246:247], a[222:223], v[32:35]\n"
      "buffer_load_dwordx4 v19, s[12:15], s52 offen offset:2048 lds\n"
      "v_mfma_f32_4x4x4_16b_bf16 v[28:31], a[250:251], a[220:221], v[28:31]\n"
      "ds_read_b64_tr_b16 a[240:241], v62 offset:13952\n"
      "v_mfma_f32_4x4x4_16b_bf16 v[32:35], a[250:251], a[222:223], v[32:35]\n"
      "ds_read_b64_tr_b16 a[242:243], v62 offset:14720\n"
      "ds_read_b64_tr_b16 a[236:237], v62 offset:7808\n"
      "ds_read_b64_tr_b16 a[238:239], v62 offset:8576\n"
      "v_mfma_f32_16x16x32_bf16 v[64:67], a[36:39], a[192:195], v[64:67]\n"
      "v_cvt_pk_bf16_f32 v164, v52, v56\n"
      "v_cvt_pk_bf16_f32 v165, v53, v57\n"
      "v_mfma_f32_16x16x32_bf16 v[68:71], a[40:43], a[192:195], v[68:71]\n"
      "v_cvt_pk_bf16_f32 v166, v54, v58\n"
      "v_cvt_pk_bf16_f32 v167, v55, v59\n"
      "v_mfma_f32_16x16x32_bf16 v[72:75], a[44:47], a[192:195], v[72:75]\n"
      "v_cvt_pk_bf16_f32 v160, v44, v48\n"
      "v_cvt_pk_bf16_f32 v161, v45, v49\n"
      "v_mfma_f32_16x16x32_bf16 v[76:79], v[252:255], a[196:199], 0\n"
      "s_waitcnt vmcnt(14)\n"
      "v_cvt_pk_bf16_f32 v162, v46, v50\n"
      "v_cvt_pk_bf16_f32 v163, v47, v51\n"
      "s_barrier\n"
      "s_add_u32 m0, s63, 71936\n"
      "s_add_u32 s52, s63, 159744\n"
      "v_mfma_f32_4x4x4_16b_bf16 v[44:47], a[244:245], a[52:53], 0\n"
      "v_mfma_f32_4x4x4_16b_bf16 v[48:51], a[244:245], a[54:55], 0\n"
      "buffer_load_dwordx4 v17, s[12:15], s52 offen offset:0 lds\n"
      "v_mfma_f32_4x4x4_16b_bf16 v[44:47], a[248:249], a[232:233], v[44:47]\n"
      "ds_read_b64_tr_b16 a[60:61], v62 offset:19968\n"
      "v_mfma_f32_4x4x4_16b_bf16 v[48:51], a[248:249], a[234:235], v[48:51]\n"
      "buffer_load_dwordx4 v18, s[12:15], s52 offen offset:1024 lds\n"
      "s_nop 0\n"
      "s_waitcnt lgkmcnt(1)\n"
      "v_mfma_f32_4x4x4_16b_bf16 v[52:55], a[246:247], a[228:229], v[44:47]\n"
      "ds_read_b64_tr_b16 a[62:63], v62 offset:20736\n"
      "v_mfma_f32_4x4x4_16b_bf16 v[56:59], a[246:247], a[230:231], v[48:51]\n"
      "buffer_load_dwordx4 v19, s[12:15], s52 offen offset:2048 lds\n"
      "v_mfma_f32_4x4x4_16b_bf16 v[44:47], a[250:251], a[228:229], v[44:47]\n"
      "ds_read_b64_tr_b16 a[224:225], v60 offset:0\n"
      "v_mfma_f32_4x4x4_16b_bf16 v[48:51], a[250:251], a[230:231], v[48:51]\n"
      "ds_read_b64_tr_b16 a[226:227], v60 offset:768\n"
      "ds_read_b64_tr_b16 a[220:221], v62 offset:26112\n"
      "ds_read_b64_tr_b16 a[222:223], v62 offset:26880\n"
      "ds_read_b128 a[192:195], v0 offset:4224\n"
      "v_mfma_f32_16x16x32_bf16 v[80:83], v[244:247], a[196:199], 0\n"
      "v_cvt_pk_bf16_f32 v156, v36, v40\n"
      "v_cvt_pk_bf16_f32 v157, v37, v41\n"
      "v_mfma_f32_16x16x32_bf16 v[84:87], v[236:239], a[196:199], 0\n"
      "v_cvt_pk_bf16_f32 v158, v38, v42\n"
      "v_cvt_pk_bf16_f32 v159, v39, v43\n"
      "v_mfma_f32_16x16x32_bf16 v[88:91], v[248:251], a[200:203], 0\n"
      "v_cvt_pk_bf16_f32 v152, v28, v32\n"
      "v_cvt_pk_bf16_f32 v153, v29, v33\n"
      "v_mfma_f32_16x16x32_bf16 v[92:95], v[240:243], a[200:203], 0\n"
      "v_cvt_pk_bf16_f32 v154, v30, v34\n"
      "v_cvt_pk_bf16_f32 v155, v31, v35\n"
      "v_mfma_f32_4x4x4_16b_bf16 v[28:31], a[244:245], a[56:57], 0\n"
      "v_mfma_f32_4x4x4_16b_bf16 v[32:35], a[244:245], a[58:59], 0\n"
      "ds_read_b64_tr_b16 a[64:65], v62 offset:20032\n"
      "v_mfma_f32_4x4x4_16b_bf16 v[28:31], a[248:249], a[240:241], v[28:31]\n"
      "ds_read_b64_tr_b16 a[66:67], v62 offset:20800\n"
      "v_mfma_f32_4x4x4_16b_bf16 v[32:35], a[248:249], a[242:243], v[32:35]\n"
      "ds_read_b64_tr_b16 a[232:233], v60 offset:64\n"
      "v_mfma_f32_4x4x4_16b_bf16 v[36:39], a[246:247], a[236:237], v[28:31]\n"
      "ds_read_b64_tr_b16 a[234:235], v60 offset:832\n"
      "v_mfma_f32_4x4x4_16b_bf16 v[40:43], a[246:247], a[238:239], v[32:35]\n"
      "ds_read_b64_tr_b16 a[228:229], v62 offset:26176\n"
      "v_mfma_f32_4x4x4_16b_bf16 v[28:31], a[250:251], a[236:237], v[28:31]\n"
      "ds_read_b64_tr_b16 a[230:231], v62 offset:26944\n"
      "v_mfma_f32_4x4x4_16b_bf16 v[32:35], a[250:251], a[238:239], v[32:35]\n"
      "ds_read_b128 a[196:199], v1 offset:0\n"
      "v_mfma_f32_16x16x32_bf16 v[96:99], v[232:235], a[200:203], 0\n"
      "v_cvt_pk_bf16_f32 v148, v52, v56\n"
      "v_cvt_pk_bf16_f32 v149, v53, v57\n"
      "v_mfma_f32_16x16x32_bf16 v[76:79], v[228:231], a[204:207], v[76:79]\n"
      "v_cvt_pk_bf16_f32 v150, v54, v58\n"
      "v_cvt_pk_bf16_f32 v151, v55, v59\n"
      "v_mfma_f32_16x16x32_bf16 v[80:83], v[220:223], a[204:207], v[80:83]\n"
      "s_nop 0\n"
      "v_cvt_pk_bf16_f32 v144, v44, v48\n"
      "v_cvt_pk_bf16_f32 v145, v45, v49\n"
      "v_mfma_f32_16x16x32_bf16 v[84:87], v[212:215], a[204:207], v[84:87]\n"
      "s_waitcnt lgkmcnt(1)\n"
      "v_cvt_pk_bf16_f32 v146, v46, v50\n"
      "v_cvt_pk_bf16_f32 v147, v47, v51\n"
      "v_mfma_f32_4x4x4_16b_bf16 v[44:47], a[244:245], a[60:61], 0\n"
      "v_mfma_f32_4x4x4_16b_bf16 v[48:51], a[244:245], a[62:63], 0\n"
      "ds_read_b64_tr_b16 a[68:69], v62 offset:20096\n"
      "v_mfma_f32_4x4x4_16b_bf16 v[44:47], a[248:249], a[224:225], v[44:47]\n"
      "ds_read_b64_tr_b16 a[70:71], v62 offset:20864\n"
      "v_mfma_f32_4x4x4_16b_bf16 v[48:51], a[248:249], a[226:227], v[48:51]\n"
      "ds_read_b64_tr_b16 a[240:241], v60 offset:128\n"
      "v_mfma_f32_4x4x4_16b_bf16 v[52:55], a[246:247], a[220:221], v[44:47]\n"
      "ds_read_b64_tr_b16 a[242:243], v60 offset:896\n"
      "v_mfma_f32_4x4x4_16b_bf16 v[56:59], a[246:247], a[222:223], v[48:51]\n"
      "ds_read_b64_tr_b16 a[236:237], v62 offset:26240\n"
      "v_mfma_f32_4x4x4_16b_bf16 v[44:47], a[250:251], a[220:221], v[44:47]\n"
      "ds_read_b64_tr_b16 a[238:239], v62 offset:27008\n"
      "v_mfma_f32_4x4x4_16b_bf16 v[48:51], a[250:251], a[222:223], v[48:51]\n"
      "ds_read_b128 a[200:203], v1 offset:4096\n"
      "ds_read_b128 a[204:207], v4 offset:55232\n"
      "v_mfma_f32_16x16x32_bf16 v[88:91], v[224:227], a[208:211], v[88:91]\n"
      "v_cvt_pk_bf16_f32 v140, v36, v40\n"
      "v_cvt_pk_bf16_f32 v141, v37, v41\n"
      "v_mfma_f32_16x16x32_bf16 v[92:95], v[216:219], a[208:211], v[92:95]\n"
      "v_cvt_pk_bf16_f32 v142, v38, v42\n"
      "v_cvt_pk_bf16_f32 v143, v39, v43\n"
      "v_mfma_f32_16x16x32_bf16 v[96:99], v[208:211], a[208:211], v[96:99]\n"
      "v_cvt_pk_bf16_f32 v136, v28, v32\n"
      "v_cvt_pk_bf16_f32 v137, v29, v33\n"
      "v_mfma_f32_16x16x32_bf16 v[76:79], v[204:207], a[212:215], v[76:79]\n"
      "s_waitcnt vmcnt(6)\n"
      "v_cvt_pk_bf16_f32 v138, v30, v34\n"
      "v_cvt_pk_bf16_f32 v139, v31, v35\n"
      "s_barrier\n"
      "s_add_u32 s53, s54, 1769472\n"
      "s_add_u32 m0, s37, 116224\n"
      "v_mfma_f32_4x4x4_16b_bf16 v[28:31], a[244:245], a[64:65], 0\n"
      "v_mfma_f32_4x4x4_16b_bf16 v[32:35], a[244:245], a[66:67], 0\n"
      "buffer_load_dwordx4 v8, s[4:7], s53 offen offset:0 nt lds\n"
      "buffer_load_dwordx4 v9, s[4:7], s53 offen offset:1024 nt lds\n"
      "v_mfma_f32_4x4x4_16b_bf16 v[28:31], a[248:249], a[232:233], v[28:31]\n"
      "ds_read_b64_tr_b16 a[72:73], v60 offset:6144\n"
      "v_mfma_f32_4x4x4_16b_bf16 v[32:35], a[248:249], a[234:235], v[32:35]\n"
      "ds_read_b64_tr_b16 a[74:75], v60 offset:6912\n"
      "v_mfma_f32_4x4x4_16b_bf16 v[36:39], a[246:247], a[228:229], v[28:31]\n"
      "ds_read_b64_tr_b16 a[224:225], v60 offset:18432\n"
      "v_mfma_f32_4x4x4_16b_bf16 v[40:43], a[246:247], a[230:231], v[32:35]\n"
      "ds_read_b64_tr_b16 a[226:227], v60 offset:19200\n"
      "v_mfma_f32_4x4x4_16b_bf16 v[28:31], a[250:251], a[228:229], v[28:31]\n"
      "ds_read_b64_tr_b16 a[220:221], v60 offset:12288\n"
      "v_mfma_f32_4x4x4_16b_bf16 v[32:35], a[250:251], a[230:231], v[32:35]\n"
      "ds_read_b64_tr_b16 a[222:223], v60 offset:13056\n"
      "ds_read_b128 a[208:211], v1 offset:64\n"
      "v_mfma_f32_16x16x32_bf16 v[80:83], v[196:199], a[212:215], v[80:83]\n"
      "v_cvt_pk_bf16_f32 v132, v52, v56\n"
      "v_cvt_pk_bf16_f32 v133, v53, v57\n"
      "v_mfma_f32_16x16x32_bf16 v[84:87], v[188:191], a[212:215], v[84:87]\n"
      "v_cvt_pk_bf16_f32 v134, v54, v58\n"
      "v_cvt_pk_bf16_f32 v135, v55, v59\n"
      "v_mfma_f32_16x16x32_bf16 v[88:91], v[200:203], a[192:195], v[88:91]\n"
      "s_add_u32 m0, s37, 120328\n"
      "v_cvt_pk_bf16_f32 v128, v44, v48\n"
      "v_cvt_pk_bf16_f32 v129, v45, v49\n"
      "v_mfma_f32_16x16x32_bf16 v[92:95], v[192:195], a[192:195], v[92:95]\n"
      "s_add_u32 s53, s54, 1966080\n"
      "v_cvt_pk_bf16_f32 v130, v46, v50\n"
      "v_cvt_pk_bf16_f32 v131, v47, v51\n"
      "s_nop 0\n"
      "s_waitcnt lgkmcnt(1)\n"
      "v_mfma_f32_4x4x4_16b_bf16 v[44:47], a[244:245], a[68:69], 0\n"
      "v_mfma_f32_4x4x4_16b_bf16 v[48:51], a[244:245], a[70:71], 0\n"
      "buffer_load_dwordx4 v8, s[4:7], s53 offen offset:0 nt lds\n"
      "buffer_load_dwordx4 v9, s[4:7], s53 offen offset:1024 nt lds\n"
      "v_mfma_f32_4x4x4_16b_bf16 v[44:47], a[248:249], a[240:241], v[44:47]\n"
      "ds_read_b64_tr_b16 a[76:77], v60 offset:6208\n"
      "v_mfma_f32_4x4x4_16b_bf16 v[48:51], a[248:249], a[242:243], v[48:51]\n"
      "ds_read_b64_tr_b16 a[78:79], v60 offset:6976\n"
      "v_mfma_f32_4x4x4_16b_bf16 v[52:55], a[246:247], a[236:237], v[44:47]\n"
      "ds_read_b64_tr_b16 a[232:233], v60 offset:18496\n"
      "v_mfma_f32_4x4x4_16b_bf16 v[56:59], a[246:247], a[238:239], v[48:51]\n"
      "ds_read_b64_tr_b16 a[234:235], v60 offset:19264\n"
      "v_mfma_f32_4x4x4_16b_bf16 v[44:47], a[250:251], a[236:237], v[44:47]\n"
      "ds_read_b64_tr_b16 a[228:229], v60 offset:12352\n"
      "v_mfma_f32_4x4x4_16b_bf16 v[48:51], a[250:251], a[238:239], v[48:51]\n"
      "ds_read_b64_tr_b16 a[230:231], v60 offset:13120\n"
      "ds_read_b128 a[212:215], v1 offset:4160\n"
      "v_mfma_f32_16x16x32_bf16 v[96:99], v[184:187], a[192:195], v[96:99]\n"
      "s_nop 0\n"
      "v_cvt_pk_bf16_f32 v124, v36, v40\n"
      "v_cvt_pk_bf16_f32 v125, v37, v41\n"
      "v_mfma_f32_16x16x32_bf16 v[76:79], v[180:183], a[196:199], v[76:79]\n"
      "s_nop 0\n"
      "v_cvt_pk_bf16_f32 v126, v38, v42\n"
      "v_cvt_pk_bf16_f32 v127, v39, v43\n"
      "v_mfma_f32_16x16x32_bf16 v[80:83], v[172:175], a[196:199], v[80:83]\n"
      "s_add_u32 m0, s37, 124552\n"
      "v_cvt_pk_bf16_f32 v120, v28, v32\n"
      "v_cvt_pk_bf16_f32 v121, v29, v33\n"
      "v_mfma_f32_16x16x32_bf16 v[84:87], v[164:167], a[196:199], v[84:87]\n"
      "s_add_u32 s53, s54, 2162688\n"
      "v_cvt_pk_bf16_f32 v122, v30, v34\n"
      "v_cvt_pk_bf16_f32 v123, v31, v35\n"
      "v_mfma_f32_4x4x4_16b_bf16 v[28:31], a[244:245], a[72:73], 0\n"
      "v_mfma_f32_4x4x4_16b_bf16 v[32:35], a[244:245], a[74:75], 0\n"
      "buffer_load_dwordx4 v8, s[4:7], s53 offen offset:0 nt lds\n"
      "buffer_load_dwordx4 v9, s[4:7], s53 offen offset:1024 nt lds\n"
      "v_mfma_f32_4x4x4_16b_bf16 v[28:31], a[248:249], a[224:225], v[28:31]\n"
      "ds_read_b64_tr_b16 a[80:81], v60 offset:6272\n"
      "v_mfma_f32_4x4x4_16b_bf16 v[32:35], a[248:249], a[226:227], v[32:35]\n"
      "ds_read_b64_tr_b16 a[82:83], v60 offset:7040\n"
      "v_mfma_f32_4x4x4_16b_bf16 v[36:39], a[246:247], a[220:221], v[28:31]\n"
      "ds_read_b64_tr_b16 a[240:241], v60 offset:18560\n"
      "v_mfma_f32_4x4x4_16b_bf16 v[40:43], a[246:247], a[222:223], v[32:35]\n"
      "ds_read_b64_tr_b16 a[242:243], v60 offset:19328\n"
      "v_mfma_f32_4x4x4_16b_bf16 v[28:31], a[250:251], a[220:221], v[28:31]\n"
      "ds_read_b64_tr_b16 a[236:237], v60 offset:12416\n"
      "v_mfma_f32_4x4x4_16b_bf16 v[32:35], a[250:251], a[222:223], v[32:35]\n"
      "ds_read_b64_tr_b16 a[238:239], v60 offset:13184\n"
      "ds_read_b128 a[192:195], v4 offset:55296\n"
      "ds_read_b128 a[196:199], v1 offset:128\n"
      "v_mfma_f32_16x16x32_bf16 v[88:91], v[176:179], a[200:203], v[88:91]\n"
      "s_nop 0\n"
      "v_cvt_pk_bf16_f32 v116, v52, v56\n"
      "v_cvt_pk_bf16_f32 v117, v53, v57\n"
      "v_mfma_f32_16x16x32_bf16 v[92:95], v[168:171], a[200:203], v[92:95]\n"
      "s_nop 0\n"
      "v_cvt_pk_bf16_f32 v118, v54, v58\n"
      "v_cvt_pk_bf16_f32 v119, v55, v59\n"
      "v_mfma_f32_16x16x32_bf16 v[96:99], v[160:163], a[200:203], v[96:99]\n"
      "s_nop 0\n"
      "v_cvt_pk_bf16_f32 v112, v44, v48\n"
      "v_cvt_pk_bf16_f32 v113, v45, v49\n"
      "v_mfma_f32_16x16x32_bf16 v[64:67], a[48:51], a[204:207], v[64:67]\n"
      "s_waitcnt vmcnt(9)\n"
      "v_cvt_pk_bf16_f32 v114, v46, v50\n"
      "v_cvt_pk_bf16_f32 v115, v47, v51\n"
      "s_barrier\n"
      "s_add_u32 s53, s54, 2359296\n"
      "s_add_u32 m0, s37, 128896\n"
      "s_waitcnt lgkmcnt(0)\n"
      "v_mfma_f32_4x4x4_16b_bf16 v[44:47], a[244:245], a[76:77], 0\n"
      "v_mfma_f32_4x4x4_16b_bf16 v[48:51], a[244:245], a[78:79], 0\n"
      "buffer_load_dwordx4 v8, s[4:7], s53 offen offset:0 nt lds\n"
      "buffer_load_dwordx4 v9, s[4:7], s53 offen offset:1024 nt lds\n"
      "v_mfma_f32_4x4x4_16b_bf16 v[44:47], a[248:249], a[232:233], v[44:47]\n"
      "ds_read_b64_tr_b16 a[84:85], v60 offset:41216\n"
      "v_mfma_f32_4x4x4_16b_bf16 v[48:51], a[248:249], a[234:235], v[48:51]\n"
      "ds_read_b64_tr_b16 a[86:87], v60 offset:41984\n"
      "v_mfma_f32_4x4x4_16b_bf16 v[52:55], a[246:247], a[228:229], v[44:47]\n"
      "ds_read_b64_tr_b16 a[224:225], v60 offset:53504\n"
      "v_mfma_f32_4x4x4_16b_bf16 v[56:59], a[246:247], a[230:231], v[48:51]\n"
      "ds_read_b64_tr_b16 a[226:227], v60 offset:54272\n"
      "v_mfma_f32_4x4x4_16b_bf16 v[44:47], a[250:251], a[228:229], v[44:47]\n"
      "ds_read_b64_tr_b16 a[220:221], v60 offset:47360\n"
      "v_mfma_f32_4x4x4_16b_bf16 v[48:51], a[250:251], a[230:231], v[48:51]\n"
      "ds_read_b64_tr_b16 a[222:223], v60 offset:48128\n"
      "ds_read_b128 a[200:203], v1 offset:4224\n"
      "v_mfma_f32_16x16x32_bf16 v[68:71], a[52:55], a[204:207], v[68:71]\n"
      "v_cvt_pk_bf16_f32 v108, v36, v40\n"
      "v_cvt_pk_bf16_f32 v109, v37, v41\n"
      "v_mfma_f32_16x16x32_bf16 v[72:75], a[56:59], a[204:207], v[72:75]\n"
      "v_cvt_pk_bf16_f32 v110, v38, v42\n"
      "v_cvt_pk_bf16_f32 v111, v39, v43\n"
      "v_mfma_f32_16x16x32_bf16 v[76:79], v[156:159], a[208:211], v[76:79]\n"
      "s_add_u32 m0, s37, 133120\n"
      "v_cvt_pk_bf16_f32 v104, v28, v32\n"
      "v_cvt_pk_bf16_f32 v105, v29, v33\n"
      "v_mfma_f32_16x16x32_bf16 v[80:83], v[148:151], a[208:211], v[80:83]\n"
      "s_add_u32 s53, s54, 2555904\n"
      "v_cvt_pk_bf16_f32 v106, v30, v34\n"
      "v_cvt_pk_bf16_f32 v107, v31, v35\n"
      "v_mfma_f32_4x4x4_16b_bf16 v[28:31], a[244:245], a[80:81], 0\n"
      "v_mfma_f32_4x4x4_16b_bf16 v[32:35], a[244:245], a[82:83], 0\n"
      "buffer_load_dwordx4 v8, s[4:7], s53 offen offset:0 nt lds\n"
      "buffer_load_dwordx4 v9, s[4:7], s53 offen offset:1024 nt lds\n"
      "v_mfma_f32_4x4x4_16b_bf16 v[28:31], a[248:249], a[240:241], v[28:31]\n"
      "ds_read_b64_tr_b16 a[88:89], v60 offset:41280\n"
      "v_mfma_f32_4x4x4_16b_bf16 v[32:35], a[248:249], a[242:243], v[32:35]\n"
      "ds_read_b64_tr_b16 a[90:91], v60 offset:42048\n"
      "v_mfma_f32_4x4x4_16b_bf16 v[36:39], a[246:247], a[236:237], v[28:31]\n"
      "ds_read_b64_tr_b16 a[232:233], v60 offset:53568\n"
      "v_mfma_f32_4x4x4_16b_bf16 v[40:43], a[246:247], a[238:239], v[32:35]\n"
      "ds_read_b64_tr_b16 a[234:235], v60 offset:54336\n"
      "v_mfma_f32_4x4x4_16b_bf16 v[28:31], a[250:251], a[236:237], v[28:31]\n"
      "ds_read_b64_tr_b16 a[228:229], v60 offset:47424\n"
      "v_mfma_f32_4x4x4_16b_bf16 v[32:35], a[250:251], a[238:239], v[32:35]\n"
      "ds_read_b64_tr_b16 a[230:231], v60 offset:48192\n"
      "ds_read_b128 a[204:207], v5 offset:55168\n"
      "v_mfma_f32_16x16x32_bf16 v[84:87], v[140:143], a[208:211], v[84:87]\n"
      "v_cvt_pk_bf16_f32 v100, v52, v56\n"
      "v_cvt_pk_bf16_f32 v101, v53, v57\n"
      "v_mfma_f32_16x16x32_bf16 v[88:91], v[152:155], a[212:215], v[88:91]\n"
      "v_cvt_pk_bf16_f32 v102, v54, v58\n"
      "v_cvt_pk_bf16_f32 v103, v55, v59\n"
      "v_mfma_f32_16x16x32_bf16 v[92:95], v[144:147], a[212:215], v[92:95]\n"
      "v_cvt_pk_bf16_f32 v44, v44, v48\n"
      "v_cvt_pk_bf16_f32 v45, v45, v49\n"
      "v_mfma_f32_16x16x32_bf16 v[96:99], v[136:139], a[212:215], v[96:99]\n"
      "v_cvt_pk_bf16_f32 v46, v46, v50\n"
      "v_cvt_pk_bf16_f32 v47, v47, v51\n"
      "v_mfma_f32_16x16x32_bf16 v[64:67], a[60:63], a[192:195], v[64:67]\n"
      "s_add_u32 m0, s37, 137224\n"
      "v_accvgpr_write_b32 a164, v44\n"
      "v_accvgpr_write_b32 a165, v45\n"
      "v_mfma_f32_16x16x32_bf16 v[68:71], a[64:67], a[192:195], v[68:71]\n"
      "s_add_u32 s53, s54, 2752512\n"
      "v_accvgpr_write_b32 a166, v46\n"
      "v_accvgpr_write_b32 a167, v47\n"
      "s_nop 0\n"
      "s_waitcnt lgkmcnt(0)\n"
      "v_mfma_f32_4x4x4_16b_bf16 v[44:47], a[244:245], a[84:85], 0\n"
      "v_mfma_f32_4x4x4_16b_bf16 v[48:51], a[244:245], a[86:87], 0\n"
      "buffer_load_dwordx4 v8, s[4:7], s53 offen offset:0 nt lds\n"
      "buffer_load_dwordx4 v9, s[4:7], s53 offen offset:1024 nt lds\n"
      "v_mfma_f32_4x4x4_16b_bf16 v[44:47], a[248:249], a[224:225], v[44:47]\n"
      "ds_read_b64_tr_b16 a[92:93], v60 offset:41344\n"
      "v_mfma_f32_4x4x4_16b_bf16 v[48:51], a[248:249], a[226:227], v[48:51]\n"
      "ds_read_b64_tr_b16 a[94:95], v60 offset:42112\n"
      "v_mfma_f32_4x4x4_16b_bf16 v[52:55], a[246:247], a[220:221], v[44:47]\n"
      "ds_read_b64_tr_b16 a[240:241], v60 offset:53632\n"
      "v_mfma_f32_4x4x4_16b_bf16 v[56:59], a[246:247], a[222:223], v[48:51]\n"
      "ds_read_b64_tr_b16 a[242:243], v60 offset:54400\n"
      "v_mfma_f32_4x4x4_16b_bf16 v[44:47], a[250:251], a[220:221], v[44:47]\n"
      "ds_read_b64_tr_b16 a[236:237], v60 offset:47488\n"
      "v_mfma_f32_4x4x4_16b_bf16 v[48:51], a[250:251], a[222:223], v[48:51]\n"
      "ds_read_b64_tr_b16 a[238:239], v60 offset:48256\n"
      "ds_read_b128 a[208:211], v2 offset:0\n"
      "ds_read_b128 a[212:215], v2 offset:4096\n"
      "v_cvt_pk_bf16_f32 v36, v36, v40\n"
      "v_cvt_pk_bf16_f32 v37, v37, v41\n"
      "v_cvt_pk_bf16_f32 v38, v38, v42\n"
      "v_cvt_pk_bf16_f32 v39, v39, v43\n"
      "v_mfma_f32_16x16x32_bf16 v[72:75], a[68:71], a[192:195], v[72:75]\n"
      "v_accvgpr_write_b32 a160, v36\n"
      "v_accvgpr_write_b32 a161, v37\n"
      "v_mfma_f32_16x16x32_bf16 v[76:79], v[132:135], a[196:199], v[76:79]\n"
      "s_nop 0\n"
      "v_accvgpr_write_b32 a162, v38\n"
      "v_accvgpr_write_b32 a163, v39\n"
      "v_mfma_f32_16x16x32_bf16 v[80:83], v[124:127], a[196:199], v[80:83]\n"
      "s_nop 0\n"
      "v_cvt_pk_bf16_f32 v28, v28, v32\n"
      "v_cvt_pk_bf16_f32 v29, v29, v33\n"
      "v_mfma_f32_16x16x32_bf16 v[84:87], v[116:119], a[196:199], v[84:87]\n"
      "s_nop 0\n"
      "v_cvt_pk_bf16_f32 v30, v30, v34\n"
      "v_cvt_pk_bf16_f32 v31, v31, v35\n"
      "v_mfma_f32_16x16x32_bf16 v[88:91], v[128:131], a[200:203], v[88:91]\n"
      "s_nop 0\n"
      "v_accvgpr_write_b32 a156, v28\n"
      "v_accvgpr_write_b32 a157, v29\n"
      "v_mfma_f32_16x16x32_bf16 v[92:95], v[120:123], a[200:203], v[92:95]\n"
      "s_waitcnt vmcnt(12)\n"
      "v_accvgpr_write_b32 a158, v30\n"
      "v_accvgpr_write_b32 a159, v31\n"
      "s_barrier\n"
      "s_add_u32 s53, s54, 2949120\n"
      "s_add_u32 m0, s37, 141448\n"
      "v_mfma_f32_4x4x4_16b_bf16 v[28:31], a[244:245], a[88:89], 0\n"
      "v_mfma_f32_4x4x4_16b_bf16 v[32:35], a[244:245], a[90:91], 0\n"
      "buffer_load_dwordx4 v8, s[4:7], s53 offen offset:0 nt lds\n"
      "buffer_load_dwordx4 v9, s[4:7], s53 offen offset:1024 nt lds\n"
      "v_mfma_f32_4x4x4_16b_bf16 v[28:31], a[248:249], a[232:233], v[28:31]\n"
      "ds_read_b64_tr_b16 a[96:97], v61 offset:2304\n"
      "v_mfma_f32_4x4x4_16b_bf16 v[32:35], a[248:249], a[234:235], v[32:35]\n"
      "ds_read_b64_tr_b16 a[98:99], v61 offset:3072\n"
      "v_mfma_f32_4x4x4_16b_bf16 v[36:39], a[246:247], a[228:229], v[28:31]\n"
      "ds_read_b64_tr_b16 a[224:225], v61 offset:14592\n"
      "v_mfma_f32_4x4x4_16b_bf16 v[40:43], a[246:247], a[230:231], v[32:35]\n"
      "ds_read_b64_tr_b16 a[226:227], v61 offset:15360\n"
      "v_mfma_f32_4x4x4_16b_bf16 v[28:31], a[250:251], a[228:229], v[28:31]\n"
      "ds_read_b64_tr_b16 a[220:221], v61 offset:8448\n"
      "v_mfma_f32_4x4x4_16b_bf16 v[32:35], a[250:251], a[230:231], v[32:35]\n"
      "ds_read_b64_tr_b16 a[222:223], v61 offset:9216\n"
      "ds_read_b128 a[192:195], v5 offset:55232\n"
      "v_cvt_pk_bf16_f32 v52, v52, v56\n"
      "v_cvt_pk_bf16_f32 v53, v53, v57\n"
      "v_cvt_pk_bf16_f32 v54, v54, v58\n"
      "v_cvt_pk_bf16_f32 v55, v55, v59\n"
      "v_mfma_f32_16x16x32_bf16 v[96:99], v[112:115], a[200:203], v[96:99]\n"
      "s_nop 0\n"
      "v_accvgpr_write_b32 a152, v52\n"
      "v_accvgpr_write_b32 a153, v53\n"
      "v_mfma_f32_16x16x32_bf16 v[64:67], a[72:75], a[204:207], v[64:67]\n"
      "s_nop 0\n"
      "v_accvgpr_write_b32 a154, v54\n"
      "v_accvgpr_write_b32 a155, v55\n"
      "v_mfma_f32_16x16x32_bf16 v[68:71], a[76:79], a[204:207], v[68:71]\n"
      "s_nop 0\n"
      "v_cvt_pk_bf16_f32 v44, v44, v48\n"
      "v_cvt_pk_bf16_f32 v45, v45, v49\n"
      "v_mfma_f32_16x16x32_bf16 v[72:75], a[80:83], a[204:207], v[72:75]\n"
      "s_waitcnt lgkmcnt(0)\n"
      "v_cvt_pk_bf16_f32 v46, v46, v50\n"
      "v_cvt_pk_bf16_f32 v47, v47, v51\n"
      "v_mfma_f32_16x16x32_bf16 v[76:79], v[108:111], a[208:211], v[76:79]\n"
      "s_add_u32 m0, s37, 145792\n"
      "v_accvgpr_write_b32 a148, v44\n"
      "v_accvgpr_write_b32 a149, v45\n"
      "v_mfma_f32_16x16x32_bf16 v[80:83], v[100:103], a[208:211], v[80:83]\n"
      "s_add_u32 s53, s54, 3145728\n"
      "v_accvgpr_write_b32 a150, v46\n"
      "v_accvgpr_write_b32 a151, v47\n"
      "v_mfma_f32_4x4x4_16b_bf16 v[44:47], a[244:245], a[92:93], 0\n"
      "v_mfma_f32_4x4x4_16b_bf16 v[48:51], a[244:245], a[94:95], 0\n"
      "buffer_load_dwordx4 v8, s[4:7], s53 offen offset:0 nt lds\n"
      "buffer_load_dwordx4 v9, s[4:7], s53 offen offset:1024 nt lds\n"
      "v_mfma_f32_4x4x4_16b_bf16 v[44:47], a[248:249], a[240:241], v[44:47]\n"
      "ds_read_b64_tr_b16 a[100:101], v61 offset:2368\n"
      "v_mfma_f32_4x4x4_16b_bf16 v[48:51], a[248:249], a[242:243], v[48:51]\n"
      "ds_read_b64_tr_b16 a[102:103], v61 offset:3136\n"
      "v_mfma_f32_4x4x4_16b_bf16 v[52:55], a[246:247], a[236:237], v[44:47]\n"
      "ds_read_b64_tr_b16 a[232:233], v61 offset:14656\n"
      "v_mfma_f32_4x4x4_16b_bf16 v[56:59], a[246:247], a[238:239], v[48:51]\n"
      "ds_read_b64_tr_b16 a[234:235], v61 offset:15424\n"
      "v_mfma_f32_4x4x4_16b_bf16 v[44:47], a[250:251], a[236:237], v[44:47]\n"
      "ds_read_b64_tr_b16 a[228:229], v61 offset:8512\n"
      "v_mfma_f32_4x4x4_16b_bf16 v[48:51], a[250:251], a[238:239], v[48:51]\n"
      "ds_read_b64_tr_b16 a[230:231], v61 offset:9280\n"
      "ds_read_b128 a[196:199], v2 offset:64\n"
      "ds_read_b128 a[200:203], v2 offset:4160\n"
      "v_cvt_pk_bf16_f32 v36, v36, v40\n"
      "v_cvt_pk_bf16_f32 v37, v37, v41\n"
      "v_cvt_pk_bf16_f32 v38, v38, v42\n"
      "v_cvt_pk_bf16_f32 v39, v39, v43\n"
      "v_accvgpr_write_b32 a144, v36\n"
      "v_accvgpr_write_b32 a145, v37\n"
      "v_accvgpr_write_b32 a146, v38\n"
      "v_accvgpr_write_b32 a147, v39\n"
      "v_mfma_f32_16x16x32_bf16 v[84:87], a[160:163], a[208:211], v[84:87]\n"
      "s_nop 0\n"
      "v_cvt_pk_bf16_f32 v28, v28, v32\n"
      "v_cvt_pk_bf16_f32 v29, v29, v33\n"
      "v_mfma_f32_16x16x32_bf16 v[88:91], v[104:107], a[212:215], v[88:91]\n"
      "s_nop 0\n"
      "v_cvt_pk_bf16_f32 v30, v30, v34\n"
      "v_cvt_pk_bf16_f32 v31, v31, v35\n"
      "v_mfma_f32_16x16x32_bf16 v[92:95], a[164:167], a[212:215], v[92:95]\n"
      "s_nop 0\n"
      "v_accvgpr_write_b32 a140, v28\n"
      "v_accvgpr_write_b32 a141, v29\n"
      "v_mfma_f32_16x16x32_bf16 v[96:99], a[156:159], a[212:215], v[96:99]\n"
      "s_nop 0\n"
      "v_accvgpr_write_b32 a142, v30\n"
      "v_accvgpr_write_b32 a143, v31\n"
      "v_mfma_f32_4x4x4_16b_bf16 v[28:31], a[244:245], a[96:97], 0\n"
      "v_mfma_f32_4x4x4_16b_bf16 v[32:35], a[244:245], a[98:99], 0\n"
      "ds_read_b64_tr_b16 a[104:105], v61 offset:2432\n"
      "v_mfma_f32_4x4x4_16b_bf16 v[28:31], a[248:249], a[224:225], v[28:31]\n"
      "ds_read_b64_tr_b16 a[106:107], v61 offset:3200\n"
      "v_mfma_f32_4x4x4_16b_bf16 v[32:35], a[248:249], a[226:227], v[32:35]\n"
      "ds_read_b64_tr_b16 a[240:241], v61 offset:14720\n"
      "v_mfma_f32_4x4x4_16b_bf16 v[36:39], a[246:247], a[220:221], v[28:31]\n"
      "ds_read_b64_tr_b16 a[242:243], v61 offset:15488\n"
      "v_mfma_f32_4x4x4_16b_bf16 v[40:43], a[246:247], a[222:223], v[32:35]\n"
      "ds_read_b64_tr_b16 a[236:237], v61 offset:8576\n"
      "v_mfma_f32_4x4x4_16b_bf16 v[28:31], a[250:251], a[220:221], v[28:31]\n"
      "ds_read_b64_tr_b16 a[238:239], v61 offset:9344\n"
      "v_mfma_f32_4x4x4_16b_bf16 v[32:35], a[250:251], a[222:223], v[32:35]\n"
      "v_cvt_pk_bf16_f32 v52, v52, v56\n"
      "v_cvt_pk_bf16_f32 v53, v53, v57\n"
      "v_cvt_pk_bf16_f32 v54, v54, v58\n"
      "v_cvt_pk_bf16_f32 v55, v55, v59\n"
      "v_accvgpr_write_b32 a136, v52\n"
      "v_accvgpr_write_b32 a137, v53\n"
      "v_accvgpr_write_b32 a138, v54\n"
      "v_accvgpr_write_b32 a139, v55\n"
      "v_cvt_pk_bf16_f32 v44, v44, v48\n"
      "v_cvt_pk_bf16_f32 v45, v45, v49\n"
      "v_mfma_f32_16x16x32_bf16 v[64:67], a[84:87], a[192:195], v[64:67]\n"
      "s_nop 0\n"
      "v_cvt_pk_bf16_f32 v46, v46, v50\n"
      "v_cvt_pk_bf16_f32 v47, v47, v51\n"
      "v_mfma_f32_16x16x32_bf16 v[68:71], a[88:91], a[192:195], v[68:71]\n"
      "s_nop 0\n"
      "v_accvgpr_write_b32 a132, v44\n"
      "v_accvgpr_write_b32 a133, v45\n"
      "v_mfma_f32_16x16x32_bf16 v[72:75], a[92:95], a[192:195], v[72:75]\n"
      "s_waitcnt lgkmcnt(0)\n"
      "v_accvgpr_write_b32 a134, v46\n"
      "v_accvgpr_write_b32 a135, v47\n"
      "v_mfma_f32_4x4x4_16b_bf16 v[44:47], a[244:245], a[100:101], 0\n"
      "v_mfma_f32_4x4x4_16b_bf16 v[48:51], a[244:245], a[102:103], 0\n"
      "ds_read_b128 a[204:207], v5 offset:55296\n"
      "v_mfma_f32_4x4x4_16b_bf16 v[44:47], a[248:249], a[232:233], v[44:47]\n"
      "v_mfma_f32_4x4x4_16b_bf16 v[48:51], a[248:249], a[234:235], v[48:51]\n"
      "s_nop 0\n"
      "v_mfma_f32_4x4x4_16b_bf16 v[52:55], a[246:247], a[228:229], v[44:47]\n"
      "v_mfma_f32_4x4x4_16b_bf16 v[56:59], a[246:247], a[230:231], v[48:51]\n"
      "v_mfma_f32_4x4x4_16b_bf16 v[44:47], a[250:251], a[228:229], v[44:47]\n"
      "v_mfma_f32_4x4x4_16b_bf16 v[48:51], a[250:251], a[230:231], v[48:51]\n"
      "v_cvt_pk_bf16_f32 v36, v36, v40\n"
      "v_cvt_pk_bf16_f32 v37, v37, v41\n"
      "v_cvt_pk_bf16_f32 v38, v38, v42\n"
      "v_cvt_pk_bf16_f32 v39, v39, v43\n"
      "v_mfma_f32_16x16x32_bf16 v[76:79], a[152:155], a[196:199], v[76:79]\n"
      "v_accvgpr_write_b32 a128, v36\n"
      "v_accvgpr_write_b32 a129, v37\n"
      "v_mfma_f32_16x16x32_bf16 v[80:83], a[144:147], a[196:199], v[80:83]\n"
      "v_accvgpr_write_b32 a130, v38\n"
      "v_accvgpr_write_b32 a131, v39\n"
      "v_mfma_f32_16x16x32_bf16 v[84:87], a[136:139], a[196:199], v[84:87]\n"
      "v_cvt_pk_bf16_f32 v28, v28, v32\n"
      "v_cvt_pk_bf16_f32 v29, v29, v33\n"
      "v_mfma_f32_16x16x32_bf16 v[88:91], a[148:151], a[200:203], v[88:91]\n"
      "v_cvt_pk_bf16_f32 v30, v30, v34\n"
      "v_cvt_pk_bf16_f32 v31, v31, v35\n"
      "v_mfma_f32_16x16x32_bf16 v[92:95], a[140:143], a[200:203], v[92:95]\n"
      "v_accvgpr_write_b32 a124, v28\n"
      "v_accvgpr_write_b32 a125, v29\n"
      "v_mfma_f32_16x16x32_bf16 v[96:99], a[132:135], a[200:203], v[96:99]\n"
      "v_accvgpr_write_b32 a126, v30\n"
      "v_accvgpr_write_b32 a127, v31\n"
      "v_mfma_f32_4x4x4_16b_bf16 v[28:31], a[244:245], a[104:105], 0\n"
      "v_mfma_f32_4x4x4_16b_bf16 v[32:35], a[244:245], a[106:107], 0\n"
      "ds_read_b128 a[208:211], v2 offset:128\n"
      "v_mfma_f32_4x4x4_16b_bf16 v[28:31], a[248:249], a[240:241], v[28:31]\n"
      "ds_read_b128 a[212:215], v2 offset:4224\n"
      "v_mfma_f32_4x4x4_16b_bf16 v[32:35], a[248:249], a[242:243], v[32:35]\n"
      "v_mfma_f32_4x4x4_16b_bf16 v[36:39], a[246:247], a[236:237], v[28:31]\n"
      "s_nop 0\n"
      "s_nop 0\n"
      "v_mfma_f32_4x4x4_16b_bf16 v[40:43], a[246:247], a[238:239], v[32:35]\n"
      "v_mfma_f32_4x4x4_16b_bf16 v[28:31], a[250:251], a[236:237], v[28:31]\n"
      "v_mfma_f32_4x4x4_16b_bf16 v[32:35], a[250:251], a[238:239], v[32:35]\n"
      "v_cvt_pk_bf16_f32 v52, v52, v56\n"
      "v_cvt_pk_bf16_f32 v53, v53, v57\n"
      "v_cvt_pk_bf16_f32 v54, v54, v58\n"
      "v_cvt_pk_bf16_f32 v55, v55, v59\n"
      "v_accvgpr_write_b32 a120, v52\n"
      "v_accvgpr_write_b32 a121, v53\n"
      "v_accvgpr_write_b32 a122, v54\n"
      "v_accvgpr_write_b32 a123, v55\n"
      "v_cvt_pk_bf16_f32 v44, v44, v48\n"
      "v_cvt_pk_bf16_f32 v45, v45, v49\n"
      "v_cvt_pk_bf16_f32 v46, v46, v50\n"
      "v_cvt_pk_bf16_f32 v47, v47, v51\n"
      "v_accvgpr_write_b32 a116, v44\n"
      "v_accvgpr_write_b32 a117, v45\n"
      "v_accvgpr_write_b32 a118, v46\n"
      "v_accvgpr_write_b32 a119, v47\n"
      "s_waitcnt lgkmcnt(0)\n"
      "s_barrier\n"
      "s_mov_b32 m0, s64\n"
      "s_nop 0\n"
      "v_mfma_f32_16x16x32_bf16 v[64:67], a[96:99], a[204:207], v[64:67]\n"
      "s_add_u32 s53, s54, 3342336\n"
      "ds_read_b128 a[192:195], v3 offset:0\n"
      "ds_read_b64_tr_b16 a[220:221], v7 offset:18944\n"
      "ds_read_b64_tr_b16 a[222:223], v7 offset:18952\n"
      "v_cvt_pk_bf16_f32 v36, v36, v40\n"
      "v_cvt_pk_bf16_f32 v37, v37, v41\n"
      "v_cvt_pk_bf16_f32 v38, v38, v42\n"
      "v_cvt_pk_bf16_f32 v39, v39, v43\n"
      "v_mfma_f32_16x16x32_bf16 v[68:71], a[100:103], a[204:207], v[68:71]\n"
      "s_add_u32 m0, s37, 82432\n"
      "ds_read_b128 a[196:199], v3 offset:64\n"
      "v_accvgpr_write_b32 a112, v36\n"
      "v_accvgpr_write_b32 a113, v37\n"
      "v_accvgpr_write_b32 a114, v38\n"
      "v_accvgpr_write_b32 a115, v39\n"
      "v_cvt_pk_bf16_f32 v28, v28, v32\n"
      "v_cvt_pk_bf16_f32 v29, v29, v33\n"
      "v_mfma_f32_16x16x32_bf16 v[72:75], a[104:107], a[204:207], v[72:75]\n"
      "s_nop 0\n"
      "buffer_load_dwordx4 v8, s[4:7], s53 offen offset:0 nt lds\n"
      "buffer_load_dwordx4 v9, s[4:7], s53 offen offset:1024 nt lds\n"
      "v_cvt_pk_bf16_f32 v30, v30, v34\n"
      "v_mfma_f32_16x16x32_bf16 v[76:79], a[128:131], a[208:211], v[76:79]\n"
      "s_mov_b32 m0, s64\n"
      "ds_read_b128 a[200:203], v3 offset:128\n"
      "ds_read_b64_tr_b16 a[224:225], v7 offset:19456\n"
      "ds_read_b64_tr_b16 a[226:227], v7 offset:19464\n"
      "v_cvt_pk_bf16_f32 v31, v31, v35\n"
      "v_accvgpr_write_b32 a108, v28\n"
      "v_accvgpr_write_b32 a109, v29\n"
      "v_accvgpr_write_b32 a110, v30\n"
      "v_accvgpr_write_b32 a111, v31\n"
      "v_mfma_f32_16x16x32_bf16 v[80:83], a[120:123], a[208:211], v[80:83]\n"
      "s_nop 0\n"
      "v_mfma_f32_16x16x32_bf16 v[84:87], a[112:115], a[208:211], v[84:87]\n"
      "s_waitcnt lgkmcnt(3)\n"
      "v_mfma_f32_4x4x4_16b_bf16 v[20:23], a[216:217], a[220:221], 0\n"
      "v_mfma_f32_4x4x4_16b_bf16 v[24:27], a[216:217], a[222:223], 0\n"
      "v_mfma_f32_16x16x32_bf16 v[88:91], a[124:127], a[212:215], v[88:91]\n"
      "s_add_u32 s53, s54, 3538944\n"
      "ds_read_b128 a[204:207], v4 offset:0\n"
      "v_mfma_f32_16x16x32_bf16 v[92:95], a[116:119], a[212:215], v[92:95]\n"
      "s_add_u32 m0, s37, 86536\n"
      "v_cvt_pk_bf16_f32 v20, v20, v24\n"
      "v_cvt_pk_bf16_f32 v21, v21, v25\n"
      "v_cvt_pk_bf16_f32 v22, v22, v26\n"
      "v_mfma_f32_16x16x32_bf16 v[96:99], a[108:111], a[212:215], v[96:99]\n"
      "buffer_load_dwordx4 v8, s[4:7], s53 offen offset:0 nt lds\n"
      "buffer_load_dwordx4 v9, s[4:7], s53 offen offset:1024 nt lds\n"
      "ds_read_b64_tr_b16 a[228:229], v7 offset:19968\n"
      "ds_read_b64_tr_b16 a[230:231], v7 offset:19976\n"
      "v_mfma_f32_16x16x32_bf16 v[52:55], a[0:3], a[192:195], 0\n"
      "s_mov_b32 m0, s64\n"
      "ds_read_b128 a[208:211], v4 offset:64\n"
      "ds_write_addtid_b32 v20 offset:18432\n"
      "ds_write_addtid_b32 v21 offset:22528\n"
      "v_mfma_f32_16x16x32_bf16 v[56:59], a[4:7], a[192:195], 0\n"
      "s_waitcnt lgkmcnt(5)\n"
      "ds_write_addtid_b32 v22 offset:26624\n"
      "v_add_f32_e64 v76, v76, v88\n"
      "v_add_f32_e64 v77, v77, v89\n"
      "v_add_f32_e64 v78, v78, v90\n"
      "v_mfma_f32_4x4x4_16b_bf16 v[28:31], a[216:217], a[224:225], 0\n"
      "v_cvt_pk_bf16_f32 v23, v23, v27\n"
      "v_mfma_f32_4x4x4_16b_bf16 v[32:35], a[216:217], a[226:227], 0\n"
      "ds_write_addtid_b32 v76 offset:45312\n"
      "ds_write_addtid_b32 v77 offset:45824\n"
      "ds_write_addtid_b32 v78 offset:46336\n"
      "v_mfma_f32_16x16x32_bf16 v[60:63], a[8:11], a[192:195], 0\n"
      "ds_write_addtid_b32 v23 offset:30720\n"
      "v_add_f32_e64 v79, v79, v91\n"
      "v_add_f32_e64 v80, v80, v92\n"
      "v_cvt_pk_bf16_f32 v28, v28, v32\n"
      "v_mfma_f32_16x16x32_bf16 v[52:55], a[12:15], a[196:199], v[52:55]\n"
      "s_add_u32 s53, s54, 3735552\n"
      "ds_read_b128 a[212:215], v4 offset:128\n"
      "ds_write_addtid_b32 v28 offset:18944\n"
      "v_mfma_f32_16x16x32_bf16 v[56:59], a[16:19], a[196:199], v[56:59]\n"
      "s_add_u32 m0, s37, 90760\n"
      "ds_read_b64_tr_b16 a[232:233], v7 offset:20480\n"
      "ds_read_b64_tr_b16 a[234:235], v7 offset:20488\n"
      "v_add_f32_e64 v81, v81, v93\n"
      "v_mfma_f32_16x16x32_bf16 v[60:63], a[20:23], a[196:199], v[60:63]\n"
      "s_waitcnt lgkmcnt(5)\n"
      "buffer_load_dwordx4 v8, s[4:7], s53 offen offset:0 nt lds\n"
      "buffer_load_dwordx4 v9, s[4:7], s53 offen offset:1024 nt lds\n"
      "v_cvt_pk_bf16_f32 v29, v29, v33\n"
      "v_mfma_f32_16x16x32_bf16 v[52:55], a[24:27], a[200:203], v[52:55]\n"
      "s_mov_b32 m0, s64\n"
      "ds_read_b128 a[192:195], v5 offset:0\n"
      "ds_write_addtid_b32 v79 offset:46848\n"
      "v_mfma_f32_4x4x4_16b_bf16 v[20:23], a[216:217], a[228:229], 0\n"
      "ds_write_addtid_b32 v80 offset:47360\n"
      "ds_write_addtid_b32 v29 offset:23040\n"
      "v_mfma_f32_4x4x4_16b_bf16 v[24:27], a[216:217], a[230:231], 0\n"
      "ds_write_addtid_b32 v81 offset:47872\n"
      "v_add_f32 v82, v82, v94\n"
      "v_add_f32 v83, v83, v95\n"
      "v_mfma_f32_16x16x32_bf16 v[56:59], a[28:31], a[200:203], v[56:59]\n"
      "ds_write_addtid_b32 v82 offset:48384\n"
      "ds_write_addtid_b32 v83 offset:48896\n"
      "v_cvt_pk_bf16_f32 v30, v30, v34\n"
      "v_mfma_f32_16x16x32_bf16 v[60:63], a[32:35], a[200:203], v[60:63]\n"
      "ds_write_addtid_b32 v30 offset:27136\n"
      "v_add_f32 v84, v84, v96\n"
      "v_cvt_pk_bf16_f32 v20, v20, v24\n"
      "v_cvt_pk_bf16_f32 v31, v31, v35\n"
      "v_mfma_f32_16x16x32_bf16 v[52:55], a[36:39], a[204:207], v[52:55]\n"
      "s_nop 0\n"
      "ds_read_b128 a[196:199], v5 offset:64\n"
      "ds_write_addtid_b32 v20 offset:19456\n"
      "v_mfma_f32_16x16x32_bf16 v[56:59], a[40:43], a[204:207], v[56:59]\n"
      "ds_write_addtid_b32 v31 offset:31232\n"
      "ds_write_addtid_b32 v84 offset:49408\n"
      "v_add_f32 v85, v85, v97\n"
      "v_mfma_f32_16x16x32_bf16 v[60:63], a[44:47], a[204:207], v[60:63]\n"
      "s_waitcnt lgkmcnt(5)\n"
      "ds_write_addtid_b32 v85 offset:49920\n"
      "v_add_f32 v86, v86, v98\n"
      "v_add_f32 v87, v87, v99\n"
      "v_cvt_pk_bf16_f32 v21, v21, v25\n"
      "v_mfma_f32_4x4x4_16b_bf16 v[28:31], a[216:217], a[232:233], 0\n"
      "v_cvt_pk_bf16_f32 v22, v22, v26\n"
      "v_mfma_f32_4x4x4_16b_bf16 v[32:35], a[216:217], a[234:235], 0\n"
      "ds_write_addtid_b32 v21 offset:23552\n"
      "ds_write_addtid_b32 v86 offset:50432\n"
      "ds_write_addtid_b32 v22 offset:27648\n"
      "v_mfma_f32_16x16x32_bf16 v[52:55], a[48:51], a[208:211], v[52:55]\n"
      "ds_read_b128 a[200:203], v5 offset:128\n"
      "ds_write_addtid_b32 v87 offset:50944\n"
      "v_cvt_pk_bf16_f32 v28, v28, v32\n"
      "v_mfma_f32_16x16x32_bf16 v[56:59], a[52:55], a[208:211], v[56:59]\n"
      "ds_write_addtid_b32 v28 offset:19968\n"
      "v_cvt_pk_bf16_f32 v29, v29, v33\n"
      "v_cvt_pk_bf16_f32 v30, v30, v34\n"
      "v_cvt_pk_bf16_f32 v23, v23, v27\n"
      "v_cvt_pk_bf16_f32 v31, v31, v35\n"
      "v_mfma_f32_16x16x32_bf16 v[60:63], a[56:59], a[208:211], v[60:63]\n"
      "ds_write_addtid_b32 v29 offset:24064\n"
      "ds_write_addtid_b32 v23 offset:31744\n"
      "v_mfma_f32_16x16x32_bf16 v[52:55], a[60:63], a[212:215], v[52:55]\n"
      "ds_write_addtid_b32 v30 offset:28160\n"
      "ds_write_addtid_b32 v31 offset:32256\n"
      "v_fma_f32 v40, -2.0, v88, v76\n"
      "v_mfma_f32_16x16x32_bf16 v[56:59], a[64:67], a[212:215], v[56:59]\n"
      "s_waitcnt vmcnt(14)\n"
      "v_fma_f32 v41, -2.0, v89, v77\n"
      "v_fma_f32 v42, -2.0, v90, v78\n"
      "v_fma_f32 v43, -2.0, v91, v79\n"
      "v_mfma_f32_16x16x32_bf16 v[60:63], a[68:71], a[212:215], v[60:63]\n"
      "s_waitcnt lgkmcnt(0)\n"
      "v_fma_f32 v44, -2.0, v92, v80\n"
      "v_fma_f32 v45, -2.0, v93, v81\n"
      "s_nop 0\n"
      "s_barrier\n"
      "s_add_u32 s54, s54, 3735552\n"
      "buffer_load_dword a252, v8, s[4:7], s40 offen\n"
      "buffer_load_dword a252, v8, s[4:7], s40 offen\n"
      "buffer_load_dword a252, v8, s[4:7], s40 offen\n"
      "buffer_load_dword a252, v8, s[4:7], s40 offen\n"
      "buffer_load_dword a252, v8, s[4:7], s40 offen\n"
      "buffer_load_dword a252, v8, s[4:7], s40 offen\n"
      "LDloop_%=:\n"
      "v_mfma_f32_16x16x32_bf16 v[52:55], a[72:75], a[192:195], v[52:55]\n"
      "s_add_u32 s58, s40, 0\n"
      "ds_read_b128 a[204:207], v0 offset:16384\n"
      "ds_read2st64_b32 v[28:29], v6 offset0:0 offset1:2\n"
      "ds_read2st64_b32 v[30:31], v6 offset0:4 offset1:6\n"
      "v_mfma_f32_16x16x32_bf16 v[56:59], a[76:79], a[192:195], v[56:59]\n"
      "s_mov_b32 s40, s62\n"
      "ds_read_b128 a[208:211], v0 offset:20480\n"
      "ds_read2st64_b32 v[32:33], v6 offset0:8 offset1:10\n"
      "v_mfma_f32_16x16x32_bf16 v[60:63], a[80:83], a[192:195], v[60:63]\n"
      "s_add_u32 s62, s62, 786432\n"
      "ds_read2st64_b32 v[34:35], v6 offset0:12 offset1:14\n"
      "v_fma_f32 v46, -2.0, v94, v82\n"
      "v_mfma_f32_16x16x32_bf16 v[52:55], a[84:87], a[196:199], v[52:55]\n"
      "s_add_u32 s59, s58, 196608\n"
      "ds_read_b128 a[212:215], v0 offset:16448\n"
      "v_fma_f32 v47, -2.0, v95, v83\n"
      "v_fma_f32 v48, -2.0, v96, v84\n"
      "v_mfma_f32_16x16x32_bf16 v[56:59], a[88:91], a[196:199], v[56:59]\n"
      "ds_read2st64_b32 a[220:221], v10 offset0:181 offset1:183\n"
      "v_fma_f32 v49, -2.0, v97, v85\n"
      "v_mfma_f32_16x16x32_bf16 v[60:63], a[92:95], a[196:199], v[60:63]\n"
      "ds_read2st64_b32 a[222:223], v13 offset0:181 offset1:183\n"
      "v_fma_f32 v50, -2.0, v98, v86\n"
      "v_fma_f32 v51, -2.0, v99, v87\n"
      "v_mfma_f32_16x16x32_bf16 v[52:55], a[96:99], a[200:203], v[52:55]\n"
      "ds_read_b128 a[192:195], v0 offset:20544\n"
      "v_mfma_f32_16x16x32_bf16 v[56:59], a[100:103], a[200:203], v[56:59]\n"
      "ds_read2st64_b32 v[36:37], v6 offset0:16 offset1:18\n"
      "ds_read2st64_b32 v[38:39], v6 offset0:20 offset1:22\n"
      "v_mfma_f32_16x16x32_bf16 v[60:63], a[104:107], a[200:203], v[60:63]\n"
      "s_waitcnt lgkmcnt(7)\n"
      "v_fmac_f32 v40, s46, v28\n"
      "v_fmac_f32 v41, s46, v29\n"
      "v_fmac_f32 v42, s46, v30\n"
      "v_fmac_f32 v43, s46, v31\n"
      "v_mfma_f32_16x16x32_bf16 v[76:79], v[252:255], a[204:207], 0\n"
      "s_nop 0\n"
      "ds_read_b128 a[196:199], v0 offset:16512\n"
      "v_fma_f32 v28, 0.5, v28, v64\n"
      "v_mfma_f32_16x16x32_bf16 v[80:83], v[244:247], a[204:207], 0\n"
      "s_add_u32 s54, s54, 196608\n"
      "ds_read_b64_tr_b16 a[244:245], v7 offset:36864\n"
      "ds_read_b64_tr_b16 a[246:247], v7 offset:36872\n"
      "v_fma_f32 v29, 0.5, v29, v65\n"
      "v_mfma_f32_16x16x32_bf16 v[84:87], v[236:239], a[204:207], 0\n"
      "s_add_u32 m0, s37, 95104\n"
      "v_fma_f32 v30, 0.5, v30, v66\n"
      "v_fma_f32 v31, 0.5, v31, v67\n"
      "v_mfma_f32_16x16x32_bf16 v[88:91], v[248:251], a[208:211], 0\n"
      "buffer_load_dwordx4 v8, s[4:7], s54 offen offset:0 nt lds\n"
      "buffer_load_dwordx4 v9, s[4:7], s54 offen offset:1024 nt lds\n"
      "ds_read_b128 a[200:203], v0 offset:20608\n"
      "v_mfma_f32_16x16x32_bf16 v[92:95], v[240:243], a[208:211], 0\n"
      "s_mov_b32 m0, s64\n"
      "v_fmac_f32 v40, s48, v52\n"
      "v_fmac_f32 v41, s48, v53\n"
      "v_fmac_f32 v42, s48, v54\n"
      "v_mfma_f32_16x16x32_bf16 v[96:99], v[232:235], a[208:211], 0\n"
      "s_waitcnt lgkmcnt(3)\n"
      "v_fmac_f32 v43, s48, v55\n"
      "v_fmac_f32 v28, s45, v40\n"
      "v_mfma_f32_16x16x32_bf16 v[76:79], v[228:231], a[212:215], v[76:79]\n"
      "s_nop 0\n"
      "ds_read_b128 a[204:207], v1 offset:16384\n"
      "v_fmac_f32 v29, s45, v41\n"
      "v_mfma_f32_16x16x32_bf16 v[80:83], v[220:223], a[212:215], v[80:83]\n"
      "v_fmac_f32 v30, s45, v42\n"
      "v_fmac_f32 v31, s45, v43\n"
      "v_mfma_f32_16x16x32_bf16 v[84:87], v[212:215], a[212:215], v[84:87]\n"
      "s_waitcnt lgkmcnt(1)\n"
      "ds_read2st64_b32 a[224:225], v11 offset0:181 offset1:183\n"
      "ds_read2st64_b32 a[226:227], v14 offset0:181 offset1:183\n"
      "v_fma_f32 v40, s47, v40, -v52\n"
      "v_mfma_f32_4x4x4_16b_bf16 v[20:23], a[216:217], a[244:245], 0\n"
      "v_fma_f32 v41, s47, v41, -v53\n"
      "v_mfma_f32_4x4x4_16b_bf16 v[24:27], a[216:217], a[246:247], 0\n"
      "v_fma_f32 v42, s47, v42, -v54\n"
      "v_fma_f32 v43, s47, v43, -v55\n"
      "v_mfma_f32_16x16x32_bf16 v[88:91], v[224:227], a[192:195], v[88:91]\n"
      "buffer_store_dwordx4 a[220:223], v16, s[8:11], s58 offen offset:0 sc1\n"
      "ds_read_b128 a[208:211], v1 offset:20480\n"
      "v_mfma_f32_16x16x32_bf16 v[92:95], v[216:219], a[192:195], v[92:95]\n"
      "v_cvt_pk_bf16_f32 v20, v20, v24\n"
      "v_cvt_pk_bf16_f32 v28, v28, v29\n"
      "v_mfma_f32_16x16x32_bf16 v[96:99], v[208:211], a[192:195], v[96:99]\n"
      "ds_write_addtid_b32 v28 offset:32768\n"
      "ds_write_addtid_b32 v20 offset:3072\n"
      "v_mfma_f32_16x16x32_bf16 v[76:79], v[204:207], a[196:199], v[76:79]\n"
      "s_add_u32 s54, s54, 196608\n"
      "ds_read_b128 a[212:215], v1 offset:16448\n"
      "v_cvt_pk_bf16_f32 v30, v30, v31\n"
      "v_cvt_pk_bf16_f32 v21, v21, v25\n"
      "v_mfma_f32_16x16x32_bf16 v[80:83], v[196:199], a[196:199], v[80:83]\n"
      "s_add_u32 m0, s37, 99328\n"
      "v_cvt_pk_bf16_f32 v40, v40, v41\n"
      "v_cvt_pk_bf16_f32 v42, v42, v43\n"
      "v_mfma_f32_16x16x32_bf16 v[84:87], v[188:191], a[196:199], v[84:87]\n"
      "buffer_load_dwordx4 v8, s[4:7], s54 offen offset:0 nt lds\n"
      "buffer_load_dwordx4 v9, s[4:7], s54 offen offset:1024 nt lds\n"
      "v_mfma_f32_16x16x32_bf16 v[88:91], v[200:203], a[200:203], v[88:91]\n"
      "s_mov_b32 m0, s64\n"
      "ds_read_b128 a[192:195], v1 offset:20544\n"
      "ds_write_addtid_b32 v30 offset:33280\n"
      "ds_write_addtid_b32 v40 offset:34816\n"
      "v_mfma_f32_16x16x32_bf16 v[92:95], v[192:195], a[200:203], v[92:95]\n"
      "s_nop 0\n"
      "ds_write_addtid_b32 v42 offset:35328\n"
      "ds_write_addtid_b32 v21 offset:7168\n"
      "v_fmac_f32 v44, s46, v32\n"
      "v_cvt_pk_bf16_f32 v22, v22, v26\n"
      "v_mfma_f32_16x16x32_bf16 v[96:99], v[184:187], a[200:203], v[96:99]\n"
      "s_waitcnt lgkmcnt(5)\n"
      "ds_write_addtid_b32 v22 offset:11264\n"
      "v_fmac_f32 v45, s46, v33\n"
      "v_mfma_f32_16x16x32_bf16 v[76:79], v[180:183], a[204:207], v[76:79]\n"
      "s_nop 0\n"
      "ds_read_b128 a[196:199], v1 offset:16512\n"
      "v_fmac_f32 v46, s46, v34\n"
      "v_mfma_f32_16x16x32_bf16 v[80:83], v[172:175], a[204:207], v[80:83]\n"
      "ds_read2st64_b32 a[228:229], v12 offset0:181 offset1:183\n"
      "ds_read2st64_b32 a[230:231], v15 offset0:181 offset1:183\n"
      "v_fmac_f32 v47, s46, v35\n"
      "v_cvt_pk_bf16_f32 v23, v23, v27\n"
      "v_mfma_f32_16x16x32_bf16 v[84:87], v[164:167], a[204:207], v[84:87]\n"
      "ds_write_addtid_b32 v23 offset:15360\n"
      "v_fma_f32 v32, 0.5, v32, v68\n"
      "v_mfma_f32_16x16x32_bf16 v[88:91], v[176:179], a[208:211], v[88:91]\n"
      "ds_read_b128 a[200:203], v1 offset:20608\n"
      "v_fma_f32 v33, 0.5, v33, v69\n"
      "v_mfma_f32_16x16x32_bf16 v[92:95], v[168:171], a[208:211], v[92:95]\n"
      "buffer_store_dwordx4 a[224:227], v16, s[8:11], s58 offen offset:128 sc1\n"
      "v_mfma_f32_16x16x32_bf16 v[96:99], v[160:163], a[208:211], v[96:99]\n"
      "v_fma_f32 v34, 0.5, v34, v70\n"
      "v_fma_f32 v35, 0.5, v35, v71\n"
      "v_mfma_f32_16x16x32_bf16 v[76:79], v[156:159], a[212:215], v[76:79]\n"
      "s_waitcnt lgkmcnt(1)\n"
      "ds_read_b128 a[204:207], v2 offset:16384\n"
      "v_fmac_f32 v44, s48, v56\n"
      "v_mfma_f32_16x16x32_bf16 v[80:83], v[148:151], a[212:215], v[80:83]\n"
      "s_add_u32 s54, s54, 196608\n"
      "v_fmac_f32 v45, s48, v57\n"
      "v_fmac_f32 v46, s48, v58\n"
      "v_mfma_f32_16x16x32_bf16 v[84:87], v[140:143], a[212:215], v[84:87]\n"
      "s_add_u32 m0, s37, 103432\n"
      "v_fmac_f32 v47, s48, v59\n"
      "v_fmac_f32 v32, s45, v44\n"
      "v_mfma_f32_16x16x32_bf16 v[88:91], v[152:155], a[192:195], v[88:91]\n"
      "buffer_load_dwordx4 v8, s[4:7], s54 offen offset:0 nt lds\n"
      "buffer_load_dwordx4 v9, s[4:7], s54 offen offset:1024 nt lds\n"
      "ds_read_b128 a[208:211], v2 offset:20480\n"
      "v_mfma_f32_16x16x32_bf16 v[92:95], v[144:147], a[192:195], v[92:95]\n"
      "s_mov_b32 m0, s64\n"
      "v_fmac_f32 v33, s45, v45\n"
      "v_fmac_f32 v34, s45, v46\n"
      "v_mfma_f32_16x16x32_bf16 v[96:99], v[136:139], a[192:195], v[96:99]\n"
      "ds_read_b64_tr_b16 a[248:249], v7 offset:37376\n"
      "ds_read_b64_tr_b16 a[250:251], v7 offset:37384\n"
      "v_fmac_f32 v35, s45, v47\n"
      "v_mfma_f32_16x16x32_bf16 v[76:79], v[132:135], a[196:199], v[76:79]\n"
      "ds_read_b128 a[212:215], v2 offset:16448\n"
      "v_fma_f32 v44, s47, v44, -v56\n"
      "v_mfma_f32_16x16x32_bf16 v[80:83], v[124:127], a[196:199], v[80:83]\n"
      "v_fma_f32 v45, s47, v45, -v57\n"
      "v_fma_f32 v46, s47, v46, -v58\n"
      "v_mfma_f32_16x16x32_bf16 v[84:87], v[116:119], a[196:199], v[84:87]\n"
      "s_waitcnt lgkmcnt(3)\n"
      "v_fma_f32 v47, s47, v47, -v59\n"
      "v_cvt_pk_bf16_f32 v32, v32, v33\n"
      "v_mfma_f32_16x16x32_bf16 v[88:91], v[128:131], a[200:203], v[88:91]\n"
      "s_nop 0\n"
      "buffer_store_dwordx4 a[228:231], v16, s[8:11], s58 offen offset:256 sc1\n"
      "ds_read_b128 a[192:195], v2 offset:20544\n"
      "v_mfma_f32_16x16x32_bf16 v[92:95], v[120:123], a[200:203], v[92:95]\n"
      "ds_write_addtid_b32 v32 offset:40964\n"
      "ds_read2st64_b32 a[232:233], v10 offset0:4 offset1:6\n"
      "ds_read2st64_b32 a[234:235], v13 offset0:4 offset1:6\n"
      "v_cvt_pk_bf16_f32 v34, v34, v35\n"
      "v_mfma_f32_16x16x32_bf16 v[96:99], v[112:115], a[200:203], v[96:99]\n"
      "ds_write_addtid_b32 v34 offset:41476\n"
      "v_cvt_pk_bf16_f32 v44, v44, v45\n"
      "v_mfma_f32_16x16x32_bf16 v[76:79], v[108:111], a[204:207], v[76:79]\n"
      "s_waitcnt lgkmcnt(5)\n"
      "ds_read_b128 a[196:199], v2 offset:16512\n"
      "ds_write_addtid_b32 v44 offset:43012\n"
      "v_cvt_pk_bf16_f32 v46, v46, v47\n"
      "v_mfma_f32_4x4x4_16b_bf16 v[20:23], a[216:217], a[248:249], 0\n"
      "v_fmac_f32 v48, s46, v36\n"
      "v_mfma_f32_4x4x4_16b_bf16 v[24:27], a[216:217], a[250:251], 0\n"
      "ds_write_addtid_b32 v46 offset:43524\n"
      "v_fmac_f32 v49, s46, v37\n"
      "v_fmac_f32 v50, s46, v38\n"
      "v_mfma_f32_16x16x32_bf16 v[80:83], v[100:103], a[204:207], v[80:83]\n"
      "v_fmac_f32 v51, s46, v39\n"
      "v_cvt_pk_bf16_f32 v20, v20, v24\n"
      "v_mfma_f32_16x16x32_bf16 v[84:87], a[160:163], a[204:207], v[84:87]\n"
      "s_nop 0\n"
      "ds_write_addtid_b32 v20 offset:3584\n"
      "v_cvt_pk_bf16_f32 v21, v21, v25\n"
      "v_fma_f32 v36, 0.5, v36, v72\n"
      "v_mfma_f32_16x16x32_bf16 v[88:91], v[104:107], a[208:211], v[88:91]\n"
      "s_add_u32 s54, s54, 196608\n"
      "ds_read_b128 a[200:203], v2 offset:20608\n"
      "ds_write_addtid_b32 v21 offset:7680\n"
      "v_mfma_f32_16x16x32_bf16 v[92:95], a[164:167], a[208:211], v[92:95]\n"
      "s_add_u32 m0, s37, 107656\n"
      "v_fma_f32 v37, 0.5, v37, v73\n"
      "v_cvt_pk_bf16_f32 v22, v22, v26\n"
      "v_mfma_f32_16x16x32_bf16 v[96:99], a[156:159], a[208:211], v[96:99]\n"
      "s_nop 0\n"
      "buffer_load_dwordx4 v8, s[4:7], s54 offen offset:0 nt lds\n"
      "buffer_load_dwordx4 v9, s[4:7], s54 offen offset:1024 nt lds\n"
      "ds_read2st64_b32 a[236:237], v11 offset0:4 offset1:6\n"
      "ds_read2st64_b32 a[238:239], v14 offset0:4 offset1:6\n"
      "v_mfma_f32_16x16x32_bf16 v[76:79], a[152:155], a[212:215], v[76:79]\n"
      "s_mov_b32 m0, s64\n"
      "ds_read_b128 a[204:207], v3 offset:16896\n"
      "ds_write_addtid_b32 v22 offset:11776\n"
      "v_mfma_f32_16x16x32_bf16 v[80:83], a[144:147], a[212:215], v[80:83]\n"
      "s_nop 0\n"
      "v_fma_f32 v38, 0.5, v38, v74\n"
      "v_cvt_pk_bf16_f32 v23, v23, v27\n"
      "v_mfma_f32_16x16x32_bf16 v[84:87], a[136:139], a[212:215], v[84:87]\n"
      "s_waitcnt lgkmcnt(4)\n"
      "ds_write_addtid_b32 v23 offset:15872\n"
      "v_fma_f32 v39, 0.5, v39, v75\n"
      "v_mfma_f32_16x16x32_bf16 v[88:91], a[148:151], a[192:195], v[88:91]\n"
      "ds_read_b128 a[208:211], v3 offset:16960\n"
      "v_mfma_f32_16x16x32_bf16 v[92:95], a[140:143], a[192:195], v[92:95]\n"
      "v_fmac_f32 v48, s48, v60\n"
      "v_fmac_f32 v49, s48, v61\n"
      "v_fmac_f32 v50, s48, v62\n"
      "v_mfma_f32_16x16x32_bf16 v[96:99], a[132:135], a[192:195], v[96:99]\n"
      "s_nop 0\n"
      "buffer_store_dwordx4 a[232:235], v16, s[8:11], s59 offen offset:0 sc1\n"
      "v_mfma_f32_16x16x32_bf16 v[76:79], a[128:131], a[196:199], v[76:79]\n"
      "ds_read_b128 a[212:215], v3 offset:17024\n"
      "v_fmac_f32 v51, s48, v63\n"
      "v_mfma_f32_16x16x32_bf16 v[80:83], a[120:123], a[196:199], v[80:83]\n"
      "ds_read2st64_b32 a[240:241], v12 offset0:4 offset1:6\n"
      "ds_read2st64_b32 a[242:243], v15 offset0:4 offset1:6\n"
      "v_fmac_f32 v36, s45, v48\n"
      "v_fmac_f32 v37, s45, v49\n"
      "v_mfma_f32_16x16x32_bf16 v[84:87], a[112:115], a[196:199], v[84:87]\n"
      "v_fmac_f32 v38, s45, v50\n"
      "v_fmac_f32 v39, s45, v51\n"
      "v_mfma_f32_16x16x32_bf16 v[88:91], a[124:127], a[200:203], v[88:91]\n"
      "ds_read_b128 a[192:195], v4 offset:16896\n"
      "v_fma_f32 v48, s47, v48, -v60\n"
      "v_mfma_f32_16x16x32_bf16 v[92:95], a[116:119], a[200:203], v[92:95]\n"
      "v_fma_f32 v49, s47, v49, -v61\n"
      "v_fma_f32 v50, s47, v50, -v62\n"
      "v_mfma_f32_16x16x32_bf16 v[96:99], a[108:111], a[200:203], v[96:99]\n"
      "s_waitcnt lgkmcnt(2)\n"
      "v_fma_f32 v51, s47, v51, -v63\n"
      "v_add_f32 v76, v76, v88\n"
      "v_add_f32 v77, v77, v89\n"
      "v_mfma_f32_16x16x32_bf16 v[64:67], a[0:3], a[204:207], 0\n"
      "ds_read_b128 a[196:199], v4 offset:16960\n"
      "ds_write_addtid_b32 v76 offset:51456\n"
      "ds_write_addtid_b32 v77 offset:51968\n"
      "v_mfma_f32_16x16x32_bf16 v[68:71], a[4:7], a[204:207], 0\n"
      "buffer_store_dwordx4 a[236:239], v16, s[8:11], s59 offen offset:128 sc1\n"
      "v_add_f32 v78, v78, v90\n"
      "v_mfma_f32_16x16x32_bf16 v[72:75], a[8:11], a[204:207], 0\n"
      "ds_write_addtid_b32 v78 offset:52480\n"
      "v_cvt_pk_bf16_f32 v36, v36, v37\n"
      "v_add_f32 v79, v79, v91\n"
      "v_mfma_f32_16x16x32_bf16 v[64:67], a[12:15], a[208:211], v[64:67]\n"
      "ds_read_b128 a[200:203], v4 offset:17024\n"
      "ds_write_addtid_b32 v36 offset:36864\n"
      "ds_write_addtid_b32 v79 offset:52992\n"
      "v_mfma_f32_16x16x32_bf16 v[68:71], a[16:19], a[208:211], v[68:71]\n"
      "v_add_f32 v80, v80, v92\n"
      "v_cvt_pk_bf16_f32 v38, v38, v39\n"
      "v_mfma_f32_16x16x32_bf16 v[72:75], a[20:23], a[208:211], v[72:75]\n"
      "ds_write_addtid_b32 v38 offset:37376\n"
      "ds_write_addtid_b32 v80 offset:53504\n"
      "v_add_f32 v81, v81, v93\n"
      "v_mfma_f32_16x16x32_bf16 v[64:67], a[24:27], a[212:215], v[64:67]\n"
      "ds_read_b128 a[204:207], v5 offset:16896\n"
      "ds_write_addtid_b32 v81 offset:54016\n"
      "v_mfma_f32_16x16x32_bf16 v[68:71], a[28:31], a[212:215], v[68:71]\n"
      "v_add_f32 v82, v82, v94\n"
      "v_cvt_pk_bf16_f32 v48, v48, v49\n"
      "v_add_f32_e64 v83, v83, v95\n"
      "v_mfma_f32_16x16x32_bf16 v[72:75], a[32:35], a[212:215], v[72:75]\n"
      "s_waitcnt lgkmcnt(4)\n"
      "ds_write_addtid_b32 v48 offset:38912\n"
      "ds_write_addtid_b32 v82 offset:54528\n"
      "v_mfma_f32_16x16x32_bf16 v[64:67], a[36:39], a[192:195], v[64:67]\n"
      "s_nop 0\n"
      "buffer_store_dwordx4 a[240:243], v16, s[8:11], s59 offen offset:256 sc1\n"
      "ds_read_b128 a[208:211], v5 offset:16960\n"
      "v_cvt_pk_bf16_f32 v50, v50, v51\n"
      "v_mfma_f32_16x16x32_bf16 v[68:71], a[40:43], a[192:195], v[68:71]\n"
      "s_nop 0\n"
      "ds_write_addtid_b32 v50 offset:39424\n"
      "ds_write_addtid_b32 v83 offset:55040\n"
      "v_mfma_f32_16x16x32_bf16 v[72:75], a[44:47], a[192:195], v[72:75]\n"
      "s_nop 0\n"
      "v_add_f32_e64 v84, v84, v96\n"
      "v_add_f32_e64 v85, v85, v97\n"
      "v_add_f32_e64 v86, v86, v98\n"
      "v_add_f32_e64 v87, v87, v99\n"
      "v_mfma_f32_16x16x32_bf16 v[64:67], a[48:51], a[196:199], v[64:67]\n"
      "s_nop 0\n"
      "ds_read_b128 a[212:215], v5 offset:17024\n"
      "ds_write_addtid_b32 v84 offset:55552\n"
      "v_mfma_f32_16x16x32_bf16 v[68:71], a[52:55], a[196:199], v[68:71]\n"
      "s_nop 0\n"
      "ds_write_addtid_b32 v85 offset:56064\n"
      "ds_write_addtid_b32 v86 offset:56576\n"
      "v_mfma_f32_16x16x32_bf16 v[72:75], a[56:59], a[196:199], v[72:75]\n"
      "s_nop 0\n"
      "ds_write_addtid_b32 v87 offset:57088\n"
      "v_fma_f32 v40, -2.0, v88, v76\n"
      "v_mfma_f32_16x16x32_bf16 v[64:67], a[60:63], a[200:203], v[64:67]\n"
      "s_nop 0\n"
      "v_fma_f32 v41, -2.0, v89, v77\n"
      "v_fma_f32 v42, -2.0, v90, v78\n"
      "v_mfma_f32_16x16x32_bf16 v[68:71], a[64:67], a[200:203], v[68:71]\n"
      "s_waitcnt vmcnt(26)\n"
      "v_fma_f32 v43, -2.0, v91, v79\n"
      "v_fma_f32 v44, -2.0, v92, v80\n"
      "v_mfma_f32_16x16x32_bf16 v[72:75], a[68:71], a[200:203], v[72:75]\n"
      "s_waitcnt lgkmcnt(0)\n"
      "v_fma_f32 v45, -2.0, v93, v81\n"
      "s_barrier\n"
      "LDmid_%=:\n"
      "v_mfma_f32_16x16x32_bf16 v[64:67], a[72:75], a[204:207], v[64:67]\n"
      "s_add_u32 s58, s40, 0\n"
      "ds_read_b128 a[192:195], v0 offset:0\n"
      "ds_read2st64_b32 v[28:29], v6 offset0:24 offset1:26\n"
      "ds_read2st64_b32 v[30:31], v6 offset0:28 offset1:30\n"
      "v_mfma_f32_16x16x32_bf16 v[68:71], a[76:79], a[204:207], v[68:71]\n"
      "s_mov_b32 s40, s62\n"
      "ds_read_b128 a[196:199], v0 offset:4096\n"
      "ds_read2st64_b32 v[32:33], v6 offset0:32 offset1:34\n"
      "v_mfma_f32_16x16x32_bf16 v[72:75], a[80:83], a[204:207], v[72:75]\n"
      "s_add_u32 s62, s62, 786432\n"
      "ds_read2st64_b32 v[34:35], v6 offset0:36 offset1:38\n"
      "v_fma_f32 v46, -2.0, v94, v82\n"
      "v_mfma_f32_16x16x32_bf16 v[64:67], a[84:87], a[208:211], v[64:67]\n"
      "s_add_u32 s59, s58, 196608\n"
      "ds_read_b128 a[200:203], v0 offset:64\n"
      "v_fma_f32 v47, -2.0, v95, v83\n"
      "v_fma_f32 v48, -2.0, v96, v84\n"
      "v_mfma_f32_16x16x32_bf16 v[68:71], a[88:91], a[208:211], v[68:71]\n"
      "ds_read2st64_b32 a[220:221], v10 offset0:177 offset1:179\n"
      "v_fma_f32 v49, -2.0, v97, v85\n"
      "v_mfma_f32_16x16x32_bf16 v[72:75], a[92:95], a[208:211], v[72:75]\n"
      "ds_read2st64_b32 a[222:223], v13 offset0:177 offset1:179\n"
      "v_fma_f32 v50, -2.0, v98, v86\n"
      "v_fma_f32 v51, -2.0, v99, v87\n"
      "v_mfma_f32_16x16x32_bf16 v[64:67], a[96:99], a[212:215], v[64:67]\n"
      "ds_read_b128 a[204:207], v0 offset:4160\n"
      "v_mfma_f32_16x16x32_bf16 v[68:71], a[100:103], a[212:215], v[68:71]\n"
      "ds_read2st64_b32 v[36:37], v6 offset0:40 offset1:42\n"
      "ds_read2st64_b32 v[38:39], v6 offset0:44 offset1:46\n"
      "v_mfma_f32_16x16x32_bf16 v[72:75], a[104:107], a[212:215], v[72:75]\n"
      "s_waitcnt lgkmcnt(7)\n"
      "v_fmac_f32 v40, s46, v28\n"
      "v_fmac_f32 v41, s46, v29\n"
      "v_fmac_f32 v42, s46, v30\n"
      "v_fmac_f32 v43, s46, v31\n"
      "v_mfma_f32_16x16x32_bf16 v[76:79], v[252:255], a[192:195], 0\n"
      "s_nop 0\n"
      "ds_read_b128 a[208:211], v0 offset:128\n"
      "v_fma_f32 v28, 0.5, v28, v52\n"
      "v_mfma_f32_16x16x32_bf16 v[80:83], v[244:247], a[192:195], 0\n"
      "s_add_u32 s54, s54, 196608\n"
      "ds_read_b64_tr_b16 a[244:245], v7 offset:53760\n"
      "ds_read_b64_tr_b16 a[246:247], v7 offset:53768\n"
      "v_fma_f32 v29, 0.5, v29, v53\n"
      "v_mfma_f32_16x16x32_bf16 v[84:87], v[236:239], a[192:195], 0\n"
      "s_add_u32 m0, s37, 112000\n"
      "v_fma_f32 v30, 0.5, v30, v54\n"
      "v_fma_f32 v31, 0.5, v31, v55\n"
      "v_mfma_f32_16x16x32_bf16 v[88:91], v[248:251], a[196:199], 0\n"
      "buffer_load_dwordx4 v8, s[4:7], s54 offen offset:0 nt lds\n"
      "buffer_load_dwordx4 v9, s[4:7], s54 offen offset:1024 nt lds\n"
      "ds_read_b128 a[212:215], v0 offset:4224\n"
      "v_mfma_f32_16x16x32_bf16 v[92:95], v[240:243], a[196:199], 0\n"
      "s_mov_b32 m0, s64\n"
      "v_fmac_f32 v40, s48, v64\n"
      "v_fmac_f32 v41, s48, v65\n"
      "v_fmac_f32 v42, s48, v66\n"
      "v_mfma_f32_16x16x32_bf16 v[96:99], v[232:235], a[196:199], 0\n"
      "s_waitcnt lgkmcnt(3)\n"
      "v_fmac_f32 v43, s48, v67\n"
      "v_fmac_f32 v28, s45, v40\n"
      "v_mfma_f32_16x16x32_bf16 v[76:79], v[228:231], a[200:203], v[76:79]\n"
      "ds_read_b128 a[192:195], v1 offset:0\n"
      "v_fmac_f32 v29, s45, v41\n"
      "v_mfma_f32_16x16x32_bf16 v[80:83], v[220:223], a[200:203], v[80:83]\n"
      "v_fmac_f32 v30, s45, v42\n"
      "v_fmac_f32_e64 v31, s45, v43\n"
      "v_mfma_f32_16x16x32_bf16 v[84:87], v[212:215], a[200:203], v[84:87]\n"
      "s_waitcnt lgkmcnt(1)\n"
      "ds_read2st64_b32 a[224:225], v11 offset0:177 offset1:179\n"
      "ds_read2st64_b32 a[226:227], v14 offset0:177 offset1:179\n"
      "v_fma_f32 v40, s47, v40, -v64\n"
      "v_mfma_f32_4x4x4_16b_bf16 v[20:23], a[216:217], a[244:245], 0\n"
      "v_fma_f32 v41, s47, v41, -v65\n"
      "v_mfma_f32_4x4x4_16b_bf16 v[24:27], a[216:217], a[246:247], 0\n"
      "v_fma_f32 v42, s47, v42, -v66\n"
      "v_fma_f32 v43, s47, v43, -v67\n"
      "v_mfma_f32_16x16x32_bf16 v[88:91], v[224:227], a[204:207], v[88:91]\n"
      "buffer_store_dwordx4 a[220:223], v16, s[8:11], s58 offen offset:0 sc1\n"
      "ds_read_b128 a[196:199], v1 offset:4096\n"
      "v_mfma_f32_16x16x32_bf16 v[92:95], v[216:219], a[204:207], v[92:95]\n"
      "v_cvt_pk_bf16_f32 v20, v20, v24\n"
      "v_cvt_pk_bf16_f32 v28, v28, v29\n"
      "v_mfma_f32_16x16x32_bf16 v[96:99], v[208:211], a[204:207], v[96:99]\n"
      "ds_write_addtid_b32 v28 offset:33792\n"
      "ds_write_addtid_b32 v20 offset:19456\n"
      "v_mfma_f32_16x16x32_bf16 v[76:79], v[204:207], a[208:211], v[76:79]\n"
      "s_add_u32 s54, s54, 196608\n"
      "ds_read_b128 a[200:203], v1 offset:64\n"
      "v_cvt_pk_bf16_f32 v30, v30, v31\n"
      "v_cvt_pk_bf16_f32 v21, v21, v25\n"
      "v_mfma_f32_16x16x32_bf16 v[80:83], v[196:199], a[208:211], v[80:83]\n"
      "s_add_u32 m0, s37, 116224\n"
      "v_cvt_pk_bf16_f32 v40, v40, v41\n"
      "v_cvt_pk_bf16_f32 v42, v42, v43\n"
      "v_mfma_f32_16x16x32_bf16 v[84:87], v[188:191], a[208:211], v[84:87]\n"
      "s_nop 0\n"
      "buffer_load_dwordx4 v8, s[4:7], s54 offen offset:0 nt lds\n"
      "buffer_load_dwordx4 v9, s[4:7], s54 offen offset:1024 nt lds\n"
      "v_mfma_f32_16x16x32_bf16 v[88:91], v[200:203], a[212:215], v[88:91]\n"
      "s_mov_b32 m0, s64\n"
      "ds_read_b128 a[204:207], v1 offset:4160\n"
      "ds_write_addtid_b32 v30 offset:34304\n"
      "ds_write_addtid_b32 v40 offset:35840\n"
      "v_mfma_f32_16x16x32_bf16 v[92:95], v[192:195], a[212:215], v[92:95]\n"
      "ds_write_addtid_b32 v42 offset:36352\n"
      "ds_write_addtid_b32 v21 offset:23552\n"
      "v_fmac_f32 v44, s46, v32\n"
      "v_cvt_pk_bf16_f32 v22, v22, v26\n"
      "v_mfma_f32_16x16x32_bf16 v[96:99], v[184:187], a[212:215], v[96:99]\n"
      "s_waitcnt lgkmcnt(5)\n"
      "ds_write_addtid_b32 v22 offset:27648\n"
      "v_fmac_f32 v45, s46, v33\n"
      "v_mfma_f32_16x16x32_bf16 v[76:79], v[180:183], a[192:195], v[76:79]\n"
      "ds_read_b128 a[208:211], v1 offset:128\n"
      "v_fmac_f32 v46, s46, v34\n"
      "v_mfma_f32_16x16x32_bf16 v[80:83], v[172:175], a[192:195], v[80:83]\n"
      "ds_read2st64_b32 a[228:229], v12 offset0:177 offset1:179\n"
      "ds_read2st64_b32 a[230:231], v15 offset0:177 offset1:179\n"
      "v_fmac_f32_e64 v47, s46, v35\n"
      "v_cvt_pk_bf16_f32 v23, v23, v27\n"
      "v_mfma_f32_16x16x32_bf16 v[84:87], v[164:167], a[192:195], v[84:87]\n"
      "ds_write_addtid_b32 v23 offset:31744\n"
      "v_fma_f32 v32, 0.5, v32, v56\n"
      "v_mfma_f32_16x16x32_bf16 v[88:91], v[176:179], a[196:199], v[88:91]\n"
      "ds_read_b128 a[212:215], v1 offset:4224\n"
      "v_fma_f32 v33, 0.5, v33, v57\n"
      "v_mfma_f32_16x16x32_bf16 v[92:95], v[168:171], a[196:199], v[92:95]\n"
      "buffer_store_dwordx4 a[224:227], v16, s[8:11], s58 offen offset:128 sc1\n"
      "v_mfma_f32_16x16x32_bf16 v[96:99], v[160:163], a[196:199], v[96:99]\n"
      "v_fma_f32 v34, 0.5, v34, v58\n"
      "v_fma_f32 v35, 0.5, v35, v59\n"
      "v_mfma_f32_16x16x32_bf16 v[76:79], v[156:159], a[200:203], v[76:79]\n"
      "s_waitcnt lgkmcnt(1)\n"
      "ds_read_b128 a[192:195], v2 offset:0\n"
      "v_fmac_f32 v44, s48, v68\n"
      "v_mfma_f32_16x16x32_bf16 v[80:83], v[148:151], a[200:203], v[80:83]\n"
      "s_add_u32 s54, s54, 196608\n"
      "v_fmac_f32 v45, s48, v69\n"
      "v_fmac_f32 v46, s48, v70\n"
      "v_mfma_f32_16x16x32_bf16 v[84:87], v[140:143], a[200:203], v[84:87]\n"
      "s_add_u32 m0, s37, 120328\n"
      "v_fmac_f32 v47, s48, v71\n"
      "v_fmac_f32 v32, s45, v44\n"
      "v_mfma_f32_16x16x32_bf16 v[88:91], v[152:155], a[204:207], v[88:91]\n"
      "buffer_load_dwordx4 v8, s[4:7], s54 offen offset:0 nt lds\n"
      "buffer_load_dwordx4 v9, s[4:7], s54 offen offset:1024 nt lds\n"
      "ds_read_b128 a[196:199], v2 offset:4096\n"
      "v_mfma_f32_16x16x32_bf16 v[92:95], v[144:147], a[204:207], v[92:95]\n"
      "s_mov_b32 m0, s64\n"
      "v_fmac_f32 v33, s45, v45\n"
      "v_fmac_f32 v34, s45, v46\n"
      "v_mfma_f32_16x16x32_bf16 v[96:99], v[136:139], a[204:207], v[96:99]\n"
      "ds_read_b64_tr_b16 a[248:249], v7 offset:54272\n"
      "ds_read_b64_tr_b16 a[250:251], v7 offset:54280\n"
      "v_fmac_f32 v35, s45, v47\n"
      "v_mfma_f32_16x16x32_bf16 v[76:79], v[132:135], a[208:211], v[76:79]\n"
      "ds_read_b128 a[200:203], v2 offset:64\n"
      "v_fma_f32 v44, s47, v44, -v68\n"
      "v_mfma_f32_16x16x32_bf16 v[80:83], v[124:127], a[208:211], v[80:83]\n"
      "s_nop 0\n"
      "v_fma_f32 v45, s47, v45, -v69\n"
      "v_fma_f32 v46, s47, v46, -v70\n"
      "v_mfma_f32_16x16x32_bf16 v[84:87], v[116:119], a[208:211], v[84:87]\n"
      "s_waitcnt lgkmcnt(3)\n"
      "v_fma_f32 v47, s47, v47, -v71\n"
      "v_cvt_pk_bf16_f32 v32, v32, v33\n"
      "v_mfma_f32_16x16x32_bf16 v[88:91], v[128:131], a[212:215], v[88:91]\n"
      "buffer_store_dwordx4 a[228:231], v16, s[8:11], s58 offen offset:256 sc1\n"
      "ds_read_b128 a[204:207], v2 offset:4160\n"
      "v_mfma_f32_16x16x32_bf16 v[92:95], v[120:123], a[212:215], v[92:95]\n"
      "ds_write_addtid_b32 v32 offset:41988\n"
      "ds_read2st64_b32 a[232:233], v10 offset0:0 offset1:2\n"
      "ds_read2st64_b32 a[234:235], v13 offset0:0 offset1:2\n"
      "v_cvt_pk_bf16_f32 v34, v34, v35\n"
      "v_mfma_f32_16x16x32_bf16 v[96:99], v[112:115], a[212:215], v[96:99]\n"
      "s_nop 0\n"
      "ds_write_addtid_b32 v34 offset:42500\n"
      "v_cvt_pk_bf16_f32 v44, v44, v45\n"
      "v_mfma_f32_16x16x32_bf16 v[76:79], v[108:111], a[192:195], v[76:79]\n"
      "s_waitcnt lgkmcnt(5)\n"
      "ds_read_b128 a[208:211], v2 offset:128\n"
      "ds_write_addtid_b32 v44 offset:44036\n"
      "v_cvt_pk_bf16_f32 v46, v46, v47\n"
      "v_mfma_f32_4x4x4_16b_bf16 v[20:23], a[216:217], a[248:249], 0\n"
      "v_fmac_f32 v48, s46, v36\n"
      "v_mfma_f32_4x4x4_16b_bf16 v[24:27], a[216:217], a[250:251], 0\n"
      "ds_write_addtid_b32 v46 offset:44548\n"
      "v_fmac_f32 v49, s46, v37\n"
      "v_fmac_f32 v50, s46, v38\n"
      "v_mfma_f32_16x16x32_bf16 v[80:83], v[100:103], a[192:195], v[80:83]\n"
      "v_fmac_f32 v51, s46, v39\n"
      "v_cvt_pk_bf16_f32 v20, v20, v24\n"
      "v_mfma_f32_16x16x32_bf16 v[84:87], a[160:163], a[192:195], v[84:87]\n"
      "ds_write_addtid_b32 v20 offset:19968\n"
      "v_cvt_pk_bf16_f32 v21, v21, v25\n"
      "v_fma_f32 v36, 0.5, v36, v60\n"
      "v_mfma_f32_16x16x32_bf16 v[88:91], v[104:107], a[196:199], v[88:91]\n"
      "s_add_u32 s54, s54, 196608\n"
      "ds_read_b128 a[212:215], v2 offset:4224\n"
      "ds_write_addtid_b32 v21 offset:24064\n"
      "v_mfma_f32_16x16x32_bf16 v[92:95], a[164:167], a[196:199], v[92:95]\n"
      "s_add_u32 m0, s37, 124552\n"
      "v_fma_f32 v37, 0.5, v37, v61\n"
      "v_cvt_pk_bf16_f32 v22, v22, v26\n"
      "v_mfma_f32_16x16x32_bf16 v[96:99], a[156:159], a[196:199], v[96:99]\n"
      "buffer_load_dwordx4 v8, s[4:7], s54 offen offset:0 nt lds\n"
      "buffer_load_dwordx4 v9, s[4:7], s54 offen offset:1024 nt lds\n"
      "ds_read2st64_b32 a[236:237], v11 offset0:0 offset1:2\n"
      "ds_read2st64_b32 a[238:239], v14 offset0:0 offset1:2\n"
      "v_mfma_f32_16x16x32_bf16 v[76:79], a[152:155], a[200:203], v[76:79]\n"
      "s_mov_b32 m0, s64\n"
      "ds_read_b128 a[192:195], v3 offset:33792\n"
      "ds_write_addtid_b32 v22 offset:28160\n"
      "v_mfma_f32_16x16x32_bf16 v[80:83], a[144:147], a[200:203], v[80:83]\n"
      "v_fma_f32 v38, 0.5, v38, v62\n"
      "v_cvt_pk_bf16_f32 v23, v23, v27\n"
      "v_mfma_f32_16x16x32_bf16 v[84:87], a[136:139], a[200:203], v[84:87]\n"
      "s_waitcnt lgkmcnt(4)\n"
      "ds_write_addtid_b32 v23 offset:32256\n"
      "v_fma_f32 v39, 0.5, v39, v63\n"
      "v_mfma_f32_16x16x32_bf16 v[88:91], a[148:151], a[204:207], v[88:91]\n"
      "ds_read_b128 a[196:199], v3 offset:33856\n"
      "v_mfma_f32_16x16x32_bf16 v[92:95], a[140:143], a[204:207], v[92:95]\n"
      "v_fmac_f32 v48, s48, v72\n"
      "v_fmac_f32 v49, s48, v73\n"
      "v_fmac_f32 v50, s48, v74\n"
      "v_mfma_f32_16x16x32_bf16 v[96:99], a[132:135], a[204:207], v[96:99]\n"
      "s_nop 0\n"
      "buffer_store_dwordx4 a[232:235], v16, s[8:11], s59 offen offset:0 sc1\n"
      "v_mfma_f32_16x16x32_bf16 v[76:79], a[128:131], a[208:211], v[76:79]\n"
      "ds_read_b128 a[200:203], v3 offset:33920\n"
      "v_fmac_f32 v51, s48, v75\n"
      "v_mfma_f32_16x16x32_bf16 v[80:83], a[120:123], a[208:211], v[80:83]\n"
      "ds_read2st64_b32 a[240:241], v12 offset0:0 offset1:2\n"
      "ds_read2st64_b32 a[242:243], v15 offset0:0 offset1:2\n"
      "v_fmac_f32 v36, s45, v48\n"
      "v_fmac_f32_e64 v37, s45, v49\n"
      "v_mfma_f32_16x16x32_bf16 v[84:87], a[112:115], a[208:211], v[84:87]\n"
      "v_fmac_f32 v38, s45, v50\n"
      "v_fmac_f32 v39, s45, v51\n"
      "v_mfma_f32_16x16x32_bf16 v[88:91], a[124:127], a[212:215], v[88:91]\n"
      "ds_read_b128 a[204:207], v4 offset:33792\n"
      "v_fma_f32 v48, s47, v48, -v72\n"
      "v_mfma_f32_16x16x32_bf16 v[92:95], a[116:119], a[212:215], v[92:95]\n"
      "v_fma_f32 v49, s47, v49, -v73\n"
      "v_fma_f32 v50, s47, v50, -v74\n"
      "v_mfma_f32_16x16x32_bf16 v[96:99], a[108:111], a[212:215], v[96:99]\n"
      "s_waitcnt lgkmcnt(2)\n"
      "v_fma_f32 v51, s47, v51, -v75\n"
      "v_add_f32 v76, v76, v88\n"
      "v_add_f32 v77, v77, v89\n"
      "v_mfma_f32_16x16x32_bf16 v[52:55], a[0:3], a[192:195], 0\n"
      "s_nop 0\n"
      "ds_read_b128 a[208:211], v4 offset:33856\n"
      "ds_write_addtid_b32 v76 offset:45312\n"
      "ds_write_addtid_b32 v77 offset:45824\n"
      "v_mfma_f32_16x16x32_bf16 v[56:59], a[4:7], a[192:195], 0\n"
      "buffer_store_dwordx4 a[236:239], v16, s[8:11], s59 offen offset:128 sc1\n"
      "v_add_f32 v78, v78, v90\n"
      "v_mfma_f32_16x16x32_bf16 v[60:63], a[8:11], a[192:195], 0\n"
      "ds_write_addtid_b32 v78 offset:46336\n"
      "v_cvt_pk_bf16_f32 v36, v36, v37\n"
      "v_add_f32 v79, v79, v91\n"
      "v_mfma_f32_16x16x32_bf16 v[52:55], a[12:15], a[196:199], v[52:55]\n"
      "ds_read_b128 a[212:215], v4 offset:33920\n"
      "ds_write_addtid_b32 v36 offset:37888\n"
      "ds_write_addtid_b32 v79 offset:46848\n"
      "v_mfma_f32_16x16x32_bf16 v[56:59], a[16:19], a[196:199], v[56:59]\n"
      "v_add_f32 v80, v80, v92\n"
      "v_cvt_pk_bf16_f32 v38, v38, v39\n"
      "v_mfma_f32_16x16x32_bf16 v[60:63], a[20:23], a[196:199], v[60:63]\n"
      "ds_write_addtid_b32 v38 offset:38400\n"
      "ds_write_addtid_b32 v80 offset:47360\n"
      "v_add_f32 v81, v81, v93\n"
      "v_mfma_f32_16x16x32_bf16 v[52:55], a[24:27], a[200:203], v[52:55]\n"
      "ds_read_b128 a[192:195], v5 offset:33792\n"
      "ds_write_addtid_b32 v81 offset:47872\n"
      "v_mfma_f32_16x16x32_bf16 v[56:59], a[28:31], a[200:203], v[56:59]\n"
      "v_add_f32 v82, v82, v94\n"
      "v_cvt_pk_bf16_f32 v48, v48, v49\n"
      "v_add_f32_e64 v83, v83, v95\n"
      "v_mfma_f32_16x16x32_bf16 v[60:63], a[32:35], a[200:203], v[60:63]\n"
      "s_waitcnt lgkmcnt(4)\n"
      "ds_write_addtid_b32 v48 offset:39936\n"
      "ds_write_addtid_b32 v82 offset:48384\n"
      "v_mfma_f32_16x16x32_bf16 v[52:55], a[36:39], a[204:207], v[52:55]\n"
      "buffer_store_dwordx4 a[240:243], v16, s[8:11], s59 offen offset:256 sc1\n"
      "ds_read_b128 a[196:199], v5 offset:33856\n"
      "v_cvt_pk_bf16_f32 v50, v50, v51\n"
      "v_mfma_f32_16x16x32_bf16 v[56:59], a[40:43], a[204:207], v[56:59]\n"
      "ds_write_addtid_b32 v50 offset:40448\n"
      "ds_write_addtid_b32 v83 offset:48896\n"
      "v_mfma_f32_16x16x32_bf16 v[60:63], a[44:47], a[204:207], v[60:63]\n"
      "v_add_f32 v84, v84, v96\n"
      "v_add_f32_e64 v85, v85, v97\n"
      "v_add_f32_e64 v86, v86, v98\n"
      "v_add_f32_e64 v87, v87, v99\n"
      "v_mfma_f32_16x16x32_bf16 v[52:55], a[48:51], a[208:211], v[52:55]\n"
      "s_nop 0\n"
      "ds_read_b128 a[200:203], v5 offset:33920\n"
      "ds_write_addtid_b32 v84 offset:49408\n"
      "v_mfma_f32_16x16x32_bf16 v[56:59], a[52:55], a[208:211], v[56:59]\n"
      "s_nop 0\n"
      "ds_write_addtid_b32 v85 offset:49920\n"
      "ds_write_addtid_b32 v86 offset:50432\n"
      "v_mfma_f32_16x16x32_bf16 v[60:63], a[56:59], a[208:211], v[60:63]\n"
      "s_nop 0\n"
      "ds_write_addtid_b32 v87 offset:50944\n"
      "v_fma_f32 v40, -2.0, v88, v76\n"
      "v_mfma_f32_16x16x32_bf16 v[52:55], a[60:63], a[212:215], v[52:55]\n"
      "s_nop 0\n"
      "v_fma_f32 v41, -2.0, v89, v77\n"
      "v_fma_f32 v42, -2.0, v90, v78\n"
      "v_mfma_f32_16x16x32_bf16 v[56:59], a[64:67], a[212:215], v[56:59]\n"
      "s_waitcnt vmcnt(26)\n"
      "v_fma_f32 v43, -2.0, v91, v79\n"
      "v_fma_f32 v44, -2.0, v92, v80\n"
      "v_mfma_f32_16x16x32_bf16 v[60:63], a[68:71], a[212:215], v[60:63]\n"
      "s_waitcnt lgkmcnt(0)\n"
      "v_fma_f32 v45, -2.0, v93, v81\n"
      "s_barrier\n"
      "v_mfma_f32_16x16x32_bf16 v[52:55], a[72:75], a[192:195], v[52:55]\n"
      "s_add_u32 s58, s40, 0\n"
      "ds_read_b128 a[204:207], v0 offset:16384\n"
      "ds_read2st64_b32 v[28:29], v6 offset0:0 offset1:2\n"
      "ds_read2st64_b32 v[30:31], v6 offset0:4 offset1:6\n"
      "v_mfma_f32_16x16x32_bf16 v[56:59], a[76:79], a[192:195], v[56:59]\n"
      "s_mov_b32 s40, s62\n"
      "ds_read_b128 a[208:211], v0 offset:20480\n"
      "ds_read2st64_b32 v[32:33], v6 offset0:8 offset1:10\n"
      "v_mfma_f32_16x16x32_bf16 v[60:63], a[80:83], a[192:195], v[60:63]\n"
      "s_add_u32 s62, s62, 786432\n"
      "ds_read2st64_b32 v[34:35], v6 offset0:12 offset1:14\n"
      "v_fma_f32 v46, -2.0, v94, v82\n"
      "v_mfma_f32_16x16x32_bf16 v[52:55], a[84:87], a[196:199], v[52:55]\n"
      "s_add_u32 s59, s58, 196608\n"
      "ds_read_b128 a[212:215], v0 offset:16448\n"
      "v_fma_f32 v47, -2.0, v95, v83\n"
      "v_fma_f32 v48, -2.0, v96, v84\n"
      "v_mfma_f32_16x16x32_bf16 v[56:59], a[88:91], a[196:199], v[56:59]\n"
      "ds_read2st64_b32 a[220:221], v10 offset0:181 offset1:183\n"
      "v_fma_f32 v49, -2.0, v97, v85\n"
      "v_mfma_f32_16x16x32_bf16 v[60:63], a[92:95], a[196:199], v[60:63]\n"
      "ds_read2st64_b32 a[222:223], v13 offset0:181 offset1:183\n"
      "v_fma_f32 v50, -2.0, v98, v86\n"
      "v_fma_f32 v51, -2.0, v99, v87\n"
      "v_mfma_f32_16x16x32_bf16 v[52:55], a[96:99], a[200:203], v[52:55]\n"
      "ds_read_b128 a[192:195], v0 offset:20544\n"
      "v_mfma_f32_16x16x32_bf16 v[56:59], a[100:103], a[200:203], v[56:59]\n"
      "ds_read2st64_b32 v[36:37], v6 offset0:16 offset1:18\n"
      "ds_read2st64_b32 v[38:39], v6 offset0:20 offset1:22\n"
      "v_mfma_f32_16x16x32_bf16 v[60:63], a[104:107], a[200:203], v[60:63]\n"
      "s_waitcnt lgkmcnt(7)\n"
      "v_fmac_f32 v40, s46, v28\n"
      "v_fmac_f32 v41, s46, v29\n"
      "v_fmac_f32 v42, s46, v30\n"
      "v_fmac_f32 v43, s46, v31\n"
      "v_mfma_f32_16x16x32_bf16 v[76:79], v[252:255], a[204:207], 0\n"
      "s_nop 0\n"
      "ds_read_b128 a[196:199], v0 offset:16512\n"
      "v_fma_f32 v28, 0.5, v28, v64\n"
      "v_mfma_f32_16x16x32_bf16 v[80:83], v[244:247], a[204:207], 0\n"
      "s_add_u32 s54, s54, 196608\n"
      "ds_read_b64_tr_b16 a[244:245], v7 offset:3072\n"
      "ds_read_b64_tr_b16 a[246:247], v7 offset:3080\n"
      "v_fma_f32 v29, 0.5, v29, v65\n"
      "v_mfma_f32_16x16x32_bf16 v[84:87], v[236:239], a[204:207], 0\n"
      "s_add_u32 m0, s37, 128896\n"
      "v_fma_f32 v30, 0.5, v30, v66\n"
      "v_fma_f32 v31, 0.5, v31, v67\n"
      "v_mfma_f32_16x16x32_bf16 v[88:91], v[248:251], a[208:211], 0\n"
      "buffer_load_dwordx4 v8, s[4:7], s54 offen offset:0 nt lds\n"
      "buffer_load_dwordx4 v9, s[4:7], s54 offen offset:1024 nt lds\n"
      "ds_read_b128 a[200:203], v0 offset:20608\n"
      "v_mfma_f32_16x16x32_bf16 v[92:95], v[240:243], a[208:211], 0\n"
      "s_mov_b32 m0, s64\n"
      "v_fmac_f32 v40, s48, v52\n"
      "v_fmac_f32 v41, s48, v53\n"
      "v_fmac_f32 v42, s48, v54\n"
      "v_mfma_f32_16x16x32_bf16 v[96:99], v[232:235], a[208:211], 0\n"
      "s_waitcnt lgkmcnt(3)\n"
      "v_fmac_f32 v43, s48, v55\n"
      "v_fmac_f32 v28, s45, v40\n"
      "v_mfma_f32_16x16x32_bf16 v[76:79], v[228:231], a[212:215], v[76:79]\n"
      "ds_read_b128 a[204:207], v1 offset:16384\n"
      "v_fmac_f32 v29, s45, v41\n"
      "v_mfma_f32_16x16x32_bf16 v[80:83], v[220:223], a[212:215], v[80:83]\n"
      "v_fmac_f32 v30, s45, v42\n"
      "v_fmac_f32_e64 v31, s45, v43\n"
      "v_mfma_f32_16x16x32_bf16 v[84:87], v[212:215], a[212:215], v[84:87]\n"
      "s_waitcnt lgkmcnt(1)\n"
      "ds_read2st64_b32 a[224:225], v11 offset0:181 offset1:183\n"
      "ds_read2st64_b32 a[226:227], v14 offset0:181 offset1:183\n"
      "v_fma_f32 v40, s47, v40, -v52\n"
      "v_mfma_f32_4x4x4_16b_bf16 v[20:23], a[216:217], a[244:245], 0\n"
      "v_fma_f32 v41, s47, v41, -v53\n"
      "v_mfma_f32_4x4x4_16b_bf16 v[24:27], a[216:217], a[246:247], 0\n"
      "v_fma_f32 v42, s47, v42, -v54\n"
      "v_fma_f32 v43, s47, v43, -v55\n"
      "v_mfma_f32_16x16x32_bf16 v[88:91], v[224:227], a[192:195], v[88:91]\n"
      "buffer_store_dwordx4 a[220:223], v16, s[8:11], s58 offen offset:0 sc1\n"
      "ds_read_b128 a[208:211], v1 offset:20480\n"
      "v_mfma_f32_16x16x32_bf16 v[92:95], v[216:219], a[192:195], v[92:95]\n"
      "v_cvt_pk_bf16_f32 v20, v20, v24\n"
      "v_cvt_pk_bf16_f32 v28, v28, v29\n"
      "v_mfma_f32_16x16x32_bf16 v[96:99], v[208:211], a[192:195], v[96:99]\n"
      "ds_write_addtid_b32 v28 offset:32768\n"
      "ds_write_addtid_b32 v20 offset:3072\n"
      "v_mfma_f32_16x16x32_bf16 v[76:79], v[204:207], a[196:199], v[76:79]\n"
      "s_add_u32 s54, s54, 196608\n"
      "ds_read_b128 a[212:215], v1 offset:16448\n"
      "v_cvt_pk_bf16_f32 v30, v30, v31\n"
      "v_cvt_pk_bf16_f32 v21, v21, v25\n"
      "v_mfma_f32_16x16x32_bf16 v[80:83], v[196:199], a[196:199], v[80:83]\n"
      "s_add_u32 m0, s37, 133120\n"
      "v_cvt_pk_bf16_f32 v40, v40, v41\n"
      "v_cvt_pk_bf16_f32 v42, v42, v43\n"
      "v_mfma_f32_16x16x32_bf16 v[84:87], v[188:191], a[196:199], v[84:87]\n"
      "s_nop 0\n"
      "buffer_load_dwordx4 v8, s[4:7], s54 offen offset:0 nt lds\n"
      "buffer_load_dwordx4 v9, s[4:7], s54 offen offset:1024 nt lds\n"
      "v_mfma_f32_16x16x32_bf16 v[88:91], v[200:203], a[200:203], v[88:91]\n"
      "s_mov_b32 m0, s64\n"
      "ds_read_b128 a[192:195], v1 offset:20544\n"
      "ds_write_addtid_b32 v30 offset:33280\n"
      "ds_write_addtid_b32 v40 offset:34816\n"
      "v_mfma_f32_16x16x32_bf16 v[92:95], v[192:195], a[200:203], v[92:95]\n"
      "ds_write_addtid_b32 v42 offset:35328\n"
      "ds_write_addtid_b32 v21 offset:7168\n"
      "v_fmac_f32 v44, s46, v32\n"
      "v_cvt_pk_bf16_f32 v22, v22, v26\n"
      "v_mfma_f32_16x16x32_bf16 v[96:99], v[184:187], a[200:203], v[96:99]\n"
      "s_waitcnt lgkmcnt(5)\n"
      "ds_write_addtid_b32 v22 offset:11264\n"
      "v_fmac_f32 v45, s46, v33\n"
      "v_mfma_f32_16x16x32_bf16 v[76:79], v[180:183], a[204:207], v[76:79]\n"
      "ds_read_b128 a[196:199], v1 offset:16512\n"
      "v_fmac_f32 v46, s46, v34\n"
      "v_mfma_f32_16x16x32_bf16 v[80:83], v[172:175], a[204:207], v[80:83]\n"
      "ds_read2st64_b32 a[228:229], v12 offset0:181 offset1:183\n"
      "ds_read2st64_b32 a[230:231], v15 offset0:181 offset1:183\n"
      "v_fmac_f32_e64 v47, s46, v35\n"
      "v_cvt_pk_bf16_f32 v23, v23, v27\n"
      "v_mfma_f32_16x16x32_bf16 v[84:87], v[164:167], a[204:207], v[84:87]\n"
      "ds_write_addtid_b32 v23 offset:15360\n"
      "v_fma_f32 v32, 0.5, v32, v68\n"
      "v_mfma_f32_16x16x32_bf16 v[88:91], v[176:179], a[208:211], v[88:91]\n"
      "ds_read_b128 a[200:203], v1 offset:20608\n"
      "v_fma_f32 v33, 0.5, v33, v69\n"
      "v_mfma_f32_16x16x32_bf16 v[92:95], v[168:171], a[208:211], v[92:95]\n"
      "buffer_store_dwordx4 a[224:227], v16, s[8:11], s58 offen offset:128 sc1\n"
      "v_mfma_f32_16x16x32_bf16 v[96:99], v[160:163], a[208:211], v[96:99]\n"
      "v_fma_f32 v34, 0.5, v34, v70\n"
      "v_fma_f32 v35, 0.5, v35, v71\n"
      "v_mfma_f32_16x16x32_bf16 v[76:79], v[156:159], a[212:215], v[76:79]\n"
      "s_waitcnt lgkmcnt(1)\n"
      "ds_read_b128 a[204:207], v2 offset:16384\n"
      "v_fmac_f32 v44, s48, v56\n"
      "v_mfma_f32_16x16x32_bf16 v[80:83], v[148:151], a[212:215], v[80:83]\n"
      "s_add_u32 s54, s54, 196608\n"
      "v_fmac_f32 v45, s48, v57\n"
      "v_fmac_f32 v46, s48, v58\n"
      "v_mfma_f32_16x16x32_bf16 v[84:87], v[140:143], a[212:215], v[84:87]\n"
      "s_add_u32 m0, s37, 137224\n"
      "v_fmac_f32 v47, s48, v59\n"
      "v_fmac_f32 v32, s45, v44\n"
      "v_mfma_f32_16x16x32_bf16 v[88:91], v[152:155], a[192:195], v[88:91]\n"
      "buffer_load_dwordx4 v8, s[4:7], s54 offen offset:0 nt lds\n"
      "buffer_load_dwordx4 v9, s[4:7], s54 offen offset:1024 nt lds\n"
      "ds_read_b128 a[208:211], v2 offset:20480\n"
      "v_mfma_f32_16x16x32_bf16 v[92:95], v[144:147], a[192:195], v[92:95]\n"
      "s_mov_b32 m0, s64\n"
      "v_fmac_f32 v33, s45, v45\n"
      "v_fmac_f32 v34, s45, v46\n"
      "v_mfma_f32_16x16x32_bf16 v[96:99], v[136:139], a[192:195], v[96:99]\n"
      "ds_read_b64_tr_b16 a[248:249], v7 offset:3584\n"
      "ds_read_b64_tr_b16 a[250:251], v7 offset:3592\n"
      "v_fmac_f32 v35, s45, v47\n"
      "v_mfma_f32_16x16x32_bf16 v[76:79], v[132:135], a[196:199], v[76:79]\n"
      "ds_read_b128 a[212:215], v2 offset:16448\n"
      "v_fma_f32 v44, s47, v44, -v56\n"
      "v_mfma_f32_16x16x32_bf16 v[80:83], v[124:127], a[196:199], v[80:83]\n"
      "s_nop 0\n"
      "v_fma_f32 v45, s47, v45, -v57\n"
      "v_fma_f32 v46, s47, v46, -v58\n"
      "v_mfma_f32_16x16x32_bf16 v[84:87], v[116:119], a[196:199], v[84:87]\n"
      "s_waitcnt lgkmcnt(3)\n"
      "v_fma_f32 v47, s47, v47, -v59\n"
      "v_cvt_pk_bf16_f32 v32, v32, v33\n"
      "v_mfma_f32_16x16x32_bf16 v[88:91], v[128:131], a[200:203], v[88:91]\n"
      "buffer_store_dwordx4 a[228:231], v16, s[8:11], s58 offen offset:256 sc1\n"
      "ds_read_b128 a[192:195], v2 offset:20544\n"
      "v_mfma_f32_16x16x32_bf16 v[92:95], v[120:123], a[200:203], v[92:95]\n"
      "ds_write_addtid_b32 v32 offset:40964\n"
      "ds_read2st64_b32 a[232:233], v10 offset0:4 offset1:6\n"
      "ds_read2st64_b32 a[234:235], v13 offset0:4 offset1:6\n"
      "v_cvt_pk_bf16_f32 v34, v34, v35\n"
      "v_mfma_f32_16x16x32_bf16 v[96:99], v[112:115], a[200:203], v[96:99]\n"
      "s_nop 0\n"
      "ds_write_addtid_b32 v34 offset:41476\n"
      "v_cvt_pk_bf16_f32 v44, v44, v45\n"
      "v_mfma_f32_16x16x32_bf16 v[76:79], v[108:111], a[204:207], v[76:79]\n"
      "s_waitcnt lgkmcnt(5)\n"
      "ds_read_b128 a[196:199], v2 offset:16512\n"
      "ds_write_addtid_b32 v44 offset:43012\n"
      "v_cvt_pk_bf16_f32 v46, v46, v47\n"
      "v_mfma_f32_4x4x4_16b_bf16 v[20:23], a[216:217], a[248:249], 0\n"
      "v_fmac_f32 v48, s46, v36\n"
      "v_mfma_f32_4x4x4_16b_bf16 v[24:27], a[216:217], a[250:251], 0\n"
      "ds_write_addtid_b32 v46 offset:43524\n"
      "v_fmac_f32 v49, s46, v37\n"
      "v_fmac_f32 v50, s46, v38\n"
      "v_mfma_f32_16x16x32_bf16 v[80:83], v[100:103], a[204:207], v[80:83]\n"
      "v_fmac_f32 v51, s46, v39\n"
      "v_cvt_pk_bf16_f32 v20, v20, v24\n"
      "v_mfma_f32_16x16x32_bf16 v[84:87], a[160:163], a[204:207], v[84:87]\n"
      "ds_write_addtid_b32 v20 offset:3584\n"
      "v_cvt_pk_bf16_f32 v21, v21, v25\n"
      "v_fma_f32 v36, 0.5, v36, v72\n"
      "v_mfma_f32_16x16x32_bf16 v[88:91], v[104:107], a[208:211], v[88:91]\n"
      "s_add_u32 s54, s54, 196608\n"
      "ds_read_b128 a[200:203], v2 offset:20608\n"
      "ds_write_addtid_b32 v21 offset:7680\n"
      "v_mfma_f32_16x16x32_bf16 v[92:95], a[164:167], a[208:211], v[92:95]\n"
      "s_add_u32 m0, s37, 141448\n"
      "v_fma_f32 v37, 0.5, v37, v73\n"
      "v_cvt_pk_bf16_f32 v22, v22, v26\n"
      "v_mfma_f32_16x16x32_bf16 v[96:99], a[156:159], a[208:211], v[96:99]\n"
      "buffer_load_dwordx4 v8, s[4:7], s54 offen offset:0 nt lds\n"
      "buffer_load_dwordx4 v9, s[4:7], s54 offen offset:1024 nt lds\n"
      "ds_read2st64_b32 a[236:237], v11 offset0:4 offset1:6\n"
      "ds_read2st64_b32 a[238:239], v14 offset0:4 offset1:6\n"
      "v_mfma_f32_16x16x32_bf16 v[76:79], a[152:155], a[212:215], v[76:79]\n"
      "s_mov_b32 m0, s64\n"
      "ds_read_b128 a[204:207], v3 offset:50688\n"
      "ds_write_addtid_b32 v22 offset:11776\n"
      "v_mfma_f32_16x16x32_bf16 v[80:83], a[144:147], a[212:215], v[80:83]\n"
      "v_fma_f32 v38, 0.5, v38, v74\n"
      "v_cvt_pk_bf16_f32 v23, v23, v27\n"
      "v_mfma_f32_16x16x32_bf16 v[84:87], a[136:139], a[212:215], v[84:87]\n"
      "s_waitcnt lgkmcnt(4)\n"
      "ds_write_addtid_b32 v23 offset:15872\n"
      "v_fma_f32 v39, 0.5, v39, v75\n"
      "v_mfma_f32_16x16x32_bf16 v[88:91], a[148:151], a[192:195], v[88:91]\n"
      "ds_read_b128 a[208:211], v3 offset:50752\n"
      "v_mfma_f32_16x16x32_bf16 v[92:95], a[140:143], a[192:195], v[92:95]\n"
      "v_fmac_f32 v48, s48, v60\n"
      "v_fmac_f32 v49, s48, v61\n"
      "v_fmac_f32 v50, s48, v62\n"
      "v_mfma_f32_16x16x32_bf16 v[96:99], a[132:135], a[192:195], v[96:99]\n"
      "s_nop 0\n"
      "buffer_store_dwordx4 a[232:235], v16, s[8:11], s59 offen offset:0 sc1\n"
      "v_mfma_f32_16x16x32_bf16 v[76:79], a[128:131], a[196:199], v[76:79]\n"
      "ds_read_b128 a[212:215], v3 offset:50816\n"
      "v_fmac_f32 v51, s48, v63\n"
      "v_mfma_f32_16x16x32_bf16 v[80:83], a[120:123], a[196:199], v[80:83]\n"
      "ds_read2st64_b32 a[240:241], v12 offset0:4 offset1:6\n"
      "ds_read2st64_b32 a[242:243], v15 offset0:4 offset1:6\n"
      "v_fmac_f32 v36, s45, v48\n"
      "v_fmac_f32_e64 v37, s45, v49\n"
      "v_mfma_f32_16x16x32_bf16 v[84:87], a[112:115], a[196:199], v[84:87]\n"
      "v_fmac_f32 v38, s45, v50\n"
      "v_fmac_f32 v39, s45, v51\n"
      "v_mfma_f32_16x16x32_bf16 v[88:91], a[124:127], a[200:203], v[88:91]\n"
      "ds_read_b128 a[192:195], v4 offset:50688\n"
      "v_fma_f32 v48, s47, v48, -v60\n"
      "v_mfma_f32_16x16x32_bf16 v[92:95], a[116:119], a[200:203], v[92:95]\n"
      "v_fma_f32 v49, s47, v49, -v61\n"
      "v_fma_f32 v50, s47, v50, -v62\n"
      "v_mfma_f32_16x16x32_bf16 v[96:99], a[108:111], a[200:203], v[96:99]\n"
      "s_waitcnt lgkmcnt(2)\n"
      "v_fma_f32 v51, s47, v51, -v63\n"
      "v_add_f32 v76, v76, v88\n"
      "v_add_f32 v77, v77, v89\n"
      "v_mfma_f32_16x16x32_bf16 v[64:67], a[0:3], a[204:207], 0\n"
      "s_nop 0\n"
      "ds_read_b128 a[196:199], v4 offset:50752\n"
      "ds_write_addtid_b32 v76 offset:51456\n"
      "ds_write_addtid_b32 v77 offset:51968\n"
      "v_mfma_f32_16x16x32_bf16 v[68:71], a[4:7], a[204:207], 0\n"
      "buffer_store_dwordx4 a[236:239], v16, s[8:11], s59 offen offset:128 sc1\n"
      "v_add_f32 v78, v78, v90\n"
      "v_mfma_f32_16x16x32_bf16 v[72:75], a[8:11], a[204:207], 0\n"
      "ds_write_addtid_b32 v78 offset:52480\n"
      "v_cvt_pk_bf16_f32 v36, v36, v37\n"
      "v_add_f32 v79, v79, v91\n"
      "v_mfma_f32_16x16x32_bf16 v[64:67], a[12:15], a[208:211], v[64:67]\n"
      "ds_read_b128 a[200:203], v4 offset:50816\n"
      "ds_write_addtid_b32 v36 offset:36864\n"
      "ds_write_addtid_b32 v79 offset:52992\n"
      "v_mfma_f32_16x16x32_bf16 v[68:71], a[16:19], a[208:211], v[68:71]\n"
      "v_add_f32 v80, v80, v92\n"
      "v_cvt_pk_bf16_f32 v38, v38, v39\n"
      "v_mfma_f32_16x16x32_bf16 v[72:75], a[20:23], a[208:211], v[72:75]\n"
      "ds_write_addtid_b32 v38 offset:37376\n"
      "ds_write_addtid_b32 v80 offset:53504\n"
      "v_add_f32 v81, v81, v93\n"
      "v_mfma_f32_16x16x32_bf16 v[64:67], a[24:27], a[212:215], v[64:67]\n"
      "ds_read_b128 a[204:207], v5 offset:50688\n"
      "ds_write_addtid_b32 v81 offset:54016\n"
      "v_mfma_f32_16x16x32_bf16 v[68:71], a[28:31], a[212:215], v[68:71]\n"
      "v_add_f32 v82, v82, v94\n"
      "v_cvt_pk_bf16_f32 v48, v48, v49\n"
      "v_add_f32_e64 v83, v83, v95\n"
      "v_mfma_f32_16x16x32_bf16 v[72:75], a[32:35], a[212:215], v[72:75]\n"
      "s_waitcnt lgkmcnt(4)\n"
      "ds_write_addtid_b32 v48 offset:38912\n"
      "ds_write_addtid_b32 v82 offset:54528\n"
      "v_mfma_f32_16x16x32_bf16 v[64:67], a[36:39], a[192:195], v[64:67]\n"
      "buffer_store_dwordx4 a[240:243], v16, s[8:11], s59 offen offset:256 sc1\n"
      "ds_read_b128 a[208:211], v5 offset:50752\n"
      "v_cvt_pk_bf16_f32 v50, v50, v51\n"
      "v_mfma_f32_16x16x32_bf16 v[68:71], a[40:43], a[192:195], v[68:71]\n"
      "ds_write_addtid_b32 v50 offset:39424\n"
      "ds_write_addtid_b32 v83 offset:55040\n"
      "v_mfma_f32_16x16x32_bf16 v[72:75], a[44:47], a[192:195], v[72:75]\n"
      "v_add_f32 v84, v84, v96\n"
      "v_add_f32_e64 v85, v85, v97\n"
      "v_add_f32_e64 v86, v86, v98\n"
      "v_add_f32_e64 v87, v87, v99\n"
      "v_mfma_f32_16x16x32_bf16 v[64:67], a[48:51], a[196:199], v[64:67]\n"
      "s_nop 0\n"
      "ds_read_b128 a[212:215], v5 offset:50816\n"
      "ds_write_addtid_b32 v84 offset:55552\n"
      "v_mfma_f32_16x16x32_bf16 v[68:71], a[52:55], a[196:199], v[68:71]\n"
      "s_nop 0\n"
      "ds_write_addtid_b32 v85 offset:56064\n"
      "ds_write_addtid_b32 v86 offset:56576\n"
      "v_mfma_f32_16x16x32_bf16 v[72:75], a[56:59], a[196:199], v[72:75]\n"
      "s_nop 0\n"
      "ds_write_addtid_b32 v87 offset:57088\n"
      "v_fma_f32 v40, -2.0, v88, v76\n"
      "v_mfma_f32_16x16x32_bf16 v[64:67], a[60:63], a[200:203], v[64:67]\n"
      "s_nop 0\n"
      "v_fma_f32 v41, -2.0, v89, v77\n"
      "v_fma_f32 v42, -2.0, v90, v78\n"
      "v_mfma_f32_16x16x32_bf16 v[68:71], a[64:67], a[200:203], v[68:71]\n"
      "s_waitcnt vmcnt(26)\n"
      "v_fma_f32 v43, -2.0, v91, v79\n"
      "v_fma_f32 v44, -2.0, v92, v80\n"
      "v_mfma_f32_16x16x32_bf16 v[72:75], a[68:71], a[200:203], v[72:75]\n"
      "s_waitcnt lgkmcnt(0)\n"
      "v_fma_f32 v45, -2.0, v93, v81\n"
      "s_barrier\n"
      "v_mfma_f32_16x16x32_bf16 v[64:67], a[72:75], a[204:207], v[64:67]\n"
      "s_add_u32 s58, s40, 0\n"
      "ds_read_b128 a[192:195], v0 offset:0\n"
      "ds_read2st64_b32 v[28:29], v6 offset0:24 offset1:26\n"
      "ds_read2st64_b32 v[30:31], v6 offset0:28 offset1:30\n"
      "v_mfma_f32_16x16x32_bf16 v[68:71], a[76:79], a[204:207], v[68:71]\n"
      "s_mov_b32 s40, s62\n"
      "ds_read_b128 a[196:199], v0 offset:4096\n"
      "ds_read2st64_b32 v[32:33], v6 offset0:32 offset1:34\n"
      "v_mfma_f32_16x16x32_bf16 v[72:75], a[80:83], a[204:207], v[72:75]\n"
      "s_add_u32 s62, s62, 786432\n"
      "ds_read2st64_b32 v[34:35], v6 offset0:36 offset1:38\n"
      "v_fma_f32 v46, -2.0, v94, v82\n"
      "v_mfma_f32_16x16x32_bf16 v[64:67], a[84:87], a[208:211], v[64:67]\n"
      "s_add_u32 s59, s58, 196608\n"
      "ds_read_b128 a[200:203], v0 offset:64\n"
      "v_fma_f32 v47, -2.0, v95, v83\n"
      "v_fma_f32 v48, -2.0, v96, v84\n"
      "v_mfma_f32_16x16x32_bf16 v[68:71], a[88:91], a[208:211], v[68:71]\n"
      "ds_read2st64_b32 a[220:221], v10 offset0:177 offset1:179\n"
      "v_fma_f32 v49, -2.0, v97, v85\n"
      "v_mfma_f32_16x16x32_bf16 v[72:75], a[92:95], a[208:211], v[72:75]\n"
      "ds_read2st64_b32 a[222:223], v13 offset0:177 offset1:179\n"
      "v_fma_f32 v50, -2.0, v98, v86\n"
      "v_fma_f32 v51, -2.0, v99, v87\n"
      "v_mfma_f32_16x16x32_bf16 v[64:67], a[96:99], a[212:215], v[64:67]\n"
      "ds_read_b128 a[204:207], v0 offset:4160\n"
      "v_mfma_f32_16x16x32_bf16 v[68:71], a[100:103], a[212:215], v[68:71]\n"
      "ds_read2st64_b32 v[36:37], v6 offset0:40 offset1:42\n"
      "ds_read2st64_b32 v[38:39], v6 offset0:44 offset1:46\n"
      "v_mfma_f32_16x16x32_bf16 v[72:75], a[104:107], a[212:215], v[72:75]\n"
      "s_waitcnt lgkmcnt(7)\n"
      "v_fmac_f32 v40, s46, v28\n"
      "v_fmac_f32 v41, s46, v29\n"
      "v_fmac_f32 v42, s46, v30\n"
      "v_fmac_f32 v43, s46, v31\n"
      "v_mfma_f32_16x16x32_bf16 v[76:79], v[252:255], a[192:195], 0\n"
      "s_nop 0\n"
      "ds_read_b128 a[208:211], v0 offset:128\n"
      "v_fma_f32 v28, 0.5, v28, v52\n"
      "v_mfma_f32_16x16x32_bf16 v[80:83], v[244:247], a[192:195], 0\n"
      "s_add_u32 s54, s54, 196608\n"
      "ds_read_b64_tr_b16 a[244:245], v7 offset:19968\n"
      "ds_read_b64_tr_b16 a[246:247], v7 offset:19976\n"
      "v_fma_f32 v29, 0.5, v29, v53\n"
      "v_mfma_f32_16x16x32_bf16 v[84:87], v[236:239], a[192:195], 0\n"
      "s_add_u32 m0, s37, 145792\n"
      "v_fma_f32 v30, 0.5, v30, v54\n"
      "v_fma_f32 v31, 0.5, v31, v55\n"
      "v_mfma_f32_16x16x32_bf16 v[88:91], v[248:251], a[196:199], 0\n"
      "buffer_load_dwordx4 v8, s[4:7], s54 offen offset:0 nt lds\n"
      "buffer_load_dwordx4 v9, s[4:7], s54 offen offset:1024 nt lds\n"
      "ds_read_b128 a[212:215], v0 offset:4224\n"
      "v_mfma_f32_16x16x32_bf16 v[92:95], v[240:243], a[196:199], 0\n"
      "s_mov_b32 m0, s64\n"
      "v_fmac_f32 v40, s48, v64\n"
      "v_fmac_f32 v41, s48, v65\n"
      "v_fmac_f32 v42, s48, v66\n"
      "v_mfma_f32_16x16x32_bf16 v[96:99], v[232:235], a[196:199], 0\n"
      "s_waitcnt lgkmcnt(3)\n"
      "v_fmac_f32 v43, s48, v67\n"
      "v_fmac_f32 v28, s45, v40\n"
      "v_mfma_f32_16x16x32_bf16 v[76:79], v[228:231], a[200:203], v[76:79]\n"
      "ds_read_b128 a[192:195], v1 offset:0\n"
      "v_fmac_f32 v29, s45, v41\n"
      "v_mfma_f32_16x16x32_bf16 v[80:83], v[220:223], a[200:203], v[80:83]\n"
      "v_fmac_f32 v30, s45, v42\n"
      "v_fmac_f32_e64 v31, s45, v43\n"
      "v_mfma_f32_16x16x32_bf16 v[84:87], v[212:215], a[200:203], v[84:87]\n"
      "s_waitcnt lgkmcnt(1)\n"
      "ds_read2st64_b32 a[224:225], v11 offset0:177 offset1:179\n"
      "ds_read2st64_b32 a[226:227], v14 offset0:177 offset1:179\n"
      "v_fma_f32 v40, s47, v40, -v64\n"
      "v_mfma_f32_4x4x4_16b_bf16 v[20:23], a[216:217], a[244:245], 0\n"
      "v_fma_f32 v41, s47, v41, -v65\n"
      "v_mfma_f32_4x4x4_16b_bf16 v[24:27], a[216:217], a[246:247], 0\n"
      "v_fma_f32 v42, s47, v42, -v66\n"
      "v_fma_f32 v43, s47, v43, -v67\n"
      "v_mfma_f32_16x16x32_bf16 v[88:91], v[224:227], a[204:207], v[88:91]\n"
      "buffer_store_dwordx4 a[220:223], v16, s[8:11], s58 offen offset:0 sc1\n"
      "ds_read_b128 a[196:199], v1 offset:4096\n"
      "v_mfma_f32_16x16x32_bf16 v[92:95], v[216:219], a[204:207], v[92:95]\n"
      "v_cvt_pk_bf16_f32 v20, v20, v24\n"
      "v_cvt_pk_bf16_f32 v28, v28, v29\n"
      "v_mfma_f32_16x16x32_bf16 v[96:99], v[208:211], a[204:207], v[96:99]\n"
      "ds_write_addtid_b32 v28 offset:33792\n"
      "ds_write_addtid_b32 v20 offset:19456\n"
      "v_mfma_f32_16x16x32_bf16 v[76:79], v[204:207], a[208:211], v[76:79]\n"
      "s_add_u32 s54, s54, 196608\n"
      "ds_read_b128 a[200:203], v1 offset:64\n"
      "v_cvt_pk_bf16_f32 v30, v30, v31\n"
      "v_cvt_pk_bf16_f32 v21, v21, v25\n"
      "v_mfma_f32_16x16x32_bf16 v[80:83], v[196:199], a[208:211], v[80:83]\n"
      "s_add_u32 m0, s37, 82432\n"
      "v_cvt_pk_bf16_f32 v40, v40, v41\n"
      "v_cvt_pk_bf16_f32 v42, v42, v43\n"
      "v_mfma_f32_16x16x32_bf16 v[84:87], v[188:191], a[208:211], v[84:87]\n"
      "s_nop 0\n"
      "buffer_load_dwordx4 v8, s[4:7], s54 offen offset:0 nt lds\n"
      "buffer_load_dwordx4 v9, s[4:7], s54 offen offset:1024 nt lds\n"
      "v_mfma_f32_16x16x32_bf16 v[88:91], v[200:203], a[212:215], v[88:91]\n"
      "s_mov_b32 m0, s64\n"
      "ds_read_b128 a[204:207], v1 offset:4160\n"
      "ds_write_addtid_b32 v30 offset:34304\n"
      "ds_write_addtid_b32 v40 offset:35840\n"
      "v_mfma_f32_16x16x32_bf16 v[92:95], v[192:195], a[212:215], v[92:95]\n"
      "ds_write_addtid_b32 v42 offset:36352\n"
      "ds_write_addtid_b32 v21 offset:23552\n"
      "v_fmac_f32 v44, s46, v32\n"
      "v_cvt_pk_bf16_f32 v22, v22, v26\n"
      "v_mfma_f32_16x16x32_bf16 v[96:99], v[184:187], a[212:215], v[96:99]\n"
      "s_waitcnt lgkmcnt(5)\n"
      "ds_write_addtid_b32 v22 offset:27648\n"
      "v_fmac_f32 v45, s46, v33\n"
      "v_mfma_f32_16x16x32_bf16 v[76:79], v[180:183], a[192:195], v[76:79]\n"
      "ds_read_b128 a[208:211], v1 offset:128\n"
      "v_fmac_f32 v46, s46, v34\n"
      "v_mfma_f32_16x16x32_bf16 v[80:83], v[172:175], a[192:195], v[80:83]\n"
      "ds_read2st64_b32 a[228:229], v12 offset0:177 offset1:179\n"
      "ds_read2st64_b32 a[230:231], v15 offset0:177 offset1:179\n"
      "v_fmac_f32_e64 v47, s46, v35\n"
      "v_cvt_pk_bf16_f32 v23, v23, v27\n"
      "v_mfma_f32_16x16x32_bf16 v[84:87], v[164:167], a[192:195], v[84:87]\n"
      "ds_write_addtid_b32 v23 offset:31744\n"
      "v_fma_f32 v32, 0.5, v32, v56\n"
      "v_mfma_f32_16x16x32_bf16 v[88:91], v[176:179], a[196:199], v[88:91]\n"
      "ds_read_b128 a[212:215], v1 offset:4224\n"
      "v_fma_f32 v33, 0.5, v33, v57\n"
      "v_mfma_f32_16x16x32_bf16 v[92:95], v[168:171], a[196:199], v[92:95]\n"
      "buffer_store_dwordx4 a[224:227], v16, s[8:11], s58 offen offset:128 sc1\n"
      "v_mfma_f32_16x16x32_bf16 v[96:99], v[160:163], a[196:199], v[96:99]\n"
      "v_fma_f32 v34, 0.5, v34, v58\n"
      "v_fma_f32 v35, 0.5, v35, v59\n"
      "v_mfma_f32_16x16x32_bf16 v[76:79], v[156:159], a[200:203], v[76:79]\n"
      "s_waitcnt lgkmcnt(1)\n"
      "ds_read_b128 a[192:195], v2 offset:0\n"
      "v_fmac_f32 v44, s48, v68\n"
      "v_mfma_f32_16x16x32_bf16 v[80:83], v[148:151], a[200:203], v[80:83]\n"
      "s_add_u32 s54, s54, 196608\n"
      "v_fmac_f32 v45, s48, v69\n"
      "v_fmac_f32 v46, s48, v70\n"
      "v_mfma_f32_16x16x32_bf16 v[84:87], v[140:143], a[200:203], v[84:87]\n"
      "s_add_u32 m0, s37, 86536\n"
      "v_fmac_f32 v47, s48, v71\n"
      "v_fmac_f32 v32, s45, v44\n"
      "v_mfma_f32_16x16x32_bf16 v[88:91], v[152:155], a[204:207], v[88:91]\n"
      "buffer_load_dwordx4 v8, s[4:7], s54 offen offset:0 nt lds\n"
      "buffer_load_dwordx4 v9, s[4:7], s54 offen offset:1024 nt lds\n"
      "ds_read_b128 a[196:199], v2 offset:4096\n"
      "v_mfma_f32_16x16x32_bf16 v[92:95], v[144:147], a[204:207], v[92:95]\n"
      "s_mov_b32 m0, s64\n"
      "v_fmac_f32 v33, s45, v45\n"
      "v_fmac_f32 v34, s45, v46\n"
      "v_mfma_f32_16x16x32_bf16 v[96:99], v[136:139], a[204:207], v[96:99]\n"
      "ds_read_b64_tr_b16 a[248:249], v7 offset:20480\n"
      "ds_read_b64_tr_b16 a[250:251], v7 offset:20488\n"
      "v_fmac_f32 v35, s45, v47\n"
      "v_mfma_f32_16x16x32_bf16 v[76:79], v[132:135], a[208:211], v[76:79]\n"
      "ds_read_b128 a[200:203], v2 offset:64\n"
      "v_fma_f32 v44, s47, v44, -v68\n"
      "v_mfma_f32_16x16x32_bf16 v[80:83], v[124:127], a[208:211], v[80:83]\n"
      "s_nop 0\n"
      "v_fma_f32 v45, s47, v45, -v69\n"
      "v_fma_f32 v46, s47, v46, -v70\n"
      "v_mfma_f32_16x16x32_bf16 v[84:87], v[116:119], a[208:211], v[84:87]\n"
      "s_waitcnt lgkmcnt(3)\n"
      "v_fma_f32 v47, s47, v47, -v71\n"
      "v_cvt_pk_bf16_f32 v32, v32, v33\n"
      "v_mfma_f32_16x16x32_bf16 v[88:91], v[128:131], a[212:215], v[88:91]\n"
      "buffer_store_dwordx4 a[228:231], v16, s[8:11], s58 offen offset:256 sc1\n"
      "ds_read_b128 a[204:207], v2 offset:4160\n"
      "v_mfma_f32_16x16x32_bf16 v[92:95], v[120:123], a[212:215], v[92:95]\n"
      "ds_write_addtid_b32 v32 offset:41988\n"
      "ds_read2st64_b32 a[232:233], v10 offset0:0 offset1:2\n"
      "ds_read2st64_b32 a[234:235], v13 offset0:0 offset1:2\n"
      "v_cvt_pk_bf16_f32 v34, v34, v35\n"
      "v_mfma_f32_16x16x32_bf16 v[96:99], v[112:115], a[212:215], v[96:99]\n"
      "s_nop 0\n"
      "ds_write_addtid_b32 v34 offset:42500\n"
      "v_cvt_pk_bf16_f32 v44, v44, v45\n"
      "v_mfma_f32_16x16x32_bf16 v[76:79], v[108:111], a[192:195], v[76:79]\n"
      "s_waitcnt lgkmcnt(5)\n"
      "ds_read_b128 a[208:211], v2 offset:128\n"
      "ds_write_addtid_b32 v44 offset:44036\n"
      "v_cvt_pk_bf16_f32 v46, v46, v47\n"
      "v_mfma_f32_4x4x4_16b_bf16 v[20:23], a[216:217], a[248:249], 0\n"
      "v_fmac_f32 v48, s46, v36\n"
      "v_mfma_f32_4x4x4_16b_bf16 v[24:27], a[216:217], a[250:251], 0\n"
      "ds_write_addtid_b32 v46 offset:44548\n"
      "v_fmac_f32 v49, s46, v37\n"
      "v_fmac_f32 v50, s46, v38\n"
      "v_mfma_f32_16x16x32_bf16 v[80:83], v[100:103], a[192:195], v[80:83]\n"
      "v_fmac_f32 v51, s46, v39\n"
      "v_cvt_pk_bf16_f32 v20, v20, v24\n"
      "v_mfma_f32_16x16x32_bf16 v[84:87], a[160:163], a[192:195], v[84:87]\n"
      "ds_write_addtid_b32 v20 offset:19968\n"
      "v_cvt_pk_bf16_f32 v21, v21, v25\n"
      "v_fma_f32 v36, 0.5, v36, v60\n"
      "v_mfma_f32_16x16x32_bf16 v[88:91], v[104:107], a[196:199], v[88:91]\n"
      "s_add_u32 s54, s54, 196608\n"
      "ds_read_b128 a[212:215], v2 offset:4224\n"
      "ds_write_addtid_b32 v21 offset:24064\n"
      "v_mfma_f32_16x16x32_bf16 v[92:95], a[164:167], a[196:199], v[92:95]\n"
      "s_add_u32 m0, s37, 90760\n"
      "v_fma_f32 v37, 0.5, v37, v61\n"
      "v_cvt_pk_bf16_f32 v22, v22, v26\n"
      "v_mfma_f32_16x16x32_bf16 v[96:99], a[156:159], a[196:199], v[96:99]\n"
      "buffer_load_dwordx4 v8, s[4:7], s54 offen offset:0 nt lds\n"
      "buffer_load_dwordx4 v9, s[4:7], s54 offen offset:1024 nt lds\n"
      "ds_read2st64_b32 a[236:237], v11 offset0:0 offset1:2\n"
      "ds_read2st64_b32 a[238:239], v14 offset0:0 offset1:2\n"
      "v_mfma_f32_16x16x32_bf16 v[76:79], a[152:155], a[200:203], v[76:79]\n"
      "s_mov_b32 m0, s64\n"
      "ds_read_b128 a[192:195], v3 offset:0\n"
      "ds_write_addtid_b32 v22 offset:28160\n"
      "v_mfma_f32_16x16x32_bf16 v[80:83], a[144:147], a[200:203], v[80:83]\n"
      "v_fma_f32 v38, 0.5, v38, v62\n"
      "v_cvt_pk_bf16_f32 v23, v23, v27\n"
      "v_mfma_f32_16x16x32_bf16 v[84:87], a[136:139], a[200:203], v[84:87]\n"
      "s_waitcnt lgkmcnt(4)\n"
      "ds_write_addtid_b32 v23 offset:32256\n"
      "v_fma_f32 v39, 0.5, v39, v63\n"
      "v_mfma_f32_16x16x32_bf16 v[88:91], a[148:151], a[204:207], v[88:91]\n"
      "ds_read_b128 a[196:199], v3 offset:64\n"
      "v_mfma_f32_16x16x32_bf16 v[92:95], a[140:143], a[204:207], v[92:95]\n"
      "v_fmac_f32 v48, s48, v72\n"
      "v_fmac_f32 v49, s48, v73\n"
      "v_fmac_f32 v50, s48, v74\n"
      "v_mfma_f32_16x16x32_bf16 v[96:99], a[132:135], a[204:207], v[96:99]\n"
      "s_nop 0\n"
      "buffer_store_dwordx4 a[232:235], v16, s[8:11], s59 offen offset:0 sc1\n"
      "v_mfma_f32_16x16x32_bf16 v[76:79], a[128:131], a[208:211], v[76:79]\n"
      "ds_read_b128 a[200:203], v3 offset:128\n"
      "v_fmac_f32 v51, s48, v75\n"
      "v_mfma_f32_16x16x32_bf16 v[80:83], a[120:123], a[208:211], v[80:83]\n"
      "ds_read2st64_b32 a[240:241], v12 offset0:0 offset1:2\n"
      "ds_read2st64_b32 a[242:243], v15 offset0:0 offset1:2\n"
      "v_fmac_f32 v36, s45, v48\n"
      "v_fmac_f32_e64 v37, s45, v49\n"
      "v_mfma_f32_16x16x32_bf16 v[84:87], a[112:115], a[208:211], v[84:87]\n"
      "v_fmac_f32 v38, s45, v50\n"
      "v_fmac_f32 v39, s45, v51\n"
      "v_mfma_f32_16x16x32_bf16 v[88:91], a[124:127], a[212:215], v[88:91]\n"
      "ds_read_b128 a[204:207], v4 offset:0\n"
      "v_fma_f32 v48, s47, v48, -v72\n"
      "v_mfma_f32_16x16x32_bf16 v[92:95], a[116:119], a[212:215], v[92:95]\n"
      "v_fma_f32 v49, s47, v49, -v73\n"
      "v_fma_f32 v50, s47, v50, -v74\n"
      "v_mfma_f32_16x16x32_bf16 v[96:99], a[108:111], a[212:215], v[96:99]\n"
      "s_waitcnt lgkmcnt(2)\n"
      "v_fma_f32 v51, s47, v51, -v75\n"
      "v_add_f32 v76, v76, v88\n"
      "v_add_f32 v77, v77, v89\n"
      "v_mfma_f32_16x16x32_bf16 v[52:55], a[0:3], a[192:195], 0\n"
      "s_nop 0\n"
      "ds_read_b128 a[208:211], v4 offset:64\n"
      "ds_write_addtid_b32 v76 offset:45312\n"
      "ds_write_addtid_b32 v77 offset:45824\n"
      "v_mfma_f32_16x16x32_bf16 v[56:59], a[4:7], a[192:195], 0\n"
      "buffer_store_dwordx4 a[236:239], v16, s[8:11], s59 offen offset:128 sc1\n"
      "v_add_f32 v78, v78, v90\n"
      "v_mfma_f32_16x16x32_bf16 v[60:63], a[8:11], a[192:195], 0\n"
      "ds_write_addtid_b32 v78 offset:46336\n"
      "v_cvt_pk_bf16_f32 v36, v36, v37\n"
      "v_add_f32 v79, v79, v91\n"
      "v_mfma_f32_16x16x32_bf16 v[52:55], a[12:15], a[196:199], v[52:55]\n"
      "ds_read_b128 a[212:215], v4 offset:128\n"
      "ds_write_addtid_b32 v36 offset:37888\n"
      "ds_write_addtid_b32 v79 offset:46848\n"
      "v_mfma_f32_16x16x32_bf16 v[56:59], a[16:19], a[196:199], v[56:59]\n"
      "v_add_f32 v80, v80, v92\n"
      "v_cvt_pk_bf16_f32 v38, v38, v39\n"
      "v_mfma_f32_16x16x32_bf16 v[60:63], a[20:23], a[196:199], v[60:63]\n"
      "ds_write_addtid_b32 v38 offset:38400\n"
      "ds_write_addtid_b32 v80 offset:47360\n"
      "v_add_f32 v81, v81, v93\n"
      "v_mfma_f32_16x16x32_bf16 v[52:55], a[24:27], a[200:203], v[52:55]\n"
      "ds_read_b128 a[192:195], v5 offset:0\n"
      "ds_write_addtid_b32 v81 offset:47872\n"
      "v_mfma_f32_16x16x32_bf16 v[56:59], a[28:31], a[200:203], v[56:59]\n"
      "v_add_f32 v82, v82, v94\n"
      "v_cvt_pk_bf16_f32 v48, v48, v49\n"
      "v_add_f32_e64 v83, v83, v95\n"
      "v_mfma_f32_16x16x32_bf16 v[60:63], a[32:35], a[200:203], v[60:63]\n"
      "s_waitcnt lgkmcnt(4)\n"
      "ds_write_addtid_b32 v48 offset:39936\n"
      "ds_write_addtid_b32 v82 offset:48384\n"
      "v_mfma_f32_16x16x32_bf16 v[52:55], a[36:39], a[204:207], v[52:55]\n"
      "buffer_store_dwordx4 a[240:243], v16, s[8:11], s59 offen offset:256 sc1\n"
      "ds_read_b128 a[196:199], v5 offset:64\n"
      "v_cvt_pk_bf16_f32 v50, v50, v51\n"
      "v_mfma_f32_16x16x32_bf16 v[56:59], a[40:43], a[204:207], v[56:59]\n"
      "ds_write_addtid_b32 v50 offset:40448\n"
      "ds_write_addtid_b32 v83 offset:48896\n"
      "v_mfma_f32_16x16x32_bf16 v[60:63], a[44:47], a[204:207], v[60:63]\n"
      "v_add_f32 v84, v84, v96\n"
      "v_add_f32 v85, v85, v97\n"
      "v_add_f32 v86, v86, v98\n"
      "v_add_f32 v87, v87, v99\n"
      "v_mfma_f32_16x16x32_bf16 v[52:55], a[48:51], a[208:211], v[52:55]\n"
      "ds_read_b128 a[200:203], v5 offset:128\n"
      "ds_write_addtid_b32 v84 offset:49408\n"
      "v_mfma_f32_16x16x32_bf16 v[56:59], a[52:55], a[208:211], v[56:59]\n"
      "ds_write_addtid_b32 v85 offset:49920\n"
      "ds_write_addtid_b32 v86 offset:50432\n"
      "v_mfma_f32_16x16x32_bf16 v[60:63], a[56:59], a[208:211], v[60:63]\n"
      "ds_write_addtid_b32 v87 offset:50944\n"
      "v_fma_f32 v40, -2.0, v88, v76\n"
      "v_mfma_f32_16x16x32_bf16 v[52:55], a[60:63], a[212:215], v[52:55]\n"
      "v_fma_f32 v41, -2.0, v89, v77\n"
      "v_fma_f32 v42, -2.0, v90, v78\n"
      "v_mfma_f32_16x16x32_bf16 v[56:59], a[64:67], a[212:215], v[56:59]\n"
      "s_waitcnt vmcnt(26)\n"
      "v_fma_f32 v43, -2.0, v91, v79\n"
      "v_fma_f32 v44, -2.0, v92, v80\n"
      "v_mfma_f32_16x16x32_bf16 v[60:63], a[68:71], a[212:215], v[60:63]\n"
      "s_waitcnt lgkmcnt(0)\n"
      "v_fma_f32 v45, -2.0, v93, v81\n"
      "s_barrier\n"
      "s_add_u32 s55, s55, 2\n"
      "s_cmp_lt_u32 s55, 6\n"
      "s_cbranch_scc1 LDloop_%=\n"
      "LDlast_%=:\n"
      "v_mfma_f32_16x16x32_bf16 v[52:55], a[72:75], a[192:195], v[52:55]\n"
      "s_add_u32 s58, s40, 0\n"
      "ds_read_b128 a[204:207], v0 offset:16384\n"
      "ds_read2st64_b32 v[28:29], v6 offset0:0 offset1:2\n"
      "v_fma_f32 v46, -2.0, v94, v82\n"
      "v_mfma_f32_16x16x32_bf16 v[56:59], a[76:79], a[192:195], v[56:59]\n"
      "s_mov_b32 s40, s62\n"
      "ds_read_b128 a[208:211], v0 offset:20480\n"
      "ds_read2st64_b32 v[30:31], v6 offset0:4 offset1:6\n"
      "v_mfma_f32_16x16x32_bf16 v[60:63], a[80:83], a[192:195], v[60:63]\n"
      "s_add_u32 s62, s62, 786432\n"
      "ds_read2st64_b32 v[32:33], v6 offset0:8 offset1:10\n"
      "ds_read2st64_b32 v[34:35], v6 offset0:12 offset1:14\n"
      "v_mfma_f32_16x16x32_bf16 v[52:55], a[84:87], a[196:199], v[52:55]\n"
      "s_add_u32 s59, s58, 196608\n"
      "ds_read_b128 a[212:215], v0 offset:16448\n"
      "v_fma_f32 v47, -2.0, v95, v83\n"
      "v_mfma_f32_16x16x32_bf16 v[56:59], a[88:91], a[196:199], v[56:59]\n"
      "ds_read2st64_b32 v[36:37], v6 offset0:16 offset1:18\n"
      "v_fma_f32 v48, -2.0, v96, v84\n"
      "v_fma_f32 v49, -2.0, v97, v85\n"
      "v_mfma_f32_16x16x32_bf16 v[60:63], a[92:95], a[196:199], v[60:63]\n"
      "v_fma_f32 v50, -2.0, v98, v86\n"
      "v_fma_f32 v51, -2.0, v99, v87\n"
      "v_mfma_f32_16x16x32_bf16 v[52:55], a[96:99], a[200:203], v[52:55]\n"
      "ds_read_b128 a[192:195], v0 offset:20544\n"
      "v_mfma_f32_16x16x32_bf16 v[56:59], a[100:103], a[200:203], v[56:59]\n"
      "ds_read2st64_b32 v[38:39], v6 offset0:20 offset1:22\n"
      "v_mfma_f32_16x16x32_bf16 v[60:63], a[104:107], a[200:203], v[60:63]\n"
      "s_waitcnt lgkmcnt(6)\n"
      "ds_read_b64_tr_b16 a[244:245], v7 offset:36864\n"
      "ds_read_b64_tr_b16 a[246:247], v7 offset:36872\n"
      "v_fmac_f32 v40, s46, v28\n"
      "v_fmac_f32 v41, s46, v29\n"
      "v_fmac_f32 v42, s46, v30\n"
      "v_mfma_f32_16x16x32_bf16 v[76:79], v[252:255], a[204:207], 0\n"
      "ds_read_b128 a[196:199], v0 offset:16512\n"
      "v_fmac_f32 v43, s46, v31\n"
      "v_mfma_f32_16x16x32_bf16 v[80:83], v[244:247], a[204:207], 0\n"
      "v_fma_f32 v28, 0.5, v28, v64\n"
      "v_fma_f32 v29, 0.5, v29, v65\n"
      "v_mfma_f32_16x16x32_bf16 v[84:87], v[236:239], a[204:207], 0\n"
      "s_nop 0\n"
      "v_fma_f32 v30, 0.5, v30, v66\n"
      "v_fma_f32 v31, 0.5, v31, v67\n"
      "v_mfma_f32_16x16x32_bf16 v[88:91], v[248:251], a[208:211], 0\n"
      "ds_read_b128 a[200:203], v0 offset:20608\n"
      "v_fmac_f32 v40, s48, v52\n"
      "v_fmac_f32 v41, s48, v53\n"
      "v_mfma_f32_16x16x32_bf16 v[92:95], v[240:243], a[208:211], 0\n"
      "v_fmac_f32 v42, s48, v54\n"
      "v_fmac_f32 v43, s48, v55\n"
      "v_mfma_f32_16x16x32_bf16 v[96:99], v[232:235], a[208:211], 0\n"
      "s_waitcnt lgkmcnt(2)\n"
      "v_fmac_f32 v28, s45, v40\n"
      "v_fmac_f32 v29, s45, v41\n"
      "v_mfma_f32_16x16x32_bf16 v[76:79], v[228:231], a[212:215], v[76:79]\n"
      "ds_read_b128 a[204:207], v1 offset:16384\n"
      "v_fmac_f32 v30, s45, v42\n"
      "v_mfma_f32_4x4x4_16b_bf16 v[20:23], a[216:217], a[244:245], 0\n"
      "v_fmac_f32_e64 v31, s45, v43\n"
      "v_mfma_f32_4x4x4_16b_bf16 v[24:27], a[216:217], a[246:247], 0\n"
      "v_fma_f32 v40, s47, v40, -v52\n"
      "v_fma_f32 v41, s47, v41, -v53\n"
      "v_mfma_f32_16x16x32_bf16 v[80:83], v[220:223], a[212:215], v[80:83]\n"
      "v_fma_f32 v42, s47, v42, -v54\n"
      "v_fma_f32 v43, s47, v43, -v55\n"
      "v_cvt_pk_bf16_f32 v20, v20, v24\n"
      "v_mfma_f32_16x16x32_bf16 v[84:87], v[212:215], a[212:215], v[84:87]\n"
      "ds_write_addtid_b32 v20 offset:3072\n"
      "ds_read2st64_b32 a[220:221], v10 offset0:181 offset1:183\n"
      "v_mfma_f32_16x16x32_bf16 v[88:91], v[224:227], a[192:195], v[88:91]\n"
      "ds_read_b128 a[208:211], v1 offset:20480\n"
      "ds_read2st64_b32 a[222:223], v13 offset0:181 offset1:183\n"
      "v_mfma_f32_16x16x32_bf16 v[92:95], v[216:219], a[192:195], v[92:95]\n"
      "s_nop 0\n"
      "v_cvt_pk_bf16_f32 v21, v21, v25\n"
      "v_cvt_pk_bf16_f32 v28, v28, v29\n"
      "v_mfma_f32_16x16x32_bf16 v[96:99], v[208:211], a[192:195], v[96:99]\n"
      "s_waitcnt lgkmcnt(4)\n"
      "ds_write_addtid_b32 v28 offset:32768\n"
      "ds_write_addtid_b32 v21 offset:7168\n"
      "v_mfma_f32_16x16x32_bf16 v[76:79], v[204:207], a[196:199], v[76:79]\n"
      "ds_read_b128 a[212:215], v1 offset:16448\n"
      "v_cvt_pk_bf16_f32 v30, v30, v31\n"
      "v_mfma_f32_16x16x32_bf16 v[80:83], v[196:199], a[196:199], v[80:83]\n"
      "ds_write_addtid_b32 v30 offset:33280\n"
      "v_cvt_pk_bf16_f32 v40, v40, v41\n"
      "v_cvt_pk_bf16_f32 v22, v22, v26\n"
      "v_mfma_f32_16x16x32_bf16 v[84:87], v[188:191], a[196:199], v[84:87]\n"
      "ds_write_addtid_b32 v40 offset:34816\n"
      "ds_write_addtid_b32 v22 offset:11264\n"
      "v_cvt_pk_bf16_f32 v42, v42, v43\n"
      "v_mfma_f32_16x16x32_bf16 v[88:91], v[200:203], a[200:203], v[88:91]\n"
      "ds_read_b128 a[192:195], v1 offset:20544\n"
      "ds_write_addtid_b32 v42 offset:35328\n"
      "v_mfma_f32_16x16x32_bf16 v[92:95], v[192:195], a[200:203], v[92:95]\n"
      "v_fmac_f32 v44, s46, v32\n"
      "v_cvt_pk_bf16_f32 v23, v23, v27\n"
      "v_mfma_f32_16x16x32_bf16 v[96:99], v[184:187], a[200:203], v[96:99]\n"
      "ds_write_addtid_b32 v23 offset:15360\n"
      "v_fmac_f32 v45, s46, v33\n"
      "v_fmac_f32 v46, s46, v34\n"
      "v_mfma_f32_16x16x32_bf16 v[76:79], v[180:183], a[204:207], v[76:79]\n"
      "ds_read_b128 a[196:199], v1 offset:16512\n"
      "v_fmac_f32 v47, s46, v35\n"
      "v_mfma_f32_16x16x32_bf16 v[80:83], v[172:175], a[204:207], v[80:83]\n"
      "ds_read2st64_b32 a[224:225], v11 offset0:181 offset1:183\n"
      "ds_read2st64_b32 a[226:227], v14 offset0:181 offset1:183\n"
      "v_mfma_f32_16x16x32_bf16 v[84:87], v[164:167], a[204:207], v[84:87]\n"
      "s_waitcnt lgkmcnt(5)\n"
      "v_fma_f32 v32, 0.5, v32, v68\n"
      "v_fma_f32 v33, 0.5, v33, v69\n"
      "v_mfma_f32_16x16x32_bf16 v[88:91], v[176:179], a[208:211], v[88:91]\n"
      "s_nop 0\n"
      "ds_read_b128 a[200:203], v1 offset:20608\n"
      "v_fma_f32 v34, 0.5, v34, v70\n"
      "v_mfma_f32_16x16x32_bf16 v[92:95], v[168:171], a[208:211], v[92:95]\n"
      "buffer_store_dwordx4 a[220:223], v16, s[8:11], s58 offen offset:0 sc1\n"
      "v_mfma_f32_16x16x32_bf16 v[96:99], v[160:163], a[208:211], v[96:99]\n"
      "v_fma_f32 v35, 0.5, v35, v71\n"
      "v_fmac_f32 v44, s48, v56\n"
      "v_mfma_f32_16x16x32_bf16 v[76:79], v[156:159], a[212:215], v[76:79]\n"
      "ds_read_b128 a[204:207], v2 offset:16384\n"
      "v_fmac_f32 v45, s48, v57\n"
      "v_mfma_f32_16x16x32_bf16 v[80:83], v[148:151], a[212:215], v[80:83]\n"
      "v_fmac_f32 v46, s48, v58\n"
      "v_fmac_f32 v47, s48, v59\n"
      "v_mfma_f32_16x16x32_bf16 v[84:87], v[140:143], a[212:215], v[84:87]\n"
      "v_fmac_f32 v32, s45, v44\n"
      "v_fmac_f32 v33, s45, v45\n"
      "v_mfma_f32_16x16x32_bf16 v[88:91], v[152:155], a[192:195], v[88:91]\n"
      "ds_read_b128 a[208:211], v2 offset:20480\n"
      "ds_read_b64_tr_b16 a[248:249], v7 offset:37376\n"
      "v_mfma_f32_16x16x32_bf16 v[92:95], v[144:147], a[192:195], v[92:95]\n"
      "ds_read_b64_tr_b16 a[250:251], v7 offset:37384\n"
      "v_fmac_f32 v34, s45, v46\n"
      "v_mfma_f32_16x16x32_bf16 v[96:99], v[136:139], a[192:195], v[96:99]\n"
      "s_waitcnt lgkmcnt(3)\n"
      "v_fmac_f32 v35, s45, v47\n"
      "v_fma_f32 v44, s47, v44, -v56\n"
      "v_mfma_f32_16x16x32_bf16 v[76:79], v[132:135], a[196:199], v[76:79]\n"
      "s_nop 0\n"
      "ds_read_b128 a[212:215], v2 offset:16448\n"
      "v_fma_f32 v45, s47, v45, -v57\n"
      "v_mfma_f32_16x16x32_bf16 v[80:83], v[124:127], a[196:199], v[80:83]\n"
      "s_nop 0\n"
      "v_fma_f32 v46, s47, v46, -v58\n"
      "v_fma_f32 v47, s47, v47, -v59\n"
      "v_mfma_f32_16x16x32_bf16 v[84:87], v[116:119], a[196:199], v[84:87]\n"
      "s_nop 0\n"
      "v_cvt_pk_bf16_f32 v32, v32, v33\n"
      "v_cvt_pk_bf16_f32 v34, v34, v35\n"
      "v_mfma_f32_16x16x32_bf16 v[88:91], v[128:131], a[200:203], v[88:91]\n"
      "s_nop 0\n"
      "buffer_store_dwordx4 a[224:227], v16, s[8:11], s58 offen offset:128 sc1\n"
      "ds_read_b128 a[192:195], v2 offset:20544\n"
      "v_mfma_f32_16x16x32_bf16 v[92:95], v[120:123], a[200:203], v[92:95]\n"
      "s_waitcnt lgkmcnt(1)\n"
      "ds_write_addtid_b32 v32 offset:40964\n"
      "ds_write_addtid_b32 v34 offset:41476\n"
      "v_mfma_f32_4x4x4_16b_bf16 v[20:23], a[216:217], a[248:249], 0\n"
      "ds_read2st64_b32 a[228:229], v12 offset0:181 offset1:183\n"
      "ds_read2st64_b32 a[230:231], v15 offset0:181 offset1:183\n"
      "v_cvt_pk_bf16_f32 v44, v44, v45\n"
      "v_mfma_f32_4x4x4_16b_bf16 v[24:27], a[216:217], a[250:251], 0\n"
      "ds_write_addtid_b32 v44 offset:43012\n"
      "v_cvt_pk_bf16_f32 v46, v46, v47\n"
      "v_mfma_f32_16x16x32_bf16 v[96:99], v[112:115], a[200:203], v[96:99]\n"
      "ds_write_addtid_b32 v46 offset:43524\n"
      "v_fmac_f32_e64 v48, s46, v36\n"
      "v_cvt_pk_bf16_f32 v20, v20, v24\n"
      "v_mfma_f32_16x16x32_bf16 v[76:79], v[108:111], a[204:207], v[76:79]\n"
      "ds_read_b128 a[196:199], v2 offset:16512\n"
      "ds_write_addtid_b32 v20 offset:3584\n"
      "v_fmac_f32 v49, s46, v37\n"
      "v_mfma_f32_16x16x32_bf16 v[80:83], v[100:103], a[204:207], v[80:83]\n"
      "v_fmac_f32 v50, s46, v38\n"
      "v_cvt_pk_bf16_f32 v21, v21, v25\n"
      "v_mfma_f32_16x16x32_bf16 v[84:87], a[160:163], a[204:207], v[84:87]\n"
      "ds_write_addtid_b32 v21 offset:7680\n"
      "v_fmac_f32 v51, s46, v39\n"
      "v_mfma_f32_16x16x32_bf16 v[88:91], v[104:107], a[208:211], v[88:91]\n"
      "ds_read_b128 a[200:203], v2 offset:20608\n"
      "v_cvt_pk_bf16_f32 v22, v22, v26\n"
      "v_mfma_f32_16x16x32_bf16 v[92:95], a[164:167], a[208:211], v[92:95]\n"
      "s_nop 0\n"
      "ds_write_addtid_b32 v22 offset:11776\n"
      "v_fma_f32 v36, 0.5, v36, v72\n"
      "v_mfma_f32_16x16x32_bf16 v[96:99], a[156:159], a[208:211], v[96:99]\n"
      "ds_read2st64_b32 a[232:233], v10 offset0:4 offset1:6\n"
      "ds_read2st64_b32 a[234:235], v13 offset0:4 offset1:6\n"
      "v_fma_f32 v37, 0.5, v37, v73\n"
      "v_mfma_f32_16x16x32_bf16 v[76:79], a[152:155], a[212:215], v[76:79]\n"
      "ds_read_b128 a[204:207], v3 offset:16896\n"
      "v_cvt_pk_bf16_f32 v23, v23, v27\n"
      "v_mfma_f32_16x16x32_bf16 v[80:83], a[144:147], a[212:215], v[80:83]\n"
      "ds_write_addtid_b32 v23 offset:15872\n"
      "v_fma_f32 v38, 0.5, v38, v74\n"
      "v_mfma_f32_16x16x32_bf16 v[84:87], a[136:139], a[212:215], v[84:87]\n"
      "s_waitcnt lgkmcnt(4)\n"
      "v_fma_f32 v39, 0.5, v39, v75\n"
      "v_mfma_f32_16x16x32_bf16 v[88:91], a[148:151], a[192:195], v[88:91]\n"
      "ds_read_b128 a[208:211], v3 offset:16960\n"
      "v_mfma_f32_16x16x32_bf16 v[92:95], a[140:143], a[192:195], v[92:95]\n"
      "v_fmac_f32 v48, s48, v60\n"
      "v_fmac_f32 v49, s48, v61\n"
      "v_fmac_f32 v50, s48, v62\n"
      "v_mfma_f32_16x16x32_bf16 v[96:99], a[132:135], a[192:195], v[96:99]\n"
      "buffer_store_dwordx4 a[228:231], v16, s[8:11], s58 offen offset:256 sc1\n"
      "v_mfma_f32_16x16x32_bf16 v[76:79], a[128:131], a[196:199], v[76:79]\n"
      "ds_read_b128 a[212:215], v3 offset:17024\n"
      "v_fmac_f32 v51, s48, v63\n"
      "v_mfma_f32_16x16x32_bf16 v[80:83], a[120:123], a[196:199], v[80:83]\n"
      "s_nop 0\n"
      "ds_read2st64_b32 a[236:237], v11 offset0:4 offset1:6\n"
      "ds_read2st64_b32 a[238:239], v14 offset0:4 offset1:6\n"
      "v_fmac_f32 v36, s45, v48\n"
      "v_fmac_f32 v37, s45, v49\n"
      "v_mfma_f32_16x16x32_bf16 v[84:87], a[112:115], a[196:199], v[84:87]\n"
      "v_fmac_f32 v38, s45, v50\n"
      "v_fmac_f32 v39, s45, v51\n"
      "v_mfma_f32_16x16x32_bf16 v[88:91], a[124:127], a[200:203], v[88:91]\n"
      "ds_read_b128 a[192:195], v4 offset:16896\n"
      "v_fma_f32 v48, s47, v48, -v60\n"
      "v_mfma_f32_16x16x32_bf16 v[92:95], a[116:119], a[200:203], v[92:95]\n"
      "v_fma_f32 v49, s47, v49, -v61\n"
      "v_fma_f32 v50, s47, v50, -v62\n"
      "v_mfma_f32_16x16x32_bf16 v[96:99], a[108:111], a[200:203], v[96:99]\n"
      "s_waitcnt lgkmcnt(2)\n"
      "v_fma_f32 v51, s47, v51, -v63\n"
      "v_add_f32 v76, v76, v88\n"
      "v_add_f32 v77, v77, v89\n"
      "v_mfma_f32_16x16x32_bf16 v[64:67], a[0:3], a[204:207], 0\n"
      "s_nop 0\n"
      "ds_read_b128 a[196:199], v4 offset:16960\n"
      "ds_write_addtid_b32 v76 offset:51456\n"
      "ds_write_addtid_b32 v77 offset:51968\n"
      "v_mfma_f32_16x16x32_bf16 v[68:71], a[4:7], a[204:207], 0\n"
      "buffer_store_dwordx4 a[232:235], v16, s[8:11], s59 offen offset:0 sc1\n"
      "v_add_f32 v78, v78, v90\n"
      "v_mfma_f32_16x16x32_bf16 v[72:75], a[8:11], a[204:207], 0\n"
      "ds_write_addtid_b32 v78 offset:52480\n"
      "v_cvt_pk_bf16_f32 v36, v36, v37\n"
      "v_add_f32 v79, v79, v91\n"
      "v_mfma_f32_16x16x32_bf16 v[64:67], a[12:15], a[208:211], v[64:67]\n"
      "ds_read_b128 a[200:203], v4 offset:17024\n"
      "ds_write_addtid_b32 v36 offset:36864\n"
      "ds_write_addtid_b32 v79 offset:52992\n"
      "v_mfma_f32_16x16x32_bf16 v[68:71], a[16:19], a[208:211], v[68:71]\n"
      "v_add_f32 v80, v80, v92\n"
      "v_cvt_pk_bf16_f32 v38, v38, v39\n"
      "v_mfma_f32_16x16x32_bf16 v[72:75], a[20:23], a[208:211], v[72:75]\n"
      "s_nop 0\n"
      "ds_write_addtid_b32 v38 offset:37376\n"
      "ds_write_addtid_b32 v80 offset:53504\n"
      "v_add_f32 v81, v81, v93\n"
      "v_mfma_f32_16x16x32_bf16 v[64:67], a[24:27], a[212:215], v[64:67]\n"
      "ds_read_b128 a[204:207], v5 offset:16896\n"
      "ds_write_addtid_b32 v81 offset:54016\n"
      "v_cvt_pk_bf16_f32 v48, v48, v49\n"
      "v_mfma_f32_16x16x32_bf16 v[68:71], a[28:31], a[212:215], v[68:71]\n"
      "s_nop 0\n"
      "ds_write_addtid_b32 v48 offset:38912\n"
      "v_add_f32 v82, v82, v94\n"
      "v_mfma_f32_16x16x32_bf16 v[72:75], a[32:35], a[212:215], v[72:75]\n"
      "s_waitcnt lgkmcnt(5)\n"
      "ds_write_addtid_b32 v82 offset:54528\n"
      "v_add_f32 v83, v83, v95\n"
      "v_cvt_pk_bf16_f32 v50, v50, v51\n"
      "v_mfma_f32_16x16x32_bf16 v[64:67], a[36:39], a[192:195], v[64:67]\n"
      "s_nop 0\n"
      "buffer_store_dwordx4 a[236:239], v16, s[8:11], s59 offen offset:128 sc1\n"
      "ds_read_b128 a[208:211], v5 offset:16960\n"
      "v_mfma_f32_16x16x32_bf16 v[68:71], a[40:43], a[192:195], v[68:71]\n"
      "ds_write_addtid_b32 v50 offset:39424\n"
      "ds_write_addtid_b32 v83 offset:55040\n"
      "v_mfma_f32_16x16x32_bf16 v[72:75], a[44:47], a[192:195], v[72:75]\n"
      "v_add_f32 v84, v84, v96\n"
      "v_add_f32 v85, v85, v97\n"
      "v_add_f32 v86, v86, v98\n"
      "v_add_f32 v87, v87, v99\n"
      "v_mfma_f32_16x16x32_bf16 v[64:67], a[48:51], a[196:199], v[64:67]\n"
      "ds_read_b128 a[212:215], v5 offset:17024\n"
      "ds_write_addtid_b32 v84 offset:55552\n"
      "v_mfma_f32_16x16x32_bf16 v[68:71], a[52:55], a[196:199], v[68:71]\n"
      "ds_write_addtid_b32 v85 offset:56064\n"
      "ds_write_addtid_b32 v86 offset:56576\n"
      "v_mfma_f32_16x16x32_bf16 v[72:75], a[56:59], a[196:199], v[72:75]\n"
      "ds_write_addtid_b32 v87 offset:57088\n"
      "v_fma_f32 v40, -2.0, v88, v76\n"
      "v_mfma_f32_16x16x32_bf16 v[64:67], a[60:63], a[200:203], v[64:67]\n"
      "s_nop 0\n"
      "v_fma_f32 v41, -2.0, v89, v77\n"
      "v_fma_f32 v42, -2.0, v90, v78\n"
      "v_mfma_f32_16x16x32_bf16 v[68:71], a[64:67], a[200:203], v[68:71]\n"
      "s_waitcnt vmcnt(17)\n"
      "v_fma_f32 v43, -2.0, v91, v79\n"
      "v_fma_f32 v44, -2.0, v92, v80\n"
      "v_mfma_f32_16x16x32_bf16 v[72:75], a[68:71], a[200:203], v[72:75]\n"
      "s_waitcnt lgkmcnt(0)\n"
      "v_fma_f32 v45, -2.0, v93, v81\n"
      "s_barrier\n"
      "v_mfma_f32_16x16x32_bf16 v[64:67], a[72:75], a[204:207], v[64:67]\n"
      "s_add_u32 s58, s40, 0\n"
      "ds_read_b128 a[192:195], v0 offset:0\n"
      "ds_read2st64_b32 v[28:29], v6 offset0:24 offset1:26\n"
      "v_fma_f32 v46, -2.0, v94, v82\n"
      "v_mfma_f32_16x16x32_bf16 v[68:71], a[76:79], a[204:207], v[68:71]\n"
      "s_mov_b32 s40, s62\n"
      "ds_read_b128 a[196:199], v0 offset:4096\n"
      "ds_read2st64_b32 v[30:31], v6 offset0:28 offset1:30\n"
      "v_mfma_f32_16x16x32_bf16 v[72:75], a[80:83], a[204:207], v[72:75]\n"
      "s_add_u32 s62, s62, 786432\n"
      "ds_read2st64_b32 v[32:33], v6 offset0:32 offset1:34\n"
      "ds_read2st64_b32 v[34:35], v6 offset0:36 offset1:38\n"
      "v_mfma_f32_16x16x32_bf16 v[64:67], a[84:87], a[208:211], v[64:67]\n"
      "s_add_u32 s59, s58, 196608\n"
      "ds_read_b128 a[200:203], v0 offset:64\n"
      "v_fma_f32 v47, -2.0, v95, v83\n"
      "v_mfma_f32_16x16x32_bf16 v[68:71], a[88:91], a[208:211], v[68:71]\n"
      "ds_read2st64_b32 v[36:37], v6 offset0:40 offset1:42\n"
      "v_fma_f32 v48, -2.0, v96, v84\n"
      "v_fma_f32 v49, -2.0, v97, v85\n"
      "v_mfma_f32_16x16x32_bf16 v[72:75], a[92:95], a[208:211], v[72:75]\n"
      "v_fma_f32 v50, -2.0, v98, v86\n"
      "v_fma_f32 v51, -2.0, v99, v87\n"
      "v_mfma_f32_16x16x32_bf16 v[64:67], a[96:99], a[212:215], v[64:67]\n"
      "ds_read_b128 a[204:207], v0 offset:4160\n"
      "v_mfma_f32_16x16x32_bf16 v[68:71], a[100:103], a[212:215], v[68:71]\n"
      "s_nop 0\n"
      "ds_read2st64_b32 v[38:39], v6 offset0:44 offset1:46\n"
      "v_mfma_f32_16x16x32_bf16 v[72:75], a[104:107], a[212:215], v[72:75]\n"
      "s_waitcnt lgkmcnt(6)\n"
      "ds_read_b64_tr_b16 a[244:245], v7 offset:53760\n"
      "ds_read_b64_tr_b16 a[246:247], v7 offset:53768\n"
      "v_fmac_f32 v40, s46, v28\n"
      "v_fmac_f32 v41, s46, v29\n"
      "v_fmac_f32 v42, s46, v30\n"
      "v_mfma_f32_16x16x32_bf16 v[76:79], v[252:255], a[192:195], 0\n"
      "ds_read_b128 a[208:211], v0 offset:128\n"
      "v_fmac_f32 v43, s46, v31\n"
      "v_mfma_f32_16x16x32_bf16 v[80:83], v[244:247], a[192:195], 0\n"
      "v_fma_f32 v28, 0.5, v28, v52\n"
      "v_fma_f32 v29, 0.5, v29, v53\n"
      "v_mfma_f32_16x16x32_bf16 v[84:87], v[236:239], a[192:195], 0\n"
      "v_fma_f32 v30, 0.5, v30, v54\n"
      "v_fma_f32 v31, 0.5, v31, v55\n"
      "v_mfma_f32_16x16x32_bf16 v[88:91], v[248:251], a[196:199], 0\n"
      "ds_read_b128 a[212:215], v0 offset:4224\n"
      "v_fmac_f32 v40, s48, v64\n"
      "v_fmac_f32 v41, s48, v65\n"
      "v_mfma_f32_16x16x32_bf16 v[92:95], v[240:243], a[196:199], 0\n"
      "v_fmac_f32 v42, s48, v66\n"
      "v_fmac_f32 v43, s48, v67\n"
      "v_mfma_f32_16x16x32_bf16 v[96:99], v[232:235], a[196:199], 0\n"
      "s_waitcnt lgkmcnt(2)\n"
      "v_fmac_f32 v28, s45, v40\n"
      "v_fmac_f32 v29, s45, v41\n"
      "v_mfma_f32_16x16x32_bf16 v[76:79], v[228:231], a[200:203], v[76:79]\n"
      "ds_read_b128 a[192:195], v1 offset:0\n"
      "v_fmac_f32 v30, s45, v42\n"
      "v_mfma_f32_4x4x4_16b_bf16 v[20:23], a[216:217], a[244:245], 0\n"
      "v_fmac_f32_e64 v31, s45, v43\n"
      "v_mfma_f32_4x4x4_16b_bf16 v[24:27], a[216:217], a[246:247], 0\n"
      "v_fma_f32 v40, s47, v40, -v64\n"
      "v_fma_f32 v41, s47, v41, -v65\n"
      "v_mfma_f32_16x16x32_bf16 v[80:83], v[220:223], a[200:203], v[80:83]\n"
      "v_fma_f32 v42, s47, v42, -v66\n"
      "v_fma_f32 v43, s47, v43, -v67\n"
      "v_cvt_pk_bf16_f32 v20, v20, v24\n"
      "v_mfma_f32_16x16x32_bf16 v[84:87], v[212:215], a[200:203], v[84:87]\n"
      "ds_write_addtid_b32 v20 offset:19456\n"
      "ds_read2st64_b32 a[220:221], v10 offset0:177 offset1:179\n"
      "v_mfma_f32_16x16x32_bf16 v[88:91], v[224:227], a[204:207], v[88:91]\n"
      "ds_read_b128 a[196:199], v1 offset:4096\n"
      "ds_read2st64_b32 a[222:223], v13 offset0:177 offset1:179\n"
      "v_mfma_f32_16x16x32_bf16 v[92:95], v[216:219], a[204:207], v[92:95]\n"
      "v_cvt_pk_bf16_f32 v21, v21, v25\n"
      "v_cvt_pk_bf16_f32 v28, v28, v29\n"
      "v_mfma_f32_16x16x32_bf16 v[96:99], v[208:211], a[204:207], v[96:99]\n"
      "s_waitcnt lgkmcnt(4)\n"
      "ds_write_addtid_b32 v28 offset:33792\n"
      "ds_write_addtid_b32 v21 offset:23552\n"
      "v_mfma_f32_16x16x32_bf16 v[76:79], v[204:207], a[208:211], v[76:79]\n"
      "ds_read_b128 a[200:203], v1 offset:64\n"
      "v_cvt_pk_bf16_f32 v30, v30, v31\n"
      "v_mfma_f32_16x16x32_bf16 v[80:83], v[196:199], a[208:211], v[80:83]\n"
      "s_nop 0\n"
      "ds_write_addtid_b32 v30 offset:34304\n"
      "v_cvt_pk_bf16_f32 v40, v40, v41\n"
      "v_cvt_pk_bf16_f32 v22, v22, v26\n"
      "v_mfma_f32_16x16x32_bf16 v[84:87], v[188:191], a[208:211], v[84:87]\n"
      "ds_write_addtid_b32 v40 offset:35840\n"
      "ds_write_addtid_b32 v22 offset:27648\n"
      "v_cvt_pk_bf16_f32 v42, v42, v43\n"
      "v_mfma_f32_16x16x32_bf16 v[88:91], v[200:203], a[212:215], v[88:91]\n"
      "ds_read_b128 a[204:207], v1 offset:4160\n"
      "ds_write_addtid_b32 v42 offset:36352\n"
      "v_mfma_f32_16x16x32_bf16 v[92:95], v[192:195], a[212:215], v[92:95]\n"
      "v_fmac_f32 v44, s46, v32\n"
      "v_cvt_pk_bf16_f32 v23, v23, v27\n"
      "v_mfma_f32_16x16x32_bf16 v[96:99], v[184:187], a[212:215], v[96:99]\n"
      "ds_write_addtid_b32 v23 offset:31744\n"
      "v_fmac_f32 v45, s46, v33\n"
      "v_fmac_f32 v46, s46, v34\n"
      "v_mfma_f32_16x16x32_bf16 v[76:79], v[180:183], a[192:195], v[76:79]\n"
      "s_nop 0\n"
      "ds_read_b128 a[208:211], v1 offset:128\n"
      "v_fmac_f32 v47, s46, v35\n"
      "v_mfma_f32_16x16x32_bf16 v[80:83], v[172:175], a[192:195], v[80:83]\n"
      "ds_read2st64_b32 a[224:225], v11 offset0:177 offset1:179\n"
      "ds_read2st64_b32 a[226:227], v14 offset0:177 offset1:179\n"
      "v_mfma_f32_16x16x32_bf16 v[84:87], v[164:167], a[192:195], v[84:87]\n"
      "s_waitcnt lgkmcnt(5)\n"
      "v_fma_f32 v32, 0.5, v32, v56\n"
      "v_fma_f32 v33, 0.5, v33, v57\n"
      "v_mfma_f32_16x16x32_bf16 v[88:91], v[176:179], a[196:199], v[88:91]\n"
      "ds_read_b128 a[212:215], v1 offset:4224\n"
      "v_fma_f32 v34, 0.5, v34, v58\n"
      "v_mfma_f32_16x16x32_bf16 v[92:95], v[168:171], a[196:199], v[92:95]\n"
      "buffer_store_dwordx4 a[220:223], v16, s[8:11], s58 offen offset:0 sc1\n"
      "v_mfma_f32_16x16x32_bf16 v[96:99], v[160:163], a[196:199], v[96:99]\n"
      "v_fma_f32 v35, 0.5, v35, v59\n"
      "v_fmac_f32_e64 v44, s48, v68\n"
      "v_mfma_f32_16x16x32_bf16 v[76:79], v[156:159], a[200:203], v[76:79]\n"
      "ds_read_b128 a[192:195], v2 offset:0\n"
      "v_fmac_f32 v45, s48, v69\n"
      "v_mfma_f32_16x16x32_bf16 v[80:83], v[148:151], a[200:203], v[80:83]\n"
      "v_fmac_f32 v46, s48, v70\n"
      "v_fmac_f32 v47, s48, v71\n"
      "v_mfma_f32_16x16x32_bf16 v[84:87], v[140:143], a[200:203], v[84:87]\n"
      "v_fmac_f32 v32, s45, v44\n"
      "v_fmac_f32 v33, s45, v45\n"
      "v_mfma_f32_16x16x32_bf16 v[88:91], v[152:155], a[204:207], v[88:91]\n"
      "s_nop 0\n"
      "ds_read_b128 a[196:199], v2 offset:4096\n"
      "ds_read_b64_tr_b16 a[248:249], v7 offset:54272\n"
      "v_mfma_f32_16x16x32_bf16 v[92:95], v[144:147], a[204:207], v[92:95]\n"
      "ds_read_b64_tr_b16 a[250:251], v7 offset:54280\n"
      "v_fmac_f32 v34, s45, v46\n"
      "v_mfma_f32_16x16x32_bf16 v[96:99], v[136:139], a[204:207], v[96:99]\n"
      "s_waitcnt lgkmcnt(3)\n"
      "v_fmac_f32_e64 v35, s45, v47\n"
      "v_fma_f32 v44, s47, v44, -v68\n"
      "v_mfma_f32_16x16x32_bf16 v[76:79], v[132:135], a[208:211], v[76:79]\n"
      "ds_read_b128 a[200:203], v2 offset:64\n"
      "v_fma_f32 v45, s47, v45, -v69\n"
      "v_mfma_f32_16x16x32_bf16 v[80:83], v[124:127], a[208:211], v[80:83]\n"
      "v_fma_f32 v46, s47, v46, -v70\n"
      "v_fma_f32 v47, s47, v47, -v71\n"
      "v_mfma_f32_16x16x32_bf16 v[84:87], v[116:119], a[208:211], v[84:87]\n"
      "v_cvt_pk_bf16_f32 v32, v32, v33\n"
      "v_cvt_pk_bf16_f32 v34, v34, v35\n"
      "v_mfma_f32_16x16x32_bf16 v[88:91], v[128:131], a[212:215], v[88:91]\n"
      "s_nop 0\n"
      "buffer_store_dwordx4 a[224:227], v16, s[8:11], s58 offen offset:128 sc1\n"
      "ds_read_b128 a[204:207], v2 offset:4160\n"
      "v_mfma_f32_16x16x32_bf16 v[92:95], v[120:123], a[212:215], v[92:95]\n"
      "s_waitcnt lgkmcnt(1)\n"
      "ds_write_addtid_b32 v32 offset:41988\n"
      "ds_write_addtid_b32 v34 offset:42500\n"
      "v_mfma_f32_4x4x4_16b_bf16 v[20:23], a[216:217], a[248:249], 0\n"
      "ds_read2st64_b32 a[228:229], v12 offset0:177 offset1:179\n"
      "ds_read2st64_b32 a[230:231], v15 offset0:177 offset1:179\n"
      "v_cvt_pk_bf16_f32 v44, v44, v45\n"
      "v_mfma_f32_4x4x4_16b_bf16 v[24:27], a[216:217], a[250:251], 0\n"
      "ds_write_addtid_b32 v44 offset:44036\n"
      "v_cvt_pk_bf16_f32 v46, v46, v47\n"
      "v_mfma_f32_16x16x32_bf16 v[96:99], v[112:115], a[212:215], v[96:99]\n"
      "ds_write_addtid_b32 v46 offset:44548\n"
      "v_fmac_f32 v48, s46, v36\n"
      "v_cvt_pk_bf16_f32 v20, v20, v24\n"
      "v_mfma_f32_16x16x32_bf16 v[76:79], v[108:111], a[192:195], v[76:79]\n"
      "ds_read_b128 a[208:211], v2 offset:128\n"
      "ds_write_addtid_b32 v20 offset:19968\n"
      "v_fmac_f32 v49, s46, v37\n"
      "v_mfma_f32_16x16x32_bf16 v[80:83], v[100:103], a[192:195], v[80:83]\n"
      "v_fmac_f32_e64 v50, s46, v38\n"
      "v_cvt_pk_bf16_f32 v21, v21, v25\n"
      "v_mfma_f32_16x16x32_bf16 v[84:87], a[160:163], a[192:195], v[84:87]\n"
      "ds_write_addtid_b32 v21 offset:24064\n"
      "v_fmac_f32 v51, s46, v39\n"
      "v_mfma_f32_16x16x32_bf16 v[88:91], v[104:107], a[196:199], v[88:91]\n"
      "ds_read_b128 a[212:215], v2 offset:4224\n"
      "v_cvt_pk_bf16_f32 v22, v22, v26\n"
      "v_mfma_f32_16x16x32_bf16 v[92:95], a[164:167], a[196:199], v[92:95]\n"
      "s_nop 0\n"
      "ds_write_addtid_b32 v22 offset:28160\n"
      "v_fma_f32 v36, 0.5, v36, v60\n"
      "v_mfma_f32_16x16x32_bf16 v[96:99], a[156:159], a[196:199], v[96:99]\n"
      "ds_read2st64_b32 a[232:233], v10 offset0:0 offset1:2\n"
      "ds_read2st64_b32 a[234:235], v13 offset0:0 offset1:2\n"
      "v_fma_f32 v37, 0.5, v37, v61\n"
      "v_mfma_f32_16x16x32_bf16 v[76:79], a[152:155], a[200:203], v[76:79]\n"
      "ds_read_b128 a[192:195], v3 offset:33792\n"
      "v_cvt_pk_bf16_f32 v23, v23, v27\n"
      "v_mfma_f32_16x16x32_bf16 v[80:83], a[144:147], a[200:203], v[80:83]\n"
      "ds_write_addtid_b32 v23 offset:32256\n"
      "v_fma_f32 v38, 0.5, v38, v62\n"
      "v_mfma_f32_16x16x32_bf16 v[84:87], a[136:139], a[200:203], v[84:87]\n"
      "s_waitcnt lgkmcnt(4)\n"
      "v_fma_f32 v39, 0.5, v39, v63\n"
      "v_mfma_f32_16x16x32_bf16 v[88:91], a[148:151], a[204:207], v[88:91]\n"
      "s_nop 0\n"
      "ds_read_b128 a[196:199], v3 offset:33856\n"
      "v_mfma_f32_16x16x32_bf16 v[92:95], a[140:143], a[204:207], v[92:95]\n"
      "v_fmac_f32 v48, s48, v72\n"
      "v_fmac_f32 v49, s48, v73\n"
      "v_fmac_f32 v50, s48, v74\n"
      "v_mfma_f32_16x16x32_bf16 v[96:99], a[132:135], a[204:207], v[96:99]\n"
      "buffer_store_dwordx4 a[228:231], v16, s[8:11], s58 offen offset:256 sc1\n"
      "v_mfma_f32_16x16x32_bf16 v[76:79], a[128:131], a[208:211], v[76:79]\n"
      "ds_read_b128 a[200:203], v3 offset:33920\n"
      "v_fmac_f32 v51, s48, v75\n"
      "v_mfma_f32_16x16x32_bf16 v[80:83], a[120:123], a[208:211], v[80:83]\n"
      "ds_read2st64_b32 a[236:237], v11 offset0:0 offset1:2\n"
      "ds_read2st64_b32 a[238:239], v14 offset0:0 offset1:2\n"
      "v_fmac_f32 v36, s45, v48\n"
      "v_fmac_f32 v37, s45, v49\n"
      "v_mfma_f32_16x16x32_bf16 v[84:87], a[112:115], a[208:211], v[84:87]\n"
      "v_fmac_f32 v38, s45, v50\n"
      "v_fmac_f32 v39, s45, v51\n"
      "v_mfma_f32_16x16x32_bf16 v[88:91], a[124:127], a[212:215], v[88:91]\n"
      "ds_read_b128 a[204:207], v4 offset:33792\n"
      "v_fma_f32 v48, s47, v48, -v72\n"
      "v_mfma_f32_16x16x32_bf16 v[92:95], a[116:119], a[212:215], v[92:95]\n"
      "v_fma_f32 v49, s47, v49, -v73\n"
      "v_fma_f32 v50, s47, v50, -v74\n"
      "v_mfma_f32_16x16x32_bf16 v[96:99], a[108:111], a[212:215], v[96:99]\n"
      "s_waitcnt lgkmcnt(2)\n"
      "v_fma_f32 v51, s47, v51, -v75\n"
      "v_add_f32 v76, v76, v88\n"
      "v_add_f32_e64 v77, v77, v89\n"
      "v_mfma_f32_16x16x32_bf16 v[52:55], a[0:3], a[192:195], 0\n"
      "ds_read_b128 a[208:211], v4 offset:33856\n"
      "ds_write_addtid_b32 v76 offset:45312\n"
      "ds_write_addtid_b32 v77 offset:45824\n"
      "v_mfma_f32_16x16x32_bf16 v[56:59], a[4:7], a[192:195], 0\n"
      "buffer_store_dwordx4 a[232:235], v16, s[8:11], s59 offen offset:0 sc1\n"
      "v_add_f32 v78, v78, v90\n"
      "v_mfma_f32_16x16x32_bf16 v[60:63], a[8:11], a[192:195], 0\n"
      "s_nop 0\n"
      "ds_write_addtid_b32 v78 offset:46336\n"
      "v_cvt_pk_bf16_f32 v36, v36, v37\n"
      "v_add_f32 v79, v79, v91\n"
      "v_mfma_f32_16x16x32_bf16 v[52:55], a[12:15], a[196:199], v[52:55]\n"
      "ds_read_b128 a[212:215], v4 offset:33920\n"
      "ds_write_addtid_b32 v36 offset:37888\n"
      "ds_write_addtid_b32 v79 offset:46848\n"
      "v_mfma_f32_16x16x32_bf16 v[56:59], a[16:19], a[196:199], v[56:59]\n"
      "v_add_f32 v80, v80, v92\n"
      "v_cvt_pk_bf16_f32 v38, v38, v39\n"
      "v_mfma_f32_16x16x32_bf16 v[60:63], a[20:23], a[196:199], v[60:63]\n"
      "ds_write_addtid_b32 v38 offset:38400\n"
      "ds_write_addtid_b32 v80 offset:47360\n"
      "v_add_f32 v81, v81, v93\n"
      "v_mfma_f32_16x16x32_bf16 v[52:55], a[24:27], a[200:203], v[52:55]\n"
      "s_nop 0\n"
      "ds_read_b128 a[192:195], v5 offset:33792\n"
      "ds_write_addtid_b32 v81 offset:47872\n"
      "v_cvt_pk_bf16_f32 v48, v48, v49\n"
      "v_mfma_f32_16x16x32_bf16 v[56:59], a[28:31], a[200:203], v[56:59]\n"
      "s_nop 0\n"
      "ds_write_addtid_b32 v48 offset:39936\n"
      "v_add_f32_e64 v82, v82, v94\n"
      "v_mfma_f32_16x16x32_bf16 v[60:63], a[32:35], a[200:203], v[60:63]\n"
      "s_waitcnt lgkmcnt(5)\n"
      "ds_write_addtid_b32 v82 offset:48384\n"
      "v_add_f32_e64 v83, v83, v95\n"
      "v_cvt_pk_bf16_f32 v50, v50, v51\n"
      "v_mfma_f32_16x16x32_bf16 v[52:55], a[36:39], a[204:207], v[52:55]\n"
      "s_nop 0\n"
      "buffer_store_dwordx4 a[236:239], v16, s[8:11], s59 offen offset:128 sc1\n"
      "ds_read_b128 a[196:199], v5 offset:33856\n"
      "v_mfma_f32_16x16x32_bf16 v[56:59], a[40:43], a[204:207], v[56:59]\n"
      "s_nop 0\n"
      "ds_write_addtid_b32 v50 offset:40448\n"
      "ds_write_addtid_b32 v83 offset:48896\n"
      "v_mfma_f32_16x16x32_bf16 v[60:63], a[44:47], a[204:207], v[60:63]\n"
      "s_nop 0\n"
      "v_add_f32_e64 v84, v84, v96\n"
      "v_add_f32_e64 v85, v85, v97\n"
      "v_add_f32_e64 v86, v86, v98\n"
      "v_add_f32_e64 v87, v87, v99\n"
      "v_mfma_f32_16x16x32_bf16 v[52:55], a[48:51], a[208:211], v[52:55]\n"
      "s_nop 0\n"
      "ds_read_b128 a[200:203], v5 offset:33920\n"
      "ds_write_addtid_b32 v84 offset:49408\n"
      "v_mfma_f32_16x16x32_bf16 v[56:59], a[52:55], a[208:211], v[56:59]\n"
      "s_nop 0\n"
      "ds_write_addtid_b32 v85 offset:49920\n"
      "ds_write_addtid_b32 v86 offset:50432\n"
      "v_mfma_f32_16x16x32_bf16 v[60:63], a[56:59], a[208:211], v[60:63]\n"
      "s_nop 0\n"
      "ds_write_addtid_b32 v87 offset:50944\n"
      "v_fma_f32 v40, -2.0, v88, v76\n"
      "v_mfma_f32_16x16x32_bf16 v[52:55], a[60:63], a[212:215], v[52:55]\n"
      "s_nop 0\n"
      "v_fma_f32 v41, -2.0, v89, v77\n"
      "v_fma_f32 v42, -2.0, v90, v78\n"
      "v_mfma_f32_16x16x32_bf16 v[56:59], a[64:67], a[212:215], v[56:59]\n"
      "s_waitcnt vmcnt(13)\n"
      "v_fma_f32 v43, -2.0, v91, v79\n"
      "v_fma_f32 v44, -2.0, v92, v80\n"
      "v_mfma_f32_16x16x32_bf16 v[60:63], a[68:71], a[212:215], v[60:63]\n"
      "s_waitcnt lgkmcnt(0)\n"
      "v_fma_f32 v45, -2.0, v93, v81\n"
      "s_barrier\n"
      "v_mfma_f32_16x16x32_bf16 v[52:55], a[72:75], a[192:195], v[52:55]\n"
      "s_add_u32 s58, s40, 0\n"
      "ds_read_b128 a[204:207], v0 offset:16384\n"
      "ds_read2st64_b32 v[28:29], v6 offset0:0 offset1:2\n"
      "v_mfma_f32_16x16x32_bf16 v[56:59], a[76:79], a[192:195], v[56:59]\n"
      "s_mov_b32 s40, s62\n"
      "ds_read_b128 a[208:211], v0 offset:20480\n"
      "ds_read2st64_b32 v[30:31], v6 offset0:4 offset1:6\n"
      "v_mfma_f32_16x16x32_bf16 v[60:63], a[80:83], a[192:195], v[60:63]\n"
      "s_add_u32 s62, s62, 786432\n"
      "ds_read2st64_b32 v[32:33], v6 offset0:8 offset1:10\n"
      "v_fma_f32 v46, -2.0, v94, v82\n"
      "v_mfma_f32_16x16x32_bf16 v[52:55], a[84:87], a[196:199], v[52:55]\n"
      "ds_read_b128 a[212:215], v0 offset:16448\n"
      "v_fma_f32 v47, -2.0, v95, v83\n"
      "v_mfma_f32_16x16x32_bf16 v[56:59], a[88:91], a[196:199], v[56:59]\n"
      "ds_read2st64_b32 v[34:35], v6 offset0:12 offset1:14\n"
      "v_fma_f32 v48, -2.0, v96, v84\n"
      "v_mfma_f32_16x16x32_bf16 v[60:63], a[92:95], a[196:199], v[60:63]\n"
      "ds_read2st64_b32 v[36:37], v6 offset0:16 offset1:18\n"
      "v_fma_f32 v49, -2.0, v97, v85\n"
      "v_mfma_f32_16x16x32_bf16 v[52:55], a[96:99], a[200:203], v[52:55]\n"
      "ds_read_b128 a[192:195], v0 offset:20544\n"
      "v_fma_f32 v50, -2.0, v98, v86\n"
      "v_mfma_f32_16x16x32_bf16 v[56:59], a[100:103], a[200:203], v[56:59]\n"
      "ds_read2st64_b32 a[220:221], v10 offset0:181 offset1:183\n"
      "ds_read2st64_b32 a[222:223], v13 offset0:181 offset1:183\n"
      "v_fma_f32 v51, -2.0, v99, v87\n"
      "v_mfma_f32_16x16x32_bf16 v[60:63], a[104:107], a[200:203], v[60:63]\n"
      "s_waitcnt lgkmcnt(7)\n"
      "ds_read2st64_b32 v[38:39], v6 offset0:20 offset1:22\n"
      "v_fmac_f32 v40, s46, v28\n"
      "v_mfma_f32_16x16x32_bf16 v[76:79], v[252:255], a[204:207], 0\n"
      "ds_read_b128 a[196:199], v0 offset:16512\n"
      "v_fmac_f32 v41, s46, v29\n"
      "v_mfma_f32_16x16x32_bf16 v[80:83], v[244:247], a[204:207], 0\n"
      "v_fmac_f32 v42, s46, v30\n"
      "v_fmac_f32 v43, s46, v31\n"
      "v_mfma_f32_16x16x32_bf16 v[84:87], v[236:239], a[204:207], 0\n"
      "s_nop 0\n"
      "v_fma_f32 v28, 0.5, v28, v64\n"
      "v_fma_f32 v29, 0.5, v29, v65\n"
      "v_mfma_f32_16x16x32_bf16 v[88:91], v[248:251], a[208:211], 0\n"
      "ds_read_b128 a[200:203], v0 offset:20608\n"
      "v_fma_f32 v30, 0.5, v30, v66\n"
      "v_mfma_f32_16x16x32_bf16 v[92:95], v[240:243], a[208:211], 0\n"
      "v_fma_f32 v31, 0.5, v31, v67\n"
      "v_fmac_f32 v40, s48, v52\n"
      "v_mfma_f32_16x16x32_bf16 v[96:99], v[232:235], a[208:211], 0\n"
      "s_waitcnt lgkmcnt(3)\n"
      "v_fmac_f32 v41, s48, v53\n"
      "v_fmac_f32 v42, s48, v54\n"
      "v_mfma_f32_16x16x32_bf16 v[76:79], v[228:231], a[212:215], v[76:79]\n"
      "ds_read_b128 a[204:207], v1 offset:16384\n"
      "v_fmac_f32 v43, s48, v55\n"
      "v_mfma_f32_16x16x32_bf16 v[80:83], v[220:223], a[212:215], v[80:83]\n"
      "v_fmac_f32 v28, s45, v40\n"
      "v_fmac_f32 v29, s45, v41\n"
      "v_mfma_f32_16x16x32_bf16 v[84:87], v[212:215], a[212:215], v[84:87]\n"
      "v_fmac_f32 v30, s45, v42\n"
      "v_fmac_f32 v31, s45, v43\n"
      "v_mfma_f32_16x16x32_bf16 v[88:91], v[224:227], a[192:195], v[88:91]\n"
      "s_nop 0\n"
      "ds_read_b128 a[208:211], v1 offset:20480\n"
      "v_fma_f32 v40, s47, v40, -v52\n"
      "v_mfma_f32_16x16x32_bf16 v[92:95], v[216:219], a[192:195], v[92:95]\n"
      "s_nop 0\n"
      "v_fma_f32 v41, s47, v41, -v53\n"
      "v_fma_f32 v42, s47, v42, -v54\n"
      "v_mfma_f32_16x16x32_bf16 v[96:99], v[208:211], a[192:195], v[96:99]\n"
      "s_waitcnt lgkmcnt(1)\n"
      "buffer_store_dwordx4 a[220:223], v16, s[8:11], s58 offen offset:0 sc1\n"
      "v_mfma_f32_16x16x32_bf16 v[76:79], v[204:207], a[196:199], v[76:79]\n"
      "ds_read_b128 a[212:215], v1 offset:16448\n"
      "v_fma_f32 v43, s47, v43, -v55\n"
      "v_mfma_f32_16x16x32_bf16 v[80:83], v[196:199], a[196:199], v[80:83]\n"
      "ds_read2st64_b32 a[224:225], v11 offset0:181 offset1:183\n"
      "ds_read2st64_b32 a[226:227], v14 offset0:181 offset1:183\n"
      "v_mfma_f32_16x16x32_bf16 v[84:87], v[188:191], a[196:199], v[84:87]\n"
      "v_cvt_pk_bf16_f32 v28, v28, v29\n"
      "v_cvt_pk_bf16_f32 v30, v30, v31\n"
      "v_mfma_f32_16x16x32_bf16 v[88:91], v[200:203], a[200:203], v[88:91]\n"
      "ds_read_b128 a[192:195], v1 offset:20544\n"
      "ds_write_addtid_b32 v28 offset:32768\n"
      "v_mfma_f32_16x16x32_bf16 v[92:95], v[192:195], a[200:203], v[92:95]\n"
      "ds_write_addtid_b32 v30 offset:33280\n"
      "v_cvt_pk_bf16_f32 v40, v40, v41\n"
      "v_mfma_f32_16x16x32_bf16 v[96:99], v[184:187], a[200:203], v[96:99]\n"
      "ds_write_addtid_b32 v40 offset:34816\n"
      "v_cvt_pk_bf16_f32 v42, v42, v43\n"
      "v_mfma_f32_16x16x32_bf16 v[76:79], v[180:183], a[204:207], v[76:79]\n"
      "ds_read_b128 a[196:199], v1 offset:16512\n"
      "ds_write_addtid_b32 v42 offset:35328\n"
      "v_mfma_f32_16x16x32_bf16 v[80:83], v[172:175], a[204:207], v[80:83]\n"
      "v_fmac_f32 v44, s46, v32\n"
      "v_fmac_f32 v45, s46, v33\n"
      "v_mfma_f32_16x16x32_bf16 v[84:87], v[164:167], a[204:207], v[84:87]\n"
      "s_waitcnt lgkmcnt(5)\n"
      "v_fmac_f32 v46, s46, v34\n"
      "v_fmac_f32 v47, s46, v35\n"
      "v_mfma_f32_16x16x32_bf16 v[88:91], v[176:179], a[208:211], v[88:91]\n"
      "ds_read_b128 a[200:203], v1 offset:20608\n"
      "v_fma_f32 v32, 0.5, v32, v68\n"
      "v_mfma_f32_16x16x32_bf16 v[92:95], v[168:171], a[208:211], v[92:95]\n"
      "s_nop 0\n"
      "v_fma_f32 v33, 0.5, v33, v69\n"
      "v_fma_f32 v34, 0.5, v34, v70\n"
      "v_mfma_f32_16x16x32_bf16 v[96:99], v[160:163], a[208:211], v[96:99]\n"
      "v_fma_f32 v35, 0.5, v35, v71\n"
      "v_fmac_f32 v44, s48, v56\n"
      "v_mfma_f32_16x16x32_bf16 v[76:79], v[156:159], a[212:215], v[76:79]\n"
      "ds_read_b128 a[204:207], v2 offset:16384\n"
      "v_mfma_f32_16x16x32_bf16 v[80:83], v[148:151], a[212:215], v[80:83]\n"
      "v_fmac_f32 v45, s48, v57\n"
      "v_fmac_f32 v46, s48, v58\n"
      "v_mfma_f32_16x16x32_bf16 v[84:87], v[140:143], a[212:215], v[84:87]\n"
      "v_fmac_f32 v47, s48, v59\n"
      "v_mfma_f32_16x16x32_bf16 v[88:91], v[152:155], a[192:195], v[88:91]\n"
      "buffer_store_dwordx4 a[224:227], v16, s[8:11], s58 offen offset:128 sc1\n"
      "ds_read_b128 a[208:211], v2 offset:20480\n"
      "v_mfma_f32_16x16x32_bf16 v[92:95], v[144:147], a[192:195], v[92:95]\n"
      "v_fmac_f32 v32, s45, v44\n"
      "v_fmac_f32_e64 v33, s45, v45\n"
      "v_mfma_f32_16x16x32_bf16 v[96:99], v[136:139], a[192:195], v[96:99]\n"
      "s_waitcnt lgkmcnt(1)\n"
      "ds_read2st64_b32 a[228:229], v12 offset0:181 offset1:183\n"
      "ds_read2st64_b32 a[230:231], v15 offset0:181 offset1:183\n"
      "v_mfma_f32_16x16x32_bf16 v[76:79], v[132:135], a[196:199], v[76:79]\n"
      "ds_read_b128 a[212:215], v2 offset:16448\n"
      "v_fmac_f32 v34, s45, v46\n"
      "v_mfma_f32_16x16x32_bf16 v[80:83], v[124:127], a[196:199], v[80:83]\n"
      "v_fmac_f32 v35, s45, v47\n"
      "v_fma_f32 v44, s47, v44, -v56\n"
      "v_mfma_f32_16x16x32_bf16 v[84:87], v[116:119], a[196:199], v[84:87]\n"
      "v_fma_f32 v45, s47, v45, -v57\n"
      "v_mfma_f32_16x16x32_bf16 v[88:91], v[128:131], a[200:203], v[88:91]\n"
      "ds_read_b128 a[192:195], v2 offset:20544\n"
      "v_fma_f32 v46, s47, v46, -v58\n"
      "v_mfma_f32_16x16x32_bf16 v[92:95], v[120:123], a[200:203], v[92:95]\n"
      "v_fma_f32 v47, s47, v47, -v59\n"
      "v_mfma_f32_16x16x32_bf16 v[96:99], v[112:115], a[200:203], v[96:99]\n"
      "v_cvt_pk_bf16_f32 v32, v32, v33\n"
      "v_cvt_pk_bf16_f32 v34, v34, v35\n"
      "v_mfma_f32_16x16x32_bf16 v[76:79], v[108:111], a[204:207], v[76:79]\n"
      "ds_read_b128 a[196:199], v2 offset:16512\n"
      "ds_write_addtid_b32 v32 offset:40964\n"
      "v_mfma_f32_16x16x32_bf16 v[80:83], v[100:103], a[204:207], v[80:83]\n"
      "s_nop 0\n"
      "ds_write_addtid_b32 v34 offset:41476\n"
      "v_cvt_pk_bf16_f32 v44, v44, v45\n"
      "v_mfma_f32_16x16x32_bf16 v[84:87], a[160:163], a[204:207], v[84:87]\n"
      "s_waitcnt lgkmcnt(3)\n"
      "ds_write_addtid_b32 v44 offset:43012\n"
      "v_cvt_pk_bf16_f32 v46, v46, v47\n"
      "v_mfma_f32_16x16x32_bf16 v[88:91], v[104:107], a[208:211], v[88:91]\n"
      "ds_read_b128 a[200:203], v2 offset:20608\n"
      "ds_write_addtid_b32 v46 offset:43524\n"
      "v_mfma_f32_16x16x32_bf16 v[92:95], a[164:167], a[208:211], v[92:95]\n"
      "v_fmac_f32 v48, s46, v36\n"
      "v_fmac_f32 v49, s46, v37\n"
      "v_mfma_f32_16x16x32_bf16 v[96:99], a[156:159], a[208:211], v[96:99]\n"
      "v_fmac_f32 v50, s46, v38\n"
      "v_fmac_f32 v51, s46, v39\n"
      "v_mfma_f32_16x16x32_bf16 v[76:79], a[152:155], a[212:215], v[76:79]\n"
      "ds_read_b128 a[204:207], v3 offset:50688\n"
      "v_fma_f32 v36, 0.5, v36, v72\n"
      "v_mfma_f32_16x16x32_bf16 v[80:83], a[144:147], a[212:215], v[80:83]\n"
      "buffer_store_dwordx4 a[228:231], v16, s[8:11], s58 offen offset:256 sc1\n"
      "v_mfma_f32_16x16x32_bf16 v[84:87], a[136:139], a[212:215], v[84:87]\n"
      "v_fma_f32 v37, 0.5, v37, v73\n"
      "v_fma_f32 v38, 0.5, v38, v74\n"
      "v_mfma_f32_16x16x32_bf16 v[88:91], a[148:151], a[192:195], v[88:91]\n"
      "ds_read_b128 a[208:211], v3 offset:50752\n"
      "v_fma_f32 v39, 0.5, v39, v75\n"
      "v_mfma_f32_16x16x32_bf16 v[92:95], a[140:143], a[192:195], v[92:95]\n"
      "v_fmac_f32 v48, s48, v60\n"
      "v_fmac_f32 v49, s48, v61\n"
      "v_fmac_f32 v50, s48, v62\n"
      "v_fmac_f32 v51, s48, v63\n"
      "v_fmac_f32_e64 v36, s45, v48\n"
      "v_mfma_f32_16x16x32_bf16 v[96:99], a[132:135], a[192:195], v[96:99]\n"
      "s_waitcnt lgkmcnt(1)\n"
      "v_fmac_f32 v37, s45, v49\n"
      "v_fmac_f32 v38, s45, v50\n"
      "v_fmac_f32 v39, s45, v51\n"
      "v_fma_f32 v48, s47, v48, -v60\n"
      "v_fma_f32 v49, s47, v49, -v61\n"
      "v_fma_f32 v50, s47, v50, -v62\n"
      "v_mfma_f32_16x16x32_bf16 v[76:79], a[128:131], a[196:199], v[76:79]\n"
      "ds_read_b128 a[212:215], v3 offset:50816\n"
      "v_fma_f32 v51, s47, v51, -v63\n"
      "v_cvt_pk_bf16_f32 v36, v36, v37\n"
      "v_cvt_pk_bf16_f32 v38, v38, v39\n"
      "v_cvt_pk_bf16_f32 v48, v48, v49\n"
      "v_cvt_pk_bf16_f32 v50, v50, v51\n"
      "v_mfma_f32_16x16x32_bf16 v[80:83], a[120:123], a[196:199], v[80:83]\n"
      "ds_write_addtid_b32 v36 offset:36864\n"
      "ds_write_addtid_b32 v38 offset:37376\n"
      "v_mfma_f32_16x16x32_bf16 v[84:87], a[112:115], a[196:199], v[84:87]\n"
      "ds_write_addtid_b32 v48 offset:38912\n"
      "ds_write_addtid_b32 v50 offset:39424\n"
      "v_mfma_f32_16x16x32_bf16 v[88:91], a[124:127], a[200:203], v[88:91]\n"
      "ds_read_b128 a[192:195], v4 offset:50688\n"
      "v_mfma_f32_16x16x32_bf16 v[92:95], a[116:119], a[200:203], v[92:95]\n"
      "v_mfma_f32_16x16x32_bf16 v[96:99], a[108:111], a[200:203], v[96:99]\n"
      "v_mfma_f32_16x16x32_bf16 v[64:67], a[0:3], a[204:207], 0\n"
      "ds_read_b128 a[196:199], v4 offset:50752\n"
      "v_mfma_f32_16x16x32_bf16 v[68:71], a[4:7], a[204:207], 0\n"
      "v_mfma_f32_16x16x32_bf16 v[72:75], a[8:11], a[204:207], 0\n"
      "s_waitcnt lgkmcnt(1)\n"
      "v_add_f32 v76, v76, v88\n"
      "v_add_f32 v77, v77, v89\n"
      "v_add_f32 v78, v78, v90\n"
      "v_add_f32 v79, v79, v91\n"
      "v_add_f32 v80, v80, v92\n"
      "v_add_f32 v81, v81, v93\n"
      "v_add_f32 v82, v82, v94\n"
      "v_mfma_f32_16x16x32_bf16 v[64:67], a[12:15], a[208:211], v[64:67]\n"
      "ds_read_b128 a[200:203], v4 offset:50816\n"
      "ds_write_addtid_b32 v76 offset:51456\n"
      "ds_write_addtid_b32 v77 offset:51968\n"
      "v_add_f32 v83, v83, v95\n"
      "v_add_f32 v84, v84, v96\n"
      "v_add_f32 v85, v85, v97\n"
      "v_add_f32 v86, v86, v98\n"
      "v_mfma_f32_16x16x32_bf16 v[68:71], a[16:19], a[208:211], v[68:71]\n"
      "ds_write_addtid_b32 v78 offset:52480\n"
      "ds_write_addtid_b32 v79 offset:52992\n"
      "ds_write_addtid_b32 v80 offset:53504\n"
      "v_mfma_f32_16x16x32_bf16 v[72:75], a[20:23], a[208:211], v[72:75]\n"
      "ds_write_addtid_b32 v81 offset:54016\n"
      "ds_write_addtid_b32 v82 offset:54528\n"
      "ds_write_addtid_b32 v83 offset:55040\n"
      "v_add_f32 v87, v87, v99\n"
      "v_mfma_f32_16x16x32_bf16 v[64:67], a[24:27], a[212:215], v[64:67]\n"
      "s_nop 0\n"
      "ds_write_addtid_b32 v84 offset:55552\n"
      "ds_write_addtid_b32 v85 offset:56064\n"
      "v_mfma_f32_16x16x32_bf16 v[68:71], a[28:31], a[212:215], v[68:71]\n"
      "ds_write_addtid_b32 v86 offset:56576\n"
      "ds_write_addtid_b32 v87 offset:57088\n"
      "v_mfma_f32_16x16x32_bf16 v[72:75], a[32:35], a[212:215], v[72:75]\n"
      "s_waitcnt lgkmcnt(0)\n"
      "s_barrier\n"
      "v_mfma_f32_16x16x32_bf16 v[64:67], a[36:39], a[192:195], v[64:67]\n"
      "s_add_u32 s58, s58, 786432\n"
      "ds_read_b128 a[204:207], v5 offset:50688\n"
      "ds_read2st64_b32 v[28:29], v6 offset0:24 offset1:26\n"
      "v_mfma_f32_16x16x32_bf16 v[68:71], a[40:43], a[192:195], v[68:71]\n"
      "ds_read2st64_b32 v[30:31], v6 offset0:28 offset1:30\n"
      "ds_read2st64_b32 v[32:33], v6 offset0:32 offset1:34\n"
      "v_mfma_f32_16x16x32_bf16 v[72:75], a[44:47], a[192:195], v[72:75]\n"
      "ds_read2st64_b32 v[34:35], v6 offset0:36 offset1:38\n"
      "ds_read2st64_b32 v[36:37], v6 offset0:40 offset1:42\n"
      "v_mfma_f32_16x16x32_bf16 v[64:67], a[48:51], a[196:199], v[64:67]\n"
      "ds_read_b128 a[208:211], v5 offset:50752\n"
      "ds_read2st64_b32 v[38:39], v6 offset0:44 offset1:46\n"
      "v_mfma_f32_16x16x32_bf16 v[68:71], a[52:55], a[196:199], v[68:71]\n"
      "v_fma_f32 v40, -2.0, v88, v76\n"
      "v_fma_f32 v41, -2.0, v89, v77\n"
      "v_mfma_f32_16x16x32_bf16 v[72:75], a[56:59], a[196:199], v[72:75]\n"
      "ds_read2st64_b32 a[220:221], v10 offset0:177 offset1:179\n"
      "ds_read2st64_b32 a[222:223], v13 offset0:177 offset1:179\n"
      "v_mfma_f32_16x16x32_bf16 v[64:67], a[60:63], a[200:203], v[64:67]\n"
      "ds_read_b128 a[212:215], v5 offset:50816\n"
      "v_fma_f32 v42, -2.0, v90, v78\n"
      "v_mfma_f32_16x16x32_bf16 v[68:71], a[64:67], a[200:203], v[68:71]\n"
      "ds_read2st64_b32 a[224:225], v11 offset0:177 offset1:179\n"
      "ds_read2st64_b32 a[226:227], v14 offset0:177 offset1:179\n"
      "v_mfma_f32_16x16x32_bf16 v[72:75], a[68:71], a[200:203], v[72:75]\n"
      "s_waitcnt lgkmcnt(9)\n"
      "v_fma_f32 v43, -2.0, v91, v79\n"
      "v_fma_f32 v44, -2.0, v92, v80\n"
      "v_mfma_f32_16x16x32_bf16 v[64:67], a[72:75], a[204:207], v[64:67]\n"
      "s_waitcnt lgkmcnt(3)\n"
      "buffer_store_dwordx4 a[220:223], v16, s[8:11], s58 offen offset:0 sc1\n"
      "v_mfma_f32_16x16x32_bf16 v[68:71], a[76:79], a[204:207], v[68:71]\n"
      "v_fma_f32 v45, -2.0, v93, v81\n"
      "v_fma_f32 v46, -2.0, v94, v82\n"
      "v_mfma_f32_16x16x32_bf16 v[72:75], a[80:83], a[204:207], v[72:75]\n"
      "ds_read2st64_b32 a[228:229], v12 offset0:177 offset1:179\n"
      "ds_read2st64_b32 a[230:231], v15 offset0:177 offset1:179\n"
      "v_mfma_f32_16x16x32_bf16 v[64:67], a[84:87], a[208:211], v[64:67]\n"
      "s_waitcnt lgkmcnt(2)\n"
      "buffer_store_dwordx4 a[224:227], v16, s[8:11], s58 offen offset:128 sc1\n"
      "v_mfma_f32_16x16x32_bf16 v[68:71], a[88:91], a[208:211], v[68:71]\n"
      "s_nop 0\n"
      "v_fma_f32 v47, -2.0, v95, v83\n"
      "v_fma_f32 v48, -2.0, v96, v84\n"
      "v_mfma_f32_16x16x32_bf16 v[72:75], a[92:95], a[208:211], v[72:75]\n"
      "v_fma_f32 v49, -2.0, v97, v85\n"
      "v_fma_f32 v50, -2.0, v98, v86\n"
      "v_mfma_f32_16x16x32_bf16 v[64:67], a[96:99], a[212:215], v[64:67]\n"
      "s_waitcnt lgkmcnt(0)\n"
      "buffer_store_dwordx4 a[228:231], v16, s[8:11], s58 offen offset:256 sc1\n"
      "v_mfma_f32_16x16x32_bf16 v[68:71], a[100:103], a[212:215], v[68:71]\n"
      "s_nop 0\n"
      "v_fma_f32 v51, -2.0, v99, v87\n"
      "v_fmac_f32 v40, s46, v28\n"
      "v_mfma_f32_16x16x32_bf16 v[72:75], a[104:107], a[212:215], v[72:75]\n"
      "v_fmac_f32 v41, s46, v29\n"
      "v_fmac_f32 v42, s46, v30\n"
      "v_fmac_f32 v43, s46, v31\n"
      "v_fma_f32 v28, 0.5, v28, v52\n"
      "v_fma_f32 v29, 0.5, v29, v53\n"
      "v_fma_f32 v30, 0.5, v30, v54\n"
      "v_fma_f32 v31, 0.5, v31, v55\n"
      "v_fmac_f32 v44, s46, v32\n"
      "v_fmac_f32 v45, s46, v33\n"
      "v_pk_fma_f32 v[46:47], v[34:35], s[46:47], v[46:47] op_sel:[0,0,0] op_sel_hi:[1,0,1]\n"
      "v_pk_fma_f32 v[32:33], v[32:33], s[48:49], v[56:57] op_sel:[0,1,0] op_sel_hi:[1,1,1]\n"
      "v_pk_fma_f32 v[34:35], v[34:35], s[48:49], v[58:59] op_sel:[0,1,0] op_sel_hi:[1,1,1]\n"
      "v_pk_fma_f32 v[48:49], v[36:37], s[46:47], v[48:49] op_sel:[0,0,0] op_sel_hi:[1,0,1]\n"
      "v_pk_fma_f32 v[50:51], v[38:39], s[46:47], v[50:51] op_sel:[0,0,0] op_sel_hi:[1,0,1]\n"
      "v_pk_fma_f32 v[36:37], v[36:37], s[48:49], v[60:61] op_sel:[0,1,0] op_sel_hi:[1,1,1]\n"
      "v_pk_fma_f32 v[38:39], v[38:39], s[48:49], v[62:63] op_sel:[0,1,0] op_sel_hi:[1,1,1]\n"
      "v_pk_fma_f32 v[40:41], v[64:65], s[48:49], v[40:41] op_sel:[0,0,0] op_sel_hi:[1,0,1]\n"
      "v_pk_fma_f32 v[42:43], v[66:67], s[48:49], v[42:43] op_sel:[0,0,0] op_sel_hi:[1,0,1]\n"
      "v_pk_fma_f32 v[28:29], v[40:41], s[44:45], v[28:29] op_sel:[0,1,0] op_sel_hi:[1,1,1]\n"
      "v_pk_fma_f32 v[30:31], v[42:43], s[44:45], v[30:31] op_sel:[0,1,0] op_sel_hi:[1,1,1]\n"
      "v_pk_fma_f32 v[40:41], v[40:41], s[46:47], v[64:65] op_sel:[0,1,0] op_sel_hi:[1,1,1] neg_lo:[0,0,1] neg_hi:[0,0,1]\n"
      "v_pk_fma_f32 v[42:43], v[42:43], s[46:47], v[66:67] op_sel:[0,1,0] op_sel_hi:[1,1,1] neg_lo:[0,0,1] neg_hi:[0,0,1]\n"
      "v_cvt_pk_bf16_f32 v28, v28, v29\n"
      "v_cvt_pk_bf16_f32 v30, v30, v31\n"
      "v_cvt_pk_bf16_f32 v40, v40, v41\n"
      "v_cvt_pk_bf16_f32 v42, v42, v43\n"
      "v_pk_fma_f32 v[44:45], v[68:69], s[48:49], v[44:45] op_sel:[0,0,0] op_sel_hi:[1,0,1]\n"
      "v_pk_fma_f32 v[46:47], v[70:71], s[48:49], v[46:47] op_sel:[0,0,0] op_sel_hi:[1,0,1]\n"
      "v_pk_fma_f32 v[32:33], v[44:45], s[44:45], v[32:33] op_sel:[0,1,0] op_sel_hi:[1,1,1]\n"
      "v_pk_fma_f32 v[34:35], v[46:47], s[44:45], v[34:35] op_sel:[0,1,0] op_sel_hi:[1,1,1]\n"
      "v_pk_fma_f32 v[44:45], v[44:45], s[46:47], v[68:69] op_sel:[0,1,0] op_sel_hi:[1,1,1] neg_lo:[0,0,1] neg_hi:[0,0,1]\n"
      "v_pk_fma_f32 v[46:47], v[46:47], s[46:47], v[70:71] op_sel:[0,1,0] op_sel_hi:[1,1,1] neg_lo:[0,0,1] neg_hi:[0,0,1]\n"
      "v_cvt_pk_bf16_f32 v32, v32, v33\n"
      "v_cvt_pk_bf16_f32 v34, v34, v35\n"
      "ds_write_addtid_b32 v28 offset:33792\n"
      "ds_write_addtid_b32 v30 offset:34304\n"
      "ds_write_addtid_b32 v40 offset:35840\n"
      "ds_write_addtid_b32 v42 offset:36352\n"
      "ds_write_addtid_b32 v32 offset:41988\n"
      "ds_write_addtid_b32 v34 offset:42500\n"
      "v_cvt_pk_bf16_f32 v44, v44, v45\n"
      "v_cvt_pk_bf16_f32 v46, v46, v47\n"
      "v_pk_fma_f32 v[48:49], v[72:73], s[48:49], v[48:49] op_sel:[0,0,0] op_sel_hi:[1,0,1]\n"
      "v_pk_fma_f32 v[50:51], v[74:75], s[48:49], v[50:51] op_sel:[0,0,0] op_sel_hi:[1,0,1]\n"
      "v_pk_fma_f32 v[36:37], v[48:49], s[44:45], v[36:37] op_sel:[0,1,0] op_sel_hi:[1,1,1]\n"
      "v_pk_fma_f32 v[38:39], v[50:51], s[44:45], v[38:39] op_sel:[0,1,0] op_sel_hi:[1,1,1]\n"
      "v_pk_fma_f32 v[48:49], v[48:49], s[46:47], v[72:73] op_sel:[0,1,0] op_sel_hi:[1,1,1] neg_lo:[0,0,1] neg_hi:[0,0,1]\n"
      "v_pk_fma_f32 v[50:51], v[50:51], s[46:47], v[74:75] op_sel:[0,1,0] op_sel_hi:[1,1,1] neg_lo:[0,0,1] neg_hi:[0,0,1]\n"
      "v_cvt_pk_bf16_f32 v36, v36, v37\n"
      "v_cvt_pk_bf16_f32 v38, v38, v39\n"
      "v_cvt_pk_bf16_f32 v48, v48, v49\n"
      "v_cvt_pk_bf16_f32 v50, v50, v51\n"
      "ds_write_addtid_b32 v44 offset:44036\n"
      "ds_write_addtid_b32 v46 offset:44548\n"
      "ds_write_addtid_b32 v36 offset:37888\n"
      "ds_write_addtid_b32 v38 offset:38400\n"
      "ds_write_addtid_b32 v48 offset:39936\n"
      "ds_write_addtid_b32 v50 offset:40448\n"
      "s_waitcnt lgkmcnt(0)\n"
      "s_barrier\n"
      "ds_read2st64_b32 a[220:221], v10 offset0:181 offset1:183\n"
      "ds_read2st64_b32 a[222:223], v13 offset0:181 offset1:183\n"
      "ds_read2st64_b32 a[224:225], v11 offset0:181 offset1:183\n"
      "ds_read2st64_b32 a[226:227], v14 offset0:181 offset1:183\n"
      "ds_read2st64_b32 a[228:229], v12 offset0:181 offset1:183\n"
      "ds_read2st64_b32 a[230:231], v15 offset0:181 offset1:183\n"
      "s_add_u32 s58, s58, 786432\n"
      "s_waitcnt lgkmcnt(4)\n"
      "buffer_store_dwordx4 a[220:223], v16, s[8:11], s58 offen offset:0 sc1\n"
      "s_waitcnt lgkmcnt(2)\n"
      "buffer_store_dwordx4 a[224:227], v16, s[8:11], s58 offen offset:128 sc1\n"
      "s_waitcnt lgkmcnt(0)\n"
      "buffer_store_dwordx4 a[228:231], v16, s[8:11], s58 offen offset:256 sc1\n"
      "LDend_%=:\n"
      "s_branch Lfin_%=\n"
      ".p2align 6\n"
      "LS_%=:\n"
      "v_lshlrev_b32 v43, 4, v37\n"
      "v_lshlrev_b32 v44, 2, v37\n"
      "v_and_b32 v43, 16, v43\n"
      "v_and_or_b32 v43, v44, 8, v43\n"
      "v_mul_u32_u24 v44, 192, v38\n"
      "v_mul_u32_u24 v45, 1536, v39\n"
      "s_lshl_b32 s27, s18, 5\n"
      "v_lshlrev_b32 v46, 1, v36\n"
      "v_add3_u32 v43, v43, v44, v45\n"
      "v_bitop3_b32 v46, v46, s27, 32 bitop3:0x6c\n"
      "v_add_u32 v60, v43, v46\n"
      "v_add_u32 v61, 57344, v60\n"
      "v_add_u32 v62, 114688, v60\n"
      "s_lshl_b32 s26, s18, 3\n"
      "v_and_b32 v42, 15, v36\n"
      "v_xor_b32 v42, s26, v42\n"
      "s_lshl_b32 s26, s17, 13\n"
      "s_add_u32 s26, s26, 24832\n"
      "s_mul_i32 s27, s17, 12672\n"
      "v_mul_u32_u24 v44, 192, v42\n"
      "v_lshlrev_b32 v43, 3, v42\n"
      "v_and_b32 v45, 48, v36\n"
      "s_add_u32 s27, s27, 82432\n"
      "v_bitop3_b32 v46, v43, v45, 32 bitop3:0x6c\n"
      "v_add3_u32 v0, v46, v44, s26\n"
      "v_add3_u32 v3, v46, v44, s27\n"
      "v_add_u32 v43, 16, v43\n"
      "v_add_u32 v44, 384, v44\n"
      "v_bitop3_b32 v46, v43, v45, 32 bitop3:0x6c\n"
      "v_add3_u32 v1, v46, v44, s26\n"
      "v_add3_u32 v4, v46, v44, s27\n"
      "v_add_u32 v43, 16, v43\n"
      "v_add_u32 v44, 384, v44\n"
      "v_bitop3_b32 v46, v43, v45, 32 bitop3:0x6c\n"
      "v_add3_u32 v2, v46, v44, s26\n"
      "v_add3_u32 v5, v46, v44, s27\n"
      "v_lshlrev_b32 v43, 31, v36\n"
      "v_and_b32 v44, 2, v36\n"
      "v_or_b32 v43, 0x3f803f80, v43\n"
      "v_mul_u32_u24 v45, 0xc00080, v44\n"
      "v_lshl_add_u32 v44, v44, 22, v43\n"
      "v_add_u32 v45, v45, v43\n"
      "v_accvgpr_write_b32 a216, v44\n"
      "v_accvgpr_write_b32 a217, v45\n"
      "v_lshlrev_b32 v43, 3, v38\n"
      "s_mov_b32 s26, 806420736\n"
      "v_bfe_u32 v43, s26, v43, 8\n"
      "v_lshlrev_b32 v44, 12, v38\n"
      "s_lshl_b32 s28, s18, 8\n"
      "s_add_u32 s28, s28, 82432\n"
      "v_lshl_add_u32 v43, v43, 3, v44\n"
      "v_lshl_add_u32 v44, v39, 2, v37\n"
      "v_lshl_add_u32 v43, v44, 4, v43\n"
      "v_add_u32 v7, s28, v43\n"
      "s_mul_i32 s26, s17, 70144\n"
      "s_lshl_b32 s27, s18, 8\n"
      "s_sub_u32 s26, 70144, s26\n"
      "s_add_u32 s26, s26, s27\n"
      "v_lshl_add_u32 v6, v36, 2, s26\n"
      "v_lshrrev_b32 v43, 3, v36\n"
      "v_and_b32 v44, 7, v36\n"
      "v_mul_u32_u24 v45, 384, v43\n"
      "v_lshl_add_u32 v11, v44, 4, v45\n"
      "v_and_b32 v44, 2, v36\n"
      "v_lshlrev_b32 v43, 3, v43\n"
      "v_lshlrev_b32 v45, 4, v44\n"
      "v_xor_b32 v43, v43, v45\n"
      "v_lshl_add_u32 v43, v44, 7, v43\n"
      "v_and_b32 v44, 1, v36\n"
      "s_lshl_b32 s26, s18, 11\n"
      "v_lshl_add_u32 v43, v44, 6, v43\n"
      "s_add_u32 s26, s26, 12288\n"
      "v_bfe_u32 v44, v36, 2, 1\n"
      "v_add_u32 v43, s26, v43\n"
      "v_mul_i32_i24 v45, -4100, v44\n"
      "s_mov_b32 s27, 8200\n"
      "v_add3_u32 v8, v43, v45, s27\n"
      "v_add_u32 v9, 128, v8\n"
      "s_mul_i32 s62, s18, 393216\n"
      "s_add_u32 s62, s62, s19\n"
      "s_add_u32 s62, s62, s21\n"
      "s_mov_b32 s40, 0x80000000\n"
      "s_and_b32 s9, s9, 0xffff\n"
      "s_mov_b32 s45, 0xbeaaaaab\n"
      "s_mov_b32 s46, 0xbe800000\n"
      "s_mov_b32 s47, 0x3eaaaaab\n"
      "s_add_u32 m0, s63, 12288\n"
      "s_add_u32 s52, s63, 61440\n"
      "buffer_load_dwordx4 v17, s[12:15], s52 offen offset:0 lds\n"
      "buffer_load_dwordx4 v18, s[12:15], s52 offen offset:1024 lds\n"
      "buffer_load_dwordx4 v19, s[12:15], s52 offen offset:2048 lds\n"
      "s_add_u32 m0, s63, 18432\n"
      "s_add_u32 s52, s63, 116736\n"
      "buffer_load_dwordx4 v17, s[12:15], s52 offen offset:0 lds\n"
      "buffer_load_dwordx4 v18, s[12:15], s52 offen offset:1024 lds\n"
      "buffer_load_dwordx4 v19, s[12:15], s52 offen offset:2048 lds\n"
      "LSpro_%=:\n"
      "s_waitcnt vmcnt(6)\n"
      "s_barrier\n"
      "ds_read_b64_tr_b16 a[224:225], v60 offset:24832\n"
      "ds_read_b64_tr_b16 a[226:227], v60 offset:25600\n"
      "ds_read_b64_tr_b16 a[0:1], v60 offset:0\n"
      "ds_read_b64_tr_b16 a[2:3], v60 offset:768\n"
      "ds_read_b64_tr_b16 a[220:221], v60 offset:30976\n"
      "ds_read_b64_tr_b16 a[222:223], v60 offset:31744\n"
      "ds_read_b64_tr_b16 a[232:233], v60 offset:24896\n"
      "ds_read_b64_tr_b16 a[234:235], v60 offset:25664\n"
      "ds_read_b64_tr_b16 a[4:5], v60 offset:64\n"
      "ds_read_b64_tr_b16 a[6:7], v60 offset:832\n"
      "ds_read_b64_tr_b16 a[228:229], v60 offset:31040\n"
      "ds_read_b64_tr_b16 a[230:231], v60 offset:31808\n"
      "s_add_u32 m0, s63, 41216\n"
      "s_add_u32 s52, s63, 12288\n"
      "s_nop 0\n"
      "s_waitcnt lgkmcnt(10)\n"
      "v_mfma_f32_4x4x4_16b_bf16 v[28:31], a[244:245], a[224:225], 0\n"
      "v_mfma_f32_4x4x4_16b_bf16 v[32:35], a[244:245], a[226:227], 0\n"
      "buffer_load_dwordx4 v17, s[12:15], s52 offen offset:0 lds\n"
      "s_waitcnt lgkmcnt(8)\n"
      "v_mfma_f32_4x4x4_16b_bf16 v[28:31], a[248:249], a[0:1], v[28:31]\n"
      "ds_read_b64_tr_b16 a[240:241], v60 offset:24960\n"
      "v_mfma_f32_4x4x4_16b_bf16 v[32:35], a[248:249], a[2:3], v[32:35]\n"
      "buffer_load_dwordx4 v18, s[12:15], s52 offen offset:1024 lds\n"
      "s_waitcnt lgkmcnt(7)\n"
      "v_mfma_f32_4x4x4_16b_bf16 v[36:39], a[246:247], a[220:221], v[28:31]\n"
      "ds_read_b64_tr_b16 a[242:243], v60 offset:25728\n"
      "v_mfma_f32_4x4x4_16b_bf16 v[40:43], a[246:247], a[222:223], v[32:35]\n"
      "buffer_load_dwordx4 v19, s[12:15], s52 offen offset:2048 lds\n"
      "v_mfma_f32_4x4x4_16b_bf16 v[28:31], a[250:251], a[220:221], v[28:31]\n"
      "ds_read_b64_tr_b16 a[8:9], v60 offset:128\n"
      "v_mfma_f32_4x4x4_16b_bf16 v[32:35], a[250:251], a[222:223], v[32:35]\n"
      "ds_read_b64_tr_b16 a[10:11], v60 offset:896\n"
      "ds_read_b64_tr_b16 a[236:237], v60 offset:31104\n"
      "ds_read_b64_tr_b16 a[238:239], v60 offset:31872\n"
      "s_add_u32 m0, s63, 47360\n"
      "s_add_u32 s52, s63, 67584\n"
      "s_nop 0\n"
      "s_waitcnt lgkmcnt(5)\n"
      "v_mfma_f32_4x4x4_16b_bf16 v[44:47], a[244:245], a[232:233], 0\n"
      "v_mfma_f32_4x4x4_16b_bf16 v[48:51], a[244:245], a[234:235], 0\n"
      "buffer_load_dwordx4 v17, s[12:15], s52 offen offset:0 lds\n"
      "v_mfma_f32_4x4x4_16b_bf16 v[44:47], a[248:249], a[4:5], v[44:47]\n"
      "buffer_load_dwordx4 v18, s[12:15], s52 offen offset:1024 lds\n"
      "v_mfma_f32_4x4x4_16b_bf16 v[48:51], a[248:249], a[6:7], v[48:51]\n"
      "buffer_load_dwordx4 v19, s[12:15], s52 offen offset:2048 lds\n"
      "v_mfma_f32_4x4x4_16b_bf16 v[52:55], a[246:247], a[228:229], v[44:47]\n"
      "v_mfma_f32_4x4x4_16b_bf16 v[56:59], a[246:247], a[230:231], v[48:51]\n"
      "v_mfma_f32_4x4x4_16b_bf16 v[44:47], a[250:251], a[228:229], v[44:47]\n"
      "v_mfma_f32_4x4x4_16b_bf16 v[48:51], a[250:251], a[230:231], v[48:51]\n"
      "v_cvt_pk_bf16_f32 v252, v36, v40\n"
      "v_cvt_pk_bf16_f32 v253, v37, v41\n"
      "v_cvt_pk_bf16_f32 v254, v38, v42\n"
      "v_cvt_pk_bf16_f32 v255, v39, v43\n"
      "v_cvt_pk_bf16_f32 v248, v28, v32\n"
      "v_cvt_pk_bf16_f32 v249, v29, v33\n"
      "v_cvt_pk_bf16_f32 v250, v30, v34\n"
      "v_cvt_pk_bf16_f32 v251, v31, v35\n"
      "s_add_u32 m0, s63, 59648\n"
      "s_add_u32 s52, s63, 18432\n"
      "s_nop 0\n"
      "s_waitcnt lgkmcnt(0)\n"
      "v_mfma_f32_4x4x4_16b_bf16 v[28:31], a[244:245], a[240:241], 0\n"
      "v_mfma_f32_4x4x4_16b_bf16 v[32:35], a[244:245], a[242:243], 0\n"
      "buffer_load_dwordx4 v17, s[12:15], s52 offen offset:0 lds\n"
      "v_mfma_f32_4x4x4_16b_bf16 v[28:31], a[248:249], a[8:9], v[28:31]\n"
      "buffer_load_dwordx4 v18, s[12:15], s52 offen offset:1024 lds\n"
      "v_mfma_f32_4x4x4_16b_bf16 v[32:35], a[248:249], a[10:11], v[32:35]\n"
      "buffer_load_dwordx4 v19, s[12:15], s52 offen offset:2048 lds\n"
      "v_mfma_f32_4x4x4_16b_bf16 v[36:39], a[246:247], a[236:237], v[28:31]\n"
      "v_mfma_f32_4x4x4_16b_bf16 v[40:43], a[246:247], a[238:239], v[32:35]\n"
      "v_mfma_f32_4x4x4_16b_bf16 v[28:31], a[250:251], a[236:237], v[28:31]\n"
      "v_mfma_f32_4x4x4_16b_bf16 v[32:35], a[250:251], a[238:239], v[32:35]\n"
      "v_cvt_pk_bf16_f32 v244, v52, v56\n"
      "v_cvt_pk_bf16_f32 v245, v53, v57\n"
      "v_cvt_pk_bf16_f32 v246, v54, v58\n"
      "v_cvt_pk_bf16_f32 v247, v55, v59\n"
      "v_cvt_pk_bf16_f32 v240, v44, v48\n"
      "v_cvt_pk_bf16_f32 v241, v45, v49\n"
      "v_cvt_pk_bf16_f32 v242, v46, v50\n"
      "v_cvt_pk_bf16_f32 v243, v47, v51\n"
      "s_waitcnt vmcnt(9)\n"
      "s_barrier\n"
      "ds_read_b64_tr_b16 a[224:225], v60 offset:6144\n"
      "ds_read_b64_tr_b16 a[226:227], v60 offset:6912\n"
      "ds_read_b64_tr_b16 a[12:13], v60 offset:18432\n"
      "ds_read_b64_tr_b16 a[14:15], v60 offset:19200\n"
      "ds_read_b64_tr_b16 a[220:221], v60 offset:12288\n"
      "ds_read_b64_tr_b16 a[222:223], v60 offset:13056\n"
      "s_add_u32 m0, s63, 65792\n"
      "s_add_u32 s52, s63, 73728\n"
      "s_nop 0\n"
      "s_waitcnt lgkmcnt(4)\n"
      "v_mfma_f32_4x4x4_16b_bf16 v[44:47], a[244:245], a[224:225], 0\n"
      "v_mfma_f32_4x4x4_16b_bf16 v[48:51], a[244:245], a[226:227], 0\n"
      "buffer_load_dwordx4 v17, s[12:15], s52 offen offset:0 lds\n"
      "s_waitcnt lgkmcnt(2)\n"
      "v_mfma_f32_4x4x4_16b_bf16 v[44:47], a[248:249], a[12:13], v[44:47]\n"
      "ds_read_b64_tr_b16 a[232:233], v60 offset:6208\n"
      "v_mfma_f32_4x4x4_16b_bf16 v[48:51], a[248:249], a[14:15], v[48:51]\n"
      "buffer_load_dwordx4 v18, s[12:15], s52 offen offset:1024 lds\n"
      "s_waitcnt lgkmcnt(1)\n"
      "v_mfma_f32_4x4x4_16b_bf16 v[52:55], a[246:247], a[220:221], v[44:47]\n"
      "ds_read_b64_tr_b16 a[234:235], v60 offset:6976\n"
      "v_mfma_f32_4x4x4_16b_bf16 v[56:59], a[246:247], a[222:223], v[48:51]\n"
      "buffer_load_dwordx4 v19, s[12:15], s52 offen offset:2048 lds\n"
      "v_mfma_f32_4x4x4_16b_bf16 v[44:47], a[250:251], a[220:221], v[44:47]\n"
      "ds_read_b64_tr_b16 a[16:17], v60 offset:18496\n"
      "v_mfma_f32_4x4x4_16b_bf16 v[48:51], a[250:251], a[222:223], v[48:51]\n"
      "ds_read_b64_tr_b16 a[18:19], v60 offset:19264\n"
      "ds_read_b64_tr_b16 a[228:229], v60 offset:12352\n"
      "ds_read_b64_tr_b16 a[230:231], v60 offset:13120\n"
      "ds_read_b64_tr_b16 a[240:241], v60 offset:6272\n"
      "ds_read_b64_tr_b16 a[242:243], v60 offset:7040\n"
      "ds_read_b64_tr_b16 a[20:21], v60 offset:18560\n"
      "ds_read_b64_tr_b16 a[22:23], v60 offset:19328\n"
      "ds_read_b64_tr_b16 a[236:237], v60 offset:12416\n"
      "ds_read_b64_tr_b16 a[238:239], v60 offset:13184\n"
      "ds_read_b64_tr_b16 a[164:165], v7 offset:0\n"
      "ds_read_b64_tr_b16 a[166:167], v7 offset:8\n"
      "ds_read_b128 a[204:207], v3 offset:0\n"
      "v_cvt_pk_bf16_f32 v236, v36, v40\n"
      "v_cvt_pk_bf16_f32 v237, v37, v41\n"
      "v_cvt_pk_bf16_f32 v238, v38, v42\n"
      "v_cvt_pk_bf16_f32 v239, v39, v43\n"
      "v_cvt_pk_bf16_f32 v232, v28, v32\n"
      "v_cvt_pk_bf16_f32 v233, v29, v33\n"
      "v_cvt_pk_bf16_f32 v234, v30, v34\n"
      "v_cvt_pk_bf16_f32 v235, v31, v35\n"
      "s_waitcnt vmcnt(6)\n"
      "s_barrier\n"
      "s_add_u32 m0, s63, 116224\n"
      "s_add_u32 s52, s63, 24576\n"
      "s_waitcnt lgkmcnt(7)\n"
      "v_mfma_f32_4x4x4_16b_bf16 v[28:31], a[244:245], a[232:233], 0\n"
      "v_mfma_f32_4x4x4_16b_bf16 v[32:35], a[244:245], a[234:235], 0\n"
      "buffer_load_dwordx4 v17, s[12:15], s52 offen offset:0 lds\n"
      "v_mfma_f32_4x4x4_16b_bf16 v[28:31], a[248:249], a[16:17], v[28:31]\n"
      "ds_read_b64_tr_b16 a[224:225], v60 offset:41216\n"
      "v_mfma_f32_4x4x4_16b_bf16 v[32:35], a[248:249], a[18:19], v[32:35]\n"
      "buffer_load_dwordx4 v18, s[12:15], s52 offen offset:1024 lds\n"
      "v_mfma_f32_4x4x4_16b_bf16 v[36:39], a[246:247], a[228:229], v[28:31]\n"
      "ds_read_b64_tr_b16 a[226:227], v60 offset:41984\n"
      "v_mfma_f32_4x4x4_16b_bf16 v[40:43], a[246:247], a[230:231], v[32:35]\n"
      "buffer_load_dwordx4 v19, s[12:15], s52 offen offset:2048 lds\n"
      "v_mfma_f32_4x4x4_16b_bf16 v[28:31], a[250:251], a[228:229], v[28:31]\n"
      "ds_read_b64_tr_b16 a[24:25], v60 offset:53504\n"
      "v_mfma_f32_4x4x4_16b_bf16 v[32:35], a[250:251], a[230:231], v[32:35]\n"
      "ds_read_b64_tr_b16 a[26:27], v60 offset:54272\n"
      "s_waitcnt lgkmcnt(4)\n"
      "v_mfma_f32_4x4x4_16b_bf16 v[20:23], a[216:217], a[164:165], 0\n"
      "ds_read_b64_tr_b16 a[220:221], v60 offset:47360\n"
      "v_mfma_f32_4x4x4_16b_bf16 v[24:27], a[216:217], a[166:167], 0\n"
      "ds_read_b64_tr_b16 a[222:223], v60 offset:48128\n"
      "ds_read_b64_tr_b16 a[160:161], v7 offset:512\n"
      "ds_read_b64_tr_b16 a[162:163], v7 offset:520\n"
      "ds_read_b128 a[208:211], v3 offset:64\n"
      "v_cvt_pk_bf16_f32 v228, v52, v56\n"
      "v_cvt_pk_bf16_f32 v229, v53, v57\n"
      "v_cvt_pk_bf16_f32 v230, v54, v58\n"
      "v_cvt_pk_bf16_f32 v231, v55, v59\n"
      "v_cvt_pk_bf16_f32 v224, v44, v48\n"
      "v_cvt_pk_bf16_f32 v225, v45, v49\n"
      "v_cvt_pk_bf16_f32 v226, v46, v50\n"
      "v_cvt_pk_bf16_f32 v227, v47, v51\n"
      "s_add_u32 m0, s63, 122368\n"
      "s_add_u32 s52, s63, 79872\n"
      "v_mfma_f32_4x4x4_16b_bf16 v[44:47], a[244:245], a[240:241], 0\n"
      "v_mfma_f32_4x4x4_16b_bf16 v[48:51], a[244:245], a[242:243], 0\n"
      "buffer_load_dwordx4 v17, s[12:15], s52 offen offset:0 lds\n"
      "v_mfma_f32_4x4x4_16b_bf16 v[44:47], a[248:249], a[20:21], v[44:47]\n"
      "ds_read_b64_tr_b16 a[232:233], v60 offset:41280\n"
      "v_mfma_f32_4x4x4_16b_bf16 v[48:51], a[248:249], a[22:23], v[48:51]\n"
      "buffer_load_dwordx4 v18, s[12:15], s52 offen offset:1024 lds\n"
      "v_mfma_f32_4x4x4_16b_bf16 v[52:55], a[246:247], a[236:237], v[44:47]\n"
      "ds_read_b64_tr_b16 a[234:235], v60 offset:42048\n"
      "v_mfma_f32_4x4x4_16b_bf16 v[56:59], a[246:247], a[238:239], v[48:51]\n"
      "buffer_load_dwordx4 v19, s[12:15], s52 offen offset:2048 lds\n"
      "v_mfma_f32_4x4x4_16b_bf16 v[44:47], a[250:251], a[236:237], v[44:47]\n"
      "ds_read_b64_tr_b16 a[28:29], v60 offset:53568\n"
      "v_mfma_f32_4x4x4_16b_bf16 v[48:51], a[250:251], a[238:239], v[48:51]\n"
      "ds_read_b64_tr_b16 a[30:31], v60 offset:54336\n"
      "s_nop 0\n"
      "s_waitcnt lgkmcnt(4)\n"
      "v_mfma_f32_4x4x4_16b_bf16 v[100:103], a[216:217], a[160:161], 0\n"
      "ds_read_b64_tr_b16 a[228:229], v60 offset:47424\n"
      "v_mfma_f32_4x4x4_16b_bf16 v[104:107], a[216:217], a[162:163], 0\n"
      "ds_read_b64_tr_b16 a[230:231], v60 offset:48192\n"
      "ds_read_b64_tr_b16 a[156:157], v7 offset:1024\n"
      "ds_read_b64_tr_b16 a[158:159], v7 offset:1032\n"
      "v_cvt_pk_bf16_f32 v220, v36, v40\n"
      "v_cvt_pk_bf16_f32 v221, v37, v41\n"
      "v_cvt_pk_bf16_f32 v222, v38, v42\n"
      "v_cvt_pk_bf16_f32 v223, v39, v43\n"
      "v_cvt_pk_bf16_f32 v216, v28, v32\n"
      "v_cvt_pk_bf16_f32 v217, v29, v33\n"
      "v_mfma_f32_16x16x32_bf16 v[64:67], a[0:3], a[204:207], 0\n"
      "v_cvt_pk_bf16_f32 v218, v30, v34\n"
      "v_cvt_pk_bf16_f32 v219, v31, v35\n"
      "v_mfma_f32_16x16x32_bf16 v[68:71], a[4:7], a[204:207], 0\n"
      "v_cvt_pk_bf16_f32 v20, v20, v24\n"
      "v_cvt_pk_bf16_f32 v21, v21, v25\n"
      "v_mfma_f32_16x16x32_bf16 v[72:75], a[8:11], a[204:207], 0\n"
      "s_add_u32 m0, s63, 134656\n"
      "v_cvt_pk_bf16_f32 v22, v22, v26\n"
      "v_cvt_pk_bf16_f32 v23, v23, v27\n"
      "s_add_u32 s52, s63, 30720\n"
      "v_mfma_f32_4x4x4_16b_bf16 v[28:31], a[244:245], a[224:225], 0\n"
      "v_mfma_f32_4x4x4_16b_bf16 v[32:35], a[244:245], a[226:227], 0\n"
      "buffer_load_dwordx4 v17, s[12:15], s52 offen offset:0 lds\n"
      "v_mfma_f32_4x4x4_16b_bf16 v[28:31], a[248:249], a[24:25], v[28:31]\n"
      "ds_read_b64_tr_b16 a[240:241], v60 offset:41344\n"
      "v_mfma_f32_4x4x4_16b_bf16 v[32:35], a[248:249], a[26:27], v[32:35]\n"
      "buffer_load_dwordx4 v18, s[12:15], s52 offen offset:1024 lds\n"
      "v_mfma_f32_4x4x4_16b_bf16 v[36:39], a[246:247], a[220:221], v[28:31]\n"
      "ds_read_b64_tr_b16 a[242:243], v60 offset:42112\n"
      "v_mfma_f32_4x4x4_16b_bf16 v[40:43], a[246:247], a[222:223], v[32:35]\n"
      "buffer_load_dwordx4 v19, s[12:15], s52 offen offset:2048 lds\n"
      "v_mfma_f32_4x4x4_16b_bf16 v[28:31], a[250:251], a[220:221], v[28:31]\n"
      "ds_read_b64_tr_b16 a[32:33], v60 offset:53632\n"
      "v_mfma_f32_4x4x4_16b_bf16 v[32:35], a[250:251], a[222:223], v[32:35]\n"
      "ds_read_b64_tr_b16 a[34:35], v60 offset:54400\n"
      "s_nop 0\n"
      "s_waitcnt lgkmcnt(4)\n"
      "v_mfma_f32_4x4x4_16b_bf16 v[108:111], a[216:217], a[156:157], 0\n"
      "ds_read_b64_tr_b16 a[236:237], v60 offset:47488\n"
      "v_mfma_f32_4x4x4_16b_bf16 v[112:115], a[216:217], a[158:159], 0\n"
      "ds_read_b64_tr_b16 a[238:239], v60 offset:48256\n"
      "ds_read_b64_tr_b16 a[152:153], v7 offset:1536\n"
      "ds_read_b64_tr_b16 a[154:155], v7 offset:1544\n"
      "s_mov_b32 m0, s64\n"
      "s_nop 0\n"
      "ds_write_addtid_b32 v20 offset:24832\n"
      "ds_write_addtid_b32 v21 offset:28928\n"
      "ds_write_addtid_b32 v22 offset:33024\n"
      "ds_write_addtid_b32 v23 offset:37120\n"
      "v_cvt_pk_bf16_f32 v212, v52, v56\n"
      "v_cvt_pk_bf16_f32 v213, v53, v57\n"
      "v_cvt_pk_bf16_f32 v214, v54, v58\n"
      "v_cvt_pk_bf16_f32 v215, v55, v59\n"
      "v_cvt_pk_bf16_f32 v208, v44, v48\n"
      "v_cvt_pk_bf16_f32 v209, v45, v49\n"
      "v_mfma_f32_16x16x32_bf16 v[64:67], a[12:15], a[208:211], v[64:67]\n"
      "v_cvt_pk_bf16_f32 v210, v46, v50\n"
      "v_cvt_pk_bf16_f32 v211, v47, v51\n"
      "v_mfma_f32_16x16x32_bf16 v[68:71], a[16:19], a[208:211], v[68:71]\n"
      "v_cvt_pk_bf16_f32 v100, v100, v104\n"
      "v_cvt_pk_bf16_f32 v101, v101, v105\n"
      "v_mfma_f32_16x16x32_bf16 v[72:75], a[20:23], a[208:211], v[72:75]\n"
      "s_waitcnt vmcnt(9)\n"
      "v_cvt_pk_bf16_f32 v102, v102, v106\n"
      "v_cvt_pk_bf16_f32 v103, v103, v107\n"
      "s_barrier\n"
      "s_add_u32 m0, s63, 140800\n"
      "s_add_u32 s52, s63, 86016\n"
      "v_mfma_f32_4x4x4_16b_bf16 v[44:47], a[244:245], a[232:233], 0\n"
      "v_mfma_f32_4x4x4_16b_bf16 v[48:51], a[244:245], a[234:235], 0\n"
      "buffer_load_dwordx4 v17, s[12:15], s52 offen offset:0 lds\n"
      "v_mfma_f32_4x4x4_16b_bf16 v[44:47], a[248:249], a[28:29], v[44:47]\n"
      "ds_read_b64_tr_b16 a[224:225], v61 offset:2304\n"
      "v_mfma_f32_4x4x4_16b_bf16 v[48:51], a[248:249], a[30:31], v[48:51]\n"
      "buffer_load_dwordx4 v18, s[12:15], s52 offen offset:1024 lds\n"
      "v_mfma_f32_4x4x4_16b_bf16 v[52:55], a[246:247], a[228:229], v[44:47]\n"
      "ds_read_b64_tr_b16 a[226:227], v61 offset:3072\n"
      "v_mfma_f32_4x4x4_16b_bf16 v[56:59], a[246:247], a[230:231], v[48:51]\n"
      "buffer_load_dwordx4 v19, s[12:15], s52 offen offset:2048 lds\n"
      "v_mfma_f32_4x4x4_16b_bf16 v[44:47], a[250:251], a[228:229], v[44:47]\n"
      "ds_read_b64_tr_b16 a[36:37], v61 offset:14592\n"
      "v_mfma_f32_4x4x4_16b_bf16 v[48:51], a[250:251], a[230:231], v[48:51]\n"
      "ds_read_b64_tr_b16 a[38:39], v61 offset:15360\n"
      "s_waitcnt lgkmcnt(4)\n"
      "v_mfma_f32_4x4x4_16b_bf16 v[116:119], a[216:217], a[152:153], 0\n"
      "ds_read_b64_tr_b16 a[220:221], v61 offset:8448\n"
      "v_mfma_f32_4x4x4_16b_bf16 v[120:123], a[216:217], a[154:155], 0\n"
      "ds_read_b64_tr_b16 a[222:223], v61 offset:9216\n"
      "s_mov_b32 m0, s64\n"
      "s_nop 0\n"
      "s_nop 0\n"
      "ds_write_addtid_b32 v100 offset:25344\n"
      "ds_write_addtid_b32 v101 offset:29440\n"
      "ds_write_addtid_b32 v102 offset:33536\n"
      "ds_write_addtid_b32 v103 offset:37632\n"
      "ds_read_b128 a[212:215], v3 offset:128\n"
      "v_cvt_pk_bf16_f32 v204, v36, v40\n"
      "v_cvt_pk_bf16_f32 v205, v37, v41\n"
      "v_cvt_pk_bf16_f32 v206, v38, v42\n"
      "v_cvt_pk_bf16_f32 v207, v39, v43\n"
      "v_cvt_pk_bf16_f32 v200, v28, v32\n"
      "v_cvt_pk_bf16_f32 v201, v29, v33\n"
      "v_cvt_pk_bf16_f32 v202, v30, v34\n"
      "v_cvt_pk_bf16_f32 v203, v31, v35\n"
      "v_cvt_pk_bf16_f32 v108, v108, v112\n"
      "v_cvt_pk_bf16_f32 v109, v109, v113\n"
      "v_cvt_pk_bf16_f32 v110, v110, v114\n"
      "v_cvt_pk_bf16_f32 v111, v111, v115\n"
      "s_add_u32 m0, s63, 6144\n"
      "s_add_u32 s52, s63, 36864\n"
      "v_mfma_f32_4x4x4_16b_bf16 v[28:31], a[244:245], a[240:241], 0\n"
      "v_mfma_f32_4x4x4_16b_bf16 v[32:35], a[244:245], a[242:243], 0\n"
      "buffer_load_dwordx4 v17, s[12:15], s52 offen offset:0 lds\n"
      "v_mfma_f32_4x4x4_16b_bf16 v[28:31], a[248:249], a[32:33], v[28:31]\n"
      "ds_read_b64_tr_b16 a[232:233], v61 offset:2368\n"
      "v_mfma_f32_4x4x4_16b_bf16 v[32:35], a[248:249], a[34:35], v[32:35]\n"
      "buffer_load_dwordx4 v18, s[12:15], s52 offen offset:1024 lds\n"
      "v_mfma_f32_4x4x4_16b_bf16 v[36:39], a[246:247], a[236:237], v[28:31]\n"
      "ds_read_b64_tr_b16 a[234:235], v61 offset:3136\n"
      "v_mfma_f32_4x4x4_16b_bf16 v[40:43], a[246:247], a[238:239], v[32:35]\n"
      "ds_read_b64_tr_b16 a[40:41], v61 offset:14656\n"
      "v_mfma_f32_4x4x4_16b_bf16 v[28:31], a[250:251], a[236:237], v[28:31]\n"
      "ds_read_b64_tr_b16 a[42:43], v61 offset:15424\n"
      "v_mfma_f32_4x4x4_16b_bf16 v[32:35], a[250:251], a[238:239], v[32:35]\n"
      "ds_read_b64_tr_b16 a[228:229], v61 offset:8512\n"
      "ds_read_b64_tr_b16 a[230:231], v61 offset:9280\n"
      "s_mov_b32 m0, s64\n"
      "s_nop 0\n"
      "ds_write_addtid_b32 v108 offset:25856\n"
      "ds_write_addtid_b32 v109 offset:29952\n"
      "ds_write_addtid_b32 v110 offset:34048\n"
      "ds_write_addtid_b32 v111 offset:38144\n"
      "v_cvt_pk_bf16_f32 v196, v52, v56\n"
      "v_cvt_pk_bf16_f32 v197, v53, v57\n"
      "v_cvt_pk_bf16_f32 v198, v54, v58\n"
      "v_cvt_pk_bf16_f32 v199, v55, v59\n"
      "v_cvt_pk_bf16_f32 v192, v44, v48\n"
      "v_cvt_pk_bf16_f32 v193, v45, v49\n"
      "v_cvt_pk_bf16_f32 v194, v46, v50\n"
      "v_cvt_pk_bf16_f32 v195, v47, v51\n"
      "v_cvt_pk_bf16_f32 v116, v116, v120\n"
      "v_cvt_pk_bf16_f32 v117, v117, v121\n"
      "v_cvt_pk_bf16_f32 v118, v118, v122\n"
      "v_cvt_pk_bf16_f32 v119, v119, v123\n"
      "s_waitcnt lgkmcnt(4)\n"
      "v_mfma_f32_4x4x4_16b_bf16 v[44:47], a[244:245], a[224:225], 0\n"
      "v_mfma_f32_4x4x4_16b_bf16 v[48:51], a[244:245], a[226:227], 0\n"
      "s_add_u32 m0, s63, 6144\n"
      "s_nop 0\n"
      "buffer_load_dwordx4 v19, s[12:15], s52 offen offset:2048 lds\n"
      "v_mfma_f32_4x4x4_16b_bf16 v[44:47], a[248:249], a[36:37], v[44:47]\n"
      "ds_read_b64_tr_b16 a[240:241], v61 offset:2432\n"
      "v_mfma_f32_4x4x4_16b_bf16 v[48:51], a[248:249], a[38:39], v[48:51]\n"
      "s_add_u32 m0, s63, 12288\n"
      "v_mfma_f32_4x4x4_16b_bf16 v[52:55], a[246:247], a[220:221], v[44:47]\n"
      "ds_read_b64_tr_b16 a[242:243], v61 offset:3200\n"
      "v_mfma_f32_4x4x4_16b_bf16 v[56:59], a[246:247], a[222:223], v[48:51]\n"
      "s_add_u32 s52, s63, 92160\n"
      "v_mfma_f32_4x4x4_16b_bf16 v[44:47], a[250:251], a[220:221], v[44:47]\n"
      "ds_read_b64_tr_b16 a[44:45], v61 offset:14720\n"
      "v_mfma_f32_4x4x4_16b_bf16 v[48:51], a[250:251], a[222:223], v[48:51]\n"
      "buffer_load_dwordx4 v17, s[12:15], s52 offen offset:0 lds\n"
      "ds_read_b64_tr_b16 a[46:47], v61 offset:15488\n"
      "ds_read_b64_tr_b16 a[236:237], v61 offset:8576\n"
      "ds_read_b64_tr_b16 a[238:239], v61 offset:9344\n"
      "s_mov_b32 m0, s64\n"
      "s_nop 0\n"
      "ds_write_addtid_b32 v116 offset:26368\n"
      "ds_write_addtid_b32 v117 offset:30464\n"
      "ds_write_addtid_b32 v118 offset:34560\n"
      "ds_write_addtid_b32 v119 offset:38656\n"
      "v_cvt_pk_bf16_f32 v188, v36, v40\n"
      "v_cvt_pk_bf16_f32 v189, v37, v41\n"
      "v_mfma_f32_16x16x32_bf16 v[64:67], a[24:27], a[212:215], v[64:67]\n"
      "v_cvt_pk_bf16_f32 v190, v38, v42\n"
      "v_cvt_pk_bf16_f32 v191, v39, v43\n"
      "v_mfma_f32_16x16x32_bf16 v[68:71], a[28:31], a[212:215], v[68:71]\n"
      "s_waitcnt vmcnt(10)\n"
      "v_cvt_pk_bf16_f32 v184, v28, v32\n"
      "v_cvt_pk_bf16_f32 v185, v29, v33\n"
      "v_mfma_f32_16x16x32_bf16 v[72:75], a[32:35], a[212:215], v[72:75]\n"
      "s_waitcnt lgkmcnt(0)\n"
      "v_cvt_pk_bf16_f32 v186, v30, v34\n"
      "v_cvt_pk_bf16_f32 v187, v31, v35\n"
      "s_barrier\n"
      "v_mfma_f32_4x4x4_16b_bf16 v[28:31], a[244:245], a[232:233], 0\n"
      "v_mfma_f32_4x4x4_16b_bf16 v[32:35], a[244:245], a[234:235], 0\n"
      "s_add_u32 m0, s63, 12288\n"
      "s_nop 0\n"
      "buffer_load_dwordx4 v18, s[12:15], s52 offen offset:1024 lds\n"
      "v_mfma_f32_4x4x4_16b_bf16 v[28:31], a[248:249], a[40:41], v[28:31]\n"
      "ds_read_b64_tr_b16 a[224:225], v62 offset:1536\n"
      "v_mfma_f32_4x4x4_16b_bf16 v[32:35], a[248:249], a[42:43], v[32:35]\n"
      "buffer_load_dwordx4 v19, s[12:15], s52 offen offset:2048 lds\n"
      "v_mfma_f32_4x4x4_16b_bf16 v[36:39], a[246:247], a[228:229], v[28:31]\n"
      "ds_read_b64_tr_b16 a[226:227], v62 offset:2304\n"
      "v_mfma_f32_4x4x4_16b_bf16 v[40:43], a[246:247], a[230:231], v[32:35]\n"
      "ds_read_b64_tr_b16 a[48:49], v62 offset:13824\n"
      "v_mfma_f32_4x4x4_16b_bf16 v[28:31], a[250:251], a[228:229], v[28:31]\n"
      "ds_read_b64_tr_b16 a[50:51], v62 offset:14592\n"
      "v_mfma_f32_4x4x4_16b_bf16 v[32:35], a[250:251], a[230:231], v[32:35]\n"
      "ds_read_b64_tr_b16 a[220:221], v62 offset:7680\n"
      "ds_read_b64_tr_b16 a[222:223], v62 offset:8448\n"
      "ds_read_b128 a[192:195], v4 offset:0\n"
      "ds_read_b128 a[196:199], v0 offset:0\n"
      "ds_read_b128 a[200:203], v0 offset:4096\n"
      "v_cvt_pk_bf16_f32 v180, v52, v56\n"
      "v_cvt_pk_bf16_f32 v181, v53, v57\n"
      "v_cvt_pk_bf16_f32 v182, v54, v58\n"
      "v_cvt_pk_bf16_f32 v183, v55, v59\n"
      "v_cvt_pk_bf16_f32 v176, v44, v48\n"
      "v_cvt_pk_bf16_f32 v177, v45, v49\n"
      "v_cvt_pk_bf16_f32 v178, v46, v50\n"
      "v_cvt_pk_bf16_f32 v179, v47, v51\n"
      "s_add_u32 m0, s63, 41216\n"
      "s_add_u32 s52, s63, 43008\n"
      "v_mfma_f32_4x4x4_16b_bf16 v[44:47], a[244:245], a[240:241], 0\n"
      "v_mfma_f32_4x4x4_16b_bf16 v[48:51], a[244:245], a[242:243], 0\n"
      "buffer_load_dwordx4 v17, s[12:15], s52 offen offset:0 lds\n"
      "v_mfma_f32_4x4x4_16b_bf16 v[44:47], a[248:249], a[44:45], v[44:47]\n"
      "ds_read_b64_tr_b16 a[232:233], v62 offset:1600\n"
      "v_mfma_f32_4x4x4_16b_bf16 v[48:51], a[248:249], a[46:47], v[48:51]\n"
      "buffer_load_dwordx4 v18, s[12:15], s52 offen offset:1024 lds\n"
      "v_mfma_f32_4x4x4_16b_bf16 v[52:55], a[246:247], a[236:237], v[44:47]\n"
      "ds_read_b64_tr_b16 a[234:235], v62 offset:2368\n"
      "v_mfma_f32_4x4x4_16b_bf16 v[56:59], a[246:247], a[238:239], v[48:51]\n"
      "ds_read_b64_tr_b16 a[52:53], v62 offset:13888\n"
      "v_mfma_f32_4x4x4_16b_bf16 v[44:47], a[250:251], a[236:237], v[44:47]\n"
      "ds_read_b64_tr_b16 a[54:55], v62 offset:14656\n"
      "v_mfma_f32_4x4x4_16b_bf16 v[48:51], a[250:251], a[238:239], v[48:51]\n"
      "ds_read_b64_tr_b16 a[228:229], v62 offset:7744\n"
      "ds_read_b64_tr_b16 a[230:231], v62 offset:8512\n"
      "ds_read_b128 a[204:207], v0 offset:64\n"
      "ds_read_b128 a[208:211], v0 offset:4160\n"
      "ds_read_b128 a[212:215], v0 offset:128\n"
      "v_cvt_pk_bf16_f32 v172, v36, v40\n"
      "v_cvt_pk_bf16_f32 v173, v37, v41\n"
      "v_cvt_pk_bf16_f32 v174, v38, v42\n"
      "v_cvt_pk_bf16_f32 v175, v39, v43\n"
      "v_cvt_pk_bf16_f32 v168, v28, v32\n"
      "v_cvt_pk_bf16_f32 v169, v29, v33\n"
      "v_cvt_pk_bf16_f32 v170, v30, v34\n"
      "v_cvt_pk_bf16_f32 v171, v31, v35\n"
      "s_nop 0\n"
      "s_waitcnt lgkmcnt(6)\n"
      "v_mfma_f32_4x4x4_16b_bf16 v[28:31], a[244:245], a[224:225], 0\n"
      "v_mfma_f32_4x4x4_16b_bf16 v[32:35], a[244:245], a[226:227], 0\n"
      "buffer_load_dwordx4 v19, s[12:15], s52 offen offset:2048 lds\n"
      "v_mfma_f32_4x4x4_16b_bf16 v[28:31], a[248:249], a[48:49], v[28:31]\n"
      "ds_read_b64_tr_b16 a[240:241], v62 offset:1664\n"
      "v_mfma_f32_4x4x4_16b_bf16 v[32:35], a[248:249], a[50:51], v[32:35]\n"
      "s_add_u32 m0, s63, 47360\n"
      "v_mfma_f32_4x4x4_16b_bf16 v[36:39], a[246:247], a[220:221], v[28:31]\n"
      "ds_read_b64_tr_b16 a[242:243], v62 offset:2432\n"
      "v_mfma_f32_4x4x4_16b_bf16 v[40:43], a[246:247], a[222:223], v[32:35]\n"
      "s_add_u32 s52, s63, 98304\n"
      "v_mfma_f32_4x4x4_16b_bf16 v[28:31], a[250:251], a[220:221], v[28:31]\n"
      "ds_read_b64_tr_b16 a[56:57], v62 offset:13952\n"
      "v_mfma_f32_4x4x4_16b_bf16 v[32:35], a[250:251], a[222:223], v[32:35]\n"
      "buffer_load_dwordx4 v17, s[12:15], s52 offen offset:0 lds\n"
      "ds_read_b64_tr_b16 a[58:59], v62 offset:14720\n"
      "ds_read_b64_tr_b16 a[236:237], v62 offset:7808\n"
      "ds_read_b64_tr_b16 a[238:239], v62 offset:8576\n"
      "v_mfma_f32_16x16x32_bf16 v[64:67], a[36:39], a[192:195], v[64:67]\n"
      "v_cvt_pk_bf16_f32 v164, v52, v56\n"
      "v_cvt_pk_bf16_f32 v165, v53, v57\n"
      "v_mfma_f32_16x16x32_bf16 v[68:71], a[40:43], a[192:195], v[68:71]\n"
      "v_cvt_pk_bf16_f32 v166, v54, v58\n"
      "v_cvt_pk_bf16_f32 v167, v55, v59\n"
      "v_mfma_f32_16x16x32_bf16 v[72:75], a[44:47], a[192:195], v[72:75]\n"
      "s_nop 0\n"
      "v_cvt_pk_bf16_f32 v160, v44, v48\n"
      "v_cvt_pk_bf16_f32 v161, v45, v49\n"
      "v_mfma_f32_16x16x32_bf16 v[76:79], v[252:255], a[196:199], 0\n"
      "s_waitcnt vmcnt(10)\n"
      "v_cvt_pk_bf16_f32 v162, v46, v50\n"
      "v_cvt_pk_bf16_f32 v163, v47, v51\n"
      "s_barrier\n"
      "v_mfma_f32_4x4x4_16b_bf16 v[44:47], a[244:245], a[232:233], 0\n"
      "v_mfma_f32_4x4x4_16b_bf16 v[48:51], a[244:245], a[234:235], 0\n"
      "buffer_load_dwordx4 v18, s[12:15], s52 offen offset:1024 lds\n"
      "s_waitcnt lgkmcnt(0)\n"
      "v_mfma_f32_4x4x4_16b_bf16 v[44:47], a[248:249], a[52:53], v[44:47]\n"
      "ds_read_b64_tr_b16 a[224:225], v62 offset:19968\n"
      "v_mfma_f32_4x4x4_16b_bf16 v[48:51], a[248:249], a[54:55], v[48:51]\n"
      "buffer_load_dwordx4 v19, s[12:15], s52 offen offset:2048 lds\n"
      "v_mfma_f32_4x4x4_16b_bf16 v[52:55], a[246:247], a[228:229], v[44:47]\n"
      "ds_read_b64_tr_b16 a[226:227], v62 offset:20736\n"
      "v_mfma_f32_4x4x4_16b_bf16 v[56:59], a[246:247], a[230:231], v[48:51]\n"
      "ds_read_b64_tr_b16 a[60:61], v60 offset:0\n"
      "v_mfma_f32_4x4x4_16b_bf16 v[44:47], a[250:251], a[228:229], v[44:47]\n"
      "ds_read_b64_tr_b16 a[62:63], v60 offset:768\n"
      "v_mfma_f32_4x4x4_16b_bf16 v[48:51], a[250:251], a[230:231], v[48:51]\n"
      "ds_read_b64_tr_b16 a[220:221], v62 offset:26112\n"
      "ds_read_b64_tr_b16 a[222:223], v62 offset:26880\n"
      "ds_read_b128 a[192:195], v0 offset:4224\n"
      "v_mfma_f32_16x16x32_bf16 v[80:83], v[244:247], a[196:199], 0\n"
      "v_cvt_pk_bf16_f32 v156, v36, v40\n"
      "v_cvt_pk_bf16_f32 v157, v37, v41\n"
      "v_mfma_f32_16x16x32_bf16 v[84:87], v[236:239], a[196:199], 0\n"
      "v_cvt_pk_bf16_f32 v158, v38, v42\n"
      "v_cvt_pk_bf16_f32 v159, v39, v43\n"
      "v_mfma_f32_16x16x32_bf16 v[88:91], v[248:251], a[200:203], 0\n"
      "v_cvt_pk_bf16_f32 v152, v28, v32\n"
      "v_cvt_pk_bf16_f32 v153, v29, v33\n"
      "v_mfma_f32_16x16x32_bf16 v[92:95], v[240:243], a[200:203], 0\n"
      "s_add_u32 m0, s63, 59648\n"
      "v_cvt_pk_bf16_f32 v154, v30, v34\n"
      "v_cvt_pk_bf16_f32 v155, v31, v35\n"
      "s_add_u32 s52, s63, 49152\n"
      "v_mfma_f32_4x4x4_16b_bf16 v[28:31], a[244:245], a[240:241], 0\n"
      "v_mfma_f32_4x4x4_16b_bf16 v[32:35], a[244:245], a[242:243], 0\n"
      "buffer_load_dwordx4 v17, s[12:15], s52 offen offset:0 lds\n"
      "v_mfma_f32_4x4x4_16b_bf16 v[28:31], a[248:249], a[56:57], v[28:31]\n"
      "ds_read_b64_tr_b16 a[232:233], v62 offset:20032\n"
      "v_mfma_f32_4x4x4_16b_bf16 v[32:35], a[248:249], a[58:59], v[32:35]\n"
      "buffer_load_dwordx4 v18, s[12:15], s52 offen offset:1024 lds\n"
      "v_mfma_f32_4x4x4_16b_bf16 v[36:39], a[246:247], a[236:237], v[28:31]\n"
      "ds_read_b64_tr_b16 a[234:235], v62 offset:20800\n"
      "v_mfma_f32_4x4x4_16b_bf16 v[40:43], a[246:247], a[238:239], v[32:35]\n"
      "ds_read_b64_tr_b16 a[64:65], v60 offset:64\n"
      "v_mfma_f32_4x4x4_16b_bf16 v[28:31], a[250:251], a[236:237], v[28:31]\n"
      "ds_read_b64_tr_b16 a[66:67], v60 offset:832\n"
      "v_mfma_f32_4x4x4_16b_bf16 v[32:35], a[250:251], a[238:239], v[32:35]\n"
      "ds_read_b64_tr_b16 a[228:229], v62 offset:26176\n"
      "ds_read_b64_tr_b16 a[230:231], v62 offset:26944\n"
      "ds_read_b128 a[196:199], v1 offset:0\n"
      "v_mfma_f32_16x16x32_bf16 v[96:99], v[232:235], a[200:203], 0\n"
      "s_nop 0\n"
      "v_cvt_pk_bf16_f32 v148, v52, v56\n"
      "v_cvt_pk_bf16_f32 v149, v53, v57\n"
      "v_mfma_f32_16x16x32_bf16 v[76:79], v[228:231], a[204:207], v[76:79]\n"
      "s_nop 0\n"
      "v_cvt_pk_bf16_f32 v150, v54, v58\n"
      "v_cvt_pk_bf16_f32 v151, v55, v59\n"
      "v_mfma_f32_16x16x32_bf16 v[80:83], v[220:223], a[204:207], v[80:83]\n"
      "s_nop 0\n"
      "v_cvt_pk_bf16_f32 v144, v44, v48\n"
      "v_cvt_pk_bf16_f32 v145, v45, v49\n"
      "v_mfma_f32_16x16x32_bf16 v[84:87], v[212:215], a[204:207], v[84:87]\n"
      "s_waitcnt lgkmcnt(2)\n"
      "v_cvt_pk_bf16_f32 v146, v46, v50\n"
      "v_cvt_pk_bf16_f32 v147, v47, v51\n"
      "v_mfma_f32_4x4x4_16b_bf16 v[44:47], a[244:245], a[224:225], 0\n"
      "v_mfma_f32_4x4x4_16b_bf16 v[48:51], a[244:245], a[226:227], 0\n"
      "buffer_load_dwordx4 v19, s[12:15], s52 offen offset:2048 lds\n"
      "v_mfma_f32_4x4x4_16b_bf16 v[44:47], a[248:249], a[60:61], v[44:47]\n"
      "ds_read_b64_tr_b16 a[240:241], v62 offset:20096\n"
      "v_mfma_f32_4x4x4_16b_bf16 v[48:51], a[248:249], a[62:63], v[48:51]\n"
      "s_add_u32 m0, s63, 65792\n"
      "v_mfma_f32_4x4x4_16b_bf16 v[52:55], a[246:247], a[220:221], v[44:47]\n"
      "ds_read_b64_tr_b16 a[242:243], v62 offset:20864\n"
      "v_mfma_f32_4x4x4_16b_bf16 v[56:59], a[246:247], a[222:223], v[48:51]\n"
      "s_add_u32 s52, s63, 104448\n"
      "v_mfma_f32_4x4x4_16b_bf16 v[44:47], a[250:251], a[220:221], v[44:47]\n"
      "ds_read_b64_tr_b16 a[68:69], v60 offset:128\n"
      "v_mfma_f32_4x4x4_16b_bf16 v[48:51], a[250:251], a[222:223], v[48:51]\n"
      "buffer_load_dwordx4 v17, s[12:15], s52 offen offset:0 lds\n"
      "ds_read_b64_tr_b16 a[70:71], v60 offset:896\n"
      "ds_read_b64_tr_b16 a[236:237], v62 offset:26240\n"
      "ds_read_b64_tr_b16 a[238:239], v62 offset:27008\n"
      "ds_read_b128 a[200:203], v1 offset:4096\n"
      "ds_read_b128 a[204:207], v4 offset:64\n"
      "v_mfma_f32_16x16x32_bf16 v[88:91], v[224:227], a[208:211], v[88:91]\n"
      "v_cvt_pk_bf16_f32 v140, v36, v40\n"
      "v_cvt_pk_bf16_f32 v141, v37, v41\n"
      "v_mfma_f32_16x16x32_bf16 v[92:95], v[216:219], a[208:211], v[92:95]\n"
      "s_nop 0\n"
      "v_cvt_pk_bf16_f32 v142, v38, v42\n"
      "v_cvt_pk_bf16_f32 v143, v39, v43\n"
      "v_mfma_f32_16x16x32_bf16 v[96:99], v[208:211], a[208:211], v[96:99]\n"
      "s_nop 0\n"
      "v_cvt_pk_bf16_f32 v136, v28, v32\n"
      "v_cvt_pk_bf16_f32 v137, v29, v33\n"
      "v_mfma_f32_16x16x32_bf16 v[76:79], v[204:207], a[212:215], v[76:79]\n"
      "s_waitcnt vmcnt(10)\n"
      "v_cvt_pk_bf16_f32 v138, v30, v34\n"
      "v_cvt_pk_bf16_f32 v139, v31, v35\n"
      "s_barrier\n"
      "v_mfma_f32_4x4x4_16b_bf16 v[28:31], a[244:245], a[232:233], 0\n"
      "v_mfma_f32_4x4x4_16b_bf16 v[32:35], a[244:245], a[234:235], 0\n"
      "buffer_load_dwordx4 v18, s[12:15], s52 offen offset:1024 lds\n"
      "v_mfma_f32_4x4x4_16b_bf16 v[28:31], a[248:249], a[64:65], v[28:31]\n"
      "ds_read_b64_tr_b16 a[224:225], v60 offset:6144\n"
      "v_mfma_f32_4x4x4_16b_bf16 v[32:35], a[248:249], a[66:67], v[32:35]\n"
      "buffer_load_dwordx4 v19, s[12:15], s52 offen offset:2048 lds\n"
      "s_nop 0\n"
      "s_waitcnt lgkmcnt(1)\n"
      "v_mfma_f32_4x4x4_16b_bf16 v[36:39], a[246:247], a[228:229], v[28:31]\n"
      "ds_read_b64_tr_b16 a[226:227], v60 offset:6912\n"
      "v_mfma_f32_4x4x4_16b_bf16 v[40:43], a[246:247], a[230:231], v[32:35]\n"
      "ds_read_b64_tr_b16 a[72:73], v60 offset:18432\n"
      "v_mfma_f32_4x4x4_16b_bf16 v[28:31], a[250:251], a[228:229], v[28:31]\n"
      "ds_read_b64_tr_b16 a[74:75], v60 offset:19200\n"
      "v_mfma_f32_4x4x4_16b_bf16 v[32:35], a[250:251], a[230:231], v[32:35]\n"
      "ds_read_b64_tr_b16 a[220:221], v60 offset:12288\n"
      "ds_read_b64_tr_b16 a[222:223], v60 offset:13056\n"
      "ds_read_b128 a[208:211], v1 offset:64\n"
      "v_mfma_f32_16x16x32_bf16 v[80:83], v[196:199], a[212:215], v[80:83]\n"
      "v_cvt_pk_bf16_f32 v132, v52, v56\n"
      "v_cvt_pk_bf16_f32 v133, v53, v57\n"
      "v_mfma_f32_16x16x32_bf16 v[84:87], v[188:191], a[212:215], v[84:87]\n"
      "v_cvt_pk_bf16_f32 v134, v54, v58\n"
      "v_cvt_pk_bf16_f32 v135, v55, v59\n"
      "v_mfma_f32_16x16x32_bf16 v[88:91], v[200:203], a[192:195], v[88:91]\n"
      "v_cvt_pk_bf16_f32 v128, v44, v48\n"
      "v_cvt_pk_bf16_f32 v129, v45, v49\n"
      "v_mfma_f32_16x16x32_bf16 v[92:95], v[192:195], a[192:195], v[92:95]\n"
      "v_cvt_pk_bf16_f32 v130, v46, v50\n"
      "v_cvt_pk_bf16_f32 v131, v47, v51\n"
      "v_mfma_f32_4x4x4_16b_bf16 v[44:47], a[244:245], a[240:241], 0\n"
      "v_mfma_f32_4x4x4_16b_bf16 v[48:51], a[244:245], a[242:243], 0\n"
      "ds_read_b64_tr_b16 a[232:233], v60 offset:6208\n"
      "v_mfma_f32_4x4x4_16b_bf16 v[44:47], a[248:249], a[68:69], v[44:47]\n"
      "ds_read_b64_tr_b16 a[234:235], v60 offset:6976\n"
      "v_mfma_f32_4x4x4_16b_bf16 v[48:51], a[248:249], a[70:71], v[48:51]\n"
      "ds_read_b64_tr_b16 a[76:77], v60 offset:18496\n"
      "v_mfma_f32_4x4x4_16b_bf16 v[52:55], a[246:247], a[236:237], v[44:47]\n"
      "ds_read_b64_tr_b16 a[78:79], v60 offset:19264\n"
      "v_mfma_f32_4x4x4_16b_bf16 v[56:59], a[246:247], a[238:239], v[48:51]\n"
      "ds_read_b64_tr_b16 a[228:229], v60 offset:12352\n"
      "v_mfma_f32_4x4x4_16b_bf16 v[44:47], a[250:251], a[236:237], v[44:47]\n"
      "ds_read_b64_tr_b16 a[230:231], v60 offset:13120\n"
      "v_mfma_f32_4x4x4_16b_bf16 v[48:51], a[250:251], a[238:239], v[48:51]\n"
      "ds_read_b128 a[212:215], v1 offset:4160\n"
      "v_mfma_f32_16x16x32_bf16 v[96:99], v[184:187], a[192:195], v[96:99]\n"
      "s_nop 0\n"
      "v_cvt_pk_bf16_f32 v124, v36, v40\n"
      "v_cvt_pk_bf16_f32 v125, v37, v41\n"
      "v_mfma_f32_16x16x32_bf16 v[76:79], v[180:183], a[196:199], v[76:79]\n"
      "s_nop 0\n"
      "v_cvt_pk_bf16_f32 v126, v38, v42\n"
      "v_cvt_pk_bf16_f32 v127, v39, v43\n"
      "v_mfma_f32_16x16x32_bf16 v[80:83], v[172:175], a[196:199], v[80:83]\n"
      "s_nop 0\n"
      "v_cvt_pk_bf16_f32 v120, v28, v32\n"
      "v_cvt_pk_bf16_f32 v121, v29, v33\n"
      "v_mfma_f32_16x16x32_bf16 v[84:87], v[164:167], a[196:199], v[84:87]\n"
      "s_waitcnt lgkmcnt(1)\n"
      "v_cvt_pk_bf16_f32 v122, v30, v34\n"
      "v_cvt_pk_bf16_f32 v123, v31, v35\n"
      "v_mfma_f32_4x4x4_16b_bf16 v[28:31], a[244:245], a[224:225], 0\n"
      "v_mfma_f32_4x4x4_16b_bf16 v[32:35], a[244:245], a[226:227], 0\n"
      "ds_read_b64_tr_b16 a[240:241], v60 offset:6272\n"
      "v_mfma_f32_4x4x4_16b_bf16 v[28:31], a[248:249], a[72:73], v[28:31]\n"
      "ds_read_b64_tr_b16 a[242:243], v60 offset:7040\n"
      "v_mfma_f32_4x4x4_16b_bf16 v[32:35], a[248:249], a[74:75], v[32:35]\n"
      "ds_read_b64_tr_b16 a[80:81], v60 offset:18560\n"
      "v_mfma_f32_4x4x4_16b_bf16 v[36:39], a[246:247], a[220:221], v[28:31]\n"
      "ds_read_b64_tr_b16 a[82:83], v60 offset:19328\n"
      "v_mfma_f32_4x4x4_16b_bf16 v[40:43], a[246:247], a[222:223], v[32:35]\n"
      "ds_read_b64_tr_b16 a[236:237], v60 offset:12416\n"
      "v_mfma_f32_4x4x4_16b_bf16 v[28:31], a[250:251], a[220:221], v[28:31]\n"
      "ds_read_b64_tr_b16 a[238:239], v60 offset:13184\n"
      "v_mfma_f32_4x4x4_16b_bf16 v[32:35], a[250:251], a[222:223], v[32:35]\n"
      "ds_read_b128 a[192:195], v4 offset:128\n"
      "ds_read_b128 a[196:199], v1 offset:128\n"
      "v_mfma_f32_16x16x32_bf16 v[88:91], v[176:179], a[200:203], v[88:91]\n"
      "v_cvt_pk_bf16_f32 v116, v52, v56\n"
      "v_cvt_pk_bf16_f32 v117, v53, v57\n"
      "v_mfma_f32_16x16x32_bf16 v[92:95], v[168:171], a[200:203], v[92:95]\n"
      "v_cvt_pk_bf16_f32 v118, v54, v58\n"
      "v_cvt_pk_bf16_f32 v119, v55, v59\n"
      "v_mfma_f32_16x16x32_bf16 v[96:99], v[160:163], a[200:203], v[96:99]\n"
      "v_cvt_pk_bf16_f32 v112, v44, v48\n"
      "v_cvt_pk_bf16_f32 v113, v45, v49\n"
      "v_mfma_f32_16x16x32_bf16 v[64:67], a[48:51], a[204:207], v[64:67]\n"
      "s_waitcnt vmcnt(6)\n"
      "v_cvt_pk_bf16_f32 v114, v46, v50\n"
      "v_cvt_pk_bf16_f32 v115, v47, v51\n"
      "s_barrier\n"
      "v_mfma_f32_4x4x4_16b_bf16 v[44:47], a[244:245], a[232:233], 0\n"
      "v_mfma_f32_4x4x4_16b_bf16 v[48:51], a[244:245], a[234:235], 0\n"
      "ds_read_b64_tr_b16 a[224:225], v60 offset:41216\n"
      "v_mfma_f32_4x4x4_16b_bf16 v[44:47], a[248:249], a[76:77], v[44:47]\n"
      "ds_read_b64_tr_b16 a[226:227], v60 offset:41984\n"
      "v_mfma_f32_4x4x4_16b_bf16 v[48:51], a[248:249], a[78:79], v[48:51]\n"
      "ds_read_b64_tr_b16 a[84:85], v60 offset:53504\n"
      "v_mfma_f32_4x4x4_16b_bf16 v[52:55], a[246:247], a[228:229], v[44:47]\n"
      "ds_read_b64_tr_b16 a[86:87], v60 offset:54272\n"
      "v_mfma_f32_4x4x4_16b_bf16 v[56:59], a[246:247], a[230:231], v[48:51]\n"
      "ds_read_b64_tr_b16 a[220:221], v60 offset:47360\n"
      "v_mfma_f32_4x4x4_16b_bf16 v[44:47], a[250:251], a[228:229], v[44:47]\n"
      "ds_read_b64_tr_b16 a[222:223], v60 offset:48128\n"
      "v_mfma_f32_4x4x4_16b_bf16 v[48:51], a[250:251], a[230:231], v[48:51]\n"
      "ds_read_b128 a[200:203], v1 offset:4224\n"
      "v_mfma_f32_16x16x32_bf16 v[68:71], a[52:55], a[204:207], v[68:71]\n"
      "v_cvt_pk_bf16_f32 v108, v36, v40\n"
      "v_cvt_pk_bf16_f32 v109, v37, v41\n"
      "v_mfma_f32_16x16x32_bf16 v[72:75], a[56:59], a[204:207], v[72:75]\n"
      "v_cvt_pk_bf16_f32 v110, v38, v42\n"
      "v_cvt_pk_bf16_f32 v111, v39, v43\n"
      "v_mfma_f32_16x16x32_bf16 v[76:79], v[156:159], a[208:211], v[76:79]\n"
      "s_nop 0\n"
      "v_cvt_pk_bf16_f32 v104, v28, v32\n"
      "v_cvt_pk_bf16_f32 v105, v29, v33\n"
      "v_mfma_f32_16x16x32_bf16 v[80:83], v[148:151], a[208:211], v[80:83]\n"
      "s_waitcnt lgkmcnt(1)\n"
      "v_cvt_pk_bf16_f32 v106, v30, v34\n"
      "v_cvt_pk_bf16_f32 v107, v31, v35\n"
      "v_mfma_f32_4x4x4_16b_bf16 v[28:31], a[244:245], a[240:241], 0\n"
      "v_mfma_f32_4x4x4_16b_bf16 v[32:35], a[244:245], a[242:243], 0\n"
      "ds_read_b64_tr_b16 a[232:233], v60 offset:41280\n"
      "v_mfma_f32_4x4x4_16b_bf16 v[28:31], a[248:249], a[80:81], v[28:31]\n"
      "ds_read_b64_tr_b16 a[234:235], v60 offset:42048\n"
      "v_mfma_f32_4x4x4_16b_bf16 v[32:35], a[248:249], a[82:83], v[32:35]\n"
      "ds_read_b64_tr_b16 a[88:89], v60 offset:53568\n"
      "v_mfma_f32_4x4x4_16b_bf16 v[36:39], a[246:247], a[236:237], v[28:31]\n"
      "ds_read_b64_tr_b16 a[90:91], v60 offset:54336\n"
      "v_mfma_f32_4x4x4_16b_bf16 v[40:43], a[246:247], a[238:239], v[32:35]\n"
      "ds_read_b64_tr_b16 a[228:229], v60 offset:47424\n"
      "v_mfma_f32_4x4x4_16b_bf16 v[28:31], a[250:251], a[236:237], v[28:31]\n"
      "ds_read_b64_tr_b16 a[230:231], v60 offset:48192\n"
      "v_mfma_f32_4x4x4_16b_bf16 v[32:35], a[250:251], a[238:239], v[32:35]\n"
      "ds_read_b128 a[204:207], v5 offset:0\n"
      "v_mfma_f32_16x16x32_bf16 v[84:87], v[140:143], a[208:211], v[84:87]\n"
      "v_cvt_pk_bf16_f32 v100, v52, v56\n"
      "v_cvt_pk_bf16_f32 v101, v53, v57\n"
      "v_mfma_f32_16x16x32_bf16 v[88:91], v[152:155], a[212:215], v[88:91]\n"
      "s_nop 0\n"
      "v_cvt_pk_bf16_f32 v102, v54, v58\n"
      "v_cvt_pk_bf16_f32 v103, v55, v59\n"
      "v_mfma_f32_16x16x32_bf16 v[92:95], v[144:147], a[212:215], v[92:95]\n"
      "s_nop 0\n"
      "v_cvt_pk_bf16_f32 v44, v44, v48\n"
      "v_cvt_pk_bf16_f32 v45, v45, v49\n"
      "v_mfma_f32_16x16x32_bf16 v[96:99], v[136:139], a[212:215], v[96:99]\n"
      "v_cvt_pk_bf16_f32 v46, v46, v50\n"
      "v_cvt_pk_bf16_f32 v47, v47, v51\n"
      "v_mfma_f32_16x16x32_bf16 v[64:67], a[60:63], a[192:195], v[64:67]\n"
      "s_nop 0\n"
      "v_accvgpr_write_b32 a164, v44\n"
      "v_accvgpr_write_b32 a165, v45\n"
      "v_mfma_f32_16x16x32_bf16 v[68:71], a[64:67], a[192:195], v[68:71]\n"
      "s_nop 0\n"
      "v_accvgpr_write_b32 a166, v46\n"
      "v_accvgpr_write_b32 a167, v47\n"
      "v_mfma_f32_4x4x4_16b_bf16 v[44:47], a[244:245], a[224:225], 0\n"
      "v_mfma_f32_4x4x4_16b_bf16 v[48:51], a[244:245], a[226:227], 0\n"
      "ds_read_b64_tr_b16 a[240:241], v60 offset:41344\n"
      "v_mfma_f32_4x4x4_16b_bf16 v[44:47], a[248:249], a[84:85], v[44:47]\n"
      "ds_read_b64_tr_b16 a[242:243], v60 offset:42112\n"
      "v_mfma_f32_4x4x4_16b_bf16 v[48:51], a[248:249], a[86:87], v[48:51]\n"
      "ds_read_b64_tr_b16 a[92:93], v60 offset:53632\n"
      "v_mfma_f32_4x4x4_16b_bf16 v[52:55], a[246:247], a[220:221], v[44:47]\n"
      "ds_read_b64_tr_b16 a[94:95], v60 offset:54400\n"
      "v_mfma_f32_4x4x4_16b_bf16 v[56:59], a[246:247], a[222:223], v[48:51]\n"
      "ds_read_b64_tr_b16 a[236:237], v60 offset:47488\n"
      "v_mfma_f32_4x4x4_16b_bf16 v[44:47], a[250:251], a[220:221], v[44:47]\n"
      "ds_read_b64_tr_b16 a[238:239], v60 offset:48256\n"
      "v_mfma_f32_4x4x4_16b_bf16 v[48:51], a[250:251], a[222:223], v[48:51]\n"
      "ds_read_b128 a[208:211], v2 offset:0\n"
      "ds_read_b128 a[212:215], v2 offset:4096\n"
      "v_cvt_pk_bf16_f32 v36, v36, v40\n"
      "v_cvt_pk_bf16_f32 v37, v37, v41\n"
      "v_cvt_pk_bf16_f32 v38, v38, v42\n"
      "v_cvt_pk_bf16_f32 v39, v39, v43\n"
      "v_mfma_f32_16x16x32_bf16 v[72:75], a[68:71], a[192:195], v[72:75]\n"
      "v_accvgpr_write_b32 a160, v36\n"
      "v_accvgpr_write_b32 a161, v37\n"
      "v_mfma_f32_16x16x32_bf16 v[76:79], v[132:135], a[196:199], v[76:79]\n"
      "s_nop 0\n"
      "v_accvgpr_write_b32 a162, v38\n"
      "v_accvgpr_write_b32 a163, v39\n"
      "v_mfma_f32_16x16x32_bf16 v[80:83], v[124:127], a[196:199], v[80:83]\n"
      "s_nop 0\n"
      "v_cvt_pk_bf16_f32 v28, v28, v32\n"
      "v_cvt_pk_bf16_f32 v29, v29, v33\n"
      "v_mfma_f32_16x16x32_bf16 v[84:87], v[116:119], a[196:199], v[84:87]\n"
      "s_waitcnt lgkmcnt(1)\n"
      "v_cvt_pk_bf16_f32 v30, v30, v34\n"
      "v_cvt_pk_bf16_f32 v31, v31, v35\n"
      "v_mfma_f32_16x16x32_bf16 v[88:91], v[128:131], a[200:203], v[88:91]\n"
      "s_nop 0\n"
      "v_accvgpr_write_b32 a156, v28\n"
      "v_accvgpr_write_b32 a157, v29\n"
      "v_mfma_f32_16x16x32_bf16 v[92:95], v[120:123], a[200:203], v[92:95]\n"
      "s_waitcnt vmcnt(0)\n"
      "v_accvgpr_write_b32 a158, v30\n"
      "v_accvgpr_write_b32 a159, v31\n"
      "s_barrier\n"
      "v_mfma_f32_4x4x4_16b_bf16 v[28:31], a[244:245], a[232:233], 0\n"
      "v_mfma_f32_4x4x4_16b_bf16 v[32:35], a[244:245], a[234:235], 0\n"
      "ds_read_b64_tr_b16 a[224:225], v61 offset:2304\n"
      "v_mfma_f32_4x4x4_16b_bf16 v[28:31], a[248:249], a[88:89], v[28:31]\n"
      "ds_read_b64_tr_b16 a[226:227], v61 offset:3072\n"
      "v_mfma_f32_4x4x4_16b_bf16 v[32:35], a[248:249], a[90:91], v[32:35]\n"
      "ds_read_b64_tr_b16 a[96:97], v61 offset:14592\n"
      "v_mfma_f32_4x4x4_16b_bf16 v[36:39], a[246:247], a[228:229], v[28:31]\n"
      "ds_read_b64_tr_b16 a[98:99], v61 offset:15360\n"
      "v_mfma_f32_4x4x4_16b_bf16 v[40:43], a[246:247], a[230:231], v[32:35]\n"
      "ds_read_b64_tr_b16 a[220:221], v61 offset:8448\n"
      "v_mfma_f32_4x4x4_16b_bf16 v[28:31], a[250:251], a[228:229], v[28:31]\n"
      "ds_read_b64_tr_b16 a[222:223], v61 offset:9216\n"
      "v_mfma_f32_4x4x4_16b_bf16 v[32:35], a[250:251], a[230:231], v[32:35]\n"
      "ds_read_b128 a[192:195], v5 offset:64\n"
      "v_cvt_pk_bf16_f32 v52, v52, v56\n"
      "v_cvt_pk_bf16_f32 v53, v53, v57\n"
      "v_cvt_pk_bf16_f32 v54, v54, v58\n"
      "v_cvt_pk_bf16_f32 v55, v55, v59\n"
      "v_mfma_f32_16x16x32_bf16 v[96:99], v[112:115], a[200:203], v[96:99]\n"
      "v_accvgpr_write_b32 a152, v52\n"
      "v_accvgpr_write_b32 a153, v53\n"
      "v_mfma_f32_16x16x32_bf16 v[64:67], a[72:75], a[204:207], v[64:67]\n"
      "s_nop 0\n"
      "v_accvgpr_write_b32 a154, v54\n"
      "v_accvgpr_write_b32 a155, v55\n"
      "v_mfma_f32_16x16x32_bf16 v[68:71], a[76:79], a[204:207], v[68:71]\n"
      "s_nop 0\n"
      "v_cvt_pk_bf16_f32 v44, v44, v48\n"
      "v_cvt_pk_bf16_f32 v45, v45, v49\n"
      "v_mfma_f32_16x16x32_bf16 v[72:75], a[80:83], a[204:207], v[72:75]\n"
      "v_cvt_pk_bf16_f32 v46, v46, v50\n"
      "v_cvt_pk_bf16_f32 v47, v47, v51\n"
      "v_mfma_f32_16x16x32_bf16 v[76:79], v[108:111], a[208:211], v[76:79]\n"
      "s_nop 0\n"
      "v_accvgpr_write_b32 a148, v44\n"
      "v_accvgpr_write_b32 a149, v45\n"
      "v_mfma_f32_16x16x32_bf16 v[80:83], v[100:103], a[208:211], v[80:83]\n"
      "s_nop 0\n"
      "v_accvgpr_write_b32 a150, v46\n"
      "v_accvgpr_write_b32 a151, v47\n"
      "v_mfma_f32_4x4x4_16b_bf16 v[44:47], a[244:245], a[240:241], 0\n"
      "v_mfma_f32_4x4x4_16b_bf16 v[48:51], a[244:245], a[242:243], 0\n"
      "ds_read_b64_tr_b16 a[232:233], v61 offset:2368\n"
      "v_mfma_f32_4x4x4_16b_bf16 v[44:47], a[248:249], a[92:93], v[44:47]\n"
      "ds_read_b64_tr_b16 a[234:235], v61 offset:3136\n"
      "v_mfma_f32_4x4x4_16b_bf16 v[48:51], a[248:249], a[94:95], v[48:51]\n"
      "ds_read_b64_tr_b16 a[100:101], v61 offset:14656\n"
      "v_mfma_f32_4x4x4_16b_bf16 v[52:55], a[246:247], a[236:237], v[44:47]\n"
      "ds_read_b64_tr_b16 a[102:103], v61 offset:15424\n"
      "v_mfma_f32_4x4x4_16b_bf16 v[56:59], a[246:247], a[238:239], v[48:51]\n"
      "ds_read_b64_tr_b16 a[228:229], v61 offset:8512\n"
      "v_mfma_f32_4x4x4_16b_bf16 v[44:47], a[250:251], a[236:237], v[44:47]\n"
      "ds_read_b64_tr_b16 a[230:231], v61 offset:9280\n"
      "v_mfma_f32_4x4x4_16b_bf16 v[48:51], a[250:251], a[238:239], v[48:51]\n"
      "ds_read_b128 a[196:199], v2 offset:64\n"
      "ds_read_b128 a[200:203], v2 offset:4160\n"
      "v_cvt_pk_bf16_f32 v36, v36, v40\n"
      "v_cvt_pk_bf16_f32 v37, v37, v41\n"
      "v_cvt_pk_bf16_f32 v38, v38, v42\n"
      "v_cvt_pk_bf16_f32 v39, v39, v43\n"
      "v_accvgpr_write_b32 a144, v36\n"
      "v_accvgpr_write_b32 a145, v37\n"
      "v_accvgpr_write_b32 a146, v38\n"
      "v_accvgpr_write_b32 a147, v39\n"
      "v_mfma_f32_16x16x32_bf16 v[84:87], a[160:163], a[208:211], v[84:87]\n"
      "s_waitcnt lgkmcnt(3)\n"
      "v_cvt_pk_bf16_f32 v28, v28, v32\n"
      "v_cvt_pk_bf16_f32 v29, v29, v33\n"
      "v_mfma_f32_16x16x32_bf16 v[88:91], v[104:107], a[212:215], v[88:91]\n"
      "s_nop 0\n"
      "v_cvt_pk_bf16_f32 v30, v30, v34\n"
      "v_cvt_pk_bf16_f32 v31, v31, v35\n"
      "v_mfma_f32_16x16x32_bf16 v[92:95], a[164:167], a[212:215], v[92:95]\n"
      "s_nop 0\n"
      "v_accvgpr_write_b32 a140, v28\n"
      "v_accvgpr_write_b32 a141, v29\n"
      "v_mfma_f32_16x16x32_bf16 v[96:99], a[156:159], a[212:215], v[96:99]\n"
      "s_nop 0\n"
      "v_accvgpr_write_b32 a142, v30\n"
      "v_accvgpr_write_b32 a143, v31\n"
      "v_mfma_f32_4x4x4_16b_bf16 v[28:31], a[244:245], a[224:225], 0\n"
      "v_mfma_f32_4x4x4_16b_bf16 v[32:35], a[244:245], a[226:227], 0\n"
      "ds_read_b64_tr_b16 a[240:241], v61 offset:2432\n"
      "v_mfma_f32_4x4x4_16b_bf16 v[28:31], a[248:249], a[96:97], v[28:31]\n"
      "ds_read_b64_tr_b16 a[242:243], v61 offset:3200\n"
      "v_mfma_f32_4x4x4_16b_bf16 v[32:35], a[248:249], a[98:99], v[32:35]\n"
      "ds_read_b64_tr_b16 a[104:105], v61 offset:14720\n"
      "v_mfma_f32_4x4x4_16b_bf16 v[36:39], a[246:247], a[220:221], v[28:31]\n"
      "ds_read_b64_tr_b16 a[106:107], v61 offset:15488\n"
      "v_mfma_f32_4x4x4_16b_bf16 v[40:43], a[246:247], a[222:223], v[32:35]\n"
      "ds_read_b64_tr_b16 a[236:237], v61 offset:8576\n"
      "v_mfma_f32_4x4x4_16b_bf16 v[28:31], a[250:251], a[220:221], v[28:31]\n"
      "ds_read_b64_tr_b16 a[238:239], v61 offset:9344\n"
      "v_mfma_f32_4x4x4_16b_bf16 v[32:35], a[250:251], a[222:223], v[32:35]\n"
      "v_cvt_pk_bf16_f32 v52, v52, v56\n"
      "v_cvt_pk_bf16_f32 v53, v53, v57\n"
      "v_cvt_pk_bf16_f32 v54, v54, v58\n"
      "v_cvt_pk_bf16_f32 v55, v55, v59\n"
      "v_accvgpr_write_b32 a136, v52\n"
      "v_accvgpr_write_b32 a137, v53\n"
      "v_accvgpr_write_b32 a138, v54\n"
      "v_accvgpr_write_b32 a139, v55\n"
      "v_cvt_pk_bf16_f32 v44, v44, v48\n"
      "v_cvt_pk_bf16_f32 v45, v45, v49\n"
      "v_mfma_f32_16x16x32_bf16 v[64:67], a[84:87], a[192:195], v[64:67]\n"
      "s_nop 0\n"
      "v_cvt_pk_bf16_f32 v46, v46, v50\n"
      "v_cvt_pk_bf16_f32 v47, v47, v51\n"
      "v_mfma_f32_16x16x32_bf16 v[68:71], a[88:91], a[192:195], v[68:71]\n"
      "s_nop 0\n"
      "v_accvgpr_write_b32 a132, v44\n"
      "v_accvgpr_write_b32 a133, v45\n"
      "v_mfma_f32_16x16x32_bf16 v[72:75], a[92:95], a[192:195], v[72:75]\n"
      "s_nop 0\n"
      "v_accvgpr_write_b32 a134, v46\n"
      "v_accvgpr_write_b32 a135, v47\n"
      "v_mfma_f32_4x4x4_16b_bf16 v[44:47], a[244:245], a[232:233], 0\n"
      "v_mfma_f32_4x4x4_16b_bf16 v[48:51], a[244:245], a[234:235], 0\n"
      "ds_read_b128 a[204:207], v5 offset:128\n"
      "v_mfma_f32_4x4x4_16b_bf16 v[44:47], a[248:249], a[100:101], v[44:47]\n"
      "v_mfma_f32_4x4x4_16b_bf16 v[48:51], a[248:249], a[102:103], v[48:51]\n"
      "s_waitcnt lgkmcnt(1)\n"
      "v_mfma_f32_4x4x4_16b_bf16 v[52:55], a[246:247], a[228:229], v[44:47]\n"
      "v_mfma_f32_4x4x4_16b_bf16 v[56:59], a[246:247], a[230:231], v[48:51]\n"
      "v_mfma_f32_4x4x4_16b_bf16 v[44:47], a[250:251], a[228:229], v[44:47]\n"
      "v_mfma_f32_4x4x4_16b_bf16 v[48:51], a[250:251], a[230:231], v[48:51]\n"
      "v_cvt_pk_bf16_f32 v36, v36, v40\n"
      "v_cvt_pk_bf16_f32 v37, v37, v41\n"
      "v_cvt_pk_bf16_f32 v38, v38, v42\n"
      "v_cvt_pk_bf16_f32 v39, v39, v43\n"
      "v_mfma_f32_16x16x32_bf16 v[76:79], a[152:155], a[196:199], v[76:79]\n"
      "v_accvgpr_write_b32 a128, v36\n"
      "v_accvgpr_write_b32 a129, v37\n"
      "v_mfma_f32_16x16x32_bf16 v[80:83], a[144:147], a[196:199], v[80:83]\n"
      "v_accvgpr_write_b32 a130, v38\n"
      "v_accvgpr_write_b32 a131, v39\n"
      "v_mfma_f32_16x16x32_bf16 v[84:87], a[136:139], a[196:199], v[84:87]\n"
      "v_cvt_pk_bf16_f32 v28, v28, v32\n"
      "v_cvt_pk_bf16_f32 v29, v29, v33\n"
      "v_mfma_f32_16x16x32_bf16 v[88:91], a[148:151], a[200:203], v[88:91]\n"
      "v_cvt_pk_bf16_f32 v30, v30, v34\n"
      "v_cvt_pk_bf16_f32 v31, v31, v35\n"
      "v_mfma_f32_16x16x32_bf16 v[92:95], a[140:143], a[200:203], v[92:95]\n"
      "v_accvgpr_write_b32 a124, v28\n"
      "v_accvgpr_write_b32 a125, v29\n"
      "v_mfma_f32_16x16x32_bf16 v[96:99], a[132:135], a[200:203], v[96:99]\n"
      "v_accvgpr_write_b32 a126, v30\n"
      "v_accvgpr_write_b32 a127, v31\n"
      "v_mfma_f32_4x4x4_16b_bf16 v[28:31], a[244:245], a[240:241], 0\n"
      "v_mfma_f32_4x4x4_16b_bf16 v[32:35], a[244:245], a[242:243], 0\n"
      "ds_read_b128 a[208:211], v2 offset:128\n"
      "v_mfma_f32_4x4x4_16b_bf16 v[28:31], a[248:249], a[104:105], v[28:31]\n"
      "ds_read_b128 a[212:215], v2 offset:4224\n"
      "v_mfma_f32_4x4x4_16b_bf16 v[32:35], a[248:249], a[106:107], v[32:35]\n"
      "v_mfma_f32_4x4x4_16b_bf16 v[36:39], a[246:247], a[236:237], v[28:31]\n"
      "s_nop 0\n"
      "s_nop 0\n"
      "v_mfma_f32_4x4x4_16b_bf16 v[40:43], a[246:247], a[238:239], v[32:35]\n"
      "v_mfma_f32_4x4x4_16b_bf16 v[28:31], a[250:251], a[236:237], v[28:31]\n"
      "v_mfma_f32_4x4x4_16b_bf16 v[32:35], a[250:251], a[238:239], v[32:35]\n"
      "v_cvt_pk_bf16_f32 v52, v52, v56\n"
      "v_cvt_pk_bf16_f32 v53, v53, v57\n"
      "v_cvt_pk_bf16_f32 v54, v54, v58\n"
      "v_cvt_pk_bf16_f32 v55, v55, v59\n"
      "v_accvgpr_write_b32 a120, v52\n"
      "v_accvgpr_write_b32 a121, v53\n"
      "v_accvgpr_write_b32 a122, v54\n"
      "v_accvgpr_write_b32 a123, v55\n"
      "v_cvt_pk_bf16_f32 v44, v44, v48\n"
      "v_cvt_pk_bf16_f32 v45, v45, v49\n"
      "v_cvt_pk_bf16_f32 v46, v46, v50\n"
      "v_cvt_pk_bf16_f32 v47, v47, v51\n"
      "v_accvgpr_write_b32 a116, v44\n"
      "v_accvgpr_write_b32 a117, v45\n"
      "v_accvgpr_write_b32 a118, v46\n"
      "v_accvgpr_write_b32 a119, v47\n"
      "s_waitcnt lgkmcnt(0)\n"
      "s_barrier\n"
      "s_mov_b32 m0, s64\n"
      "s_nop 0\n"
      "v_mfma_f32_16x16x32_bf16 v[64:67], a[96:99], a[204:207], v[64:67]\n"
      "ds_read_b128 a[192:195], v3 offset:16896\n"
      "ds_read_b64_tr_b16 a[220:221], v7 offset:16896\n"
      "ds_read_b64_tr_b16 a[222:223], v7 offset:16904\n"
      "v_cvt_pk_bf16_f32 v36, v36, v40\n"
      "v_cvt_pk_bf16_f32 v37, v37, v41\n"
      "v_mfma_f32_16x16x32_bf16 v[68:71], a[100:103], a[204:207], v[68:71]\n"
      "ds_read_b128 a[196:199], v3 offset:16960\n"
      "v_cvt_pk_bf16_f32 v38, v38, v42\n"
      "v_cvt_pk_bf16_f32 v39, v39, v43\n"
      "v_accvgpr_write_b32 a112, v36\n"
      "v_accvgpr_write_b32 a113, v37\n"
      "v_mfma_f32_16x16x32_bf16 v[72:75], a[104:107], a[204:207], v[72:75]\n"
      "v_accvgpr_write_b32 a114, v38\n"
      "v_accvgpr_write_b32 a115, v39\n"
      "v_cvt_pk_bf16_f32 v28, v28, v32\n"
      "v_cvt_pk_bf16_f32 v29, v29, v33\n"
      "v_cvt_pk_bf16_f32 v30, v30, v34\n"
      "v_cvt_pk_bf16_f32 v31, v31, v35\n"
      "v_accvgpr_write_b32 a108, v28\n"
      "v_mfma_f32_16x16x32_bf16 v[76:79], a[128:131], a[208:211], v[76:79]\n"
      "ds_read_b128 a[200:203], v3 offset:17024\n"
      "ds_read_b64_tr_b16 a[224:225], v7 offset:17408\n"
      "ds_read_b64_tr_b16 a[226:227], v7 offset:17416\n"
      "v_accvgpr_write_b32 a109, v29\n"
      "v_accvgpr_write_b32 a110, v30\n"
      "v_accvgpr_write_b32 a111, v31\n"
      "v_mfma_f32_16x16x32_bf16 v[80:83], a[120:123], a[208:211], v[80:83]\n"
      "v_mfma_f32_16x16x32_bf16 v[84:87], a[112:115], a[208:211], v[84:87]\n"
      "s_waitcnt lgkmcnt(3)\n"
      "v_mfma_f32_4x4x4_16b_bf16 v[20:23], a[216:217], a[220:221], 0\n"
      "v_mfma_f32_4x4x4_16b_bf16 v[24:27], a[216:217], a[222:223], 0\n"
      "v_mfma_f32_16x16x32_bf16 v[88:91], a[124:127], a[212:215], v[88:91]\n"
      "s_nop 0\n"
      "ds_read_b128 a[204:207], v4 offset:16896\n"
      "v_mfma_f32_16x16x32_bf16 v[92:95], a[116:119], a[212:215], v[92:95]\n"
      "v_mfma_f32_16x16x32_bf16 v[96:99], a[108:111], a[212:215], v[96:99]\n"
      "ds_read_b64_tr_b16 a[228:229], v7 offset:17920\n"
      "ds_read_b64_tr_b16 a[230:231], v7 offset:17928\n"
      "v_cvt_pk_bf16_f32 v20, v20, v24\n"
      "v_cvt_pk_bf16_f32 v21, v21, v25\n"
      "v_cvt_pk_bf16_f32 v22, v22, v26\n"
      "v_sub_f32 v76, v88, v76\n"
      "v_sub_f32 v77, v89, v77\n"
      "v_cvt_pk_bf16_f32 v23, v23, v27\n"
      "v_mfma_f32_16x16x32_bf16 v[52:55], a[0:3], a[192:195], 0\n"
      "ds_read_b128 a[208:211], v4 offset:16960\n"
      "ds_write_addtid_b32 v20 offset:41216\n"
      "ds_write_addtid_b32 v21 offset:45312\n"
      "ds_write_addtid_b32 v22 offset:49408\n"
      "v_mfma_f32_16x16x32_bf16 v[56:59], a[4:7], a[192:195], 0\n"
      "s_waitcnt lgkmcnt(6)\n"
      "ds_write_addtid_b32 v76 offset:0\n"
      "ds_write_addtid_b32 v77 offset:512\n"
      "ds_write_addtid_b32 v23 offset:53504\n"
      "v_sub_f32 v78, v90, v78\n"
      "v_mfma_f32_4x4x4_16b_bf16 v[28:31], a[216:217], a[224:225], 0\n"
      "v_add_f32 v88, v88, v64\n"
      "v_mfma_f32_4x4x4_16b_bf16 v[32:35], a[216:217], a[226:227], 0\n"
      "ds_write_addtid_b32 v78 offset:1024\n"
      "v_sub_f32 v79, v91, v79\n"
      "v_add_f32 v89, v89, v65\n"
      "v_sub_f32 v80, v92, v80\n"
      "v_mfma_f32_16x16x32_bf16 v[60:63], a[8:11], a[192:195], 0\n"
      "ds_write_addtid_b32 v79 offset:1536\n"
      "ds_write_addtid_b32 v80 offset:2048\n"
      "v_add_f32 v90, v90, v66\n"
      "v_mfma_f32_16x16x32_bf16 v[52:55], a[12:15], a[196:199], v[52:55]\n"
      "ds_read_b128 a[212:215], v4 offset:17024\n"
      "v_sub_f32 v81, v93, v81\n"
      "v_cvt_pk_bf16_f32 v28, v28, v32\n"
      "v_mfma_f32_16x16x32_bf16 v[56:59], a[16:19], a[196:199], v[56:59]\n"
      "ds_write_addtid_b32 v28 offset:41728\n"
      "ds_read_b64_tr_b16 a[232:233], v7 offset:18432\n"
      "ds_write_addtid_b32 v81 offset:2560\n"
      "ds_read_b64_tr_b16 a[234:235], v7 offset:18440\n"
      "v_mfma_f32_16x16x32_bf16 v[60:63], a[20:23], a[196:199], v[60:63]\n"
      "v_add_f32 v91, v91, v67\n"
      "v_sub_f32 v82, v94, v82\n"
      "v_add_f32_e64 v92, v92, v68\n"
      "v_mfma_f32_16x16x32_bf16 v[52:55], a[24:27], a[200:203], v[52:55]\n"
      "s_waitcnt lgkmcnt(5)\n"
      "ds_read_b128 a[192:195], v5 offset:16896\n"
      "ds_write_addtid_b32 v82 offset:3072\n"
      "v_cvt_pk_bf16_f32 v29, v29, v33\n"
      "v_mfma_f32_4x4x4_16b_bf16 v[20:23], a[216:217], a[228:229], 0\n"
      "v_sub_f32 v83, v95, v83\n"
      "v_add_f32_e64 v93, v93, v69\n"
      "v_mfma_f32_4x4x4_16b_bf16 v[24:27], a[216:217], a[230:231], 0\n"
      "ds_write_addtid_b32 v29 offset:45824\n"
      "ds_write_addtid_b32 v83 offset:3584\n"
      "v_sub_f32 v84, v96, v84\n"
      "v_mfma_f32_16x16x32_bf16 v[56:59], a[28:31], a[200:203], v[56:59]\n"
      "ds_write_addtid_b32 v84 offset:4096\n"
      "v_cvt_pk_bf16_f32 v30, v30, v34\n"
      "v_add_f32 v94, v94, v70\n"
      "v_sub_f32 v85, v97, v85\n"
      "v_mfma_f32_16x16x32_bf16 v[60:63], a[32:35], a[200:203], v[60:63]\n"
      "s_nop 0\n"
      "ds_write_addtid_b32 v30 offset:49920\n"
      "ds_write_addtid_b32 v85 offset:4608\n"
      "v_cvt_pk_bf16_f32 v20, v20, v24\n"
      "v_mfma_f32_16x16x32_bf16 v[52:55], a[36:39], a[204:207], v[52:55]\n"
      "ds_read_b128 a[196:199], v5 offset:16960\n"
      "ds_write_addtid_b32 v20 offset:42240\n"
      "v_cvt_pk_bf16_f32 v31, v31, v35\n"
      "v_mfma_f32_16x16x32_bf16 v[56:59], a[40:43], a[204:207], v[56:59]\n"
      "ds_write_addtid_b32 v31 offset:54016\n"
      "v_sub_f32 v86, v98, v86\n"
      "v_add_f32 v95, v95, v71\n"
      "v_mfma_f32_16x16x32_bf16 v[60:63], a[44:47], a[204:207], v[60:63]\n"
      "s_waitcnt lgkmcnt(5)\n"
      "ds_write_addtid_b32 v86 offset:5120\n"
      "v_sub_f32 v87, v99, v87\n"
      "v_cvt_pk_bf16_f32 v21, v21, v25\n"
      "v_add_f32_e64 v96, v96, v72\n"
      "v_mfma_f32_4x4x4_16b_bf16 v[28:31], a[216:217], a[232:233], 0\n"
      "v_cvt_pk_bf16_f32 v22, v22, v26\n"
      "v_add_f32 v97, v97, v73\n"
      "v_mfma_f32_4x4x4_16b_bf16 v[32:35], a[216:217], a[234:235], 0\n"
      "ds_write_addtid_b32 v21 offset:46336\n"
      "ds_write_addtid_b32 v22 offset:50432\n"
      "ds_write_addtid_b32 v87 offset:5632\n"
      "v_mfma_f32_16x16x32_bf16 v[52:55], a[48:51], a[208:211], v[52:55]\n"
      "s_nop 0\n"
      "ds_read_b128 a[200:203], v5 offset:17024\n"
      "v_cvt_pk_bf16_f32 v28, v28, v32\n"
      "v_cvt_pk_bf16_f32 v23, v23, v27\n"
      "v_mfma_f32_16x16x32_bf16 v[56:59], a[52:55], a[208:211], v[56:59]\n"
      "ds_write_addtid_b32 v28 offset:42752\n"
      "ds_write_addtid_b32 v23 offset:54528\n"
      "v_cvt_pk_bf16_f32 v29, v29, v33\n"
      "v_cvt_pk_bf16_f32 v30, v30, v34\n"
      "v_cvt_pk_bf16_f32 v31, v31, v35\n"
      "v_mfma_f32_16x16x32_bf16 v[60:63], a[56:59], a[208:211], v[60:63]\n"
      "ds_write_addtid_b32 v29 offset:46848\n"
      "ds_write_addtid_b32 v30 offset:50944\n"
      "v_mfma_f32_16x16x32_bf16 v[52:55], a[60:63], a[212:215], v[52:55]\n"
      "ds_write_addtid_b32 v31 offset:55040\n"
      "v_add_f32 v98, v98, v74\n"
      "v_add_f32 v99, v99, v75\n"
      "v_fma_f32 v40, 0.5, v76, -v88\n"
      "v_mfma_f32_16x16x32_bf16 v[56:59], a[64:67], a[212:215], v[56:59]\n"
      "v_fma_f32 v41, 0.5, v77, -v89\n"
      "v_fma_f32 v42, 0.5, v78, -v90\n"
      "v_fma_f32 v43, 0.5, v79, -v91\n"
      "v_mfma_f32_16x16x32_bf16 v[60:63], a[68:71], a[212:215], v[60:63]\n"
      "s_waitcnt lgkmcnt(0)\n"
      "v_fma_f32 v44, 0.5, v80, -v92\n"
      "v_fma_f32 v45, 0.5, v81, -v93\n"
      "s_barrier\n"
      "LSloop_%=:\n"
      "v_mfma_f32_16x16x32_bf16 v[52:55], a[72:75], a[192:195], v[52:55]\n"
      "ds_read_b128 a[204:207], v0 offset:16384\n"
      "ds_read2st64_b32 v[28:29], v6 offset0:0 offset1:2\n"
      "v_fma_f32 v46, 0.5, v82, -v94\n"
      "v_mfma_f32_16x16x32_bf16 v[56:59], a[76:79], a[192:195], v[56:59]\n"
      "ds_read_b128 a[208:211], v0 offset:20480\n"
      "ds_read2st64_b32 v[30:31], v6 offset0:4 offset1:6\n"
      "v_mfma_f32_16x16x32_bf16 v[60:63], a[80:83], a[192:195], v[60:63]\n"
      "v_fma_f32 v47, 0.5, v83, -v95\n"
      "v_fma_f32 v48, 0.5, v84, -v96\n"
      "v_mfma_f32_16x16x32_bf16 v[52:55], a[84:87], a[196:199], v[52:55]\n"
      "ds_read_b128 a[212:215], v0 offset:16448\n"
      "ds_read_b64_tr_b16 a[220:221], v7 offset:33792\n"
      "v_mfma_f32_16x16x32_bf16 v[56:59], a[88:91], a[196:199], v[56:59]\n"
      "ds_read_b64_tr_b16 a[222:223], v7 offset:33800\n"
      "ds_read2st64_b32 v[32:33], v6 offset0:8 offset1:10\n"
      "v_mfma_f32_16x16x32_bf16 v[60:63], a[92:95], a[196:199], v[60:63]\n"
      "ds_read2st64_b32 v[34:35], v6 offset0:12 offset1:14\n"
      "v_fma_f32 v49, 0.5, v85, -v97\n"
      "v_mfma_f32_16x16x32_bf16 v[52:55], a[96:99], a[200:203], v[52:55]\n"
      "ds_read_b128 a[192:195], v0 offset:20544\n"
      "ds_read2st64_b32 v[36:37], v6 offset0:16 offset1:18\n"
      "v_mfma_f32_16x16x32_bf16 v[56:59], a[100:103], a[200:203], v[56:59]\n"
      "v_fma_f32 v50, 0.5, v86, -v98\n"
      "v_fma_f32 v51, 0.5, v87, -v99\n"
      "v_mfma_f32_16x16x32_bf16 v[60:63], a[104:107], a[200:203], v[60:63]\n"
      "s_waitcnt lgkmcnt(8)\n"
      "s_nop 0\n"
      "v_mfma_f32_16x16x32_bf16 v[76:79], v[252:255], a[204:207], v[52:55]\n"
      "s_waitcnt lgkmcnt(4)\n"
      "ds_read_b128 a[196:199], v0 offset:16512\n"
      "ds_read2st64_b32 v[38:39], v6 offset0:20 offset1:22\n"
      "v_fmac_f32 v28, s46, v64\n"
      "v_fmac_f32 v29, s46, v65\n"
      "v_fmac_f32 v30, s46, v66\n"
      "v_mfma_f32_4x4x4_16b_bf16 v[12:15], a[216:217], a[220:221], 0\n"
      "v_fmac_f32 v31, s46, v67\n"
      "v_mfma_f32_4x4x4_16b_bf16 v[16:19], a[216:217], a[222:223], 0\n"
      "v_sub_f32 v28, v28, v40\n"
      "v_sub_f32 v29, v29, v41\n"
      "v_mfma_f32_16x16x32_bf16 v[80:83], v[244:247], a[204:207], v[56:59]\n"
      "v_sub_f32 v30, v30, v42\n"
      "v_sub_f32 v31, v31, v43\n"
      "v_mfma_f32_16x16x32_bf16 v[84:87], v[236:239], a[204:207], v[60:63]\n"
      "v_fmac_f32 v40, s45, v28\n"
      "v_cvt_pk_bf16_f32 v12, v12, v16\n"
      "v_mfma_f32_16x16x32_bf16 v[88:91], v[248:251], a[208:211], v[52:55]\n"
      "ds_read_b128 a[200:203], v0 offset:20608\n"
      "ds_write_addtid_b32 v12 offset:24832\n"
      "v_fmac_f32 v41, s45, v29\n"
      "v_mfma_f32_16x16x32_bf16 v[92:95], v[240:243], a[208:211], v[56:59]\n"
      "ds_read_b64_tr_b16 a[224:225], v7 offset:34304\n"
      "ds_read_b64_tr_b16 a[226:227], v7 offset:34312\n"
      "v_mfma_f32_16x16x32_bf16 v[96:99], v[232:235], a[208:211], v[60:63]\n"
      "s_nop 0\n"
      "v_fmac_f32_e64 v42, s45, v30\n"
      "v_cvt_pk_bf16_f32 v13, v13, v17\n"
      "v_mfma_f32_16x16x32_bf16 v[76:79], v[228:231], a[212:215], v[76:79]\n"
      "s_nop 0\n"
      "ds_read_b128 a[204:207], v1 offset:16384\n"
      "ds_write_addtid_b32 v13 offset:28928\n"
      "v_mfma_f32_16x16x32_bf16 v[80:83], v[220:223], a[212:215], v[80:83]\n"
      "s_nop 0\n"
      "v_fmac_f32_e64 v43, s45, v31\n"
      "v_fma_f32 v28, s47, v28, v52\n"
      "v_mfma_f32_16x16x32_bf16 v[84:87], v[212:215], a[212:215], v[84:87]\n"
      "s_waitcnt lgkmcnt(4)\n"
      "v_cvt_pk_bf16_f32 v14, v14, v18\n"
      "v_fma_f32 v29, s47, v29, v53\n"
      "v_mfma_f32_16x16x32_bf16 v[88:91], v[224:227], a[192:195], v[88:91]\n"
      "s_nop 0\n"
      "ds_read_b128 a[208:211], v1 offset:20480\n"
      "ds_write_addtid_b32 v14 offset:33024\n"
      "v_mfma_f32_16x16x32_bf16 v[92:95], v[216:219], a[192:195], v[92:95]\n"
      "s_nop 0\n"
      "v_fma_f32 v30, s47, v30, v54\n"
      "v_cvt_pk_bf16_f32 v15, v15, v19\n"
      "v_mfma_f32_16x16x32_bf16 v[96:99], v[208:211], a[192:195], v[96:99]\n"
      "s_waitcnt lgkmcnt(2)\n"
      "ds_write_addtid_b32 v15 offset:37120\n"
      "v_fma_f32 v31, s47, v31, v55\n"
      "v_mfma_f32_4x4x4_16b_bf16 v[20:23], a[216:217], a[224:225], 0\n"
      "v_cvt_pk_bf16_f32 v40, v40, v41\n"
      "v_mfma_f32_4x4x4_16b_bf16 v[24:27], a[216:217], a[226:227], 0\n"
      "ds_write_addtid_b32 v40 offset:12288\n"
      "v_cvt_pk_bf16_f32 v42, v42, v43\n"
      "v_mfma_f32_16x16x32_bf16 v[76:79], v[204:207], a[196:199], v[76:79]\n"
      "ds_read_b128 a[212:215], v1 offset:16448\n"
      "ds_write_addtid_b32 v42 offset:12800\n"
      "v_cvt_pk_bf16_f32 v20, v20, v24\n"
      "v_mfma_f32_16x16x32_bf16 v[80:83], v[196:199], a[196:199], v[80:83]\n"
      "ds_write_addtid_b32 v20 offset:25344\n"
      "v_cvt_pk_bf16_f32 v28, v28, v29\n"
      "v_mfma_f32_16x16x32_bf16 v[84:87], v[188:191], a[196:199], v[84:87]\n"
      "ds_write_addtid_b32 v28 offset:14336\n"
      "v_cvt_pk_bf16_f32 v30, v30, v31\n"
      "v_mfma_f32_16x16x32_bf16 v[88:91], v[200:203], a[200:203], v[88:91]\n"
      "ds_read_b128 a[192:195], v1 offset:20544\n"
      "ds_write_addtid_b32 v30 offset:14848\n"
      "v_mfma_f32_16x16x32_bf16 v[92:95], v[192:195], a[200:203], v[92:95]\n"
      "ds_read_b64_tr_b16 a[228:229], v7 offset:34816\n"
      "ds_read_b64_tr_b16 a[230:231], v7 offset:34824\n"
      "v_fmac_f32 v32, s46, v68\n"
      "v_mfma_f32_16x16x32_bf16 v[96:99], v[184:187], a[200:203], v[96:99]\n"
      "v_cvt_pk_bf16_f32 v21, v21, v25\n"
      "v_fmac_f32 v33, s46, v69\n"
      "v_mfma_f32_16x16x32_bf16 v[76:79], v[180:183], a[204:207], v[76:79]\n"
      "ds_read_b128 a[196:199], v1 offset:16512\n"
      "ds_write_addtid_b32 v21 offset:29440\n"
      "v_mfma_f32_16x16x32_bf16 v[80:83], v[172:175], a[204:207], v[80:83]\n"
      "v_fmac_f32 v34, s46, v70\n"
      "v_cvt_pk_bf16_f32 v22, v22, v26\n"
      "v_mfma_f32_16x16x32_bf16 v[84:87], v[164:167], a[204:207], v[84:87]\n"
      "s_waitcnt lgkmcnt(3)\n"
      "ds_write_addtid_b32 v22 offset:33536\n"
      "v_fmac_f32 v35, s46, v71\n"
      "v_mfma_f32_16x16x32_bf16 v[88:91], v[176:179], a[208:211], v[88:91]\n"
      "s_nop 0\n"
      "ds_read_b128 a[200:203], v1 offset:20608\n"
      "v_sub_f32 v32, v32, v44\n"
      "v_mfma_f32_16x16x32_bf16 v[92:95], v[168:171], a[208:211], v[92:95]\n"
      "s_waitcnt lgkmcnt(4)\n"
      "v_sub_f32 v33, v33, v45\n"
      "v_cvt_pk_bf16_f32 v23, v23, v27\n"
      "v_mfma_f32_4x4x4_16b_bf16 v[12:15], a[216:217], a[228:229], 0\n"
      "v_sub_f32 v34, v34, v46\n"
      "v_mfma_f32_4x4x4_16b_bf16 v[16:19], a[216:217], a[230:231], 0\n"
      "ds_write_addtid_b32 v23 offset:37632\n"
      "v_sub_f32 v35, v35, v47\n"
      "v_mfma_f32_16x16x32_bf16 v[96:99], v[160:163], a[208:211], v[96:99]\n"
      "v_fmac_f32 v44, s45, v32\n"
      "v_fmac_f32 v45, s45, v33\n"
      "v_cvt_pk_bf16_f32 v12, v12, v16\n"
      "v_mfma_f32_16x16x32_bf16 v[76:79], v[156:159], a[212:215], v[76:79]\n"
      "ds_read_b128 a[204:207], v2 offset:16384\n"
      "ds_write_addtid_b32 v12 offset:25856\n"
      "v_mfma_f32_16x16x32_bf16 v[80:83], v[148:151], a[212:215], v[80:83]\n"
      "s_nop 0\n"
      "v_cvt_pk_bf16_f32 v13, v13, v17\n"
      "v_fmac_f32_e64 v46, s45, v34\n"
      "v_mfma_f32_16x16x32_bf16 v[84:87], v[140:143], a[212:215], v[84:87]\n"
      "s_nop 0\n"
      "ds_read_b64_tr_b16 a[232:233], v7 offset:35328\n"
      "ds_write_addtid_b32 v13 offset:29952\n"
      "v_mfma_f32_16x16x32_bf16 v[88:91], v[152:155], a[192:195], v[88:91]\n"
      "s_nop 0\n"
      "ds_read_b128 a[208:211], v2 offset:20480\n"
      "ds_read_b64_tr_b16 a[234:235], v7 offset:35336\n"
      "v_mfma_f32_16x16x32_bf16 v[92:95], v[144:147], a[192:195], v[92:95]\n"
      "s_nop 0\n"
      "v_fmac_f32_e64 v47, s45, v35\n"
      "v_fma_f32 v32, s47, v32, v56\n"
      "v_mfma_f32_16x16x32_bf16 v[96:99], v[136:139], a[192:195], v[96:99]\n"
      "s_waitcnt lgkmcnt(4)\n"
      "v_cvt_pk_bf16_f32 v14, v14, v18\n"
      "v_fma_f32 v33, s47, v33, v57\n"
      "v_mfma_f32_16x16x32_bf16 v[76:79], v[132:135], a[196:199], v[76:79]\n"
      "s_nop 0\n"
      "ds_read_b128 a[212:215], v2 offset:16448\n"
      "ds_write_addtid_b32 v14 offset:34048\n"
      "v_mfma_f32_16x16x32_bf16 v[80:83], v[124:127], a[196:199], v[80:83]\n"
      "s_nop 0\n"
      "v_fma_f32 v34, s47, v34, v58\n"
      "v_cvt_pk_bf16_f32 v15, v15, v19\n"
      "v_mfma_f32_16x16x32_bf16 v[84:87], v[116:119], a[196:199], v[84:87]\n"
      "s_nop 0\n"
      "ds_write_addtid_b32 v15 offset:38144\n"
      "v_fma_f32 v35, s47, v35, v59\n"
      "v_mfma_f32_16x16x32_bf16 v[88:91], v[128:131], a[200:203], v[88:91]\n"
      "s_waitcnt lgkmcnt(3)\n"
      "ds_read_b128 a[192:195], v2 offset:20544\n"
      "v_cvt_pk_bf16_f32 v44, v44, v45\n"
      "v_mfma_f32_4x4x4_16b_bf16 v[20:23], a[216:217], a[232:233], 0\n"
      "v_cvt_pk_bf16_f32 v46, v46, v47\n"
      "v_mfma_f32_4x4x4_16b_bf16 v[24:27], a[216:217], a[234:235], 0\n"
      "ds_write_addtid_b32 v44 offset:20484\n"
      "ds_write_addtid_b32 v46 offset:20996\n"
      "v_mfma_f32_16x16x32_bf16 v[92:95], v[120:123], a[200:203], v[92:95]\n"
      "v_cvt_pk_bf16_f32 v32, v32, v33\n"
      "v_cvt_pk_bf16_f32 v34, v34, v35\n"
      "v_cvt_pk_bf16_f32 v20, v20, v24\n"
      "v_mfma_f32_16x16x32_bf16 v[96:99], v[112:115], a[200:203], v[96:99]\n"
      "ds_write_addtid_b32 v32 offset:22532\n"
      "ds_write_addtid_b32 v34 offset:23044\n"
      "v_mfma_f32_16x16x32_bf16 v[76:79], v[108:111], a[204:207], v[76:79]\n"
      "ds_read_b128 a[196:199], v2 offset:16512\n"
      "ds_write_addtid_b32 v20 offset:26368\n"
      "v_mfma_f32_16x16x32_bf16 v[80:83], v[100:103], a[204:207], v[80:83]\n"
      "ds_read_b64_tr_b16 a[220:221], v7 offset:35840\n"
      "ds_read_b64_tr_b16 a[222:223], v7 offset:35848\n"
      "v_fmac_f32 v36, s46, v72\n"
      "v_cvt_pk_bf16_f32 v21, v21, v25\n"
      "v_mfma_f32_16x16x32_bf16 v[84:87], a[160:163], a[204:207], v[84:87]\n"
      "ds_write_addtid_b32 v21 offset:30464\n"
      "v_fmac_f32 v37, s46, v73\n"
      "v_mfma_f32_16x16x32_bf16 v[88:91], v[104:107], a[208:211], v[88:91]\n"
      "ds_read_b128 a[200:203], v2 offset:20608\n"
      "v_fmac_f32 v38, s46, v74\n"
      "v_mfma_f32_16x16x32_bf16 v[92:95], a[164:167], a[208:211], v[92:95]\n"
      "s_nop 0\n"
      "v_cvt_pk_bf16_f32 v22, v22, v26\n"
      "v_fmac_f32 v39, s46, v75\n"
      "v_mfma_f32_16x16x32_bf16 v[96:99], a[156:159], a[208:211], v[96:99]\n"
      "s_waitcnt lgkmcnt(4)\n"
      "ds_write_addtid_b32 v22 offset:34560\n"
      "v_sub_f32 v36, v36, v48\n"
      "v_mfma_f32_16x16x32_bf16 v[76:79], a[152:155], a[212:215], v[76:79]\n"
      "s_nop 0\n"
      "ds_read_b128 a[204:207], v3 offset:33792\n"
      "v_cvt_pk_bf16_f32 v23, v23, v27\n"
      "v_mfma_f32_16x16x32_bf16 v[80:83], a[144:147], a[212:215], v[80:83]\n"
      "ds_write_addtid_b32 v23 offset:38656\n"
      "v_sub_f32 v37, v37, v49\n"
      "v_mfma_f32_16x16x32_bf16 v[84:87], a[136:139], a[212:215], v[84:87]\n"
      "s_waitcnt lgkmcnt(3)\n"
      "v_sub_f32 v38, v38, v50\n"
      "v_sub_f32 v39, v39, v51\n"
      "v_mfma_f32_4x4x4_16b_bf16 v[12:15], a[216:217], a[220:221], 0\n"
      "v_mfma_f32_4x4x4_16b_bf16 v[16:19], a[216:217], a[222:223], 0\n"
      "v_mfma_f32_16x16x32_bf16 v[88:91], a[148:151], a[192:195], v[88:91]\n"
      "ds_read_b128 a[208:211], v3 offset:33856\n"
      "v_mfma_f32_16x16x32_bf16 v[92:95], a[140:143], a[192:195], v[92:95]\n"
      "v_fmac_f32 v48, s45, v36\n"
      "v_fmac_f32 v49, s45, v37\n"
      "v_cvt_pk_bf16_f32 v12, v12, v16\n"
      "v_cvt_pk_bf16_f32 v13, v13, v17\n"
      "v_mfma_f32_16x16x32_bf16 v[96:99], a[132:135], a[192:195], v[96:99]\n"
      "ds_write_addtid_b32 v12 offset:26880\n"
      "ds_write_addtid_b32 v13 offset:30976\n"
      "v_fmac_f32 v50, s45, v38\n"
      "v_mfma_f32_16x16x32_bf16 v[76:79], a[128:131], a[196:199], v[76:79]\n"
      "ds_read_b128 a[212:215], v3 offset:33920\n"
      "ds_read_b64_tr_b16 a[224:225], v7 offset:36352\n"
      "ds_read_b64_tr_b16 a[226:227], v7 offset:36360\n"
      "v_fmac_f32 v51, s45, v39\n"
      "v_mfma_f32_16x16x32_bf16 v[80:83], a[120:123], a[196:199], v[80:83]\n"
      "v_fma_f32 v36, s47, v36, v60\n"
      "v_cvt_pk_bf16_f32 v14, v14, v18\n"
      "v_mfma_f32_16x16x32_bf16 v[84:87], a[112:115], a[196:199], v[84:87]\n"
      "ds_write_addtid_b32 v14 offset:35072\n"
      "v_fma_f32 v37, s47, v37, v61\n"
      "v_mfma_f32_16x16x32_bf16 v[88:91], a[124:127], a[200:203], v[88:91]\n"
      "ds_read_b128 a[192:195], v4 offset:33792\n"
      "v_fma_f32 v38, s47, v38, v62\n"
      "v_mfma_f32_16x16x32_bf16 v[92:95], a[116:119], a[200:203], v[92:95]\n"
      "v_fma_f32 v39, s47, v39, v63\n"
      "v_cvt_pk_bf16_f32 v15, v15, v19\n"
      "v_mfma_f32_16x16x32_bf16 v[96:99], a[108:111], a[200:203], v[96:99]\n"
      "s_waitcnt lgkmcnt(2)\n"
      "ds_write_addtid_b32 v15 offset:39168\n"
      "v_sub_f32 v76, v88, v76\n"
      "v_sub_f32 v77, v89, v77\n"
      "v_cvt_pk_bf16_f32 v48, v48, v49\n"
      "v_mfma_f32_16x16x32_bf16 v[64:67], a[0:3], a[204:207], 0\n"
      "s_nop 0\n"
      "ds_read_b128 a[196:199], v4 offset:33856\n"
      "ds_write_addtid_b32 v48 offset:16384\n"
      "ds_write_addtid_b32 v76 offset:6144\n"
      "v_mfma_f32_16x16x32_bf16 v[68:71], a[4:7], a[204:207], 0\n"
      "ds_write_addtid_b32 v77 offset:6656\n"
      "v_sub_f32 v78, v90, v78\n"
      "v_cvt_pk_bf16_f32 v50, v50, v51\n"
      "v_sub_f32 v79, v91, v79\n"
      "v_mfma_f32_4x4x4_16b_bf16 v[20:23], a[216:217], a[224:225], 0\n"
      "v_sub_f32_e64 v80, v92, v80\n"
      "v_mfma_f32_4x4x4_16b_bf16 v[24:27], a[216:217], a[226:227], 0\n"
      "ds_write_addtid_b32 v50 offset:16896\n"
      "ds_write_addtid_b32 v78 offset:7168\n"
      "ds_write_addtid_b32 v79 offset:7680\n"
      "v_mfma_f32_16x16x32_bf16 v[72:75], a[8:11], a[204:207], 0\n"
      "ds_write_addtid_b32 v80 offset:8192\n"
      "v_cvt_pk_bf16_f32 v20, v20, v24\n"
      "v_mfma_f32_16x16x32_bf16 v[64:67], a[12:15], a[208:211], v[64:67]\n"
      "ds_read_b128 a[200:203], v4 offset:33920\n"
      "ds_write_addtid_b32 v20 offset:27392\n"
      "v_sub_f32 v81, v93, v81\n"
      "v_mfma_f32_16x16x32_bf16 v[68:71], a[16:19], a[208:211], v[68:71]\n"
      "ds_write_addtid_b32 v81 offset:8704\n"
      "v_sub_f32 v82, v94, v82\n"
      "v_mfma_f32_16x16x32_bf16 v[72:75], a[20:23], a[208:211], v[72:75]\n"
      "ds_write_addtid_b32 v82 offset:9216\n"
      "v_cvt_pk_bf16_f32 v36, v36, v37\n"
      "v_sub_f32 v83, v95, v83\n"
      "v_mfma_f32_16x16x32_bf16 v[64:67], a[24:27], a[212:215], v[64:67]\n"
      "ds_read_b128 a[204:207], v5 offset:33792\n"
      "ds_write_addtid_b32 v36 offset:18432\n"
      "v_mfma_f32_16x16x32_bf16 v[68:71], a[28:31], a[212:215], v[68:71]\n"
      "ds_write_addtid_b32 v83 offset:9728\n"
      "v_sub_f32_e64 v84, v96, v84\n"
      "v_mfma_f32_16x16x32_bf16 v[72:75], a[32:35], a[212:215], v[72:75]\n"
      "s_waitcnt lgkmcnt(5)\n"
      "ds_write_addtid_b32 v84 offset:10240\n"
      "v_cvt_pk_bf16_f32 v21, v21, v25\n"
      "v_cvt_pk_bf16_f32 v38, v38, v39\n"
      "v_mfma_f32_16x16x32_bf16 v[64:67], a[36:39], a[192:195], v[64:67]\n"
      "ds_read_b128 a[208:211], v5 offset:33856\n"
      "ds_write_addtid_b32 v38 offset:18944\n"
      "v_mfma_f32_16x16x32_bf16 v[68:71], a[40:43], a[192:195], v[68:71]\n"
      "ds_write_addtid_b32 v21 offset:31488\n"
      "v_sub_f32 v85, v97, v85\n"
      "v_sub_f32 v86, v98, v86\n"
      "v_mfma_f32_16x16x32_bf16 v[72:75], a[44:47], a[192:195], v[72:75]\n"
      "ds_write_addtid_b32 v85 offset:10752\n"
      "ds_write_addtid_b32 v86 offset:11264\n"
      "v_sub_f32 v87, v99, v87\n"
      "v_mfma_f32_16x16x32_bf16 v[64:67], a[48:51], a[196:199], v[64:67]\n"
      "s_nop 0\n"
      "ds_read_b128 a[212:215], v5 offset:33920\n"
      "ds_write_addtid_b32 v87 offset:11776\n"
      "v_mfma_f32_16x16x32_bf16 v[68:71], a[52:55], a[196:199], v[68:71]\n"
      "v_cvt_pk_bf16_f32 v22, v22, v26\n"
      "v_cvt_pk_bf16_f32 v23, v23, v27\n"
      "v_mfma_f32_16x16x32_bf16 v[72:75], a[56:59], a[196:199], v[72:75]\n"
      "ds_write_addtid_b32 v22 offset:35584\n"
      "ds_write_addtid_b32 v23 offset:39680\n"
      "v_fma_f32 v40, 0.5, v76, -v88\n"
      "v_mfma_f32_16x16x32_bf16 v[64:67], a[60:63], a[200:203], v[64:67]\n"
      "v_fma_f32 v41, 0.5, v77, -v89\n"
      "v_fma_f32 v42, 0.5, v78, -v90\n"
      "v_mfma_f32_16x16x32_bf16 v[68:71], a[64:67], a[200:203], v[68:71]\n"
      "v_fma_f32 v43, 0.5, v79, -v91\n"
      "v_fma_f32 v44, 0.5, v80, -v92\n"
      "v_mfma_f32_16x16x32_bf16 v[72:75], a[68:71], a[200:203], v[72:75]\n"
      "s_waitcnt lgkmcnt(0)\n"
      "v_fma_f32 v45, 0.5, v81, -v93\n"
      "s_barrier\n"
      "LSmid_%=:\n"
      "v_mfma_f32_16x16x32_bf16 v[64:67], a[72:75], a[204:207], v[64:67]\n"
      "ds_read_b128 a[192:195], v0 offset:0\n"
      "ds_read2st64_b32 v[28:29], v6 offset0:24 offset1:26\n"
      "v_fma_f32 v46, 0.5, v82, -v94\n"
      "v_mfma_f32_16x16x32_bf16 v[68:71], a[76:79], a[204:207], v[68:71]\n"
      "ds_read_b128 a[196:199], v0 offset:4096\n"
      "ds_read2st64_b32 v[30:31], v6 offset0:28 offset1:30\n"
      "v_mfma_f32_16x16x32_bf16 v[72:75], a[80:83], a[204:207], v[72:75]\n"
      "v_fma_f32 v47, 0.5, v83, -v95\n"
      "v_fma_f32 v48, 0.5, v84, -v96\n"
      "v_mfma_f32_16x16x32_bf16 v[64:67], a[84:87], a[208:211], v[64:67]\n"
      "ds_read_b128 a[200:203], v0 offset:64\n"
      "ds_read_b64_tr_b16 a[220:221], v7 offset:50688\n"
      "v_mfma_f32_16x16x32_bf16 v[68:71], a[88:91], a[208:211], v[68:71]\n"
      "ds_read_b64_tr_b16 a[222:223], v7 offset:50696\n"
      "ds_read2st64_b32 v[32:33], v6 offset0:32 offset1:34\n"
      "v_mfma_f32_16x16x32_bf16 v[72:75], a[92:95], a[208:211], v[72:75]\n"
      "ds_read2st64_b32 v[34:35], v6 offset0:36 offset1:38\n"
      "v_fma_f32 v49, 0.5, v85, -v97\n"
      "v_mfma_f32_16x16x32_bf16 v[64:67], a[96:99], a[212:215], v[64:67]\n"
      "ds_read_b128 a[204:207], v0 offset:4160\n"
      "ds_read2st64_b32 v[36:37], v6 offset0:40 offset1:42\n"
      "v_mfma_f32_16x16x32_bf16 v[68:71], a[100:103], a[212:215], v[68:71]\n"
      "v_fma_f32 v50, 0.5, v86, -v98\n"
      "v_fma_f32 v51, 0.5, v87, -v99\n"
      "v_mfma_f32_16x16x32_bf16 v[72:75], a[104:107], a[212:215], v[72:75]\n"
      "s_waitcnt lgkmcnt(8)\n"
      "s_nop 0\n"
      "v_mfma_f32_16x16x32_bf16 v[76:79], v[252:255], a[192:195], v[64:67]\n"
      "s_waitcnt lgkmcnt(4)\n"
      "ds_read_b128 a[208:211], v0 offset:128\n"
      "ds_read2st64_b32 v[38:39], v6 offset0:44 offset1:46\n"
      "v_fmac_f32 v28, s46, v52\n"
      "v_fmac_f32 v29, s46, v53\n"
      "v_fmac_f32 v30, s46, v54\n"
      "v_mfma_f32_4x4x4_16b_bf16 v[12:15], a[216:217], a[220:221], 0\n"
      "v_fmac_f32 v31, s46, v55\n"
      "v_mfma_f32_4x4x4_16b_bf16 v[16:19], a[216:217], a[222:223], 0\n"
      "v_sub_f32 v28, v28, v40\n"
      "v_sub_f32 v29, v29, v41\n"
      "v_mfma_f32_16x16x32_bf16 v[80:83], v[244:247], a[192:195], v[68:71]\n"
      "v_sub_f32 v30, v30, v42\n"
      "v_sub_f32 v31, v31, v43\n"
      "v_mfma_f32_16x16x32_bf16 v[84:87], v[236:239], a[192:195], v[72:75]\n"
      "v_fmac_f32 v40, s45, v28\n"
      "v_cvt_pk_bf16_f32 v12, v12, v16\n"
      "v_mfma_f32_16x16x32_bf16 v[88:91], v[248:251], a[196:199], v[64:67]\n"
      "ds_read_b128 a[212:215], v0 offset:4224\n"
      "ds_write_addtid_b32 v12 offset:41216\n"
      "v_fmac_f32 v41, s45, v29\n"
      "v_mfma_f32_16x16x32_bf16 v[92:95], v[240:243], a[196:199], v[68:71]\n"
      "ds_read_b64_tr_b16 a[224:225], v7 offset:51200\n"
      "ds_read_b64_tr_b16 a[226:227], v7 offset:51208\n"
      "v_mfma_f32_16x16x32_bf16 v[96:99], v[232:235], a[196:199], v[72:75]\n"
      "v_fmac_f32 v42, s45, v30\n"
      "v_cvt_pk_bf16_f32 v13, v13, v17\n"
      "v_mfma_f32_16x16x32_bf16 v[76:79], v[228:231], a[200:203], v[76:79]\n"
      "s_nop 0\n"
      "ds_read_b128 a[192:195], v1 offset:0\n"
      "ds_write_addtid_b32 v13 offset:45312\n"
      "v_mfma_f32_16x16x32_bf16 v[80:83], v[220:223], a[200:203], v[80:83]\n"
      "s_nop 0\n"
      "v_fmac_f32_e64 v43, s45, v31\n"
      "v_fma_f32 v28, s47, v28, v64\n"
      "v_mfma_f32_16x16x32_bf16 v[84:87], v[212:215], a[200:203], v[84:87]\n"
      "s_waitcnt lgkmcnt(4)\n"
      "v_cvt_pk_bf16_f32 v14, v14, v18\n"
      "v_fma_f32 v29, s47, v29, v65\n"
      "v_mfma_f32_16x16x32_bf16 v[88:91], v[224:227], a[204:207], v[88:91]\n"
      "s_nop 0\n"
      "ds_read_b128 a[196:199], v1 offset:4096\n"
      "ds_write_addtid_b32 v14 offset:49408\n"
      "v_mfma_f32_16x16x32_bf16 v[92:95], v[216:219], a[204:207], v[92:95]\n"
      "s_nop 0\n"
      "v_fma_f32 v30, s47, v30, v66\n"
      "v_cvt_pk_bf16_f32 v15, v15, v19\n"
      "v_mfma_f32_16x16x32_bf16 v[96:99], v[208:211], a[204:207], v[96:99]\n"
      "s_waitcnt lgkmcnt(2)\n"
      "ds_write_addtid_b32 v15 offset:53504\n"
      "v_fma_f32 v31, s47, v31, v67\n"
      "v_mfma_f32_4x4x4_16b_bf16 v[20:23], a[216:217], a[224:225], 0\n"
      "v_cvt_pk_bf16_f32 v40, v40, v41\n"
      "v_mfma_f32_4x4x4_16b_bf16 v[24:27], a[216:217], a[226:227], 0\n"
      "ds_write_addtid_b32 v40 offset:13312\n"
      "v_cvt_pk_bf16_f32 v42, v42, v43\n"
      "v_mfma_f32_16x16x32_bf16 v[76:79], v[204:207], a[208:211], v[76:79]\n"
      "ds_read_b128 a[200:203], v1 offset:64\n"
      "ds_write_addtid_b32 v42 offset:13824\n"
      "v_cvt_pk_bf16_f32 v20, v20, v24\n"
      "v_mfma_f32_16x16x32_bf16 v[80:83], v[196:199], a[208:211], v[80:83]\n"
      "ds_write_addtid_b32 v20 offset:41728\n"
      "v_cvt_pk_bf16_f32 v28, v28, v29\n"
      "v_mfma_f32_16x16x32_bf16 v[84:87], v[188:191], a[208:211], v[84:87]\n"
      "ds_write_addtid_b32 v28 offset:15360\n"
      "v_cvt_pk_bf16_f32 v30, v30, v31\n"
      "v_mfma_f32_16x16x32_bf16 v[88:91], v[200:203], a[212:215], v[88:91]\n"
      "ds_read_b128 a[204:207], v1 offset:4160\n"
      "ds_write_addtid_b32 v30 offset:15872\n"
      "v_mfma_f32_16x16x32_bf16 v[92:95], v[192:195], a[212:215], v[92:95]\n"
      "ds_read_b64_tr_b16 a[228:229], v7 offset:51712\n"
      "ds_read_b64_tr_b16 a[230:231], v7 offset:51720\n"
      "v_fmac_f32 v32, s46, v56\n"
      "v_mfma_f32_16x16x32_bf16 v[96:99], v[184:187], a[212:215], v[96:99]\n"
      "v_cvt_pk_bf16_f32 v21, v21, v25\n"
      "v_fmac_f32 v33, s46, v57\n"
      "v_mfma_f32_16x16x32_bf16 v[76:79], v[180:183], a[192:195], v[76:79]\n"
      "ds_read_b128 a[208:211], v1 offset:128\n"
      "ds_write_addtid_b32 v21 offset:45824\n"
      "v_mfma_f32_16x16x32_bf16 v[80:83], v[172:175], a[192:195], v[80:83]\n"
      "v_fmac_f32 v34, s46, v58\n"
      "v_cvt_pk_bf16_f32 v22, v22, v26\n"
      "v_mfma_f32_16x16x32_bf16 v[84:87], v[164:167], a[192:195], v[84:87]\n"
      "s_waitcnt lgkmcnt(3)\n"
      "ds_write_addtid_b32 v22 offset:49920\n"
      "v_fmac_f32 v35, s46, v59\n"
      "v_mfma_f32_16x16x32_bf16 v[88:91], v[176:179], a[196:199], v[88:91]\n"
      "s_nop 0\n"
      "ds_read_b128 a[212:215], v1 offset:4224\n"
      "v_sub_f32 v32, v32, v44\n"
      "v_mfma_f32_16x16x32_bf16 v[92:95], v[168:171], a[196:199], v[92:95]\n"
      "s_waitcnt lgkmcnt(4)\n"
      "v_sub_f32 v33, v33, v45\n"
      "v_cvt_pk_bf16_f32 v23, v23, v27\n"
      "v_mfma_f32_4x4x4_16b_bf16 v[12:15], a[216:217], a[228:229], 0\n"
      "v_sub_f32 v34, v34, v46\n"
      "v_mfma_f32_4x4x4_16b_bf16 v[16:19], a[216:217], a[230:231], 0\n"
      "ds_write_addtid_b32 v23 offset:54016\n"
      "v_sub_f32 v35, v35, v47\n"
      "v_mfma_f32_16x16x32_bf16 v[96:99], v[160:163], a[196:199], v[96:99]\n"
      "v_fmac_f32 v44, s45, v32\n"
      "v_fmac_f32 v45, s45, v33\n"
      "v_cvt_pk_bf16_f32 v12, v12, v16\n"
      "v_mfma_f32_16x16x32_bf16 v[76:79], v[156:159], a[200:203], v[76:79]\n"
      "ds_read_b128 a[192:195], v2 offset:0\n"
      "ds_write_addtid_b32 v12 offset:42240\n"
      "v_mfma_f32_16x16x32_bf16 v[80:83], v[148:151], a[200:203], v[80:83]\n"
      "s_nop 0\n"
      "v_cvt_pk_bf16_f32 v13, v13, v17\n"
      "v_fmac_f32_e64 v46, s45, v34\n"
      "v_mfma_f32_16x16x32_bf16 v[84:87], v[140:143], a[200:203], v[84:87]\n"
      "s_nop 0\n"
      "ds_read_b64_tr_b16 a[232:233], v7 offset:52224\n"
      "ds_write_addtid_b32 v13 offset:46336\n"
      "v_mfma_f32_16x16x32_bf16 v[88:91], v[152:155], a[204:207], v[88:91]\n"
      "s_nop 0\n"
      "ds_read_b128 a[196:199], v2 offset:4096\n"
      "ds_read_b64_tr_b16 a[234:235], v7 offset:52232\n"
      "v_mfma_f32_16x16x32_bf16 v[92:95], v[144:147], a[204:207], v[92:95]\n"
      "s_nop 0\n"
      "v_fmac_f32_e64 v47, s45, v35\n"
      "v_fma_f32 v32, s47, v32, v68\n"
      "v_mfma_f32_16x16x32_bf16 v[96:99], v[136:139], a[204:207], v[96:99]\n"
      "s_waitcnt lgkmcnt(4)\n"
      "v_cvt_pk_bf16_f32 v14, v14, v18\n"
      "v_fma_f32 v33, s47, v33, v69\n"
      "v_mfma_f32_16x16x32_bf16 v[76:79], v[132:135], a[208:211], v[76:79]\n"
      "s_nop 0\n"
      "ds_read_b128 a[200:203], v2 offset:64\n"
      "ds_write_addtid_b32 v14 offset:50432\n"
      "v_mfma_f32_16x16x32_bf16 v[80:83], v[124:127], a[208:211], v[80:83]\n"
      "s_nop 0\n"
      "v_fma_f32 v34, s47, v34, v70\n"
      "v_cvt_pk_bf16_f32 v15, v15, v19\n"
      "v_mfma_f32_16x16x32_bf16 v[84:87], v[116:119], a[208:211], v[84:87]\n"
      "s_nop 0\n"
      "ds_write_addtid_b32 v15 offset:54528\n"
      "v_fma_f32 v35, s47, v35, v71\n"
      "v_mfma_f32_16x16x32_bf16 v[88:91], v[128:131], a[212:215], v[88:91]\n"
      "s_waitcnt lgkmcnt(3)\n"
      "ds_read_b128 a[204:207], v2 offset:4160\n"
      "v_cvt_pk_bf16_f32 v44, v44, v45\n"
      "v_mfma_f32_4x4x4_16b_bf16 v[20:23], a[216:217], a[232:233], 0\n"
      "v_cvt_pk_bf16_f32 v46, v46, v47\n"
      "v_mfma_f32_4x4x4_16b_bf16 v[24:27], a[216:217], a[234:235], 0\n"
      "ds_write_addtid_b32 v44 offset:21508\n"
      "ds_write_addtid_b32 v46 offset:22020\n"
      "v_mfma_f32_16x16x32_bf16 v[92:95], v[120:123], a[212:215], v[92:95]\n"
      "v_cvt_pk_bf16_f32 v32, v32, v33\n"
      "v_cvt_pk_bf16_f32 v34, v34, v35\n"
      "v_cvt_pk_bf16_f32 v20, v20, v24\n"
      "v_mfma_f32_16x16x32_bf16 v[96:99], v[112:115], a[212:215], v[96:99]\n"
      "ds_write_addtid_b32 v32 offset:23556\n"
      "ds_write_addtid_b32 v34 offset:24068\n"
      "v_mfma_f32_16x16x32_bf16 v[76:79], v[108:111], a[192:195], v[76:79]\n"
      "ds_read_b128 a[208:211], v2 offset:128\n"
      "ds_write_addtid_b32 v20 offset:42752\n"
      "v_mfma_f32_16x16x32_bf16 v[80:83], v[100:103], a[192:195], v[80:83]\n"
      "ds_read_b64_tr_b16 a[220:221], v7 offset:52736\n"
      "ds_read_b64_tr_b16 a[222:223], v7 offset:52744\n"
      "v_fmac_f32 v36, s46, v60\n"
      "v_cvt_pk_bf16_f32 v21, v21, v25\n"
      "v_mfma_f32_16x16x32_bf16 v[84:87], a[160:163], a[192:195], v[84:87]\n"
      "ds_write_addtid_b32 v21 offset:46848\n"
      "v_fmac_f32 v37, s46, v61\n"
      "v_mfma_f32_16x16x32_bf16 v[88:91], v[104:107], a[196:199], v[88:91]\n"
      "ds_read_b128 a[212:215], v2 offset:4224\n"
      "v_fmac_f32 v38, s46, v62\n"
      "v_mfma_f32_16x16x32_bf16 v[92:95], a[164:167], a[196:199], v[92:95]\n"
      "s_nop 0\n"
      "v_cvt_pk_bf16_f32 v22, v22, v26\n"
      "v_fmac_f32 v39, s46, v63\n"
      "v_mfma_f32_16x16x32_bf16 v[96:99], a[156:159], a[196:199], v[96:99]\n"
      "s_waitcnt lgkmcnt(4)\n"
      "ds_write_addtid_b32 v22 offset:50944\n"
      "v_sub_f32 v36, v36, v48\n"
      "v_mfma_f32_16x16x32_bf16 v[76:79], a[152:155], a[200:203], v[76:79]\n"
      "s_nop 0\n"
      "ds_read_b128 a[192:195], v3 offset:50688\n"
      "v_cvt_pk_bf16_f32 v23, v23, v27\n"
      "v_mfma_f32_16x16x32_bf16 v[80:83], a[144:147], a[200:203], v[80:83]\n"
      "ds_write_addtid_b32 v23 offset:55040\n"
      "v_sub_f32 v37, v37, v49\n"
      "v_mfma_f32_16x16x32_bf16 v[84:87], a[136:139], a[200:203], v[84:87]\n"
      "s_waitcnt lgkmcnt(3)\n"
      "v_sub_f32 v38, v38, v50\n"
      "v_sub_f32 v39, v39, v51\n"
      "v_mfma_f32_4x4x4_16b_bf16 v[12:15], a[216:217], a[220:221], 0\n"
      "v_mfma_f32_4x4x4_16b_bf16 v[16:19], a[216:217], a[222:223], 0\n"
      "v_mfma_f32_16x16x32_bf16 v[88:91], a[148:151], a[204:207], v[88:91]\n"
      "ds_read_b128 a[196:199], v3 offset:50752\n"
      "v_mfma_f32_16x16x32_bf16 v[92:95], a[140:143], a[204:207], v[92:95]\n"
      "v_fmac_f32 v48, s45, v36\n"
      "v_fmac_f32 v49, s45, v37\n"
      "v_cvt_pk_bf16_f32 v12, v12, v16\n"
      "v_cvt_pk_bf16_f32 v13, v13, v17\n"
      "v_mfma_f32_16x16x32_bf16 v[96:99], a[132:135], a[204:207], v[96:99]\n"
      "ds_write_addtid_b32 v12 offset:43264\n"
      "ds_write_addtid_b32 v13 offset:47360\n"
      "v_fmac_f32 v50, s45, v38\n"
      "v_mfma_f32_16x16x32_bf16 v[76:79], a[128:131], a[208:211], v[76:79]\n"
      "ds_read_b128 a[200:203], v3 offset:50816\n"
      "ds_read_b64_tr_b16 a[224:225], v7 offset:53248\n"
      "ds_read_b64_tr_b16 a[226:227], v7 offset:53256\n"
      "v_fmac_f32 v51, s45, v39\n"
      "v_mfma_f32_16x16x32_bf16 v[80:83], a[120:123], a[208:211], v[80:83]\n"
      "v_fma_f32 v36, s47, v36, v72\n"
      "v_cvt_pk_bf16_f32 v14, v14, v18\n"
      "v_mfma_f32_16x16x32_bf16 v[84:87], a[112:115], a[208:211], v[84:87]\n"
      "ds_write_addtid_b32 v14 offset:51456\n"
      "v_fma_f32 v37, s47, v37, v73\n"
      "v_mfma_f32_16x16x32_bf16 v[88:91], a[124:127], a[212:215], v[88:91]\n"
      "ds_read_b128 a[204:207], v4 offset:50688\n"
      "v_fma_f32 v38, s47, v38, v74\n"
      "v_mfma_f32_16x16x32_bf16 v[92:95], a[116:119], a[212:215], v[92:95]\n"
      "v_fma_f32 v39, s47, v39, v75\n"
      "v_cvt_pk_bf16_f32 v15, v15, v19\n"
      "v_mfma_f32_16x16x32_bf16 v[96:99], a[108:111], a[212:215], v[96:99]\n"
      "s_waitcnt lgkmcnt(2)\n"
      "ds_write_addtid_b32 v15 offset:55552\n"
      "v_sub_f32 v76, v88, v76\n"
      "v_sub_f32 v77, v89, v77\n"
      "v_cvt_pk_bf16_f32 v48, v48, v49\n"
      "v_mfma_f32_16x16x32_bf16 v[52:55], a[0:3], a[192:195], 0\n"
      "s_nop 0\n"
      "ds_read_b128 a[208:211], v4 offset:50752\n"
      "ds_write_addtid_b32 v48 offset:17408\n"
      "ds_write_addtid_b32 v76 offset:0\n"
      "v_mfma_f32_16x16x32_bf16 v[56:59], a[4:7], a[192:195], 0\n"
      "ds_write_addtid_b32 v77 offset:512\n"
      "v_sub_f32 v78, v90, v78\n"
      "v_cvt_pk_bf16_f32 v50, v50, v51\n"
      "v_sub_f32 v79, v91, v79\n"
      "v_mfma_f32_4x4x4_16b_bf16 v[20:23], a[216:217], a[224:225], 0\n"
      "v_sub_f32_e64 v80, v92, v80\n"
      "v_mfma_f32_4x4x4_16b_bf16 v[24:27], a[216:217], a[226:227], 0\n"
      "ds_write_addtid_b32 v50 offset:17920\n"
      "ds_write_addtid_b32 v78 offset:1024\n"
      "ds_write_addtid_b32 v79 offset:1536\n"
      "v_mfma_f32_16x16x32_bf16 v[60:63], a[8:11], a[192:195], 0\n"
      "ds_write_addtid_b32 v80 offset:2048\n"
      "v_cvt_pk_bf16_f32 v20, v20, v24\n"
      "v_mfma_f32_16x16x32_bf16 v[52:55], a[12:15], a[196:199], v[52:55]\n"
      "ds_read_b128 a[212:215], v4 offset:50816\n"
      "ds_write_addtid_b32 v20 offset:43776\n"
      "v_sub_f32 v81, v93, v81\n"
      "v_mfma_f32_16x16x32_bf16 v[56:59], a[16:19], a[196:199], v[56:59]\n"
      "ds_write_addtid_b32 v81 offset:2560\n"
      "v_sub_f32 v82, v94, v82\n"
      "v_mfma_f32_16x16x32_bf16 v[60:63], a[20:23], a[196:199], v[60:63]\n"
      "ds_write_addtid_b32 v82 offset:3072\n"
      "v_cvt_pk_bf16_f32 v36, v36, v37\n"
      "v_sub_f32 v83, v95, v83\n"
      "v_mfma_f32_16x16x32_bf16 v[52:55], a[24:27], a[200:203], v[52:55]\n"
      "ds_read_b128 a[192:195], v5 offset:50688\n"
      "ds_write_addtid_b32 v36 offset:19456\n"
      "v_mfma_f32_16x16x32_bf16 v[56:59], a[28:31], a[200:203], v[56:59]\n"
      "ds_write_addtid_b32 v83 offset:3584\n"
      "v_sub_f32_e64 v84, v96, v84\n"
      "v_mfma_f32_16x16x32_bf16 v[60:63], a[32:35], a[200:203], v[60:63]\n"
      "s_waitcnt lgkmcnt(5)\n"
      "ds_write_addtid_b32 v84 offset:4096\n"
      "v_cvt_pk_bf16_f32 v21, v21, v25\n"
      "v_cvt_pk_bf16_f32 v38, v38, v39\n"
      "v_mfma_f32_16x16x32_bf16 v[52:55], a[36:39], a[204:207], v[52:55]\n"
      "ds_read_b128 a[196:199], v5 offset:50752\n"
      "ds_write_addtid_b32 v38 offset:19968\n"
      "v_mfma_f32_16x16x32_bf16 v[56:59], a[40:43], a[204:207], v[56:59]\n"
      "ds_write_addtid_b32 v21 offset:47872\n"
      "v_sub_f32 v85, v97, v85\n"
      "v_sub_f32 v86, v98, v86\n"
      "v_mfma_f32_16x16x32_bf16 v[60:63], a[44:47], a[204:207], v[60:63]\n"
      "ds_write_addtid_b32 v85 offset:4608\n"
      "ds_write_addtid_b32 v86 offset:5120\n"
      "v_sub_f32 v87, v99, v87\n"
      "v_mfma_f32_16x16x32_bf16 v[52:55], a[48:51], a[208:211], v[52:55]\n"
      "s_nop 0\n"
      "ds_read_b128 a[200:203], v5 offset:50816\n"
      "ds_write_addtid_b32 v87 offset:5632\n"
      "v_mfma_f32_16x16x32_bf16 v[56:59], a[52:55], a[208:211], v[56:59]\n"
      "v_cvt_pk_bf16_f32 v22, v22, v26\n"
      "v_cvt_pk_bf16_f32 v23, v23, v27\n"
      "v_mfma_f32_16x16x32_bf16 v[60:63], a[56:59], a[208:211], v[60:63]\n"
      "ds_write_addtid_b32 v22 offset:51968\n"
      "ds_write_addtid_b32 v23 offset:56064\n"
      "v_fma_f32 v40, 0.5, v76, -v88\n"
      "v_mfma_f32_16x16x32_bf16 v[52:55], a[60:63], a[212:215], v[52:55]\n"
      "v_fma_f32 v41, 0.5, v77, -v89\n"
      "v_fma_f32 v42, 0.5, v78, -v90\n"
      "v_mfma_f32_16x16x32_bf16 v[56:59], a[64:67], a[212:215], v[56:59]\n"
      "v_fma_f32 v43, 0.5, v79, -v91\n"
      "v_fma_f32 v44, 0.5, v80, -v92\n"
      "v_mfma_f32_16x16x32_bf16 v[60:63], a[68:71], a[212:215], v[60:63]\n"
      "s_waitcnt lgkmcnt(0)\n"
      "v_fma_f32 v45, 0.5, v81, -v93\n"
      "s_barrier\n"
      "v_mfma_f32_16x16x32_bf16 v[52:55], a[72:75], a[192:195], v[52:55]\n"
      "ds_read_b128 a[204:207], v0 offset:16384\n"
      "ds_read2st64_b32 v[28:29], v6 offset0:0 offset1:2\n"
      "v_fma_f32 v46, 0.5, v82, -v94\n"
      "v_mfma_f32_16x16x32_bf16 v[56:59], a[76:79], a[192:195], v[56:59]\n"
      "ds_read_b128 a[208:211], v0 offset:20480\n"
      "ds_read2st64_b32 v[30:31], v6 offset0:4 offset1:6\n"
      "v_mfma_f32_16x16x32_bf16 v[60:63], a[80:83], a[192:195], v[60:63]\n"
      "v_fma_f32 v47, 0.5, v83, -v95\n"
      "v_fma_f32 v48, 0.5, v84, -v96\n"
      "v_mfma_f32_16x16x32_bf16 v[52:55], a[84:87], a[196:199], v[52:55]\n"
      "ds_read_b128 a[212:215], v0 offset:16448\n"
      "ds_read_b64_tr_b16 a[220:221], v7 offset:0\n"
      "v_mfma_f32_16x16x32_bf16 v[56:59], a[88:91], a[196:199], v[56:59]\n"
      "ds_read_b64_tr_b16 a[222:223], v7 offset:8\n"
      "ds_read2st64_b32 v[32:33], v6 offset0:8 offset1:10\n"
      "v_mfma_f32_16x16x32_bf16 v[60:63], a[92:95], a[196:199], v[60:63]\n"
      "ds_read2st64_b32 v[34:35], v6 offset0:12 offset1:14\n"
      "v_fma_f32 v49, 0.5, v85, -v97\n"
      "v_mfma_f32_16x16x32_bf16 v[52:55], a[96:99], a[200:203], v[52:55]\n"
      "ds_read_b128 a[192:195], v0 offset:20544\n"
      "ds_read2st64_b32 v[36:37], v6 offset0:16 offset1:18\n"
      "v_mfma_f32_16x16x32_bf16 v[56:59], a[100:103], a[200:203], v[56:59]\n"
      "v_fma_f32 v50, 0.5, v86, -v98\n"
      "v_fma_f32 v51, 0.5, v87, -v99\n"
      "v_mfma_f32_16x16x32_bf16 v[60:63], a[104:107], a[200:203], v[60:63]\n"
      "s_waitcnt lgkmcnt(8)\n"
      "s_nop 0\n"
      "v_mfma_f32_16x16x32_bf16 v[76:79], v[252:255], a[204:207], v[52:55]\n"
      "s_waitcnt lgkmcnt(4)\n"
      "ds_read_b128 a[196:199], v0 offset:16512\n"
      "ds_read2st64_b32 v[38:39], v6 offset0:20 offset1:22\n"
      "v_fmac_f32 v28, s46, v64\n"
      "v_fmac_f32 v29, s46, v65\n"
      "v_fmac_f32 v30, s46, v66\n"
      "v_mfma_f32_4x4x4_16b_bf16 v[12:15], a[216:217], a[220:221], 0\n"
      "v_fmac_f32 v31, s46, v67\n"
      "v_mfma_f32_4x4x4_16b_bf16 v[16:19], a[216:217], a[222:223], 0\n"
      "v_sub_f32 v28, v28, v40\n"
      "v_sub_f32 v29, v29, v41\n"
      "v_mfma_f32_16x16x32_bf16 v[80:83], v[244:247], a[204:207], v[56:59]\n"
      "v_sub_f32 v30, v30, v42\n"
      "v_sub_f32 v31, v31, v43\n"
      "v_mfma_f32_16x16x32_bf16 v[84:87], v[236:239], a[204:207], v[60:63]\n"
      "v_fmac_f32 v40, s45, v28\n"
      "v_cvt_pk_bf16_f32 v12, v12, v16\n"
      "v_mfma_f32_16x16x32_bf16 v[88:91], v[248:251], a[208:211], v[52:55]\n"
      "ds_read_b128 a[200:203], v0 offset:20608\n"
      "ds_write_addtid_b32 v12 offset:24832\n"
      "v_fmac_f32 v41, s45, v29\n"
      "v_mfma_f32_16x16x32_bf16 v[92:95], v[240:243], a[208:211], v[56:59]\n"
      "ds_read_b64_tr_b16 a[224:225], v7 offset:512\n"
      "ds_read_b64_tr_b16 a[226:227], v7 offset:520\n"
      "v_mfma_f32_16x16x32_bf16 v[96:99], v[232:235], a[208:211], v[60:63]\n"
      "v_fmac_f32 v42, s45, v30\n"
      "v_cvt_pk_bf16_f32 v13, v13, v17\n"
      "v_mfma_f32_16x16x32_bf16 v[76:79], v[228:231], a[212:215], v[76:79]\n"
      "s_nop 0\n"
      "ds_read_b128 a[204:207], v1 offset:16384\n"
      "ds_write_addtid_b32 v13 offset:28928\n"
      "v_mfma_f32_16x16x32_bf16 v[80:83], v[220:223], a[212:215], v[80:83]\n"
      "s_nop 0\n"
      "v_fmac_f32_e64 v43, s45, v31\n"
      "v_fma_f32 v28, s47, v28, v52\n"
      "v_mfma_f32_16x16x32_bf16 v[84:87], v[212:215], a[212:215], v[84:87]\n"
      "s_waitcnt lgkmcnt(4)\n"
      "v_cvt_pk_bf16_f32 v14, v14, v18\n"
      "v_fma_f32 v29, s47, v29, v53\n"
      "v_mfma_f32_16x16x32_bf16 v[88:91], v[224:227], a[192:195], v[88:91]\n"
      "s_nop 0\n"
      "ds_read_b128 a[208:211], v1 offset:20480\n"
      "ds_write_addtid_b32 v14 offset:33024\n"
      "v_mfma_f32_16x16x32_bf16 v[92:95], v[216:219], a[192:195], v[92:95]\n"
      "s_nop 0\n"
      "v_fma_f32 v30, s47, v30, v54\n"
      "v_cvt_pk_bf16_f32 v15, v15, v19\n"
      "v_mfma_f32_16x16x32_bf16 v[96:99], v[208:211], a[192:195], v[96:99]\n"
      "s_waitcnt lgkmcnt(2)\n"
      "ds_write_addtid_b32 v15 offset:37120\n"
      "v_fma_f32 v31, s47, v31, v55\n"
      "v_mfma_f32_4x4x4_16b_bf16 v[20:23], a[216:217], a[224:225], 0\n"
      "v_cvt_pk_bf16_f32 v40, v40, v41\n"
      "v_mfma_f32_4x4x4_16b_bf16 v[24:27], a[216:217], a[226:227], 0\n"
      "ds_write_addtid_b32 v40 offset:12288\n"
      "v_cvt_pk_bf16_f32 v42, v42, v43\n"
      "v_mfma_f32_16x16x32_bf16 v[76:79], v[204:207], a[196:199], v[76:79]\n"
      "ds_read_b128 a[212:215], v1 offset:16448\n"
      "ds_write_addtid_b32 v42 offset:12800\n"
      "v_cvt_pk_bf16_f32 v20, v20, v24\n"
      "v_mfma_f32_16x16x32_bf16 v[80:83], v[196:199], a[196:199], v[80:83]\n"
      "ds_write_addtid_b32 v20 offset:25344\n"
      "v_cvt_pk_bf16_f32 v28, v28, v29\n"
      "v_mfma_f32_16x16x32_bf16 v[84:87], v[188:191], a[196:199], v[84:87]\n"
      "ds_write_addtid_b32 v28 offset:14336\n"
      "v_cvt_pk_bf16_f32 v30, v30, v31\n"
      "v_mfma_f32_16x16x32_bf16 v[88:91], v[200:203], a[200:203], v[88:91]\n"
      "ds_read_b128 a[192:195], v1 offset:20544\n"
      "ds_write_addtid_b32 v30 offset:14848\n"
      "v_mfma_f32_16x16x32_bf16 v[92:95], v[192:195], a[200:203], v[92:95]\n"
      "ds_read_b64_tr_b16 a[228:229], v7 offset:1024\n"
      "ds_read_b64_tr_b16 a[230:231], v7 offset:1032\n"
      "v_fmac_f32 v32, s46, v68\n"
      "v_mfma_f32_16x16x32_bf16 v[96:99], v[184:187], a[200:203], v[96:99]\n"
      "v_cvt_pk_bf16_f32 v21, v21, v25\n"
      "v_fmac_f32 v33, s46, v69\n"
      "v_mfma_f32_16x16x32_bf16 v[76:79], v[180:183], a[204:207], v[76:79]\n"
      "ds_read_b128 a[196:199], v1 offset:16512\n"
      "ds_write_addtid_b32 v21 offset:29440\n"
      "v_mfma_f32_16x16x32_bf16 v[80:83], v[172:175], a[204:207], v[80:83]\n"
      "v_fmac_f32 v34, s46, v70\n"
      "v_cvt_pk_bf16_f32 v22, v22, v26\n"
      "v_mfma_f32_16x16x32_bf16 v[84:87], v[164:167], a[204:207], v[84:87]\n"
      "s_waitcnt lgkmcnt(3)\n"
      "ds_write_addtid_b32 v22 offset:33536\n"
      "v_fmac_f32 v35, s46, v71\n"
      "v_mfma_f32_16x16x32_bf16 v[88:91], v[176:179], a[208:211], v[88:91]\n"
      "s_nop 0\n"
      "ds_read_b128 a[200:203], v1 offset:20608\n"
      "v_sub_f32 v32, v32, v44\n"
      "v_mfma_f32_16x16x32_bf16 v[92:95], v[168:171], a[208:211], v[92:95]\n"
      "s_waitcnt lgkmcnt(4)\n"
      "v_sub_f32 v33, v33, v45\n"
      "v_cvt_pk_bf16_f32 v23, v23, v27\n"
      "v_mfma_f32_4x4x4_16b_bf16 v[12:15], a[216:217], a[228:229], 0\n"
      "v_sub_f32 v34, v34, v46\n"
      "v_mfma_f32_4x4x4_16b_bf16 v[16:19], a[216:217], a[230:231], 0\n"
      "ds_write_addtid_b32 v23 offset:37632\n"
      "v_sub_f32 v35, v35, v47\n"
      "v_mfma_f32_16x16x32_bf16 v[96:99], v[160:163], a[208:211], v[96:99]\n"
      "v_fmac_f32 v44, s45, v32\n"
      "v_fmac_f32 v45, s45, v33\n"
      "v_cvt_pk_bf16_f32 v12, v12, v16\n"
      "v_mfma_f32_16x16x32_bf16 v[76:79], v[156:159], a[212:215], v[76:79]\n"
      "ds_read_b128 a[204:207], v2 offset:16384\n"
      "ds_write_addtid_b32 v12 offset:25856\n"
      "v_mfma_f32_16x16x32_bf16 v[80:83], v[148:151], a[212:215], v[80:83]\n"
      "s_nop 0\n"
      "v_cvt_pk_bf16_f32 v13, v13, v17\n"
      "v_fmac_f32_e64 v46, s45, v34\n"
      "v_mfma_f32_16x16x32_bf16 v[84:87], v[140:143], a[212:215], v[84:87]\n"
      "s_nop 0\n"
      "ds_read_b64_tr_b16 a[232:233], v7 offset:1536\n"
      "ds_write_addtid_b32 v13 offset:29952\n"
      "v_mfma_f32_16x16x32_bf16 v[88:91], v[152:155], a[192:195], v[88:91]\n"
      "s_nop 0\n"
      "ds_read_b128 a[208:211], v2 offset:20480\n"
      "ds_read_b64_tr_b16 a[234:235], v7 offset:1544\n"
      "v_mfma_f32_16x16x32_bf16 v[92:95], v[144:147], a[192:195], v[92:95]\n"
      "s_nop 0\n"
      "v_fmac_f32_e64 v47, s45, v35\n"
      "v_fma_f32 v32, s47, v32, v56\n"
      "v_mfma_f32_16x16x32_bf16 v[96:99], v[136:139], a[192:195], v[96:99]\n"
      "s_waitcnt lgkmcnt(4)\n"
      "v_cvt_pk_bf16_f32 v14, v14, v18\n"
      "v_fma_f32 v33, s47, v33, v57\n"
      "v_mfma_f32_16x16x32_bf16 v[76:79], v[132:135], a[196:199], v[76:79]\n"
      "s_nop 0\n"
      "ds_read_b128 a[212:215], v2 offset:16448\n"
      "ds_write_addtid_b32 v14 offset:34048\n"
      "v_mfma_f32_16x16x32_bf16 v[80:83], v[124:127], a[196:199], v[80:83]\n"
      "s_nop 0\n"
      "v_fma_f32 v34, s47, v34, v58\n"
      "v_cvt_pk_bf16_f32 v15, v15, v19\n"
      "v_mfma_f32_16x16x32_bf16 v[84:87], v[116:119], a[196:199], v[84:87]\n"
      "s_nop 0\n"
      "ds_write_addtid_b32 v15 offset:38144\n"
      "v_fma_f32 v35, s47, v35, v59\n"
      "v_mfma_f32_16x16x32_bf16 v[88:91], v[128:131], a[200:203], v[88:91]\n"
      "s_waitcnt lgkmcnt(3)\n"
      "ds_read_b128 a[192:195], v2 offset:20544\n"
      "v_cvt_pk_bf16_f32 v44, v44, v45\n"
      "v_mfma_f32_4x4x4_16b_bf16 v[20:23], a[216:217], a[232:233], 0\n"
      "v_cvt_pk_bf16_f32 v46, v46, v47\n"
      "v_mfma_f32_4x4x4_16b_bf16 v[24:27], a[216:217], a[234:235], 0\n"
      "ds_write_addtid_b32 v44 offset:20484\n"
      "ds_write_addtid_b32 v46 offset:20996\n"
      "v_mfma_f32_16x16x32_bf16 v[92:95], v[120:123], a[200:203], v[92:95]\n"
      "v_cvt_pk_bf16_f32 v32, v32, v33\n"
      "v_cvt_pk_bf16_f32 v34, v34, v35\n"
      "v_cvt_pk_bf16_f32 v20, v20, v24\n"
      "v_mfma_f32_16x16x32_bf16 v[96:99], v[112:115], a[200:203], v[96:99]\n"
      "ds_write_addtid_b32 v32 offset:22532\n"
      "ds_write_addtid_b32 v34 offset:23044\n"
      "v_mfma_f32_16x16x32_bf16 v[76:79], v[108:111], a[204:207], v[76:79]\n"
      "ds_read_b128 a[196:199], v2 offset:16512\n"
      "ds_write_addtid_b32 v20 offset:26368\n"
      "v_mfma_f32_16x16x32_bf16 v[80:83], v[100:103], a[204:207], v[80:83]\n"
      "ds_read_b64_tr_b16 a[220:221], v7 offset:2048\n"
      "ds_read_b64_tr_b16 a[222:223], v7 offset:2056\n"
      "v_fmac_f32 v36, s46, v72\n"
      "v_cvt_pk_bf16_f32 v21, v21, v25\n"
      "v_mfma_f32_16x16x32_bf16 v[84:87], a[160:163], a[204:207], v[84:87]\n"
      "ds_write_addtid_b32 v21 offset:30464\n"
      "v_fmac_f32 v37, s46, v73\n"
      "v_mfma_f32_16x16x32_bf16 v[88:91], v[104:107], a[208:211], v[88:91]\n"
      "ds_read_b128 a[200:203], v2 offset:20608\n"
      "v_fmac_f32 v38, s46, v74\n"
      "v_mfma_f32_16x16x32_bf16 v[92:95], a[164:167], a[208:211], v[92:95]\n"
      "s_nop 0\n"
      "v_cvt_pk_bf16_f32 v22, v22, v26\n"
      "v_fmac_f32 v39, s46, v75\n"
      "v_mfma_f32_16x16x32_bf16 v[96:99], a[156:159], a[208:211], v[96:99]\n"
      "s_waitcnt lgkmcnt(4)\n"
      "ds_write_addtid_b32 v22 offset:34560\n"
      "v_sub_f32 v36, v36, v48\n"
      "v_mfma_f32_16x16x32_bf16 v[76:79], a[152:155], a[212:215], v[76:79]\n"
      "s_nop 0\n"
      "ds_read_b128 a[204:207], v3 offset:0\n"
      "v_cvt_pk_bf16_f32 v23, v23, v27\n"
      "v_mfma_f32_16x16x32_bf16 v[80:83], a[144:147], a[212:215], v[80:83]\n"
      "ds_write_addtid_b32 v23 offset:38656\n"
      "v_sub_f32 v37, v37, v49\n"
      "v_mfma_f32_16x16x32_bf16 v[84:87], a[136:139], a[212:215], v[84:87]\n"
      "s_waitcnt lgkmcnt(3)\n"
      "v_sub_f32 v38, v38, v50\n"
      "v_sub_f32 v39, v39, v51\n"
      "v_mfma_f32_4x4x4_16b_bf16 v[12:15], a[216:217], a[220:221], 0\n"
      "v_mfma_f32_4x4x4_16b_bf16 v[16:19], a[216:217], a[222:223], 0\n"
      "v_mfma_f32_16x16x32_bf16 v[88:91], a[148:151], a[192:195], v[88:91]\n"
      "ds_read_b128 a[208:211], v3 offset:64\n"
      "v_mfma_f32_16x16x32_bf16 v[92:95], a[140:143], a[192:195], v[92:95]\n"
      "v_fmac_f32 v48, s45, v36\n"
      "v_fmac_f32 v49, s45, v37\n"
      "v_cvt_pk_bf16_f32 v12, v12, v16\n"
      "v_cvt_pk_bf16_f32 v13, v13, v17\n"
      "v_mfma_f32_16x16x32_bf16 v[96:99], a[132:135], a[192:195], v[96:99]\n"
      "ds_write_addtid_b32 v12 offset:26880\n"
      "ds_write_addtid_b32 v13 offset:30976\n"
      "v_fmac_f32 v50, s45, v38\n"
      "v_mfma_f32_16x16x32_bf16 v[76:79], a[128:131], a[196:199], v[76:79]\n"
      "ds_read_b128 a[212:215], v3 offset:128\n"
      "ds_read_b64_tr_b16 a[224:225], v7 offset:2560\n"
      "ds_read_b64_tr_b16 a[226:227], v7 offset:2568\n"
      "v_fmac_f32 v51, s45, v39\n"
      "v_mfma_f32_16x16x32_bf16 v[80:83], a[120:123], a[196:199], v[80:83]\n"
      "v_fma_f32 v36, s47, v36, v60\n"
      "v_cvt_pk_bf16_f32 v14, v14, v18\n"
      "v_mfma_f32_16x16x32_bf16 v[84:87], a[112:115], a[196:199], v[84:87]\n"
      "ds_write_addtid_b32 v14 offset:35072\n"
      "v_fma_f32 v37, s47, v37, v61\n"
      "v_mfma_f32_16x16x32_bf16 v[88:91], a[124:127], a[200:203], v[88:91]\n"
      "ds_read_b128 a[192:195], v4 offset:0\n"
      "v_fma_f32 v38, s47, v38, v62\n"
      "v_mfma_f32_16x16x32_bf16 v[92:95], a[116:119], a[200:203], v[92:95]\n"
      "v_fma_f32 v39, s47, v39, v63\n"
      "v_cvt_pk_bf16_f32 v15, v15, v19\n"
      "v_mfma_f32_16x16x32_bf16 v[96:99], a[108:111], a[200:203], v[96:99]\n"
      "s_waitcnt lgkmcnt(2)\n"
      "ds_write_addtid_b32 v15 offset:39168\n"
      "v_sub_f32 v76, v88, v76\n"
      "v_sub_f32 v77, v89, v77\n"
      "v_cvt_pk_bf16_f32 v48, v48, v49\n"
      "v_mfma_f32_16x16x32_bf16 v[64:67], a[0:3], a[204:207], 0\n"
      "s_nop 0\n"
      "ds_read_b128 a[196:199], v4 offset:64\n"
      "ds_write_addtid_b32 v48 offset:16384\n"
      "ds_write_addtid_b32 v76 offset:6144\n"
      "v_mfma_f32_16x16x32_bf16 v[68:71], a[4:7], a[204:207], 0\n"
      "ds_write_addtid_b32 v77 offset:6656\n"
      "v_sub_f32 v78, v90, v78\n"
      "v_cvt_pk_bf16_f32 v50, v50, v51\n"
      "v_sub_f32 v79, v91, v79\n"
      "v_mfma_f32_4x4x4_16b_bf16 v[20:23], a[216:217], a[224:225], 0\n"
      "v_sub_f32_e64 v80, v92, v80\n"
      "v_mfma_f32_4x4x4_16b_bf16 v[24:27], a[216:217], a[226:227], 0\n"
      "ds_write_addtid_b32 v50 offset:16896\n"
      "ds_write_addtid_b32 v78 offset:7168\n"
      "ds_write_addtid_b32 v79 offset:7680\n"
      "v_mfma_f32_16x16x32_bf16 v[72:75], a[8:11], a[204:207], 0\n"
      "ds_write_addtid_b32 v80 offset:8192\n"
      "v_cvt_pk_bf16_f32 v20, v20, v24\n"
      "v_mfma_f32_16x16x32_bf16 v[64:67], a[12:15], a[208:211], v[64:67]\n"
      "ds_read_b128 a[200:203], v4 offset:128\n"
      "ds_write_addtid_b32 v20 offset:27392\n"
      "v_sub_f32 v81, v93, v81\n"
      "v_mfma_f32_16x16x32_bf16 v[68:71], a[16:19], a[208:211], v[68:71]\n"
      "ds_write_addtid_b32 v81 offset:8704\n"
      "v_sub_f32 v82, v94, v82\n"
      "v_mfma_f32_16x16x32_bf16 v[72:75], a[20:23], a[208:211], v[72:75]\n"
      "ds_write_addtid_b32 v82 offset:9216\n"
      "v_cvt_pk_bf16_f32 v36, v36, v37\n"
      "v_sub_f32 v83, v95, v83\n"
      "v_mfma_f32_16x16x32_bf16 v[64:67], a[24:27], a[212:215], v[64:67]\n"
      "ds_read_b128 a[204:207], v5 offset:0\n"
      "ds_write_addtid_b32 v36 offset:18432\n"
      "v_mfma_f32_16x16x32_bf16 v[68:71], a[28:31], a[212:215], v[68:71]\n"
      "ds_write_addtid_b32 v83 offset:9728\n"
      "v_sub_f32_e64 v84, v96, v84\n"
      "v_mfma_f32_16x16x32_bf16 v[72:75], a[32:35], a[212:215], v[72:75]\n"
      "s_waitcnt lgkmcnt(5)\n"
      "ds_write_addtid_b32 v84 offset:10240\n"
      "v_cvt_pk_bf16_f32 v21, v21, v25\n"
      "v_cvt_pk_bf16_f32 v38, v38, v39\n"
      "v_mfma_f32_16x16x32_bf16 v[64:67], a[36:39], a[192:195], v[64:67]\n"
      "ds_read_b128 a[208:211], v5 offset:64\n"
      "ds_write_addtid_b32 v38 offset:18944\n"
      "v_mfma_f32_16x16x32_bf16 v[68:71], a[40:43], a[192:195], v[68:71]\n"
      "ds_write_addtid_b32 v21 offset:31488\n"
      "v_sub_f32 v85, v97, v85\n"
      "v_sub_f32 v86, v98, v86\n"
      "v_mfma_f32_16x16x32_bf16 v[72:75], a[44:47], a[192:195], v[72:75]\n"
      "ds_write_addtid_b32 v85 offset:10752\n"
      "ds_write_addtid_b32 v86 offset:11264\n"
      "v_sub_f32 v87, v99, v87\n"
      "v_mfma_f32_16x16x32_bf16 v[64:67], a[48:51], a[196:199], v[64:67]\n"
      "s_nop 0\n"
      "ds_read_b128 a[212:215], v5 offset:128\n"
      "ds_write_addtid_b32 v87 offset:11776\n"
      "v_mfma_f32_16x16x32_bf16 v[68:71], a[52:55], a[196:199], v[68:71]\n"
      "v_cvt_pk_bf16_f32 v22, v22, v26\n"
      "v_cvt_pk_bf16_f32 v23, v23, v27\n"
      "v_mfma_f32_16x16x32_bf16 v[72:75], a[56:59], a[196:199], v[72:75]\n"
      "ds_write_addtid_b32 v22 offset:35584\n"
      "ds_write_addtid_b32 v23 offset:39680\n"
      "v_fma_f32 v40, 0.5, v76, -v88\n"
      "v_mfma_f32_16x16x32_bf16 v[64:67], a[60:63], a[200:203], v[64:67]\n"
      "v_fma_f32 v41, 0.5, v77, -v89\n"
      "v_fma_f32 v42, 0.5, v78, -v90\n"
      "v_mfma_f32_16x16x32_bf16 v[68:71], a[64:67], a[200:203], v[68:71]\n"
      "v_fma_f32 v43, 0.5, v79, -v91\n"
      "v_fma_f32 v44, 0.5, v80, -v92\n"
      "v_mfma_f32_16x16x32_bf16 v[72:75], a[68:71], a[200:203], v[72:75]\n"
      "s_waitcnt lgkmcnt(0)\n"
      "v_fma_f32 v45, 0.5, v81, -v93\n"
      "s_barrier\n"
      "v_mfma_f32_16x16x32_bf16 v[64:67], a[72:75], a[204:207], v[64:67]\n"
      "ds_read_b128 a[192:195], v0 offset:0\n"
      "ds_read2st64_b32 v[28:29], v6 offset0:24 offset1:26\n"
      "v_fma_f32 v46, 0.5, v82, -v94\n"
      "v_mfma_f32_16x16x32_bf16 v[68:71], a[76:79], a[204:207], v[68:71]\n"
      "ds_read_b128 a[196:199], v0 offset:4096\n"
      "ds_read2st64_b32 v[30:31], v6 offset0:28 offset1:30\n"
      "v_mfma_f32_16x16x32_bf16 v[72:75], a[80:83], a[204:207], v[72:75]\n"
      "v_fma_f32 v47, 0.5, v83, -v95\n"
      "v_fma_f32 v48, 0.5, v84, -v96\n"
      "v_mfma_f32_16x16x32_bf16 v[64:67], a[84:87], a[208:211], v[64:67]\n"
      "ds_read_b128 a[200:203], v0 offset:64\n"
      "ds_read_b64_tr_b16 a[220:221], v7 offset:16896\n"
      "v_mfma_f32_16x16x32_bf16 v[68:71], a[88:91], a[208:211], v[68:71]\n"
      "ds_read_b64_tr_b16 a[222:223], v7 offset:16904\n"
      "ds_read2st64_b32 v[32:33], v6 offset0:32 offset1:34\n"
      "v_mfma_f32_16x16x32_bf16 v[72:75], a[92:95], a[208:211], v[72:75]\n"
      "ds_read2st64_b32 v[34:35], v6 offset0:36 offset1:38\n"
      "v_fma_f32 v49, 0.5, v85, -v97\n"
      "v_mfma_f32_16x16x32_bf16 v[64:67], a[96:99], a[212:215], v[64:67]\n"
      "ds_read_b128 a[204:207], v0 offset:4160\n"
      "ds_read2st64_b32 v[36:37], v6 offset0:40 offset1:42\n"
      "v_mfma_f32_16x16x32_bf16 v[68:71], a[100:103], a[212:215], v[68:71]\n"
      "v_fma_f32 v50, 0.5, v86, -v98\n"
      "v_fma_f32 v51, 0.5, v87, -v99\n"
      "v_mfma_f32_16x16x32_bf16 v[72:75], a[104:107], a[212:215], v[72:75]\n"
      "s_waitcnt lgkmcnt(8)\n"
      "s_nop 0\n"
      "v_mfma_f32_16x16x32_bf16 v[76:79], v[252:255], a[192:195], v[64:67]\n"
      "s_waitcnt lgkmcnt(4)\n"
      "ds_read_b128 a[208:211], v0 offset:128\n"
      "ds_read2st64_b32 v[38:39], v6 offset0:44 offset1:46\n"
      "v_fmac_f32 v28, s46, v52\n"
      "v_fmac_f32 v29, s46, v53\n"
      "v_fmac_f32 v30, s46, v54\n"
      "v_mfma_f32_4x4x4_16b_bf16 v[12:15], a[216:217], a[220:221], 0\n"
      "v_fmac_f32 v31, s46, v55\n"
      "v_mfma_f32_4x4x4_16b_bf16 v[16:19], a[216:217], a[222:223], 0\n"
      "v_sub_f32 v28, v28, v40\n"
      "v_sub_f32 v29, v29, v41\n"
      "v_mfma_f32_16x16x32_bf16 v[80:83], v[244:247], a[192:195], v[68:71]\n"
      "v_sub_f32 v30, v30, v42\n"
      "v_sub_f32 v31, v31, v43\n"
      "v_mfma_f32_16x16x32_bf16 v[84:87], v[236:239], a[192:195], v[72:75]\n"
      "v_fmac_f32 v40, s45, v28\n"
      "v_cvt_pk_bf16_f32 v12, v12, v16\n"
      "v_mfma_f32_16x16x32_bf16 v[88:91], v[248:251], a[196:199], v[64:67]\n"
      "ds_read_b128 a[212:215], v0 offset:4224\n"
      "ds_write_addtid_b32 v12 offset:41216\n"
      "v_fmac_f32 v41, s45, v29\n"
      "v_mfma_f32_16x16x32_bf16 v[92:95], v[240:243], a[196:199], v[68:71]\n"
      "ds_read_b64_tr_b16 a[224:225], v7 offset:17408\n"
      "ds_read_b64_tr_b16 a[226:227], v7 offset:17416\n"
      "v_mfma_f32_16x16x32_bf16 v[96:99], v[232:235], a[196:199], v[72:75]\n"
      "v_fmac_f32 v42, s45, v30\n"
      "v_cvt_pk_bf16_f32 v13, v13, v17\n"
      "v_mfma_f32_16x16x32_bf16 v[76:79], v[228:231], a[200:203], v[76:79]\n"
      "s_nop 0\n"
      "ds_read_b128 a[192:195], v1 offset:0\n"
      "ds_write_addtid_b32 v13 offset:45312\n"
      "v_mfma_f32_16x16x32_bf16 v[80:83], v[220:223], a[200:203], v[80:83]\n"
      "s_nop 0\n"
      "v_fmac_f32_e64 v43, s45, v31\n"
      "v_fma_f32 v28, s47, v28, v64\n"
      "v_mfma_f32_16x16x32_bf16 v[84:87], v[212:215], a[200:203], v[84:87]\n"
      "s_waitcnt lgkmcnt(4)\n"
      "v_cvt_pk_bf16_f32 v14, v14, v18\n"
      "v_fma_f32 v29, s47, v29, v65\n"
      "v_mfma_f32_16x16x32_bf16 v[88:91], v[224:227], a[204:207], v[88:91]\n"
      "s_nop 0\n"
      "ds_read_b128 a[196:199], v1 offset:4096\n"
      "ds_write_addtid_b32 v14 offset:49408\n"
      "v_mfma_f32_16x16x32_bf16 v[92:95], v[216:219], a[204:207], v[92:95]\n"
      "s_nop 0\n"
      "v_fma_f32 v30, s47, v30, v66\n"
      "v_cvt_pk_bf16_f32 v15, v15, v19\n"
      "v_mfma_f32_16x16x32_bf16 v[96:99], v[208:211], a[204:207], v[96:99]\n"
      "s_waitcnt lgkmcnt(2)\n"
      "ds_write_addtid_b32 v15 offset:53504\n"
      "v_fma_f32 v31, s47, v31, v67\n"
      "v_mfma_f32_4x4x4_16b_bf16 v[20:23], a[216:217], a[224:225], 0\n"
      "v_cvt_pk_bf16_f32 v40, v40, v41\n"
      "v_mfma_f32_4x4x4_16b_bf16 v[24:27], a[216:217], a[226:227], 0\n"
      "ds_write_addtid_b32 v40 offset:13312\n"
      "v_cvt_pk_bf16_f32 v42, v42, v43\n"
      "v_mfma_f32_16x16x32_bf16 v[76:79], v[204:207], a[208:211], v[76:79]\n"
      "ds_read_b128 a[200:203], v1 offset:64\n"
      "ds_write_addtid_b32 v42 offset:13824\n"
      "v_cvt_pk_bf16_f32 v20, v20, v24\n"
      "v_mfma_f32_16x16x32_bf16 v[80:83], v[196:199], a[208:211], v[80:83]\n"
      "ds_write_addtid_b32 v20 offset:41728\n"
      "v_cvt_pk_bf16_f32 v28, v28, v29\n"
      "v_mfma_f32_16x16x32_bf16 v[84:87], v[188:191], a[208:211], v[84:87]\n"
      "ds_write_addtid_b32 v28 offset:15360\n"
      "v_cvt_pk_bf16_f32 v30, v30, v31\n"
      "v_mfma_f32_16x16x32_bf16 v[88:91], v[200:203], a[212:215], v[88:91]\n"
      "ds_read_b128 a[204:207], v1 offset:4160\n"
      "ds_write_addtid_b32 v30 offset:15872\n"
      "v_mfma_f32_16x16x32_bf16 v[92:95], v[192:195], a[212:215], v[92:95]\n"
      "ds_read_b64_tr_b16 a[228:229], v7 offset:17920\n"
      "ds_read_b64_tr_b16 a[230:231], v7 offset:17928\n"
      "v_fmac_f32 v32, s46, v56\n"
      "v_mfma_f32_16x16x32_bf16 v[96:99], v[184:187], a[212:215], v[96:99]\n"
      "v_cvt_pk_bf16_f32 v21, v21, v25\n"
      "v_fmac_f32 v33, s46, v57\n"
      "v_mfma_f32_16x16x32_bf16 v[76:79], v[180:183], a[192:195], v[76:79]\n"
      "ds_read_b128 a[208:211], v1 offset:128\n"
      "ds_write_addtid_b32 v21 offset:45824\n"
      "v_mfma_f32_16x16x32_bf16 v[80:83], v[172:175], a[192:195], v[80:83]\n"
      "v_fmac_f32 v34, s46, v58\n"
      "v_cvt_pk_bf16_f32 v22, v22, v26\n"
      "v_mfma_f32_16x16x32_bf16 v[84:87], v[164:167], a[192:195], v[84:87]\n"
      "s_waitcnt lgkmcnt(3)\n"
      "ds_write_addtid_b32 v22 offset:49920\n"
      "v_fmac_f32 v35, s46, v59\n"
      "v_mfma_f32_16x16x32_bf16 v[88:91], v[176:179], a[196:199], v[88:91]\n"
      "s_nop 0\n"
      "ds_read_b128 a[212:215], v1 offset:4224\n"
      "v_sub_f32 v32, v32, v44\n"
      "v_mfma_f32_16x16x32_bf16 v[92:95], v[168:171], a[196:199], v[92:95]\n"
      "s_waitcnt lgkmcnt(4)\n"
      "v_sub_f32 v33, v33, v45\n"
      "v_cvt_pk_bf16_f32 v23, v23, v27\n"
      "v_mfma_f32_4x4x4_16b_bf16 v[12:15], a[216:217], a[228:229], 0\n"
      "v_sub_f32 v34, v34, v46\n"
      "v_mfma_f32_4x4x4_16b_bf16 v[16:19], a[216:217], a[230:231], 0\n"
      "ds_write_addtid_b32 v23 offset:54016\n"
      "v_sub_f32 v35, v35, v47\n"
      "v_mfma_f32_16x16x32_bf16 v[96:99], v[160:163], a[196:199], v[96:99]\n"
      "v_fmac_f32 v44, s45, v32\n"
      "v_fmac_f32 v45, s45, v33\n"
      "v_cvt_pk_bf16_f32 v12, v12, v16\n"
      "v_mfma_f32_16x16x32_bf16 v[76:79], v[156:159], a[200:203], v[76:79]\n"
      "ds_read_b128 a[192:195], v2 offset:0\n"
      "ds_write_addtid_b32 v12 offset:42240\n"
      "v_mfma_f32_16x16x32_bf16 v[80:83], v[148:151], a[200:203], v[80:83]\n"
      "s_nop 0\n"
      "v_cvt_pk_bf16_f32 v13, v13, v17\n"
      "v_fmac_f32_e64 v46, s45, v34\n"
      "v_mfma_f32_16x16x32_bf16 v[84:87], v[140:143], a[200:203], v[84:87]\n"
      "s_nop 0\n"
      "ds_read_b64_tr_b16 a[232:233], v7 offset:18432\n"
      "ds_write_addtid_b32 v13 offset:46336\n"
      "v_mfma_f32_16x16x32_bf16 v[88:91], v[152:155], a[204:207], v[88:91]\n"
      "s_nop 0\n"
      "ds_read_b128 a[196:199], v2 offset:4096\n"
      "ds_read_b64_tr_b16 a[234:235], v7 offset:18440\n"
      "v_mfma_f32_16x16x32_bf16 v[92:95], v[144:147], a[204:207], v[92:95]\n"
      "s_nop 0\n"
      "v_fmac_f32_e64 v47, s45, v35\n"
      "v_fma_f32 v32, s47, v32, v68\n"
      "v_mfma_f32_16x16x32_bf16 v[96:99], v[136:139], a[204:207], v[96:99]\n"
      "s_waitcnt lgkmcnt(4)\n"
      "v_cvt_pk_bf16_f32 v14, v14, v18\n"
      "v_fma_f32 v33, s47, v33, v69\n"
      "v_mfma_f32_16x16x32_bf16 v[76:79], v[132:135], a[208:211], v[76:79]\n"
      "s_nop 0\n"
      "ds_read_b128 a[200:203], v2 offset:64\n"
      "ds_write_addtid_b32 v14 offset:50432\n"
      "v_mfma_f32_16x16x32_bf16 v[80:83], v[124:127], a[208:211], v[80:83]\n"
      "s_nop 0\n"
      "v_fma_f32 v34, s47, v34, v70\n"
      "v_cvt_pk_bf16_f32 v15, v15, v19\n"
      "v_mfma_f32_16x16x32_bf16 v[84:87], v[116:119], a[208:211], v[84:87]\n"
      "s_nop 0\n"
      "ds_write_addtid_b32 v15 offset:54528\n"
      "v_fma_f32 v35, s47, v35, v71\n"
      "v_mfma_f32_16x16x32_bf16 v[88:91], v[128:131], a[212:215], v[88:91]\n"
      "s_waitcnt lgkmcnt(3)\n"
      "ds_read_b128 a[204:207], v2 offset:4160\n"
      "v_cvt_pk_bf16_f32 v44, v44, v45\n"
      "v_mfma_f32_4x4x4_16b_bf16 v[20:23], a[216:217], a[232:233], 0\n"
      "v_cvt_pk_bf16_f32 v46, v46, v47\n"
      "v_mfma_f32_4x4x4_16b_bf16 v[24:27], a[216:217], a[234:235], 0\n"
      "ds_write_addtid_b32 v44 offset:21508\n"
      "ds_write_addtid_b32 v46 offset:22020\n"
      "v_mfma_f32_16x16x32_bf16 v[92:95], v[120:123], a[212:215], v[92:95]\n"
      "v_cvt_pk_bf16_f32 v32, v32, v33\n"
      "v_cvt_pk_bf16_f32 v34, v34, v35\n"
      "v_cvt_pk_bf16_f32 v20, v20, v24\n"
      "v_mfma_f32_16x16x32_bf16 v[96:99], v[112:115], a[212:215], v[96:99]\n"
      "ds_write_addtid_b32 v32 offset:23556\n"
      "ds_write_addtid_b32 v34 offset:24068\n"
      "v_mfma_f32_16x16x32_bf16 v[76:79], v[108:111], a[192:195], v[76:79]\n"
      "ds_read_b128 a[208:211], v2 offset:128\n"
      "ds_write_addtid_b32 v20 offset:42752\n"
      "v_mfma_f32_16x16x32_bf16 v[80:83], v[100:103], a[192:195], v[80:83]\n"
      "ds_read_b64_tr_b16 a[220:221], v7 offset:18944\n"
      "ds_read_b64_tr_b16 a[222:223], v7 offset:18952\n"
      "v_fmac_f32 v36, s46, v60\n"
      "v_cvt_pk_bf16_f32 v21, v21, v25\n"
      "v_mfma_f32_16x16x32_bf16 v[84:87], a[160:163], a[192:195], v[84:87]\n"
      "ds_write_addtid_b32 v21 offset:46848\n"
      "v_fmac_f32 v37, s46, v61\n"
      "v_mfma_f32_16x16x32_bf16 v[88:91], v[104:107], a[196:199], v[88:91]\n"
      "ds_read_b128 a[212:215], v2 offset:4224\n"
      "v_fmac_f32 v38, s46, v62\n"
      "v_mfma_f32_16x16x32_bf16 v[92:95], a[164:167], a[196:199], v[92:95]\n"
      "s_nop 0\n"
      "v_cvt_pk_bf16_f32 v22, v22, v26\n"
      "v_fmac_f32 v39, s46, v63\n"
      "v_mfma_f32_16x16x32_bf16 v[96:99], a[156:159], a[196:199], v[96:99]\n"
      "s_waitcnt lgkmcnt(4)\n"
      "ds_write_addtid_b32 v22 offset:50944\n"
      "v_sub_f32 v36, v36, v48\n"
      "v_mfma_f32_16x16x32_bf16 v[76:79], a[152:155], a[200:203], v[76:79]\n"
      "s_nop 0\n"
      "ds_read_b128 a[192:195], v3 offset:16896\n"
      "v_cvt_pk_bf16_f32 v23, v23, v27\n"
      "v_mfma_f32_16x16x32_bf16 v[80:83], a[144:147], a[200:203], v[80:83]\n"
      "ds_write_addtid_b32 v23 offset:55040\n"
      "v_sub_f32 v37, v37, v49\n"
      "v_mfma_f32_16x16x32_bf16 v[84:87], a[136:139], a[200:203], v[84:87]\n"
      "s_waitcnt lgkmcnt(3)\n"
      "v_sub_f32 v38, v38, v50\n"
      "v_sub_f32 v39, v39, v51\n"
      "v_mfma_f32_4x4x4_16b_bf16 v[12:15], a[216:217], a[220:221], 0\n"
      "v_mfma_f32_4x4x4_16b_bf16 v[16:19], a[216:217], a[222:223], 0\n"
      "v_mfma_f32_16x16x32_bf16 v[88:91], a[148:151], a[204:207], v[88:91]\n"
      "ds_read_b128 a[196:199], v3 offset:16960\n"
      "v_mfma_f32_16x16x32_bf16 v[92:95], a[140:143], a[204:207], v[92:95]\n"
      "v_fmac_f32 v48, s45, v36\n"
      "v_fmac_f32 v49, s45, v37\n"
      "v_cvt_pk_bf16_f32 v12, v12, v16\n"
      "v_cvt_pk_bf16_f32 v13, v13, v17\n"
      "v_mfma_f32_16x16x32_bf16 v[96:99], a[132:135], a[204:207], v[96:99]\n"
      "ds_write_addtid_b32 v12 offset:43264\n"
      "ds_write_addtid_b32 v13 offset:47360\n"
      "v_fmac_f32 v50, s45, v38\n"
      "v_mfma_f32_16x16x32_bf16 v[76:79], a[128:131], a[208:211], v[76:79]\n"
      "ds_read_b128 a[200:203], v3 offset:17024\n"
      "ds_read_b64_tr_b16 a[224:225], v7 offset:19456\n"
      "ds_read_b64_tr_b16 a[226:227], v7 offset:19464\n"
      "v_fmac_f32 v51, s45, v39\n"
      "v_mfma_f32_16x16x32_bf16 v[80:83], a[120:123], a[208:211], v[80:83]\n"
      "v_fma_f32 v36, s47, v36, v72\n"
      "v_cvt_pk_bf16_f32 v14, v14, v18\n"
      "v_mfma_f32_16x16x32_bf16 v[84:87], a[112:115], a[208:211], v[84:87]\n"
      "ds_write_addtid_b32 v14 offset:51456\n"
      "v_fma_f32 v37, s47, v37, v73\n"
      "v_mfma_f32_16x16x32_bf16 v[88:91], a[124:127], a[212:215], v[88:91]\n"
      "ds_read_b128 a[204:207], v4 offset:16896\n"
      "v_fma_f32 v38, s47, v38, v74\n"
      "v_mfma_f32_16x16x32_bf16 v[92:95], a[116:119], a[212:215], v[92:95]\n"
      "v_fma_f32 v39, s47, v39, v75\n"
      "v_cvt_pk_bf16_f32 v15, v15, v19\n"
      "v_mfma_f32_16x16x32_bf16 v[96:99], a[108:111], a[212:215], v[96:99]\n"
      "s_waitcnt lgkmcnt(2)\n"
      "ds_write_addtid_b32 v15 offset:55552\n"
      "v_sub_f32 v76, v88, v76\n"
      "v_sub_f32 v77, v89, v77\n"
      "v_cvt_pk_bf16_f32 v48, v48, v49\n"
      "v_mfma_f32_16x16x32_bf16 v[52:55], a[0:3], a[192:195], 0\n"
      "s_nop 0\n"
      "ds_read_b128 a[208:211], v4 offset:16960\n"
      "ds_write_addtid_b32 v48 offset:17408\n"
      "ds_write_addtid_b32 v76 offset:0\n"
      "v_mfma_f32_16x16x32_bf16 v[56:59], a[4:7], a[192:195], 0\n"
      "ds_write_addtid_b32 v77 offset:512\n"
      "v_sub_f32 v78, v90, v78\n"
      "v_cvt_pk_bf16_f32 v50, v50, v51\n"
      "v_sub_f32 v79, v91, v79\n"
      "v_mfma_f32_4x4x4_16b_bf16 v[20:23], a[216:217], a[224:225], 0\n"
      "v_sub_f32_e64 v80, v92, v80\n"
      "v_mfma_f32_4x4x4_16b_bf16 v[24:27], a[216:217], a[226:227], 0\n"
      "ds_write_addtid_b32 v50 offset:17920\n"
      "ds_write_addtid_b32 v78 offset:1024\n"
      "ds_write_addtid_b32 v79 offset:1536\n"
      "v_mfma_f32_16x16x32_bf16 v[60:63], a[8:11], a[192:195], 0\n"
      "ds_write_addtid_b32 v80 offset:2048\n"
      "v_cvt_pk_bf16_f32 v20, v20, v24\n"
      "v_mfma_f32_16x16x32_bf16 v[52:55], a[12:15], a[196:199], v[52:55]\n"
      "ds_read_b128 a[212:215], v4 offset:17024\n"
      "ds_write_addtid_b32 v20 offset:43776\n"
      "v_sub_f32 v81, v93, v81\n"
      "v_mfma_f32_16x16x32_bf16 v[56:59], a[16:19], a[196:199], v[56:59]\n"
      "ds_write_addtid_b32 v81 offset:2560\n"
      "v_sub_f32 v82, v94, v82\n"
      "v_mfma_f32_16x16x32_bf16 v[60:63], a[20:23], a[196:199], v[60:63]\n"
      "ds_write_addtid_b32 v82 offset:3072\n"
      "v_cvt_pk_bf16_f32 v36, v36, v37\n"
      "v_sub_f32 v83, v95, v83\n"
      "v_mfma_f32_16x16x32_bf16 v[52:55], a[24:27], a[200:203], v[52:55]\n"
      "ds_read_b128 a[192:195], v5 offset:16896\n"
      "ds_write_addtid_b32 v36 offset:19456\n"
      "v_mfma_f32_16x16x32_bf16 v[56:59], a[28:31], a[200:203], v[56:59]\n"
      "ds_write_addtid_b32 v83 offset:3584\n"
      "v_sub_f32_e64 v84, v96, v84\n"
      "v_mfma_f32_16x16x32_bf16 v[60:63], a[32:35], a[200:203], v[60:63]\n"
      "s_waitcnt lgkmcnt(5)\n"
      "ds_write_addtid_b32 v84 offset:4096\n"
      "v_cvt_pk_bf16_f32 v21, v21, v25\n"
      "v_cvt_pk_bf16_f32 v38, v38, v39\n"
      "v_mfma_f32_16x16x32_bf16 v[52:55], a[36:39], a[204:207], v[52:55]\n"
      "ds_read_b128 a[196:199], v5 offset:16960\n"
      "ds_write_addtid_b32 v38 offset:19968\n"
      "v_mfma_f32_16x16x32_bf16 v[56:59], a[40:43], a[204:207], v[56:59]\n"
      "ds_write_addtid_b32 v21 offset:47872\n"
      "v_sub_f32 v85, v97, v85\n"
      "v_sub_f32 v86, v98, v86\n"
      "v_mfma_f32_16x16x32_bf16 v[60:63], a[44:47], a[204:207], v[60:63]\n"
      "ds_write_addtid_b32 v85 offset:4608\n"
      "ds_write_addtid_b32 v86 offset:5120\n"
      "v_sub_f32 v87, v99, v87\n"
      "v_mfma_f32_16x16x32_bf16 v[52:55], a[48:51], a[208:211], v[52:55]\n"
      "s_nop 0\n"
      "ds_read_b128 a[200:203], v5 offset:17024\n"
      "ds_write_addtid_b32 v87 offset:5632\n"
      "v_mfma_f32_16x16x32_bf16 v[56:59], a[52:55], a[208:211], v[56:59]\n"
      "v_cvt_pk_bf16_f32 v22, v22, v26\n"
      "v_cvt_pk_bf16_f32 v23, v23, v27\n"
      "v_mfma_f32_16x16x32_bf16 v[60:63], a[56:59], a[208:211], v[60:63]\n"
      "ds_write_addtid_b32 v22 offset:51968\n"
      "ds_write_addtid_b32 v23 offset:56064\n"
      "v_fma_f32 v40, 0.5, v76, -v88\n"
      "v_mfma_f32_16x16x32_bf16 v[52:55], a[60:63], a[212:215], v[52:55]\n"
      "v_fma_f32 v41, 0.5, v77, -v89\n"
      "v_fma_f32 v42, 0.5, v78, -v90\n"
      "v_mfma_f32_16x16x32_bf16 v[56:59], a[64:67], a[212:215], v[56:59]\n"
      "v_fma_f32 v43, 0.5, v79, -v91\n"
      "v_fma_f32 v44, 0.5, v80, -v92\n"
      "v_mfma_f32_16x16x32_bf16 v[60:63], a[68:71], a[212:215], v[60:63]\n"
      "s_waitcnt lgkmcnt(0)\n"
      "v_fma_f32 v45, 0.5, v81, -v93\n"
      "s_barrier\n"
      "s_add_u32 s55, s55, 2\n"
      "s_cmp_lt_u32 s55, 6\n"
      "s_cbranch_scc1 LSloop_%=\n"
      "LSlast_%=:\n"
      "v_mfma_f32_16x16x32_bf16 v[52:55], a[72:75], a[192:195], v[52:55]\n"
      "s_add_u32 s59, s62, 8847360\n"
      "ds_read_b128 a[204:207], v0 offset:16384\n"
      "ds_read2st64_b32 v[28:29], v6 offset0:0 offset1:2\n"
      "v_fma_f32 v46, 0.5, v82, -v94\n"
      "v_mfma_f32_16x16x32_bf16 v[56:59], a[76:79], a[192:195], v[56:59]\n"
      "s_nop 0\n"
      "ds_read_b128 a[208:211], v0 offset:20480\n"
      "ds_read2st64_b32 v[30:31], v6 offset0:4 offset1:6\n"
      "v_mfma_f32_16x16x32_bf16 v[60:63], a[80:83], a[192:195], v[60:63]\n"
      "ds_read_b64_tr_b16 a[220:221], v7 offset:33792\n"
      "v_fma_f32 v47, 0.5, v83, -v95\n"
      "v_mfma_f32_16x16x32_bf16 v[52:55], a[84:87], a[196:199], v[52:55]\n"
      "ds_read_b128 a[212:215], v0 offset:16448\n"
      "v_fma_f32 v48, 0.5, v84, -v96\n"
      "v_mfma_f32_16x16x32_bf16 v[56:59], a[88:91], a[196:199], v[56:59]\n"
      "ds_read_b64_tr_b16 a[222:223], v7 offset:33800\n"
      "ds_read2st64_b32 v[32:33], v6 offset0:8 offset1:10\n"
      "v_mfma_f32_16x16x32_bf16 v[60:63], a[92:95], a[196:199], v[60:63]\n"
      "ds_read2st64_b32 v[34:35], v6 offset0:12 offset1:14\n"
      "ds_read2st64_b32 v[36:37], v6 offset0:16 offset1:18\n"
      "v_fma_f32 v49, 0.5, v85, -v97\n"
      "v_mfma_f32_16x16x32_bf16 v[52:55], a[96:99], a[200:203], v[52:55]\n"
      "ds_read_b128 a[192:195], v0 offset:20544\n"
      "v_fma_f32 v50, 0.5, v86, -v98\n"
      "v_mfma_f32_16x16x32_bf16 v[56:59], a[100:103], a[200:203], v[56:59]\n"
      "ds_read2st64_b32 v[38:39], v6 offset0:20 offset1:22\n"
      "v_fma_f32 v51, 0.5, v87, -v99\n"
      "v_mfma_f32_16x16x32_bf16 v[60:63], a[104:107], a[200:203], v[60:63]\n"
      "s_waitcnt lgkmcnt(8)\n"
      "v_fmac_f32 v28, s46, v64\n"
      "v_fmac_f32_e64 v29, s46, v65\n"
      "v_mfma_f32_16x16x32_bf16 v[76:79], v[252:255], a[204:207], v[52:55]\n"
      "s_waitcnt lgkmcnt(5)\n"
      "ds_read_b128 a[196:199], v0 offset:16512\n"
      "ds_read2st64_b32 a[240:241], v8 offset0:4 offset1:6\n"
      "v_fmac_f32 v30, s46, v66\n"
      "v_mfma_f32_4x4x4_16b_bf16 v[12:15], a[216:217], a[220:221], 0\n"
      "v_fmac_f32 v31, s46, v67\n"
      "v_mfma_f32_4x4x4_16b_bf16 v[16:19], a[216:217], a[222:223], 0\n"
      "ds_read2st64_b32 a[242:243], v9 offset0:4 offset1:6\n"
      "v_sub_f32 v28, v28, v40\n"
      "v_mfma_f32_16x16x32_bf16 v[80:83], v[244:247], a[204:207], v[56:59]\n"
      "v_sub_f32 v29, v29, v41\n"
      "v_sub_f32 v30, v30, v42\n"
      "v_mfma_f32_16x16x32_bf16 v[84:87], v[236:239], a[204:207], v[60:63]\n"
      "v_sub_f32 v31, v31, v43\n"
      "v_cvt_pk_bf16_f32 v12, v12, v16\n"
      "v_mfma_f32_16x16x32_bf16 v[88:91], v[248:251], a[208:211], v[52:55]\n"
      "ds_read_b128 a[200:203], v0 offset:20608\n"
      "ds_write_addtid_b32 v12 offset:24832\n"
      "v_fmac_f32 v40, s45, v28\n"
      "v_mfma_f32_16x16x32_bf16 v[92:95], v[240:243], a[208:211], v[56:59]\n"
      "ds_read_b64_tr_b16 a[224:225], v7 offset:34304\n"
      "ds_read_b64_tr_b16 a[226:227], v7 offset:34312\n"
      "v_mfma_f32_16x16x32_bf16 v[96:99], v[232:235], a[208:211], v[60:63]\n"
      "v_fmac_f32 v41, s45, v29\n"
      "v_fmac_f32 v42, s45, v30\n"
      "v_mfma_f32_16x16x32_bf16 v[76:79], v[228:231], a[212:215], v[76:79]\n"
      "ds_read_b128 a[204:207], v1 offset:16384\n"
      "v_cvt_pk_bf16_f32 v13, v13, v17\n"
      "v_mfma_f32_16x16x32_bf16 v[80:83], v[220:223], a[212:215], v[80:83]\n"
      "ds_write_addtid_b32 v13 offset:28928\n"
      "v_fmac_f32 v43, s45, v31\n"
      "v_mfma_f32_16x16x32_bf16 v[84:87], v[212:215], a[212:215], v[84:87]\n"
      "s_waitcnt lgkmcnt(4)\n"
      "v_fma_f32 v28, s47, v28, v52\n"
      "v_cvt_pk_bf16_f32 v14, v14, v18\n"
      "v_mfma_f32_16x16x32_bf16 v[88:91], v[224:227], a[192:195], v[88:91]\n"
      "ds_read_b128 a[208:211], v1 offset:20480\n"
      "ds_write_addtid_b32 v14 offset:33024\n"
      "v_mfma_f32_16x16x32_bf16 v[92:95], v[216:219], a[192:195], v[92:95]\n"
      "s_nop 0\n"
      "v_fma_f32 v29, s47, v29, v53\n"
      "v_fma_f32 v30, s47, v30, v54\n"
      "v_mfma_f32_16x16x32_bf16 v[96:99], v[208:211], a[192:195], v[96:99]\n"
      "s_waitcnt lgkmcnt(3)\n"
      "v_cvt_pk_bf16_f32 v15, v15, v19\n"
      "v_fma_f32 v31, s47, v31, v55\n"
      "v_mfma_f32_4x4x4_16b_bf16 v[20:23], a[216:217], a[224:225], 0\n"
      "v_cvt_pk_bf16_f32 v40, v40, v41\n"
      "v_mfma_f32_4x4x4_16b_bf16 v[24:27], a[216:217], a[226:227], 0\n"
      "ds_write_addtid_b32 v40 offset:12288\n"
      "ds_write_addtid_b32 v15 offset:37120\n"
      "v_mfma_f32_16x16x32_bf16 v[76:79], v[204:207], a[196:199], v[76:79]\n"
      "ds_read_b128 a[212:215], v1 offset:16448\n"
      "v_cvt_pk_bf16_f32 v42, v42, v43\n"
      "v_cvt_pk_bf16_f32 v20, v20, v24\n"
      "v_mfma_f32_16x16x32_bf16 v[80:83], v[196:199], a[196:199], v[80:83]\n"
      "ds_write_addtid_b32 v42 offset:12800\n"
      "ds_write_addtid_b32 v20 offset:25344\n"
      "v_mfma_f32_16x16x32_bf16 v[84:87], v[188:191], a[196:199], v[84:87]\n"
      "buffer_store_dwordx4 a[240:243], v11, s[8:11], s59 offen offset:256 sc1\n"
      "v_mfma_f32_16x16x32_bf16 v[88:91], v[200:203], a[200:203], v[88:91]\n"
      "ds_read_b128 a[192:195], v1 offset:20544\n"
      "ds_read_b64_tr_b16 a[228:229], v7 offset:34816\n"
      "v_cvt_pk_bf16_f32 v28, v28, v29\n"
      "v_mfma_f32_16x16x32_bf16 v[92:95], v[192:195], a[200:203], v[92:95]\n"
      "ds_write_addtid_b32 v28 offset:14336\n"
      "ds_read_b64_tr_b16 a[230:231], v7 offset:34824\n"
      "v_mfma_f32_16x16x32_bf16 v[96:99], v[184:187], a[200:203], v[96:99]\n"
      "v_cvt_pk_bf16_f32 v30, v30, v31\n"
      "v_cvt_pk_bf16_f32 v21, v21, v25\n"
      "v_mfma_f32_16x16x32_bf16 v[76:79], v[180:183], a[204:207], v[76:79]\n"
      "ds_read_b128 a[196:199], v1 offset:16512\n"
      "ds_write_addtid_b32 v30 offset:14848\n"
      "v_fmac_f32 v32, s46, v68\n"
      "v_mfma_f32_16x16x32_bf16 v[80:83], v[172:175], a[204:207], v[80:83]\n"
      "ds_write_addtid_b32 v21 offset:29440\n"
      "v_fmac_f32 v33, s46, v69\n"
      "v_mfma_f32_16x16x32_bf16 v[84:87], v[164:167], a[204:207], v[84:87]\n"
      "s_waitcnt lgkmcnt(4)\n"
      "v_fmac_f32 v34, s46, v70\n"
      "v_cvt_pk_bf16_f32 v22, v22, v26\n"
      "v_mfma_f32_16x16x32_bf16 v[88:91], v[176:179], a[208:211], v[88:91]\n"
      "ds_read_b128 a[200:203], v1 offset:20608\n"
      "ds_write_addtid_b32 v22 offset:33536\n"
      "v_mfma_f32_16x16x32_bf16 v[92:95], v[168:171], a[208:211], v[92:95]\n"
      "s_waitcnt lgkmcnt(5)\n"
      "v_fmac_f32 v35, s46, v71\n"
      "v_sub_f32_e64 v32, v32, v44\n"
      "v_mfma_f32_4x4x4_16b_bf16 v[12:15], a[216:217], a[228:229], 0\n"
      "v_cvt_pk_bf16_f32 v23, v23, v27\n"
      "v_mfma_f32_4x4x4_16b_bf16 v[16:19], a[216:217], a[230:231], 0\n"
      "ds_write_addtid_b32 v23 offset:37632\n"
      "v_sub_f32 v33, v33, v45\n"
      "v_mfma_f32_16x16x32_bf16 v[96:99], v[160:163], a[208:211], v[96:99]\n"
      "v_sub_f32 v34, v34, v46\n"
      "v_sub_f32 v35, v35, v47\n"
      "v_cvt_pk_bf16_f32 v12, v12, v16\n"
      "v_mfma_f32_16x16x32_bf16 v[76:79], v[156:159], a[212:215], v[76:79]\n"
      "s_nop 0\n"
      "ds_read_b128 a[204:207], v2 offset:16384\n"
      "ds_write_addtid_b32 v12 offset:25856\n"
      "v_mfma_f32_16x16x32_bf16 v[80:83], v[148:151], a[212:215], v[80:83]\n"
      "v_fmac_f32 v44, s45, v32\n"
      "v_cvt_pk_bf16_f32 v13, v13, v17\n"
      "v_mfma_f32_16x16x32_bf16 v[84:87], v[140:143], a[212:215], v[84:87]\n"
      "ds_read_b64_tr_b16 a[232:233], v7 offset:35328\n"
      "ds_write_addtid_b32 v13 offset:29952\n"
      "v_fmac_f32 v45, s45, v33\n"
      "v_mfma_f32_16x16x32_bf16 v[88:91], v[152:155], a[192:195], v[88:91]\n"
      "ds_read_b128 a[208:211], v2 offset:20480\n"
      "ds_read_b64_tr_b16 a[234:235], v7 offset:35336\n"
      "v_mfma_f32_16x16x32_bf16 v[92:95], v[144:147], a[192:195], v[92:95]\n"
      "v_fmac_f32 v46, s45, v34\n"
      "v_fmac_f32_e64 v47, s45, v35\n"
      "v_mfma_f32_16x16x32_bf16 v[96:99], v[136:139], a[192:195], v[96:99]\n"
      "s_waitcnt lgkmcnt(4)\n"
      "v_cvt_pk_bf16_f32 v14, v14, v18\n"
      "v_fma_f32 v32, s47, v32, v56\n"
      "v_mfma_f32_16x16x32_bf16 v[76:79], v[132:135], a[196:199], v[76:79]\n"
      "ds_read_b128 a[212:215], v2 offset:16448\n"
      "ds_write_addtid_b32 v14 offset:34048\n"
      "v_mfma_f32_16x16x32_bf16 v[80:83], v[124:127], a[196:199], v[80:83]\n"
      "v_fma_f32 v33, s47, v33, v57\n"
      "v_cvt_pk_bf16_f32 v15, v15, v19\n"
      "v_mfma_f32_16x16x32_bf16 v[84:87], v[116:119], a[196:199], v[84:87]\n"
      "s_nop 0\n"
      "ds_write_addtid_b32 v15 offset:38144\n"
      "v_fma_f32 v34, s47, v34, v58\n"
      "v_mfma_f32_16x16x32_bf16 v[88:91], v[128:131], a[200:203], v[88:91]\n"
      "s_waitcnt lgkmcnt(3)\n"
      "ds_read_b128 a[192:195], v2 offset:20544\n"
      "v_fma_f32 v35, s47, v35, v59\n"
      "v_mfma_f32_4x4x4_16b_bf16 v[20:23], a[216:217], a[232:233], 0\n"
      "v_cvt_pk_bf16_f32 v44, v44, v45\n"
      "v_mfma_f32_4x4x4_16b_bf16 v[24:27], a[216:217], a[234:235], 0\n"
      "ds_write_addtid_b32 v44 offset:20484\n"
      "v_cvt_pk_bf16_f32 v46, v46, v47\n"
      "v_mfma_f32_16x16x32_bf16 v[92:95], v[120:123], a[200:203], v[92:95]\n"
      "ds_write_addtid_b32 v46 offset:20996\n"
      "v_cvt_pk_bf16_f32 v32, v32, v33\n"
      "v_cvt_pk_bf16_f32 v20, v20, v24\n"
      "v_mfma_f32_16x16x32_bf16 v[96:99], v[112:115], a[200:203], v[96:99]\n"
      "ds_write_addtid_b32 v32 offset:22532\n"
      "ds_write_addtid_b32 v20 offset:26368\n"
      "v_mfma_f32_16x16x32_bf16 v[76:79], v[108:111], a[204:207], v[76:79]\n"
      "ds_read_b128 a[196:199], v2 offset:16512\n"
      "v_cvt_pk_bf16_f32 v34, v34, v35\n"
      "v_mfma_f32_16x16x32_bf16 v[80:83], v[100:103], a[204:207], v[80:83]\n"
      "ds_write_addtid_b32 v34 offset:23044\n"
      "ds_read_b64_tr_b16 a[220:221], v7 offset:35840\n"
      "v_cvt_pk_bf16_f32 v21, v21, v25\n"
      "v_fmac_f32 v36, s46, v72\n"
      "v_mfma_f32_16x16x32_bf16 v[84:87], a[160:163], a[204:207], v[84:87]\n"
      "ds_write_addtid_b32 v21 offset:30464\n"
      "ds_read_b64_tr_b16 a[222:223], v7 offset:35848\n"
      "v_mfma_f32_16x16x32_bf16 v[88:91], v[104:107], a[208:211], v[88:91]\n"
      "ds_read_b128 a[200:203], v2 offset:20608\n"
      "v_fmac_f32 v37, s46, v73\n"
      "v_mfma_f32_16x16x32_bf16 v[92:95], a[164:167], a[208:211], v[92:95]\n"
      "v_fmac_f32 v38, s46, v74\n"
      "v_cvt_pk_bf16_f32 v22, v22, v26\n"
      "v_mfma_f32_16x16x32_bf16 v[96:99], a[156:159], a[208:211], v[96:99]\n"
      "s_waitcnt lgkmcnt(3)\n"
      "ds_write_addtid_b32 v22 offset:34560\n"
      "v_fmac_f32 v39, s46, v75\n"
      "v_mfma_f32_16x16x32_bf16 v[76:79], a[152:155], a[212:215], v[76:79]\n"
      "ds_read_b128 a[204:207], v3 offset:33792\n"
      "v_sub_f32 v36, v36, v48\n"
      "v_mfma_f32_16x16x32_bf16 v[80:83], a[144:147], a[212:215], v[80:83]\n"
      "v_sub_f32 v37, v37, v49\n"
      "v_cvt_pk_bf16_f32 v23, v23, v27\n"
      "v_mfma_f32_16x16x32_bf16 v[84:87], a[136:139], a[212:215], v[84:87]\n"
      "s_waitcnt lgkmcnt(2)\n"
      "ds_write_addtid_b32 v23 offset:38656\n"
      "v_sub_f32 v38, v38, v50\n"
      "v_mfma_f32_4x4x4_16b_bf16 v[12:15], a[216:217], a[220:221], 0\n"
      "v_sub_f32 v39, v39, v51\n"
      "v_mfma_f32_4x4x4_16b_bf16 v[16:19], a[216:217], a[222:223], 0\n"
      "v_mfma_f32_16x16x32_bf16 v[88:91], a[148:151], a[192:195], v[88:91]\n"
      "ds_read_b128 a[208:211], v3 offset:33856\n"
      "v_mfma_f32_16x16x32_bf16 v[92:95], a[140:143], a[192:195], v[92:95]\n"
      "v_fmac_f32 v48, s45, v36\n"
      "v_fmac_f32 v49, s45, v37\n"
      "v_cvt_pk_bf16_f32 v12, v12, v16\n"
      "v_cvt_pk_bf16_f32 v13, v13, v17\n"
      "v_mfma_f32_16x16x32_bf16 v[96:99], a[132:135], a[192:195], v[96:99]\n"
      "ds_write_addtid_b32 v12 offset:26880\n"
      "ds_write_addtid_b32 v13 offset:30976\n"
      "v_fmac_f32 v50, s45, v38\n"
      "v_mfma_f32_16x16x32_bf16 v[76:79], a[128:131], a[196:199], v[76:79]\n"
      "ds_read_b128 a[212:215], v3 offset:33920\n"
      "ds_read_b64_tr_b16 a[224:225], v7 offset:36352\n"
      "ds_read_b64_tr_b16 a[226:227], v7 offset:36360\n"
      "v_fmac_f32 v51, s45, v39\n"
      "v_mfma_f32_16x16x32_bf16 v[80:83], a[120:123], a[196:199], v[80:83]\n"
      "v_fma_f32 v36, s47, v36, v60\n"
      "v_cvt_pk_bf16_f32 v14, v14, v18\n"
      "v_mfma_f32_16x16x32_bf16 v[84:87], a[112:115], a[196:199], v[84:87]\n"
      "ds_write_addtid_b32 v14 offset:35072\n"
      "v_fma_f32 v37, s47, v37, v61\n"
      "v_mfma_f32_16x16x32_bf16 v[88:91], a[124:127], a[200:203], v[88:91]\n"
      "ds_read_b128 a[192:195], v4 offset:33792\n"
      "v_fma_f32 v38, s47, v38, v62\n"
      "v_mfma_f32_16x16x32_bf16 v[92:95], a[116:119], a[200:203], v[92:95]\n"
      "v_fma_f32 v39, s47, v39, v63\n"
      "v_cvt_pk_bf16_f32 v15, v15, v19\n"
      "v_mfma_f32_16x16x32_bf16 v[96:99], a[108:111], a[200:203], v[96:99]\n"
      "s_waitcnt lgkmcnt(2)\n"
      "ds_write_addtid_b32 v15 offset:39168\n"
      "v_sub_f32 v76, v88, v76\n"
      "v_sub_f32 v77, v89, v77\n"
      "v_cvt_pk_bf16_f32 v48, v48, v49\n"
      "v_mfma_f32_16x16x32_bf16 v[64:67], a[0:3], a[204:207], 0\n"
      "s_nop 0\n"
      "ds_read_b128 a[196:199], v4 offset:33856\n"
      "ds_write_addtid_b32 v48 offset:16384\n"
      "ds_write_addtid_b32 v76 offset:6144\n"
      "v_mfma_f32_16x16x32_bf16 v[68:71], a[4:7], a[204:207], 0\n"
      "ds_write_addtid_b32 v77 offset:6656\n"
      "v_sub_f32 v78, v90, v78\n"
      "v_cvt_pk_bf16_f32 v50, v50, v51\n"
      "v_sub_f32 v79, v91, v79\n"
      "v_mfma_f32_4x4x4_16b_bf16 v[20:23], a[216:217], a[224:225], 0\n"
      "v_sub_f32_e64 v80, v92, v80\n"
      "v_mfma_f32_4x4x4_16b_bf16 v[24:27], a[216:217], a[226:227], 0\n"
      "ds_write_addtid_b32 v50 offset:16896\n"
      "ds_write_addtid_b32 v78 offset:7168\n"
      "ds_write_addtid_b32 v79 offset:7680\n"
      "v_mfma_f32_16x16x32_bf16 v[72:75], a[8:11], a[204:207], 0\n"
      "ds_write_addtid_b32 v80 offset:8192\n"
      "v_cvt_pk_bf16_f32 v20, v20, v24\n"
      "v_sub_f32_e64 v81, v93, v81\n"
      "v_mfma_f32_16x16x32_bf16 v[64:67], a[12:15], a[208:211], v[64:67]\n"
      "ds_read_b128 a[200:203], v4 offset:33920\n"
      "ds_write_addtid_b32 v20 offset:27392\n"
      "v_mfma_f32_16x16x32_bf16 v[68:71], a[16:19], a[208:211], v[68:71]\n"
      "ds_write_addtid_b32 v81 offset:8704\n"
      "v_sub_f32 v82, v94, v82\n"
      "v_mfma_f32_16x16x32_bf16 v[72:75], a[20:23], a[208:211], v[72:75]\n"
      "s_nop 0\n"
      "ds_write_addtid_b32 v82 offset:9216\n"
      "v_cvt_pk_bf16_f32 v36, v36, v37\n"
      "v_sub_f32 v83, v95, v83\n"
      "v_mfma_f32_16x16x32_bf16 v[64:67], a[24:27], a[212:215], v[64:67]\n"
      "ds_read_b128 a[204:207], v5 offset:33792\n"
      "ds_write_addtid_b32 v36 offset:18432\n"
      "v_mfma_f32_16x16x32_bf16 v[68:71], a[28:31], a[212:215], v[68:71]\n"
      "ds_write_addtid_b32 v83 offset:9728\n"
      "v_sub_f32 v84, v96, v84\n"
      "v_cvt_pk_bf16_f32 v21, v21, v25\n"
      "v_mfma_f32_16x16x32_bf16 v[72:75], a[32:35], a[212:215], v[72:75]\n"
      "s_waitcnt lgkmcnt(4)\n"
      "ds_write_addtid_b32 v21 offset:31488\n"
      "ds_write_addtid_b32 v84 offset:10240\n"
      "v_mfma_f32_16x16x32_bf16 v[64:67], a[36:39], a[192:195], v[64:67]\n"
      "ds_read_b128 a[208:211], v5 offset:33856\n"
      "v_cvt_pk_bf16_f32 v38, v38, v39\n"
      "v_mfma_f32_16x16x32_bf16 v[68:71], a[40:43], a[192:195], v[68:71]\n"
      "s_nop 0\n"
      "ds_write_addtid_b32 v38 offset:18944\n"
      "v_sub_f32 v85, v97, v85\n"
      "v_sub_f32 v86, v98, v86\n"
      "v_mfma_f32_16x16x32_bf16 v[72:75], a[44:47], a[192:195], v[72:75]\n"
      "ds_write_addtid_b32 v85 offset:10752\n"
      "ds_write_addtid_b32 v86 offset:11264\n"
      "v_sub_f32 v87, v99, v87\n"
      "v_mfma_f32_16x16x32_bf16 v[64:67], a[48:51], a[196:199], v[64:67]\n"
      "s_nop 0\n"
      "ds_read_b128 a[212:215], v5 offset:33920\n"
      "ds_write_addtid_b32 v87 offset:11776\n"
      "v_mfma_f32_16x16x32_bf16 v[68:71], a[52:55], a[196:199], v[68:71]\n"
      "v_cvt_pk_bf16_f32 v22, v22, v26\n"
      "v_cvt_pk_bf16_f32 v23, v23, v27\n"
      "v_mfma_f32_16x16x32_bf16 v[72:75], a[56:59], a[196:199], v[72:75]\n"
      "ds_write_addtid_b32 v22 offset:35584\n"
      "ds_write_addtid_b32 v23 offset:39680\n"
      "v_fma_f32 v40, 0.5, v76, -v88\n"
      "v_mfma_f32_16x16x32_bf16 v[64:67], a[60:63], a[200:203], v[64:67]\n"
      "v_fma_f32 v41, 0.5, v77, -v89\n"
      "v_fma_f32 v42, 0.5, v78, -v90\n"
      "v_mfma_f32_16x16x32_bf16 v[68:71], a[64:67], a[200:203], v[68:71]\n"
      "s_nop 0\n"
      "v_fma_f32 v43, 0.5, v79, -v91\n"
      "v_fma_f32 v44, 0.5, v80, -v92\n"
      "v_mfma_f32_16x16x32_bf16 v[72:75], a[68:71], a[200:203], v[72:75]\n"
      "s_waitcnt lgkmcnt(0)\n"
      "v_fma_f32 v45, 0.5, v81, -v93\n"
      "s_barrier\n"
      "v_mfma_f32_16x16x32_bf16 v[64:67], a[72:75], a[204:207], v[64:67]\n"
      "s_add_u32 s59, s62, 9633792\n"
      "ds_read_b128 a[192:195], v0 offset:0\n"
      "ds_read2st64_b32 v[28:29], v6 offset0:24 offset1:26\n"
      "v_fma_f32 v46, 0.5, v82, -v94\n"
      "v_mfma_f32_16x16x32_bf16 v[68:71], a[76:79], a[204:207], v[68:71]\n"
      "s_nop 0\n"
      "ds_read_b128 a[196:199], v0 offset:4096\n"
      "ds_read2st64_b32 v[30:31], v6 offset0:28 offset1:30\n"
      "v_mfma_f32_16x16x32_bf16 v[72:75], a[80:83], a[204:207], v[72:75]\n"
      "ds_read_b64_tr_b16 a[220:221], v7 offset:50688\n"
      "v_fma_f32 v47, 0.5, v83, -v95\n"
      "v_mfma_f32_16x16x32_bf16 v[64:67], a[84:87], a[208:211], v[64:67]\n"
      "ds_read_b128 a[200:203], v0 offset:64\n"
      "v_fma_f32 v48, 0.5, v84, -v96\n"
      "v_mfma_f32_16x16x32_bf16 v[68:71], a[88:91], a[208:211], v[68:71]\n"
      "ds_read_b64_tr_b16 a[222:223], v7 offset:50696\n"
      "ds_read2st64_b32 v[32:33], v6 offset0:32 offset1:34\n"
      "v_mfma_f32_16x16x32_bf16 v[72:75], a[92:95], a[208:211], v[72:75]\n"
      "ds_read2st64_b32 v[34:35], v6 offset0:36 offset1:38\n"
      "ds_read2st64_b32 v[36:37], v6 offset0:40 offset1:42\n"
      "v_fma_f32 v49, 0.5, v85, -v97\n"
      "v_mfma_f32_16x16x32_bf16 v[64:67], a[96:99], a[212:215], v[64:67]\n"
      "ds_read_b128 a[204:207], v0 offset:4160\n"
      "v_fma_f32 v50, 0.5, v86, -v98\n"
      "v_mfma_f32_16x16x32_bf16 v[68:71], a[100:103], a[212:215], v[68:71]\n"
      "ds_read2st64_b32 v[38:39], v6 offset0:44 offset1:46\n"
      "v_fma_f32 v51, 0.5, v87, -v99\n"
      "v_mfma_f32_16x16x32_bf16 v[72:75], a[104:107], a[212:215], v[72:75]\n"
      "s_waitcnt lgkmcnt(8)\n"
      "v_fmac_f32 v28, s46, v52\n"
      "v_fmac_f32_e64 v29, s46, v53\n"
      "v_mfma_f32_16x16x32_bf16 v[76:79], v[252:255], a[192:195], v[64:67]\n"
      "s_waitcnt lgkmcnt(5)\n"
      "ds_read_b128 a[208:211], v0 offset:128\n"
      "ds_read2st64_b32 a[240:241], v8 offset0:0 offset1:2\n"
      "v_fmac_f32 v30, s46, v54\n"
      "v_mfma_f32_4x4x4_16b_bf16 v[12:15], a[216:217], a[220:221], 0\n"
      "v_fmac_f32 v31, s46, v55\n"
      "v_mfma_f32_4x4x4_16b_bf16 v[16:19], a[216:217], a[222:223], 0\n"
      "ds_read2st64_b32 a[242:243], v9 offset0:0 offset1:2\n"
      "v_sub_f32 v28, v28, v40\n"
      "v_mfma_f32_16x16x32_bf16 v[80:83], v[244:247], a[192:195], v[68:71]\n"
      "v_sub_f32 v29, v29, v41\n"
      "v_sub_f32 v30, v30, v42\n"
      "v_mfma_f32_16x16x32_bf16 v[84:87], v[236:239], a[192:195], v[72:75]\n"
      "v_sub_f32 v31, v31, v43\n"
      "v_cvt_pk_bf16_f32 v12, v12, v16\n"
      "v_mfma_f32_16x16x32_bf16 v[88:91], v[248:251], a[196:199], v[64:67]\n"
      "ds_read_b128 a[212:215], v0 offset:4224\n"
      "ds_write_addtid_b32 v12 offset:41216\n"
      "v_fmac_f32 v40, s45, v28\n"
      "v_mfma_f32_16x16x32_bf16 v[92:95], v[240:243], a[196:199], v[68:71]\n"
      "ds_read_b64_tr_b16 a[224:225], v7 offset:51200\n"
      "ds_read_b64_tr_b16 a[226:227], v7 offset:51208\n"
      "v_mfma_f32_16x16x32_bf16 v[96:99], v[232:235], a[196:199], v[72:75]\n"
      "v_fmac_f32 v41, s45, v29\n"
      "v_fmac_f32 v42, s45, v30\n"
      "v_mfma_f32_16x16x32_bf16 v[76:79], v[228:231], a[200:203], v[76:79]\n"
      "ds_read_b128 a[192:195], v1 offset:0\n"
      "v_cvt_pk_bf16_f32 v13, v13, v17\n"
      "v_mfma_f32_16x16x32_bf16 v[80:83], v[220:223], a[200:203], v[80:83]\n"
      "ds_write_addtid_b32 v13 offset:45312\n"
      "v_fmac_f32 v43, s45, v31\n"
      "v_mfma_f32_16x16x32_bf16 v[84:87], v[212:215], a[200:203], v[84:87]\n"
      "s_waitcnt lgkmcnt(4)\n"
      "v_fma_f32 v28, s47, v28, v64\n"
      "v_cvt_pk_bf16_f32 v14, v14, v18\n"
      "v_mfma_f32_16x16x32_bf16 v[88:91], v[224:227], a[204:207], v[88:91]\n"
      "ds_read_b128 a[196:199], v1 offset:4096\n"
      "ds_write_addtid_b32 v14 offset:49408\n"
      "v_mfma_f32_16x16x32_bf16 v[92:95], v[216:219], a[204:207], v[92:95]\n"
      "s_nop 0\n"
      "v_fma_f32 v29, s47, v29, v65\n"
      "v_fma_f32 v30, s47, v30, v66\n"
      "v_mfma_f32_16x16x32_bf16 v[96:99], v[208:211], a[204:207], v[96:99]\n"
      "s_waitcnt lgkmcnt(3)\n"
      "v_cvt_pk_bf16_f32 v15, v15, v19\n"
      "v_fma_f32 v31, s47, v31, v67\n"
      "v_mfma_f32_4x4x4_16b_bf16 v[20:23], a[216:217], a[224:225], 0\n"
      "v_cvt_pk_bf16_f32 v40, v40, v41\n"
      "v_mfma_f32_4x4x4_16b_bf16 v[24:27], a[216:217], a[226:227], 0\n"
      "ds_write_addtid_b32 v40 offset:13312\n"
      "ds_write_addtid_b32 v15 offset:53504\n"
      "v_mfma_f32_16x16x32_bf16 v[76:79], v[204:207], a[208:211], v[76:79]\n"
      "ds_read_b128 a[200:203], v1 offset:64\n"
      "v_cvt_pk_bf16_f32 v42, v42, v43\n"
      "v_cvt_pk_bf16_f32 v20, v20, v24\n"
      "v_mfma_f32_16x16x32_bf16 v[80:83], v[196:199], a[208:211], v[80:83]\n"
      "ds_write_addtid_b32 v42 offset:13824\n"
      "ds_write_addtid_b32 v20 offset:41728\n"
      "v_mfma_f32_16x16x32_bf16 v[84:87], v[188:191], a[208:211], v[84:87]\n"
      "buffer_store_dwordx4 a[240:243], v11, s[8:11], s59 offen offset:256 sc1\n"
      "v_mfma_f32_16x16x32_bf16 v[88:91], v[200:203], a[212:215], v[88:91]\n"
      "ds_read_b128 a[204:207], v1 offset:4160\n"
      "ds_read_b64_tr_b16 a[228:229], v7 offset:51712\n"
      "v_cvt_pk_bf16_f32 v28, v28, v29\n"
      "v_mfma_f32_16x16x32_bf16 v[92:95], v[192:195], a[212:215], v[92:95]\n"
      "ds_write_addtid_b32 v28 offset:15360\n"
      "ds_read_b64_tr_b16 a[230:231], v7 offset:51720\n"
      "v_mfma_f32_16x16x32_bf16 v[96:99], v[184:187], a[212:215], v[96:99]\n"
      "v_cvt_pk_bf16_f32 v30, v30, v31\n"
      "v_cvt_pk_bf16_f32 v21, v21, v25\n"
      "v_mfma_f32_16x16x32_bf16 v[76:79], v[180:183], a[192:195], v[76:79]\n"
      "ds_read_b128 a[208:211], v1 offset:128\n"
      "ds_write_addtid_b32 v30 offset:15872\n"
      "v_fmac_f32 v32, s46, v56\n"
      "v_mfma_f32_16x16x32_bf16 v[80:83], v[172:175], a[192:195], v[80:83]\n"
      "ds_write_addtid_b32 v21 offset:45824\n"
      "v_fmac_f32 v33, s46, v57\n"
      "v_mfma_f32_16x16x32_bf16 v[84:87], v[164:167], a[192:195], v[84:87]\n"
      "s_waitcnt lgkmcnt(4)\n"
      "v_fmac_f32 v34, s46, v58\n"
      "v_cvt_pk_bf16_f32 v22, v22, v26\n"
      "v_mfma_f32_16x16x32_bf16 v[88:91], v[176:179], a[196:199], v[88:91]\n"
      "ds_read_b128 a[212:215], v1 offset:4224\n"
      "ds_write_addtid_b32 v22 offset:49920\n"
      "v_mfma_f32_16x16x32_bf16 v[92:95], v[168:171], a[196:199], v[92:95]\n"
      "s_waitcnt lgkmcnt(5)\n"
      "v_fmac_f32 v35, s46, v59\n"
      "v_sub_f32_e64 v32, v32, v44\n"
      "v_mfma_f32_4x4x4_16b_bf16 v[12:15], a[216:217], a[228:229], 0\n"
      "v_cvt_pk_bf16_f32 v23, v23, v27\n"
      "v_mfma_f32_4x4x4_16b_bf16 v[16:19], a[216:217], a[230:231], 0\n"
      "ds_write_addtid_b32 v23 offset:54016\n"
      "v_sub_f32 v33, v33, v45\n"
      "v_mfma_f32_16x16x32_bf16 v[96:99], v[160:163], a[196:199], v[96:99]\n"
      "v_sub_f32 v34, v34, v46\n"
      "v_sub_f32 v35, v35, v47\n"
      "v_cvt_pk_bf16_f32 v12, v12, v16\n"
      "v_mfma_f32_16x16x32_bf16 v[76:79], v[156:159], a[200:203], v[76:79]\n"
      "s_nop 0\n"
      "ds_read_b128 a[192:195], v2 offset:0\n"
      "ds_write_addtid_b32 v12 offset:42240\n"
      "v_mfma_f32_16x16x32_bf16 v[80:83], v[148:151], a[200:203], v[80:83]\n"
      "v_fmac_f32 v44, s45, v32\n"
      "v_cvt_pk_bf16_f32 v13, v13, v17\n"
      "v_mfma_f32_16x16x32_bf16 v[84:87], v[140:143], a[200:203], v[84:87]\n"
      "ds_read_b64_tr_b16 a[232:233], v7 offset:52224\n"
      "ds_write_addtid_b32 v13 offset:46336\n"
      "v_fmac_f32 v45, s45, v33\n"
      "v_mfma_f32_16x16x32_bf16 v[88:91], v[152:155], a[204:207], v[88:91]\n"
      "ds_read_b128 a[196:199], v2 offset:4096\n"
      "ds_read_b64_tr_b16 a[234:235], v7 offset:52232\n"
      "v_mfma_f32_16x16x32_bf16 v[92:95], v[144:147], a[204:207], v[92:95]\n"
      "v_fmac_f32 v46, s45, v34\n"
      "v_fmac_f32_e64 v47, s45, v35\n"
      "v_mfma_f32_16x16x32_bf16 v[96:99], v[136:139], a[204:207], v[96:99]\n"
      "s_waitcnt lgkmcnt(4)\n"
      "v_cvt_pk_bf16_f32 v14, v14, v18\n"
      "v_fma_f32 v32, s47, v32, v68\n"
      "v_mfma_f32_16x16x32_bf16 v[76:79], v[132:135], a[208:211], v[76:79]\n"
      "ds_read_b128 a[200:203], v2 offset:64\n"
      "ds_write_addtid_b32 v14 offset:50432\n"
      "v_mfma_f32_16x16x32_bf16 v[80:83], v[124:127], a[208:211], v[80:83]\n"
      "v_fma_f32 v33, s47, v33, v69\n"
      "v_cvt_pk_bf16_f32 v15, v15, v19\n"
      "v_mfma_f32_16x16x32_bf16 v[84:87], v[116:119], a[208:211], v[84:87]\n"
      "s_nop 0\n"
      "ds_write_addtid_b32 v15 offset:54528\n"
      "v_fma_f32 v34, s47, v34, v70\n"
      "v_mfma_f32_16x16x32_bf16 v[88:91], v[128:131], a[212:215], v[88:91]\n"
      "s_waitcnt lgkmcnt(3)\n"
      "ds_read_b128 a[204:207], v2 offset:4160\n"
      "v_fma_f32 v35, s47, v35, v71\n"
      "v_mfma_f32_4x4x4_16b_bf16 v[20:23], a[216:217], a[232:233], 0\n"
      "v_cvt_pk_bf16_f32 v44, v44, v45\n"
      "v_mfma_f32_4x4x4_16b_bf16 v[24:27], a[216:217], a[234:235], 0\n"
      "ds_write_addtid_b32 v44 offset:21508\n"
      "v_cvt_pk_bf16_f32 v46, v46, v47\n"
      "v_mfma_f32_16x16x32_bf16 v[92:95], v[120:123], a[212:215], v[92:95]\n"
      "ds_write_addtid_b32 v46 offset:22020\n"
      "v_cvt_pk_bf16_f32 v32, v32, v33\n"
      "v_cvt_pk_bf16_f32 v20, v20, v24\n"
      "v_mfma_f32_16x16x32_bf16 v[96:99], v[112:115], a[212:215], v[96:99]\n"
      "ds_write_addtid_b32 v32 offset:23556\n"
      "ds_write_addtid_b32 v20 offset:42752\n"
      "v_mfma_f32_16x16x32_bf16 v[76:79], v[108:111], a[192:195], v[76:79]\n"
      "ds_read_b128 a[208:211], v2 offset:128\n"
      "v_cvt_pk_bf16_f32 v34, v34, v35\n"
      "v_mfma_f32_16x16x32_bf16 v[80:83], v[100:103], a[192:195], v[80:83]\n"
      "ds_write_addtid_b32 v34 offset:24068\n"
      "ds_read_b64_tr_b16 a[220:221], v7 offset:52736\n"
      "v_cvt_pk_bf16_f32 v21, v21, v25\n"
      "v_fmac_f32 v36, s46, v60\n"
      "v_mfma_f32_16x16x32_bf16 v[84:87], a[160:163], a[192:195], v[84:87]\n"
      "ds_write_addtid_b32 v21 offset:46848\n"
      "ds_read_b64_tr_b16 a[222:223], v7 offset:52744\n"
      "v_mfma_f32_16x16x32_bf16 v[88:91], v[104:107], a[196:199], v[88:91]\n"
      "ds_read_b128 a[212:215], v2 offset:4224\n"
      "v_fmac_f32 v37, s46, v61\n"
      "v_mfma_f32_16x16x32_bf16 v[92:95], a[164:167], a[196:199], v[92:95]\n"
      "v_fmac_f32 v38, s46, v62\n"
      "v_cvt_pk_bf16_f32 v22, v22, v26\n"
      "v_mfma_f32_16x16x32_bf16 v[96:99], a[156:159], a[196:199], v[96:99]\n"
      "s_waitcnt lgkmcnt(3)\n"
      "ds_write_addtid_b32 v22 offset:50944\n"
      "v_fmac_f32 v39, s46, v63\n"
      "v_mfma_f32_16x16x32_bf16 v[76:79], a[152:155], a[200:203], v[76:79]\n"
      "ds_read_b128 a[192:195], v3 offset:50688\n"
      "v_sub_f32 v36, v36, v48\n"
      "v_mfma_f32_16x16x32_bf16 v[80:83], a[144:147], a[200:203], v[80:83]\n"
      "v_sub_f32 v37, v37, v49\n"
      "v_cvt_pk_bf16_f32 v23, v23, v27\n"
      "v_mfma_f32_16x16x32_bf16 v[84:87], a[136:139], a[200:203], v[84:87]\n"
      "s_waitcnt lgkmcnt(2)\n"
      "ds_write_addtid_b32 v23 offset:55040\n"
      "v_sub_f32 v38, v38, v50\n"
      "v_mfma_f32_4x4x4_16b_bf16 v[12:15], a[216:217], a[220:221], 0\n"
      "v_sub_f32 v39, v39, v51\n"
      "v_mfma_f32_4x4x4_16b_bf16 v[16:19], a[216:217], a[222:223], 0\n"
      "v_mfma_f32_16x16x32_bf16 v[88:91], a[148:151], a[204:207], v[88:91]\n"
      "ds_read_b128 a[196:199], v3 offset:50752\n"
      "v_mfma_f32_16x16x32_bf16 v[92:95], a[140:143], a[204:207], v[92:95]\n"
      "v_fmac_f32 v48, s45, v36\n"
      "v_fmac_f32 v49, s45, v37\n"
      "v_cvt_pk_bf16_f32 v12, v12, v16\n"
      "v_cvt_pk_bf16_f32 v13, v13, v17\n"
      "v_mfma_f32_16x16x32_bf16 v[96:99], a[132:135], a[204:207], v[96:99]\n"
      "ds_write_addtid_b32 v12 offset:43264\n"
      "ds_write_addtid_b32 v13 offset:47360\n"
      "v_fmac_f32 v50, s45, v38\n"
      "v_mfma_f32_16x16x32_bf16 v[76:79], a[128:131], a[208:211], v[76:79]\n"
      "ds_read_b128 a[200:203], v3 offset:50816\n"
      "ds_read_b64_tr_b16 a[224:225], v7 offset:53248\n"
      "ds_read_b64_tr_b16 a[226:227], v7 offset:53256\n"
      "v_fmac_f32 v51, s45, v39\n"
      "v_mfma_f32_16x16x32_bf16 v[80:83], a[120:123], a[208:211], v[80:83]\n"
      "v_fma_f32 v36, s47, v36, v72\n"
      "v_cvt_pk_bf16_f32 v14, v14, v18\n"
      "v_mfma_f32_16x16x32_bf16 v[84:87], a[112:115], a[208:211], v[84:87]\n"
      "ds_write_addtid_b32 v14 offset:51456\n"
      "v_fma_f32 v37, s47, v37, v73\n"
      "v_mfma_f32_16x16x32_bf16 v[88:91], a[124:127], a[212:215], v[88:91]\n"
      "ds_read_b128 a[204:207], v4 offset:50688\n"
      "v_fma_f32 v38, s47, v38, v74\n"
      "v_mfma_f32_16x16x32_bf16 v[92:95], a[116:119], a[212:215], v[92:95]\n"
      "v_fma_f32 v39, s47, v39, v75\n"
      "v_cvt_pk_bf16_f32 v15, v15, v19\n"
      "v_mfma_f32_16x16x32_bf16 v[96:99], a[108:111], a[212:215], v[96:99]\n"
      "s_waitcnt lgkmcnt(2)\n"
      "ds_write_addtid_b32 v15 offset:55552\n"
      "v_sub_f32 v76, v88, v76\n"
      "v_sub_f32 v77, v89, v77\n"
      "v_cvt_pk_bf16_f32 v48, v48, v49\n"
      "v_mfma_f32_16x16x32_bf16 v[52:55], a[0:3], a[192:195], 0\n"
      "s_nop 0\n"
      "ds_read_b128 a[208:211], v4 offset:50752\n"
      "ds_write_addtid_b32 v48 offset:17408\n"
      "ds_write_addtid_b32 v76 offset:0\n"
      "v_mfma_f32_16x16x32_bf16 v[56:59], a[4:7], a[192:195], 0\n"
      "ds_write_addtid_b32 v77 offset:512\n"
      "v_sub_f32 v78, v90, v78\n"
      "v_cvt_pk_bf16_f32 v50, v50, v51\n"
      "v_sub_f32 v79, v91, v79\n"
      "v_mfma_f32_4x4x4_16b_bf16 v[20:23], a[216:217], a[224:225], 0\n"
      "v_sub_f32_e64 v80, v92, v80\n"
      "v_mfma_f32_4x4x4_16b_bf16 v[24:27], a[216:217], a[226:227], 0\n"
      "ds_write_addtid_b32 v50 offset:17920\n"
      "ds_write_addtid_b32 v78 offset:1024\n"
      "ds_write_addtid_b32 v79 offset:1536\n"
      "v_mfma_f32_16x16x32_bf16 v[60:63], a[8:11], a[192:195], 0\n"
      "ds_write_addtid_b32 v80 offset:2048\n"
      "v_cvt_pk_bf16_f32 v20, v20, v24\n"
      "v_sub_f32_e64 v81, v93, v81\n"
      "v_mfma_f32_16x16x32_bf16 v[52:55], a[12:15], a[196:199], v[52:55]\n"
      "ds_read_b128 a[212:215], v4 offset:50816\n"
      "ds_write_addtid_b32 v20 offset:43776\n"
      "v_mfma_f32_16x16x32_bf16 v[56:59], a[16:19], a[196:199], v[56:59]\n"
      "ds_write_addtid_b32 v81 offset:2560\n"
      "v_sub_f32 v82, v94, v82\n"
      "v_mfma_f32_16x16x32_bf16 v[60:63], a[20:23], a[196:199], v[60:63]\n"
      "s_nop 0\n"
      "ds_write_addtid_b32 v82 offset:3072\n"
      "v_cvt_pk_bf16_f32 v36, v36, v37\n"
      "v_sub_f32 v83, v95, v83\n"
      "v_mfma_f32_16x16x32_bf16 v[52:55], a[24:27], a[200:203], v[52:55]\n"
      "ds_read_b128 a[192:195], v5 offset:50688\n"
      "ds_write_addtid_b32 v36 offset:19456\n"
      "v_mfma_f32_16x16x32_bf16 v[56:59], a[28:31], a[200:203], v[56:59]\n"
      "ds_write_addtid_b32 v83 offset:3584\n"
      "v_sub_f32 v84, v96, v84\n"
      "v_cvt_pk_bf16_f32 v21, v21, v25\n"
      "v_mfma_f32_16x16x32_bf16 v[60:63], a[32:35], a[200:203], v[60:63]\n"
      "s_waitcnt lgkmcnt(4)\n"
      "ds_write_addtid_b32 v21 offset:47872\n"
      "ds_write_addtid_b32 v84 offset:4096\n"
      "v_mfma_f32_16x16x32_bf16 v[52:55], a[36:39], a[204:207], v[52:55]\n"
      "ds_read_b128 a[196:199], v5 offset:50752\n"
      "v_cvt_pk_bf16_f32 v38, v38, v39\n"
      "v_mfma_f32_16x16x32_bf16 v[56:59], a[40:43], a[204:207], v[56:59]\n"
      "s_nop 0\n"
      "ds_write_addtid_b32 v38 offset:19968\n"
      "v_sub_f32 v85, v97, v85\n"
      "v_sub_f32 v86, v98, v86\n"
      "v_mfma_f32_16x16x32_bf16 v[60:63], a[44:47], a[204:207], v[60:63]\n"
      "ds_write_addtid_b32 v85 offset:4608\n"
      "ds_write_addtid_b32 v86 offset:5120\n"
      "v_sub_f32 v87, v99, v87\n"
      "v_mfma_f32_16x16x32_bf16 v[52:55], a[48:51], a[208:211], v[52:55]\n"
      "s_nop 0\n"
      "ds_read_b128 a[200:203], v5 offset:50816\n"
      "ds_write_addtid_b32 v87 offset:5632\n"
      "v_mfma_f32_16x16x32_bf16 v[56:59], a[52:55], a[208:211], v[56:59]\n"
      "v_cvt_pk_bf16_f32 v22, v22, v26\n"
      "v_cvt_pk_bf16_f32 v23, v23, v27\n"
      "v_mfma_f32_16x16x32_bf16 v[60:63], a[56:59], a[208:211], v[60:63]\n"
      "ds_write_addtid_b32 v22 offset:51968\n"
      "ds_write_addtid_b32 v23 offset:56064\n"
      "v_fma_f32 v40, 0.5, v76, -v88\n"
      "v_mfma_f32_16x16x32_bf16 v[52:55], a[60:63], a[212:215], v[52:55]\n"
      "v_fma_f32 v41, 0.5, v77, -v89\n"
      "v_fma_f32 v42, 0.5, v78, -v90\n"
      "v_mfma_f32_16x16x32_bf16 v[56:59], a[64:67], a[212:215], v[56:59]\n"
      "s_nop 0\n"
      "v_fma_f32 v43, 0.5, v79, -v91\n"
      "v_fma_f32 v44, 0.5, v80, -v92\n"
      "v_mfma_f32_16x16x32_bf16 v[60:63], a[68:71], a[212:215], v[60:63]\n"
      "s_waitcnt lgkmcnt(0)\n"
      "v_fma_f32 v45, 0.5, v81, -v93\n"
      "s_barrier\n"
      "v_mfma_f32_16x16x32_bf16 v[52:55], a[72:75], a[192:195], v[52:55]\n"
      "s_add_u32 s59, s62, 10420224\n"
      "ds_read_b128 a[204:207], v0 offset:16384\n"
      "ds_read2st64_b32 v[28:29], v6 offset0:0 offset1:2\n"
      "v_fma_f32 v46, 0.5, v82, -v94\n"
      "v_mfma_f32_16x16x32_bf16 v[56:59], a[76:79], a[192:195], v[56:59]\n"
      "s_nop 0\n"
      "ds_read_b128 a[208:211], v0 offset:20480\n"
      "ds_read2st64_b32 v[30:31], v6 offset0:4 offset1:6\n"
      "v_mfma_f32_16x16x32_bf16 v[60:63], a[80:83], a[192:195], v[60:63]\n"
      "ds_read2st64_b32 v[32:33], v6 offset0:8 offset1:10\n"
      "ds_read2st64_b32 v[34:35], v6 offset0:12 offset1:14\n"
      "v_mfma_f32_16x16x32_bf16 v[52:55], a[84:87], a[196:199], v[52:55]\n"
      "ds_read_b128 a[212:215], v0 offset:16448\n"
      "v_fma_f32 v47, 0.5, v83, -v95\n"
      "v_mfma_f32_16x16x32_bf16 v[56:59], a[88:91], a[196:199], v[56:59]\n"
      "v_fma_f32 v48, 0.5, v84, -v96\n"
      "v_fma_f32 v49, 0.5, v85, -v97\n"
      "v_mfma_f32_16x16x32_bf16 v[60:63], a[92:95], a[196:199], v[60:63]\n"
      "ds_read2st64_b32 v[36:37], v6 offset0:16 offset1:18\n"
      "ds_read2st64_b32 v[38:39], v6 offset0:20 offset1:22\n"
      "v_bfe_u32 v16, v11, 6, 1\n"
      "v_mul_i32_i24 v17, 12296, v16\n"
      "s_mov_b32 s50, -8200\n"
      "v_add3_u32 v12, v8, v17, s50\n"
      "v_add_u32 v14, 128, v12\n"
      "v_mul_i32_i24 v17, 8, v16\n"
      "s_mov_b32 s50, -4104\n"
      "v_add3_u32 v13, v8, v17, s50\n"
      "v_add_u32 v15, 128, v13\n"
      "v_mfma_f32_16x16x32_bf16 v[52:55], a[96:99], a[200:203], v[52:55]\n"
      "s_nop 0\n"
      "ds_read_b128 a[192:195], v0 offset:20544\n"
      "ds_read2st64_b32 a[232:233], v12 offset0:4 offset1:6\n"
      "v_mfma_f32_16x16x32_bf16 v[56:59], a[100:103], a[200:203], v[56:59]\n"
      "ds_read2st64_b32 a[234:235], v14 offset0:4 offset1:6\n"
      "v_fma_f32 v50, 0.5, v86, -v98\n"
      "v_fma_f32 v51, 0.5, v87, -v99\n"
      "v_mfma_f32_16x16x32_bf16 v[60:63], a[104:107], a[200:203], v[60:63]\n"
      "s_waitcnt lgkmcnt(5)\n"
      "v_fmac_f32 v28, s46, v64\n"
      "v_fmac_f32 v29, s46, v65\n"
      "v_mfma_f32_16x16x32_bf16 v[76:79], v[252:255], a[204:207], v[52:55]\n"
      "ds_read_b128 a[196:199], v0 offset:16512\n"
      "v_fmac_f32 v30, s46, v66\n"
      "v_mfma_f32_16x16x32_bf16 v[80:83], v[244:247], a[204:207], v[56:59]\n"
      "v_fmac_f32 v31, s46, v67\n"
      "v_sub_f32 v28, v28, v40\n"
      "v_mfma_f32_16x16x32_bf16 v[84:87], v[236:239], a[204:207], v[60:63]\n"
      "v_sub_f32 v29, v29, v41\n"
      "v_sub_f32 v30, v30, v42\n"
      "v_mfma_f32_16x16x32_bf16 v[88:91], v[248:251], a[208:211], v[52:55]\n"
      "ds_read_b128 a[200:203], v0 offset:20608\n"
      "v_sub_f32 v31, v31, v43\n"
      "v_mfma_f32_16x16x32_bf16 v[92:95], v[240:243], a[208:211], v[56:59]\n"
      "v_fmac_f32 v40, s45, v28\n"
      "v_mfma_f32_16x16x32_bf16 v[96:99], v[232:235], a[208:211], v[60:63]\n"
      "v_fmac_f32 v41, s45, v29\n"
      "v_mfma_f32_16x16x32_bf16 v[76:79], v[228:231], a[212:215], v[76:79]\n"
      "ds_read_b128 a[204:207], v1 offset:16384\n"
      "v_fmac_f32 v42, s45, v30\n"
      "v_mfma_f32_16x16x32_bf16 v[80:83], v[220:223], a[212:215], v[80:83]\n"
      "v_fmac_f32 v43, s45, v31\n"
      "v_mfma_f32_16x16x32_bf16 v[84:87], v[212:215], a[212:215], v[84:87]\n"
      "s_waitcnt lgkmcnt(1)\n"
      "v_fma_f32 v28, s47, v28, v52\n"
      "v_mfma_f32_16x16x32_bf16 v[88:91], v[224:227], a[192:195], v[88:91]\n"
      "ds_read_b128 a[208:211], v1 offset:20480\n"
      "v_mfma_f32_16x16x32_bf16 v[92:95], v[216:219], a[192:195], v[92:95]\n"
      "v_fma_f32 v29, s47, v29, v53\n"
      "v_mfma_f32_16x16x32_bf16 v[96:99], v[208:211], a[192:195], v[96:99]\n"
      "buffer_store_dwordx4 a[232:235], v11, s[8:11], s59 offen offset:0 sc1\n"
      "v_mfma_f32_16x16x32_bf16 v[76:79], v[204:207], a[196:199], v[76:79]\n"
      "ds_read_b128 a[212:215], v1 offset:16448\n"
      "v_fma_f32 v30, s47, v30, v54\n"
      "v_mfma_f32_16x16x32_bf16 v[80:83], v[196:199], a[196:199], v[80:83]\n"
      "ds_read2st64_b32 a[236:237], v13 offset0:4 offset1:6\n"
      "ds_read2st64_b32 a[238:239], v15 offset0:4 offset1:6\n"
      "v_mfma_f32_16x16x32_bf16 v[84:87], v[188:191], a[196:199], v[84:87]\n"
      "v_fma_f32 v31, s47, v31, v55\n"
      "v_cvt_pk_bf16_f32 v40, v40, v41\n"
      "v_mfma_f32_16x16x32_bf16 v[88:91], v[200:203], a[200:203], v[88:91]\n"
      "ds_read_b128 a[192:195], v1 offset:20544\n"
      "ds_write_addtid_b32 v40 offset:12288\n"
      "v_mfma_f32_16x16x32_bf16 v[92:95], v[192:195], a[200:203], v[92:95]\n"
      "v_cvt_pk_bf16_f32 v42, v42, v43\n"
      "v_cvt_pk_bf16_f32 v28, v28, v29\n"
      "v_mfma_f32_16x16x32_bf16 v[96:99], v[184:187], a[200:203], v[96:99]\n"
      "s_waitcnt lgkmcnt(4)\n"
      "ds_write_addtid_b32 v42 offset:12800\n"
      "ds_write_addtid_b32 v28 offset:14336\n"
      "v_mfma_f32_16x16x32_bf16 v[76:79], v[180:183], a[204:207], v[76:79]\n"
      "ds_read_b128 a[196:199], v1 offset:16512\n"
      "v_cvt_pk_bf16_f32 v30, v30, v31\n"
      "v_mfma_f32_16x16x32_bf16 v[80:83], v[172:175], a[204:207], v[80:83]\n"
      "s_nop 0\n"
      "ds_write_addtid_b32 v30 offset:14848\n"
      "v_fmac_f32 v32, s46, v68\n"
      "v_mfma_f32_16x16x32_bf16 v[84:87], v[164:167], a[204:207], v[84:87]\n"
      "v_fmac_f32 v33, s46, v69\n"
      "v_fmac_f32 v34, s46, v70\n"
      "v_mfma_f32_16x16x32_bf16 v[88:91], v[176:179], a[208:211], v[88:91]\n"
      "ds_read_b128 a[200:203], v1 offset:20608\n"
      "v_fmac_f32 v35, s46, v71\n"
      "v_mfma_f32_16x16x32_bf16 v[92:95], v[168:171], a[208:211], v[92:95]\n"
      "v_sub_f32_e64 v32, v32, v44\n"
      "v_mfma_f32_16x16x32_bf16 v[96:99], v[160:163], a[208:211], v[96:99]\n"
      "v_sub_f32 v33, v33, v45\n"
      "v_mfma_f32_16x16x32_bf16 v[76:79], v[156:159], a[212:215], v[76:79]\n"
      "ds_read_b128 a[204:207], v2 offset:16384\n"
      "v_sub_f32 v34, v34, v46\n"
      "v_mfma_f32_16x16x32_bf16 v[80:83], v[148:151], a[212:215], v[80:83]\n"
      "v_sub_f32 v35, v35, v47\n"
      "v_fmac_f32 v44, s45, v32\n"
      "v_mfma_f32_16x16x32_bf16 v[84:87], v[140:143], a[212:215], v[84:87]\n"
      "s_waitcnt lgkmcnt(1)\n"
      "v_fmac_f32 v45, s45, v33\n"
      "v_mfma_f32_16x16x32_bf16 v[88:91], v[152:155], a[192:195], v[88:91]\n"
      "buffer_store_dwordx4 a[236:239], v11, s[8:11], s59 offen offset:128 sc1\n"
      "ds_read_b128 a[208:211], v2 offset:20480\n"
      "v_mfma_f32_16x16x32_bf16 v[92:95], v[144:147], a[192:195], v[92:95]\n"
      "v_fmac_f32 v46, s45, v34\n"
      "v_fmac_f32 v47, s45, v35\n"
      "v_mfma_f32_16x16x32_bf16 v[96:99], v[136:139], a[192:195], v[96:99]\n"
      "ds_read2st64_b32 a[240:241], v8 offset0:4 offset1:6\n"
      "ds_read2st64_b32 a[242:243], v9 offset0:4 offset1:6\n"
      "v_mfma_f32_16x16x32_bf16 v[76:79], v[132:135], a[196:199], v[76:79]\n"
      "ds_read_b128 a[212:215], v2 offset:16448\n"
      "v_fma_f32 v32, s47, v32, v56\n"
      "v_mfma_f32_16x16x32_bf16 v[80:83], v[124:127], a[196:199], v[80:83]\n"
      "v_fma_f32 v33, s47, v33, v57\n"
      "v_mfma_f32_16x16x32_bf16 v[84:87], v[116:119], a[196:199], v[84:87]\n"
      "v_fma_f32 v34, s47, v34, v58\n"
      "v_fma_f32 v35, s47, v35, v59\n"
      "v_mfma_f32_16x16x32_bf16 v[88:91], v[128:131], a[200:203], v[88:91]\n"
      "ds_read_b128 a[192:195], v2 offset:20544\n"
      "v_mfma_f32_16x16x32_bf16 v[92:95], v[120:123], a[200:203], v[92:95]\n"
      "s_nop 0\n"
      "v_cvt_pk_bf16_f32 v44, v44, v45\n"
      "v_mfma_f32_16x16x32_bf16 v[96:99], v[112:115], a[200:203], v[96:99]\n"
      "s_waitcnt lgkmcnt(1)\n"
      "ds_write_addtid_b32 v44 offset:20484\n"
      "v_cvt_pk_bf16_f32 v46, v46, v47\n"
      "v_mfma_f32_16x16x32_bf16 v[76:79], v[108:111], a[204:207], v[76:79]\n"
      "ds_read_b128 a[196:199], v2 offset:16512\n"
      "ds_write_addtid_b32 v46 offset:20996\n"
      "v_mfma_f32_16x16x32_bf16 v[80:83], v[100:103], a[204:207], v[80:83]\n"
      "v_cvt_pk_bf16_f32 v32, v32, v33\n"
      "v_cvt_pk_bf16_f32 v34, v34, v35\n"
      "v_mfma_f32_16x16x32_bf16 v[84:87], a[160:163], a[204:207], v[84:87]\n"
      "ds_write_addtid_b32 v32 offset:22532\n"
      "ds_write_addtid_b32 v34 offset:23044\n"
      "v_mfma_f32_16x16x32_bf16 v[88:91], v[104:107], a[208:211], v[88:91]\n"
      "ds_read_b128 a[200:203], v2 offset:20608\n"
      "v_fmac_f32 v36, s46, v72\n"
      "v_mfma_f32_16x16x32_bf16 v[92:95], a[164:167], a[208:211], v[92:95]\n"
      "v_fmac_f32 v37, s46, v73\n"
      "v_fmac_f32 v38, s46, v74\n"
      "v_mfma_f32_16x16x32_bf16 v[96:99], a[156:159], a[208:211], v[96:99]\n"
      "v_fmac_f32 v39, s46, v75\n"
      "v_sub_f32_e64 v36, v36, v48\n"
      "v_mfma_f32_16x16x32_bf16 v[76:79], a[152:155], a[212:215], v[76:79]\n"
      "ds_read_b128 a[204:207], v3 offset:0\n"
      "v_sub_f32 v37, v37, v49\n"
      "v_mfma_f32_16x16x32_bf16 v[80:83], a[144:147], a[212:215], v[80:83]\n"
      "buffer_store_dwordx4 a[240:243], v11, s[8:11], s59 offen offset:256 sc1\n"
      "v_mfma_f32_16x16x32_bf16 v[84:87], a[136:139], a[212:215], v[84:87]\n"
      "s_waitcnt lgkmcnt(1)\n"
      "v_sub_f32 v38, v38, v50\n"
      "v_sub_f32 v39, v39, v51\n"
      "v_mfma_f32_16x16x32_bf16 v[88:91], a[148:151], a[192:195], v[88:91]\n"
      "ds_read_b128 a[208:211], v3 offset:64\n"
      "v_mfma_f32_16x16x32_bf16 v[92:95], a[140:143], a[192:195], v[92:95]\n"
      "v_fmac_f32 v48, s45, v36\n"
      "v_fmac_f32 v49, s45, v37\n"
      "v_fmac_f32 v50, s45, v38\n"
      "v_mfma_f32_16x16x32_bf16 v[96:99], a[132:135], a[192:195], v[96:99]\n"
      "v_fmac_f32 v51, s45, v39\n"
      "v_fma_f32 v36, s47, v36, v60\n"
      "v_fma_f32 v37, s47, v37, v61\n"
      "v_fma_f32 v38, s47, v38, v62\n"
      "v_fma_f32 v39, s47, v39, v63\n"
      "v_mfma_f32_16x16x32_bf16 v[76:79], a[128:131], a[196:199], v[76:79]\n"
      "ds_read_b128 a[212:215], v3 offset:128\n"
      "v_cvt_pk_bf16_f32 v48, v48, v49\n"
      "v_cvt_pk_bf16_f32 v50, v50, v51\n"
      "v_cvt_pk_bf16_f32 v36, v36, v37\n"
      "v_cvt_pk_bf16_f32 v38, v38, v39\n"
      "v_mfma_f32_16x16x32_bf16 v[80:83], a[120:123], a[196:199], v[80:83]\n"
      "ds_write_addtid_b32 v48 offset:16384\n"
      "ds_write_addtid_b32 v50 offset:16896\n"
      "v_mfma_f32_16x16x32_bf16 v[84:87], a[112:115], a[196:199], v[84:87]\n"
      "ds_write_addtid_b32 v36 offset:18432\n"
      "ds_write_addtid_b32 v38 offset:18944\n"
      "v_mfma_f32_16x16x32_bf16 v[88:91], a[124:127], a[200:203], v[88:91]\n"
      "ds_read_b128 a[192:195], v4 offset:0\n"
      "v_mfma_f32_16x16x32_bf16 v[92:95], a[116:119], a[200:203], v[92:95]\n"
      "v_mfma_f32_16x16x32_bf16 v[96:99], a[108:111], a[200:203], v[96:99]\n"
      "s_waitcnt lgkmcnt(5)\n"
      "v_mfma_f32_16x16x32_bf16 v[64:67], a[0:3], a[204:207], 0\n"
      "ds_read_b128 a[196:199], v4 offset:64\n"
      "v_mfma_f32_16x16x32_bf16 v[68:71], a[4:7], a[204:207], 0\n"
      "v_mfma_f32_16x16x32_bf16 v[72:75], a[8:11], a[204:207], 0\n"
      "v_sub_f32 v76, v88, v76\n"
      "v_sub_f32 v77, v89, v77\n"
      "v_sub_f32 v78, v90, v78\n"
      "v_sub_f32 v79, v91, v79\n"
      "v_sub_f32 v80, v92, v80\n"
      "v_sub_f32 v81, v93, v81\n"
      "v_sub_f32 v82, v94, v82\n"
      "v_mfma_f32_16x16x32_bf16 v[64:67], a[12:15], a[208:211], v[64:67]\n"
      "ds_read_b128 a[200:203], v4 offset:128\n"
      "ds_write_addtid_b32 v76 offset:6144\n"
      "ds_write_addtid_b32 v77 offset:6656\n"
      "v_sub_f32 v83, v95, v83\n"
      "v_sub_f32 v84, v96, v84\n"
      "v_sub_f32 v85, v97, v85\n"
      "v_sub_f32 v86, v98, v86\n"
      "v_mfma_f32_16x16x32_bf16 v[68:71], a[16:19], a[208:211], v[68:71]\n"
      "ds_write_addtid_b32 v78 offset:7168\n"
      "ds_write_addtid_b32 v79 offset:7680\n"
      "ds_write_addtid_b32 v80 offset:8192\n"
      "v_mfma_f32_16x16x32_bf16 v[72:75], a[20:23], a[208:211], v[72:75]\n"
      "ds_write_addtid_b32 v81 offset:8704\n"
      "ds_write_addtid_b32 v82 offset:9216\n"
      "ds_write_addtid_b32 v83 offset:9728\n"
      "v_sub_f32_e64 v87, v99, v87\n"
      "v_mfma_f32_16x16x32_bf16 v[64:67], a[24:27], a[212:215], v[64:67]\n"
      "ds_write_addtid_b32 v84 offset:10240\n"
      "ds_write_addtid_b32 v85 offset:10752\n"
      "v_mfma_f32_16x16x32_bf16 v[68:71], a[28:31], a[212:215], v[68:71]\n"
      "ds_write_addtid_b32 v86 offset:11264\n"
      "ds_write_addtid_b32 v87 offset:11776\n"
      "v_mfma_f32_16x16x32_bf16 v[72:75], a[32:35], a[212:215], v[72:75]\n"
      "s_waitcnt lgkmcnt(0)\n"
      "s_barrier\n"
      "v_mfma_f32_16x16x32_bf16 v[64:67], a[36:39], a[192:195], v[64:67]\n"
      "s_add_u32 s59, s59, 786432\n"
      "ds_read_b128 a[204:207], v5 offset:0\n"
      "ds_read2st64_b32 v[28:29], v6 offset0:24 offset1:26\n"
      "v_mfma_f32_16x16x32_bf16 v[68:71], a[40:43], a[192:195], v[68:71]\n"
      "ds_read2st64_b32 v[30:31], v6 offset0:28 offset1:30\n"
      "ds_read2st64_b32 v[32:33], v6 offset0:32 offset1:34\n"
      "v_mfma_f32_16x16x32_bf16 v[72:75], a[44:47], a[192:195], v[72:75]\n"
      "ds_read2st64_b32 v[34:35], v6 offset0:36 offset1:38\n"
      "ds_read2st64_b32 v[36:37], v6 offset0:40 offset1:42\n"
      "v_mfma_f32_16x16x32_bf16 v[64:67], a[48:51], a[196:199], v[64:67]\n"
      "ds_read_b128 a[208:211], v5 offset:64\n"
      "ds_read2st64_b32 v[38:39], v6 offset0:44 offset1:46\n"
      "v_mfma_f32_16x16x32_bf16 v[68:71], a[52:55], a[196:199], v[68:71]\n"
      "v_fma_f32 v40, 0.5, v76, -v88\n"
      "v_fma_f32 v41, 0.5, v77, -v89\n"
      "v_mfma_f32_16x16x32_bf16 v[72:75], a[56:59], a[196:199], v[72:75]\n"
      "ds_read2st64_b32 a[232:233], v12 offset0:0 offset1:2\n"
      "ds_read2st64_b32 a[234:235], v14 offset0:0 offset1:2\n"
      "v_mfma_f32_16x16x32_bf16 v[64:67], a[60:63], a[200:203], v[64:67]\n"
      "ds_read_b128 a[212:215], v5 offset:128\n"
      "v_fma_f32 v42, 0.5, v78, -v90\n"
      "v_mfma_f32_16x16x32_bf16 v[68:71], a[64:67], a[200:203], v[68:71]\n"
      "ds_read2st64_b32 a[236:237], v13 offset0:0 offset1:2\n"
      "ds_read2st64_b32 a[238:239], v15 offset0:0 offset1:2\n"
      "v_mfma_f32_16x16x32_bf16 v[72:75], a[68:71], a[200:203], v[72:75]\n"
      "s_waitcnt lgkmcnt(9)\n"
      "v_fma_f32 v43, 0.5, v79, -v91\n"
      "v_fma_f32 v44, 0.5, v80, -v92\n"
      "v_mfma_f32_16x16x32_bf16 v[64:67], a[72:75], a[204:207], v[64:67]\n"
      "s_waitcnt lgkmcnt(3)\n"
      "buffer_store_dwordx4 a[232:235], v11, s[8:11], s59 offen offset:0 sc1\n"
      "v_mfma_f32_16x16x32_bf16 v[68:71], a[76:79], a[204:207], v[68:71]\n"
      "v_fma_f32 v45, 0.5, v81, -v93\n"
      "v_fma_f32 v46, 0.5, v82, -v94\n"
      "v_mfma_f32_16x16x32_bf16 v[72:75], a[80:83], a[204:207], v[72:75]\n"
      "s_nop 0\n"
      "ds_read2st64_b32 a[240:241], v8 offset0:0 offset1:2\n"
      "ds_read2st64_b32 a[242:243], v9 offset0:0 offset1:2\n"
      "v_mfma_f32_16x16x32_bf16 v[64:67], a[84:87], a[208:211], v[64:67]\n"
      "s_waitcnt lgkmcnt(2)\n"
      "buffer_store_dwordx4 a[236:239], v11, s[8:11], s59 offen offset:128 sc1\n"
      "v_mfma_f32_16x16x32_bf16 v[68:71], a[88:91], a[208:211], v[68:71]\n"
      "v_fma_f32 v47, 0.5, v83, -v95\n"
      "v_fma_f32 v48, 0.5, v84, -v96\n"
      "v_mfma_f32_16x16x32_bf16 v[72:75], a[92:95], a[208:211], v[72:75]\n"
      "s_nop 0\n"
      "v_fma_f32 v49, 0.5, v85, -v97\n"
      "v_fma_f32 v50, 0.5, v86, -v98\n"
      "v_mfma_f32_16x16x32_bf16 v[64:67], a[96:99], a[212:215], v[64:67]\n"
      "s_waitcnt lgkmcnt(0)\n"
      "buffer_store_dwordx4 a[240:243], v11, s[8:11], s59 offen offset:256 sc1\n"
      "v_mfma_f32_16x16x32_bf16 v[68:71], a[100:103], a[212:215], v[68:71]\n"
      "v_fma_f32 v51, 0.5, v87, -v99\n"
      "v_fmac_f32 v28, s46, v52\n"
      "v_mfma_f32_16x16x32_bf16 v[72:75], a[104:107], a[212:215], v[72:75]\n"
      "v_fmac_f32 v29, s46, v53\n"
      "v_fmac_f32 v30, s46, v54\n"
      "v_fmac_f32 v31, s46, v55\n"
      "v_sub_f32 v28, v28, v40\n"
      "v_sub_f32 v29, v29, v41\n"
      "v_sub_f32 v30, v30, v42\n"
      "v_sub_f32 v31, v31, v43\n"
      "v_fmac_f32_e64 v40, s45, v28\n"
      "v_fmac_f32_e64 v41, s45, v29\n"
      "v_pk_fma_f32 v[42:43], v[30:31], s[44:45], v[42:43] op_sel:[0,1,0] op_sel_hi:[1,1,1]\n"
      "v_cvt_pk_bf16_f32 v40, v40, v41\n"
      "v_cvt_pk_bf16_f32 v42, v42, v43\n"
      "v_pk_fma_f32 v[32:33], v[56:57], s[46:47], v[32:33] op_sel:[0,0,0] op_sel_hi:[1,0,1]\n"
      "v_pk_fma_f32 v[34:35], v[58:59], s[46:47], v[34:35] op_sel:[0,0,0] op_sel_hi:[1,0,1]\n"
      "v_pk_add_f32 v[32:33], v[32:33], v[44:45] neg_lo:[0,1] neg_hi:[0,1]\n"
      "v_pk_add_f32 v[34:35], v[34:35], v[46:47] neg_lo:[0,1] neg_hi:[0,1]\n"
      "v_pk_fma_f32 v[44:45], v[32:33], s[44:45], v[44:45] op_sel:[0,1,0] op_sel_hi:[1,1,1]\n"
      "v_pk_fma_f32 v[46:47], v[34:35], s[44:45], v[46:47] op_sel:[0,1,0] op_sel_hi:[1,1,1]\n"
      "v_cvt_pk_bf16_f32 v44, v44, v45\n"
      "v_cvt_pk_bf16_f32 v46, v46, v47\n"
      "v_pk_fma_f32 v[36:37], v[60:61], s[46:47], v[36:37] op_sel:[0,0,0] op_sel_hi:[1,0,1]\n"
      "v_pk_fma_f32 v[38:39], v[62:63], s[46:47], v[38:39] op_sel:[0,0,0] op_sel_hi:[1,0,1]\n"
      "v_pk_add_f32 v[36:37], v[36:37], v[48:49] neg_lo:[0,1] neg_hi:[0,1]\n"
      "v_pk_add_f32 v[38:39], v[38:39], v[50:51] neg_lo:[0,1] neg_hi:[0,1]\n"
      "v_pk_fma_f32 v[48:49], v[36:37], s[44:45], v[48:49] op_sel:[0,1,0] op_sel_hi:[1,1,1]\n"
      "v_pk_fma_f32 v[50:51], v[38:39], s[44:45], v[50:51] op_sel:[0,1,0] op_sel_hi:[1,1,1]\n"
      "v_cvt_pk_bf16_f32 v48, v48, v49\n"
      "v_cvt_pk_bf16_f32 v50, v50, v51\n"
      "ds_write_addtid_b32 v40 offset:13312\n"
      "ds_write_addtid_b32 v42 offset:13824\n"
      "ds_write_addtid_b32 v44 offset:21508\n"
      "ds_write_addtid_b32 v46 offset:22020\n"
      "ds_write_addtid_b32 v48 offset:17408\n"
      "ds_write_addtid_b32 v50 offset:17920\n"
      "v_pk_fma_f32 v[28:29], v[28:29], s[46:47], v[64:65] op_sel:[0,1,0] op_sel_hi:[1,1,1]\n"
      "v_pk_fma_f32 v[30:31], v[30:31], s[46:47], v[66:67] op_sel:[0,1,0] op_sel_hi:[1,1,1]\n"
      "v_cvt_pk_bf16_f32 v28, v28, v29\n"
      "v_cvt_pk_bf16_f32 v30, v30, v31\n"
      "v_pk_fma_f32 v[32:33], v[32:33], s[46:47], v[68:69] op_sel:[0,1,0] op_sel_hi:[1,1,1]\n"
      "v_pk_fma_f32 v[34:35], v[34:35], s[46:47], v[70:71] op_sel:[0,1,0] op_sel_hi:[1,1,1]\n"
      "v_cvt_pk_bf16_f32 v32, v32, v33\n"
      "v_cvt_pk_bf16_f32 v34, v34, v35\n"
      "v_pk_fma_f32 v[36:37], v[36:37], s[46:47], v[72:73] op_sel:[0,1,0] op_sel_hi:[1,1,1]\n"
      "v_pk_fma_f32 v[38:39], v[38:39], s[46:47], v[74:75] op_sel:[0,1,0] op_sel_hi:[1,1,1]\n"
      "v_cvt_pk_bf16_f32 v36, v36, v37\n"
      "v_cvt_pk_bf16_f32 v38, v38, v39\n"
      "ds_write_addtid_b32 v28 offset:15360\n"
      "ds_write_addtid_b32 v30 offset:15872\n"
      "ds_write_addtid_b32 v32 offset:23556\n"
      "ds_write_addtid_b32 v34 offset:24068\n"
      "ds_write_addtid_b32 v36 offset:19456\n"
      "ds_write_addtid_b32 v38 offset:19968\n"
      "s_waitcnt lgkmcnt(0)\n"
      "s_barrier\n"
      "ds_read2st64_b32 a[232:233], v12 offset0:4 offset1:6\n"
      "ds_read2st64_b32 a[234:235], v14 offset0:4 offset1:6\n"
      "ds_read2st64_b32 a[236:237], v13 offset0:4 offset1:6\n"
      "ds_read2st64_b32 a[238:239], v15 offset0:4 offset1:6\n"
      "ds_read2st64_b32 a[240:241], v8 offset0:4 offset1:6\n"
      "ds_read2st64_b32 a[242:243], v9 offset0:4 offset1:6\n"
      "s_add_u32 s59, s59, 786432\n"
      "s_waitcnt lgkmcnt(4)\n"
      "buffer_store_dwordx4 a[232:235], v11, s[8:11], s59 offen offset:0 sc1\n"
      "s_waitcnt lgkmcnt(2)\n"
      "buffer_store_dwordx4 a[236:239], v11, s[8:11], s59 offen offset:128 sc1\n"
      "s_waitcnt lgkmcnt(0)\n"
      "buffer_store_dwordx4 a[240:243], v11, s[8:11], s59 offen offset:256 sc1\n"
      "LSend_%=:\n"
      "Lfin_%=:\n"
      "s_waitcnt vmcnt(0) lgkmcnt(0)\n"
      "s_endpgm\n"
      ".rept 512\n"
      "s_nop 0\n"
      ".endr\n"
      ".Lwg_end:\n"
      : "+{v0}"(tid)
      : "{s[0:1]}"(__builtin_amdgcn_kernarg_segment_ptr()), "{s2}"(blockIdx.x)
      : "v1", "v2", "v3", "v4", "v5", "v6", "v7", "v8", "v9", "v10", "v11", "v12", "v13", "v14", "v15", "v16", "v17", "v18", "v19", "v20", "v21", "v22", "v23", "v24", "v25", "v26", "v27", "v28", "v29", "v30", "v31", "v32", "v33", "v34", "v35", "v36", "v37", "v38", "v39", "v40", "v41", "v42", "v43", "v44", "v45", "v46", "v47", "v48", "v49", "v50", "v51", "v52", "v53", "v54", "v55", "v56", "v57", "v58", "v59", "v60", "v61", "v62", "v63", "v64", "v65", "v66", "v67", "v68", "v69", "v70", "v71", "v72", "v73", "v74", "v75", "v76", "v77", "v78", "v79", "v80", "v81", "v82", "v83", "v84", "v85", "v86", "v87", "v88", "v89", "v90", "v91", "v92", "v93", "v94", "v95", "v96", "v97", "v98", "v99", "v100", "v101", "v102", "v103", "v104", "v105", "v106", "v107", "v108", "v109", "v110", "v111", "v112", "v113", "v114", "v115", "v116", "v117", "v118", "v119", "v120", "v121", "v122", "v123", "v124", "v125", "v126", "v127", "v128", "v129", "v130", "v131", "v132", "v133", "v134", "v135", "v136", "v137", "v138", "v139", "v140", "v141", "v142", "v143", "v144", "v145", "v146", "v147", "v148", "v149", "v150", "v151", "v152", "v153", "v154", "v155", "v156", "v157", "v158", "v159", "v160", "v161", "v162", "v163", "v164", "v165", "v166", "v167", "v168", "v169", "v170", "v171", "v172", "v173", "v174", "v175", "v176", "v177", "v178", "v179", "v180", "v181", "v182", "v183", "v184", "v185", "v186", "v187", "v188", "v189", "v190", "v191", "v192", "v193", "v194", "v195", "v196", "v197", "v198", "v199", "v200", "v201", "v202", "v203", "v204", "v205", "v206", "v207", "v208", "v209", "v210", "v211", "v212", "v213", "v214", "v215", "v216", "v217", "v218", "v219", "v220", "v221", "v222", "v223", "v224", "v225", "v226", "v227", "v228", "v229", "v230", "v231", "v232", "v233", "v234", "v235", "v236", "v237", "v238", "v239", "v240", "v241", "v242", "v243", "v244", "v245", "v246", "v247", "v248", "v249", "v250", "v251", "v252", "v253", "v254", "v255", "a0", "a1", "a2", "a3", "a4", "a5", "a6", "a7", "a8", "a9", "a10", "a11", "a12", "a13", "a14", "a15", "a16", "a17", "a18", "a19", "a20", "a21", "a22", "a23", "a24", "a25", "a26", "a27", "a28", "a29", "a30", "a31", "a32", "a33", "a34", "a35", "a36", "a37", "a38", "a39", "a40", "a41", "a42", "a43", "a44", "a45", "a46", "a47", "a48", "a49", "a50", "a51", "a52", "a53", "a54", "a55", "a56", "a57", "a58", "a59", "a60", "a61", "a62", "a63", "a64", "a65", "a66", "a67", "a68", "a69", "a70", "a71", "a72", "a73", "a74", "a75", "a76", "a77", "a78", "a79", "a80", "a81", "a82", "a83", "a84", "a85", "a86", "a87", "a88", "a89", "a90", "a91", "a92", "a93", "a94", "a95", "a96", "a97", "a98", "a99", "a100", "a101", "a102", "a103", "a104", "a105", "a106", "a107", "a108", "a109", "a110", "a111", "a112", "a113", "a114", "a115", "a116", "a117", "a118", "a119", "a120", "a121", "a122", "a123", "a124", "a125", "a126", "a127", "a128", "a129", "a130", "a131", "a132", "a133", "a134", "a135", "a136", "a137", "a138", "a139", "a140", "a141", "a142", "a143", "a144", "a145", "a146", "a147", "a148", "a149", "a150", "a151", "a152", "a153", "a154", "a155", "a156", "a157", "a158", "a159", "a160", "a161", "a162", "a163", "a164", "a165", "a166", "a167", "a168", "a169", "a170", "a171", "a172", "a173", "a174", "a175", "a176", "a177", "a178", "a179", "a180", "a181", "a182", "a183", "a184", "a185", "a186", "a187", "a188", "a189", "a190", "a191", "a192", "a193", "a194", "a195", "a196", "a197", "a198", "a199", "a200", "a201", "a202", "a203", "a204", "a205", "a206", "a207", "a208", "a209", "a210", "a211", "a212", "a213", "a214", "a215", "a216", "a217", "a218", "a219", "a220", "a221", "a222", "a223", "a224", "a225", "a226", "a227", "a228", "a229", "a230", "a231", "a232", "a233", "a234", "a235", "a236", "a237", "a238", "a239", "a240", "a241", "a242", "a243", "a244", "a245", "a246", "a247", "a248", "a249", "a250", "a251", "a252", "a253", "a254", "a255", "s3", "s4", "s5", "s6", "s7", "s8", "s9", "s10", "s11", "s12", "s13", "s14", "s15", "s16", "s17", "s18", "s19", "s20", "s21", "s22", "s23", "s24", "s25", "s26", "s27", "s28", "s29", "s30", "s31", "s33", "s34", "s35", "s36", "s37", "s38", "s39", "s40", "s41", "s42", "s43", "s44", "s45", "s46", "s47", "s48", "s49", "s50", "s51", "s52", "s53", "s54", "s55", "s56", "s57", "s58", "s59", "s60", "s61", "s62", "s63", "s64", "s65", "s66", "s67", "s68", "s69", "s70", "s71", "s72", "s73", "s74", "s75", "s76", "s77", "s78", "s79", "s80", "s81", "s82", "s83", "s84", "s85", "s86", "s87", "s88", "s89", "s90", "s91", "s92", "s93", "s94", "s95", "s96", "s97", "s98", "s99", "scc", "vcc", "memory");
  asm volatile("" :: "v"((unsigned)(uintptr_t)smem) : "memory");
}

extern "C" void kernel_launch(void* const* d_in, const int* in_sizes, int n_in, void* d_out,
                              int out_size, void* d_ws, size_t ws_size, hipStream_t stream) {
  conv_kernel<<<256, 256, 0, stream>>>((const char*)d_in[0], (const char*)d_in[1], (char*)d_out, (char*)d_ws);
}
```

```cpp
#include <hip/hip_runtime.h>
#include <hip/hip_bf16.h>
#include <stdint.h>

__global__ void __launch_bounds__(256, 1)
conv_kernel(const char* __restrict__ x, const char* __restrict__ w, char* __restrict__ out, char* __restrict__ dbg) {
  __shared__ __attribute__((aligned(1024))) char smem[163840];
  unsigned tid = threadIdx.x;
  asm volatile(
      "s_load_dwordx2 s[12:13], s[0:1], 8\n"
      "s_load_dwordx2 s[4:5], s[0:1], 0\n"
      "s_load_dwordx2 s[8:9], s[0:1], 16\n"
      "v_readfirstlane_b32 s16, v0\n"
      "s_getpc_b64 s[26:27]\n"
      ".Lwg_pc:\n"
      "v_and_b32 v37, 3, v0\n"
      "s_lshr_b32 s23, s2, 3\n"
      "s_lshl_b32 s28, s16, 3\n"
      "v_lshlrev_b32 v40, 7, v37\n"
      "s_lshl_b32 s29, s23, 11\n"
      "s_mov_b32 m0, 150016\n"
      "s_add_u32 s28, s28, s29\n"
      "v_and_b32 v36, 63, v0\n"
      "v_add_u32 v40, s28, v40\n"
      "s_lshr_b32 s16, s16, 6\n"
      "v_min_u32 v40, .Lwg_end-.Lwg_pc-4, v40\n"
      "s_and_b32 s18, s16, 1\n"
      "global_load_lds_dword v40, s[26:27]\n"
      "v_lshlrev_b32 v17, 4, v36\n"
      "s_lshr_b32 s17, s16, 1\n"
      "s_bfe_u32 s28, s2, 0x20001\n"
      "s_mul_i32 s19, s28, 12582912\n"
      "s_and_b32 s28, s23, 1\n"
      "s_mul_i32 s20, s28, 98304\n"
      "s_lshr_b32 s28, s23, 1\n"
      "s_mul_i32 s29, s28, 3072\n"
      "s_bitcmp1_b32 s2, 0\n"
      "s_cselect_b32 s28, 49152, 0\n"
      "s_add_u32 s21, s20, s29\n"
      "s_add_u32 s21, s21, s28\n"
      "s_add_u32 s6, s19, 12783616\n"
      "s_min_u32 s6, s6, 50335744\n"
      "s_mov_b32 s7, 0x20000\n"
      "s_mov_b32 s10, 50331648\n"
      "s_mov_b32 s11, 0x20000\n"
      "s_mov_b32 s14, 165888\n"
      "s_mov_b32 s15, 0x20000\n"
      "s_mul_i32 s63, s18, 3072\n"
      "s_add_u32 s54, s19, s20\n"
      "s_sub_u32 s54, s54, 196608\n"
      "s_lshl_b32 s64, s18, 8\n"
      "s_mul_i32 s28, s17, 24832\n"
      "s_add_u32 s64, s64, s28\n"
      "s_mov_b32 s55, 0\n"
      "v_bitop3_b32 v18, v36, v17, 32 bitop3:0x6c\n"
      "v_xor_b32 v19, 32, v17\n"
      "v_bfe_u32 v38, v36, 2, 2\n"
      "v_lshrrev_b32 v39, 4, v36\n"
      "s_cmp_eq_u32 s17, 1\n"
      "s_cbranch_scc1 LBD_%=\n"
      "v_lshlrev_b32 v40, 3, v17\n"
      "s_mul_i32 s53, s18, 2688\n"
      "s_mul_i32 s34, s23, 5184\n"
      "v_min_u32 v40, 2560, v40\n"
      "s_add_u32 s53, s53, s34\n"
      "v_lshlrev_b32 v43, 4, v37\n"
      "s_mul_i32 s26, s17, 0x8080\n"
      "s_sub_u32 s26, 0xbf80, s26\n"
      "s_mov_b32 s27, 0\n"
      "s_lshl_b32 s34, s17, 7\n"
      "s_mov_b32 s29, 0\n"
      "s_sub_u32 s28, s26, s34\n"
      "s_mov_b32 s31, 0\n"
      "s_sub_u32 s30, s28, s34\n"
      "v_lshlrev_b64 v[44:45], v43, s[26:27]\n"
      "v_lshlrev_b64 v[46:47], v43, s[28:29]\n"
      "s_xor_b32 s28, s28, 0x8000\n"
      "v_lshlrev_b64 v[48:49], v43, s[30:31]\n"
      "v_lshlrev_b64 v[50:51], v43, s[28:29]\n"
      "v_accvgpr_write_b32 a244, v44\n"
      "v_accvgpr_write_b32 a245, v45\n"
      "v_accvgpr_write_b32 a246, v46\n"
      "v_accvgpr_write_b32 a247, v47\n"
      "v_accvgpr_write_b32 a248, v48\n"
      "v_accvgpr_write_b32 a249, v49\n"
      "v_accvgpr_write_b32 a250, v50\n"
      "v_accvgpr_write_b32 a251, v51\n"
      "s_waitcnt lgkmcnt(0)\n"
      "s_and_b32 s13, s13, 0xffff\n"
      "buffer_load_dword v40, s[12:15], s53 offen lds\n"
      "s_add_u32 m0, s63, 30976\n"
      "s_add_u32 s52, s63, 55296\n"
      "buffer_load_dwordx4 v17, s[12:15], s52 offen offset:0 lds\n"
      "buffer_load_dwordx4 v18, s[12:15], s52 offen offset:1024 lds\n"
      "buffer_load_dwordx4 v19, s[12:15], s52 offen offset:2048 lds\n"
      "s_add_u32 m0, s63, 140800\n"
      "s_add_u32 s52, s63, 110592\n"
      "buffer_load_dwordx4 v17, s[12:15], s52 offen offset:0 lds\n"
      "buffer_load_dwordx4 v18, s[12:15], s52 offen offset:1024 lds\n"
      "buffer_load_dwordx4 v19, s[12:15], s52 offen offset:2048 lds\n"
      "s_branch LS_%=\n"
      "LBD_%=:\n"
      "s_sub_u32 s26, s21, s20\n"
      "s_lshl_b32 s37, s18, 11\n"
      "s_add_u32 s26, s26, s37\n"
      "s_add_u32 s27, s26, 3712\n"
      "v_add_u32 v44, s27, v17\n"
      "s_cmp_eq_u32 s18, 1\n"
      "s_cselect_b32 s28, 16, 48\n"
      "s_cselect_b32 s29, 0, 32\n"
      "s_cselect_b32 s27, 48, 64\n"
      "v_cmp_le_u32 vcc, s28, v36\n"
      "v_cmp_gt_u32 s[30:31], s29, v36\n"
      "v_xor_b32 v45, 32, v44\n"
      "s_cmp_eq_u32 s26, 0\n"
      "s_cselect_b32 s28, 24, 0\n"
      "v_cndmask_b32 v47, v44, v45, vcc\n"
      "v_cndmask_b32 v46, v44, v45, s[30:31]\n"
      "v_cmp_le_u32 vcc, s28, v36\n"
      "v_cmp_gt_u32 s[30:31], s27, v36\n"
      "s_cmp_eq_u32 s26, 97280\n"
      "s_cselect_b32 s27, 24, 64\n"
      "v_cmp_gt_u32 s[34:35], s27, v36\n"
      "v_subrev_u32 v45, 256, v44\n"
      "v_cndmask_b32 v8, 2.0, v47, vcc\n"
      "v_cndmask_b32 v48, v45, v46, s[30:31]\n"
      "v_cndmask_b32 v9, 2.0, v48, s[34:35]\n"
      "s_add_u32 s34, s19, s21\n"
      "s_cmp_eq_u32 s19, 0\n"
      "s_cselect_b32 s36, 0x80000000, s54\n"
      "s_waitcnt lgkmcnt(0)\n"
      "s_load_dword s44, s[4:5], s34\n"
      "s_and_b32 s13, s13, 0xffff\n"
      "s_add_u32 m0, s63, 24832\n"
      "s_add_u32 s52, s63, 0\n"
      "buffer_load_dwordx4 v17, s[12:15], s52 offen offset:0 lds\n"
      "buffer_load_dwordx4 v18, s[12:15], s52 offen offset:1024 lds\n"
      "buffer_load_dwordx4 v19, s[12:15], s52 offen offset:2048 lds\n"
      "s_add_u32 s4, s4, -4096\n"
      "s_addc_u32 s5, s5, -1\n"
      "s_and_b32 s5, s5, 0xffff\n"
      "s_add_u32 s53, s54, 196608\n"
      "s_add_u32 m0, s37, 82432\n"
      "s_nop 0\n"
      "buffer_load_dwordx4 v8, s[4:7], s53 offen offset:0 nt lds\n"
      "buffer_load_dwordx4 v9, s[4:7], s53 offen offset:1024 nt lds\n"
      "s_add_u32 s53, s54, 393216\n"
      "s_add_u32 m0, s37, 86536\n"
      "s_nop 0\n"
      "buffer_load_dwordx4 v8, s[4:7], s53 offen offset:0 nt lds\n"
      "buffer_load_dwordx4 v9, s[4:7], s53 offen offset:1024 nt lds\n"
      "s_add_u32 s53, s54, 589824\n"
      "s_add_u32 m0, s37, 90760\n"
      "s_nop 0\n"
      "buffer_load_dwordx4 v8, s[4:7], s53 offen offset:0 nt lds\n"
      "buffer_load_dwordx4 v9, s[4:7], s53 offen offset:1024 nt lds\n"
      "s_add_u32 s53, s54, 786432\n"
      "s_add_u32 m0, s37, 95104\n"
      "s_nop 0\n"
      "buffer_load_dwordx4 v8, s[4:7], s53 offen offset:0 nt lds\n"
      "buffer_load_dwordx4 v9, s[4:7], s53 offen offset:1024 nt lds\n"
      "s_add_u32 s53, s36, 0\n"
      "s_add_u32 m0, s37, 150272\n"
      "s_nop 0\n"
      "buffer_load_dwordx4 v8, s[4:7], s53 offen offset:0 nt lds\n"
      "buffer_load_dwordx4 v9, s[4:7], s53 offen offset:1024 nt lds\n"
      "v_lshlrev_b32 v43, 4, v37\n"
      "v_lshlrev_b32 v44, 2, v37\n"
      "v_and_b32 v43, 16, v43\n"
      "v_and_or_b32 v43, v44, 8, v43\n"
      "v_mul_u32_u24 v44, 192, v38\n"
      "v_mul_u32_u24 v45, 1536, v39\n"
      "s_lshl_b32 s27, s18, 5\n"
      "v_lshlrev_b32 v46, 1, v36\n"
      "v_add3_u32 v43, v43, v44, v45\n"
      "v_bitop3_b32 v46, v46, s27, 32 bitop3:0x6c\n"
      "v_add_u32 v60, v43, v46\n"
      "v_add_u32 v61, 57344, v60\n"
      "v_add_u32 v62, 114688, v60\n"
      "v_lshlrev_b32 v43, 4, v37\n"
      "s_mul_i32 s26, s17, 0x8080\n"
      "s_sub_u32 s26, 0xbf80, s26\n"
      "s_mov_b32 s27, 0\n"
      "s_lshl_b32 s34, s17, 7\n"
      "s_mov_b32 s29, 0\n"
      "s_sub_u32 s28, s26, s34\n"
      "s_mov_b32 s31, 0\n"
      "s_sub_u32 s30, s28, s34\n"
      "v_lshlrev_b64 v[44:45], v43, s[26:27]\n"
      "v_lshlrev_b64 v[46:47], v43, s[28:29]\n"
      "s_xor_b32 s28, s28, 0x8000\n"
      "v_lshlrev_b64 v[48:49], v43, s[30:31]\n"
      "v_lshlrev_b64 v[50:51], v43, s[28:29]\n"
      "v_accvgpr_write_b32 a244, v44\n"
      "v_accvgpr_write_b32 a245, v45\n"
      "v_accvgpr_write_b32 a246, v46\n"
      "v_accvgpr_write_b32 a247, v47\n"
      "v_accvgpr_write_b32 a248, v48\n"
      "v_accvgpr_write_b32 a249, v49\n"
      "v_accvgpr_write_b32 a250, v50\n"
      "v_accvgpr_write_b32 a251, v51\n"
      "s_lshl_b32 s26, s18, 3\n"
      "v_and_b32 v42, 15, v36\n"
      "v_xor_b32 v42, s26, v42\n"
      "s_lshl_b32 s26, s17, 13\n"
      "s_add_u32 s26, s26, 24832\n"
      "s_mul_i32 s27, s17, 12672\n"
      "v_mul_u32_u24 v44, 192, v42\n"
      "v_lshlrev_b32 v43, 3, v42\n"
      "v_and_b32 v45, 48, v36\n"
      "s_add_u32 s27, s27, 82432\n"
      "v_bitop3_b32 v46, v43, v45, 32 bitop3:0x6c\n"
      "v_add3_u32 v0, v46, v44, s26\n"
      "v_add3_u32 v3, v46, v44, s27\n"
      "v_add_u32 v43, 16, v43\n"
      "v_add_u32 v44, 384, v44\n"
      "v_bitop3_b32 v46, v43, v45, 32 bitop3:0x6c\n"
      "v_add3_u32 v1, v46, v44, s26\n"
      "v_add3_u32 v4, v46, v44, s27\n"
      "v_add_u32 v43, 16, v43\n"
      "v_add_u32 v44, 384, v44\n"
      "v_bitop3_b32 v46, v43, v45, 32 bitop3:0x6c\n"
      "v_add3_u32 v2, v46, v44, s26\n"
      "v_add3_u32 v5, v46, v44, s27\n"
      "v_lshlrev_b32 v43, 31, v36\n"
      "v_and_b32 v44, 2, v36\n"
      "v_or_b32 v43, 0x3f803f80, v43\n"
      "v_mul_u32_u24 v45, 0xc00080, v44\n"
      "v_lshl_add_u32 v44, v44, 22, v43\n"
      "v_add_u32 v45, v45, v43\n"
      "v_accvgpr_write_b32 a216, v44\n"
      "v_accvgpr_write_b32 a217, v45\n"
      "v_lshlrev_b32 v43, 3, v38\n"
      "s_mov_b32 s26, 806420736\n"
      "v_bfe_u32 v43, s26, v43, 8\n"
      "v_lshlrev_b32 v44, 12, v38\n"
      "s_lshl_b32 s28, s18, 8\n"
      "s_add_u32 s28, s28, 82432\n"
      "v_lshl_add_u32 v43, v43, 3, v44\n"
      "v_lshl_add_u32 v44, v39, 2, v37\n"
      "v_lshl_add_u32 v43, v44, 4, v43\n"
      "v_add_u32 v7, s28, v43\n"
      "s_mul_i32 s26, s17, 70144\n"
      "s_lshl_b32 s27, s18, 8\n"
      "s_sub_u32 s26, 70144, s26\n"
      "s_add_u32 s26, s26, s27\n"
      "v_lshl_add_u32 v6, v36, 2, s26\n"
      "v_lshrrev_b32 v43, 3, v36\n"
      "v_and_b32 v44, 7, v36\n"
      "v_mul_u32_u24 v45, 384, v43\n"
      "v_lshl_add_u32 v16, v44, 4, v45\n"
      "v_and_b32 v44, 2, v36\n"
      "v_lshlrev_b32 v43, 3, v43\n"
      "v_lshlrev_b32 v45, 4, v44\n"
      "v_xor_b32 v43, v43, v45\n"
      "v_lshl_add_u32 v43, v44, 7, v43\n"
      "v_and_b32 v44, 1, v36\n"
      "s_lshl_b32 s26, s18, 11\n"
      "v_lshl_add_u32 v43, v44, 6, v43\n"
      "s_add_u32 s26, s26, 12288\n"
      "v_bfe_u32 v44, v36, 2, 1\n"
      "v_add_u32 v43, s26, v43\n"
      "v_mul_i32_i24 v45, 8196, v44\n"
      "s_mov_b32 s27, 0\n"
      "v_add3_u32 v10, v43, v45, s27\n"
      "v_add_u32 v13, 128, v10\n"
      "v_mul_i32_i24 v45, -4092, v44\n"
      "s_mov_b32 s27, 4096\n"
      "v_add3_u32 v11, v43, v45, s27\n"
      "v_add_u32 v14, 128, v11\n"
      "v_mul_i32_i24 v45, -4100, v44\n"
      "s_mov_b32 s27, 8200\n"
      "v_add3_u32 v12, v43, v45, s27\n"
      "v_add_u32 v15, 128, v12\n"
      "s_mul_i32 s62, s18, 393216\n"
      "s_add_u32 s62, s62, s19\n"
      "s_add_u32 s62, s62, s21\n"
      "s_mov_b32 s40, 0x80000000\n"
      "s_and_b32 s9, s9, 0xffff\n"
      "s_mov_b32 s45, 0xbf2aaaab\n"
      "s_mov_b32 s46, 0xbe800000\n"
      "s_mov_b32 s47, 0x3f2aaaab\n"
      "s_mov_b32 s48, 0xc0c00000\n"
      "s_mov_b32 s49, 0x3f000000\n"
      "s_add_u32 m0, s63, 0\n"
      "s_add_u32 s52, s63, 6144\n"
      "buffer_load_dwordx4 v17, s[12:15], s52 offen offset:0 lds\n"
      "buffer_load_dwordx4 v18, s[12:15], s52 offen offset:1024 lds\n"
      "buffer_load_dwordx4 v19, s[12:15], s52 offen offset:2048 lds\n"
      "LDpro_%=:\n"
      "s_waitcnt vmcnt(13)\n"
      "s_barrier\n"
      "ds_read_b64_tr_b16 a[0:1], v60 offset:24832\n"
      "ds_read_b64_tr_b16 a[2:3], v60 offset:25600\n"
      "ds_read_b64_tr_b16 a[224:225], v62 offset:26112\n"
      "ds_read_b64_tr_b16 a[226:227], v62 offset:26880\n"
      "ds_read_b64_tr_b16 a[220:221], v60 offset:30976\n"
      "ds_read_b64_tr_b16 a[222:223], v60 offset:31744\n"
      "ds_read_b64_tr_b16 a[4:5], v60 offset:24896\n"
      "ds_read_b64_tr_b16 a[6:7], v60 offset:25664\n"
      "ds_read_b64_tr_b16 a[232:233], v62 offset:26176\n"
      "ds_read_b64_tr_b16 a[234:235], v62 offset:26944\n"
      "ds_read_b64_tr_b16 a[228:229], v60 offset:31040\n"
      "ds_read_b64_tr_b16 a[230:231], v60 offset:31808\n"
      "s_add_u32 m0, s63, 63744\n"
      "s_add_u32 s52, s63, 122880\n"
      "s_waitcnt lgkmcnt(10)\n"
      "v_mfma_f32_4x4x4_16b_bf16 v[28:31], a[244:245], a[0:1], 0\n"
      "v_mfma_f32_4x4x4_16b_bf16 v[32:35], a[244:245], a[2:3], 0\n"
      "buffer_load_dwordx4 v17, s[12:15], s52 offen offset:0 lds\n"
      "s_waitcnt lgkmcnt(8)\n"
      "v_mfma_f32_4x4x4_16b_bf16 v[28:31], a[248:249], a[224:225], v[28:31]\n"
      "ds_read_b64_tr_b16 a[8:9], v60 offset:24960\n"
      "v_mfma_f32_4x4x4_16b_bf16 v[32:35], a[248:249], a[226:227], v[32:35]\n"
      "buffer_load_dwordx4 v18, s[12:15], s52 offen offset:1024 lds\n"
      "s_waitcnt lgkmcnt(7)\n"
      "v_mfma_f32_4x4x4_16b_bf16 v[36:39], a[246:247], a[220:221], v[28:31]\n"
      "ds_read_b64_tr_b16 a[10:11], v60 offset:25728\n"
      "v_mfma_f32_4x4x4_16b_bf16 v[40:43], a[246:247], a[222:223], v[32:35]\n"
      "buffer_load_dwordx4 v19, s[12:15], s52 offen offset:2048 lds\n"
      "v_mfma_f32_4x4x4_16b_bf16 v[28:31], a[250:251], a[220:221], v[28:31]\n"
      "ds_read_b64_tr_b16 a[240:241], v62 offset:26240\n"
      "v_mfma_f32_4x4x4_16b_bf16 v[32:35], a[250:251], a[222:223], v[32:35]\n"
      "ds_read_b64_tr_b16 a[242:243], v62 offset:27008\n"
      "ds_read_b64_tr_b16 a[236:237], v60 offset:31104\n"
      "ds_read_b64_tr_b16 a[238:239], v60 offset:31872\n"
      "s_add_u32 m0, s63, 154368\n"
      "s_add_u32 s52, s63, 129024\n"
      "s_nop 0\n"
      "s_waitcnt lgkmcnt(5)\n"
      "v_mfma_f32_4x4x4_16b_bf16 v[44:47], a[244:245], a[4:5], 0\n"
      "v_mfma_f32_4x4x4_16b_bf16 v[48:51], a[244:245], a[6:7], 0\n"
      "buffer_load_dwordx4 v17, s[12:15], s52 offen offset:0 lds\n"
      "v_mfma_f32_4x4x4_16b_bf16 v[44:47], a[248:249], a[232:233], v[44:47]\n"
      "buffer_load_dwordx4 v18, s[12:15], s52 offen offset:1024 lds\n"
      "v_mfma_f32_4x4x4_16b_bf16 v[48:51], a[248:249], a[234:235], v[48:51]\n"
      "buffer_load_dwordx4 v19, s[12:15], s52 offen offset:2048 lds\n"
      "v_mfma_f32_4x4x4_16b_bf16 v[52:55], a[246:247], a[228:229], v[44:47]\n"
      "v_mfma_f32_4x4x4_16b_bf16 v[56:59], a[246:247], a[230:231], v[48:51]\n"
      "v_mfma_f32_4x4x4_16b_bf16 v[44:47], a[250:251], a[228:229], v[44:47]\n"
      "v_mfma_f32_4x4x4_16b_bf16 v[48:51], a[250:251], a[230:231], v[48:51]\n"
      "v_cvt_pk_bf16_f32 v252, v36, v40\n"
      "v_cvt_pk_bf16_f32 v253, v37, v41\n"
      "v_cvt_pk_bf16_f32 v254, v38, v42\n"
      "v_cvt_pk_bf16_f32 v255, v39, v43\n"
      "v_cvt_pk_bf16_f32 v248, v28, v32\n"
      "v_cvt_pk_bf16_f32 v249, v29, v33\n"
      "v_cvt_pk_bf16_f32 v250, v30, v34\n"
      "v_cvt_pk_bf16_f32 v251, v31, v35\n"
      "s_nop 0\n"
      "s_waitcnt lgkmcnt(0)\n"
      "v_mfma_f32_4x4x4_16b_bf16 v[28:31], a[244:245], a[8:9], 0\n"
      "v_mfma_f32_4x4x4_16b_bf16 v[32:35], a[244:245], a[10:11], 0\n"
      "s_nop 0\n"
      "v_mfma_f32_4x4x4_16b_bf16 v[28:31], a[248:249], a[240:241], v[28:31]\n"
      "v_mfma_f32_4x4x4_16b_bf16 v[32:35], a[248:249], a[242:243], v[32:35]\n"
      "s_nop 0\n"
      "v_mfma_f32_4x4x4_16b_bf16 v[36:39], a[246:247], a[236:237], v[28:31]\n"
      "v_mfma_f32_4x4x4_16b_bf16 v[40:43], a[246:247], a[238:239], v[32:35]\n"
      "v_mfma_f32_4x4x4_16b_bf16 v[28:31], a[250:251], a[236:237], v[28:31]\n"
      "v_mfma_f32_4x4x4_16b_bf16 v[32:35], a[250:251], a[238:239], v[32:35]\n"
      "v_cvt_pk_bf16_f32 v244, v52, v56\n"
      "v_cvt_pk_bf16_f32 v245, v53, v57\n"
      "v_cvt_pk_bf16_f32 v246, v54, v58\n"
      "v_cvt_pk_bf16_f32 v247, v55, v59\n"
      "v_cvt_pk_bf16_f32 v240, v44, v48\n"
      "v_cvt_pk_bf16_f32 v241, v45, v49\n"
      "v_cvt_pk_bf16_f32 v242, v46, v50\n"
      "v_cvt_pk_bf16_f32 v243, v47, v51\n"
      "s_waitcnt vmcnt(6)\n"
      "s_barrier\n"
      "ds_read_b64_tr_b16 a[12:13], v60 offset:0\n"
      "ds_read_b64_tr_b16 a[14:15], v60 offset:768\n"
      "ds_read_b64_tr_b16 a[224:225], v60 offset:12288\n"
      "ds_read_b64_tr_b16 a[226:227], v60 offset:13056\n"
      "ds_read_b64_tr_b16 a[220:221], v60 offset:6144\n"
      "ds_read_b64_tr_b16 a[222:223], v60 offset:6912\n"
      "s_add_u32 m0, s63, 128512\n"
      "s_add_u32 s52, s63, 135168\n"
      "s_waitcnt lgkmcnt(4)\n"
      "v_mfma_f32_4x4x4_16b_bf16 v[44:47], a[244:245], a[12:13], 0\n"
      "v_mfma_f32_4x4x4_16b_bf16 v[48:51], a[244:245], a[14:15], 0\n"
      "buffer_load_dwordx4 v17, s[12:15], s52 offen offset:0 lds\n"
      "s_waitcnt lgkmcnt(2)\n"
      "v_mfma_f32_4x4x4_16b_bf16 v[44:47], a[248:249], a[224:225], v[44:47]\n"
      "ds_read_b64_tr_b16 a[16:17], v60 offset:64\n"
      "v_mfma_f32_4x4x4_16b_bf16 v[48:51], a[248:249], a[226:227], v[48:51]\n"
      "buffer_load_dwordx4 v18, s[12:15], s52 offen offset:1024 lds\n"
      "s_nop 0\n"
      "s_waitcnt lgkmcnt(1)\n"
      "v_mfma_f32_4x4x4_16b_bf16 v[52:55], a[246:247], a[220:221], v[44:47]\n"
      "ds_read_b64_tr_b16 a[18:19], v60 offset:832\n"
      "v_mfma_f32_4x4x4_16b_bf16 v[56:59], a[246:247], a[222:223], v[48:51]\n"
      "buffer_load_dwordx4 v19, s[12:15], s52 offen offset:2048 lds\n"
      "v_mfma_f32_4x4x4_16b_bf16 v[44:47], a[250:251], a[220:221], v[44:47]\n"
      "ds_read_b64_tr_b16 a[232:233], v60 offset:12352\n"
      "v_mfma_f32_4x4x4_16b_bf16 v[48:51], a[250:251], a[222:223], v[48:51]\n"
      "ds_read_b64_tr_b16 a[234:235], v60 offset:13120\n"
      "ds_read_b64_tr_b16 a[228:229], v60 offset:6208\n"
      "ds_read_b64_tr_b16 a[230:231], v60 offset:6976\n"
      "ds_read_b64_tr_b16 a[20:21], v60 offset:128\n"
      "ds_read_b64_tr_b16 a[22:23], v60 offset:896\n"
      "ds_read_b64_tr_b16 a[240:241], v60 offset:12416\n"
      "ds_read_b64_tr_b16 a[242:243], v60 offset:13184\n"
      "ds_read_b64_tr_b16 a[236:237], v60 offset:6272\n"
      "ds_read_b64_tr_b16 a[238:239], v60 offset:7040\n"
      "ds_read_b64_tr_b16 a[164:165], v7 offset:2048\n"
      "ds_read_b64_tr_b16 a[166:167], v7 offset:2056\n"
      "ds_read_b128 a[204:207], v3 offset:55168\n"
      "v_cvt_pk_bf16_f32 v236, v36, v40\n"
      "v_cvt_pk_bf16_f32 v237, v37, v41\n"
      "v_cvt_pk_bf16_f32 v238, v38, v42\n"
      "v_cvt_pk_bf16_f32 v239, v39, v43\n"
      "v_cvt_pk_bf16_f32 v232, v28, v32\n"
      "v_cvt_pk_bf16_f32 v233, v29, v33\n"
      "v_cvt_pk_bf16_f32 v234, v30, v34\n"
      "v_cvt_pk_bf16_f32 v235, v31, v35\n"
      "s_waitcnt vmcnt(6)\n"
      "s_barrier\n"
      "s_nop 0\n"
      "s_waitcnt lgkmcnt(8)\n"
      "v_mfma_f32_4x4x4_16b_bf16 v[28:31], a[244:245], a[16:17], 0\n"
      "v_mfma_f32_4x4x4_16b_bf16 v[32:35], a[244:245], a[18:19], 0\n"
      "ds_read_b64_tr_b16 a[24:25], v60 offset:18432\n"
      "v_mfma_f32_4x4x4_16b_bf16 v[28:31], a[248:249], a[232:233], v[28:31]\n"
      "ds_read_b64_tr_b16 a[26:27], v60 offset:19200\n"
      "v_mfma_f32_4x4x4_16b_bf16 v[32:35], a[248:249], a[234:235], v[32:35]\n"
      "ds_read_b64_tr_b16 a[224:225], v61 offset:6400\n"
      "v_mfma_f32_4x4x4_16b_bf16 v[36:39], a[246:247], a[228:229], v[28:31]\n"
      "ds_read_b64_tr_b16 a[226:227], v61 offset:7168\n"
      "v_mfma_f32_4x4x4_16b_bf16 v[40:43], a[246:247], a[230:231], v[32:35]\n"
      "ds_read_b64_tr_b16 a[220:221], v61 offset:256\n"
      "v_mfma_f32_4x4x4_16b_bf16 v[28:31], a[250:251], a[228:229], v[28:31]\n"
      "ds_read_b64_tr_b16 a[222:223], v61 offset:1024\n"
      "v_mfma_f32_4x4x4_16b_bf16 v[32:35], a[250:251], a[230:231], v[32:35]\n"
      "ds_read_b64_tr_b16 a[160:161], v7 offset:2560\n"
      "s_nop 0\n"
      "s_waitcnt lgkmcnt(7)\n"
      "v_mfma_f32_4x4x4_16b_bf16 v[20:23], a[216:217], a[164:165], 0\n"
      "ds_read_b64_tr_b16 a[162:163], v7 offset:2568\n"
      "v_mfma_f32_4x4x4_16b_bf16 v[24:27], a[216:217], a[166:167], 0\n"
      "ds_read_b128 a[208:211], v3 offset:55232\n"
      "v_cvt_pk_bf16_f32 v228, v52, v56\n"
      "v_cvt_pk_bf16_f32 v229, v53, v57\n"
      "v_cvt_pk_bf16_f32 v230, v54, v58\n"
      "v_cvt_pk_bf16_f32 v231, v55, v59\n"
      "v_cvt_pk_bf16_f32 v224, v44, v48\n"
      "v_cvt_pk_bf16_f32 v225, v45, v49\n"
      "v_cvt_pk_bf16_f32 v226, v46, v50\n"
      "v_cvt_pk_bf16_f32 v227, v47, v51\n"
      "s_add_u32 m0, s63, 134656\n"
      "s_add_u32 s52, s63, 141312\n"
      "v_mfma_f32_4x4x4_16b_bf16 v[44:47], a[244:245], a[20:21], 0\n"
      "v_mfma_f32_4x4x4_16b_bf16 v[48:51], a[244:245], a[22:23], 0\n"
      "buffer_load_dwordx4 v17, s[12:15], s52 offen offset:0 lds\n"
      "v_mfma_f32_4x4x4_16b_bf16 v[44:47], a[248:249], a[240:241], v[44:47]\n"
      "ds_read_b64_tr_b16 a[28:29], v60 offset:18496\n"
      "v_mfma_f32_4x4x4_16b_bf16 v[48:51], a[248:249], a[242:243], v[48:51]\n"
      "buffer_load_dwordx4 v18, s[12:15], s52 offen offset:1024 lds\n"
      "v_mfma_f32_4x4x4_16b_bf16 v[52:55], a[246:247], a[236:237], v[44:47]\n"
      "ds_read_b64_tr_b16 a[30:31], v60 offset:19264\n"
      "v_mfma_f32_4x4x4_16b_bf16 v[56:59], a[246:247], a[238:239], v[48:51]\n"
      "buffer_load_dwordx4 v19, s[12:15], s52 offen offset:2048 lds\n"
      "v_mfma_f32_4x4x4_16b_bf16 v[44:47], a[250:251], a[236:237], v[44:47]\n"
      "ds_read_b64_tr_b16 a[232:233], v61 offset:6464\n"
      "v_mfma_f32_4x4x4_16b_bf16 v[48:51], a[250:251], a[238:239], v[48:51]\n"
      "s_add_u32 s53, s54, 983040\n"
      "s_nop 0\n"
      "s_waitcnt lgkmcnt(3)\n"
      "v_mfma_f32_4x4x4_16b_bf16 v[100:103], a[216:217], a[160:161], 0\n"
      "ds_read_b64_tr_b16 a[234:235], v61 offset:7232\n"
      "v_mfma_f32_4x4x4_16b_bf16 v[104:107], a[216:217], a[162:163], 0\n"
      "s_add_u32 m0, s37, 99328\n"
      "ds_read_b64_tr_b16 a[228:229], v61 offset:320\n"
      "buffer_load_dwordx4 v8, s[4:7], s53 offen offset:0 nt lds\n"
      "buffer_load_dwordx4 v9, s[4:7], s53 offen offset:1024 nt lds\n"
      "ds_read_b64_tr_b16 a[230:231], v61 offset:1088\n"
      "s_add_u32 s53, s54, 1179648\n"
      "ds_read_b64_tr_b16 a[156:157], v7 offset:3072\n"
      "s_add_u32 m0, s37, 103432\n"
      "ds_read_b64_tr_b16 a[158:159], v7 offset:3080\n"
      "buffer_load_dwordx4 v8, s[4:7], s53 offen offset:0 nt lds\n"
      "buffer_load_dwordx4 v9, s[4:7], s53 offen offset:1024 nt lds\n"
      "v_cvt_pk_bf16_f32 v220, v36, v40\n"
      "v_cvt_pk_bf16_f32 v221, v37, v41\n"
      "v_cvt_pk_bf16_f32 v222, v38, v42\n"
      "v_cvt_pk_bf16_f32 v223, v39, v43\n"
      "v_cvt_pk_bf16_f32 v216, v28, v32\n"
      "v_cvt_pk_bf16_f32 v217, v29, v33\n"
      "v_mfma_f32_16x16x32_bf16 v[64:67], a[0:3], a[204:207], 0\n"
      "v_cvt_pk_bf16_f32 v218, v30, v34\n"
      "v_cvt_pk_bf16_f32 v219, v31, v35\n"
      "v_mfma_f32_16x16x32_bf16 v[68:71], a[4:7], a[204:207], 0\n"
      "s_add_u32 m0, s37, 107656\n"
      "v_cvt_pk_bf16_f32 v20, v20, v24\n"
      "v_cvt_pk_bf16_f32 v21, v21, v25\n"
      "v_mfma_f32_16x16x32_bf16 v[72:75], a[8:11], a[204:207], 0\n"
      "s_add_u32 s53, s54, 1376256\n"
      "v_cvt_pk_bf16_f32 v22, v22, v26\n"
      "v_cvt_pk_bf16_f32 v23, v23, v27\n"
      "v_mfma_f32_4x4x4_16b_bf16 v[28:31], a[244:245], a[24:25], 0\n"
      "v_mfma_f32_4x4x4_16b_bf16 v[32:35], a[244:245], a[26:27], 0\n"
      "buffer_load_dwordx4 v8, s[4:7], s53 offen offset:0 nt lds\n"
      "buffer_load_dwordx4 v9, s[4:7], s53 offen offset:1024 nt lds\n"
      "v_mfma_f32_4x4x4_16b_bf16 v[28:31], a[248:249], a[224:225], v[28:31]\n"
      "ds_read_b64_tr_b16 a[32:33], v60 offset:18560\n"
      "v_mfma_f32_4x4x4_16b_bf16 v[32:35], a[248:249], a[226:227], v[32:35]\n"
      "s_add_u32 s53, s54, 1572864\n"
      "v_mfma_f32_4x4x4_16b_bf16 v[36:39], a[246:247], a[220:221], v[28:31]\n"
      "ds_read_b64_tr_b16 a[34:35], v60 offset:19328\n"
      "v_mfma_f32_4x4x4_16b_bf16 v[40:43], a[246:247], a[222:223], v[32:35]\n"
      "s_add_u32 m0, s37, 112000\n"
      "v_mfma_f32_4x4x4_16b_bf16 v[28:31], a[250:251], a[220:221], v[28:31]\n"
      "ds_read_b64_tr_b16 a[240:241], v61 offset:6528\n"
      "v_mfma_f32_4x4x4_16b_bf16 v[32:35], a[250:251], a[222:223], v[32:35]\n"
      "buffer_load_dwordx4 v8, s[4:7], s53 offen offset:0 nt lds\n"
      "buffer_load_dwordx4 v9, s[4:7], s53 offen offset:1024 nt lds\n"
      "s_nop 0\n"
      "s_waitcnt lgkmcnt(3)\n"
      "v_mfma_f32_4x4x4_16b_bf16 v[108:111], a[216:217], a[156:157], 0\n"
      "ds_read_b64_tr_b16 a[242:243], v61 offset:7296\n"
      "v_mfma_f32_4x4x4_16b_bf16 v[112:115], a[216:217], a[158:159], 0\n"
      "ds_read_b64_tr_b16 a[236:237], v61 offset:384\n"
      "ds_read_b64_tr_b16 a[238:239], v61 offset:1152\n"
      "ds_read_b64_tr_b16 a[152:153], v7 offset:3584\n"
      "ds_read_b64_tr_b16 a[154:155], v7 offset:3592\n"
      "s_mov_b32 m0, s64\n"
      "s_nop 0\n"
      "ds_write_addtid_b32 v20 offset:2048\n"
      "ds_write_addtid_b32 v21 offset:6144\n"
      "ds_write_addtid_b32 v22 offset:10240\n"
      "ds_write_addtid_b32 v23 offset:14336\n"
      "v_cvt_pk_bf16_f32 v212, v52, v56\n"
      "v_cvt_pk_bf16_f32 v213, v53, v57\n"
      "v_cvt_pk_bf16_f32 v214, v54, v58\n"
      "v_cvt_pk_bf16_f32 v215, v55, v59\n"
      "v_cvt_pk_bf16_f32 v208, v44, v48\n"
      "v_cvt_pk_bf16_f32 v209, v45, v49\n"
      "v_mfma_f32_16x16x32_bf16 v[64:67], a[12:15], a[208:211], v[64:67]\n"
      "v_cvt_pk_bf16_f32 v210, v46, v50\n"
      "v_cvt_pk_bf16_f32 v211, v47, v51\n"
      "v_mfma_f32_16x16x32_bf16 v[68:71], a[16:19], a[208:211], v[68:71]\n"
      "v_cvt_pk_bf16_f32 v100, v100, v104\n"
      "v_cvt_pk_bf16_f32 v101, v101, v105\n"
      "v_mfma_f32_16x16x32_bf16 v[72:75], a[20:23], a[208:211], v[72:75]\n"
      "s_waitcnt vmcnt(14)\n"
      "v_cvt_pk_bf16_f32 v102, v102, v106\n"
      "v_cvt_pk_bf16_f32 v103, v103, v107\n"
      "s_barrier\n"
      "v_mfma_f32_4x4x4_16b_bf16 v[44:47], a[244:245], a[28:29], 0\n"
      "v_mfma_f32_4x4x4_16b_bf16 v[48:51], a[244:245], a[30:31], 0\n"
      "ds_read_b64_tr_b16 a[36:37], v61 offset:12544\n"
      "v_mfma_f32_4x4x4_16b_bf16 v[44:47], a[248:249], a[232:233], v[44:47]\n"
      "ds_read_b64_tr_b16 a[38:39], v61 offset:13312\n"
      "v_mfma_f32_4x4x4_16b_bf16 v[48:51], a[248:249], a[234:235], v[48:51]\n"
      "ds_read_b64_tr_b16 a[224:225], v62 offset:39680\n"
      "v_mfma_f32_4x4x4_16b_bf16 v[52:55], a[246:247], a[228:229], v[44:47]\n"
      "ds_read_b64_tr_b16 a[226:227], v62 offset:40448\n"
      "v_mfma_f32_4x4x4_16b_bf16 v[56:59], a[246:247], a[230:231], v[48:51]\n"
      "ds_read_b64_tr_b16 a[220:221], v61 offset:18688\n"
      "v_mfma_f32_4x4x4_16b_bf16 v[44:47], a[250:251], a[228:229], v[44:47]\n"
      "ds_read_b64_tr_b16 a[222:223], v61 offset:19456\n"
      "v_mfma_f32_4x4x4_16b_bf16 v[48:51], a[250:251], a[230:231], v[48:51]\n"
      "ds_write_addtid_b32 v100 offset:2560\n"
      "s_nop 0\n"
      "s_waitcnt lgkmcnt(7)\n"
      "v_mfma_f32_4x4x4_16b_bf16 v[116:119], a[216:217], a[152:153], 0\n"
      "ds_write_addtid_b32 v101 offset:6656\n"
      "v_mfma_f32_4x4x4_16b_bf16 v[120:123], a[216:217], a[154:155], 0\n"
      "ds_write_addtid_b32 v102 offset:10752\n"
      "ds_write_addtid_b32 v103 offset:14848\n"
      "ds_read_b128 a[212:215], v3 offset:55296\n"
      "v_cvt_pk_bf16_f32 v204, v36, v40\n"
      "v_cvt_pk_bf16_f32 v205, v37, v41\n"
      "v_cvt_pk_bf16_f32 v206, v38, v42\n"
      "v_cvt_pk_bf16_f32 v207, v39, v43\n"
      "v_cvt_pk_bf16_f32 v200, v28, v32\n"
      "v_cvt_pk_bf16_f32 v201, v29, v33\n"
      "v_cvt_pk_bf16_f32 v202, v30, v34\n"
      "v_cvt_pk_bf16_f32 v203, v31, v35\n"
      "v_cvt_pk_bf16_f32 v108, v108, v112\n"
      "v_cvt_pk_bf16_f32 v109, v109, v113\n"
      "v_cvt_pk_bf16_f32 v110, v110, v114\n"
      "v_cvt_pk_bf16_f32 v111, v111, v115\n"
      "v_mfma_f32_4x4x4_16b_bf16 v[28:31], a[244:245], a[32:33], 0\n"
      "v_mfma_f32_4x4x4_16b_bf16 v[32:35], a[244:245], a[34:35], 0\n"
      "ds_read_b64_tr_b16 a[40:41], v61 offset:12608\n"
      "v_mfma_f32_4x4x4_16b_bf16 v[28:31], a[248:249], a[240:241], v[28:31]\n"
      "ds_read_b64_tr_b16 a[42:43], v61 offset:13376\n"
      "v_mfma_f32_4x4x4_16b_bf16 v[32:35], a[248:249], a[242:243], v[32:35]\n"
      "ds_read_b64_tr_b16 a[232:233], v62 offset:39744\n"
      "v_mfma_f32_4x4x4_16b_bf16 v[36:39], a[246:247], a[236:237], v[28:31]\n"
      "ds_read_b64_tr_b16 a[234:235], v62 offset:40512\n"
      "v_mfma_f32_4x4x4_16b_bf16 v[40:43], a[246:247], a[238:239], v[32:35]\n"
      "ds_read_b64_tr_b16 a[228:229], v61 offset:18752\n"
      "v_mfma_f32_4x4x4_16b_bf16 v[28:31], a[250:251], a[236:237], v[28:31]\n"
      "ds_read_b64_tr_b16 a[230:231], v61 offset:19520\n"
      "v_mfma_f32_4x4x4_16b_bf16 v[32:35], a[250:251], a[238:239], v[32:35]\n"
      "ds_write_addtid_b32 v108 offset:3072\n"
      "ds_write_addtid_b32 v109 offset:7168\n"
      "ds_write_addtid_b32 v110 offset:11264\n"
      "ds_write_addtid_b32 v111 offset:15360\n"
      "v_cvt_pk_bf16_f32 v196, v52, v56\n"
      "v_cvt_pk_bf16_f32 v197, v53, v57\n"
      "v_cvt_pk_bf16_f32 v198, v54, v58\n"
      "v_cvt_pk_bf16_f32 v199, v55, v59\n"
      "v_cvt_pk_bf16_f32 v192, v44, v48\n"
      "v_cvt_pk_bf16_f32 v193, v45, v49\n"
      "v_cvt_pk_bf16_f32 v194, v46, v50\n"
      "v_cvt_pk_bf16_f32 v195, v47, v51\n"
      "v_cvt_pk_bf16_f32 v116, v116, v120\n"
      "v_cvt_pk_bf16_f32 v117, v117, v121\n"
      "v_cvt_pk_bf16_f32 v118, v118, v122\n"
      "v_cvt_pk_bf16_f32 v119, v119, v123\n"
      "s_add_u32 m0, s63, 12288\n"
      "s_add_u32 s52, s63, 147456\n"
      "s_nop 0\n"
      "s_waitcnt lgkmcnt(3)\n"
      "v_mfma_f32_4x4x4_16b_bf16 v[44:47], a[244:245], a[36:37], 0\n"
      "v_mfma_f32_4x4x4_16b_bf16 v[48:51], a[244:245], a[38:39], 0\n"
      "buffer_load_dwordx4 v17, s[12:15], s52 offen offset:0 lds\n"
      "v_mfma_f32_4x4x4_16b_bf16 v[44:47], a[248:249], a[224:225], v[44:47]\n"
      "ds_read_b64_tr_b16 a[44:45], v61 offset:12672\n"
      "v_mfma_f32_4x4x4_16b_bf16 v[48:51], a[248:249], a[226:227], v[48:51]\n"
      "buffer_load_dwordx4 v18, s[12:15], s52 offen offset:1024 lds\n"
      "v_mfma_f32_4x4x4_16b_bf16 v[52:55], a[246:247], a[220:221], v[44:47]\n"
      "ds_read_b64_tr_b16 a[46:47], v61 offset:13440\n"
      "v_mfma_f32_4x4x4_16b_bf16 v[56:59], a[246:247], a[222:223], v[48:51]\n"
      "buffer_load_dwordx4 v19, s[12:15], s52 offen offset:2048 lds\n"
      "v_mfma_f32_4x4x4_16b_bf16 v[44:47], a[250:251], a[220:221], v[44:47]\n"
      "ds_read_b64_tr_b16 a[240:241], v62 offset:39808\n"
      "v_mfma_f32_4x4x4_16b_bf16 v[48:51], a[250:251], a[222:223], v[48:51]\n"
      "ds_read_b64_tr_b16 a[242:243], v62 offset:40576\n"
      "ds_read_b64_tr_b16 a[236:237], v61 offset:18816\n"
      "ds_read_b64_tr_b16 a[238:239], v61 offset:19584\n"
      "s_mov_b32 m0, s64\n"
      "s_nop 0\n"
      "ds_write_addtid_b32 v116 offset:3584\n"
      "ds_write_addtid_b32 v117 offset:7680\n"
      "ds_write_addtid_b32 v118 offset:11776\n"
      "ds_write_addtid_b32 v119 offset:15872\n"
      "v_cvt_pk_bf16_f32 v188, v36, v40\n"
      "v_cvt_pk_bf16_f32 v189, v37, v41\n"
      "v_mfma_f32_16x16x32_bf16 v[64:67], a[24:27], a[212:215], v[64:67]\n"
      "v_cvt_pk_bf16_f32 v190, v38, v42\n"
      "v_cvt_pk_bf16_f32 v191, v39, v43\n"
      "v_mfma_f32_16x16x32_bf16 v[68:71], a[28:31], a[212:215], v[68:71]\n"
      "s_waitcnt vmcnt(14)\n"
      "v_cvt_pk_bf16_f32 v184, v28, v32\n"
      "v_cvt_pk_bf16_f32 v185, v29, v33\n"
      "v_mfma_f32_16x16x32_bf16 v[72:75], a[32:35], a[212:215], v[72:75]\n"
      "s_waitcnt lgkmcnt(0)\n"
      "v_cvt_pk_bf16_f32 v186, v30, v34\n"
      "v_cvt_pk_bf16_f32 v187, v31, v35\n"
      "s_nop 0\n"
      "s_barrier\n"
      "v_mfma_f32_4x4x4_16b_bf16 v[28:31], a[244:245], a[40:41], 0\n"
      "v_mfma_f32_4x4x4_16b_bf16 v[32:35], a[244:245], a[42:43], 0\n"
      "ds_read_b64_tr_b16 a[48:49], v62 offset:1536\n"
      "v_mfma_f32_4x4x4_16b_bf16 v[28:31], a[248:249], a[232:233], v[28:31]\n"
      "ds_read_b64_tr_b16 a[50:51], v62 offset:2304\n"
      "v_mfma_f32_4x4x4_16b_bf16 v[32:35], a[248:249], a[234:235], v[32:35]\n"
      "ds_read_b64_tr_b16 a[224:225], v62 offset:13824\n"
      "v_mfma_f32_4x4x4_16b_bf16 v[36:39], a[246:247], a[228:229], v[28:31]\n"
      "ds_read_b64_tr_b16 a[226:227], v62 offset:14592\n"
      "v_mfma_f32_4x4x4_16b_bf16 v[40:43], a[246:247], a[230:231], v[32:35]\n"
      "ds_read_b64_tr_b16 a[220:221], v62 offset:7680\n"
      "v_mfma_f32_4x4x4_16b_bf16 v[28:31], a[250:251], a[228:229], v[28:31]\n"
      "ds_read_b64_tr_b16 a[222:223], v62 offset:8448\n"
      "v_mfma_f32_4x4x4_16b_bf16 v[32:35], a[250:251], a[230:231], v[32:35]\n"
      "ds_read_b128 a[192:195], v4 offset:55168\n"
      "ds_read_b128 a[196:199], v0 offset:0\n"
      "ds_read_b128 a[200:203], v0 offset:4096\n"
      "v_cvt_pk_bf16_f32 v180, v52, v56\n"
      "v_cvt_pk_bf16_f32 v181, v53, v57\n"
      "v_cvt_pk_bf16_f32 v182, v54, v58\n"
      "v_cvt_pk_bf16_f32 v183, v55, v59\n"
      "v_cvt_pk_bf16_f32 v176, v44, v48\n"
      "v_cvt_pk_bf16_f32 v177, v45, v49\n"
      "v_cvt_pk_bf16_f32 v178, v46, v50\n"
      "v_cvt_pk_bf16_f32 v179, v47, v51\n"
      "v_mfma_f32_4x4x4_16b_bf16 v[44:47], a[244:245], a[44:45], 0\n"
      "v_mfma_f32_4x4x4_16b_bf16 v[48:51], a[244:245], a[46:47], 0\n"
      "ds_read_b64_tr_b16 a[52:53], v62 offset:1600\n"
      "v_mfma_f32_4x4x4_16b_bf16 v[44:47], a[248:249], a[240:241], v[44:47]\n"
      "ds_read_b64_tr_b16 a[54:55], v62 offset:2368\n"
      "v_mfma_f32_4x4x4_16b_bf16 v[48:51], a[248:249], a[242:243], v[48:51]\n"
      "ds_read_b64_tr_b16 a[232:233], v62 offset:13888\n"
      "v_mfma_f32_4x4x4_16b_bf16 v[52:55], a[246:247], a[236:237], v[44:47]\n"
      "ds_read_b64_tr_b16 a[234:235], v62 offset:14656\n"
      "v_mfma_f32_4x4x4_16b_bf16 v[56:59], a[246:247], a[238:239], v[48:51]\n"
      "ds_read_b64_tr_b16 a[228:229], v62 offset:7744\n"
      "v_mfma_f32_4x4x4_16b_bf16 v[44:47], a[250:251], a[236:237], v[44:47]\n"
      "ds_read_b64_tr_b16 a[230:231], v62 offset:8512\n"
      "v_mfma_f32_4x4x4_16b_bf16 v[48:51], a[250:251], a[238:239], v[48:51]\n"
      "ds_read_b128 a[204:207], v0 offset:64\n"
      "ds_read_b128 a[208:211], v0 offset:4160\n"
      "ds_read_b128 a[212:215], v0 offset:128\n"
      "v_cvt_pk_bf16_f32 v172, v36, v40\n"
      "v_cvt_pk_bf16_f32 v173, v37, v41\n"
      "v_cvt_pk_bf16_f32 v174, v38, v42\n"
      "v_cvt_pk_bf16_f32 v175, v39, v43\n"
      "v_cvt_pk_bf16_f32 v168, v28, v32\n"
      "v_cvt_pk_bf16_f32 v169, v29, v33\n"
      "v_cvt_pk_bf16_f32 v170, v30, v34\n"
      "v_cvt_pk_bf16_f32 v171, v31, v35\n"
      "s_add_u32 m0, s63, 63744\n"
      "s_add_u32 s52, s63, 153600\n"
      "s_nop 0\n"
      "s_waitcnt lgkmcnt(4)\n"
      "v_mfma_f32_4x4x4_16b_bf16 v[28:31], a[244:245], a[48:49], 0\n"
      "v_mfma_f32_4x4x4_16b_bf16 v[32:35], a[244:245], a[50:51], 0\n"
      "buffer_load_dwordx4 v17, s[12:15], s52 offen offset:0 lds\n"
      "v_mfma_f32_4x4x4_16b_bf16 v[28:31], a[248:249], a[224:225], v[28:31]\n"
      "ds_read_b64_tr_b16 a[56:57], v62 offset:1664\n"
      "v_mfma_f32_4x4x4_16b_bf16 v[32:35], a[248:249], a[226:227], v[32:35]\n"
      "buffer_load_dwordx4 v18, s[12:15], s52 offen offset:1024 lds\n"
      "v_mfma_f32_4x4x4_16b_bf16 v[36:39], a[246:247], a[220:221], v[28:31]\n"
      "ds_read_b64_tr_b16 a[58:59], v62 offset:2432\n"
      "v_mfma_f32_4x4x4_16b_bf16 v[40:43], a[246:247], a[222:223], v[32:35]\n"
      "buffer_load_dwordx4 v19, s[12:15], s52 offen offset:2048 lds\n"
      "v_mfma_f32_4x4x4_16b_bf16 v[28:31], a[250:251], a[220:221], v[28:31]\n"
      "ds_read_b64_tr_b16 a[240:241], v62 offset:13952\n"
      "v_mfma_f32_4x4x4_16b_bf16 v[32:35], a[250:251], a[222:223], v[32:35]\n"
      "ds_read_b64_tr_b16 a[242:243], v62 offset:14720\n"
      "ds_read_b64_tr_b16 a[236:237], v62 offset:7808\n"
      "ds_read_b64_tr_b16 a[238:239], v62 offset:8576\n"
      "v_mfma_f32_16x16x32_bf16 v[64:67], a[36:39], a[192:195], v[64:67]\n"
      "v_cvt_pk_bf16_f32 v164, v52, v56\n"
      "v_cvt_pk_bf16_f32 v165, v53, v57\n"
      "v_mfma_f32_16x16x32_bf16 v[68:71], a[40:43], a[192:195], v[68:71]\n"
      "v_cvt_pk_bf16_f32 v166, v54, v58\n"
      "v_cvt_pk_bf16_f32 v167, v55, v59\n"
      "v_mfma_f32_16x16x32_bf16 v[72:75], a[44:47], a[192:195], v[72:75]\n"
      "v_cvt_pk_bf16_f32 v160, v44, v48\n"
      "v_cvt_pk_bf16_f32 v161, v45, v49\n"
      "v_mfma_f32_16x16x32_bf16 v[76:79], v[252:255], a[196:199], 0\n"
      "s_waitcnt vmcnt(14)\n"
      "v_cvt_pk_bf16_f32 v162, v46, v50\n"
      "v_cvt_pk_bf16_f32 v163, v47, v51\n"
      "s_barrier\n"
      "s_add_u32 m0, s63, 154368\n"
      "s_add_u32 s52, s63, 159744\n"
      "v_mfma_f32_4x4x4_16b_bf16 v[44:47], a[244:245], a[52:53], 0\n"
      "v_mfma_f32_4x4x4_16b_bf16 v[48:51], a[244:245], a[54:55], 0\n"
      "buffer_load_dwordx4 v17, s[12:15], s52 offen offset:0 lds\n"
      "v_mfma_f32_4x4x4_16b_bf16 v[44:47], a[248:249], a[232:233], v[44:47]\n"
      "ds_read_b64_tr_b16 a[60:61], v60 offset:41216\n"
      "v_mfma_f32_4x4x4_16b_bf16 v[48:51], a[248:249], a[234:235], v[48:51]\n"
      "buffer_load_dwordx4 v18, s[12:15], s52 offen offset:1024 lds\n"
      "s_nop 0\n"
      "s_waitcnt lgkmcnt(1)\n"
      "v_mfma_f32_4x4x4_16b_bf16 v[52:55], a[246:247], a[228:229], v[44:47]\n"
      "ds_read_b64_tr_b16 a[62:63], v60 offset:41984\n"
      "v_mfma_f32_4x4x4_16b_bf16 v[56:59], a[246:247], a[230:231], v[48:51]\n"
      "buffer_load_dwordx4 v19, s[12:15], s52 offen offset:2048 lds\n"
      "v_mfma_f32_4x4x4_16b_bf16 v[44:47], a[250:251], a[228:229], v[44:47]\n"
      "ds_read_b64_tr_b16 a[224:225], v62 offset:19968\n"
      "v_mfma_f32_4x4x4_16b_bf16 v[48:51], a[250:251], a[230:231], v[48:51]\n"
      "ds_read_b64_tr_b16 a[226:227], v62 offset:20736\n"
      "ds_read_b64_tr_b16 a[220:221], v60 offset:47360\n"
      "ds_read_b64_tr_b16 a[222:223], v60 offset:48128\n"
      "ds_read_b128 a[192:195], v0 offset:4224\n"
      "v_mfma_f32_16x16x32_bf16 v[80:83], v[244:247], a[196:199], 0\n"
      "v_cvt_pk_bf16_f32 v156, v36, v40\n"
      "v_cvt_pk_bf16_f32 v157, v37, v41\n"
      "v_mfma_f32_16x16x32_bf16 v[84:87], v[236:239], a[196:199], 0\n"
      "v_cvt_pk_bf16_f32 v158, v38, v42\n"
      "v_cvt_pk_bf16_f32 v159, v39, v43\n"
      "v_mfma_f32_16x16x32_bf16 v[88:91], v[248:251], a[200:203], 0\n"
      "v_cvt_pk_bf16_f32 v152, v28, v32\n"
      "v_cvt_pk_bf16_f32 v153, v29, v33\n"
      "v_mfma_f32_16x16x32_bf16 v[92:95], v[240:243], a[200:203], 0\n"
      "v_cvt_pk_bf16_f32 v154, v30, v34\n"
      "v_cvt_pk_bf16_f32 v155, v31, v35\n"
      "v_mfma_f32_4x4x4_16b_bf16 v[28:31], a[244:245], a[56:57], 0\n"
      "v_mfma_f32_4x4x4_16b_bf16 v[32:35], a[244:245], a[58:59], 0\n"
      "ds_read_b64_tr_b16 a[64:65], v60 offset:41280\n"
      "v_mfma_f32_4x4x4_16b_bf16 v[28:31], a[248:249], a[240:241], v[28:31]\n"
      "ds_read_b64_tr_b16 a[66:67], v60 offset:42048\n"
      "v_mfma_f32_4x4x4_16b_bf16 v[32:35], a[248:249], a[242:243], v[32:35]\n"
      "ds_read_b64_tr_b16 a[232:233], v62 offset:20032\n"
      "v_mfma_f32_4x4x4_16b_bf16 v[36:39], a[246:247], a[236:237], v[28:31]\n"
      "ds_read_b64_tr_b16 a[234:235], v62 offset:20800\n"
      "v_mfma_f32_4x4x4_16b_bf16 v[40:43], a[246:247], a[238:239], v[32:35]\n"
      "ds_read_b64_tr_b16 a[228:229], v60 offset:47424\n"
      "v_mfma_f32_4x4x4_16b_bf16 v[28:31], a[250:251], a[236:237], v[28:31]\n"
      "ds_read_b64_tr_b16 a[230:231], v60 offset:48192\n"
      "v_mfma_f32_4x4x4_16b_bf16 v[32:35], a[250:251], a[238:239], v[32:35]\n"
      "ds_read_b128 a[196:199], v1 offset:0\n"
      "v_mfma_f32_16x16x32_bf16 v[96:99], v[232:235], a[200:203], 0\n"
      "v_cvt_pk_bf16_f32 v148, v52, v56\n"
      "v_cvt_pk_bf16_f32 v149, v53, v57\n"
      "v_mfma_f32_16x16x32_bf16 v[76:79], v[228:231], a[204:207], v[76:79]\n"
      "v_cvt_pk_bf16_f32 v150, v54, v58\n"
      "v_cvt_pk_bf16_f32 v151, v55, v59\n"
      "v_mfma_f32_16x16x32_bf16 v[80:83], v[220:223], a[204:207], v[80:83]\n"
      "s_nop 0\n"
      "v_cvt_pk_bf16_f32 v144, v44, v48\n"
      "v_cvt_pk_bf16_f32 v145, v45, v49\n"
      "v_mfma_f32_16x16x32_bf16 v[84:87], v[212:215], a[204:207], v[84:87]\n"
      "s_waitcnt lgkmcnt(1)\n"
      "v_cvt_pk_bf16_f32 v146, v46, v50\n"
      "v_cvt_pk_bf16_f32 v147, v47, v51\n"
      "v_mfma_f32_4x4x4_16b_bf16 v[44:47], a[244:245], a[60:61], 0\n"
      "v_mfma_f32_4x4x4_16b_bf16 v[48:51], a[244:245], a[62:63], 0\n"
      "ds_read_b64_tr_b16 a[68:69], v60 offset:41344\n"
      "v_mfma_f32_4x4x4_16b_bf16 v[44:47], a[248:249], a[224:225], v[44:47]\n"
      "ds_read_b64_tr_b16 a[70:71], v60 offset:42112\n"
      "v_mfma_f32_4x4x4_16b_bf16 v[48:51], a[248:249], a[226:227], v[48:51]\n"
      "ds_read_b64_tr_b16 a[240:241], v62 offset:20096\n"
      "v_mfma_f32_4x4x4_16b_bf16 v[52:55], a[246:247], a[220:221], v[44:47]\n"
      "ds_read_b64_tr_b16 a[242:243], v62 offset:20864\n"
      "v_mfma_f32_4x4x4_16b_bf16 v[56:59], a[246:247], a[222:223], v[48:51]\n"
      "ds_read_b64_tr_b16 a[236:237], v60 offset:47488\n"
      "v_mfma_f32_4x4x4_16b_bf16 v[44:47], a[250:251], a[220:221], v[44:47]\n"
      "ds_read_b64_tr_b16 a[238:239], v60 offset:48256\n"
      "v_mfma_f32_4x4x4_16b_bf16 v[48:51], a[250:251], a[222:223], v[48:51]\n"
      "ds_read_b128 a[200:203], v1 offset:4096\n"
      "ds_read_b128 a[204:207], v4 offset:55232\n"
      "v_mfma_f32_16x16x32_bf16 v[88:91], v[224:227], a[208:211], v[88:91]\n"
      "v_cvt_pk_bf16_f32 v140, v36, v40\n"
      "v_cvt_pk_bf16_f32 v141, v37, v41\n"
      "v_mfma_f32_16x16x32_bf16 v[92:95], v[216:219], a[208:211], v[92:95]\n"
      "v_cvt_pk_bf16_f32 v142, v38, v42\n"
      "v_cvt_pk_bf16_f32 v143, v39, v43\n"
      "v_mfma_f32_16x16x32_bf16 v[96:99], v[208:211], a[208:211], v[96:99]\n"
      "v_cvt_pk_bf16_f32 v136, v28, v32\n"
      "v_cvt_pk_bf16_f32 v137, v29, v33\n"
      "v_mfma_f32_16x16x32_bf16 v[76:79], v[204:207], a[212:215], v[76:79]\n"
      "s_waitcnt vmcnt(6)\n"
      "v_cvt_pk_bf16_f32 v138, v30, v34\n"
      "v_cvt_pk_bf16_f32 v139, v31, v35\n"
      "s_barrier\n"
      "s_add_u32 s53, s54, 1769472\n"
      "s_add_u32 m0, s37, 116224\n"
      "v_mfma_f32_4x4x4_16b_bf16 v[28:31], a[244:245], a[64:65], 0\n"
      "v_mfma_f32_4x4x4_16b_bf16 v[32:35], a[244:245], a[66:67], 0\n"
      "buffer_load_dwordx4 v8, s[4:7], s53 offen offset:0 nt lds\n"
      "buffer_load_dwordx4 v9, s[4:7], s53 offen offset:1024 nt lds\n"
      "v_mfma_f32_4x4x4_16b_bf16 v[28:31], a[248:249], a[232:233], v[28:31]\n"
      "ds_read_b64_tr_b16 a[72:73], v60 offset:0\n"
      "v_mfma_f32_4x4x4_16b_bf16 v[32:35], a[248:249], a[234:235], v[32:35]\n"
      "ds_read_b64_tr_b16 a[74:75], v60 offset:768\n"
      "v_mfma_f32_4x4x4_16b_bf16 v[36:39], a[246:247], a[228:229], v[28:31]\n"
      "ds_read_b64_tr_b16 a[224:225], v60 offset:12288\n"
      "v_mfma_f32_4x4x4_16b_bf16 v[40:43], a[246:247], a[230:231], v[32:35]\n"
      "ds_read_b64_tr_b16 a[226:227], v60 offset:13056\n"
      "v_mfma_f32_4x4x4_16b_bf16 v[28:31], a[250:251], a[228:229], v[28:31]\n"
      "ds_read_b64_tr_b16 a[220:221], v60 offset:6144\n"
      "v_mfma_f32_4x4x4_16b_bf16 v[32:35], a[250:251], a[230:231], v[32:35]\n"
      "ds_read_b64_tr_b16 a[222:223], v60 offset:6912\n"
      "ds_read_b128 a[208:211], v1 offset:64\n"
      "v_mfma_f32_16x16x32_bf16 v[80:83], v[196:199], a[212:215], v[80:83]\n"
      "v_cvt_pk_bf16_f32 v132, v52, v56\n"
      "v_cvt_pk_bf16_f32 v133, v53, v57\n"
      "v_mfma_f32_16x16x32_bf16 v[84:87], v[188:191], a[212:215], v[84:87]\n"
      "v_cvt_pk_bf16_f32 v134, v54, v58\n"
      "v_cvt_pk_bf16_f32 v135, v55, v59\n"
      "v_mfma_f32_16x16x32_bf16 v[88:91], v[200:203], a[192:195], v[88:91]\n"
      "s_add_u32 m0, s37, 120328\n"
      "v_cvt_pk_bf16_f32 v128, v44, v48\n"
      "v_cvt_pk_bf16_f32 v129, v45, v49\n"
      "v_mfma_f32_16x16x32_bf16 v[92:95], v[192:195], a[192:195], v[92:95]\n"
      "s_add_u32 s53, s54, 1966080\n"
      "v_cvt_pk_bf16_f32 v130, v46, v50\n"
      "v_cvt_pk_bf16_f32 v131, v47, v51\n"
      "s_nop 0\n"
      "s_waitcnt lgkmcnt(1)\n"
      "v_mfma_f32_4x4x4_16b_bf16 v[44:47], a[244:245], a[68:69], 0\n"
      "v_mfma_f32_4x4x4_16b_bf16 v[48:51], a[244:245], a[70:71], 0\n"
      "buffer_load_dwordx4 v8, s[4:7], s53 offen offset:0 nt lds\n"
      "buffer_load_dwordx4 v9, s[4:7], s53 offen offset:1024 nt lds\n"
      "v_mfma_f32_4x4x4_16b_bf16 v[44:47], a[248:249], a[240:241], v[44:47]\n"
      "ds_read_b64_tr_b16 a[76:77], v60 offset:64\n"
      "v_mfma_f32_4x4x4_16b_bf16 v[48:51], a[248:249], a[242:243], v[48:51]\n"
      "ds_read_b64_tr_b16 a[78:79], v60 offset:832\n"
      "v_mfma_f32_4x4x4_16b_bf16 v[52:55], a[246:247], a[236:237], v[44:47]\n"
      "ds_read_b64_tr_b16 a[232:233], v60 offset:12352\n"
      "v_mfma_f32_4x4x4_16b_bf16 v[56:59], a[246:247], a[238:239], v[48:51]\n"
      "ds_read_b64_tr_b16 a[234:235], v60 offset:13120\n"
      "v_mfma_f32_4x4x4_16b_bf16 v[44:47], a[250:251], a[236:237], v[44:47]\n"
      "ds_read_b64_tr_b16 a[228:229], v60 offset:6208\n"
      "v_mfma_f32_4x4x4_16b_bf16 v[48:51], a[250:251], a[238:239], v[48:51]\n"
      "ds_read_b64_tr_b16 a[230:231], v60 offset:6976\n"
      "ds_read_b128 a[212:215], v1 offset:4160\n"
      "v_mfma_f32_16x16x32_bf16 v[96:99], v[184:187], a[192:195], v[96:99]\n"
      "s_nop 0\n"
      "v_cvt_pk_bf16_f32 v124, v36, v40\n"
      "v_cvt_pk_bf16_f32 v125, v37, v41\n"
      "v_mfma_f32_16x16x32_bf16 v[76:79], v[180:183], a[196:199], v[76:79]\n"
      "s_nop 0\n"
      "v_cvt_pk_bf16_f32 v126, v38, v42\n"
      "v_cvt_pk_bf16_f32 v127, v39, v43\n"
      "v_mfma_f32_16x16x32_bf16 v[80:83], v[172:175], a[196:199], v[80:83]\n"
      "s_add_u32 m0, s37, 124552\n"
      "v_cvt_pk_bf16_f32 v120, v28, v32\n"
      "v_cvt_pk_bf16_f32 v121, v29, v33\n"
      "v_mfma_f32_16x16x32_bf16 v[84:87], v[164:167], a[196:199], v[84:87]\n"
      "s_add_u32 s53, s54, 2162688\n"
      "v_cvt_pk_bf16_f32 v122, v30, v34\n"
      "v_cvt_pk_bf16_f32 v123, v31, v35\n"
      "v_mfma_f32_4x4x4_16b_bf16 v[28:31], a[244:245], a[72:73], 0\n"
      "v_mfma_f32_4x4x4_16b_bf16 v[32:35], a[244:245], a[74:75], 0\n"
      "buffer_load_dwordx4 v8, s[4:7], s53 offen offset:0 nt lds\n"
      "buffer_load_dwordx4 v9, s[4:7], s53 offen offset:1024 nt lds\n"
      "v_mfma_f32_4x4x4_16b_bf16 v[28:31], a[248:249], a[224:225], v[28:31]\n"
      "ds_read_b64_tr_b16 a[80:81], v60 offset:128\n"
      "v_mfma_f32_4x4x4_16b_bf16 v[32:35], a[248:249], a[226:227], v[32:35]\n"
      "ds_read_b64_tr_b16 a[82:83], v60 offset:896\n"
      "v_mfma_f32_4x4x4_16b_bf16 v[36:39], a[246:247], a[220:221], v[28:31]\n"
      "ds_read_b64_tr_b16 a[240:241], v60 offset:12416\n"
      "v_mfma_f32_4x4x4_16b_bf16 v[40:43], a[246:247], a[222:223], v[32:35]\n"
      "ds_read_b64_tr_b16 a[242:243], v60 offset:13184\n"
      "v_mfma_f32_4x4x4_16b_bf16 v[28:31], a[250:251], a[220:221], v[28:31]\n"
      "ds_read_b64_tr_b16 a[236:237], v60 offset:6272\n"
      "v_mfma_f32_4x4x4_16b_bf16 v[32:35], a[250:251], a[222:223], v[32:35]\n"
      "ds_read_b64_tr_b16 a[238:239], v60 offset:7040\n"
      "ds_read_b128 a[192:195], v4 offset:55296\n"
      "ds_read_b128 a[196:199], v1 offset:128\n"
      "v_mfma_f32_16x16x32_bf16 v[88:91], v[176:179], a[200:203], v[88:91]\n"
      "s_nop 0\n"
      "v_cvt_pk_bf16_f32 v116, v52, v56\n"
      "v_cvt_pk_bf16_f32 v117, v53, v57\n"
      "v_mfma_f32_16x16x32_bf16 v[92:95], v[168:171], a[200:203], v[92:95]\n"
      "s_nop 0\n"
      "v_cvt_pk_bf16_f32 v118, v54, v58\n"
      "v_cvt_pk_bf16_f32 v119, v55, v59\n"
      "v_mfma_f32_16x16x32_bf16 v[96:99], v[160:163], a[200:203], v[96:99]\n"
      "s_nop 0\n"
      "v_cvt_pk_bf16_f32 v112, v44, v48\n"
      "v_cvt_pk_bf16_f32 v113, v45, v49\n"
      "v_mfma_f32_16x16x32_bf16 v[64:67], a[48:51], a[204:207], v[64:67]\n"
      "s_waitcnt vmcnt(9)\n"
      "v_cvt_pk_bf16_f32 v114, v46, v50\n"
      "v_cvt_pk_bf16_f32 v115, v47, v51\n"
      "s_barrier\n"
      "s_add_u32 s53, s54, 2359296\n"
      "s_add_u32 m0, s37, 128896\n"
      "s_waitcnt lgkmcnt(0)\n"
      "v_mfma_f32_4x4x4_16b_bf16 v[44:47], a[244:245], a[76:77], 0\n"
      "v_mfma_f32_4x4x4_16b_bf16 v[48:51], a[244:245], a[78:79], 0\n"
      "buffer_load_dwordx4 v8, s[4:7], s53 offen offset:0 nt lds\n"
      "buffer_load_dwordx4 v9, s[4:7], s53 offen offset:1024 nt lds\n"
      "v_mfma_f32_4x4x4_16b_bf16 v[44:47], a[248:249], a[232:233], v[44:47]\n"
      "ds_read_b64_tr_b16 a[84:85], v60 offset:18432\n"
      "v_mfma_f32_4x4x4_16b_bf16 v[48:51], a[248:249], a[234:235], v[48:51]\n"
      "ds_read_b64_tr_b16 a[86:87], v60 offset:19200\n"
      "v_mfma_f32_4x4x4_16b_bf16 v[52:55], a[246:247], a[228:229], v[44:47]\n"
      "ds_read_b64_tr_b16 a[224:225], v61 offset:6400\n"
      "v_mfma_f32_4x4x4_16b_bf16 v[56:59], a[246:247], a[230:231], v[48:51]\n"
      "ds_read_b64_tr_b16 a[226:227], v61 offset:7168\n"
      "v_mfma_f32_4x4x4_16b_bf16 v[44:47], a[250:251], a[228:229], v[44:47]\n"
      "ds_read_b64_tr_b16 a[220:221], v61 offset:256\n"
      "v_mfma_f32_4x4x4_16b_bf16 v[48:51], a[250:251], a[230:231], v[48:51]\n"
      "ds_read_b64_tr_b16 a[222:223], v61 offset:1024\n"
      "ds_read_b128 a[200:203], v1 offset:4224\n"
      "v_mfma_f32_16x16x32_bf16 v[68:71], a[52:55], a[204:207], v[68:71]\n"
      "v_cvt_pk_bf16_f32 v108, v36, v40\n"
      "v_cvt_pk_bf16_f32 v109, v37, v41\n"
      "v_mfma_f32_16x16x32_bf16 v[72:75], a[56:59], a[204:207], v[72:75]\n"
      "v_cvt_pk_bf16_f32 v110, v38, v42\n"
      "v_cvt_pk_bf16_f32 v111, v39, v43\n"
      "v_mfma_f32_16x16x32_bf16 v[76:79], v[156:159], a[208:211], v[76:79]\n"
      "s_add_u32 m0, s37, 133120\n"
      "v_cvt_pk_bf16_f32 v104, v28, v32\n"
      "v_cvt_pk_bf16_f32 v105, v29, v33\n"
      "v_mfma_f32_16x16x32_bf16 v[80:83], v[148:151], a[208:211], v[80:83]\n"
      "s_add_u32 s53, s54, 2555904\n"
      "v_cvt_pk_bf16_f32 v106, v30, v34\n"
      "v_cvt_pk_bf16_f32 v107, v31, v35\n"
      "v_mfma_f32_4x4x4_16b_bf16 v[28:31], a[244:245], a[80:81], 0\n"
      "v_mfma_f32_4x4x4_16b_bf16 v[32:35], a[244:245], a[82:83], 0\n"
      "buffer_load_dwordx4 v8, s[4:7], s53 offen offset:0 nt lds\n"
      "buffer_load_dwordx4 v9, s[4:7], s53 offen offset:1024 nt lds\n"
      "v_mfma_f32_4x4x4_16b_bf16 v[28:31], a[248:249], a[240:241], v[28:31]\n"
      "ds_read_b64_tr_b16 a[88:89], v60 offset:18496\n"
      "v_mfma_f32_4x4x4_16b_bf16 v[32:35], a[248:249], a[242:243], v[32:35]\n"
      "ds_read_b64_tr_b16 a[90:91], v60 offset:19264\n"
      "v_mfma_f32_4x4x4_16b_bf16 v[36:39], a[246:247], a[236:237], v[28:31]\n"
      "ds_read_b64_tr_b16 a[232:233], v61 offset:6464\n"
      "v_mfma_f32_4x4x4_16b_bf16 v[40:43], a[246:247], a[238:239], v[32:35]\n"
      "ds_read_b64_tr_b16 a[234:235], v61 offset:7232\n"
      "v_mfma_f32_4x4x4_16b_bf16 v[28:31], a[250:251], a[236:237], v[28:31]\n"
      "ds_read_b64_tr_b16 a[228:229], v61 offset:320\n"
      "v_mfma_f32_4x4x4_16b_bf16 v[32:35], a[250:251], a[238:239], v[32:35]\n"
      "ds_read_b64_tr_b16 a[230:231], v61 offset:1088\n"
      "ds_read_b128 a[204:207], v5 offset:55168\n"
      "v_mfma_f32_16x16x32_bf16 v[84:87], v[140:143], a[208:211], v[84:87]\n"
      "v_cvt_pk_bf16_f32 v100, v52, v56\n"
      "v_cvt_pk_bf16_f32 v101, v53, v57\n"
      "v_mfma_f32_16x16x32_bf16 v[88:91], v[152:155], a[212:215], v[88:91]\n"
      "v_cvt_pk_bf16_f32 v102, v54, v58\n"
      "v_cvt_pk_bf16_f32 v103, v55, v59\n"
      "v_mfma_f32_16x16x32_bf16 v[92:95], v[144:147], a[212:215], v[92:95]\n"
      "v_cvt_pk_bf16_f32 v44, v44, v48\n"
      "v_cvt_pk_bf16_f32 v45, v45, v49\n"
      "v_mfma_f32_16x16x32_bf16 v[96:99], v[136:139], a[212:215], v[96:99]\n"
      "v_cvt_pk_bf16_f32 v46, v46, v50\n"
      "v_cvt_pk_bf16_f32 v47, v47, v51\n"
      "v_mfma_f32_16x16x32_bf16 v[64:67], a[60:63], a[192:195], v[64:67]\n"
      "s_add_u32 m0, s37, 137224\n"
      "v_accvgpr_write_b32 a164, v44\n"
      "v_accvgpr_write_b32 a165, v45\n"
      "v_mfma_f32_16x16x32_bf16 v[68:71], a[64:67], a[192:195], v[68:71]\n"
      "s_add_u32 s53, s54, 2752512\n"
      "v_accvgpr_write_b32 a166, v46\n"
      "v_accvgpr_write_b32 a167, v47\n"
      "s_nop 0\n"
      "s_waitcnt lgkmcnt(0)\n"
      "v_mfma_f32_4x4x4_16b_bf16 v[44:47], a[244:245], a[84:85], 0\n"
      "v_mfma_f32_4x4x4_16b_bf16 v[48:51], a[244:245], a[86:87], 0\n"
      "buffer_load_dwordx4 v8, s[4:7], s53 offen offset:0 nt lds\n"
      "buffer_load_dwordx4 v9, s[4:7], s53 offen offset:1024 nt lds\n"
      "v_mfma_f32_4x4x4_16b_bf16 v[44:47], a[248:249], a[224:225], v[44:47]\n"
      "ds_read_b64_tr_b16 a[92:93], v60 offset:18560\n"
      "v_mfma_f32_4x4x4_16b_bf16 v[48:51], a[248:249], a[226:227], v[48:51]\n"
      "ds_read_b64_tr_b16 a[94:95], v60 offset:19328\n"
      "v_mfma_f32_4x4x4_16b_bf16 v[52:55], a[246:247], a[220:221], v[44:47]\n"
      "ds_read_b64_tr_b16 a[240:241], v61 offset:6528\n"
      "v_mfma_f32_4x4x4_16b_bf16 v[56:59], a[246:247], a[222:223], v[48:51]\n"
      "ds_read_b64_tr_b16 a[242:243], v61 offset:7296\n"
      "v_mfma_f32_4x4x4_16b_bf16 v[44:47], a[250:251], a[220:221], v[44:47]\n"
      "ds_read_b64_tr_b16 a[236:237], v61 offset:384\n"
      "v_mfma_f32_4x4x4_16b_bf16 v[48:51], a[250:251], a[222:223], v[48:51]\n"
      "ds_read_b64_tr_b16 a[238:239], v61 offset:1152\n"
      "ds_read_b128 a[208:211], v2 offset:0\n"
      "ds_read_b128 a[212:215], v2 offset:4096\n"
      "v_cvt_pk_bf16_f32 v36, v36, v40\n"
      "v_cvt_pk_bf16_f32 v37, v37, v41\n"
      "v_cvt_pk_bf16_f32 v38, v38, v42\n"
      "v_cvt_pk_bf16_f32 v39, v39, v43\n"
      "v_mfma_f32_16x16x32_bf16 v[72:75], a[68:71], a[192:195], v[72:75]\n"
      "v_accvgpr_write_b32 a160, v36\n"
      "v_accvgpr_write_b32 a161, v37\n"
      "v_mfma_f32_16x16x32_bf16 v[76:79], v[132:135], a[196:199], v[76:79]\n"
      "s_nop 0\n"
      "v_accvgpr_write_b32 a162, v38\n"
      "v_accvgpr_write_b32 a163, v39\n"
      "v_mfma_f32_16x16x32_bf16 v[80:83], v[124:127], a[196:199], v[80:83]\n"
      "s_nop 0\n"
      "v_cvt_pk_bf16_f32 v28, v28, v32\n"
      "v_cvt_pk_bf16_f32 v29, v29, v33\n"
      "v_mfma_f32_16x16x32_bf16 v[84:87], v[116:119], a[196:199], v[84:87]\n"
      "s_nop 0\n"
      "v_cvt_pk_bf16_f32 v30, v30, v34\n"
      "v_cvt_pk_bf16_f32 v31, v31, v35\n"
      "v_mfma_f32_16x16x32_bf16 v[88:91], v[128:131], a[200:203], v[88:91]\n"
      "s_nop 0\n"
      "v_accvgpr_write_b32 a156, v28\n"
      "v_accvgpr_write_b32 a157, v29\n"
      "v_mfma_f32_16x16x32_bf16 v[92:95], v[120:123], a[200:203], v[92:95]\n"
      "s_waitcnt vmcnt(12)\n"
      "v_accvgpr_write_b32 a158, v30\n"
      "v_accvgpr_write_b32 a159, v31\n"
      "s_barrier\n"
      "s_add_u32 s53, s54, 2949120\n"
      "s_add_u32 m0, s37, 141448\n"
      "v_mfma_f32_4x4x4_16b_bf16 v[28:31], a[244:245], a[88:89], 0\n"
      "v_mfma_f32_4x4x4_16b_bf16 v[32:35], a[244:245], a[90:91], 0\n"
      "buffer_load_dwordx4 v8, s[4:7], s53 offen offset:0 nt lds\n"
      "buffer_load_dwordx4 v9, s[4:7], s53 offen offset:1024 nt lds\n"
      "v_mfma_f32_4x4x4_16b_bf16 v[28:31], a[248:249], a[232:233], v[28:31]\n"
      "ds_read_b64_tr_b16 a[96:97], v61 offset:12544\n"
      "v_mfma_f32_4x4x4_16b_bf16 v[32:35], a[248:249], a[234:235], v[32:35]\n"
      "ds_read_b64_tr_b16 a[98:99], v61 offset:13312\n"
      "v_mfma_f32_4x4x4_16b_bf16 v[36:39], a[246:247], a[228:229], v[28:31]\n"
      "ds_read_b64_tr_b16 a[224:225], v62 offset:39680\n"
      "v_mfma_f32_4x4x4_16b_bf16 v[40:43], a[246:247], a[230:231], v[32:35]\n"
      "ds_read_b64_tr_b16 a[226:227], v62 offset:40448\n"
      "v_mfma_f32_4x4x4_16b_bf16 v[28:31], a[250:251], a[228:229], v[28:31]\n"
      "ds_read_b64_tr_b16 a[220:221], v61 offset:18688\n"
      "v_mfma_f32_4x4x4_16b_bf16 v[32:35], a[250:251], a[230:231], v[32:35]\n"
      "ds_read_b64_tr_b16 a[222:223], v61 offset:19456\n"
      "ds_read_b128 a[192:195], v5 offset:55232\n"
      "v_cvt_pk_bf16_f32 v52, v52, v56\n"
      "v_cvt_pk_bf16_f32 v53, v53, v57\n"
      "v_cvt_pk_bf16_f32 v54, v54, v58\n"
      "v_cvt_pk_bf16_f32 v55, v55, v59\n"
      "v_mfma_f32_16x16x32_bf16 v[96:99], v[112:115], a[200:203], v[96:99]\n"
      "s_nop 0\n"
      "v_accvgpr_write_b32 a152, v52\n"
      "v_accvgpr_write_b32 a153, v53\n"
      "v_mfma_f32_16x16x32_bf16 v[64:67], a[72:75], a[204:207], v[64:67]\n"
      "s_nop 0\n"
      "v_accvgpr_write_b32 a154, v54\n"
      "v_accvgpr_write_b32 a155, v55\n"
      "v_mfma_f32_16x16x32_bf16 v[68:71], a[76:79], a[204:207], v[68:71]\n"
      "s_nop 0\n"
      "v_cvt_pk_bf16_f32 v44, v44, v48\n"
      "v_cvt_pk_bf16_f32 v45, v45, v49\n"
      "v_mfma_f32_16x16x32_bf16 v[72:75], a[80:83], a[204:207], v[72:75]\n"
      "s_waitcnt lgkmcnt(0)\n"
      "v_cvt_pk_bf16_f32 v46, v46, v50\n"
      "v_cvt_pk_bf16_f32 v47, v47, v51\n"
      "v_mfma_f32_16x16x32_bf16 v[76:79], v[108:111], a[208:211], v[76:79]\n"
      "s_add_u32 m0, s37, 145792\n"
      "v_accvgpr_write_b32 a148, v44\n"
      "v_accvgpr_write_b32 a149, v45\n"
      "v_mfma_f32_16x16x32_bf16 v[80:83], v[100:103], a[208:211], v[80:83]\n"
      "s_add_u32 s53, s54, 3145728\n"
      "v_accvgpr_write_b32 a150, v46\n"
      "v_accvgpr_write_b32 a151, v47\n"
      "v_mfma_f32_4x4x4_16b_bf16 v[44:47], a[244:245], a[92:93], 0\n"
      "v_mfma_f32_4x4x4_16b_bf16 v[48:51], a[244:245], a[94:95], 0\n"
      "buffer_load_dwordx4 v8, s[4:7], s53 offen offset:0 nt lds\n"
      "buffer_load_dwordx4 v9, s[4:7], s53 offen offset:1024 nt lds\n"
      "v_mfma_f32_4x4x4_16b_bf16 v[44:47], a[248:249], a[240:241], v[44:47]\n"
      "ds_read_b64_tr_b16 a[100:101], v61 offset:12608\n"
      "v_mfma_f32_4x4x4_16b_bf16 v[48:51], a[248:249], a[242:243], v[48:51]\n"
      "ds_read_b64_tr_b16 a[102:103], v61 offset:13376\n"
      "v_mfma_f32_4x4x4_16b_bf16 v[52:55], a[246:247], a[236:237], v[44:47]\n"
      "ds_read_b64_tr_b16 a[232:233], v62 offset:39744\n"
      "v_mfma_f32_4x4x4_16b_bf16 v[56:59], a[246:247], a[238:239], v[48:51]\n"
      "ds_read_b64_tr_b16 a[234:235], v62 offset:40512\n"
      "v_mfma_f32_4x4x4_16b_bf16 v[44:47], a[250:251], a[236:237], v[44:47]\n"
      "ds_read_b64_tr_b16 a[228:229], v61 offset:18752\n"
      "v_mfma_f32_4x4x4_16b_bf16 v[48:51], a[250:251], a[238:239], v[48:51]\n"
      "ds_read_b64_tr_b16 a[230:231], v61 offset:19520\n"
      "ds_read_b128 a[196:199], v2 offset:64\n"
      "ds_read_b128 a[200:203], v2 offset:4160\n"
      "v_cvt_pk_bf16_f32 v36, v36, v40\n"
      "v_cvt_pk_bf16_f32 v37, v37, v41\n"
      "v_cvt_pk_bf16_f32 v38, v38, v42\n"
      "v_cvt_pk_bf16_f32 v39, v39, v43\n"
      "v_accvgpr_write_b32 a144, v36\n"
      "v_accvgpr_write_b32 a145, v37\n"
      "v_accvgpr_write_b32 a146, v38\n"
      "v_accvgpr_write_b32 a147, v39\n"
      "v_mfma_f32_16x16x32_bf16 v[84:87], a[160:163], a[208:211], v[84:87]\n"
      "s_nop 0\n"
      "v_cvt_pk_bf16_f32 v28, v28, v32\n"
      "v_cvt_pk_bf16_f32 v29, v29, v33\n"
      "v_mfma_f32_16x16x32_bf16 v[88:91], v[104:107], a[212:215], v[88:91]\n"
      "s_nop 0\n"
      "v_cvt_pk_bf16_f32 v30, v30, v34\n"
      "v_cvt_pk_bf16_f32 v31, v31, v35\n"
      "v_mfma_f32_16x16x32_bf16 v[92:95], a[164:167], a[212:215], v[92:95]\n"
      "s_nop 0\n"
      "v_accvgpr_write_b32 a140, v28\n"
      "v_accvgpr_write_b32 a141, v29\n"
      "v_mfma_f32_16x16x32_bf16 v[96:99], a[156:159], a[212:215], v[96:99]\n"
      "s_nop 0\n"
      "v_accvgpr_write_b32 a142, v30\n"
      "v_accvgpr_write_b32 a143, v31\n"
      "v_mfma_f32_4x4x4_16b_bf16 v[28:31], a[244:245], a[96:97], 0\n"
      "v_mfma_f32_4x4x4_16b_bf16 v[32:35], a[244:245], a[98:99], 0\n"
      "ds_read_b64_tr_b16 a[104:105], v61 offset:12672\n"
      "v_mfma_f32_4x4x4_16b_bf16 v[28:31], a[248:249], a[224:225], v[28:31]\n"
      "ds_read_b64_tr_b16 a[106:107], v61 offset:13440\n"
      "v_mfma_f32_4x4x4_16b_bf16 v[32:35], a[248:249], a[226:227], v[32:35]\n"
      "ds_read_b64_tr_b16 a[240:241], v62 offset:39808\n"
      "v_mfma_f32_4x4x4_16b_bf16 v[36:39], a[246:247], a[220:221], v[28:31]\n"
      "ds_read_b64_tr_b16 a[242:243], v62 offset:40576\n"
      "v_mfma_f32_4x4x4_16b_bf16 v[40:43], a[246:247], a[222:223], v[32:35]\n"
      "ds_read_b64_tr_b16 a[236:237], v61 offset:18816\n"
      "v_mfma_f32_4x4x4_16b_bf16 v[28:31], a[250:251], a[220:221], v[28:31]\n"
      "ds_read_b64_tr_b16 a[238:239], v61 offset:19584\n"
      "v_mfma_f32_4x4x4_16b_bf16 v[32:35], a[250:251], a[222:223], v[32:35]\n"
      "v_cvt_pk_bf16_f32 v52, v52, v56\n"
      "v_cvt_pk_bf16_f32 v53, v53, v57\n"
      "v_cvt_pk_bf16_f32 v54, v54, v58\n"
      "v_cvt_pk_bf16_f32 v55, v55, v59\n"
      "v_accvgpr_write_b32 a136, v52\n"
      "v_accvgpr_write_b32 a137, v53\n"
      "v_accvgpr_write_b32 a138, v54\n"
      "v_accvgpr_write_b32 a139, v55\n"
      "v_cvt_pk_bf16_f32 v44, v44, v48\n"
      "v_cvt_pk_bf16_f32 v45, v45, v49\n"
      "v_mfma_f32_16x16x32_bf16 v[64:67], a[84:87], a[192:195], v[64:67]\n"
      "s_nop 0\n"
      "v_cvt_pk_bf16_f32 v46, v46, v50\n"
      "v_cvt_pk_bf16_f32 v47, v47, v51\n"
      "v_mfma_f32_16x16x32_bf16 v[68:71], a[88:91], a[192:195], v[68:71]\n"
      "s_nop 0\n"
      "v_accvgpr_write_b32 a132, v44\n"
      "v_accvgpr_write_b32 a133, v45\n"
      "v_mfma_f32_16x16x32_bf16 v[72:75], a[92:95], a[192:195], v[72:75]\n"
      "s_waitcnt lgkmcnt(0)\n"
      "v_accvgpr_write_b32 a134, v46\n"
      "v_accvgpr_write_b32 a135, v47\n"
      "v_mfma_f32_4x4x4_16b_bf16 v[44:47], a[244:245], a[100:101], 0\n"
      "v_mfma_f32_4x4x4_16b_bf16 v[48:51], a[244:245], a[102:103], 0\n"
      "ds_read_b128 a[204:207], v5 offset:55296\n"
      "v_mfma_f32_4x4x4_16b_bf16 v[44:47], a[248:249], a[232:233], v[44:47]\n"
      "v_mfma_f32_4x4x4_16b_bf16 v[48:51], a[248:249], a[234:235], v[48:51]\n"
      "s_nop 0\n"
      "v_mfma_f32_4x4x4_16b_bf16 v[52:55], a[246:247], a[228:229], v[44:47]\n"
      "v_mfma_f32_4x4x4_16b_bf16 v[56:59], a[246:247], a[230:231], v[48:51]\n"
      "v_mfma_f32_4x4x4_16b_bf16 v[44:47], a[250:251], a[228:229], v[44:47]\n"
      "v_mfma_f32_4x4x4_16b_bf16 v[48:51], a[250:251], a[230:231], v[48:51]\n"
      "v_cvt_pk_bf16_f32 v36, v36, v40\n"
      "v_cvt_pk_bf16_f32 v37, v37, v41\n"
      "v_cvt_pk_bf16_f32 v38, v38, v42\n"
      "v_cvt_pk_bf16_f32 v39, v39, v43\n"
      "v_mfma_f32_16x16x32_bf16 v[76:79], a[152:155], a[196:199], v[76:79]\n"
      "v_accvgpr_write_b32 a128, v36\n"
      "v_accvgpr_write_b32 a129, v37\n"
      "v_mfma_f32_16x16x32_bf16 v[80:83], a[144:147], a[196:199], v[80:83]\n"
      "v_accvgpr_write_b32 a130, v38\n"
      "v_accvgpr_write_b32 a131, v39\n"
      "v_mfma_f32_16x16x32_bf16 v[84:87], a[136:139], a[196:199], v[84:87]\n"
      "v_cvt_pk_bf16_f32 v28, v28, v32\n"
      "v_cvt_pk_bf16_f32 v29, v29, v33\n"
      "v_mfma_f32_16x16x32_bf16 v[88:91], a[148:151], a[200:203], v[88:91]\n"
      "v_cvt_pk_bf16_f32 v30, v30, v34\n"
      "v_cvt_pk_bf16_f32 v31, v31, v35\n"
      "v_mfma_f32_16x16x32_bf16 v[92:95], a[140:143], a[200:203], v[92:95]\n"
      "v_accvgpr_write_b32 a124, v28\n"
      "v_accvgpr_write_b32 a125, v29\n"
      "v_mfma_f32_16x16x32_bf16 v[96:99], a[132:135], a[200:203], v[96:99]\n"
      "v_accvgpr_write_b32 a126, v30\n"
      "v_accvgpr_write_b32 a127, v31\n"
      "v_mfma_f32_4x4x4_16b_bf16 v[28:31], a[244:245], a[104:105], 0\n"
      "v_mfma_f32_4x4x4_16b_bf16 v[32:35], a[244:245], a[106:107], 0\n"
      "ds_read_b128 a[208:211], v2 offset:128\n"
      "v_mfma_f32_4x4x4_16b_bf16 v[28:31], a[248:249], a[240:241], v[28:31]\n"
      "ds_read_b128 a[212:215], v2 offset:4224\n"
      "v_mfma_f32_4x4x4_16b_bf16 v[32:35], a[248:249], a[242:243], v[32:35]\n"
      "v_mfma_f32_4x4x4_16b_bf16 v[36:39], a[246:247], a[236:237], v[28:31]\n"
      "s_nop 0\n"
      "s_nop 0\n"
      "v_mfma_f32_4x4x4_16b_bf16 v[40:43], a[246:247], a[238:239], v[32:35]\n"
      "v_mfma_f32_4x4x4_16b_bf16 v[28:31], a[250:251], a[236:237], v[28:31]\n"
      "v_mfma_f32_4x4x4_16b_bf16 v[32:35], a[250:251], a[238:239], v[32:35]\n"
      "v_cvt_pk_bf16_f32 v52, v52, v56\n"
      "v_cvt_pk_bf16_f32 v53, v53, v57\n"
      "v_cvt_pk_bf16_f32 v54, v54, v58\n"
      "v_cvt_pk_bf16_f32 v55, v55, v59\n"
      "v_accvgpr_write_b32 a120, v52\n"
      "v_accvgpr_write_b32 a121, v53\n"
      "v_accvgpr_write_b32 a122, v54\n"
      "v_accvgpr_write_b32 a123, v55\n"
      "v_cvt_pk_bf16_f32 v44, v44, v48\n"
      "v_cvt_pk_bf16_f32 v45, v45, v49\n"
      "v_cvt_pk_bf16_f32 v46, v46, v50\n"
      "v_cvt_pk_bf16_f32 v47, v47, v51\n"
      "v_accvgpr_write_b32 a116, v44\n"
      "v_accvgpr_write_b32 a117, v45\n"
      "v_accvgpr_write_b32 a118, v46\n"
      "v_accvgpr_write_b32 a119, v47\n"
      "s_waitcnt lgkmcnt(0)\n"
      "s_barrier\n"
      "s_mov_b32 m0, s64\n"
      "s_nop 0\n"
      "v_mfma_f32_16x16x32_bf16 v[64:67], a[96:99], a[204:207], v[64:67]\n"
      "s_add_u32 s53, s54, 3342336\n"
      "ds_read_b128 a[192:195], v3 offset:0\n"
      "ds_read_b64_tr_b16 a[220:221], v7 offset:18944\n"
      "ds_read_b64_tr_b16 a[222:223], v7 offset:18952\n"
      "v_cvt_pk_bf16_f32 v36, v36, v40\n"
      "v_cvt_pk_bf16_f32 v37, v37, v41\n"
      "v_cvt_pk_bf16_f32 v38, v38, v42\n"
      "v_cvt_pk_bf16_f32 v39, v39, v43\n"
      "v_mfma_f32_16x16x32_bf16 v[68:71], a[100:103], a[204:207], v[68:71]\n"
      "s_add_u32 m0, s37, 82432\n"
      "ds_read_b128 a[196:199], v3 offset:64\n"
      "v_accvgpr_write_b32 a112, v36\n"
      "v_accvgpr_write_b32 a113, v37\n"
      "v_accvgpr_write_b32 a114, v38\n"
      "v_accvgpr_write_b32 a115, v39\n"
      "v_cvt_pk_bf16_f32 v28, v28, v32\n"
      "v_cvt_pk_bf16_f32 v29, v29, v33\n"
      "v_mfma_f32_16x16x32_bf16 v[72:75], a[104:107], a[204:207], v[72:75]\n"
      "s_nop 0\n"
      "buffer_load_dwordx4 v8, s[4:7], s53 offen offset:0 nt lds\n"
      "buffer_load_dwordx4 v9, s[4:7], s53 offen offset:1024 nt lds\n"
      "v_cvt_pk_bf16_f32 v30, v30, v34\n"
      "v_mfma_f32_16x16x32_bf16 v[76:79], a[128:131], a[208:211], v[76:79]\n"
      "s_mov_b32 m0, s64\n"
      "ds_read_b128 a[200:203], v3 offset:128\n"
      "ds_read_b64_tr_b16 a[224:225], v7 offset:19456\n"
      "ds_read_b64_tr_b16 a[226:227], v7 offset:19464\n"
      "v_cvt_pk_bf16_f32 v31, v31, v35\n"
      "v_accvgpr_write_b32 a108, v28\n"
      "v_accvgpr_write_b32 a109, v29\n"
      "v_accvgpr_write_b32 a110, v30\n"
      "v_accvgpr_write_b32 a111, v31\n"
      "v_mfma_f32_16x16x32_bf16 v[80:83], a[120:123], a[208:211], v[80:83]\n"
      "s_nop 0\n"
      "buffer_load_dword a252, v8, s[4:7], s40 offen\n"
      "v_mfma_f32_16x16x32_bf16 v[84:87], a[112:115], a[208:211], v[84:87]\n"
      "s_waitcnt lgkmcnt(3)\n"
      "buffer_load_dword a252, v8, s[4:7], s40 offen\n"
      "buffer_load_dword a252, v8, s[4:7], s40 offen\n"
      "v_mfma_f32_4x4x4_16b_bf16 v[20:23], a[216:217], a[220:221], 0\n"
      "buffer_load_dword a252, v8, s[4:7], s40 offen\n"
      "v_mfma_f32_4x4x4_16b_bf16 v[24:27], a[216:217], a[222:223], 0\n"
      "buffer_load_dword a252, v8, s[4:7], s40 offen\n"
      "v_mfma_f32_16x16x32_bf16 v[88:91], a[124:127], a[212:215], v[88:91]\n"
      "s_add_u32 s53, s54, 3538944\n"
      "ds_read_b128 a[204:207], v4 offset:0\n"
      "v_mfma_f32_16x16x32_bf16 v[92:95], a[116:119], a[212:215], v[92:95]\n"
      "s_add_u32 m0, s37, 86536\n"
      "v_cvt_pk_bf16_f32 v20, v20, v24\n"
      "v_cvt_pk_bf16_f32 v21, v21, v25\n"
      "v_cvt_pk_bf16_f32 v22, v22, v26\n"
      "v_mfma_f32_16x16x32_bf16 v[96:99], a[108:111], a[212:215], v[96:99]\n"
      "s_nop 0\n"
      "buffer_load_dwordx4 v8, s[4:7], s53 offen offset:0 nt lds\n"
      "buffer_load_dwordx4 v9, s[4:7], s53 offen offset:1024 nt lds\n"
      "ds_read_b64_tr_b16 a[228:229], v7 offset:19968\n"
      "ds_read_b64_tr_b16 a[230:231], v7 offset:19976\n"
      "v_mfma_f32_16x16x32_bf16 v[52:55], a[0:3], a[192:195], 0\n"
      "s_mov_b32 m0, s64\n"
      "ds_read_b128 a[208:211], v4 offset:64\n"
      "ds_write_addtid_b32 v20 offset:18432\n"
      "ds_write_addtid_b32 v21 offset:22528\n"
      "v_mfma_f32_16x16x32_bf16 v[56:59], a[4:7], a[192:195], 0\n"
      "s_waitcnt lgkmcnt(5)\n"
      "ds_write_addtid_b32 v22 offset:26624\n"
      "v_add_f32 v76, v76, v88\n"
      "v_add_f32 v77, v77, v89\n"
      "v_mfma_f32_4x4x4_16b_bf16 v[28:31], a[216:217], a[224:225], 0\n"
      "v_add_f32 v78, v78, v90\n"
      "v_mfma_f32_4x4x4_16b_bf16 v[32:35], a[216:217], a[226:227], 0\n"
      "ds_write_addtid_b32 v76 offset:45312\n"
      "ds_write_addtid_b32 v77 offset:45824\n"
      "ds_write_addtid_b32 v78 offset:46336\n"
      "v_add_f32 v79, v79, v91\n"
      "v_mfma_f32_16x16x32_bf16 v[60:63], a[8:11], a[192:195], 0\n"
      "s_nop 0\n"
      "ds_write_addtid_b32 v79 offset:46848\n"
      "v_cvt_pk_bf16_f32 v23, v23, v27\n"
      "v_cvt_pk_bf16_f32 v28, v28, v32\n"
      "v_mfma_f32_16x16x32_bf16 v[52:55], a[12:15], a[196:199], v[52:55]\n"
      "s_add_u32 s53, s54, 3735552\n"
      "ds_read_b128 a[212:215], v4 offset:128\n"
      "ds_write_addtid_b32 v28 offset:18944\n"
      "ds_write_addtid_b32 v23 offset:30720\n"
      "v_mfma_f32_16x16x32_bf16 v[56:59], a[16:19], a[196:199], v[56:59]\n"
      "s_add_u32 m0, s37, 90760\n"
      "ds_read_b64_tr_b16 a[232:233], v7 offset:20480\n"
      "ds_read_b64_tr_b16 a[234:235], v7 offset:20488\n"
      "v_add_f32_e64 v80, v80, v92\n"
      "v_mfma_f32_16x16x32_bf16 v[60:63], a[20:23], a[196:199], v[60:63]\n"
      "s_waitcnt lgkmcnt(6)\n"
      "buffer_load_dwordx4 v8, s[4:7], s53 offen offset:0 nt lds\n"
      "buffer_load_dwordx4 v9, s[4:7], s53 offen offset:1024 nt lds\n"
      "v_mfma_f32_16x16x32_bf16 v[52:55], a[24:27], a[200:203], v[52:55]\n"
      "s_mov_b32 m0, s64\n"
      "ds_read_b128 a[192:195], v5 offset:0\n"
      "ds_write_addtid_b32 v80 offset:47360\n"
      "v_add_f32 v81, v81, v93\n"
      "v_mfma_f32_4x4x4_16b_bf16 v[20:23], a[216:217], a[228:229], 0\n"
      "v_cvt_pk_bf16_f32 v29, v29, v33\n"
      "v_add_f32 v82, v82, v94\n"
      "v_mfma_f32_4x4x4_16b_bf16 v[24:27], a[216:217], a[230:231], 0\n"
      "ds_write_addtid_b32 v29 offset:23040\n"
      "ds_write_addtid_b32 v81 offset:47872\n"
      "ds_write_addtid_b32 v82 offset:48384\n"
      "v_mfma_f32_16x16x32_bf16 v[56:59], a[28:31], a[200:203], v[56:59]\n"
      "v_add_f32 v83, v83, v95\n"
      "v_cvt_pk_bf16_f32 v30, v30, v34\n"
      "v_add_f32 v84, v84, v96\n"
      "v_mfma_f32_16x16x32_bf16 v[60:63], a[32:35], a[200:203], v[60:63]\n"
      "ds_write_addtid_b32 v30 offset:27136\n"
      "ds_write_addtid_b32 v83 offset:48896\n"
      "ds_write_addtid_b32 v84 offset:49408\n"
      "v_mfma_f32_16x16x32_bf16 v[52:55], a[36:39], a[204:207], v[52:55]\n"
      "ds_read_b128 a[196:199], v5 offset:64\n"
      "v_cvt_pk_bf16_f32 v31, v31, v35\n"
      "v_cvt_pk_bf16_f32 v20, v20, v24\n"
      "v_mfma_f32_16x16x32_bf16 v[56:59], a[40:43], a[204:207], v[56:59]\n"
      "ds_write_addtid_b32 v20 offset:19456\n"
      "ds_write_addtid_b32 v31 offset:31232\n"
      "v_add_f32_e64 v85, v85, v97\n"
      "v_mfma_f32_16x16x32_bf16 v[60:63], a[44:47], a[204:207], v[60:63]\n"
      "s_waitcnt lgkmcnt(6)\n"
      "ds_write_addtid_b32 v85 offset:49920\n"
      "v_add_f32 v86, v86, v98\n"
      "v_add_f32_e64 v87, v87, v99\n"
      "v_mfma_f32_4x4x4_16b_bf16 v[28:31], a[216:217], a[232:233], 0\n"
      "v_cvt_pk_bf16_f32 v21, v21, v25\n"
      "v_cvt_pk_bf16_f32 v22, v22, v26\n"
      "v_mfma_f32_4x4x4_16b_bf16 v[32:35], a[216:217], a[234:235], 0\n"
      "ds_write_addtid_b32 v21 offset:23552\n"
      "ds_write_addtid_b32 v86 offset:50432\n"
      "ds_write_addtid_b32 v22 offset:27648\n"
      "v_mfma_f32_16x16x32_bf16 v[52:55], a[48:51], a[208:211], v[52:55]\n"
      "ds_read_b128 a[200:203], v5 offset:128\n"
      "ds_write_addtid_b32 v87 offset:50944\n"
      "v_cvt_pk_bf16_f32 v28, v28, v32\n"
      "v_mfma_f32_16x16x32_bf16 v[56:59], a[52:55], a[208:211], v[56:59]\n"
      "s_nop 0\n"
      "ds_write_addtid_b32 v28 offset:19968\n"
      "v_cvt_pk_bf16_f32 v29, v29, v33\n"
      "v_cvt_pk_bf16_f32 v30, v30, v34\n"
      "v_cvt_pk_bf16_f32 v31, v31, v35\n"
      "v_cvt_pk_bf16_f32 v23, v23, v27\n"
      "v_mfma_f32_16x16x32_bf16 v[60:63], a[56:59], a[208:211], v[60:63]\n"
      "s_nop 0\n"
      "ds_write_addtid_b32 v29 offset:24064\n"
      "ds_write_addtid_b32 v23 offset:31744\n"
      "v_mfma_f32_16x16x32_bf16 v[52:55], a[60:63], a[212:215], v[52:55]\n"
      "s_nop 0\n"
      "ds_write_addtid_b32 v30 offset:28160\n"
      "ds_write_addtid_b32 v31 offset:32256\n"
      "v_fma_f32 v40, -2.0, v88, v76\n"
      "v_mfma_f32_16x16x32_bf16 v[56:59], a[64:67], a[212:215], v[56:59]\n"
      "s_waitcnt vmcnt(19)\n"
      "v_fma_f32 v41, -2.0, v89, v77\n"
      "v_fma_f32 v42, -2.0, v90, v78\n"
      "v_fma_f32 v43, -2.0, v91, v79\n"
      "v_mfma_f32_16x16x32_bf16 v[60:63], a[68:71], a[212:215], v[60:63]\n"
      "s_waitcnt lgkmcnt(0)\n"
      "v_fma_f32 v44, -2.0, v92, v80\n"
      "v_fma_f32 v45, -2.0, v93, v81\n"
      "s_barrier\n"
      "s_add_u32 s54, s54, 3735552\n"
      "LDloop_%=:\n"
      "v_mfma_f32_16x16x32_bf16 v[52:55], a[72:75], a[192:195], v[52:55]\n"
      "s_add_u32 s58, s40, 0\n"
      "ds_read_b128 a[204:207], v0 offset:16384\n"
      "ds_read2st64_b32 a[220:221], v10 offset0:181 offset1:183\n"
      "ds_read2st64_b32 a[222:223], v13 offset0:181 offset1:183\n"
      "ds_read2st64_b32 a[224:225], v11 offset0:181 offset1:183\n"
      "v_mfma_f32_16x16x32_bf16 v[56:59], a[76:79], a[192:195], v[56:59]\n"
      "s_mov_b32 s40, s62\n"
      "ds_read_b128 a[208:211], v0 offset:20480\n"
      "v_fma_f32 v46, -2.0, v94, v82\n"
      "v_mfma_f32_16x16x32_bf16 v[60:63], a[80:83], a[192:195], v[60:63]\n"
      "s_add_u32 s62, s62, 786432\n"
      "ds_read2st64_b32 v[28:29], v6 offset0:0 offset1:2\n"
      "ds_read2st64_b32 a[226:227], v14 offset0:181 offset1:183\n"
      "v_mfma_f32_16x16x32_bf16 v[52:55], a[84:87], a[196:199], v[52:55]\n"
      "s_add_u32 s59, s58, 196608\n"
      "ds_read_b128 a[212:215], v0 offset:16448\n"
      "v_fma_f32 v47, -2.0, v95, v83\n"
      "v_fma_f32 v48, -2.0, v96, v84\n"
      "v_mfma_f32_16x16x32_bf16 v[56:59], a[88:91], a[196:199], v[56:59]\n"
      "s_nop 0\n"
      "s_waitcnt lgkmcnt(5)\n"
      "ds_read2st64_b32 v[30:31], v6 offset0:4 offset1:6\n"
      "v_fma_f32 v49, -2.0, v97, v85\n"
      "v_mfma_f32_16x16x32_bf16 v[60:63], a[92:95], a[196:199], v[60:63]\n"
      "s_add_u32 s54, s54, 196608\n"
      "v_fma_f32 v50, -2.0, v98, v86\n"
      "v_fma_f32 v51, -2.0, v99, v87\n"
      "v_mfma_f32_16x16x32_bf16 v[52:55], a[96:99], a[200:203], v[52:55]\n"
      "s_add_u32 m0, s37, 95104\n"
      "buffer_store_dwordx4 a[220:223], v16, s[8:11], s58 offen offset:0 sc1\n"
      "ds_read_b128 a[192:195], v0 offset:20544\n"
      "v_mfma_f32_16x16x32_bf16 v[56:59], a[100:103], a[200:203], v[56:59]\n"
      "buffer_load_dwordx4 v8, s[4:7], s54 offen offset:0 nt lds\n"
      "buffer_load_dwordx4 v9, s[4:7], s54 offen offset:1024 nt lds\n"
      "ds_read2st64_b32 v[32:33], v6 offset0:8 offset1:10\n"
      "v_mfma_f32_16x16x32_bf16 v[60:63], a[104:107], a[200:203], v[60:63]\n"
      "s_mov_b32 m0, s64\n"
      "ds_read2st64_b32 v[34:35], v6 offset0:12 offset1:14\n"
      "ds_read2st64_b32 v[36:37], v6 offset0:16 offset1:18\n"
      "ds_read2st64_b32 v[38:39], v6 offset0:20 offset1:22\n"
      "v_mfma_f32_16x16x32_bf16 v[76:79], v[252:255], a[204:207], 0\n"
      "s_nop 0\n"
      "ds_read_b128 a[196:199], v0 offset:16512\n"
      "v_mfma_f32_16x16x32_bf16 v[80:83], v[244:247], a[204:207], 0\n"
      "ds_read_b64_tr_b16 a[244:245], v7 offset:36864\n"
      "ds_read2st64_b32 a[228:229], v12 offset0:181 offset1:183\n"
      "ds_read_b64_tr_b16 a[246:247], v7 offset:36872\n"
      "v_mfma_f32_16x16x32_bf16 v[84:87], v[236:239], a[204:207], 0\n"
      "s_waitcnt lgkmcnt(9)\n"
      "ds_read2st64_b32 a[230:231], v15 offset0:181 offset1:183\n"
      "v_fmac_f32 v40, s46, v28\n"
      "v_fmac_f32_e64 v41, s46, v29\n"
      "v_mfma_f32_16x16x32_bf16 v[88:91], v[248:251], a[208:211], 0\n"
      "ds_read_b128 a[200:203], v0 offset:20608\n"
      "v_fmac_f32 v42, s46, v30\n"
      "v_mfma_f32_16x16x32_bf16 v[92:95], v[240:243], a[208:211], 0\n"
      "buffer_store_dwordx4 a[224:227], v16, s[8:11], s58 offen offset:128 sc1\n"
      "v_mfma_f32_16x16x32_bf16 v[96:99], v[232:235], a[208:211], 0\n"
      "v_fmac_f32 v43, s46, v31\n"
      "v_fma_f32 v28, 0.5, v28, v64\n"
      "v_fma_f32 v29, 0.5, v29, v65\n"
      "v_mfma_f32_16x16x32_bf16 v[76:79], v[228:231], a[212:215], v[76:79]\n"
      "ds_read_b128 a[204:207], v1 offset:16384\n"
      "v_fma_f32 v30, 0.5, v30, v66\n"
      "v_mfma_f32_16x16x32_bf16 v[80:83], v[220:223], a[212:215], v[80:83]\n"
      "v_fma_f32 v31, 0.5, v31, v67\n"
      "v_fmac_f32 v40, s48, v52\n"
      "v_fmac_f32 v41, s48, v53\n"
      "v_mfma_f32_16x16x32_bf16 v[84:87], v[212:215], a[212:215], v[84:87]\n"
      "s_waitcnt lgkmcnt(1)\n"
      "v_fmac_f32 v42, s48, v54\n"
      "v_fmac_f32 v43, s48, v55\n"
      "v_mfma_f32_4x4x4_16b_bf16 v[20:23], a[216:217], a[244:245], 0\n"
      "v_fmac_f32 v28, s45, v40\n"
      "v_mfma_f32_4x4x4_16b_bf16 v[24:27], a[216:217], a[246:247], 0\n"
      "v_fmac_f32 v29, s45, v41\n"
      "v_fmac_f32 v30, s45, v42\n"
      "v_mfma_f32_16x16x32_bf16 v[88:91], v[224:227], a[192:195], v[88:91]\n"
      "ds_read_b128 a[208:211], v1 offset:20480\n"
      "v_fmac_f32_e64 v31, s45, v43\n"
      "v_cvt_pk_bf16_f32 v20, v20, v24\n"
      "v_mfma_f32_16x16x32_bf16 v[92:95], v[216:219], a[192:195], v[92:95]\n"
      "ds_write_addtid_b32 v20 offset:3072\n"
      "v_fma_f32 v40, s47, v40, -v52\n"
      "v_mfma_f32_16x16x32_bf16 v[96:99], v[208:211], a[192:195], v[96:99]\n"
      "s_add_u32 s54, s54, 196608\n"
      "v_cvt_pk_bf16_f32 v21, v21, v25\n"
      "v_fma_f32 v41, s47, v41, -v53\n"
      "v_mfma_f32_16x16x32_bf16 v[76:79], v[204:207], a[196:199], v[76:79]\n"
      "s_add_u32 m0, s37, 99328\n"
      "ds_read_b128 a[212:215], v1 offset:16448\n"
      "v_fma_f32 v42, s47, v42, -v54\n"
      "v_mfma_f32_16x16x32_bf16 v[80:83], v[196:199], a[196:199], v[80:83]\n"
      "s_nop 0\n"
      "buffer_load_dwordx4 v8, s[4:7], s54 offen offset:0 nt lds\n"
      "buffer_load_dwordx4 v9, s[4:7], s54 offen offset:1024 nt lds\n"
      "v_mfma_f32_16x16x32_bf16 v[84:87], v[188:191], a[196:199], v[84:87]\n"
      "s_mov_b32 m0, s64\n"
      "v_fma_f32 v43, s47, v43, -v55\n"
      "v_cvt_pk_bf16_f32 v22, v22, v26\n"
      "v_mfma_f32_16x16x32_bf16 v[88:91], v[200:203], a[200:203], v[88:91]\n"
      "buffer_store_dwordx4 a[228:231], v16, s[8:11], s58 offen offset:256 sc1\n"
      "ds_read_b128 a[192:195], v1 offset:20544\n"
      "v_mfma_f32_16x16x32_bf16 v[92:95], v[192:195], a[200:203], v[92:95]\n"
      "ds_write_addtid_b32 v21 offset:7168\n"
      "ds_read2st64_b32 a[232:233], v10 offset0:4 offset1:6\n"
      "ds_write_addtid_b32 v22 offset:11264\n"
      "v_cvt_pk_bf16_f32 v28, v28, v29\n"
      "v_mfma_f32_16x16x32_bf16 v[96:99], v[184:187], a[200:203], v[96:99]\n"
      "s_waitcnt lgkmcnt(4)\n"
      "ds_write_addtid_b32 v28 offset:32768\n"
      "ds_read2st64_b32 a[234:235], v13 offset0:4 offset1:6\n"
      "v_cvt_pk_bf16_f32 v30, v30, v31\n"
      "v_mfma_f32_16x16x32_bf16 v[76:79], v[180:183], a[204:207], v[76:79]\n"
      "s_nop 0\n"
      "ds_read_b128 a[196:199], v1 offset:16512\n"
      "ds_write_addtid_b32 v30 offset:33280\n"
      "v_mfma_f32_16x16x32_bf16 v[80:83], v[172:175], a[204:207], v[80:83]\n"
      "v_cvt_pk_bf16_f32 v40, v40, v41\n"
      "v_cvt_pk_bf16_f32 v23, v23, v27\n"
      "v_cvt_pk_bf16_f32 v42, v42, v43\n"
      "v_mfma_f32_16x16x32_bf16 v[84:87], v[164:167], a[204:207], v[84:87]\n"
      "ds_write_addtid_b32 v40 offset:34816\n"
      "ds_write_addtid_b32 v42 offset:35328\n"
      "ds_write_addtid_b32 v23 offset:15360\n"
      "v_mfma_f32_16x16x32_bf16 v[88:91], v[176:179], a[208:211], v[88:91]\n"
      "ds_read_b128 a[200:203], v1 offset:20608\n"
      "v_fmac_f32 v44, s46, v32\n"
      "v_mfma_f32_16x16x32_bf16 v[92:95], v[168:171], a[208:211], v[92:95]\n"
      "v_fmac_f32 v45, s46, v33\n"
      "v_fmac_f32 v46, s46, v34\n"
      "v_mfma_f32_16x16x32_bf16 v[96:99], v[160:163], a[208:211], v[96:99]\n"
      "v_fmac_f32 v47, s46, v35\n"
      "v_fma_f32 v32, 0.5, v32, v68\n"
      "v_mfma_f32_16x16x32_bf16 v[76:79], v[156:159], a[212:215], v[76:79]\n"
      "ds_read_b128 a[204:207], v2 offset:16384\n"
      "v_fma_f32 v33, 0.5, v33, v69\n"
      "v_mfma_f32_16x16x32_bf16 v[80:83], v[148:151], a[212:215], v[80:83]\n"
      "v_fma_f32 v34, 0.5, v34, v70\n"
      "v_fma_f32 v35, 0.5, v35, v71\n"
      "v_mfma_f32_16x16x32_bf16 v[84:87], v[140:143], a[212:215], v[84:87]\n"
      "s_waitcnt lgkmcnt(1)\n"
      "v_fmac_f32 v44, s48, v56\n"
      "v_fmac_f32 v45, s48, v57\n"
      "v_fmac_f32 v46, s48, v58\n"
      "v_mfma_f32_16x16x32_bf16 v[88:91], v[152:155], a[192:195], v[88:91]\n"
      "ds_read_b128 a[208:211], v2 offset:20480\n"
      "v_fmac_f32 v47, s48, v59\n"
      "v_mfma_f32_16x16x32_bf16 v[92:95], v[144:147], a[192:195], v[92:95]\n"
      "v_fmac_f32 v32, s45, v44\n"
      "v_fmac_f32 v33, s45, v45\n"
      "v_mfma_f32_16x16x32_bf16 v[96:99], v[136:139], a[192:195], v[96:99]\n"
      "s_nop 0\n"
      "buffer_store_dwordx4 a[232:235], v16, s[8:11], s59 offen offset:0 sc1\n"
      "ds_read_b64_tr_b16 a[248:249], v7 offset:37376\n"
      "v_mfma_f32_16x16x32_bf16 v[76:79], v[132:135], a[196:199], v[76:79]\n"
      "ds_read_b128 a[212:215], v2 offset:16448\n"
      "ds_read_b64_tr_b16 a[250:251], v7 offset:37384\n"
      "v_fmac_f32 v34, s45, v46\n"
      "v_mfma_f32_16x16x32_bf16 v[80:83], v[124:127], a[196:199], v[80:83]\n"
      "v_fmac_f32 v35, s45, v47\n"
      "v_fma_f32 v44, s47, v44, -v56\n"
      "v_mfma_f32_16x16x32_bf16 v[84:87], v[116:119], a[196:199], v[84:87]\n"
      "v_fma_f32 v45, s47, v45, -v57\n"
      "v_fma_f32 v46, s47, v46, -v58\n"
      "v_mfma_f32_16x16x32_bf16 v[88:91], v[128:131], a[200:203], v[88:91]\n"
      "ds_read_b128 a[192:195], v2 offset:20544\n"
      "v_fma_f32 v47, s47, v47, -v59\n"
      "v_mfma_f32_16x16x32_bf16 v[92:95], v[120:123], a[200:203], v[92:95]\n"
      "v_cvt_pk_bf16_f32 v32, v32, v33\n"
      "v_cvt_pk_bf16_f32 v34, v34, v35\n"
      "v_mfma_f32_16x16x32_bf16 v[96:99], v[112:115], a[200:203], v[96:99]\n"
      "s_waitcnt lgkmcnt(1)\n"
      "ds_write_addtid_b32 v32 offset:40964\n"
      "ds_write_addtid_b32 v34 offset:41476\n"
      "v_cvt_pk_bf16_f32 v44, v44, v45\n"
      "v_mfma_f32_16x16x32_bf16 v[76:79], v[108:111], a[204:207], v[76:79]\n"
      "s_nop 0\n"
      "ds_read_b128 a[196:199], v2 offset:16512\n"
      "ds_write_addtid_b32 v44 offset:43012\n"
      "v_cvt_pk_bf16_f32 v46, v46, v47\n"
      "v_mfma_f32_4x4x4_16b_bf16 v[20:23], a[216:217], a[248:249], 0\n"
      "v_fmac_f32 v48, s46, v36\n"
      "v_mfma_f32_4x4x4_16b_bf16 v[24:27], a[216:217], a[250:251], 0\n"
      "ds_write_addtid_b32 v46 offset:43524\n"
      "v_fmac_f32 v49, s46, v37\n"
      "v_fmac_f32 v50, s46, v38\n"
      "v_mfma_f32_16x16x32_bf16 v[80:83], v[100:103], a[204:207], v[80:83]\n"
      "v_fmac_f32 v51, s46, v39\n"
      "v_cvt_pk_bf16_f32 v20, v20, v24\n"
      "v_fma_f32 v36, 0.5, v36, v72\n"
      "v_mfma_f32_16x16x32_bf16 v[84:87], a[160:163], a[204:207], v[84:87]\n"
      "s_add_u32 s54, s54, 196608\n"
      "ds_write_addtid_b32 v20 offset:3584\n"
      "v_fma_f32 v37, 0.5, v37, v73\n"
      "v_mfma_f32_16x16x32_bf16 v[88:91], v[104:107], a[208:211], v[88:91]\n"
      "s_add_u32 m0, s37, 103432\n"
      "ds_read_b128 a[200:203], v2 offset:20608\n"
      "ds_read2st64_b32 a[236:237], v11 offset0:4 offset1:6\n"
      "ds_read2st64_b32 a[238:239], v14 offset0:4 offset1:6\n"
      "v_mfma_f32_16x16x32_bf16 v[92:95], a[164:167], a[208:211], v[92:95]\n"
      "s_nop 0\n"
      "buffer_load_dwordx4 v8, s[4:7], s54 offen offset:0 nt lds\n"
      "buffer_load_dwordx4 v9, s[4:7], s54 offen offset:1024 nt lds\n"
      "v_mfma_f32_16x16x32_bf16 v[96:99], a[156:159], a[208:211], v[96:99]\n"
      "s_mov_b32 m0, s64\n"
      "v_fma_f32 v38, 0.5, v38, v74\n"
      "v_fma_f32 v39, 0.5, v39, v75\n"
      "v_cvt_pk_bf16_f32 v21, v21, v25\n"
      "v_mfma_f32_16x16x32_bf16 v[76:79], a[152:155], a[212:215], v[76:79]\n"
      "ds_read_b128 a[204:207], v3 offset:16896\n"
      "ds_write_addtid_b32 v21 offset:7680\n"
      "v_mfma_f32_16x16x32_bf16 v[80:83], a[144:147], a[212:215], v[80:83]\n"
      "v_fmac_f32 v48, s48, v60\n"
      "v_fmac_f32 v49, s48, v61\n"
      "v_cvt_pk_bf16_f32 v22, v22, v26\n"
      "v_mfma_f32_16x16x32_bf16 v[84:87], a[136:139], a[212:215], v[84:87]\n"
      "s_waitcnt lgkmcnt(2)\n"
      "ds_write_addtid_b32 v22 offset:11776\n"
      "v_fmac_f32 v50, s48, v62\n"
      "v_mfma_f32_16x16x32_bf16 v[88:91], a[148:151], a[192:195], v[88:91]\n"
      "ds_read_b128 a[208:211], v3 offset:16960\n"
      "v_fmac_f32 v51, s48, v63\n"
      "v_mfma_f32_16x16x32_bf16 v[92:95], a[140:143], a[192:195], v[92:95]\n"
      "v_fmac_f32 v36, s45, v48\n"
      "v_cvt_pk_bf16_f32 v23, v23, v27\n"
      "v_mfma_f32_16x16x32_bf16 v[96:99], a[132:135], a[192:195], v[96:99]\n"
      "ds_write_addtid_b32 v23 offset:15872\n"
      "ds_read2st64_b32 a[240:241], v12 offset0:4 offset1:6\n"
      "ds_read2st64_b32 a[242:243], v15 offset0:4 offset1:6\n"
      "v_mfma_f32_16x16x32_bf16 v[76:79], a[128:131], a[196:199], v[76:79]\n"
      "ds_read_b128 a[212:215], v3 offset:17024\n"
      "v_fmac_f32 v37, s45, v49\n"
      "v_fmac_f32 v38, s45, v50\n"
      "v_mfma_f32_16x16x32_bf16 v[80:83], a[120:123], a[196:199], v[80:83]\n"
      "s_nop 0\n"
      "v_fmac_f32_e64 v39, s45, v51\n"
      "v_fma_f32 v48, s47, v48, -v60\n"
      "v_mfma_f32_16x16x32_bf16 v[84:87], a[112:115], a[196:199], v[84:87]\n"
      "s_nop 0\n"
      "v_fma_f32 v49, s47, v49, -v61\n"
      "v_fma_f32 v50, s47, v50, -v62\n"
      "v_mfma_f32_16x16x32_bf16 v[88:91], a[124:127], a[200:203], v[88:91]\n"
      "s_add_u32 s54, s54, 196608\n"
      "ds_read_b128 a[192:195], v4 offset:16896\n"
      "v_fma_f32 v51, s47, v51, -v63\n"
      "v_mfma_f32_16x16x32_bf16 v[92:95], a[116:119], a[200:203], v[92:95]\n"
      "s_add_u32 m0, s37, 107656\n"
      "v_cvt_pk_bf16_f32 v36, v36, v37\n"
      "v_cvt_pk_bf16_f32 v38, v38, v39\n"
      "v_mfma_f32_16x16x32_bf16 v[96:99], a[108:111], a[200:203], v[96:99]\n"
      "s_waitcnt lgkmcnt(1)\n"
      "buffer_store_dwordx4 a[236:239], v16, s[8:11], s59 offen offset:128 sc1\n"
      "buffer_load_dwordx4 v8, s[4:7], s54 offen offset:0 nt lds\n"
      "buffer_load_dwordx4 v9, s[4:7], s54 offen offset:1024 nt lds\n"
      "v_mfma_f32_16x16x32_bf16 v[64:67], a[0:3], a[204:207], 0\n"
      "s_mov_b32 m0, s64\n"
      "ds_read_b128 a[196:199], v4 offset:16960\n"
      "ds_write_addtid_b32 v36 offset:36864\n"
      "ds_write_addtid_b32 v38 offset:37376\n"
      "v_add_f32 v76, v76, v88\n"
      "v_mfma_f32_16x16x32_bf16 v[68:71], a[4:7], a[204:207], 0\n"
      "ds_write_addtid_b32 v76 offset:51456\n"
      "v_add_f32 v77, v77, v89\n"
      "v_mfma_f32_16x16x32_bf16 v[72:75], a[8:11], a[204:207], 0\n"
      "ds_write_addtid_b32 v77 offset:51968\n"
      "v_add_f32 v78, v78, v90\n"
      "v_add_f32 v79, v79, v91\n"
      "v_mfma_f32_16x16x32_bf16 v[64:67], a[12:15], a[208:211], v[64:67]\n"
      "ds_read_b128 a[200:203], v4 offset:17024\n"
      "ds_write_addtid_b32 v78 offset:52480\n"
      "v_mfma_f32_16x16x32_bf16 v[68:71], a[16:19], a[208:211], v[68:71]\n"
      "ds_write_addtid_b32 v79 offset:52992\n"
      "v_add_f32 v80, v80, v92\n"
      "v_cvt_pk_bf16_f32 v48, v48, v49\n"
      "v_mfma_f32_16x16x32_bf16 v[72:75], a[20:23], a[208:211], v[72:75]\n"
      "s_nop 0\n"
      "ds_write_addtid_b32 v48 offset:38912\n"
      "ds_write_addtid_b32 v80 offset:53504\n"
      "v_mfma_f32_16x16x32_bf16 v[64:67], a[24:27], a[212:215], v[64:67]\n"
      "ds_read_b128 a[204:207], v5 offset:16896\n"
      "v_add_f32 v81, v81, v93\n"
      "v_mfma_f32_16x16x32_bf16 v[68:71], a[28:31], a[212:215], v[68:71]\n"
      "buffer_store_dwordx4 a[240:243], v16, s[8:11], s59 offen offset:256 sc1\n"
      "ds_write_addtid_b32 v81 offset:54016\n"
      "v_mfma_f32_16x16x32_bf16 v[72:75], a[32:35], a[212:215], v[72:75]\n"
      "s_waitcnt lgkmcnt(4)\n"
      "v_add_f32 v82, v82, v94\n"
      "v_add_f32 v83, v83, v95\n"
      "v_mfma_f32_16x16x32_bf16 v[64:67], a[36:39], a[192:195], v[64:67]\n"
      "ds_read_b128 a[208:211], v5 offset:16960\n"
      "ds_write_addtid_b32 v82 offset:54528\n"
      "ds_write_addtid_b32 v83 offset:55040\n"
      "v_cvt_pk_bf16_f32 v50, v50, v51\n"
      "v_mfma_f32_16x16x32_bf16 v[68:71], a[40:43], a[192:195], v[68:71]\n"
      "ds_write_addtid_b32 v50 offset:39424\n"
      "v_add_f32_e64 v84, v84, v96\n"
      "v_mfma_f32_16x16x32_bf16 v[72:75], a[44:47], a[192:195], v[72:75]\n"
      "ds_write_addtid_b32 v84 offset:55552\n"
      "v_add_f32 v85, v85, v97\n"
      "v_add_f32_e64 v86, v86, v98\n"
      "v_add_f32_e64 v87, v87, v99\n"
      "v_mfma_f32_16x16x32_bf16 v[64:67], a[48:51], a[196:199], v[64:67]\n"
      "s_nop 0\n"
      "ds_read_b128 a[212:215], v5 offset:17024\n"
      "ds_write_addtid_b32 v85 offset:56064\n"
      "v_mfma_f32_16x16x32_bf16 v[68:71], a[52:55], a[196:199], v[68:71]\n"
      "s_nop 0\n"
      "ds_write_addtid_b32 v86 offset:56576\n"
      "ds_write_addtid_b32 v87 offset:57088\n"
      "v_mfma_f32_16x16x32_bf16 v[72:75], a[56:59], a[196:199], v[72:75]\n"
      "s_nop 0\n"
      "v_fma_f32 v40, -2.0, v88, v76\n"
      "v_fma_f32 v41, -2.0, v89, v77\n"
      "v_mfma_f32_16x16x32_bf16 v[64:67], a[60:63], a[200:203], v[64:67]\n"
      "s_nop 0\n"
      "v_fma_f32 v42, -2.0, v90, v78\n"
      "v_fma_f32 v43, -2.0, v91, v79\n"
      "v_mfma_f32_16x16x32_bf16 v[68:71], a[64:67], a[200:203], v[68:71]\n"
      "s_waitcnt vmcnt(25)\n"
      "v_fma_f32 v44, -2.0, v92, v80\n"
      "v_fma_f32 v45, -2.0, v93, v81\n"
      "v_mfma_f32_16x16x32_bf16 v[72:75], a[68:71], a[200:203], v[72:75]\n"
      "s_waitcnt lgkmcnt(0)\n"
      "s_barrier\n"
      "LDmid_%=:\n"
      "v_mfma_f32_16x16x32_bf16 v[64:67], a[72:75], a[204:207], v[64:67]\n"
      "s_add_u32 s58, s40, 0\n"
      "ds_read_b128 a[192:195], v0 offset:0\n"
      "ds_read2st64_b32 a[220:221], v10 offset0:177 offset1:179\n"
      "ds_read2st64_b32 a[222:223], v13 offset0:177 offset1:179\n"
      "ds_read2st64_b32 a[224:225], v11 offset0:177 offset1:179\n"
      "v_mfma_f32_16x16x32_bf16 v[68:71], a[76:79], a[204:207], v[68:71]\n"
      "s_mov_b32 s40, s62\n"
      "ds_read_b128 a[196:199], v0 offset:4096\n"
      "v_fma_f32 v46, -2.0, v94, v82\n"
      "v_mfma_f32_16x16x32_bf16 v[72:75], a[80:83], a[204:207], v[72:75]\n"
      "s_add_u32 s62, s62, 786432\n"
      "ds_read2st64_b32 v[28:29], v6 offset0:24 offset1:26\n"
      "ds_read2st64_b32 a[226:227], v14 offset0:177 offset1:179\n"
      "v_mfma_f32_16x16x32_bf16 v[64:67], a[84:87], a[208:211], v[64:67]\n"
      "s_add_u32 s59, s58, 196608\n"
      "ds_read_b128 a[200:203], v0 offset:64\n"
      "v_fma_f32 v47, -2.0, v95, v83\n"
      "v_fma_f32 v48, -2.0, v96, v84\n"
      "v_mfma_f32_16x16x32_bf16 v[68:71], a[88:91], a[208:211], v[68:71]\n"
      "s_nop 0\n"
      "s_waitcnt lgkmcnt(5)\n"
      "ds_read2st64_b32 v[30:31], v6 offset0:28 offset1:30\n"
      "v_fma_f32 v49, -2.0, v97, v85\n"
      "v_mfma_f32_16x16x32_bf16 v[72:75], a[92:95], a[208:211], v[72:75]\n"
      "s_add_u32 s54, s54, 196608\n"
      "v_fma_f32 v50, -2.0, v98, v86\n"
      "v_fma_f32 v51, -2.0, v99, v87\n"
      "v_mfma_f32_16x16x32_bf16 v[64:67], a[96:99], a[212:215], v[64:67]\n"
      "s_add_u32 m0, s37, 112000\n"
      "buffer_store_dwordx4 a[220:223], v16, s[8:11], s58 offen offset:0 sc1\n"
      "ds_read_b128 a[204:207], v0 offset:4160\n"
      "v_mfma_f32_16x16x32_bf16 v[68:71], a[100:103], a[212:215], v[68:71]\n"
      "buffer_load_dwordx4 v8, s[4:7], s54 offen offset:0 nt lds\n"
      "buffer_load_dwordx4 v9, s[4:7], s54 offen offset:1024 nt lds\n"
      "ds_read2st64_b32 v[32:33], v6 offset0:32 offset1:34\n"
      "v_mfma_f32_16x16x32_bf16 v[72:75], a[104:107], a[212:215], v[72:75]\n"
      "s_mov_b32 m0, s64\n"
      "ds_read2st64_b32 v[34:35], v6 offset0:36 offset1:38\n"
      "ds_read2st64_b32 v[36:37], v6 offset0:40 offset1:42\n"
      "ds_read2st64_b32 v[38:39], v6 offset0:44 offset1:46\n"
      "v_mfma_f32_16x16x32_bf16 v[76:79], v[252:255], a[192:195], 0\n"
      "s_nop 0\n"
      "ds_read_b128 a[208:211], v0 offset:128\n"
      "v_mfma_f32_16x16x32_bf16 v[80:83], v[244:247], a[192:195], 0\n"
      "ds_read_b64_tr_b16 a[244:245], v7 offset:53760\n"
      "ds_read2st64_b32 a[228:229], v12 offset0:177 offset1:179\n"
      "ds_read_b64_tr_b16 a[246:247], v7 offset:53768\n"
      "v_mfma_f32_16x16x32_bf16 v[84:87], v[236:239], a[192:195], 0\n"
      "s_waitcnt lgkmcnt(9)\n"
      "ds_read2st64_b32 a[230:231], v15 offset0:177 offset1:179\n"
      "v_fmac_f32 v40, s46, v28\n"
      "v_fmac_f32 v41, s46, v29\n"
      "v_mfma_f32_16x16x32_bf16 v[88:91], v[248:251], a[196:199], 0\n"
      "ds_read_b128 a[212:215], v0 offset:4224\n"
      "v_fmac_f32 v42, s46, v30\n"
      "v_mfma_f32_16x16x32_bf16 v[92:95], v[240:243], a[196:199], 0\n"
      "buffer_store_dwordx4 a[224:227], v16, s[8:11], s58 offen offset:128 sc1\n"
      "v_mfma_f32_16x16x32_bf16 v[96:99], v[232:235], a[196:199], 0\n"
      "v_fmac_f32_e64 v43, s46, v31\n"
      "v_fma_f32 v28, 0.5, v28, v52\n"
      "v_fma_f32 v29, 0.5, v29, v53\n"
      "v_mfma_f32_16x16x32_bf16 v[76:79], v[228:231], a[200:203], v[76:79]\n"
      "ds_read_b128 a[192:195], v1 offset:0\n"
      "v_fma_f32 v30, 0.5, v30, v54\n"
      "v_mfma_f32_16x16x32_bf16 v[80:83], v[220:223], a[200:203], v[80:83]\n"
      "v_fma_f32 v31, 0.5, v31, v55\n"
      "v_fmac_f32 v40, s48, v64\n"
      "v_fmac_f32 v41, s48, v65\n"
      "v_mfma_f32_16x16x32_bf16 v[84:87], v[212:215], a[200:203], v[84:87]\n"
      "s_waitcnt lgkmcnt(1)\n"
      "v_fmac_f32 v42, s48, v66\n"
      "v_fmac_f32 v43, s48, v67\n"
      "v_mfma_f32_4x4x4_16b_bf16 v[20:23], a[216:217], a[244:245], 0\n"
      "v_fmac_f32 v28, s45, v40\n"
      "v_mfma_f32_4x4x4_16b_bf16 v[24:27], a[216:217], a[246:247], 0\n"
      "v_fmac_f32 v29, s45, v41\n"
      "v_fmac_f32_e64 v30, s45, v42\n"
      "v_mfma_f32_16x16x32_bf16 v[88:91], v[224:227], a[204:207], v[88:91]\n"
      "s_nop 0\n"
      "ds_read_b128 a[196:199], v1 offset:4096\n"
      "v_fmac_f32 v31, s45, v43\n"
      "v_cvt_pk_bf16_f32 v20, v20, v24\n"
      "v_mfma_f32_16x16x32_bf16 v[92:95], v[216:219], a[204:207], v[92:95]\n"
      "s_nop 0\n"
      "ds_write_addtid_b32 v20 offset:19456\n"
      "v_fma_f32 v40, s47, v40, -v64\n"
      "v_mfma_f32_16x16x32_bf16 v[96:99], v[208:211], a[204:207], v[96:99]\n"
      "s_add_u32 s54, s54, 196608\n"
      "v_cvt_pk_bf16_f32 v21, v21, v25\n"
      "v_fma_f32 v41, s47, v41, -v65\n"
      "v_mfma_f32_16x16x32_bf16 v[76:79], v[204:207], a[208:211], v[76:79]\n"
      "s_add_u32 m0, s37, 116224\n"
      "ds_read_b128 a[200:203], v1 offset:64\n"
      "v_fma_f32 v42, s47, v42, -v66\n"
      "v_mfma_f32_16x16x32_bf16 v[80:83], v[196:199], a[208:211], v[80:83]\n"
      "buffer_load_dwordx4 v8, s[4:7], s54 offen offset:0 nt lds\n"
      "buffer_load_dwordx4 v9, s[4:7], s54 offen offset:1024 nt lds\n"
      "v_mfma_f32_16x16x32_bf16 v[84:87], v[188:191], a[208:211], v[84:87]\n"
      "s_mov_b32 m0, s64\n"
      "v_fma_f32 v43, s47, v43, -v67\n"
      "v_cvt_pk_bf16_f32 v22, v22, v26\n"
      "v_mfma_f32_16x16x32_bf16 v[88:91], v[200:203], a[212:215], v[88:91]\n"
      "s_nop 0\n"
      "buffer_store_dwordx4 a[228:231], v16, s[8:11], s58 offen offset:256 sc1\n"
      "ds_read_b128 a[204:207], v1 offset:4160\n"
      "v_mfma_f32_16x16x32_bf16 v[92:95], v[192:195], a[212:215], v[92:95]\n"
      "s_nop 0\n"
      "ds_write_addtid_b32 v21 offset:23552\n"
      "ds_read2st64_b32 a[232:233], v10 offset0:0 offset1:2\n"
      "ds_write_addtid_b32 v22 offset:27648\n"
      "v_cvt_pk_bf16_f32 v28, v28, v29\n"
      "v_mfma_f32_16x16x32_bf16 v[96:99], v[184:187], a[212:215], v[96:99]\n"
      "s_waitcnt lgkmcnt(4)\n"
      "ds_write_addtid_b32 v28 offset:33792\n"
      "ds_read2st64_b32 a[234:235], v13 offset0:0 offset1:2\n"
      "v_cvt_pk_bf16_f32 v30, v30, v31\n"
      "v_mfma_f32_16x16x32_bf16 v[76:79], v[180:183], a[192:195], v[76:79]\n"
      "ds_read_b128 a[208:211], v1 offset:128\n"
      "ds_write_addtid_b32 v30 offset:34304\n"
      "v_mfma_f32_16x16x32_bf16 v[80:83], v[172:175], a[192:195], v[80:83]\n"
      "v_cvt_pk_bf16_f32 v40, v40, v41\n"
      "v_cvt_pk_bf16_f32 v23, v23, v27\n"
      "v_cvt_pk_bf16_f32 v42, v42, v43\n"
      "v_mfma_f32_16x16x32_bf16 v[84:87], v[164:167], a[192:195], v[84:87]\n"
      "ds_write_addtid_b32 v40 offset:35840\n"
      "ds_write_addtid_b32 v42 offset:36352\n"
      "ds_write_addtid_b32 v23 offset:31744\n"
      "v_mfma_f32_16x16x32_bf16 v[88:91], v[176:179], a[196:199], v[88:91]\n"
      "ds_read_b128 a[212:215], v1 offset:4224\n"
      "v_fmac_f32 v44, s46, v32\n"
      "v_mfma_f32_16x16x32_bf16 v[92:95], v[168:171], a[196:199], v[92:95]\n"
      "v_fmac_f32 v45, s46, v33\n"
      "v_fmac_f32 v46, s46, v34\n"
      "v_mfma_f32_16x16x32_bf16 v[96:99], v[160:163], a[196:199], v[96:99]\n"
      "v_fmac_f32 v47, s46, v35\n"
      "v_fma_f32 v32, 0.5, v32, v56\n"
      "v_mfma_f32_16x16x32_bf16 v[76:79], v[156:159], a[200:203], v[76:79]\n"
      "ds_read_b128 a[192:195], v2 offset:0\n"
      "v_fma_f32 v33, 0.5, v33, v57\n"
      "v_mfma_f32_16x16x32_bf16 v[80:83], v[148:151], a[200:203], v[80:83]\n"
      "v_fma_f32 v34, 0.5, v34, v58\n"
      "v_fma_f32 v35, 0.5, v35, v59\n"
      "v_mfma_f32_16x16x32_bf16 v[84:87], v[140:143], a[200:203], v[84:87]\n"
      "s_waitcnt lgkmcnt(1)\n"
      "v_fmac_f32 v44, s48, v68\n"
      "v_fmac_f32 v45, s48, v69\n"
      "v_fmac_f32 v46, s48, v70\n"
      "v_mfma_f32_16x16x32_bf16 v[88:91], v[152:155], a[204:207], v[88:91]\n"
      "ds_read_b128 a[196:199], v2 offset:4096\n"
      "v_fmac_f32 v47, s48, v71\n"
      "v_mfma_f32_16x16x32_bf16 v[92:95], v[144:147], a[204:207], v[92:95]\n"
      "v_fmac_f32 v32, s45, v44\n"
      "v_fmac_f32 v33, s45, v45\n"
      "v_mfma_f32_16x16x32_bf16 v[96:99], v[136:139], a[204:207], v[96:99]\n"
      "buffer_store_dwordx4 a[232:235], v16, s[8:11], s59 offen offset:0 sc1\n"
      "ds_read_b64_tr_b16 a[248:249], v7 offset:54272\n"
      "v_mfma_f32_16x16x32_bf16 v[76:79], v[132:135], a[208:211], v[76:79]\n"
      "ds_read_b128 a[200:203], v2 offset:64\n"
      "ds_read_b64_tr_b16 a[250:251], v7 offset:54280\n"
      "v_fmac_f32 v34, s45, v46\n"
      "v_mfma_f32_16x16x32_bf16 v[80:83], v[124:127], a[208:211], v[80:83]\n"
      "v_fmac_f32 v35, s45, v47\n"
      "v_fma_f32 v44, s47, v44, -v68\n"
      "v_mfma_f32_16x16x32_bf16 v[84:87], v[116:119], a[208:211], v[84:87]\n"
      "v_fma_f32 v45, s47, v45, -v69\n"
      "v_fma_f32 v46, s47, v46, -v70\n"
      "v_mfma_f32_16x16x32_bf16 v[88:91], v[128:131], a[212:215], v[88:91]\n"
      "s_nop 0\n"
      "ds_read_b128 a[204:207], v2 offset:4160\n"
      "v_fma_f32 v47, s47, v47, -v71\n"
      "v_mfma_f32_16x16x32_bf16 v[92:95], v[120:123], a[212:215], v[92:95]\n"
      "s_nop 0\n"
      "v_cvt_pk_bf16_f32 v32, v32, v33\n"
      "v_cvt_pk_bf16_f32 v34, v34, v35\n"
      "v_mfma_f32_16x16x32_bf16 v[96:99], v[112:115], a[212:215], v[96:99]\n"
      "s_waitcnt lgkmcnt(1)\n"
      "ds_write_addtid_b32 v32 offset:41988\n"
      "ds_write_addtid_b32 v34 offset:42500\n"
      "v_cvt_pk_bf16_f32 v44, v44, v45\n"
      "v_mfma_f32_16x16x32_bf16 v[76:79], v[108:111], a[192:195], v[76:79]\n"
      "ds_read_b128 a[208:211], v2 offset:128\n"
      "ds_write_addtid_b32 v44 offset:44036\n"
      "v_cvt_pk_bf16_f32 v46, v46, v47\n"
      "v_mfma_f32_4x4x4_16b_bf16 v[20:23], a[216:217], a[248:249], 0\n"
      "v_fmac_f32 v48, s46, v36\n"
      "v_mfma_f32_4x4x4_16b_bf16 v[24:27], a[216:217], a[250:251], 0\n"
      "ds_write_addtid_b32 v46 offset:44548\n"
      "v_fmac_f32 v49, s46, v37\n"
      "v_fmac_f32 v50, s46, v38\n"
      "v_mfma_f32_16x16x32_bf16 v[80:83], v[100:103], a[192:195], v[80:83]\n"
      "v_fmac_f32 v51, s46, v39\n"
      "v_cvt_pk_bf16_f32 v20, v20, v24\n"
      "v_fma_f32 v36, 0.5, v36, v60\n"
      "v_mfma_f32_16x16x32_bf16 v[84:87], a[160:163], a[192:195], v[84:87]\n"
      "s_add_u32 s54, s54, 196608\n"
      "ds_write_addtid_b32 v20 offset:19968\n"
      "v_fma_f32 v37, 0.5, v37, v61\n"
      "v_mfma_f32_16x16x32_bf16 v[88:91], v[104:107], a[196:199], v[88:91]\n"
      "s_add_u32 m0, s37, 120328\n"
      "ds_read_b128 a[212:215], v2 offset:4224\n"
      "ds_read2st64_b32 a[236:237], v11 offset0:0 offset1:2\n"
      "ds_read2st64_b32 a[238:239], v14 offset0:0 offset1:2\n"
      "v_mfma_f32_16x16x32_bf16 v[92:95], a[164:167], a[196:199], v[92:95]\n"
      "buffer_load_dwordx4 v8, s[4:7], s54 offen offset:0 nt lds\n"
      "buffer_load_dwordx4 v9, s[4:7], s54 offen offset:1024 nt lds\n"
      "v_mfma_f32_16x16x32_bf16 v[96:99], a[156:159], a[196:199], v[96:99]\n"
      "s_mov_b32 m0, s64\n"
      "v_fma_f32 v38, 0.5, v38, v62\n"
      "v_fma_f32 v39, 0.5, v39, v63\n"
      "v_cvt_pk_bf16_f32 v21, v21, v25\n"
      "v_mfma_f32_16x16x32_bf16 v[76:79], a[152:155], a[200:203], v[76:79]\n"
      "s_nop 0\n"
      "ds_read_b128 a[192:195], v3 offset:33792\n"
      "ds_write_addtid_b32 v21 offset:24064\n"
      "v_mfma_f32_16x16x32_bf16 v[80:83], a[144:147], a[200:203], v[80:83]\n"
      "v_fmac_f32 v48, s48, v72\n"
      "v_fmac_f32 v49, s48, v73\n"
      "v_cvt_pk_bf16_f32 v22, v22, v26\n"
      "v_mfma_f32_16x16x32_bf16 v[84:87], a[136:139], a[200:203], v[84:87]\n"
      "s_waitcnt lgkmcnt(2)\n"
      "ds_write_addtid_b32 v22 offset:28160\n"
      "v_fmac_f32 v50, s48, v74\n"
      "v_mfma_f32_16x16x32_bf16 v[88:91], a[148:151], a[204:207], v[88:91]\n"
      "ds_read_b128 a[196:199], v3 offset:33856\n"
      "v_fmac_f32 v51, s48, v75\n"
      "v_mfma_f32_16x16x32_bf16 v[92:95], a[140:143], a[204:207], v[92:95]\n"
      "v_fmac_f32 v36, s45, v48\n"
      "v_cvt_pk_bf16_f32 v23, v23, v27\n"
      "v_mfma_f32_16x16x32_bf16 v[96:99], a[132:135], a[204:207], v[96:99]\n"
      "ds_write_addtid_b32 v23 offset:32256\n"
      "ds_read2st64_b32 a[240:241], v12 offset0:0 offset1:2\n"
      "ds_read2st64_b32 a[242:243], v15 offset0:0 offset1:2\n"
      "v_mfma_f32_16x16x32_bf16 v[76:79], a[128:131], a[208:211], v[76:79]\n"
      "ds_read_b128 a[200:203], v3 offset:33920\n"
      "v_fmac_f32 v37, s45, v49\n"
      "v_fmac_f32 v38, s45, v50\n"
      "v_mfma_f32_16x16x32_bf16 v[80:83], a[120:123], a[208:211], v[80:83]\n"
      "v_fmac_f32 v39, s45, v51\n"
      "v_fma_f32 v48, s47, v48, -v72\n"
      "v_mfma_f32_16x16x32_bf16 v[84:87], a[112:115], a[208:211], v[84:87]\n"
      "s_nop 0\n"
      "v_fma_f32 v49, s47, v49, -v73\n"
      "v_fma_f32 v50, s47, v50, -v74\n"
      "v_mfma_f32_16x16x32_bf16 v[88:91], a[124:127], a[212:215], v[88:91]\n"
      "s_add_u32 s54, s54, 196608\n"
      "ds_read_b128 a[204:207], v4 offset:33792\n"
      "v_fma_f32 v51, s47, v51, -v75\n"
      "v_mfma_f32_16x16x32_bf16 v[92:95], a[116:119], a[212:215], v[92:95]\n"
      "s_add_u32 m0, s37, 124552\n"
      "v_cvt_pk_bf16_f32 v36, v36, v37\n"
      "v_cvt_pk_bf16_f32 v38, v38, v39\n"
      "v_mfma_f32_16x16x32_bf16 v[96:99], a[108:111], a[212:215], v[96:99]\n"
      "s_waitcnt lgkmcnt(1)\n"
      "buffer_store_dwordx4 a[236:239], v16, s[8:11], s59 offen offset:128 sc1\n"
      "buffer_load_dwordx4 v8, s[4:7], s54 offen offset:0 nt lds\n"
      "buffer_load_dwordx4 v9, s[4:7], s54 offen offset:1024 nt lds\n"
      "v_mfma_f32_16x16x32_bf16 v[52:55], a[0:3], a[192:195], 0\n"
      "s_mov_b32 m0, s64\n"
      "ds_read_b128 a[208:211], v4 offset:33856\n"
      "ds_write_addtid_b32 v36 offset:37888\n"
      "ds_write_addtid_b32 v38 offset:38400\n"
      "v_add_f32 v76, v76, v88\n"
      "v_mfma_f32_16x16x32_bf16 v[56:59], a[4:7], a[192:195], 0\n"
      "ds_write_addtid_b32 v76 offset:45312\n"
      "v_add_f32 v77, v77, v89\n"
      "v_mfma_f32_16x16x32_bf16 v[60:63], a[8:11], a[192:195], 0\n"
      "ds_write_addtid_b32 v77 offset:45824\n"
      "v_add_f32 v78, v78, v90\n"
      "v_add_f32 v79, v79, v91\n"
      "v_mfma_f32_16x16x32_bf16 v[52:55], a[12:15], a[196:199], v[52:55]\n"
      "ds_read_b128 a[212:215], v4 offset:33920\n"
      "ds_write_addtid_b32 v78 offset:46336\n"
      "v_mfma_f32_16x16x32_bf16 v[56:59], a[16:19], a[196:199], v[56:59]\n"
      "ds_write_addtid_b32 v79 offset:46848\n"
      "v_add_f32 v80, v80, v92\n"
      "v_cvt_pk_bf16_f32 v48, v48, v49\n"
      "v_mfma_f32_16x16x32_bf16 v[60:63], a[20:23], a[196:199], v[60:63]\n"
      "s_nop 0\n"
      "ds_write_addtid_b32 v48 offset:39936\n"
      "ds_write_addtid_b32 v80 offset:47360\n"
      "v_mfma_f32_16x16x32_bf16 v[52:55], a[24:27], a[200:203], v[52:55]\n"
      "ds_read_b128 a[192:195], v5 offset:33792\n"
      "v_add_f32 v81, v81, v93\n"
      "v_mfma_f32_16x16x32_bf16 v[56:59], a[28:31], a[200:203], v[56:59]\n"
      "buffer_store_dwordx4 a[240:243], v16, s[8:11], s59 offen offset:256 sc1\n"
      "ds_write_addtid_b32 v81 offset:47872\n"
      "v_mfma_f32_16x16x32_bf16 v[60:63], a[32:35], a[200:203], v[60:63]\n"
      "s_waitcnt lgkmcnt(4)\n"
      "v_add_f32 v82, v82, v94\n"
      "v_add_f32 v83, v83, v95\n"
      "v_mfma_f32_16x16x32_bf16 v[52:55], a[36:39], a[204:207], v[52:55]\n"
      "ds_read_b128 a[196:199], v5 offset:33856\n"
      "ds_write_addtid_b32 v82 offset:48384\n"
      "ds_write_addtid_b32 v83 offset:48896\n"
      "v_cvt_pk_bf16_f32 v50, v50, v51\n"
      "v_mfma_f32_16x16x32_bf16 v[56:59], a[40:43], a[204:207], v[56:59]\n"
      "ds_write_addtid_b32 v50 offset:40448\n"
      "v_add_f32 v84, v84, v96\n"
      "v_mfma_f32_16x16x32_bf16 v[60:63], a[44:47], a[204:207], v[60:63]\n"
      "ds_write_addtid_b32 v84 offset:49408\n"
      "v_add_f32 v85, v85, v97\n"
      "v_add_f32 v86, v86, v98\n"
      "v_add_f32_e64 v87, v87, v99\n"
      "v_mfma_f32_16x16x32_bf16 v[52:55], a[48:51], a[208:211], v[52:55]\n"
      "s_nop 0\n"
      "ds_read_b128 a[200:203], v5 offset:33920\n"
      "ds_write_addtid_b32 v85 offset:49920\n"
      "v_mfma_f32_16x16x32_bf16 v[56:59], a[52:55], a[208:211], v[56:59]\n"
      "s_nop 0\n"
      "ds_write_addtid_b32 v86 offset:50432\n"
      "ds_write_addtid_b32 v87 offset:50944\n"
      "v_mfma_f32_16x16x32_bf16 v[60:63], a[56:59], a[208:211], v[60:63]\n"
      "s_nop 0\n"
      "v_fma_f32 v40, -2.0, v88, v76\n"
      "v_fma_f32 v41, -2.0, v89, v77\n"
      "v_mfma_f32_16x16x32_bf16 v[52:55], a[60:63], a[212:215], v[52:55]\n"
      "s_nop 0\n"
      "v_fma_f32 v42, -2.0, v90, v78\n"
      "v_fma_f32 v43, -2.0, v91, v79\n"
      "v_mfma_f32_16x16x32_bf16 v[56:59], a[64:67], a[212:215], v[56:59]\n"
      "s_waitcnt vmcnt(25)\n"
      "v_fma_f32 v44, -2.0, v92, v80\n"
      "v_fma_f32 v45, -2.0, v93, v81\n"
      "v_mfma_f32_16x16x32_bf16 v[60:63], a[68:71], a[212:215], v[60:63]\n"
      "s_waitcnt lgkmcnt(0)\n"
      "s_barrier\n"
      "v_mfma_f32_16x16x32_bf16 v[52:55], a[72:75], a[192:195], v[52:55]\n"
      "s_add_u32 s58, s40, 0\n"
      "ds_read_b128 a[204:207], v0 offset:16384\n"
      "ds_read2st64_b32 a[220:221], v10 offset0:181 offset1:183\n"
      "ds_read2st64_b32 a[222:223], v13 offset0:181 offset1:183\n"
      "ds_read2st64_b32 a[224:225], v11 offset0:181 offset1:183\n"
      "v_mfma_f32_16x16x32_bf16 v[56:59], a[76:79], a[192:195], v[56:59]\n"
      "s_mov_b32 s40, s62\n"
      "ds_read_b128 a[208:211], v0 offset:20480\n"
      "v_fma_f32 v46, -2.0, v94, v82\n"
      "v_mfma_f32_16x16x32_bf16 v[60:63], a[80:83], a[192:195], v[60:63]\n"
      "s_add_u32 s62, s62, 786432\n"
      "ds_read2st64_b32 v[28:29], v6 offset0:0 offset1:2\n"
      "ds_read2st64_b32 a[226:227], v14 offset0:181 offset1:183\n"
      "v_mfma_f32_16x16x32_bf16 v[52:55], a[84:87], a[196:199], v[52:55]\n"
      "s_add_u32 s59, s58, 196608\n"
      "ds_read_b128 a[212:215], v0 offset:16448\n"
      "v_fma_f32 v47, -2.0, v95, v83\n"
      "v_fma_f32 v48, -2.0, v96, v84\n"
      "v_mfma_f32_16x16x32_bf16 v[56:59], a[88:91], a[196:199], v[56:59]\n"
      "s_nop 0\n"
      "s_waitcnt lgkmcnt(5)\n"
      "ds_read2st64_b32 v[30:31], v6 offset0:4 offset1:6\n"
      "v_fma_f32 v49, -2.0, v97, v85\n"
      "v_mfma_f32_16x16x32_bf16 v[60:63], a[92:95], a[196:199], v[60:63]\n"
      "s_add_u32 s54, s54, 196608\n"
      "v_fma_f32 v50, -2.0, v98, v86\n"
      "v_fma_f32 v51, -2.0, v99, v87\n"
      "v_mfma_f32_16x16x32_bf16 v[52:55], a[96:99], a[200:203], v[52:55]\n"
      "s_add_u32 m0, s37, 128896\n"
      "buffer_store_dwordx4 a[220:223], v16, s[8:11], s58 offen offset:0 sc1\n"
      "ds_read_b128 a[192:195], v0 offset:20544\n"
      "v_mfma_f32_16x16x32_bf16 v[56:59], a[100:103], a[200:203], v[56:59]\n"
      "buffer_load_dwordx4 v8, s[4:7], s54 offen offset:0 nt lds\n"
      "buffer_load_dwordx4 v9, s[4:7], s54 offen offset:1024 nt lds\n"
      "ds_read2st64_b32 v[32:33], v6 offset0:8 offset1:10\n"
      "v_mfma_f32_16x16x32_bf16 v[60:63], a[104:107], a[200:203], v[60:63]\n"
      "s_mov_b32 m0, s64\n"
      "ds_read2st64_b32 v[34:35], v6 offset0:12 offset1:14\n"
      "ds_read2st64_b32 v[36:37], v6 offset0:16 offset1:18\n"
      "ds_read2st64_b32 v[38:39], v6 offset0:20 offset1:22\n"
      "v_mfma_f32_16x16x32_bf16 v[76:79], v[252:255], a[204:207], 0\n"
      "s_nop 0\n"
      "ds_read_b128 a[196:199], v0 offset:16512\n"
      "v_mfma_f32_16x16x32_bf16 v[80:83], v[244:247], a[204:207], 0\n"
      "ds_read_b64_tr_b16 a[244:245], v7 offset:3072\n"
      "ds_read2st64_b32 a[228:229], v12 offset0:181 offset1:183\n"
      "ds_read_b64_tr_b16 a[246:247], v7 offset:3080\n"
      "v_mfma_f32_16x16x32_bf16 v[84:87], v[236:239], a[204:207], 0\n"
      "s_waitcnt lgkmcnt(9)\n"
      "ds_read2st64_b32 a[230:231], v15 offset0:181 offset1:183\n"
      "v_fmac_f32 v40, s46, v28\n"
      "v_fmac_f32 v41, s46, v29\n"
      "v_mfma_f32_16x16x32_bf16 v[88:91], v[248:251], a[208:211], 0\n"
      "ds_read_b128 a[200:203], v0 offset:20608\n"
      "v_fmac_f32 v42, s46, v30\n"
      "v_mfma_f32_16x16x32_bf16 v[92:95], v[240:243], a[208:211], 0\n"
      "buffer_store_dwordx4 a[224:227], v16, s[8:11], s58 offen offset:128 sc1\n"
      "v_mfma_f32_16x16x32_bf16 v[96:99], v[232:235], a[208:211], 0\n"
      "v_fmac_f32_e64 v43, s46, v31\n"
      "v_fma_f32 v28, 0.5, v28, v64\n"
      "v_fma_f32 v29, 0.5, v29, v65\n"
      "v_mfma_f32_16x16x32_bf16 v[76:79], v[228:231], a[212:215], v[76:79]\n"
      "ds_read_b128 a[204:207], v1 offset:16384\n"
      "v_fma_f32 v30, 0.5, v30, v66\n"
      "v_mfma_f32_16x16x32_bf16 v[80:83], v[220:223], a[212:215], v[80:83]\n"
      "v_fma_f32 v31, 0.5, v31, v67\n"
      "v_fmac_f32 v40, s48, v52\n"
      "v_fmac_f32 v41, s48, v53\n"
      "v_mfma_f32_16x16x32_bf16 v[84:87], v[212:215], a[212:215], v[84:87]\n"
      "s_waitcnt lgkmcnt(1)\n"
      "v_fmac_f32 v42, s48, v54\n"
      "v_fmac_f32 v43, s48, v55\n"
      "v_mfma_f32_4x4x4_16b_bf16 v[20:23], a[216:217], a[244:245], 0\n"
      "v_fmac_f32 v28, s45, v40\n"
      "v_mfma_f32_4x4x4_16b_bf16 v[24:27], a[216:217], a[246:247], 0\n"
      "v_fmac_f32 v29, s45, v41\n"
      "v_fmac_f32_e64 v30, s45, v42\n"
      "v_mfma_f32_16x16x32_bf16 v[88:91], v[224:227], a[192:195], v[88:91]\n"
      "s_nop 0\n"
      "ds_read_b128 a[208:211], v1 offset:20480\n"
      "v_fmac_f32 v31, s45, v43\n"
      "v_cvt_pk_bf16_f32 v20, v20, v24\n"
      "v_mfma_f32_16x16x32_bf16 v[92:95], v[216:219], a[192:195], v[92:95]\n"
      "s_nop 0\n"
      "ds_write_addtid_b32 v20 offset:3072\n"
      "v_fma_f32 v40, s47, v40, -v52\n"
      "v_mfma_f32_16x16x32_bf16 v[96:99], v[208:211], a[192:195], v[96:99]\n"
      "s_add_u32 s54, s54, 196608\n"
      "v_cvt_pk_bf16_f32 v21, v21, v25\n"
      "v_fma_f32 v41, s47, v41, -v53\n"
      "v_mfma_f32_16x16x32_bf16 v[76:79], v[204:207], a[196:199], v[76:79]\n"
      "s_add_u32 m0, s37, 133120\n"
      "ds_read_b128 a[212:215], v1 offset:16448\n"
      "v_fma_f32 v42, s47, v42, -v54\n"
      "v_mfma_f32_16x16x32_bf16 v[80:83], v[196:199], a[196:199], v[80:83]\n"
      "buffer_load_dwordx4 v8, s[4:7], s54 offen offset:0 nt lds\n"
      "buffer_load_dwordx4 v9, s[4:7], s54 offen offset:1024 nt lds\n"
      "v_mfma_f32_16x16x32_bf16 v[84:87], v[188:191], a[196:199], v[84:87]\n"
      "s_mov_b32 m0, s64\n"
      "v_fma_f32 v43, s47, v43, -v55\n"
      "v_cvt_pk_bf16_f32 v22, v22, v26\n"
      "v_mfma_f32_16x16x32_bf16 v[88:91], v[200:203], a[200:203], v[88:91]\n"
      "s_nop 0\n"
      "buffer_store_dwordx4 a[228:231], v16, s[8:11], s58 offen offset:256 sc1\n"
      "ds_read_b128 a[192:195], v1 offset:20544\n"
      "v_mfma_f32_16x16x32_bf16 v[92:95], v[192:195], a[200:203], v[92:95]\n"
      "s_nop 0\n"
      "ds_write_addtid_b32 v21 offset:7168\n"
      "ds_read2st64_b32 a[232:233], v10 offset0:4 offset1:6\n"
      "ds_write_addtid_b32 v22 offset:11264\n"
      "v_cvt_pk_bf16_f32 v28, v28, v29\n"
      "v_mfma_f32_16x16x32_bf16 v[96:99], v[184:187], a[200:203], v[96:99]\n"
      "s_waitcnt lgkmcnt(4)\n"
      "ds_write_addtid_b32 v28 offset:32768\n"
      "ds_read2st64_b32 a[234:235], v13 offset0:4 offset1:6\n"
      "v_cvt_pk_bf16_f32 v30, v30, v31\n"
      "v_mfma_f32_16x16x32_bf16 v[76:79], v[180:183], a[204:207], v[76:79]\n"
      "ds_read_b128 a[196:199], v1 offset:16512\n"
      "ds_write_addtid_b32 v30 offset:33280\n"
      "v_mfma_f32_16x16x32_bf16 v[80:83], v[172:175], a[204:207], v[80:83]\n"
      "v_cvt_pk_bf16_f32 v40, v40, v41\n"
      "v_cvt_pk_bf16_f32 v23, v23, v27\n"
      "v_cvt_pk_bf16_f32 v42, v42, v43\n"
      "v_mfma_f32_16x16x32_bf16 v[84:87], v[164:167], a[204:207], v[84:87]\n"
      "ds_write_addtid_b32 v40 offset:34816\n"
      "ds_write_addtid_b32 v42 offset:35328\n"
      "ds_write_addtid_b32 v23 offset:15360\n"
      "v_mfma_f32_16x16x32_bf16 v[88:91], v[176:179], a[208:211], v[88:91]\n"
      "ds_read_b128 a[200:203], v1 offset:20608\n"
      "v_fmac_f32 v44, s46, v32\n"
      "v_mfma_f32_16x16x32_bf16 v[92:95], v[168:171], a[208:211], v[92:95]\n"
      "v_fmac_f32 v45, s46, v33\n"
      "v_fmac_f32 v46, s46, v34\n"
      "v_mfma_f32_16x16x32_bf16 v[96:99], v[160:163], a[208:211], v[96:99]\n"
      "v_fmac_f32 v47, s46, v35\n"
      "v_fma_f32 v32, 0.5, v32, v68\n"
      "v_mfma_f32_16x16x32_bf16 v[76:79], v[156:159], a[212:215], v[76:79]\n"
      "ds_read_b128 a[204:207], v2 offset:16384\n"
      "v_fma_f32 v33, 0.5, v33, v69\n"
      "v_mfma_f32_16x16x32_bf16 v[80:83], v[148:151], a[212:215], v[80:83]\n"
      "v_fma_f32 v34, 0.5, v34, v70\n"
      "v_fma_f32 v35, 0.5, v35, v71\n"
      "v_mfma_f32_16x16x32_bf16 v[84:87], v[140:143], a[212:215], v[84:87]\n"
      "s_waitcnt lgkmcnt(1)\n"
      "v_fmac_f32 v44, s48, v56\n"
      "v_fmac_f32 v45, s48, v57\n"
      "v_fmac_f32 v46, s48, v58\n"
      "v_mfma_f32_16x16x32_bf16 v[88:91], v[152:155], a[192:195], v[88:91]\n"
      "ds_read_b128 a[208:211], v2 offset:20480\n"
      "v_fmac_f32 v47, s48, v59\n"
      "v_mfma_f32_16x16x32_bf16 v[92:95], v[144:147], a[192:195], v[92:95]\n"
      "v_fmac_f32 v32, s45, v44\n"
      "v_fmac_f32 v33, s45, v45\n"
      "v_mfma_f32_16x16x32_bf16 v[96:99], v[136:139], a[192:195], v[96:99]\n"
      "buffer_store_dwordx4 a[232:235], v16, s[8:11], s59 offen offset:0 sc1\n"
      "ds_read_b64_tr_b16 a[248:249], v7 offset:3584\n"
      "v_mfma_f32_16x16x32_bf16 v[76:79], v[132:135], a[196:199], v[76:79]\n"
      "ds_read_b128 a[212:215], v2 offset:16448\n"
      "ds_read_b64_tr_b16 a[250:251], v7 offset:3592\n"
      "v_fmac_f32 v34, s45, v46\n"
      "v_mfma_f32_16x16x32_bf16 v[80:83], v[124:127], a[196:199], v[80:83]\n"
      "v_fmac_f32 v35, s45, v47\n"
      "v_fma_f32 v44, s47, v44, -v56\n"
      "v_mfma_f32_16x16x32_bf16 v[84:87], v[116:119], a[196:199], v[84:87]\n"
      "v_fma_f32 v45, s47, v45, -v57\n"
      "v_fma_f32 v46, s47, v46, -v58\n"
      "v_mfma_f32_16x16x32_bf16 v[88:91], v[128:131], a[200:203], v[88:91]\n"
      "s_nop 0\n"
      "ds_read_b128 a[192:195], v2 offset:20544\n"
      "v_fma_f32 v47, s47, v47, -v59\n"
      "v_mfma_f32_16x16x32_bf16 v[92:95], v[120:123], a[200:203], v[92:95]\n"
      "s_nop 0\n"
      "v_cvt_pk_bf16_f32 v32, v32, v33\n"
      "v_cvt_pk_bf16_f32 v34, v34, v35\n"
      "v_mfma_f32_16x16x32_bf16 v[96:99], v[112:115], a[200:203], v[96:99]\n"
      "s_waitcnt lgkmcnt(1)\n"
      "ds_write_addtid_b32 v32 offset:40964\n"
      "ds_write_addtid_b32 v34 offset:41476\n"
      "v_cvt_pk_bf16_f32 v44, v44, v45\n"
      "v_mfma_f32_16x16x32_bf16 v[76:79], v[108:111], a[204:207], v[76:79]\n"
      "ds_read_b128 a[196:199], v2 offset:16512\n"
      "ds_write_addtid_b32 v44 offset:43012\n"
      "v_cvt_pk_bf16_f32 v46, v46, v47\n"
      "v_mfma_f32_4x4x4_16b_bf16 v[20:23], a[216:217], a[248:249], 0\n"
      "v_fmac_f32 v48, s46, v36\n"
      "v_mfma_f32_4x4x4_16b_bf16 v[24:27], a[216:217], a[250:251], 0\n"
      "ds_write_addtid_b32 v46 offset:43524\n"
      "v_fmac_f32 v49, s46, v37\n"
      "v_fmac_f32 v50, s46, v38\n"
      "v_mfma_f32_16x16x32_bf16 v[80:83], v[100:103], a[204:207], v[80:83]\n"
      "v_fmac_f32 v51, s46, v39\n"
      "v_cvt_pk_bf16_f32 v20, v20, v24\n"
      "v_fma_f32 v36, 0.5, v36, v72\n"
      "v_mfma_f32_16x16x32_bf16 v[84:87], a[160:163], a[204:207], v[84:87]\n"
      "s_add_u32 s54, s54, 196608\n"
      "ds_write_addtid_b32 v20 offset:3584\n"
      "v_fma_f32 v37, 0.5, v37, v73\n"
      "v_mfma_f32_16x16x32_bf16 v[88:91], v[104:107], a[208:211], v[88:91]\n"
      "s_add_u32 m0, s37, 137224\n"
      "ds_read_b128 a[200:203], v2 offset:20608\n"
      "ds_read2st64_b32 a[236:237], v11 offset0:4 offset1:6\n"
      "ds_read2st64_b32 a[238:239], v14 offset0:4 offset1:6\n"
      "v_mfma_f32_16x16x32_bf16 v[92:95], a[164:167], a[208:211], v[92:95]\n"
      "buffer_load_dwordx4 v8, s[4:7], s54 offen offset:0 nt lds\n"
      "buffer_load_dwordx4 v9, s[4:7], s54 offen offset:1024 nt lds\n"
      "v_mfma_f32_16x16x32_bf16 v[96:99], a[156:159], a[208:211], v[96:99]\n"
      "s_mov_b32 m0, s64\n"
      "v_fma_f32 v38, 0.5, v38, v74\n"
      "v_fma_f32 v39, 0.5, v39, v75\n"
      "v_cvt_pk_bf16_f32 v21, v21, v25\n"
      "v_mfma_f32_16x16x32_bf16 v[76:79], a[152:155], a[212:215], v[76:79]\n"
      "s_nop 0\n"
      "ds_read_b128 a[204:207], v3 offset:50688\n"
      "ds_write_addtid_b32 v21 offset:7680\n"
      "v_mfma_f32_16x16x32_bf16 v[80:83], a[144:147], a[212:215], v[80:83]\n"
      "v_fmac_f32 v48, s48, v60\n"
      "v_fmac_f32 v49, s48, v61\n"
      "v_cvt_pk_bf16_f32 v22, v22, v26\n"
      "v_mfma_f32_16x16x32_bf16 v[84:87], a[136:139], a[212:215], v[84:87]\n"
      "s_waitcnt lgkmcnt(2)\n"
      "ds_write_addtid_b32 v22 offset:11776\n"
      "v_fmac_f32 v50, s48, v62\n"
      "v_mfma_f32_16x16x32_bf16 v[88:91], a[148:151], a[192:195], v[88:91]\n"
      "ds_read_b128 a[208:211], v3 offset:50752\n"
      "v_fmac_f32 v51, s48, v63\n"
      "v_mfma_f32_16x16x32_bf16 v[92:95], a[140:143], a[192:195], v[92:95]\n"
      "v_fmac_f32 v36, s45, v48\n"
      "v_cvt_pk_bf16_f32 v23, v23, v27\n"
      "v_mfma_f32_16x16x32_bf16 v[96:99], a[132:135], a[192:195], v[96:99]\n"
      "ds_write_addtid_b32 v23 offset:15872\n"
      "ds_read2st64_b32 a[240:241], v12 offset0:4 offset1:6\n"
      "ds_read2st64_b32 a[242:243], v15 offset0:4 offset1:6\n"
      "v_mfma_f32_16x16x32_bf16 v[76:79], a[128:131], a[196:199], v[76:79]\n"
      "ds_read_b128 a[212:215], v3 offset:50816\n"
      "v_fmac_f32 v37, s45, v49\n"
      "v_fmac_f32 v38, s45, v50\n"
      "v_mfma_f32_16x16x32_bf16 v[80:83], a[120:123], a[196:199], v[80:83]\n"
      "v_fmac_f32 v39, s45, v51\n"
      "v_fma_f32 v48, s47, v48, -v60\n"
      "v_mfma_f32_16x16x32_bf16 v[84:87], a[112:115], a[196:199], v[84:87]\n"
      "s_nop 0\n"
      "v_fma_f32 v49, s47, v49, -v61\n"
      "v_fma_f32 v50, s47, v50, -v62\n"
      "v_mfma_f32_16x16x32_bf16 v[88:91], a[124:127], a[200:203], v[88:91]\n"
      "s_add_u32 s54, s54, 196608\n"
      "ds_read_b128 a[192:195], v4 offset:50688\n"
      "v_fma_f32 v51, s47, v51, -v63\n"
      "v_mfma_f32_16x16x32_bf16 v[92:95], a[116:119], a[200:203], v[92:95]\n"
      "s_add_u32 m0, s37, 141448\n"
      "v_cvt_pk_bf16_f32 v36, v36, v37\n"
      "v_cvt_pk_bf16_f32 v38, v38, v39\n"
      "v_mfma_f32_16x16x32_bf16 v[96:99], a[108:111], a[200:203], v[96:99]\n"
      "s_waitcnt lgkmcnt(1)\n"
      "buffer_store_dwordx4 a[236:239], v16, s[8:11], s59 offen offset:128 sc1\n"
      "buffer_load_dwordx4 v8, s[4:7], s54 offen offset:0 nt lds\n"
      "buffer_load_dwordx4 v9, s[4:7], s54 offen offset:1024 nt lds\n"
      "v_mfma_f32_16x16x32_bf16 v[64:67], a[0:3], a[204:207], 0\n"
      "s_mov_b32 m0, s64\n"
      "ds_read_b128 a[196:199], v4 offset:50752\n"
      "ds_write_addtid_b32 v36 offset:36864\n"
      "ds_write_addtid_b32 v38 offset:37376\n"
      "v_add_f32 v76, v76, v88\n"
      "v_mfma_f32_16x16x32_bf16 v[68:71], a[4:7], a[204:207], 0\n"
      "ds_write_addtid_b32 v76 offset:51456\n"
      "v_add_f32 v77, v77, v89\n"
      "v_mfma_f32_16x16x32_bf16 v[72:75], a[8:11], a[204:207], 0\n"
      "ds_write_addtid_b32 v77 offset:51968\n"
      "v_add_f32 v78, v78, v90\n"
      "v_add_f32 v79, v79, v91\n"
      "v_mfma_f32_16x16x32_bf16 v[64:67], a[12:15], a[208:211], v[64:67]\n"
      "ds_read_b128 a[200:203], v4 offset:50816\n"
      "ds_write_addtid_b32 v78 offset:52480\n"
      "v_mfma_f32_16x16x32_bf16 v[68:71], a[16:19], a[208:211], v[68:71]\n"
      "ds_write_addtid_b32 v79 offset:52992\n"
      "v_add_f32 v80, v80, v92\n"
      "v_cvt_pk_bf16_f32 v48, v48, v49\n"
      "v_mfma_f32_16x16x32_bf16 v[72:75], a[20:23], a[208:211], v[72:75]\n"
      "s_nop 0\n"
      "ds_write_addtid_b32 v48 offset:38912\n"
      "ds_write_addtid_b32 v80 offset:53504\n"
      "v_mfma_f32_16x16x32_bf16 v[64:67], a[24:27], a[212:215], v[64:67]\n"
      "ds_read_b128 a[204:207], v5 offset:50688\n"
      "v_add_f32 v81, v81, v93\n"
      "v_mfma_f32_16x16x32_bf16 v[68:71], a[28:31], a[212:215], v[68:71]\n"
      "buffer_store_dwordx4 a[240:243], v16, s[8:11], s59 offen offset:256 sc1\n"
      "ds_write_addtid_b32 v81 offset:54016\n"
      "v_mfma_f32_16x16x32_bf16 v[72:75], a[32:35], a[212:215], v[72:75]\n"
      "s_waitcnt lgkmcnt(4)\n"
      "v_add_f32 v82, v82, v94\n"
      "v_add_f32 v83, v83, v95\n"
      "v_mfma_f32_16x16x32_bf16 v[64:67], a[36:39], a[192:195], v[64:67]\n"
      "ds_read_b128 a[208:211], v5 offset:50752\n"
      "ds_write_addtid_b32 v82 offset:54528\n"
      "ds_write_addtid_b32 v83 offset:55040\n"
      "v_cvt_pk_bf16_f32 v50, v50, v51\n"
      "v_mfma_f32_16x16x32_bf16 v[68:71], a[40:43], a[192:195], v[68:71]\n"
      "ds_write_addtid_b32 v50 offset:39424\n"
      "v_add_f32 v84, v84, v96\n"
      "v_mfma_f32_16x16x32_bf16 v[72:75], a[44:47], a[192:195], v[72:75]\n"
      "ds_write_addtid_b32 v84 offset:55552\n"
      "v_add_f32 v85, v85, v97\n"
      "v_add_f32 v86, v86, v98\n"
      "v_add_f32_e64 v87, v87, v99\n"
      "v_mfma_f32_16x16x32_bf16 v[64:67], a[48:51], a[196:199], v[64:67]\n"
      "s_nop 0\n"
      "ds_read_b128 a[212:215], v5 offset:50816\n"
      "ds_write_addtid_b32 v85 offset:56064\n"
      "v_mfma_f32_16x16x32_bf16 v[68:71], a[52:55], a[196:199], v[68:71]\n"
      "s_nop 0\n"
      "ds_write_addtid_b32 v86 offset:56576\n"
      "ds_write_addtid_b32 v87 offset:57088\n"
      "v_mfma_f32_16x16x32_bf16 v[72:75], a[56:59], a[196:199], v[72:75]\n"
      "s_nop 0\n"
      "v_fma_f32 v40, -2.0, v88, v76\n"
      "v_fma_f32 v41, -2.0, v89, v77\n"
      "v_mfma_f32_16x16x32_bf16 v[64:67], a[60:63], a[200:203], v[64:67]\n"
      "s_nop 0\n"
      "v_fma_f32 v42, -2.0, v90, v78\n"
      "v_fma_f32 v43, -2.0, v91, v79\n"
      "v_mfma_f32_16x16x32_bf16 v[68:71], a[64:67], a[200:203], v[68:71]\n"
      "s_waitcnt vmcnt(25)\n"
      "v_fma_f32 v44, -2.0, v92, v80\n"
      "v_fma_f32 v45, -2.0, v93, v81\n"
      "v_mfma_f32_16x16x32_bf16 v[72:75], a[68:71], a[200:203], v[72:75]\n"
      "s_waitcnt lgkmcnt(0)\n"
      "s_barrier\n"
      "v_mfma_f32_16x16x32_bf16 v[64:67], a[72:75], a[204:207], v[64:67]\n"
      "s_add_u32 s58, s40, 0\n"
      "ds_read_b128 a[192:195], v0 offset:0\n"
      "ds_read2st64_b32 a[220:221], v10 offset0:177 offset1:179\n"
      "ds_read2st64_b32 a[222:223], v13 offset0:177 offset1:179\n"
      "ds_read2st64_b32 a[224:225], v11 offset0:177 offset1:179\n"
      "v_mfma_f32_16x16x32_bf16 v[68:71], a[76:79], a[204:207], v[68:71]\n"
      "s_mov_b32 s40, s62\n"
      "ds_read_b128 a[196:199], v0 offset:4096\n"
      "v_fma_f32 v46, -2.0, v94, v82\n"
      "v_mfma_f32_16x16x32_bf16 v[72:75], a[80:83], a[204:207], v[72:75]\n"
      "s_add_u32 s62, s62, 786432\n"
      "ds_read2st64_b32 v[28:29], v6 offset0:24 offset1:26\n"
      "ds_read2st64_b32 a[226:227], v14 offset0:177 offset1:179\n"
      "v_mfma_f32_16x16x32_bf16 v[64:67], a[84:87], a[208:211], v[64:67]\n"
      "s_add_u32 s59, s58, 196608\n"
      "ds_read_b128 a[200:203], v0 offset:64\n"
      "v_fma_f32 v47, -2.0, v95, v83\n"
      "v_fma_f32 v48, -2.0, v96, v84\n"
      "v_mfma_f32_16x16x32_bf16 v[68:71], a[88:91], a[208:211], v[68:71]\n"
      "s_nop 0\n"
      "s_waitcnt lgkmcnt(5)\n"
      "ds_read2st64_b32 v[30:31], v6 offset0:28 offset1:30\n"
      "v_fma_f32 v49, -2.0, v97, v85\n"
      "v_mfma_f32_16x16x32_bf16 v[72:75], a[92:95], a[208:211], v[72:75]\n"
      "s_add_u32 s54, s54, 196608\n"
      "v_fma_f32 v50, -2.0, v98, v86\n"
      "v_fma_f32 v51, -2.0, v99, v87\n"
      "v_mfma_f32_16x16x32_bf16 v[64:67], a[96:99], a[212:215], v[64:67]\n"
      "s_add_u32 m0, s37, 145792\n"
      "buffer_store_dwordx4 a[220:223], v16, s[8:11], s58 offen offset:0 sc1\n"
      "ds_read_b128 a[204:207], v0 offset:4160\n"
      "v_mfma_f32_16x16x32_bf16 v[68:71], a[100:103], a[212:215], v[68:71]\n"
      "buffer_load_dwordx4 v8, s[4:7], s54 offen offset:0 nt lds\n"
      "buffer_load_dwordx4 v9, s[4:7], s54 offen offset:1024 nt lds\n"
      "ds_read2st64_b32 v[32:33], v6 offset0:32 offset1:34\n"
      "v_mfma_f32_16x16x32_bf16 v[72:75], a[104:107], a[212:215], v[72:75]\n"
      "s_mov_b32 m0, s64\n"
      "ds_read2st64_b32 v[34:35], v6 offset0:36 offset1:38\n"
      "ds_read2st64_b32 v[36:37], v6 offset0:40 offset1:42\n"
      "ds_read2st64_b32 v[38:39], v6 offset0:44 offset1:46\n"
      "v_mfma_f32_16x16x32_bf16 v[76:79], v[252:255], a[192:195], 0\n"
      "s_nop 0\n"
      "ds_read_b128 a[208:211], v0 offset:128\n"
      "v_mfma_f32_16x16x32_bf16 v[80:83], v[244:247], a[192:195], 0\n"
      "ds_read_b64_tr_b16 a[244:245], v7 offset:19968\n"
      "ds_read2st64_b32 a[228:229], v12 offset0:177 offset1:179\n"
      "ds_read_b64_tr_b16 a[246:247], v7 offset:19976\n"
      "v_mfma_f32_16x16x32_bf16 v[84:87], v[236:239], a[192:195], 0\n"
      "s_waitcnt lgkmcnt(9)\n"
      "ds_read2st64_b32 a[230:231], v15 offset0:177 offset1:179\n"
      "v_fmac_f32 v40, s46, v28\n"
      "v_fmac_f32 v41, s46, v29\n"
      "v_mfma_f32_16x16x32_bf16 v[88:91], v[248:251], a[196:199], 0\n"
      "ds_read_b128 a[212:215], v0 offset:4224\n"
      "v_fmac_f32 v42, s46, v30\n"
      "v_mfma_f32_16x16x32_bf16 v[92:95], v[240:243], a[196:199], 0\n"
      "buffer_store_dwordx4 a[224:227], v16, s[8:11], s58 offen offset:128 sc1\n"
      "v_mfma_f32_16x16x32_bf16 v[96:99], v[232:235], a[196:199], 0\n"
      "v_fmac_f32_e64 v43, s46, v31\n"
      "v_fma_f32 v28, 0.5, v28, v52\n"
      "v_fma_f32 v29, 0.5, v29, v53\n"
      "v_mfma_f32_16x16x32_bf16 v[76:79], v[228:231], a[200:203], v[76:79]\n"
      "ds_read_b128 a[192:195], v1 offset:0\n"
      "v_fma_f32 v30, 0.5, v30, v54\n"
      "v_mfma_f32_16x16x32_bf16 v[80:83], v[220:223], a[200:203], v[80:83]\n"
      "v_fma_f32 v31, 0.5, v31, v55\n"
      "v_fmac_f32 v40, s48, v64\n"
      "v_fmac_f32 v41, s48, v65\n"
      "v_mfma_f32_16x16x32_bf16 v[84:87], v[212:215], a[200:203], v[84:87]\n"
      "s_waitcnt lgkmcnt(1)\n"
      "v_fmac_f32 v42, s48, v66\n"
      "v_fmac_f32 v43, s48, v67\n"
      "v_mfma_f32_4x4x4_16b_bf16 v[20:23], a[216:217], a[244:245], 0\n"
      "v_fmac_f32 v28, s45, v40\n"
      "v_mfma_f32_4x4x4_16b_bf16 v[24:27], a[216:217], a[246:247], 0\n"
      "v_fmac_f32 v29, s45, v41\n"
      "v_fmac_f32_e64 v30, s45, v42\n"
      "v_mfma_f32_16x16x32_bf16 v[88:91], v[224:227], a[204:207], v[88:91]\n"
      "s_nop 0\n"
      "ds_read_b128 a[196:199], v1 offset:4096\n"
      "v_fmac_f32 v31, s45, v43\n"
      "v_cvt_pk_bf16_f32 v20, v20, v24\n"
      "v_mfma_f32_16x16x32_bf16 v[92:95], v[216:219], a[204:207], v[92:95]\n"
      "s_nop 0\n"
      "ds_write_addtid_b32 v20 offset:19456\n"
      "v_fma_f32 v40, s47, v40, -v64\n"
      "v_mfma_f32_16x16x32_bf16 v[96:99], v[208:211], a[204:207], v[96:99]\n"
      "s_add_u32 s54, s54, 196608\n"
      "v_cvt_pk_bf16_f32 v21, v21, v25\n"
      "v_fma_f32 v41, s47, v41, -v65\n"
      "v_mfma_f32_16x16x32_bf16 v[76:79], v[204:207], a[208:211], v[76:79]\n"
      "s_add_u32 m0, s37, 82432\n"
      "ds_read_b128 a[200:203], v1 offset:64\n"
      "v_fma_f32 v42, s47, v42, -v66\n"
      "v_mfma_f32_16x16x32_bf16 v[80:83], v[196:199], a[208:211], v[80:83]\n"
      "buffer_load_dwordx4 v8, s[4:7], s54 offen offset:0 nt lds\n"
      "buffer_load_dwordx4 v9, s[4:7], s54 offen offset:1024 nt lds\n"
      "v_mfma_f32_16x16x32_bf16 v[84:87], v[188:191], a[208:211], v[84:87]\n"
      "s_mov_b32 m0, s64\n"
      "v_fma_f32 v43, s47, v43, -v67\n"
      "v_cvt_pk_bf16_f32 v22, v22, v26\n"
      "v_mfma_f32_16x16x32_bf16 v[88:91], v[200:203], a[212:215], v[88:91]\n"
      "s_nop 0\n"
      "buffer_store_dwordx4 a[228:231], v16, s[8:11], s58 offen offset:256 sc1\n"
      "ds_read_b128 a[204:207], v1 offset:4160\n"
      "v_mfma_f32_16x16x32_bf16 v[92:95], v[192:195], a[212:215], v[92:95]\n"
      "s_nop 0\n"
      "ds_write_addtid_b32 v21 offset:23552\n"
      "ds_read2st64_b32 a[232:233], v10 offset0:0 offset1:2\n"
      "ds_write_addtid_b32 v22 offset:27648\n"
      "v_cvt_pk_bf16_f32 v28, v28, v29\n"
      "v_mfma_f32_16x16x32_bf16 v[96:99], v[184:187], a[212:215], v[96:99]\n"
      "s_waitcnt lgkmcnt(4)\n"
      "ds_write_addtid_b32 v28 offset:33792\n"
      "ds_read2st64_b32 a[234:235], v13 offset0:0 offset1:2\n"
      "v_cvt_pk_bf16_f32 v30, v30, v31\n"
      "v_mfma_f32_16x16x32_bf16 v[76:79], v[180:183], a[192:195], v[76:79]\n"
      "ds_read_b128 a[208:211], v1 offset:128\n"
      "ds_write_addtid_b32 v30 offset:34304\n"
      "v_mfma_f32_16x16x32_bf16 v[80:83], v[172:175], a[192:195], v[80:83]\n"
      "v_cvt_pk_bf16_f32 v40, v40, v41\n"
      "v_cvt_pk_bf16_f32 v23, v23, v27\n"
      "v_cvt_pk_bf16_f32 v42, v42, v43\n"
      "v_mfma_f32_16x16x32_bf16 v[84:87], v[164:167], a[192:195], v[84:87]\n"
      "ds_write_addtid_b32 v40 offset:35840\n"
      "ds_write_addtid_b32 v42 offset:36352\n"
      "ds_write_addtid_b32 v23 offset:31744\n"
      "v_mfma_f32_16x16x32_bf16 v[88:91], v[176:179], a[196:199], v[88:91]\n"
      "ds_read_b128 a[212:215], v1 offset:4224\n"
      "v_fmac_f32 v44, s46, v32\n"
      "v_mfma_f32_16x16x32_bf16 v[92:95], v[168:171], a[196:199], v[92:95]\n"
      "v_fmac_f32 v45, s46, v33\n"
      "v_fmac_f32 v46, s46, v34\n"
      "v_mfma_f32_16x16x32_bf16 v[96:99], v[160:163], a[196:199], v[96:99]\n"
      "v_fmac_f32 v47, s46, v35\n"
      "v_fma_f32 v32, 0.5, v32, v56\n"
      "v_mfma_f32_16x16x32_bf16 v[76:79], v[156:159], a[200:203], v[76:79]\n"
      "ds_read_b128 a[192:195], v2 offset:0\n"
      "v_fma_f32 v33, 0.5, v33, v57\n"
      "v_mfma_f32_16x16x32_bf16 v[80:83], v[148:151], a[200:203], v[80:83]\n"
      "v_fma_f32 v34, 0.5, v34, v58\n"
      "v_fma_f32 v35, 0.5, v35, v59\n"
      "v_mfma_f32_16x16x32_bf16 v[84:87], v[140:143], a[200:203], v[84:87]\n"
      "s_waitcnt lgkmcnt(1)\n"
      "v_fmac_f32 v44, s48, v68\n"
      "v_fmac_f32 v45, s48, v69\n"
      "v_fmac_f32 v46, s48, v70\n"
      "v_mfma_f32_16x16x32_bf16 v[88:91], v[152:155], a[204:207], v[88:91]\n"
      "ds_read_b128 a[196:199], v2 offset:4096\n"
      "v_fmac_f32 v47, s48, v71\n"
      "v_mfma_f32_16x16x32_bf16 v[92:95], v[144:147], a[204:207], v[92:95]\n"
      "v_fmac_f32 v32, s45, v44\n"
      "v_fmac_f32 v33, s45, v45\n"
      "v_mfma_f32_16x16x32_bf16 v[96:99], v[136:139], a[204:207], v[96:99]\n"
      "buffer_store_dwordx4 a[232:235], v16, s[8:11], s59 offen offset:0 sc1\n"
      "ds_read_b64_tr_b16 a[248:249], v7 offset:20480\n"
      "v_mfma_f32_16x16x32_bf16 v[76:79], v[132:135], a[208:211], v[76:79]\n"
      "ds_read_b128 a[200:203], v2 offset:64\n"
      "ds_read_b64_tr_b16 a[250:251], v7 offset:20488\n"
      "v_fmac_f32 v34, s45, v46\n"
      "v_mfma_f32_16x16x32_bf16 v[80:83], v[124:127], a[208:211], v[80:83]\n"
      "v_fmac_f32 v35, s45, v47\n"
      "v_fma_f32 v44, s47, v44, -v68\n"
      "v_mfma_f32_16x16x32_bf16 v[84:87], v[116:119], a[208:211], v[84:87]\n"
      "v_fma_f32 v45, s47, v45, -v69\n"
      "v_fma_f32 v46, s47, v46, -v70\n"
      "v_mfma_f32_16x16x32_bf16 v[88:91], v[128:131], a[212:215], v[88:91]\n"
      "s_nop 0\n"
      "ds_read_b128 a[204:207], v2 offset:4160\n"
      "v_fma_f32 v47, s47, v47, -v71\n"
      "v_mfma_f32_16x16x32_bf16 v[92:95], v[120:123], a[212:215], v[92:95]\n"
      "s_nop 0\n"
      "v_cvt_pk_bf16_f32 v32, v32, v33\n"
      "v_cvt_pk_bf16_f32 v34, v34, v35\n"
      "v_mfma_f32_16x16x32_bf16 v[96:99], v[112:115], a[212:215], v[96:99]\n"
      "s_waitcnt lgkmcnt(1)\n"
      "ds_write_addtid_b32 v32 offset:41988\n"
      "ds_write_addtid_b32 v34 offset:42500\n"
      "v_cvt_pk_bf16_f32 v44, v44, v45\n"
      "v_mfma_f32_16x16x32_bf16 v[76:79], v[108:111], a[192:195], v[76:79]\n"
      "ds_read_b128 a[208:211], v2 offset:128\n"
      "ds_write_addtid_b32 v44 offset:44036\n"
      "v_cvt_pk_bf16_f32 v46, v46, v47\n"
      "v_mfma_f32_4x4x4_16b_bf16 v[20:23], a[216:217], a[248:249], 0\n"
      "v_fmac_f32 v48, s46, v36\n"
      "v_mfma_f32_4x4x4_16b_bf16 v[24:27], a[216:217], a[250:251], 0\n"
      "ds_write_addtid_b32 v46 offset:44548\n"
      "v_fmac_f32 v49, s46, v37\n"
      "v_fmac_f32 v50, s46, v38\n"
      "v_mfma_f32_16x16x32_bf16 v[80:83], v[100:103], a[192:195], v[80:83]\n"
      "v_fmac_f32 v51, s46, v39\n"
      "v_cvt_pk_bf16_f32 v20, v20, v24\n"
      "v_fma_f32 v36, 0.5, v36, v60\n"
      "v_mfma_f32_16x16x32_bf16 v[84:87], a[160:163], a[192:195], v[84:87]\n"
      "s_add_u32 s54, s54, 196608\n"
      "ds_write_addtid_b32 v20 offset:19968\n"
      "v_fma_f32 v37, 0.5, v37, v61\n"
      "v_mfma_f32_16x16x32_bf16 v[88:91], v[104:107], a[196:199], v[88:91]\n"
      "s_add_u32 m0, s37, 86536\n"
      "ds_read_b128 a[212:215], v2 offset:4224\n"
      "ds_read2st64_b32 a[236:237], v11 offset0:0 offset1:2\n"
      "ds_read2st64_b32 a[238:239], v14 offset0:0 offset1:2\n"
      "v_mfma_f32_16x16x32_bf16 v[92:95], a[164:167], a[196:199], v[92:95]\n"
      "buffer_load_dwordx4 v8, s[4:7], s54 offen offset:0 nt lds\n"
      "buffer_load_dwordx4 v9, s[4:7], s54 offen offset:1024 nt lds\n"
      "v_mfma_f32_16x16x32_bf16 v[96:99], a[156:159], a[196:199], v[96:99]\n"
      "s_mov_b32 m0, s64\n"
      "v_fma_f32 v38, 0.5, v38, v62\n"
      "v_fma_f32 v39, 0.5, v39, v63\n"
      "v_cvt_pk_bf16_f32 v21, v21, v25\n"
      "v_mfma_f32_16x16x32_bf16 v[76:79], a[152:155], a[200:203], v[76:79]\n"
      "s_nop 0\n"
      "ds_read_b128 a[192:195], v3 offset:0\n"
      "ds_write_addtid_b32 v21 offset:24064\n"
      "v_mfma_f32_16x16x32_bf16 v[80:83], a[144:147], a[200:203], v[80:83]\n"
      "v_fmac_f32 v48, s48, v72\n"
      "v_fmac_f32 v49, s48, v73\n"
      "v_cvt_pk_bf16_f32 v22, v22, v26\n"
      "v_mfma_f32_16x16x32_bf16 v[84:87], a[136:139], a[200:203], v[84:87]\n"
      "s_waitcnt lgkmcnt(2)\n"
      "ds_write_addtid_b32 v22 offset:28160\n"
      "v_fmac_f32 v50, s48, v74\n"
      "v_mfma_f32_16x16x32_bf16 v[88:91], a[148:151], a[204:207], v[88:91]\n"
      "ds_read_b128 a[196:199], v3 offset:64\n"
      "v_fmac_f32 v51, s48, v75\n"
      "v_mfma_f32_16x16x32_bf16 v[92:95], a[140:143], a[204:207], v[92:95]\n"
      "v_fmac_f32 v36, s45, v48\n"
      "v_cvt_pk_bf16_f32 v23, v23, v27\n"
      "v_mfma_f32_16x16x32_bf16 v[96:99], a[132:135], a[204:207], v[96:99]\n"
      "ds_write_addtid_b32 v23 offset:32256\n"
      "ds_read2st64_b32 a[240:241], v12 offset0:0 offset1:2\n"
      "ds_read2st64_b32 a[242:243], v15 offset0:0 offset1:2\n"
      "v_mfma_f32_16x16x32_bf16 v[76:79], a[128:131], a[208:211], v[76:79]\n"
      "ds_read_b128 a[200:203], v3 offset:128\n"
      "v_fmac_f32 v37, s45, v49\n"
      "v_fmac_f32 v38, s45, v50\n"
      "v_mfma_f32_16x16x32_bf16 v[80:83], a[120:123], a[208:211], v[80:83]\n"
      "v_fmac_f32 v39, s45, v51\n"
      "v_fma_f32 v48, s47, v48, -v72\n"
      "v_mfma_f32_16x16x32_bf16 v[84:87], a[112:115], a[208:211], v[84:87]\n"
      "s_nop 0\n"
      "v_fma_f32 v49, s47, v49, -v73\n"
      "v_fma_f32 v50, s47, v50, -v74\n"
      "v_mfma_f32_16x16x32_bf16 v[88:91], a[124:127], a[212:215], v[88:91]\n"
      "s_add_u32 s54, s54, 196608\n"
      "ds_read_b128 a[204:207], v4 offset:0\n"
      "v_fma_f32 v51, s47, v51, -v75\n"
      "v_mfma_f32_16x16x32_bf16 v[92:95], a[116:119], a[212:215], v[92:95]\n"
      "s_add_u32 m0, s37, 90760\n"
      "v_cvt_pk_bf16_f32 v36, v36, v37\n"
      "v_cvt_pk_bf16_f32 v38, v38, v39\n"
      "v_mfma_f32_16x16x32_bf16 v[96:99], a[108:111], a[212:215], v[96:99]\n"
      "s_waitcnt lgkmcnt(1)\n"
      "buffer_store_dwordx4 a[236:239], v16, s[8:11], s59 offen offset:128 sc1\n"
      "buffer_load_dwordx4 v8, s[4:7], s54 offen offset:0 nt lds\n"
      "buffer_load_dwordx4 v9, s[4:7], s54 offen offset:1024 nt lds\n"
      "v_mfma_f32_16x16x32_bf16 v[52:55], a[0:3], a[192:195], 0\n"
      "s_mov_b32 m0, s64\n"
      "ds_read_b128 a[208:211], v4 offset:64\n"
      "ds_write_addtid_b32 v36 offset:37888\n"
      "ds_write_addtid_b32 v38 offset:38400\n"
      "v_add_f32 v76, v76, v88\n"
      "v_mfma_f32_16x16x32_bf16 v[56:59], a[4:7], a[192:195], 0\n"
      "ds_write_addtid_b32 v76 offset:45312\n"
      "v_add_f32 v77, v77, v89\n"
      "v_mfma_f32_16x16x32_bf16 v[60:63], a[8:11], a[192:195], 0\n"
      "ds_write_addtid_b32 v77 offset:45824\n"
      "v_add_f32 v78, v78, v90\n"
      "v_add_f32 v79, v79, v91\n"
      "v_mfma_f32_16x16x32_bf16 v[52:55], a[12:15], a[196:199], v[52:55]\n"
      "ds_read_b128 a[212:215], v4 offset:128\n"
      "ds_write_addtid_b32 v78 offset:46336\n"
      "v_mfma_f32_16x16x32_bf16 v[56:59], a[16:19], a[196:199], v[56:59]\n"
      "ds_write_addtid_b32 v79 offset:46848\n"
      "v_add_f32 v80, v80, v92\n"
      "v_cvt_pk_bf16_f32 v48, v48, v49\n"
      "v_mfma_f32_16x16x32_bf16 v[60:63], a[20:23], a[196:199], v[60:63]\n"
      "s_nop 0\n"
      "ds_write_addtid_b32 v48 offset:39936\n"
      "ds_write_addtid_b32 v80 offset:47360\n"
      "v_mfma_f32_16x16x32_bf16 v[52:55], a[24:27], a[200:203], v[52:55]\n"
      "ds_read_b128 a[192:195], v5 offset:0\n"
      "v_add_f32 v81, v81, v93\n"
      "v_mfma_f32_16x16x32_bf16 v[56:59], a[28:31], a[200:203], v[56:59]\n"
      "buffer_store_dwordx4 a[240:243], v16, s[8:11], s59 offen offset:256 sc1\n"
      "ds_write_addtid_b32 v81 offset:47872\n"
      "v_mfma_f32_16x16x32_bf16 v[60:63], a[32:35], a[200:203], v[60:63]\n"
      "s_waitcnt lgkmcnt(4)\n"
      "v_add_f32 v82, v82, v94\n"
      "v_add_f32 v83, v83, v95\n"
      "v_mfma_f32_16x16x32_bf16 v[52:55], a[36:39], a[204:207], v[52:55]\n"
      "ds_read_b128 a[196:199], v5 offset:64\n"
      "ds_write_addtid_b32 v82 offset:48384\n"
      "ds_write_addtid_b32 v83 offset:48896\n"
      "v_cvt_pk_bf16_f32 v50, v50, v51\n"
      "v_mfma_f32_16x16x32_bf16 v[56:59], a[40:43], a[204:207], v[56:59]\n"
      "ds_write_addtid_b32 v50 offset:40448\n"
      "v_add_f32 v84, v84, v96\n"
      "v_mfma_f32_16x16x32_bf16 v[60:63], a[44:47], a[204:207], v[60:63]\n"
      "ds_write_addtid_b32 v84 offset:49408\n"
      "v_add_f32 v85, v85, v97\n"
      "v_add_f32 v86, v86, v98\n"
      "v_add_f32 v87, v87, v99\n"
      "v_mfma_f32_16x16x32_bf16 v[52:55], a[48:51], a[208:211], v[52:55]\n"
      "ds_read_b128 a[200:203], v5 offset:128\n"
      "ds_write_addtid_b32 v85 offset:49920\n"
      "v_mfma_f32_16x16x32_bf16 v[56:59], a[52:55], a[208:211], v[56:59]\n"
      "ds_write_addtid_b32 v86 offset:50432\n"
      "ds_write_addtid_b32 v87 offset:50944\n"
      "v_mfma_f32_16x16x32_bf16 v[60:63], a[56:59], a[208:211], v[60:63]\n"
      "v_fma_f32 v40, -2.0, v88, v76\n"
      "v_fma_f32 v41, -2.0, v89, v77\n"
      "v_mfma_f32_16x16x32_bf16 v[52:55], a[60:63], a[212:215], v[52:55]\n"
      "v_fma_f32 v42, -2.0, v90, v78\n"
      "v_fma_f32 v43, -2.0, v91, v79\n"
      "v_mfma_f32_16x16x32_bf16 v[56:59], a[64:67], a[212:215], v[56:59]\n"
      "s_waitcnt vmcnt(25)\n"
      "v_fma_f32 v44, -2.0, v92, v80\n"
      "v_fma_f32 v45, -2.0, v93, v81\n"
      "v_mfma_f32_16x16x32_bf16 v[60:63], a[68:71], a[212:215], v[60:63]\n"
      "s_waitcnt lgkmcnt(0)\n"
      "s_barrier\n"
      "s_add_u32 s55, s55, 2\n"
      "s_cmp_lt_u32 s55, 6\n"
      "s_cbranch_scc1 LDloop_%=\n"
      "LDlast_%=:\n"
      "v_mfma_f32_16x16x32_bf16 v[52:55], a[72:75], a[192:195], v[52:55]\n"
      "s_add_u32 s58, s40, 0\n"
      "ds_read_b128 a[204:207], v0 offset:16384\n"
      "ds_read2st64_b32 a[220:221], v10 offset0:181 offset1:183\n"
      "v_fma_f32 v46, -2.0, v94, v82\n"
      "v_mfma_f32_16x16x32_bf16 v[56:59], a[76:79], a[192:195], v[56:59]\n"
      "s_mov_b32 s40, s62\n"
      "ds_read_b128 a[208:211], v0 offset:20480\n"
      "ds_read2st64_b32 v[28:29], v6 offset0:0 offset1:2\n"
      "v_mfma_f32_16x16x32_bf16 v[60:63], a[80:83], a[192:195], v[60:63]\n"
      "s_add_u32 s62, s62, 786432\n"
      "ds_read2st64_b32 a[222:223], v13 offset0:181 offset1:183\n"
      "ds_read2st64_b32 v[30:31], v6 offset0:4 offset1:6\n"
      "v_mfma_f32_16x16x32_bf16 v[52:55], a[84:87], a[196:199], v[52:55]\n"
      "s_add_u32 s59, s58, 196608\n"
      "ds_read_b128 a[212:215], v0 offset:16448\n"
      "v_fma_f32 v47, -2.0, v95, v83\n"
      "v_mfma_f32_16x16x32_bf16 v[56:59], a[88:91], a[196:199], v[56:59]\n"
      "ds_read2st64_b32 v[32:33], v6 offset0:8 offset1:10\n"
      "v_fma_f32 v48, -2.0, v96, v84\n"
      "v_fma_f32 v49, -2.0, v97, v85\n"
      "v_mfma_f32_16x16x32_bf16 v[60:63], a[92:95], a[196:199], v[60:63]\n"
      "ds_read2st64_b32 v[34:35], v6 offset0:12 offset1:14\n"
      "v_fma_f32 v50, -2.0, v98, v86\n"
      "v_mfma_f32_16x16x32_bf16 v[52:55], a[96:99], a[200:203], v[52:55]\n"
      "ds_read_b128 a[192:195], v0 offset:20544\n"
      "v_fma_f32 v51, -2.0, v99, v87\n"
      "v_mfma_f32_16x16x32_bf16 v[56:59], a[100:103], a[200:203], v[56:59]\n"
      "ds_read2st64_b32 v[36:37], v6 offset0:16 offset1:18\n"
      "v_mfma_f32_16x16x32_bf16 v[60:63], a[104:107], a[200:203], v[60:63]\n"
      "s_waitcnt lgkmcnt(7)\n"
      "ds_read_b64_tr_b16 a[244:245], v7 offset:36864\n"
      "ds_read_b64_tr_b16 a[246:247], v7 offset:36872\n"
      "v_mfma_f32_16x16x32_bf16 v[76:79], v[252:255], a[204:207], 0\n"
      "s_nop 0\n"
      "ds_read_b128 a[196:199], v0 offset:16512\n"
      "ds_read2st64_b32 v[38:39], v6 offset0:20 offset1:22\n"
      "v_mfma_f32_16x16x32_bf16 v[80:83], v[244:247], a[204:207], 0\n"
      "v_mfma_f32_16x16x32_bf16 v[84:87], v[236:239], a[204:207], 0\n"
      "s_nop 0\n"
      "ds_read2st64_b32 a[224:225], v11 offset0:181 offset1:183\n"
      "v_mfma_f32_16x16x32_bf16 v[88:91], v[248:251], a[208:211], 0\n"
      "s_waitcnt lgkmcnt(7)\n"
      "ds_read_b128 a[200:203], v0 offset:20608\n"
      "ds_read2st64_b32 a[226:227], v14 offset0:181 offset1:183\n"
      "v_fmac_f32 v40, s46, v28\n"
      "v_fmac_f32 v41, s46, v29\n"
      "v_fmac_f32 v42, s46, v30\n"
      "v_fmac_f32 v43, s46, v31\n"
      "v_mfma_f32_16x16x32_bf16 v[92:95], v[240:243], a[208:211], 0\n"
      "buffer_store_dwordx4 a[220:223], v16, s[8:11], s58 offen offset:0 sc1\n"
      "v_fma_f32 v28, 0.5, v28, v64\n"
      "v_mfma_f32_16x16x32_bf16 v[96:99], v[232:235], a[208:211], 0\n"
      "v_fma_f32 v29, 0.5, v29, v65\n"
      "v_fma_f32 v30, 0.5, v30, v66\n"
      "v_fma_f32 v31, 0.5, v31, v67\n"
      "v_fmac_f32 v40, s48, v52\n"
      "v_mfma_f32_16x16x32_bf16 v[76:79], v[228:231], a[212:215], v[76:79]\n"
      "s_waitcnt lgkmcnt(3)\n"
      "ds_read_b128 a[204:207], v1 offset:16384\n"
      "v_fmac_f32 v41, s48, v53\n"
      "v_mfma_f32_4x4x4_16b_bf16 v[20:23], a[216:217], a[244:245], 0\n"
      "v_fmac_f32 v42, s48, v54\n"
      "v_mfma_f32_4x4x4_16b_bf16 v[24:27], a[216:217], a[246:247], 0\n"
      "v_fmac_f32 v43, s48, v55\n"
      "v_fmac_f32 v28, s45, v40\n"
      "v_mfma_f32_16x16x32_bf16 v[80:83], v[220:223], a[212:215], v[80:83]\n"
      "v_fmac_f32 v29, s45, v41\n"
      "v_fmac_f32 v30, s45, v42\n"
      "v_cvt_pk_bf16_f32 v20, v20, v24\n"
      "v_mfma_f32_16x16x32_bf16 v[84:87], v[212:215], a[212:215], v[84:87]\n"
      "ds_write_addtid_b32 v20 offset:3072\n"
      "v_fmac_f32 v31, s45, v43\n"
      "v_mfma_f32_16x16x32_bf16 v[88:91], v[224:227], a[192:195], v[88:91]\n"
      "s_nop 0\n"
      "ds_read_b128 a[208:211], v1 offset:20480\n"
      "v_cvt_pk_bf16_f32 v21, v21, v25\n"
      "v_mfma_f32_16x16x32_bf16 v[92:95], v[216:219], a[192:195], v[92:95]\n"
      "ds_write_addtid_b32 v21 offset:7168\n"
      "v_fma_f32 v40, s47, v40, -v52\n"
      "v_mfma_f32_16x16x32_bf16 v[96:99], v[208:211], a[192:195], v[96:99]\n"
      "v_fma_f32 v41, s47, v41, -v53\n"
      "v_fma_f32 v42, s47, v42, -v54\n"
      "v_mfma_f32_16x16x32_bf16 v[76:79], v[204:207], a[196:199], v[76:79]\n"
      "ds_read_b128 a[212:215], v1 offset:16448\n"
      "v_cvt_pk_bf16_f32 v22, v22, v26\n"
      "v_mfma_f32_16x16x32_bf16 v[80:83], v[196:199], a[196:199], v[80:83]\n"
      "s_nop 0\n"
      "ds_write_addtid_b32 v22 offset:11264\n"
      "v_fma_f32 v43, s47, v43, -v55\n"
      "v_mfma_f32_16x16x32_bf16 v[84:87], v[188:191], a[196:199], v[84:87]\n"
      "s_waitcnt lgkmcnt(3)\n"
      "v_cvt_pk_bf16_f32 v28, v28, v29\n"
      "v_cvt_pk_bf16_f32 v30, v30, v31\n"
      "v_mfma_f32_16x16x32_bf16 v[88:91], v[200:203], a[200:203], v[88:91]\n"
      "buffer_store_dwordx4 a[224:227], v16, s[8:11], s58 offen offset:128 sc1\n"
      "ds_read_b128 a[192:195], v1 offset:20544\n"
      "v_mfma_f32_16x16x32_bf16 v[92:95], v[192:195], a[200:203], v[92:95]\n"
      "ds_write_addtid_b32 v28 offset:32768\n"
      "ds_write_addtid_b32 v30 offset:33280\n"
      "v_cvt_pk_bf16_f32 v40, v40, v41\n"
      "v_cvt_pk_bf16_f32 v23, v23, v27\n"
      "v_mfma_f32_16x16x32_bf16 v[96:99], v[184:187], a[200:203], v[96:99]\n"
      "ds_write_addtid_b32 v40 offset:34816\n"
      "ds_read2st64_b32 a[228:229], v12 offset0:181 offset1:183\n"
      "v_cvt_pk_bf16_f32 v42, v42, v43\n"
      "v_mfma_f32_16x16x32_bf16 v[76:79], v[180:183], a[204:207], v[76:79]\n"
      "ds_read_b128 a[196:199], v1 offset:16512\n"
      "ds_write_addtid_b32 v42 offset:35328\n"
      "v_mfma_f32_16x16x32_bf16 v[80:83], v[172:175], a[204:207], v[80:83]\n"
      "ds_write_addtid_b32 v23 offset:15360\n"
      "ds_read2st64_b32 a[230:231], v15 offset0:181 offset1:183\n"
      "v_fmac_f32 v44, s46, v32\n"
      "v_mfma_f32_16x16x32_bf16 v[84:87], v[164:167], a[204:207], v[84:87]\n"
      "v_fmac_f32 v45, s46, v33\n"
      "v_fmac_f32 v46, s46, v34\n"
      "v_mfma_f32_16x16x32_bf16 v[88:91], v[176:179], a[208:211], v[88:91]\n"
      "s_nop 0\n"
      "ds_read_b128 a[200:203], v1 offset:20608\n"
      "v_fmac_f32 v47, s46, v35\n"
      "v_mfma_f32_16x16x32_bf16 v[92:95], v[168:171], a[208:211], v[92:95]\n"
      "v_fma_f32 v32, 0.5, v32, v68\n"
      "v_fma_f32 v33, 0.5, v33, v69\n"
      "v_mfma_f32_16x16x32_bf16 v[96:99], v[160:163], a[208:211], v[96:99]\n"
      "s_waitcnt lgkmcnt(5)\n"
      "v_fma_f32 v34, 0.5, v34, v70\n"
      "v_fma_f32 v35, 0.5, v35, v71\n"
      "v_mfma_f32_16x16x32_bf16 v[76:79], v[156:159], a[212:215], v[76:79]\n"
      "ds_read_b128 a[204:207], v2 offset:16384\n"
      "v_fmac_f32 v44, s48, v56\n"
      "v_mfma_f32_16x16x32_bf16 v[80:83], v[148:151], a[212:215], v[80:83]\n"
      "v_fmac_f32 v45, s48, v57\n"
      "v_fmac_f32 v46, s48, v58\n"
      "v_mfma_f32_16x16x32_bf16 v[84:87], v[140:143], a[212:215], v[84:87]\n"
      "v_fmac_f32 v47, s48, v59\n"
      "v_fmac_f32 v32, s45, v44\n"
      "v_mfma_f32_16x16x32_bf16 v[88:91], v[152:155], a[192:195], v[88:91]\n"
      "s_nop 0\n"
      "ds_read_b128 a[208:211], v2 offset:20480\n"
      "ds_read_b64_tr_b16 a[248:249], v7 offset:37376\n"
      "v_mfma_f32_16x16x32_bf16 v[92:95], v[144:147], a[192:195], v[92:95]\n"
      "s_waitcnt lgkmcnt(3)\n"
      "ds_read_b64_tr_b16 a[250:251], v7 offset:37384\n"
      "v_fmac_f32 v33, s45, v45\n"
      "v_fmac_f32 v34, s45, v46\n"
      "v_mfma_f32_16x16x32_bf16 v[96:99], v[136:139], a[192:195], v[96:99]\n"
      "s_nop 0\n"
      "buffer_store_dwordx4 a[228:231], v16, s[8:11], s58 offen offset:256 sc1\n"
      "v_mfma_f32_16x16x32_bf16 v[76:79], v[132:135], a[196:199], v[76:79]\n"
      "ds_read_b128 a[212:215], v2 offset:16448\n"
      "v_fmac_f32 v35, s45, v47\n"
      "v_mfma_f32_16x16x32_bf16 v[80:83], v[124:127], a[196:199], v[80:83]\n"
      "v_fma_f32 v44, s47, v44, -v56\n"
      "v_fma_f32 v45, s47, v45, -v57\n"
      "v_mfma_f32_16x16x32_bf16 v[84:87], v[116:119], a[196:199], v[84:87]\n"
      "s_nop 0\n"
      "v_fma_f32 v46, s47, v46, -v58\n"
      "v_fma_f32 v47, s47, v47, -v59\n"
      "v_mfma_f32_16x16x32_bf16 v[88:91], v[128:131], a[200:203], v[88:91]\n"
      "s_nop 0\n"
      "ds_read_b128 a[192:195], v2 offset:20544\n"
      "v_cvt_pk_bf16_f32 v32, v32, v33\n"
      "v_mfma_f32_16x16x32_bf16 v[92:95], v[120:123], a[200:203], v[92:95]\n"
      "s_waitcnt lgkmcnt(2)\n"
      "ds_write_addtid_b32 v32 offset:40964\n"
      "v_cvt_pk_bf16_f32 v34, v34, v35\n"
      "v_mfma_f32_4x4x4_16b_bf16 v[20:23], a[216:217], a[248:249], 0\n"
      "v_cvt_pk_bf16_f32 v44, v44, v45\n"
      "v_mfma_f32_4x4x4_16b_bf16 v[24:27], a[216:217], a[250:251], 0\n"
      "ds_write_addtid_b32 v34 offset:41476\n"
      "ds_write_addtid_b32 v44 offset:43012\n"
      "v_cvt_pk_bf16_f32 v46, v46, v47\n"
      "v_fmac_f32 v48, s46, v36\n"
      "v_mfma_f32_16x16x32_bf16 v[96:99], v[112:115], a[200:203], v[96:99]\n"
      "s_nop 0\n"
      "ds_write_addtid_b32 v46 offset:43524\n"
      "v_fmac_f32 v49, s46, v37\n"
      "v_cvt_pk_bf16_f32 v20, v20, v24\n"
      "v_fmac_f32 v50, s46, v38\n"
      "v_mfma_f32_16x16x32_bf16 v[76:79], v[108:111], a[204:207], v[76:79]\n"
      "ds_read_b128 a[196:199], v2 offset:16512\n"
      "ds_write_addtid_b32 v20 offset:3584\n"
      "v_mfma_f32_16x16x32_bf16 v[80:83], v[100:103], a[204:207], v[80:83]\n"
      "v_fmac_f32_e64 v51, s46, v39\n"
      "v_fma_f32 v36, 0.5, v36, v72\n"
      "v_mfma_f32_16x16x32_bf16 v[84:87], a[160:163], a[204:207], v[84:87]\n"
      "v_fma_f32 v37, 0.5, v37, v73\n"
      "v_cvt_pk_bf16_f32 v21, v21, v25\n"
      "v_mfma_f32_16x16x32_bf16 v[88:91], v[104:107], a[208:211], v[88:91]\n"
      "ds_read_b128 a[200:203], v2 offset:20608\n"
      "ds_write_addtid_b32 v21 offset:7680\n"
      "v_fma_f32 v38, 0.5, v38, v74\n"
      "v_mfma_f32_16x16x32_bf16 v[92:95], a[164:167], a[208:211], v[92:95]\n"
      "ds_read2st64_b32 a[232:233], v10 offset0:4 offset1:6\n"
      "ds_read2st64_b32 a[234:235], v13 offset0:4 offset1:6\n"
      "v_mfma_f32_16x16x32_bf16 v[96:99], a[156:159], a[208:211], v[96:99]\n"
      "s_waitcnt lgkmcnt(4)\n"
      "v_fma_f32 v39, 0.5, v39, v75\n"
      "v_fmac_f32 v48, s48, v60\n"
      "v_cvt_pk_bf16_f32 v22, v22, v26\n"
      "v_mfma_f32_16x16x32_bf16 v[76:79], a[152:155], a[212:215], v[76:79]\n"
      "ds_read_b128 a[204:207], v3 offset:16896\n"
      "ds_write_addtid_b32 v22 offset:11776\n"
      "v_mfma_f32_16x16x32_bf16 v[80:83], a[144:147], a[212:215], v[80:83]\n"
      "v_fmac_f32 v49, s48, v61\n"
      "v_fmac_f32 v50, s48, v62\n"
      "v_mfma_f32_16x16x32_bf16 v[84:87], a[136:139], a[212:215], v[84:87]\n"
      "v_fmac_f32 v51, s48, v63\n"
      "v_cvt_pk_bf16_f32 v23, v23, v27\n"
      "v_mfma_f32_16x16x32_bf16 v[88:91], a[148:151], a[192:195], v[88:91]\n"
      "s_nop 0\n"
      "ds_read_b128 a[208:211], v3 offset:16960\n"
      "ds_write_addtid_b32 v23 offset:15872\n"
      "v_fmac_f32 v36, s45, v48\n"
      "v_mfma_f32_16x16x32_bf16 v[92:95], a[140:143], a[192:195], v[92:95]\n"
      "v_fmac_f32 v37, s45, v49\n"
      "v_fmac_f32 v38, s45, v50\n"
      "v_mfma_f32_16x16x32_bf16 v[96:99], a[132:135], a[192:195], v[96:99]\n"
      "s_nop 0\n"
      "ds_read2st64_b32 a[236:237], v11 offset0:4 offset1:6\n"
      "ds_read2st64_b32 a[238:239], v14 offset0:4 offset1:6\n"
      "v_mfma_f32_16x16x32_bf16 v[76:79], a[128:131], a[196:199], v[76:79]\n"
      "ds_read_b128 a[212:215], v3 offset:17024\n"
      "v_fmac_f32 v39, s45, v51\n"
      "v_mfma_f32_16x16x32_bf16 v[80:83], a[120:123], a[196:199], v[80:83]\n"
      "v_fma_f32 v48, s47, v48, -v60\n"
      "v_fma_f32 v49, s47, v49, -v61\n"
      "v_mfma_f32_16x16x32_bf16 v[84:87], a[112:115], a[196:199], v[84:87]\n"
      "s_waitcnt lgkmcnt(3)\n"
      "v_fma_f32 v50, s47, v50, -v62\n"
      "v_fma_f32 v51, s47, v51, -v63\n"
      "v_mfma_f32_16x16x32_bf16 v[88:91], a[124:127], a[200:203], v[88:91]\n"
      "ds_read_b128 a[192:195], v4 offset:16896\n"
      "v_cvt_pk_bf16_f32 v36, v36, v37\n"
      "v_mfma_f32_16x16x32_bf16 v[92:95], a[116:119], a[200:203], v[92:95]\n"
      "ds_write_addtid_b32 v36 offset:36864\n"
      "v_cvt_pk_bf16_f32 v38, v38, v39\n"
      "v_mfma_f32_16x16x32_bf16 v[96:99], a[108:111], a[200:203], v[96:99]\n"
      "buffer_store_dwordx4 a[232:235], v16, s[8:11], s59 offen offset:0 sc1\n"
      "ds_write_addtid_b32 v38 offset:37376\n"
      "v_mfma_f32_16x16x32_bf16 v[64:67], a[0:3], a[204:207], 0\n"
      "ds_read_b128 a[196:199], v4 offset:16960\n"
      "v_add_f32 v76, v76, v88\n"
      "v_mfma_f32_16x16x32_bf16 v[68:71], a[4:7], a[204:207], 0\n"
      "ds_write_addtid_b32 v76 offset:51456\n"
      "v_add_f32 v77, v77, v89\n"
      "v_add_f32_e64 v78, v78, v90\n"
      "v_mfma_f32_16x16x32_bf16 v[72:75], a[8:11], a[204:207], 0\n"
      "ds_write_addtid_b32 v77 offset:51968\n"
      "ds_write_addtid_b32 v78 offset:52480\n"
      "v_add_f32 v79, v79, v91\n"
      "v_mfma_f32_16x16x32_bf16 v[64:67], a[12:15], a[208:211], v[64:67]\n"
      "ds_read_b128 a[200:203], v4 offset:17024\n"
      "ds_write_addtid_b32 v79 offset:52992\n"
      "v_mfma_f32_16x16x32_bf16 v[68:71], a[16:19], a[208:211], v[68:71]\n"
      "v_add_f32 v80, v80, v92\n"
      "v_cvt_pk_bf16_f32 v48, v48, v49\n"
      "v_add_f32 v81, v81, v93\n"
      "v_mfma_f32_16x16x32_bf16 v[72:75], a[20:23], a[208:211], v[72:75]\n"
      "s_waitcnt lgkmcnt(4)\n"
      "ds_write_addtid_b32 v48 offset:38912\n"
      "ds_write_addtid_b32 v80 offset:53504\n"
      "v_mfma_f32_16x16x32_bf16 v[64:67], a[24:27], a[212:215], v[64:67]\n"
      "ds_read_b128 a[204:207], v5 offset:16896\n"
      "ds_write_addtid_b32 v81 offset:54016\n"
      "v_mfma_f32_16x16x32_bf16 v[68:71], a[28:31], a[212:215], v[68:71]\n"
      "buffer_store_dwordx4 a[236:239], v16, s[8:11], s59 offen offset:128 sc1\n"
      "v_add_f32 v82, v82, v94\n"
      "v_mfma_f32_16x16x32_bf16 v[72:75], a[32:35], a[212:215], v[72:75]\n"
      "ds_write_addtid_b32 v82 offset:54528\n"
      "v_add_f32 v83, v83, v95\n"
      "v_mfma_f32_16x16x32_bf16 v[64:67], a[36:39], a[192:195], v[64:67]\n"
      "ds_read_b128 a[208:211], v5 offset:16960\n"
      "ds_write_addtid_b32 v83 offset:55040\n"
      "v_cvt_pk_bf16_f32 v50, v50, v51\n"
      "v_mfma_f32_16x16x32_bf16 v[68:71], a[40:43], a[192:195], v[68:71]\n"
      "ds_write_addtid_b32 v50 offset:39424\n"
      "v_add_f32 v84, v84, v96\n"
      "v_mfma_f32_16x16x32_bf16 v[72:75], a[44:47], a[192:195], v[72:75]\n"
      "ds_write_addtid_b32 v84 offset:55552\n"
      "v_add_f32 v85, v85, v97\n"
      "v_add_f32 v86, v86, v98\n"
      "v_add_f32 v87, v87, v99\n"
      "v_mfma_f32_16x16x32_bf16 v[64:67], a[48:51], a[196:199], v[64:67]\n"
      "ds_read_b128 a[212:215], v5 offset:17024\n"
      "ds_write_addtid_b32 v85 offset:56064\n"
      "v_mfma_f32_16x16x32_bf16 v[68:71], a[52:55], a[196:199], v[68:71]\n"
      "s_nop 0\n"
      "ds_write_addtid_b32 v86 offset:56576\n"
      "ds_write_addtid_b32 v87 offset:57088\n"
      "v_mfma_f32_16x16x32_bf16 v[72:75], a[56:59], a[196:199], v[72:75]\n"
      "s_waitcnt lgkmcnt(5)\n"
      "v_fma_f32 v40, -2.0, v88, v76\n"
      "v_fma_f32 v41, -2.0, v89, v77\n"
      "v_mfma_f32_16x16x32_bf16 v[64:67], a[60:63], a[200:203], v[64:67]\n"
      "s_nop 0\n"
      "v_fma_f32 v42, -2.0, v90, v78\n"
      "v_fma_f32 v43, -2.0, v91, v79\n"
      "v_mfma_f32_16x16x32_bf16 v[68:71], a[64:67], a[200:203], v[68:71]\n"
      "s_waitcnt vmcnt(16)\n"
      "v_fma_f32 v44, -2.0, v92, v80\n"
      "v_fma_f32 v45, -2.0, v93, v81\n"
      "v_mfma_f32_16x16x32_bf16 v[72:75], a[68:71], a[200:203], v[72:75]\n"
      "s_waitcnt lgkmcnt(0)\n"
      "s_barrier\n"
      "v_mfma_f32_16x16x32_bf16 v[64:67], a[72:75], a[204:207], v[64:67]\n"
      "s_add_u32 s58, s40, 0\n"
      "ds_read_b128 a[192:195], v0 offset:0\n"
      "ds_read2st64_b32 a[220:221], v10 offset0:177 offset1:179\n"
      "v_fma_f32 v46, -2.0, v94, v82\n"
      "v_mfma_f32_16x16x32_bf16 v[68:71], a[76:79], a[204:207], v[68:71]\n"
      "s_mov_b32 s40, s62\n"
      "ds_read_b128 a[196:199], v0 offset:4096\n"
      "ds_read2st64_b32 v[28:29], v6 offset0:24 offset1:26\n"
      "v_mfma_f32_16x16x32_bf16 v[72:75], a[80:83], a[204:207], v[72:75]\n"
      "s_add_u32 s62, s62, 786432\n"
      "ds_read2st64_b32 a[222:223], v13 offset0:177 offset1:179\n"
      "ds_read2st64_b32 v[30:31], v6 offset0:28 offset1:30\n"
      "v_mfma_f32_16x16x32_bf16 v[64:67], a[84:87], a[208:211], v[64:67]\n"
      "s_add_u32 s59, s58, 196608\n"
      "ds_read_b128 a[200:203], v0 offset:64\n"
      "v_fma_f32 v47, -2.0, v95, v83\n"
      "v_mfma_f32_16x16x32_bf16 v[68:71], a[88:91], a[208:211], v[68:71]\n"
      "ds_read2st64_b32 v[32:33], v6 offset0:32 offset1:34\n"
      "v_fma_f32 v48, -2.0, v96, v84\n"
      "v_fma_f32 v49, -2.0, v97, v85\n"
      "v_mfma_f32_16x16x32_bf16 v[72:75], a[92:95], a[208:211], v[72:75]\n"
      "ds_read2st64_b32 v[34:35], v6 offset0:36 offset1:38\n"
      "v_fma_f32 v50, -2.0, v98, v86\n"
      "v_mfma_f32_16x16x32_bf16 v[64:67], a[96:99], a[212:215], v[64:67]\n"
      "ds_read_b128 a[204:207], v0 offset:4160\n"
      "v_fma_f32 v51, -2.0, v99, v87\n"
      "v_mfma_f32_16x16x32_bf16 v[68:71], a[100:103], a[212:215], v[68:71]\n"
      "s_nop 0\n"
      "ds_read2st64_b32 v[36:37], v6 offset0:40 offset1:42\n"
      "v_mfma_f32_16x16x32_bf16 v[72:75], a[104:107], a[212:215], v[72:75]\n"
      "s_waitcnt lgkmcnt(7)\n"
      "ds_read_b64_tr_b16 a[244:245], v7 offset:53760\n"
      "ds_read_b64_tr_b16 a[246:247], v7 offset:53768\n"
      "v_mfma_f32_16x16x32_bf16 v[76:79], v[252:255], a[192:195], 0\n"
      "ds_read_b128 a[208:211], v0 offset:128\n"
      "ds_read2st64_b32 v[38:39], v6 offset0:44 offset1:46\n"
      "v_mfma_f32_16x16x32_bf16 v[80:83], v[244:247], a[192:195], 0\n"
      "v_mfma_f32_16x16x32_bf16 v[84:87], v[236:239], a[192:195], 0\n"
      "ds_read2st64_b32 a[224:225], v11 offset0:177 offset1:179\n"
      "v_mfma_f32_16x16x32_bf16 v[88:91], v[248:251], a[196:199], 0\n"
      "s_waitcnt lgkmcnt(7)\n"
      "ds_read_b128 a[212:215], v0 offset:4224\n"
      "ds_read2st64_b32 a[226:227], v14 offset0:177 offset1:179\n"
      "v_fmac_f32 v40, s46, v28\n"
      "v_fmac_f32 v41, s46, v29\n"
      "v_fmac_f32 v42, s46, v30\n"
      "v_fmac_f32 v43, s46, v31\n"
      "v_mfma_f32_16x16x32_bf16 v[92:95], v[240:243], a[196:199], 0\n"
      "s_nop 0\n"
      "buffer_store_dwordx4 a[220:223], v16, s[8:11], s58 offen offset:0 sc1\n"
      "v_fma_f32 v28, 0.5, v28, v52\n"
      "v_mfma_f32_16x16x32_bf16 v[96:99], v[232:235], a[196:199], 0\n"
      "v_fma_f32 v29, 0.5, v29, v53\n"
      "v_fma_f32 v30, 0.5, v30, v54\n"
      "v_fma_f32 v31, 0.5, v31, v55\n"
      "v_fmac_f32 v40, s48, v64\n"
      "v_mfma_f32_16x16x32_bf16 v[76:79], v[228:231], a[200:203], v[76:79]\n"
      "s_waitcnt lgkmcnt(3)\n"
      "ds_read_b128 a[192:195], v1 offset:0\n"
      "v_fmac_f32 v41, s48, v65\n"
      "v_mfma_f32_4x4x4_16b_bf16 v[20:23], a[216:217], a[244:245], 0\n"
      "v_fmac_f32 v42, s48, v66\n"
      "v_mfma_f32_4x4x4_16b_bf16 v[24:27], a[216:217], a[246:247], 0\n"
      "v_fmac_f32 v43, s48, v67\n"
      "v_fmac_f32 v28, s45, v40\n"
      "v_mfma_f32_16x16x32_bf16 v[80:83], v[220:223], a[200:203], v[80:83]\n"
      "v_fmac_f32 v29, s45, v41\n"
      "v_fmac_f32 v30, s45, v42\n"
      "v_cvt_pk_bf16_f32 v20, v20, v24\n"
      "v_mfma_f32_16x16x32_bf16 v[84:87], v[212:215], a[200:203], v[84:87]\n"
      "ds_write_addtid_b32 v20 offset:19456\n"
      "v_fmac_f32 v31, s45, v43\n"
      "v_mfma_f32_16x16x32_bf16 v[88:91], v[224:227], a[204:207], v[88:91]\n"
      "ds_read_b128 a[196:199], v1 offset:4096\n"
      "v_cvt_pk_bf16_f32 v21, v21, v25\n"
      "v_mfma_f32_16x16x32_bf16 v[92:95], v[216:219], a[204:207], v[92:95]\n"
      "s_nop 0\n"
      "ds_write_addtid_b32 v21 offset:23552\n"
      "v_fma_f32 v40, s47, v40, -v64\n"
      "v_mfma_f32_16x16x32_bf16 v[96:99], v[208:211], a[204:207], v[96:99]\n"
      "v_fma_f32 v41, s47, v41, -v65\n"
      "v_fma_f32 v42, s47, v42, -v66\n"
      "v_mfma_f32_16x16x32_bf16 v[76:79], v[204:207], a[208:211], v[76:79]\n"
      "ds_read_b128 a[200:203], v1 offset:64\n"
      "v_cvt_pk_bf16_f32 v22, v22, v26\n"
      "v_mfma_f32_16x16x32_bf16 v[80:83], v[196:199], a[208:211], v[80:83]\n"
      "ds_write_addtid_b32 v22 offset:27648\n"
      "v_fma_f32 v43, s47, v43, -v67\n"
      "v_mfma_f32_16x16x32_bf16 v[84:87], v[188:191], a[208:211], v[84:87]\n"
      "s_waitcnt lgkmcnt(3)\n"
      "v_cvt_pk_bf16_f32 v28, v28, v29\n"
      "v_cvt_pk_bf16_f32 v30, v30, v31\n"
      "v_mfma_f32_16x16x32_bf16 v[88:91], v[200:203], a[212:215], v[88:91]\n"
      "s_nop 0\n"
      "buffer_store_dwordx4 a[224:227], v16, s[8:11], s58 offen offset:128 sc1\n"
      "ds_read_b128 a[204:207], v1 offset:4160\n"
      "v_mfma_f32_16x16x32_bf16 v[92:95], v[192:195], a[212:215], v[92:95]\n"
      "ds_write_addtid_b32 v28 offset:33792\n"
      "ds_write_addtid_b32 v30 offset:34304\n"
      "v_cvt_pk_bf16_f32 v40, v40, v41\n"
      "v_cvt_pk_bf16_f32 v23, v23, v27\n"
      "v_mfma_f32_16x16x32_bf16 v[96:99], v[184:187], a[212:215], v[96:99]\n"
      "ds_write_addtid_b32 v40 offset:35840\n"
      "ds_read2st64_b32 a[228:229], v12 offset0:177 offset1:179\n"
      "v_cvt_pk_bf16_f32 v42, v42, v43\n"
      "v_mfma_f32_16x16x32_bf16 v[76:79], v[180:183], a[192:195], v[76:79]\n"
      "ds_read_b128 a[208:211], v1 offset:128\n"
      "ds_write_addtid_b32 v42 offset:36352\n"
      "v_mfma_f32_16x16x32_bf16 v[80:83], v[172:175], a[192:195], v[80:83]\n"
      "ds_write_addtid_b32 v23 offset:31744\n"
      "ds_read2st64_b32 a[230:231], v15 offset0:177 offset1:179\n"
      "v_fmac_f32_e64 v44, s46, v32\n"
      "v_mfma_f32_16x16x32_bf16 v[84:87], v[164:167], a[192:195], v[84:87]\n"
      "v_fmac_f32 v45, s46, v33\n"
      "v_fmac_f32 v46, s46, v34\n"
      "v_mfma_f32_16x16x32_bf16 v[88:91], v[176:179], a[196:199], v[88:91]\n"
      "ds_read_b128 a[212:215], v1 offset:4224\n"
      "v_fmac_f32 v47, s46, v35\n"
      "v_mfma_f32_16x16x32_bf16 v[92:95], v[168:171], a[196:199], v[92:95]\n"
      "s_nop 0\n"
      "v_fma_f32 v32, 0.5, v32, v56\n"
      "v_fma_f32 v33, 0.5, v33, v57\n"
      "v_mfma_f32_16x16x32_bf16 v[96:99], v[160:163], a[196:199], v[96:99]\n"
      "s_waitcnt lgkmcnt(5)\n"
      "v_fma_f32 v34, 0.5, v34, v58\n"
      "v_fma_f32 v35, 0.5, v35, v59\n"
      "v_mfma_f32_16x16x32_bf16 v[76:79], v[156:159], a[200:203], v[76:79]\n"
      "ds_read_b128 a[192:195], v2 offset:0\n"
      "v_fmac_f32 v44, s48, v68\n"
      "v_mfma_f32_16x16x32_bf16 v[80:83], v[148:151], a[200:203], v[80:83]\n"
      "v_fmac_f32 v45, s48, v69\n"
      "v_fmac_f32 v46, s48, v70\n"
      "v_mfma_f32_16x16x32_bf16 v[84:87], v[140:143], a[200:203], v[84:87]\n"
      "v_fmac_f32 v47, s48, v71\n"
      "v_fmac_f32 v32, s45, v44\n"
      "v_mfma_f32_16x16x32_bf16 v[88:91], v[152:155], a[204:207], v[88:91]\n"
      "ds_read_b128 a[196:199], v2 offset:4096\n"
      "ds_read_b64_tr_b16 a[248:249], v7 offset:54272\n"
      "v_mfma_f32_16x16x32_bf16 v[92:95], v[144:147], a[204:207], v[92:95]\n"
      "s_waitcnt lgkmcnt(3)\n"
      "ds_read_b64_tr_b16 a[250:251], v7 offset:54280\n"
      "v_fmac_f32 v33, s45, v45\n"
      "v_fmac_f32 v34, s45, v46\n"
      "v_mfma_f32_16x16x32_bf16 v[96:99], v[136:139], a[204:207], v[96:99]\n"
      "buffer_store_dwordx4 a[228:231], v16, s[8:11], s58 offen offset:256 sc1\n"
      "v_mfma_f32_16x16x32_bf16 v[76:79], v[132:135], a[208:211], v[76:79]\n"
      "ds_read_b128 a[200:203], v2 offset:64\n"
      "v_fmac_f32 v35, s45, v47\n"
      "v_mfma_f32_16x16x32_bf16 v[80:83], v[124:127], a[208:211], v[80:83]\n"
      "v_fma_f32 v44, s47, v44, -v68\n"
      "v_fma_f32 v45, s47, v45, -v69\n"
      "v_mfma_f32_16x16x32_bf16 v[84:87], v[116:119], a[208:211], v[84:87]\n"
      "v_fma_f32 v46, s47, v46, -v70\n"
      "v_fma_f32 v47, s47, v47, -v71\n"
      "v_mfma_f32_16x16x32_bf16 v[88:91], v[128:131], a[212:215], v[88:91]\n"
      "s_nop 0\n"
      "ds_read_b128 a[204:207], v2 offset:4160\n"
      "v_cvt_pk_bf16_f32 v32, v32, v33\n"
      "v_mfma_f32_16x16x32_bf16 v[92:95], v[120:123], a[212:215], v[92:95]\n"
      "s_waitcnt lgkmcnt(2)\n"
      "ds_write_addtid_b32 v32 offset:41988\n"
      "v_cvt_pk_bf16_f32 v34, v34, v35\n"
      "v_mfma_f32_4x4x4_16b_bf16 v[20:23], a[216:217], a[248:249], 0\n"
      "v_cvt_pk_bf16_f32 v44, v44, v45\n"
      "v_mfma_f32_4x4x4_16b_bf16 v[24:27], a[216:217], a[250:251], 0\n"
      "ds_write_addtid_b32 v34 offset:42500\n"
      "ds_write_addtid_b32 v44 offset:44036\n"
      "v_cvt_pk_bf16_f32 v46, v46, v47\n"
      "v_fmac_f32 v48, s46, v36\n"
      "v_mfma_f32_16x16x32_bf16 v[96:99], v[112:115], a[212:215], v[96:99]\n"
      "ds_write_addtid_b32 v46 offset:44548\n"
      "v_fmac_f32 v49, s46, v37\n"
      "v_cvt_pk_bf16_f32 v20, v20, v24\n"
      "v_fmac_f32 v50, s46, v38\n"
      "v_mfma_f32_16x16x32_bf16 v[76:79], v[108:111], a[192:195], v[76:79]\n"
      "s_nop 0\n"
      "ds_read_b128 a[208:211], v2 offset:128\n"
      "ds_write_addtid_b32 v20 offset:19968\n"
      "v_mfma_f32_16x16x32_bf16 v[80:83], v[100:103], a[192:195], v[80:83]\n"
      "v_fmac_f32 v51, s46, v39\n"
      "v_fma_f32 v36, 0.5, v36, v60\n"
      "v_mfma_f32_16x16x32_bf16 v[84:87], a[160:163], a[192:195], v[84:87]\n"
      "v_fma_f32 v37, 0.5, v37, v61\n"
      "v_cvt_pk_bf16_f32 v21, v21, v25\n"
      "v_mfma_f32_16x16x32_bf16 v[88:91], v[104:107], a[196:199], v[88:91]\n"
      "s_nop 0\n"
      "ds_read_b128 a[212:215], v2 offset:4224\n"
      "ds_write_addtid_b32 v21 offset:24064\n"
      "v_fma_f32 v38, 0.5, v38, v62\n"
      "v_mfma_f32_16x16x32_bf16 v[92:95], a[164:167], a[196:199], v[92:95]\n"
      "ds_read2st64_b32 a[232:233], v10 offset0:0 offset1:2\n"
      "ds_read2st64_b32 a[234:235], v13 offset0:0 offset1:2\n"
      "v_mfma_f32_16x16x32_bf16 v[96:99], a[156:159], a[196:199], v[96:99]\n"
      "s_waitcnt lgkmcnt(4)\n"
      "v_fma_f32 v39, 0.5, v39, v63\n"
      "v_fmac_f32 v48, s48, v72\n"
      "v_cvt_pk_bf16_f32 v22, v22, v26\n"
      "v_mfma_f32_16x16x32_bf16 v[76:79], a[152:155], a[200:203], v[76:79]\n"
      "ds_read_b128 a[192:195], v3 offset:33792\n"
      "ds_write_addtid_b32 v22 offset:28160\n"
      "v_mfma_f32_16x16x32_bf16 v[80:83], a[144:147], a[200:203], v[80:83]\n"
      "v_fmac_f32 v49, s48, v73\n"
      "v_fmac_f32 v50, s48, v74\n"
      "v_mfma_f32_16x16x32_bf16 v[84:87], a[136:139], a[200:203], v[84:87]\n"
      "v_fmac_f32_e64 v51, s48, v75\n"
      "v_cvt_pk_bf16_f32 v23, v23, v27\n"
      "v_mfma_f32_16x16x32_bf16 v[88:91], a[148:151], a[204:207], v[88:91]\n"
      "ds_read_b128 a[196:199], v3 offset:33856\n"
      "ds_write_addtid_b32 v23 offset:32256\n"
      "v_fmac_f32 v36, s45, v48\n"
      "v_mfma_f32_16x16x32_bf16 v[92:95], a[140:143], a[204:207], v[92:95]\n"
      "v_fmac_f32 v37, s45, v49\n"
      "v_fmac_f32 v38, s45, v50\n"
      "v_mfma_f32_16x16x32_bf16 v[96:99], a[132:135], a[204:207], v[96:99]\n"
      "s_nop 0\n"
      "ds_read2st64_b32 a[236:237], v11 offset0:0 offset1:2\n"
      "ds_read2st64_b32 a[238:239], v14 offset0:0 offset1:2\n"
      "v_mfma_f32_16x16x32_bf16 v[76:79], a[128:131], a[208:211], v[76:79]\n"
      "ds_read_b128 a[200:203], v3 offset:33920\n"
      "v_fmac_f32 v39, s45, v51\n"
      "v_mfma_f32_16x16x32_bf16 v[80:83], a[120:123], a[208:211], v[80:83]\n"
      "s_nop 0\n"
      "v_fma_f32 v48, s47, v48, -v72\n"
      "v_fma_f32 v49, s47, v49, -v73\n"
      "v_mfma_f32_16x16x32_bf16 v[84:87], a[112:115], a[208:211], v[84:87]\n"
      "s_waitcnt lgkmcnt(3)\n"
      "v_fma_f32 v50, s47, v50, -v74\n"
      "v_fma_f32 v51, s47, v51, -v75\n"
      "v_mfma_f32_16x16x32_bf16 v[88:91], a[124:127], a[212:215], v[88:91]\n"
      "ds_read_b128 a[204:207], v4 offset:33792\n"
      "v_cvt_pk_bf16_f32 v36, v36, v37\n"
      "v_mfma_f32_16x16x32_bf16 v[92:95], a[116:119], a[212:215], v[92:95]\n"
      "s_nop 0\n"
      "ds_write_addtid_b32 v36 offset:37888\n"
      "v_cvt_pk_bf16_f32 v38, v38, v39\n"
      "v_mfma_f32_16x16x32_bf16 v[96:99], a[108:111], a[212:215], v[96:99]\n"
      "buffer_store_dwordx4 a[232:235], v16, s[8:11], s59 offen offset:0 sc1\n"
      "ds_write_addtid_b32 v38 offset:38400\n"
      "v_mfma_f32_16x16x32_bf16 v[52:55], a[0:3], a[192:195], 0\n"
      "ds_read_b128 a[208:211], v4 offset:33856\n"
      "v_add_f32_e64 v76, v76, v88\n"
      "v_mfma_f32_16x16x32_bf16 v[56:59], a[4:7], a[192:195], 0\n"
      "ds_write_addtid_b32 v76 offset:45312\n"
      "v_add_f32 v77, v77, v89\n"
      "v_add_f32 v78, v78, v90\n"
      "v_mfma_f32_16x16x32_bf16 v[60:63], a[8:11], a[192:195], 0\n"
      "ds_write_addtid_b32 v77 offset:45824\n"
      "ds_write_addtid_b32 v78 offset:46336\n"
      "v_add_f32 v79, v79, v91\n"
      "v_mfma_f32_16x16x32_bf16 v[52:55], a[12:15], a[196:199], v[52:55]\n"
      "s_nop 0\n"
      "ds_read_b128 a[212:215], v4 offset:33920\n"
      "ds_write_addtid_b32 v79 offset:46848\n"
      "v_mfma_f32_16x16x32_bf16 v[56:59], a[16:19], a[196:199], v[56:59]\n"
      "v_add_f32 v80, v80, v92\n"
      "v_cvt_pk_bf16_f32 v48, v48, v49\n"
      "v_add_f32_e64 v81, v81, v93\n"
      "v_mfma_f32_16x16x32_bf16 v[60:63], a[20:23], a[196:199], v[60:63]\n"
      "s_waitcnt lgkmcnt(4)\n"
      "ds_write_addtid_b32 v48 offset:39936\n"
      "ds_write_addtid_b32 v80 offset:47360\n"
      "v_mfma_f32_16x16x32_bf16 v[52:55], a[24:27], a[200:203], v[52:55]\n"
      "ds_read_b128 a[192:195], v5 offset:33792\n"
      "ds_write_addtid_b32 v81 offset:47872\n"
      "v_mfma_f32_16x16x32_bf16 v[56:59], a[28:31], a[200:203], v[56:59]\n"
      "buffer_store_dwordx4 a[236:239], v16, s[8:11], s59 offen offset:128 sc1\n"
      "v_add_f32 v82, v82, v94\n"
      "v_mfma_f32_16x16x32_bf16 v[60:63], a[32:35], a[200:203], v[60:63]\n"
      "s_nop 0\n"
      "ds_write_addtid_b32 v82 offset:48384\n"
      "v_add_f32 v83, v83, v95\n"
      "v_mfma_f32_16x16x32_bf16 v[52:55], a[36:39], a[204:207], v[52:55]\n"
      "ds_read_b128 a[196:199], v5 offset:33856\n"
      "ds_write_addtid_b32 v83 offset:48896\n"
      "v_cvt_pk_bf16_f32 v50, v50, v51\n"
      "v_mfma_f32_16x16x32_bf16 v[56:59], a[40:43], a[204:207], v[56:59]\n"
      "ds_write_addtid_b32 v50 offset:40448\n"
      "v_add_f32 v84, v84, v96\n"
      "v_mfma_f32_16x16x32_bf16 v[60:63], a[44:47], a[204:207], v[60:63]\n"
      "ds_write_addtid_b32 v84 offset:49408\n"
      "v_add_f32 v85, v85, v97\n"
      "v_add_f32_e64 v86, v86, v98\n"
      "v_add_f32_e64 v87, v87, v99\n"
      "v_mfma_f32_16x16x32_bf16 v[52:55], a[48:51], a[208:211], v[52:55]\n"
      "s_nop 0\n"
      "ds_read_b128 a[200:203], v5 offset:33920\n"
      "ds_write_addtid_b32 v85 offset:49920\n"
      "v_mfma_f32_16x16x32_bf16 v[56:59], a[52:55], a[208:211], v[56:59]\n"
      "s_nop 0\n"
      "ds_write_addtid_b32 v86 offset:50432\n"
      "ds_write_addtid_b32 v87 offset:50944\n"
      "v_mfma_f32_16x16x32_bf16 v[60:63], a[56:59], a[208:211], v[60:63]\n"
      "s_waitcnt lgkmcnt(5)\n"
      "v_fma_f32 v40, -2.0, v88, v76\n"
      "v_fma_f32 v41, -2.0, v89, v77\n"
      "v_mfma_f32_16x16x32_bf16 v[52:55], a[60:63], a[212:215], v[52:55]\n"
      "s_nop 0\n"
      "v_fma_f32 v42, -2.0, v90, v78\n"
      "v_fma_f32 v43, -2.0, v91, v79\n"
      "v_mfma_f32_16x16x32_bf16 v[56:59], a[64:67], a[212:215], v[56:59]\n"
      "s_waitcnt vmcnt(11)\n"
      "v_fma_f32 v44, -2.0, v92, v80\n"
      "v_fma_f32 v45, -2.0, v93, v81\n"
      "v_mfma_f32_16x16x32_bf16 v[60:63], a[68:71], a[212:215], v[60:63]\n"
      "s_waitcnt lgkmcnt(0)\n"
      "s_barrier\n"
      "v_mfma_f32_16x16x32_bf16 v[52:55], a[72:75], a[192:195], v[52:55]\n"
      "s_add_u32 s58, s40, 0\n"
      "ds_read_b128 a[204:207], v0 offset:16384\n"
      "ds_read2st64_b32 v[28:29], v6 offset0:0 offset1:2\n"
      "v_fma_f32 v46, -2.0, v94, v82\n"
      "v_mfma_f32_16x16x32_bf16 v[56:59], a[76:79], a[192:195], v[56:59]\n"
      "s_mov_b32 s40, s62\n"
      "ds_read_b128 a[208:211], v0 offset:20480\n"
      "ds_read2st64_b32 v[30:31], v6 offset0:4 offset1:6\n"
      "v_mfma_f32_16x16x32_bf16 v[60:63], a[80:83], a[192:195], v[60:63]\n"
      "s_add_u32 s62, s62, 786432\n"
      "ds_read2st64_b32 v[32:33], v6 offset0:8 offset1:10\n"
      "ds_read2st64_b32 v[34:35], v6 offset0:12 offset1:14\n"
      "v_mfma_f32_16x16x32_bf16 v[52:55], a[84:87], a[196:199], v[52:55]\n"
      "ds_read_b128 a[212:215], v0 offset:16448\n"
      "v_fma_f32 v47, -2.0, v95, v83\n"
      "v_mfma_f32_16x16x32_bf16 v[56:59], a[88:91], a[196:199], v[56:59]\n"
      "v_fma_f32 v48, -2.0, v96, v84\n"
      "v_fma_f32 v49, -2.0, v97, v85\n"
      "v_mfma_f32_16x16x32_bf16 v[60:63], a[92:95], a[196:199], v[60:63]\n"
      "ds_read2st64_b32 v[36:37], v6 offset0:16 offset1:18\n"
      "ds_read2st64_b32 v[38:39], v6 offset0:20 offset1:22\n"
      "v_mfma_f32_16x16x32_bf16 v[52:55], a[96:99], a[200:203], v[52:55]\n"
      "ds_read_b128 a[192:195], v0 offset:20544\n"
      "ds_read2st64_b32 a[220:221], v10 offset0:181 offset1:183\n"
      "v_mfma_f32_16x16x32_bf16 v[56:59], a[100:103], a[200:203], v[56:59]\n"
      "ds_read2st64_b32 a[222:223], v13 offset0:181 offset1:183\n"
      "v_fma_f32 v50, -2.0, v98, v86\n"
      "v_fma_f32 v51, -2.0, v99, v87\n"
      "v_mfma_f32_16x16x32_bf16 v[60:63], a[104:107], a[200:203], v[60:63]\n"
      "s_waitcnt lgkmcnt(8)\n"
      "v_fmac_f32 v40, s46, v28\n"
      "v_fmac_f32 v41, s46, v29\n"
      "v_mfma_f32_16x16x32_bf16 v[76:79], v[252:255], a[204:207], 0\n"
      "s_nop 0\n"
      "ds_read_b128 a[196:199], v0 offset:16512\n"
      "v_fmac_f32 v42, s46, v30\n"
      "v_mfma_f32_16x16x32_bf16 v[80:83], v[244:247], a[204:207], 0\n"
      "v_fmac_f32 v43, s46, v31\n"
      "v_fma_f32 v28, 0.5, v28, v64\n"
      "v_mfma_f32_16x16x32_bf16 v[84:87], v[236:239], a[204:207], 0\n"
      "v_fma_f32 v29, 0.5, v29, v65\n"
      "v_fma_f32 v30, 0.5, v30, v66\n"
      "v_mfma_f32_16x16x32_bf16 v[88:91], v[248:251], a[208:211], 0\n"
      "ds_read_b128 a[200:203], v0 offset:20608\n"
      "v_fma_f32 v31, 0.5, v31, v67\n"
      "v_mfma_f32_16x16x32_bf16 v[92:95], v[240:243], a[208:211], 0\n"
      "v_fmac_f32 v40, s48, v52\n"
      "v_fmac_f32 v41, s48, v53\n"
      "v_mfma_f32_16x16x32_bf16 v[96:99], v[232:235], a[208:211], 0\n"
      "s_waitcnt lgkmcnt(2)\n"
      "v_fmac_f32 v42, s48, v54\n"
      "v_fmac_f32 v43, s48, v55\n"
      "v_mfma_f32_16x16x32_bf16 v[76:79], v[228:231], a[212:215], v[76:79]\n"
      "s_nop 0\n"
      "ds_read_b128 a[204:207], v1 offset:16384\n"
      "v_fmac_f32 v28, s45, v40\n"
      "v_mfma_f32_16x16x32_bf16 v[80:83], v[220:223], a[212:215], v[80:83]\n"
      "v_fmac_f32 v29, s45, v41\n"
      "v_fmac_f32 v30, s45, v42\n"
      "v_mfma_f32_16x16x32_bf16 v[84:87], v[212:215], a[212:215], v[84:87]\n"
      "v_fmac_f32 v31, s45, v43\n"
      "v_fma_f32 v40, s47, v40, -v52\n"
      "v_mfma_f32_16x16x32_bf16 v[88:91], v[224:227], a[192:195], v[88:91]\n"
      "ds_read_b128 a[208:211], v1 offset:20480\n"
      "v_fma_f32 v41, s47, v41, -v53\n"
      "v_mfma_f32_16x16x32_bf16 v[92:95], v[216:219], a[192:195], v[92:95]\n"
      "v_fma_f32 v42, s47, v42, -v54\n"
      "v_fma_f32 v43, s47, v43, -v55\n"
      "v_mfma_f32_16x16x32_bf16 v[96:99], v[208:211], a[192:195], v[96:99]\n"
      "s_waitcnt lgkmcnt(1)\n"
      "buffer_store_dwordx4 a[220:223], v16, s[8:11], s58 offen offset:0 sc1\n"
      "v_mfma_f32_16x16x32_bf16 v[76:79], v[204:207], a[196:199], v[76:79]\n"
      "s_nop 0\n"
      "ds_read_b128 a[212:215], v1 offset:16448\n"
      "v_cvt_pk_bf16_f32 v28, v28, v29\n"
      "v_mfma_f32_16x16x32_bf16 v[80:83], v[196:199], a[196:199], v[80:83]\n"
      "ds_write_addtid_b32 v28 offset:32768\n"
      "ds_read2st64_b32 a[224:225], v11 offset0:181 offset1:183\n"
      "v_cvt_pk_bf16_f32 v30, v30, v31\n"
      "v_mfma_f32_16x16x32_bf16 v[84:87], v[188:191], a[196:199], v[84:87]\n"
      "ds_write_addtid_b32 v30 offset:33280\n"
      "ds_read2st64_b32 a[226:227], v14 offset0:181 offset1:183\n"
      "v_cvt_pk_bf16_f32 v40, v40, v41\n"
      "v_mfma_f32_16x16x32_bf16 v[88:91], v[200:203], a[200:203], v[88:91]\n"
      "ds_read_b128 a[192:195], v1 offset:20544\n"
      "ds_write_addtid_b32 v40 offset:34816\n"
      "v_mfma_f32_16x16x32_bf16 v[92:95], v[192:195], a[200:203], v[92:95]\n"
      "v_cvt_pk_bf16_f32 v42, v42, v43\n"
      "v_fmac_f32_e64 v44, s46, v32\n"
      "v_mfma_f32_16x16x32_bf16 v[96:99], v[184:187], a[200:203], v[96:99]\n"
      "ds_write_addtid_b32 v42 offset:35328\n"
      "v_fmac_f32 v45, s46, v33\n"
      "v_mfma_f32_16x16x32_bf16 v[76:79], v[180:183], a[204:207], v[76:79]\n"
      "ds_read_b128 a[196:199], v1 offset:16512\n"
      "v_fmac_f32 v46, s46, v34\n"
      "v_mfma_f32_16x16x32_bf16 v[80:83], v[172:175], a[204:207], v[80:83]\n"
      "v_fmac_f32_e64 v47, s46, v35\n"
      "v_fma_f32 v32, 0.5, v32, v68\n"
      "v_mfma_f32_16x16x32_bf16 v[84:87], v[164:167], a[204:207], v[84:87]\n"
      "s_waitcnt lgkmcnt(4)\n"
      "v_fma_f32 v33, 0.5, v33, v69\n"
      "v_fma_f32 v34, 0.5, v34, v70\n"
      "v_mfma_f32_16x16x32_bf16 v[88:91], v[176:179], a[208:211], v[88:91]\n"
      "ds_read_b128 a[200:203], v1 offset:20608\n"
      "v_fma_f32 v35, 0.5, v35, v71\n"
      "v_fmac_f32 v44, s48, v56\n"
      "v_mfma_f32_16x16x32_bf16 v[92:95], v[168:171], a[208:211], v[92:95]\n"
      "v_fmac_f32 v45, s48, v57\n"
      "v_fmac_f32 v46, s48, v58\n"
      "v_mfma_f32_16x16x32_bf16 v[96:99], v[160:163], a[208:211], v[96:99]\n"
      "v_fmac_f32 v47, s48, v59\n"
      "v_fmac_f32 v32, s45, v44\n"
      "v_mfma_f32_16x16x32_bf16 v[76:79], v[156:159], a[212:215], v[76:79]\n"
      "ds_read_b128 a[204:207], v2 offset:16384\n"
      "v_fmac_f32 v33, s45, v45\n"
      "v_mfma_f32_16x16x32_bf16 v[80:83], v[148:151], a[212:215], v[80:83]\n"
      "v_fmac_f32 v34, s45, v46\n"
      "v_fmac_f32_e64 v35, s45, v47\n"
      "v_mfma_f32_16x16x32_bf16 v[84:87], v[140:143], a[212:215], v[84:87]\n"
      "s_waitcnt lgkmcnt(1)\n"
      "v_fma_f32 v44, s47, v44, -v56\n"
      "v_fma_f32 v45, s47, v45, -v57\n"
      "v_mfma_f32_16x16x32_bf16 v[88:91], v[152:155], a[192:195], v[88:91]\n"
      "buffer_store_dwordx4 a[224:227], v16, s[8:11], s58 offen offset:128 sc1\n"
      "ds_read_b128 a[208:211], v2 offset:20480\n"
      "v_mfma_f32_16x16x32_bf16 v[92:95], v[144:147], a[192:195], v[92:95]\n"
      "s_nop 0\n"
      "v_fma_f32 v46, s47, v46, -v58\n"
      "v_fma_f32 v47, s47, v47, -v59\n"
      "v_mfma_f32_16x16x32_bf16 v[96:99], v[136:139], a[192:195], v[96:99]\n"
      "ds_read2st64_b32 a[228:229], v12 offset0:181 offset1:183\n"
      "ds_read2st64_b32 a[230:231], v15 offset0:181 offset1:183\n"
      "v_cvt_pk_bf16_f32 v32, v32, v33\n"
      "v_mfma_f32_16x16x32_bf16 v[76:79], v[132:135], a[196:199], v[76:79]\n"
      "ds_read_b128 a[212:215], v2 offset:16448\n"
      "ds_write_addtid_b32 v32 offset:40964\n"
      "v_mfma_f32_16x16x32_bf16 v[80:83], v[124:127], a[196:199], v[80:83]\n"
      "v_cvt_pk_bf16_f32 v34, v34, v35\n"
      "v_cvt_pk_bf16_f32 v44, v44, v45\n"
      "v_mfma_f32_16x16x32_bf16 v[84:87], v[116:119], a[196:199], v[84:87]\n"
      "ds_write_addtid_b32 v34 offset:41476\n"
      "ds_write_addtid_b32 v44 offset:43012\n"
      "v_mfma_f32_16x16x32_bf16 v[88:91], v[128:131], a[200:203], v[88:91]\n"
      "ds_read_b128 a[192:195], v2 offset:20544\n"
      "v_cvt_pk_bf16_f32 v46, v46, v47\n"
      "v_mfma_f32_16x16x32_bf16 v[92:95], v[120:123], a[200:203], v[92:95]\n"
      "ds_write_addtid_b32 v46 offset:43524\n"
      "v_fmac_f32 v48, s46, v36\n"
      "v_fmac_f32 v49, s46, v37\n"
      "v_mfma_f32_16x16x32_bf16 v[96:99], v[112:115], a[200:203], v[96:99]\n"
      "s_waitcnt lgkmcnt(4)\n"
      "v_fmac_f32 v50, s46, v38\n"
      "v_fmac_f32_e64 v51, s46, v39\n"
      "v_mfma_f32_16x16x32_bf16 v[76:79], v[108:111], a[204:207], v[76:79]\n"
      "ds_read_b128 a[196:199], v2 offset:16512\n"
      "v_fma_f32 v36, 0.5, v36, v72\n"
      "v_mfma_f32_16x16x32_bf16 v[80:83], v[100:103], a[204:207], v[80:83]\n"
      "v_fma_f32 v37, 0.5, v37, v73\n"
      "v_fma_f32 v38, 0.5, v38, v74\n"
      "v_mfma_f32_16x16x32_bf16 v[84:87], a[160:163], a[204:207], v[84:87]\n"
      "v_fma_f32 v39, 0.5, v39, v75\n"
      "v_fmac_f32 v48, s48, v60\n"
      "v_mfma_f32_16x16x32_bf16 v[88:91], v[104:107], a[208:211], v[88:91]\n"
      "ds_read_b128 a[200:203], v2 offset:20608\n"
      "v_fmac_f32 v49, s48, v61\n"
      "v_mfma_f32_16x16x32_bf16 v[92:95], a[164:167], a[208:211], v[92:95]\n"
      "v_fmac_f32 v50, s48, v62\n"
      "v_fmac_f32 v51, s48, v63\n"
      "v_mfma_f32_16x16x32_bf16 v[96:99], a[156:159], a[208:211], v[96:99]\n"
      "v_fmac_f32 v36, s45, v48\n"
      "v_fmac_f32 v37, s45, v49\n"
      "v_mfma_f32_16x16x32_bf16 v[76:79], a[152:155], a[212:215], v[76:79]\n"
      "ds_read_b128 a[204:207], v3 offset:50688\n"
      "v_fmac_f32 v38, s45, v50\n"
      "v_fmac_f32 v39, s45, v51\n"
      "v_mfma_f32_16x16x32_bf16 v[80:83], a[144:147], a[212:215], v[80:83]\n"
      "s_nop 0\n"
      "buffer_store_dwordx4 a[228:231], v16, s[8:11], s58 offen offset:256 sc1\n"
      "v_mfma_f32_16x16x32_bf16 v[84:87], a[136:139], a[212:215], v[84:87]\n"
      "s_waitcnt lgkmcnt(1)\n"
      "v_fma_f32 v48, s47, v48, -v60\n"
      "v_fma_f32 v49, s47, v49, -v61\n"
      "v_mfma_f32_16x16x32_bf16 v[88:91], a[148:151], a[192:195], v[88:91]\n"
      "ds_read_b128 a[208:211], v3 offset:50752\n"
      "v_fma_f32 v50, s47, v50, -v62\n"
      "v_mfma_f32_16x16x32_bf16 v[92:95], a[140:143], a[192:195], v[92:95]\n"
      "v_fma_f32 v51, s47, v51, -v63\n"
      "v_cvt_pk_bf16_f32 v36, v36, v37\n"
      "v_mfma_f32_16x16x32_bf16 v[96:99], a[132:135], a[192:195], v[96:99]\n"
      "ds_write_addtid_b32 v36 offset:36864\n"
      "v_cvt_pk_bf16_f32 v38, v38, v39\n"
      "v_mfma_f32_16x16x32_bf16 v[76:79], a[128:131], a[196:199], v[76:79]\n"
      "ds_read_b128 a[212:215], v3 offset:50816\n"
      "ds_write_addtid_b32 v38 offset:37376\n"
      "v_cvt_pk_bf16_f32 v48, v48, v49\n"
      "v_cvt_pk_bf16_f32 v50, v50, v51\n"
      "v_mfma_f32_16x16x32_bf16 v[80:83], a[120:123], a[196:199], v[80:83]\n"
      "ds_write_addtid_b32 v48 offset:38912\n"
      "ds_write_addtid_b32 v50 offset:39424\n"
      "v_mfma_f32_16x16x32_bf16 v[84:87], a[112:115], a[196:199], v[84:87]\n"
      "v_mfma_f32_16x16x32_bf16 v[88:91], a[124:127], a[200:203], v[88:91]\n"
      "ds_read_b128 a[192:195], v4 offset:50688\n"
      "v_mfma_f32_16x16x32_bf16 v[92:95], a[116:119], a[200:203], v[92:95]\n"
      "v_mfma_f32_16x16x32_bf16 v[96:99], a[108:111], a[200:203], v[96:99]\n"
      "s_waitcnt lgkmcnt(3)\n"
      "v_mfma_f32_16x16x32_bf16 v[64:67], a[0:3], a[204:207], 0\n"
      "s_nop 0\n"
      "ds_read_b128 a[196:199], v4 offset:50752\n"
      "v_mfma_f32_16x16x32_bf16 v[68:71], a[4:7], a[204:207], 0\n"
      "v_mfma_f32_16x16x32_bf16 v[72:75], a[8:11], a[204:207], 0\n"
      "v_add_f32 v76, v76, v88\n"
      "v_add_f32 v77, v77, v89\n"
      "v_add_f32 v78, v78, v90\n"
      "v_add_f32 v79, v79, v91\n"
      "v_add_f32 v80, v80, v92\n"
      "v_add_f32 v81, v81, v93\n"
      "v_add_f32 v82, v82, v94\n"
      "v_mfma_f32_16x16x32_bf16 v[64:67], a[12:15], a[208:211], v[64:67]\n"
      "s_nop 0\n"
      "ds_read_b128 a[200:203], v4 offset:50816\n"
      "ds_write_addtid_b32 v76 offset:51456\n"
      "ds_write_addtid_b32 v77 offset:51968\n"
      "v_add_f32 v83, v83, v95\n"
      "v_add_f32 v84, v84, v96\n"
      "v_add_f32 v85, v85, v97\n"
      "v_add_f32 v86, v86, v98\n"
      "v_mfma_f32_16x16x32_bf16 v[68:71], a[16:19], a[208:211], v[68:71]\n"
      "ds_write_addtid_b32 v78 offset:52480\n"
      "ds_write_addtid_b32 v79 offset:52992\n"
      "ds_write_addtid_b32 v80 offset:53504\n"
      "v_mfma_f32_16x16x32_bf16 v[72:75], a[20:23], a[208:211], v[72:75]\n"
      "ds_write_addtid_b32 v81 offset:54016\n"
      "ds_write_addtid_b32 v82 offset:54528\n"
      "ds_write_addtid_b32 v83 offset:55040\n"
      "v_add_f32 v87, v87, v99\n"
      "v_mfma_f32_16x16x32_bf16 v[64:67], a[24:27], a[212:215], v[64:67]\n"
      "s_nop 0\n"
      "ds_write_addtid_b32 v84 offset:55552\n"
      "ds_write_addtid_b32 v85 offset:56064\n"
      "v_mfma_f32_16x16x32_bf16 v[68:71], a[28:31], a[212:215], v[68:71]\n"
      "ds_write_addtid_b32 v86 offset:56576\n"
      "ds_write_addtid_b32 v87 offset:57088\n"
      "v_mfma_f32_16x16x32_bf16 v[72:75], a[32:35], a[212:215], v[72:75]\n"
      "s_waitcnt lgkmcnt(0)\n"
      "s_barrier\n"
      "v_mfma_f32_16x16x32_bf16 v[64:67], a[36:39], a[192:195], v[64:67]\n"
      "s_add_u32 s58, s58, 786432\n"
      "ds_read_b128 a[204:207], v5 offset:50688\n"
      "ds_read2st64_b32 v[28:29], v6 offset0:24 offset1:26\n"
      "v_mfma_f32_16x16x32_bf16 v[68:71], a[40:43], a[192:195], v[68:71]\n"
      "ds_read2st64_b32 v[30:31], v6 offset0:28 offset1:30\n"
      "ds_read2st64_b32 v[32:33], v6 offset0:32 offset1:34\n"
      "v_mfma_f32_16x16x32_bf16 v[72:75], a[44:47], a[192:195], v[72:75]\n"
      "ds_read2st64_b32 v[34:35], v6 offset0:36 offset1:38\n"
      "ds_read2st64_b32 v[36:37], v6 offset0:40 offset1:42\n"
      "v_mfma_f32_16x16x32_bf16 v[64:67], a[48:51], a[196:199], v[64:67]\n"
      "ds_read_b128 a[208:211], v5 offset:50752\n"
      "ds_read2st64_b32 v[38:39], v6 offset0:44 offset1:46\n"
      "v_mfma_f32_16x16x32_bf16 v[68:71], a[52:55], a[196:199], v[68:71]\n"
      "v_fma_f32 v40, -2.0, v88, v76\n"
      "v_fma_f32 v41, -2.0, v89, v77\n"
      "v_mfma_f32_16x16x32_bf16 v[72:75], a[56:59], a[196:199], v[72:75]\n"
      "ds_read2st64_b32 a[220:221], v10 offset0:177 offset1:179\n"
      "ds_read2st64_b32 a[222:223], v13 offset0:177 offset1:179\n"
      "v_mfma_f32_16x16x32_bf16 v[64:67], a[60:63], a[200:203], v[64:67]\n"
      "ds_read_b128 a[212:215], v5 offset:50816\n"
      "v_fma_f32 v42, -2.0, v90, v78\n"
      "v_mfma_f32_16x16x32_bf16 v[68:71], a[64:67], a[200:203], v[68:71]\n"
      "ds_read2st64_b32 a[224:225], v11 offset0:177 offset1:179\n"
      "ds_read2st64_b32 a[226:227], v14 offset0:177 offset1:179\n"
      "v_mfma_f32_16x16x32_bf16 v[72:75], a[68:71], a[200:203], v[72:75]\n"
      "s_waitcnt lgkmcnt(9)\n"
      "v_fma_f32 v43, -2.0, v91, v79\n"
      "v_fma_f32 v44, -2.0, v92, v80\n"
      "v_mfma_f32_16x16x32_bf16 v[64:67], a[72:75], a[204:207], v[64:67]\n"
      "s_waitcnt lgkmcnt(3)\n"
      "buffer_store_dwordx4 a[220:223], v16, s[8:11], s58 offen offset:0 sc1\n"
      "v_mfma_f32_16x16x32_bf16 v[68:71], a[76:79], a[204:207], v[68:71]\n"
      "v_fma_f32 v45, -2.0, v93, v81\n"
      "v_fma_f32 v46, -2.0, v94, v82\n"
      "v_mfma_f32_16x16x32_bf16 v[72:75], a[80:83], a[204:207], v[72:75]\n"
      "ds_read2st64_b32 a[228:229], v12 offset0:177 offset1:179\n"
      "ds_read2st64_b32 a[230:231], v15 offset0:177 offset1:179\n"
      "v_mfma_f32_16x16x32_bf16 v[64:67], a[84:87], a[208:211], v[64:67]\n"
      "s_waitcnt lgkmcnt(2)\n"
      "buffer_store_dwordx4 a[224:227], v16, s[8:11], s58 offen offset:128 sc1\n"
      "v_mfma_f32_16x16x32_bf16 v[68:71], a[88:91], a[208:211], v[68:71]\n"
      "s_nop 0\n"
      "v_fma_f32 v47, -2.0, v95, v83\n"
      "v_fma_f32 v48, -2.0, v96, v84\n"
      "v_mfma_f32_16x16x32_bf16 v[72:75], a[92:95], a[208:211], v[72:75]\n"
      "v_fma_f32 v49, -2.0, v97, v85\n"
      "v_fma_f32 v50, -2.0, v98, v86\n"
      "v_mfma_f32_16x16x32_bf16 v[64:67], a[96:99], a[212:215], v[64:67]\n"
      "s_waitcnt lgkmcnt(0)\n"
      "buffer_store_dwordx4 a[228:231], v16, s[8:11], s58 offen offset:256 sc1\n"
      "v_mfma_f32_16x16x32_bf16 v[68:71], a[100:103], a[212:215], v[68:71]\n"
      "s_nop 0\n"
      "v_fma_f32 v51, -2.0, v99, v87\n"
      "v_fmac_f32 v40, s46, v28\n"
      "v_mfma_f32_16x16x32_bf16 v[72:75], a[104:107], a[212:215], v[72:75]\n"
      "v_fmac_f32 v41, s46, v29\n"
      "v_fmac_f32 v42, s46, v30\n"
      "v_fmac_f32 v43, s46, v31\n"
      "v_fma_f32 v28, 0.5, v28, v52\n"
      "v_fma_f32 v29, 0.5, v29, v53\n"
      "v_fma_f32 v30, 0.5, v30, v54\n"
      "v_fma_f32 v31, 0.5, v31, v55\n"
      "v_fmac_f32 v44, s46, v32\n"
      "v_fmac_f32 v45, s46, v33\n"
      "v_pk_fma_f32 v[46:47], v[34:35], s[46:47], v[46:47] op_sel:[0,0,0] op_sel_hi:[1,0,1]\n"
      "v_pk_fma_f32 v[32:33], v[32:33], s[48:49], v[56:57] op_sel:[0,1,0] op_sel_hi:[1,1,1]\n"
      "v_pk_fma_f32 v[34:35], v[34:35], s[48:49], v[58:59] op_sel:[0,1,0] op_sel_hi:[1,1,1]\n"
      "v_pk_fma_f32 v[48:49], v[36:37], s[46:47], v[48:49] op_sel:[0,0,0] op_sel_hi:[1,0,1]\n"
      "v_pk_fma_f32 v[50:51], v[38:39], s[46:47], v[50:51] op_sel:[0,0,0] op_sel_hi:[1,0,1]\n"
      "v_pk_fma_f32 v[36:37], v[36:37], s[48:49], v[60:61] op_sel:[0,1,0] op_sel_hi:[1,1,1]\n"
      "v_pk_fma_f32 v[38:39], v[38:39], s[48:49], v[62:63] op_sel:[0,1,0] op_sel_hi:[1,1,1]\n"
      "v_pk_fma_f32 v[40:41], v[64:65], s[48:49], v[40:41] op_sel:[0,0,0] op_sel_hi:[1,0,1]\n"
      "v_pk_fma_f32 v[42:43], v[66:67], s[48:49], v[42:43] op_sel:[0,0,0] op_sel_hi:[1,0,1]\n"
      "v_pk_fma_f32 v[28:29], v[40:41], s[44:45], v[28:29] op_sel:[0,1,0] op_sel_hi:[1,1,1]\n"
      "v_pk_fma_f32 v[30:31], v[42:43], s[44:45], v[30:31] op_sel:[0,1,0] op_sel_hi:[1,1,1]\n"
      "v_pk_fma_f32 v[40:41], v[40:41], s[46:47], v[64:65] op_sel:[0,1,0] op_sel_hi:[1,1,1] neg_lo:[0,0,1] neg_hi:[0,0,1]\n"
      "v_pk_fma_f32 v[42:43], v[42:43], s[46:47], v[66:67] op_sel:[0,1,0] op_sel_hi:[1,1,1] neg_lo:[0,0,1] neg_hi:[0,0,1]\n"
      "v_cvt_pk_bf16_f32 v28, v28, v29\n"
      "v_cvt_pk_bf16_f32 v30, v30, v31\n"
      "v_cvt_pk_bf16_f32 v40, v40, v41\n"
      "v_cvt_pk_bf16_f32 v42, v42, v43\n"
      "v_pk_fma_f32 v[44:45], v[68:69], s[48:49], v[44:45] op_sel:[0,0,0] op_sel_hi:[1,0,1]\n"
      "v_pk_fma_f32 v[46:47], v[70:71], s[48:49], v[46:47] op_sel:[0,0,0] op_sel_hi:[1,0,1]\n"
      "v_pk_fma_f32 v[32:33], v[44:45], s[44:45], v[32:33] op_sel:[0,1,0] op_sel_hi:[1,1,1]\n"
      "v_pk_fma_f32 v[34:35], v[46:47], s[44:45], v[34:35] op_sel:[0,1,0] op_sel_hi:[1,1,1]\n"
      "v_pk_fma_f32 v[44:45], v[44:45], s[46:47], v[68:69] op_sel:[0,1,0] op_sel_hi:[1,1,1] neg_lo:[0,0,1] neg_hi:[0,0,1]\n"
      "v_pk_fma_f32 v[46:47], v[46:47], s[46:47], v[70:71] op_sel:[0,1,0] op_sel_hi:[1,1,1] neg_lo:[0,0,1] neg_hi:[0,0,1]\n"
      "v_cvt_pk_bf16_f32 v32, v32, v33\n"
      "v_cvt_pk_bf16_f32 v34, v34, v35\n"
      "ds_write_addtid_b32 v28 offset:33792\n"
      "ds_write_addtid_b32 v30 offset:34304\n"
      "ds_write_addtid_b32 v40 offset:35840\n"
      "ds_write_addtid_b32 v42 offset:36352\n"
      "ds_write_addtid_b32 v32 offset:41988\n"
      "ds_write_addtid_b32 v34 offset:42500\n"
      "v_cvt_pk_bf16_f32 v44, v44, v45\n"
      "v_cvt_pk_bf16_f32 v46, v46, v47\n"
      "v_pk_fma_f32 v[48:49], v[72:73], s[48:49], v[48:49] op_sel:[0,0,0] op_sel_hi:[1,0,1]\n"
      "v_pk_fma_f32 v[50:51], v[74:75], s[48:49], v[50:51] op_sel:[0,0,0] op_sel_hi:[1,0,1]\n"
      "v_pk_fma_f32 v[36:37], v[48:49], s[44:45], v[36:37] op_sel:[0,1,0] op_sel_hi:[1,1,1]\n"
      "v_pk_fma_f32 v[38:39], v[50:51], s[44:45], v[38:39] op_sel:[0,1,0] op_sel_hi:[1,1,1]\n"
      "v_pk_fma_f32 v[48:49], v[48:49], s[46:47], v[72:73] op_sel:[0,1,0] op_sel_hi:[1,1,1] neg_lo:[0,0,1] neg_hi:[0,0,1]\n"
      "v_pk_fma_f32 v[50:51], v[50:51], s[46:47], v[74:75] op_sel:[0,1,0] op_sel_hi:[1,1,1] neg_lo:[0,0,1] neg_hi:[0,0,1]\n"
      "v_cvt_pk_bf16_f32 v36, v36, v37\n"
      "v_cvt_pk_bf16_f32 v38, v38, v39\n"
      "v_cvt_pk_bf16_f32 v48, v48, v49\n"
      "v_cvt_pk_bf16_f32 v50, v50, v51\n"
      "ds_write_addtid_b32 v44 offset:44036\n"
      "ds_write_addtid_b32 v46 offset:44548\n"
      "ds_write_addtid_b32 v36 offset:37888\n"
      "ds_write_addtid_b32 v38 offset:38400\n"
      "ds_write_addtid_b32 v48 offset:39936\n"
      "ds_write_addtid_b32 v50 offset:40448\n"
      "s_waitcnt lgkmcnt(0)\n"
      "s_barrier\n"
      "ds_read2st64_b32 a[220:221], v10 offset0:181 offset1:183\n"
      "ds_read2st64_b32 a[222:223], v13 offset0:181 offset1:183\n"
      "ds_read2st64_b32 a[224:225], v11 offset0:181 offset1:183\n"
      "ds_read2st64_b32 a[226:227], v14 offset0:181 offset1:183\n"
      "ds_read2st64_b32 a[228:229], v12 offset0:181 offset1:183\n"
      "ds_read2st64_b32 a[230:231], v15 offset0:181 offset1:183\n"
      "s_add_u32 s58, s58, 786432\n"
      "s_waitcnt lgkmcnt(4)\n"
      "buffer_store_dwordx4 a[220:223], v16, s[8:11], s58 offen offset:0 sc1\n"
      "s_waitcnt lgkmcnt(2)\n"
      "buffer_store_dwordx4 a[224:227], v16, s[8:11], s58 offen offset:128 sc1\n"
      "s_waitcnt lgkmcnt(0)\n"
      "buffer_store_dwordx4 a[228:231], v16, s[8:11], s58 offen offset:256 sc1\n"
      "LDend_%=:\n"
      "s_branch Lfin_%=\n"
      ".p2align 6\n"
      "LS_%=:\n"
      "v_lshlrev_b32 v43, 4, v37\n"
      "v_lshlrev_b32 v44, 2, v37\n"
      "v_and_b32 v43, 16, v43\n"
      "v_and_or_b32 v43, v44, 8, v43\n"
      "v_mul_u32_u24 v44, 192, v38\n"
      "v_mul_u32_u24 v45, 1536, v39\n"
      "s_lshl_b32 s27, s18, 5\n"
      "v_lshlrev_b32 v46, 1, v36\n"
      "v_add3_u32 v43, v43, v44, v45\n"
      "v_bitop3_b32 v46, v46, s27, 32 bitop3:0x6c\n"
      "v_add_u32 v60, v43, v46\n"
      "v_add_u32 v61, 57344, v60\n"
      "v_add_u32 v62, 114688, v60\n"
      "s_lshl_b32 s26, s18, 3\n"
      "v_and_b32 v42, 15, v36\n"
      "v_xor_b32 v42, s26, v42\n"
      "s_lshl_b32 s26, s17, 13\n"
      "s_add_u32 s26, s26, 24832\n"
      "s_mul_i32 s27, s17, 12672\n"
      "v_mul_u32_u24 v44, 192, v42\n"
      "v_lshlrev_b32 v43, 3, v42\n"
      "v_and_b32 v45, 48, v36\n"
      "s_add_u32 s27, s27, 82432\n"
      "v_bitop3_b32 v46, v43, v45, 32 bitop3:0x6c\n"
      "v_add3_u32 v0, v46, v44, s26\n"
      "v_add3_u32 v3, v46, v44, s27\n"
      "v_add_u32 v43, 16, v43\n"
      "v_add_u32 v44, 384, v44\n"
      "v_bitop3_b32 v46, v43, v45, 32 bitop3:0x6c\n"
      "v_add3_u32 v1, v46, v44, s26\n"
      "v_add3_u32 v4, v46, v44, s27\n"
      "v_add_u32 v43, 16, v43\n"
      "v_add_u32 v44, 384, v44\n"
      "v_bitop3_b32 v46, v43, v45, 32 bitop3:0x6c\n"
      "v_add3_u32 v2, v46, v44, s26\n"
      "v_add3_u32 v5, v46, v44, s27\n"
      "v_lshlrev_b32 v43, 31, v36\n"
      "v_and_b32 v44, 2, v36\n"
      "v_or_b32 v43, 0x3f803f80, v43\n"
      "v_mul_u32_u24 v45, 0xc00080, v44\n"
      "v_lshl_add_u32 v44, v44, 22, v43\n"
      "v_add_u32 v45, v45, v43\n"
      "v_accvgpr_write_b32 a216, v44\n"
      "v_accvgpr_write_b32 a217, v45\n"
      "v_lshlrev_b32 v43, 3, v38\n"
      "s_mov_b32 s26, 806420736\n"
      "v_bfe_u32 v43, s26, v43, 8\n"
      "v_lshlrev_b32 v44, 12, v38\n"
      "s_lshl_b32 s28, s18, 8\n"
      "s_add_u32 s28, s28, 82432\n"
      "v_lshl_add_u32 v43, v43, 3, v44\n"
      "v_lshl_add_u32 v44, v39, 2, v37\n"
      "v_lshl_add_u32 v43, v44, 4, v43\n"
      "v_add_u32 v7, s28, v43\n"
      "s_mul_i32 s26, s17, 70144\n"
      "s_lshl_b32 s27, s18, 8\n"
      "s_sub_u32 s26, 70144, s26\n"
      "s_add_u32 s26, s26, s27\n"
      "v_lshl_add_u32 v6, v36, 2, s26\n"
      "v_lshrrev_b32 v43, 3, v36\n"
      "v_and_b32 v44, 7, v36\n"
      "v_mul_u32_u24 v45, 384, v43\n"
      "v_lshl_add_u32 v11, v44, 4, v45\n"
      "v_and_b32 v44, 2, v36\n"
      "v_lshlrev_b32 v43, 3, v43\n"
      "v_lshlrev_b32 v45, 4, v44\n"
      "v_xor_b32 v43, v43, v45\n"
      "v_lshl_add_u32 v43, v44, 7, v43\n"
      "v_and_b32 v44, 1, v36\n"
      "s_lshl_b32 s26, s18, 11\n"
      "v_lshl_add_u32 v43, v44, 6, v43\n"
      "s_add_u32 s26, s26, 12288\n"
      "v_bfe_u32 v44, v36, 2, 1\n"
      "v_add_u32 v43, s26, v43\n"
      "v_mul_i32_i24 v45, -4100, v44\n"
      "s_mov_b32 s27, 8200\n"
      "v_add3_u32 v8, v43, v45, s27\n"
      "v_add_u32 v9, 128, v8\n"
      "s_mul_i32 s62, s18, 393216\n"
      "s_add_u32 s62, s62, s19\n"
      "s_add_u32 s62, s62, s21\n"
      "s_mov_b32 s40, 0x80000000\n"
      "s_and_b32 s9, s9, 0xffff\n"
      "s_mov_b32 s45, 0xbeaaaaab\n"
      "s_mov_b32 s46, 0xbe800000\n"
      "s_mov_b32 s47, 0x3eaaaaab\n"
      "s_add_u32 m0, s63, 6144\n"
      "s_add_u32 s52, s63, 61440\n"
      "buffer_load_dwordx4 v17, s[12:15], s52 offen offset:0 lds\n"
      "buffer_load_dwordx4 v18, s[12:15], s52 offen offset:1024 lds\n"
      "buffer_load_dwordx4 v19, s[12:15], s52 offen offset:2048 lds\n"
      "s_add_u32 m0, s63, 12288\n"
      "s_add_u32 s52, s63, 116736\n"
      "buffer_load_dwordx4 v17, s[12:15], s52 offen offset:0 lds\n"
      "buffer_load_dwordx4 v18, s[12:15], s52 offen offset:1024 lds\n"
      "buffer_load_dwordx4 v19, s[12:15], s52 offen offset:2048 lds\n"
      "LSpro_%=:\n"
      "s_waitcnt vmcnt(6)\n"
      "s_barrier\n"
      "ds_read_b64_tr_b16 a[224:225], v60 offset:24832\n"
      "ds_read_b64_tr_b16 a[226:227], v60 offset:25600\n"
      "ds_read_b64_tr_b16 a[0:1], v62 offset:26112\n"
      "ds_read_b64_tr_b16 a[2:3], v62 offset:26880\n"
      "ds_read_b64_tr_b16 a[220:221], v60 offset:30976\n"
      "ds_read_b64_tr_b16 a[222:223], v60 offset:31744\n"
      "ds_read_b64_tr_b16 a[232:233], v60 offset:24896\n"
      "ds_read_b64_tr_b16 a[234:235], v60 offset:25664\n"
      "ds_read_b64_tr_b16 a[4:5], v62 offset:26176\n"
      "ds_read_b64_tr_b16 a[6:7], v62 offset:26944\n"
      "ds_read_b64_tr_b16 a[228:229], v60 offset:31040\n"
      "ds_read_b64_tr_b16 a[230:231], v60 offset:31808\n"
      "s_add_u32 m0, s63, 18432\n"
      "s_add_u32 s52, s63, 12288\n"
      "s_nop 0\n"
      "s_waitcnt lgkmcnt(10)\n"
      "v_mfma_f32_4x4x4_16b_bf16 v[28:31], a[244:245], a[224:225], 0\n"
      "v_mfma_f32_4x4x4_16b_bf16 v[32:35], a[244:245], a[226:227], 0\n"
      "buffer_load_dwordx4 v17, s[12:15], s52 offen offset:0 lds\n"
      "s_waitcnt lgkmcnt(8)\n"
      "v_mfma_f32_4x4x4_16b_bf16 v[28:31], a[248:249], a[0:1], v[28:31]\n"
      "ds_read_b64_tr_b16 a[240:241], v60 offset:24960\n"
      "v_mfma_f32_4x4x4_16b_bf16 v[32:35], a[248:249], a[2:3], v[32:35]\n"
      "buffer_load_dwordx4 v18, s[12:15], s52 offen offset:1024 lds\n"
      "s_waitcnt lgkmcnt(7)\n"
      "v_mfma_f32_4x4x4_16b_bf16 v[36:39], a[246:247], a[220:221], v[28:31]\n"
      "ds_read_b64_tr_b16 a[242:243], v60 offset:25728\n"
      "v_mfma_f32_4x4x4_16b_bf16 v[40:43], a[246:247], a[222:223], v[32:35]\n"
      "buffer_load_dwordx4 v19, s[12:15], s52 offen offset:2048 lds\n"
      "v_mfma_f32_4x4x4_16b_bf16 v[28:31], a[250:251], a[220:221], v[28:31]\n"
      "ds_read_b64_tr_b16 a[8:9], v62 offset:26240\n"
      "v_mfma_f32_4x4x4_16b_bf16 v[32:35], a[250:251], a[222:223], v[32:35]\n"
      "ds_read_b64_tr_b16 a[10:11], v62 offset:27008\n"
      "ds_read_b64_tr_b16 a[236:237], v60 offset:31104\n"
      "ds_read_b64_tr_b16 a[238:239], v60 offset:31872\n"
      "s_add_u32 m0, s63, 57600\n"
      "s_add_u32 s52, s63, 67584\n"
      "s_nop 0\n"
      "s_waitcnt lgkmcnt(5)\n"
      "v_mfma_f32_4x4x4_16b_bf16 v[44:47], a[244:245], a[232:233], 0\n"
      "v_mfma_f32_4x4x4_16b_bf16 v[48:51], a[244:245], a[234:235], 0\n"
      "buffer_load_dwordx4 v17, s[12:15], s52 offen offset:0 lds\n"
      "v_mfma_f32_4x4x4_16b_bf16 v[44:47], a[248:249], a[4:5], v[44:47]\n"
      "buffer_load_dwordx4 v18, s[12:15], s52 offen offset:1024 lds\n"
      "v_mfma_f32_4x4x4_16b_bf16 v[48:51], a[248:249], a[6:7], v[48:51]\n"
      "buffer_load_dwordx4 v19, s[12:15], s52 offen offset:2048 lds\n"
      "v_mfma_f32_4x4x4_16b_bf16 v[52:55], a[246:247], a[228:229], v[44:47]\n"
      "v_mfma_f32_4x4x4_16b_bf16 v[56:59], a[246:247], a[230:231], v[48:51]\n"
      "v_mfma_f32_4x4x4_16b_bf16 v[44:47], a[250:251], a[228:229], v[44:47]\n"
      "v_mfma_f32_4x4x4_16b_bf16 v[48:51], a[250:251], a[230:231], v[48:51]\n"
      "v_cvt_pk_bf16_f32 v252, v36, v40\n"
      "v_cvt_pk_bf16_f32 v253, v37, v41\n"
      "v_cvt_pk_bf16_f32 v254, v38, v42\n"
      "v_cvt_pk_bf16_f32 v255, v39, v43\n"
      "v_cvt_pk_bf16_f32 v248, v28, v32\n"
      "v_cvt_pk_bf16_f32 v249, v29, v33\n"
      "v_cvt_pk_bf16_f32 v250, v30, v34\n"
      "v_cvt_pk_bf16_f32 v251, v31, v35\n"
      "s_add_u32 m0, s63, 69888\n"
      "s_add_u32 s52, s63, 18432\n"
      "s_nop 0\n"
      "s_waitcnt lgkmcnt(0)\n"
      "v_mfma_f32_4x4x4_16b_bf16 v[28:31], a[244:245], a[240:241], 0\n"
      "v_mfma_f32_4x4x4_16b_bf16 v[32:35], a[244:245], a[242:243], 0\n"
      "buffer_load_dwordx4 v17, s[12:15], s52 offen offset:0 lds\n"
      "v_mfma_f32_4x4x4_16b_bf16 v[28:31], a[248:249], a[8:9], v[28:31]\n"
      "buffer_load_dwordx4 v18, s[12:15], s52 offen offset:1024 lds\n"
      "v_mfma_f32_4x4x4_16b_bf16 v[32:35], a[248:249], a[10:11], v[32:35]\n"
      "buffer_load_dwordx4 v19, s[12:15], s52 offen offset:2048 lds\n"
      "v_mfma_f32_4x4x4_16b_bf16 v[36:39], a[246:247], a[236:237], v[28:31]\n"
      "v_mfma_f32_4x4x4_16b_bf16 v[40:43], a[246:247], a[238:239], v[32:35]\n"
      "v_mfma_f32_4x4x4_16b_bf16 v[28:31], a[250:251], a[236:237], v[28:31]\n"
      "v_mfma_f32_4x4x4_16b_bf16 v[32:35], a[250:251], a[238:239], v[32:35]\n"
      "v_cvt_pk_bf16_f32 v244, v52, v56\n"
      "v_cvt_pk_bf16_f32 v245, v53, v57\n"
      "v_cvt_pk_bf16_f32 v246, v54, v58\n"
      "v_cvt_pk_bf16_f32 v247, v55, v59\n"
      "v_cvt_pk_bf16_f32 v240, v44, v48\n"
      "v_cvt_pk_bf16_f32 v241, v45, v49\n"
      "v_cvt_pk_bf16_f32 v242, v46, v50\n"
      "v_cvt_pk_bf16_f32 v243, v47, v51\n"
      "s_waitcnt vmcnt(9)\n"
      "s_barrier\n"
      "ds_read_b64_tr_b16 a[224:225], v60 offset:0\n"
      "ds_read_b64_tr_b16 a[226:227], v60 offset:768\n"
      "ds_read_b64_tr_b16 a[12:13], v60 offset:12288\n"
      "ds_read_b64_tr_b16 a[14:15], v60 offset:13056\n"
      "ds_read_b64_tr_b16 a[220:221], v60 offset:6144\n"
      "ds_read_b64_tr_b16 a[222:223], v60 offset:6912\n"
      "s_add_u32 m0, s63, 76032\n"
      "s_add_u32 s52, s63, 73728\n"
      "s_nop 0\n"
      "s_waitcnt lgkmcnt(4)\n"
      "v_mfma_f32_4x4x4_16b_bf16 v[44:47], a[244:245], a[224:225], 0\n"
      "v_mfma_f32_4x4x4_16b_bf16 v[48:51], a[244:245], a[226:227], 0\n"
      "buffer_load_dwordx4 v17, s[12:15], s52 offen offset:0 lds\n"
      "s_waitcnt lgkmcnt(2)\n"
      "v_mfma_f32_4x4x4_16b_bf16 v[44:47], a[248:249], a[12:13], v[44:47]\n"
      "ds_read_b64_tr_b16 a[232:233], v60 offset:64\n"
      "v_mfma_f32_4x4x4_16b_bf16 v[48:51], a[248:249], a[14:15], v[48:51]\n"
      "buffer_load_dwordx4 v18, s[12:15], s52 offen offset:1024 lds\n"
      "s_waitcnt lgkmcnt(1)\n"
      "v_mfma_f32_4x4x4_16b_bf16 v[52:55], a[246:247], a[220:221], v[44:47]\n"
      "ds_read_b64_tr_b16 a[234:235], v60 offset:832\n"
      "v_mfma_f32_4x4x4_16b_bf16 v[56:59], a[246:247], a[222:223], v[48:51]\n"
      "buffer_load_dwordx4 v19, s[12:15], s52 offen offset:2048 lds\n"
      "v_mfma_f32_4x4x4_16b_bf16 v[44:47], a[250:251], a[220:221], v[44:47]\n"
      "ds_read_b64_tr_b16 a[16:17], v60 offset:12352\n"
      "v_mfma_f32_4x4x4_16b_bf16 v[48:51], a[250:251], a[222:223], v[48:51]\n"
      "ds_read_b64_tr_b16 a[18:19], v60 offset:13120\n"
      "ds_read_b64_tr_b16 a[228:229], v60 offset:6208\n"
      "ds_read_b64_tr_b16 a[230:231], v60 offset:6976\n"
      "ds_read_b64_tr_b16 a[240:241], v60 offset:128\n"
      "ds_read_b64_tr_b16 a[242:243], v60 offset:896\n"
      "ds_read_b64_tr_b16 a[20:21], v60 offset:12416\n"
      "ds_read_b64_tr_b16 a[22:23], v60 offset:13184\n"
      "ds_read_b64_tr_b16 a[236:237], v60 offset:6272\n"
      "ds_read_b64_tr_b16 a[238:239], v60 offset:7040\n"
      "ds_read_b64_tr_b16 a[164:165], v7 offset:0\n"
      "ds_read_b64_tr_b16 a[166:167], v7 offset:8\n"
      "ds_read_b128 a[204:207], v3 offset:0\n"
      "v_cvt_pk_bf16_f32 v236, v36, v40\n"
      "v_cvt_pk_bf16_f32 v237, v37, v41\n"
      "v_cvt_pk_bf16_f32 v238, v38, v42\n"
      "v_cvt_pk_bf16_f32 v239, v39, v43\n"
      "v_cvt_pk_bf16_f32 v232, v28, v32\n"
      "v_cvt_pk_bf16_f32 v233, v29, v33\n"
      "v_cvt_pk_bf16_f32 v234, v30, v34\n"
      "v_cvt_pk_bf16_f32 v235, v31, v35\n"
      "s_waitcnt vmcnt(6)\n"
      "s_barrier\n"
      "s_add_u32 m0, s63, 116224\n"
      "s_add_u32 s52, s63, 24576\n"
      "s_waitcnt lgkmcnt(7)\n"
      "v_mfma_f32_4x4x4_16b_bf16 v[28:31], a[244:245], a[232:233], 0\n"
      "v_mfma_f32_4x4x4_16b_bf16 v[32:35], a[244:245], a[234:235], 0\n"
      "buffer_load_dwordx4 v17, s[12:15], s52 offen offset:0 lds\n"
      "v_mfma_f32_4x4x4_16b_bf16 v[28:31], a[248:249], a[16:17], v[28:31]\n"
      "ds_read_b64_tr_b16 a[224:225], v60 offset:18432\n"
      "v_mfma_f32_4x4x4_16b_bf16 v[32:35], a[248:249], a[18:19], v[32:35]\n"
      "buffer_load_dwordx4 v18, s[12:15], s52 offen offset:1024 lds\n"
      "v_mfma_f32_4x4x4_16b_bf16 v[36:39], a[246:247], a[228:229], v[28:31]\n"
      "ds_read_b64_tr_b16 a[226:227], v60 offset:19200\n"
      "v_mfma_f32_4x4x4_16b_bf16 v[40:43], a[246:247], a[230:231], v[32:35]\n"
      "buffer_load_dwordx4 v19, s[12:15], s52 offen offset:2048 lds\n"
      "v_mfma_f32_4x4x4_16b_bf16 v[28:31], a[250:251], a[228:229], v[28:31]\n"
      "ds_read_b64_tr_b16 a[24:25], v61 offset:6400\n"
      "v_mfma_f32_4x4x4_16b_bf16 v[32:35], a[250:251], a[230:231], v[32:35]\n"
      "ds_read_b64_tr_b16 a[26:27], v61 offset:7168\n"
      "s_waitcnt lgkmcnt(4)\n"
      "v_mfma_f32_4x4x4_16b_bf16 v[20:23], a[216:217], a[164:165], 0\n"
      "ds_read_b64_tr_b16 a[220:221], v61 offset:256\n"
      "v_mfma_f32_4x4x4_16b_bf16 v[24:27], a[216:217], a[166:167], 0\n"
      "ds_read_b64_tr_b16 a[222:223], v61 offset:1024\n"
      "ds_read_b64_tr_b16 a[160:161], v7 offset:512\n"
      "ds_read_b64_tr_b16 a[162:163], v7 offset:520\n"
      "ds_read_b128 a[208:211], v3 offset:64\n"
      "v_cvt_pk_bf16_f32 v228, v52, v56\n"
      "v_cvt_pk_bf16_f32 v229, v53, v57\n"
      "v_cvt_pk_bf16_f32 v230, v54, v58\n"
      "v_cvt_pk_bf16_f32 v231, v55, v59\n"
      "v_cvt_pk_bf16_f32 v224, v44, v48\n"
      "v_cvt_pk_bf16_f32 v225, v45, v49\n"
      "v_cvt_pk_bf16_f32 v226, v46, v50\n"
      "v_cvt_pk_bf16_f32 v227, v47, v51\n"
      "s_add_u32 m0, s63, 122368\n"
      "s_add_u32 s52, s63, 79872\n"
      "v_mfma_f32_4x4x4_16b_bf16 v[44:47], a[244:245], a[240:241], 0\n"
      "v_mfma_f32_4x4x4_16b_bf16 v[48:51], a[244:245], a[242:243], 0\n"
      "buffer_load_dwordx4 v17, s[12:15], s52 offen offset:0 lds\n"
      "v_mfma_f32_4x4x4_16b_bf16 v[44:47], a[248:249], a[20:21], v[44:47]\n"
      "ds_read_b64_tr_b16 a[232:233], v60 offset:18496\n"
      "v_mfma_f32_4x4x4_16b_bf16 v[48:51], a[248:249], a[22:23], v[48:51]\n"
      "buffer_load_dwordx4 v18, s[12:15], s52 offen offset:1024 lds\n"
      "v_mfma_f32_4x4x4_16b_bf16 v[52:55], a[246:247], a[236:237], v[44:47]\n"
      "ds_read_b64_tr_b16 a[234:235], v60 offset:19264\n"
      "v_mfma_f32_4x4x4_16b_bf16 v[56:59], a[246:247], a[238:239], v[48:51]\n"
      "buffer_load_dwordx4 v19, s[12:15], s52 offen offset:2048 lds\n"
      "v_mfma_f32_4x4x4_16b_bf16 v[44:47], a[250:251], a[236:237], v[44:47]\n"
      "ds_read_b64_tr_b16 a[28:29], v61 offset:6464\n"
      "v_mfma_f32_4x4x4_16b_bf16 v[48:51], a[250:251], a[238:239], v[48:51]\n"
      "ds_read_b64_tr_b16 a[30:31], v61 offset:7232\n"
      "s_nop 0\n"
      "s_waitcnt lgkmcnt(4)\n"
      "v_mfma_f32_4x4x4_16b_bf16 v[100:103], a[216:217], a[160:161], 0\n"
      "ds_read_b64_tr_b16 a[228:229], v61 offset:320\n"
      "v_mfma_f32_4x4x4_16b_bf16 v[104:107], a[216:217], a[162:163], 0\n"
      "ds_read_b64_tr_b16 a[230:231], v61 offset:1088\n"
      "ds_read_b64_tr_b16 a[156:157], v7 offset:1024\n"
      "ds_read_b64_tr_b16 a[158:159], v7 offset:1032\n"
      "v_cvt_pk_bf16_f32 v220, v36, v40\n"
      "v_cvt_pk_bf16_f32 v221, v37, v41\n"
      "v_cvt_pk_bf16_f32 v222, v38, v42\n"
      "v_cvt_pk_bf16_f32 v223, v39, v43\n"
      "v_cvt_pk_bf16_f32 v216, v28, v32\n"
      "v_cvt_pk_bf16_f32 v217, v29, v33\n"
      "v_mfma_f32_16x16x32_bf16 v[64:67], a[0:3], a[204:207], 0\n"
      "v_cvt_pk_bf16_f32 v218, v30, v34\n"
      "v_cvt_pk_bf16_f32 v219, v31, v35\n"
      "v_mfma_f32_16x16x32_bf16 v[68:71], a[4:7], a[204:207], 0\n"
      "v_cvt_pk_bf16_f32 v20, v20, v24\n"
      "v_cvt_pk_bf16_f32 v21, v21, v25\n"
      "v_mfma_f32_16x16x32_bf16 v[72:75], a[8:11], a[204:207], 0\n"
      "s_add_u32 m0, s63, 41216\n"
      "v_cvt_pk_bf16_f32 v22, v22, v26\n"
      "v_cvt_pk_bf16_f32 v23, v23, v27\n"
      "s_add_u32 s52, s63, 30720\n"
      "v_mfma_f32_4x4x4_16b_bf16 v[28:31], a[244:245], a[224:225], 0\n"
      "v_mfma_f32_4x4x4_16b_bf16 v[32:35], a[244:245], a[226:227], 0\n"
      "buffer_load_dwordx4 v17, s[12:15], s52 offen offset:0 lds\n"
      "v_mfma_f32_4x4x4_16b_bf16 v[28:31], a[248:249], a[24:25], v[28:31]\n"
      "ds_read_b64_tr_b16 a[240:241], v60 offset:18560\n"
      "v_mfma_f32_4x4x4_16b_bf16 v[32:35], a[248:249], a[26:27], v[32:35]\n"
      "buffer_load_dwordx4 v18, s[12:15], s52 offen offset:1024 lds\n"
      "v_mfma_f32_4x4x4_16b_bf16 v[36:39], a[246:247], a[220:221], v[28:31]\n"
      "ds_read_b64_tr_b16 a[242:243], v60 offset:19328\n"
      "v_mfma_f32_4x4x4_16b_bf16 v[40:43], a[246:247], a[222:223], v[32:35]\n"
      "buffer_load_dwordx4 v19, s[12:15], s52 offen offset:2048 lds\n"
      "v_mfma_f32_4x4x4_16b_bf16 v[28:31], a[250:251], a[220:221], v[28:31]\n"
      "ds_read_b64_tr_b16 a[32:33], v61 offset:6528\n"
      "v_mfma_f32_4x4x4_16b_bf16 v[32:35], a[250:251], a[222:223], v[32:35]\n"
      "ds_read_b64_tr_b16 a[34:35], v61 offset:7296\n"
      "s_nop 0\n"
      "s_waitcnt lgkmcnt(4)\n"
      "v_mfma_f32_4x4x4_16b_bf16 v[108:111], a[216:217], a[156:157], 0\n"
      "ds_read_b64_tr_b16 a[236:237], v61 offset:384\n"
      "v_mfma_f32_4x4x4_16b_bf16 v[112:115], a[216:217], a[158:159], 0\n"
      "ds_read_b64_tr_b16 a[238:239], v61 offset:1152\n"
      "ds_read_b64_tr_b16 a[152:153], v7 offset:1536\n"
      "ds_read_b64_tr_b16 a[154:155], v7 offset:1544\n"
      "s_mov_b32 m0, s64\n"
      "s_nop 0\n"
      "ds_write_addtid_b32 v20 offset:24832\n"
      "ds_write_addtid_b32 v21 offset:28928\n"
      "ds_write_addtid_b32 v22 offset:33024\n"
      "ds_write_addtid_b32 v23 offset:37120\n"
      "v_cvt_pk_bf16_f32 v212, v52, v56\n"
      "v_cvt_pk_bf16_f32 v213, v53, v57\n"
      "v_cvt_pk_bf16_f32 v214, v54, v58\n"
      "v_cvt_pk_bf16_f32 v215, v55, v59\n"
      "v_cvt_pk_bf16_f32 v208, v44, v48\n"
      "v_cvt_pk_bf16_f32 v209, v45, v49\n"
      "v_mfma_f32_16x16x32_bf16 v[64:67], a[12:15], a[208:211], v[64:67]\n"
      "v_cvt_pk_bf16_f32 v210, v46, v50\n"
      "v_cvt_pk_bf16_f32 v211, v47, v51\n"
      "v_mfma_f32_16x16x32_bf16 v[68:71], a[16:19], a[208:211], v[68:71]\n"
      "v_cvt_pk_bf16_f32 v100, v100, v104\n"
      "v_cvt_pk_bf16_f32 v101, v101, v105\n"
      "v_mfma_f32_16x16x32_bf16 v[72:75], a[20:23], a[208:211], v[72:75]\n"
      "s_waitcnt vmcnt(9)\n"
      "v_cvt_pk_bf16_f32 v102, v102, v106\n"
      "v_cvt_pk_bf16_f32 v103, v103, v107\n"
      "s_barrier\n"
      "s_add_u32 m0, s63, 47360\n"
      "s_add_u32 s52, s63, 86016\n"
      "v_mfma_f32_4x4x4_16b_bf16 v[44:47], a[244:245], a[232:233], 0\n"
      "v_mfma_f32_4x4x4_16b_bf16 v[48:51], a[244:245], a[234:235], 0\n"
      "buffer_load_dwordx4 v17, s[12:15], s52 offen offset:0 lds\n"
      "v_mfma_f32_4x4x4_16b_bf16 v[44:47], a[248:249], a[28:29], v[44:47]\n"
      "ds_read_b64_tr_b16 a[224:225], v61 offset:12544\n"
      "v_mfma_f32_4x4x4_16b_bf16 v[48:51], a[248:249], a[30:31], v[48:51]\n"
      "buffer_load_dwordx4 v18, s[12:15], s52 offen offset:1024 lds\n"
      "v_mfma_f32_4x4x4_16b_bf16 v[52:55], a[246:247], a[228:229], v[44:47]\n"
      "ds_read_b64_tr_b16 a[226:227], v61 offset:13312\n"
      "v_mfma_f32_4x4x4_16b_bf16 v[56:59], a[246:247], a[230:231], v[48:51]\n"
      "buffer_load_dwordx4 v19, s[12:15], s52 offen offset:2048 lds\n"
      "v_mfma_f32_4x4x4_16b_bf16 v[44:47], a[250:251], a[228:229], v[44:47]\n"
      "ds_read_b64_tr_b16 a[36:37], v62 offset:39680\n"
      "v_mfma_f32_4x4x4_16b_bf16 v[48:51], a[250:251], a[230:231], v[48:51]\n"
      "ds_read_b64_tr_b16 a[38:39], v62 offset:40448\n"
      "s_waitcnt lgkmcnt(4)\n"
      "v_mfma_f32_4x4x4_16b_bf16 v[116:119], a[216:217], a[152:153], 0\n"
      "ds_read_b64_tr_b16 a[220:221], v61 offset:18688\n"
      "v_mfma_f32_4x4x4_16b_bf16 v[120:123], a[216:217], a[154:155], 0\n"
      "ds_read_b64_tr_b16 a[222:223], v61 offset:19456\n"
      "s_mov_b32 m0, s64\n"
      "s_nop 0\n"
      "ds_write_addtid_b32 v100 offset:25344\n"
      "ds_write_addtid_b32 v101 offset:29440\n"
      "ds_write_addtid_b32 v102 offset:33536\n"
      "ds_write_addtid_b32 v103 offset:37632\n"
      "ds_read_b128 a[212:215], v3 offset:128\n"
      "v_cvt_pk_bf16_f32 v204, v36, v40\n"
      "v_cvt_pk_bf16_f32 v205, v37, v41\n"
      "v_cvt_pk_bf16_f32 v206, v38, v42\n"
      "v_cvt_pk_bf16_f32 v207, v39, v43\n"
      "v_cvt_pk_bf16_f32 v200, v28, v32\n"
      "v_cvt_pk_bf16_f32 v201, v29, v33\n"
      "v_cvt_pk_bf16_f32 v202, v30, v34\n"
      "v_cvt_pk_bf16_f32 v203, v31, v35\n"
      "v_cvt_pk_bf16_f32 v108, v108, v112\n"
      "v_cvt_pk_bf16_f32 v109, v109, v113\n"
      "v_cvt_pk_bf16_f32 v110, v110, v114\n"
      "v_cvt_pk_bf16_f32 v111, v111, v115\n"
      "s_add_u32 m0, s63, 0\n"
      "s_add_u32 s52, s63, 36864\n"
      "v_mfma_f32_4x4x4_16b_bf16 v[28:31], a[244:245], a[240:241], 0\n"
      "v_mfma_f32_4x4x4_16b_bf16 v[32:35], a[244:245], a[242:243], 0\n"
      "buffer_load_dwordx4 v17, s[12:15], s52 offen offset:0 lds\n"
      "v_mfma_f32_4x4x4_16b_bf16 v[28:31], a[248:249], a[32:33], v[28:31]\n"
      "ds_read_b64_tr_b16 a[232:233], v61 offset:12608\n"
      "v_mfma_f32_4x4x4_16b_bf16 v[32:35], a[248:249], a[34:35], v[32:35]\n"
      "buffer_load_dwordx4 v18, s[12:15], s52 offen offset:1024 lds\n"
      "v_mfma_f32_4x4x4_16b_bf16 v[36:39], a[246:247], a[236:237], v[28:31]\n"
      "ds_read_b64_tr_b16 a[234:235], v61 offset:13376\n"
      "v_mfma_f32_4x4x4_16b_bf16 v[40:43], a[246:247], a[238:239], v[32:35]\n"
      "ds_read_b64_tr_b16 a[40:41], v62 offset:39744\n"
      "v_mfma_f32_4x4x4_16b_bf16 v[28:31], a[250:251], a[236:237], v[28:31]\n"
      "ds_read_b64_tr_b16 a[42:43], v62 offset:40512\n"
      "v_mfma_f32_4x4x4_16b_bf16 v[32:35], a[250:251], a[238:239], v[32:35]\n"
      "ds_read_b64_tr_b16 a[228:229], v61 offset:18752\n"
      "ds_read_b64_tr_b16 a[230:231], v61 offset:19520\n"
      "s_mov_b32 m0, s64\n"
      "s_nop 0\n"
      "ds_write_addtid_b32 v108 offset:25856\n"
      "ds_write_addtid_b32 v109 offset:29952\n"
      "ds_write_addtid_b32 v110 offset:34048\n"
      "ds_write_addtid_b32 v111 offset:38144\n"
      "v_cvt_pk_bf16_f32 v196, v52, v56\n"
      "v_cvt_pk_bf16_f32 v197, v53, v57\n"
      "v_cvt_pk_bf16_f32 v198, v54, v58\n"
      "v_cvt_pk_bf16_f32 v199, v55, v59\n"
      "v_cvt_pk_bf16_f32 v192, v44, v48\n"
      "v_cvt_pk_bf16_f32 v193, v45, v49\n"
      "v_cvt_pk_bf16_f32 v194, v46, v50\n"
      "v_cvt_pk_bf16_f32 v195, v47, v51\n"
      "v_cvt_pk_bf16_f32 v116, v116, v120\n"
      "v_cvt_pk_bf16_f32 v117, v117, v121\n"
      "v_cvt_pk_bf16_f32 v118, v118, v122\n"
      "v_cvt_pk_bf16_f32 v119, v119, v123\n"
      "s_waitcnt lgkmcnt(4)\n"
      "v_mfma_f32_4x4x4_16b_bf16 v[44:47], a[244:245], a[224:225], 0\n"
      "v_mfma_f32_4x4x4_16b_bf16 v[48:51], a[244:245], a[226:227], 0\n"
      "s_add_u32 m0, s63, 0\n"
      "s_nop 0\n"
      "s_nop 0\n"
      "buffer_load_dwordx4 v19, s[12:15], s52 offen offset:2048 lds\n"
      "v_mfma_f32_4x4x4_16b_bf16 v[44:47], a[248:249], a[36:37], v[44:47]\n"
      "ds_read_b64_tr_b16 a[240:241], v61 offset:12672\n"
      "v_mfma_f32_4x4x4_16b_bf16 v[48:51], a[248:249], a[38:39], v[48:51]\n"
      "s_add_u32 m0, s63, 6144\n"
      "v_mfma_f32_4x4x4_16b_bf16 v[52:55], a[246:247], a[220:221], v[44:47]\n"
      "ds_read_b64_tr_b16 a[242:243], v61 offset:13440\n"
      "v_mfma_f32_4x4x4_16b_bf16 v[56:59], a[246:247], a[222:223], v[48:51]\n"
      "s_add_u32 s52, s63, 92160\n"
      "v_mfma_f32_4x4x4_16b_bf16 v[44:47], a[250:251], a[220:221], v[44:47]\n"
      "ds_read_b64_tr_b16 a[44:45], v62 offset:39808\n"
      "v_mfma_f32_4x4x4_16b_bf16 v[48:51], a[250:251], a[222:223], v[48:51]\n"
      "buffer_load_dwordx4 v17, s[12:15], s52 offen offset:0 lds\n"
      "ds_read_b64_tr_b16 a[46:47], v62 offset:40576\n"
      "ds_read_b64_tr_b16 a[236:237], v61 offset:18816\n"
      "ds_read_b64_tr_b16 a[238:239], v61 offset:19584\n"
      "s_mov_b32 m0, s64\n"
      "s_nop 0\n"
      "ds_write_addtid_b32 v116 offset:26368\n"
      "ds_write_addtid_b32 v117 offset:30464\n"
      "ds_write_addtid_b32 v118 offset:34560\n"
      "ds_write_addtid_b32 v119 offset:38656\n"
      "v_cvt_pk_bf16_f32 v188, v36, v40\n"
      "v_cvt_pk_bf16_f32 v189, v37, v41\n"
      "v_mfma_f32_16x16x32_bf16 v[64:67], a[24:27], a[212:215], v[64:67]\n"
      "v_cvt_pk_bf16_f32 v190, v38, v42\n"
      "v_cvt_pk_bf16_f32 v191, v39, v43\n"
      "v_mfma_f32_16x16x32_bf16 v[68:71], a[28:31], a[212:215], v[68:71]\n"
      "s_waitcnt vmcnt(10)\n"
      "v_cvt_pk_bf16_f32 v184, v28, v32\n"
      "v_cvt_pk_bf16_f32 v185, v29, v33\n"
      "v_mfma_f32_16x16x32_bf16 v[72:75], a[32:35], a[212:215], v[72:75]\n"
      "s_waitcnt lgkmcnt(0)\n"
      "v_cvt_pk_bf16_f32 v186, v30, v34\n"
      "v_cvt_pk_bf16_f32 v187, v31, v35\n"
      "s_barrier\n"
      "v_mfma_f32_4x4x4_16b_bf16 v[28:31], a[244:245], a[232:233], 0\n"
      "v_mfma_f32_4x4x4_16b_bf16 v[32:35], a[244:245], a[234:235], 0\n"
      "s_add_u32 m0, s63, 6144\n"
      "s_nop 0\n"
      "buffer_load_dwordx4 v18, s[12:15], s52 offen offset:1024 lds\n"
      "v_mfma_f32_4x4x4_16b_bf16 v[28:31], a[248:249], a[40:41], v[28:31]\n"
      "ds_read_b64_tr_b16 a[224:225], v62 offset:1536\n"
      "v_mfma_f32_4x4x4_16b_bf16 v[32:35], a[248:249], a[42:43], v[32:35]\n"
      "buffer_load_dwordx4 v19, s[12:15], s52 offen offset:2048 lds\n"
      "v_mfma_f32_4x4x4_16b_bf16 v[36:39], a[246:247], a[228:229], v[28:31]\n"
      "ds_read_b64_tr_b16 a[226:227], v62 offset:2304\n"
      "v_mfma_f32_4x4x4_16b_bf16 v[40:43], a[246:247], a[230:231], v[32:35]\n"
      "ds_read_b64_tr_b16 a[48:49], v62 offset:13824\n"
      "v_mfma_f32_4x4x4_16b_bf16 v[28:31], a[250:251], a[228:229], v[28:31]\n"
      "ds_read_b64_tr_b16 a[50:51], v62 offset:14592\n"
      "v_mfma_f32_4x4x4_16b_bf16 v[32:35], a[250:251], a[230:231], v[32:35]\n"
      "ds_read_b64_tr_b16 a[220:221], v62 offset:7680\n"
      "ds_read_b64_tr_b16 a[222:223], v62 offset:8448\n"
      "ds_read_b128 a[192:195], v4 offset:0\n"
      "ds_read_b128 a[196:199], v0 offset:0\n"
      "ds_read_b128 a[200:203], v0 offset:4096\n"
      "v_cvt_pk_bf16_f32 v180, v52, v56\n"
      "v_cvt_pk_bf16_f32 v181, v53, v57\n"
      "v_cvt_pk_bf16_f32 v182, v54, v58\n"
      "v_cvt_pk_bf16_f32 v183, v55, v59\n"
      "v_cvt_pk_bf16_f32 v176, v44, v48\n"
      "v_cvt_pk_bf16_f32 v177, v45, v49\n"
      "v_cvt_pk_bf16_f32 v178, v46, v50\n"
      "v_cvt_pk_bf16_f32 v179, v47, v51\n"
      "s_add_u32 m0, s63, 18432\n"
      "s_add_u32 s52, s63, 43008\n"
      "v_mfma_f32_4x4x4_16b_bf16 v[44:47], a[244:245], a[240:241], 0\n"
      "v_mfma_f32_4x4x4_16b_bf16 v[48:51], a[244:245], a[242:243], 0\n"
      "buffer_load_dwordx4 v17, s[12:15], s52 offen offset:0 lds\n"
      "v_mfma_f32_4x4x4_16b_bf16 v[44:47], a[248:249], a[44:45], v[44:47]\n"
      "ds_read_b64_tr_b16 a[232:233], v62 offset:1600\n"
      "v_mfma_f32_4x4x4_16b_bf16 v[48:51], a[248:249], a[46:47], v[48:51]\n"
      "buffer_load_dwordx4 v18, s[12:15], s52 offen offset:1024 lds\n"
      "v_mfma_f32_4x4x4_16b_bf16 v[52:55], a[246:247], a[236:237], v[44:47]\n"
      "ds_read_b64_tr_b16 a[234:235], v62 offset:2368\n"
      "v_mfma_f32_4x4x4_16b_bf16 v[56:59], a[246:247], a[238:239], v[48:51]\n"
      "ds_read_b64_tr_b16 a[52:53], v62 offset:13888\n"
      "v_mfma_f32_4x4x4_16b_bf16 v[44:47], a[250:251], a[236:237], v[44:47]\n"
      "ds_read_b64_tr_b16 a[54:55], v62 offset:14656\n"
      "v_mfma_f32_4x4x4_16b_bf16 v[48:51], a[250:251], a[238:239], v[48:51]\n"
      "ds_read_b64_tr_b16 a[228:229], v62 offset:7744\n"
      "ds_read_b64_tr_b16 a[230:231], v62 offset:8512\n"
      "ds_read_b128 a[204:207], v0 offset:64\n"
      "ds_read_b128 a[208:211], v0 offset:4160\n"
      "ds_read_b128 a[212:215], v0 offset:128\n"
      "v_cvt_pk_bf16_f32 v172, v36, v40\n"
      "v_cvt_pk_bf16_f32 v173, v37, v41\n"
      "v_cvt_pk_bf16_f32 v174, v38, v42\n"
      "v_cvt_pk_bf16_f32 v175, v39, v43\n"
      "v_cvt_pk_bf16_f32 v168, v28, v32\n"
      "v_cvt_pk_bf16_f32 v169, v29, v33\n"
      "v_cvt_pk_bf16_f32 v170, v30, v34\n"
      "v_cvt_pk_bf16_f32 v171, v31, v35\n"
      "s_nop 0\n"
      "s_waitcnt lgkmcnt(6)\n"
      "v_mfma_f32_4x4x4_16b_bf16 v[28:31], a[244:245], a[224:225], 0\n"
      "v_mfma_f32_4x4x4_16b_bf16 v[32:35], a[244:245], a[226:227], 0\n"
      "buffer_load_dwordx4 v19, s[12:15], s52 offen offset:2048 lds\n"
      "v_mfma_f32_4x4x4_16b_bf16 v[28:31], a[248:249], a[48:49], v[28:31]\n"
      "ds_read_b64_tr_b16 a[240:241], v62 offset:1664\n"
      "v_mfma_f32_4x4x4_16b_bf16 v[32:35], a[248:249], a[50:51], v[32:35]\n"
      "s_add_u32 m0, s63, 57600\n"
      "v_mfma_f32_4x4x4_16b_bf16 v[36:39], a[246:247], a[220:221], v[28:31]\n"
      "ds_read_b64_tr_b16 a[242:243], v62 offset:2432\n"
      "v_mfma_f32_4x4x4_16b_bf16 v[40:43], a[246:247], a[222:223], v[32:35]\n"
      "s_add_u32 s52, s63, 98304\n"
      "v_mfma_f32_4x4x4_16b_bf16 v[28:31], a[250:251], a[220:221], v[28:31]\n"
      "ds_read_b64_tr_b16 a[56:57], v62 offset:13952\n"
      "v_mfma_f32_4x4x4_16b_bf16 v[32:35], a[250:251], a[222:223], v[32:35]\n"
      "buffer_load_dwordx4 v17, s[12:15], s52 offen offset:0 lds\n"
      "ds_read_b64_tr_b16 a[58:59], v62 offset:14720\n"
      "ds_read_b64_tr_b16 a[236:237], v62 offset:7808\n"
      "ds_read_b64_tr_b16 a[238:239], v62 offset:8576\n"
      "v_mfma_f32_16x16x32_bf16 v[64:67], a[36:39], a[192:195], v[64:67]\n"
      "v_cvt_pk_bf16_f32 v164, v52, v56\n"
      "v_cvt_pk_bf16_f32 v165, v53, v57\n"
      "v_mfma_f32_16x16x32_bf16 v[68:71], a[40:43], a[192:195], v[68:71]\n"
      "v_cvt_pk_bf16_f32 v166, v54, v58\n"
      "v_cvt_pk_bf16_f32 v167, v55, v59\n"
      "v_mfma_f32_16x16x32_bf16 v[72:75], a[44:47], a[192:195], v[72:75]\n"
      "s_nop 0\n"
      "v_cvt_pk_bf16_f32 v160, v44, v48\n"
      "v_cvt_pk_bf16_f32 v161, v45, v49\n"
      "v_mfma_f32_16x16x32_bf16 v[76:79], v[252:255], a[196:199], 0\n"
      "s_waitcnt vmcnt(10)\n"
      "v_cvt_pk_bf16_f32 v162, v46, v50\n"
      "v_cvt_pk_bf16_f32 v163, v47, v51\n"
      "s_barrier\n"
      "v_mfma_f32_4x4x4_16b_bf16 v[44:47], a[244:245], a[232:233], 0\n"
      "v_mfma_f32_4x4x4_16b_bf16 v[48:51], a[244:245], a[234:235], 0\n"
      "buffer_load_dwordx4 v18, s[12:15], s52 offen offset:1024 lds\n"
      "s_waitcnt lgkmcnt(0)\n"
      "v_mfma_f32_4x4x4_16b_bf16 v[44:47], a[248:249], a[52:53], v[44:47]\n"
      "ds_read_b64_tr_b16 a[224:225], v60 offset:41216\n"
      "v_mfma_f32_4x4x4_16b_bf16 v[48:51], a[248:249], a[54:55], v[48:51]\n"
      "buffer_load_dwordx4 v19, s[12:15], s52 offen offset:2048 lds\n"
      "v_mfma_f32_4x4x4_16b_bf16 v[52:55], a[246:247], a[228:229], v[44:47]\n"
      "ds_read_b64_tr_b16 a[226:227], v60 offset:41984\n"
      "v_mfma_f32_4x4x4_16b_bf16 v[56:59], a[246:247], a[230:231], v[48:51]\n"
      "ds_read_b64_tr_b16 a[60:61], v62 offset:19968\n"
      "v_mfma_f32_4x4x4_16b_bf16 v[44:47], a[250:251], a[228:229], v[44:47]\n"
      "ds_read_b64_tr_b16 a[62:63], v62 offset:20736\n"
      "v_mfma_f32_4x4x4_16b_bf16 v[48:51], a[250:251], a[230:231], v[48:51]\n"
      "ds_read_b64_tr_b16 a[220:221], v60 offset:47360\n"
      "ds_read_b64_tr_b16 a[222:223], v60 offset:48128\n"
      "ds_read_b128 a[192:195], v0 offset:4224\n"
      "v_mfma_f32_16x16x32_bf16 v[80:83], v[244:247], a[196:199], 0\n"
      "v_cvt_pk_bf16_f32 v156, v36, v40\n"
      "v_cvt_pk_bf16_f32 v157, v37, v41\n"
      "v_mfma_f32_16x16x32_bf16 v[84:87], v[236:239], a[196:199], 0\n"
      "v_cvt_pk_bf16_f32 v158, v38, v42\n"
      "v_cvt_pk_bf16_f32 v159, v39, v43\n"
      "v_mfma_f32_16x16x32_bf16 v[88:91], v[248:251], a[200:203], 0\n"
      "v_cvt_pk_bf16_f32 v152, v28, v32\n"
      "v_cvt_pk_bf16_f32 v153, v29, v33\n"
      "v_mfma_f32_16x16x32_bf16 v[92:95], v[240:243], a[200:203], 0\n"
      "s_add_u32 m0, s63, 69888\n"
      "v_cvt_pk_bf16_f32 v154, v30, v34\n"
      "v_cvt_pk_bf16_f32 v155, v31, v35\n"
      "s_add_u32 s52, s63, 49152\n"
      "v_mfma_f32_4x4x4_16b_bf16 v[28:31], a[244:245], a[240:241], 0\n"
      "v_mfma_f32_4x4x4_16b_bf16 v[32:35], a[244:245], a[242:243], 0\n"
      "buffer_load_dwordx4 v17, s[12:15], s52 offen offset:0 lds\n"
      "v_mfma_f32_4x4x4_16b_bf16 v[28:31], a[248:249], a[56:57], v[28:31]\n"
      "ds_read_b64_tr_b16 a[232:233], v60 offset:41280\n"
      "v_mfma_f32_4x4x4_16b_bf16 v[32:35], a[248:249], a[58:59], v[32:35]\n"
      "buffer_load_dwordx4 v18, s[12:15], s52 offen offset:1024 lds\n"
      "v_mfma_f32_4x4x4_16b_bf16 v[36:39], a[246:247], a[236:237], v[28:31]\n"
      "ds_read_b64_tr_b16 a[234:235], v60 offset:42048\n"
      "v_mfma_f32_4x4x4_16b_bf16 v[40:43], a[246:247], a[238:239], v[32:35]\n"
      "ds_read_b64_tr_b16 a[64:65], v62 offset:20032\n"
      "v_mfma_f32_4x4x4_16b_bf16 v[28:31], a[250:251], a[236:237], v[28:31]\n"
      "ds_read_b64_tr_b16 a[66:67], v62 offset:20800\n"
      "v_mfma_f32_4x4x4_16b_bf16 v[32:35], a[250:251], a[238:239], v[32:35]\n"
      "ds_read_b64_tr_b16 a[228:229], v60 offset:47424\n"
      "ds_read_b64_tr_b16 a[230:231], v60 offset:48192\n"
      "ds_read_b128 a[196:199], v1 offset:0\n"
      "v_mfma_f32_16x16x32_bf16 v[96:99], v[232:235], a[200:203], 0\n"
      "v_cvt_pk_bf16_f32 v148, v52, v56\n"
      "v_cvt_pk_bf16_f32 v149, v53, v57\n"
      "v_mfma_f32_16x16x32_bf16 v[76:79], v[228:231], a[204:207], v[76:79]\n"
      "v_cvt_pk_bf16_f32 v150, v54, v58\n"
      "v_cvt_pk_bf16_f32 v151, v55, v59\n"
      "v_mfma_f32_16x16x32_bf16 v[80:83], v[220:223], a[204:207], v[80:83]\n"
      "s_nop 0\n"
      "v_cvt_pk_bf16_f32 v144, v44, v48\n"
      "v_cvt_pk_bf16_f32 v145, v45, v49\n"
      "v_mfma_f32_16x16x32_bf16 v[84:87], v[212:215], a[204:207], v[84:87]\n"
      "s_waitcnt lgkmcnt(2)\n"
      "v_cvt_pk_bf16_f32 v146, v46, v50\n"
      "v_cvt_pk_bf16_f32 v147, v47, v51\n"
      "v_mfma_f32_4x4x4_16b_bf16 v[44:47], a[244:245], a[224:225], 0\n"
      "v_mfma_f32_4x4x4_16b_bf16 v[48:51], a[244:245], a[226:227], 0\n"
      "buffer_load_dwordx4 v19, s[12:15], s52 offen offset:2048 lds\n"
      "v_mfma_f32_4x4x4_16b_bf16 v[44:47], a[248:249], a[60:61], v[44:47]\n"
      "ds_read_b64_tr_b16 a[240:241], v60 offset:41344\n"
      "v_mfma_f32_4x4x4_16b_bf16 v[48:51], a[248:249], a[62:63], v[48:51]\n"
      "s_add_u32 m0, s63, 76032\n"
      "v_mfma_f32_4x4x4_16b_bf16 v[52:55], a[246:247], a[220:221], v[44:47]\n"
      "ds_read_b64_tr_b16 a[242:243], v60 offset:42112\n"
      "v_mfma_f32_4x4x4_16b_bf16 v[56:59], a[246:247], a[222:223], v[48:51]\n"
      "s_add_u32 s52, s63, 104448\n"
      "v_mfma_f32_4x4x4_16b_bf16 v[44:47], a[250:251], a[220:221], v[44:47]\n"
      "ds_read_b64_tr_b16 a[68:69], v62 offset:20096\n"
      "v_mfma_f32_4x4x4_16b_bf16 v[48:51], a[250:251], a[222:223], v[48:51]\n"
      "buffer_load_dwordx4 v17, s[12:15], s52 offen offset:0 lds\n"
      "ds_read_b64_tr_b16 a[70:71], v62 offset:20864\n"
      "ds_read_b64_tr_b16 a[236:237], v60 offset:47488\n"
      "ds_read_b64_tr_b16 a[238:239], v60 offset:48256\n"
      "ds_read_b128 a[200:203], v1 offset:4096\n"
      "ds_read_b128 a[204:207], v4 offset:64\n"
      "v_mfma_f32_16x16x32_bf16 v[88:91], v[224:227], a[208:211], v[88:91]\n"
      "v_cvt_pk_bf16_f32 v140, v36, v40\n"
      "v_cvt_pk_bf16_f32 v141, v37, v41\n"
      "v_mfma_f32_16x16x32_bf16 v[92:95], v[216:219], a[208:211], v[92:95]\n"
      "v_cvt_pk_bf16_f32 v142, v38, v42\n"
      "v_cvt_pk_bf16_f32 v143, v39, v43\n"
      "v_mfma_f32_16x16x32_bf16 v[96:99], v[208:211], a[208:211], v[96:99]\n"
      "s_nop 0\n"
      "v_cvt_pk_bf16_f32 v136, v28, v32\n"
      "v_cvt_pk_bf16_f32 v137, v29, v33\n"
      "v_mfma_f32_16x16x32_bf16 v[76:79], v[204:207], a[212:215], v[76:79]\n"
      "s_waitcnt vmcnt(10)\n"
      "v_cvt_pk_bf16_f32 v138, v30, v34\n"
      "v_cvt_pk_bf16_f32 v139, v31, v35\n"
      "s_barrier\n"
      "v_mfma_f32_4x4x4_16b_bf16 v[28:31], a[244:245], a[232:233], 0\n"
      "v_mfma_f32_4x4x4_16b_bf16 v[32:35], a[244:245], a[234:235], 0\n"
      "buffer_load_dwordx4 v18, s[12:15], s52 offen offset:1024 lds\n"
      "v_mfma_f32_4x4x4_16b_bf16 v[28:31], a[248:249], a[64:65], v[28:31]\n"
      "ds_read_b64_tr_b16 a[224:225], v60 offset:0\n"
      "v_mfma_f32_4x4x4_16b_bf16 v[32:35], a[248:249], a[66:67], v[32:35]\n"
      "buffer_load_dwordx4 v19, s[12:15], s52 offen offset:2048 lds\n"
      "s_waitcnt lgkmcnt(1)\n"
      "v_mfma_f32_4x4x4_16b_bf16 v[36:39], a[246:247], a[228:229], v[28:31]\n"
      "ds_read_b64_tr_b16 a[226:227], v60 offset:768\n"
      "v_mfma_f32_4x4x4_16b_bf16 v[40:43], a[246:247], a[230:231], v[32:35]\n"
      "ds_read_b64_tr_b16 a[72:73], v60 offset:12288\n"
      "v_mfma_f32_4x4x4_16b_bf16 v[28:31], a[250:251], a[228:229], v[28:31]\n"
      "ds_read_b64_tr_b16 a[74:75], v60 offset:13056\n"
      "v_mfma_f32_4x4x4_16b_bf16 v[32:35], a[250:251], a[230:231], v[32:35]\n"
      "ds_read_b64_tr_b16 a[220:221], v60 offset:6144\n"
      "ds_read_b64_tr_b16 a[222:223], v60 offset:6912\n"
      "ds_read_b128 a[208:211], v1 offset:64\n"
      "v_mfma_f32_16x16x32_bf16 v[80:83], v[196:199], a[212:215], v[80:83]\n"
      "v_cvt_pk_bf16_f32 v132, v52, v56\n"
      "v_cvt_pk_bf16_f32 v133, v53, v57\n"
      "v_mfma_f32_16x16x32_bf16 v[84:87], v[188:191], a[212:215], v[84:87]\n"
      "v_cvt_pk_bf16_f32 v134, v54, v58\n"
      "v_cvt_pk_bf16_f32 v135, v55, v59\n"
      "v_mfma_f32_16x16x32_bf16 v[88:91], v[200:203], a[192:195], v[88:91]\n"
      "v_cvt_pk_bf16_f32 v128, v44, v48\n"
      "v_cvt_pk_bf16_f32 v129, v45, v49\n"
      "v_mfma_f32_16x16x32_bf16 v[92:95], v[192:195], a[192:195], v[92:95]\n"
      "v_cvt_pk_bf16_f32 v130, v46, v50\n"
      "v_cvt_pk_bf16_f32 v131, v47, v51\n"
      "v_mfma_f32_4x4x4_16b_bf16 v[44:47], a[244:245], a[240:241], 0\n"
      "v_mfma_f32_4x4x4_16b_bf16 v[48:51], a[244:245], a[242:243], 0\n"
      "ds_read_b64_tr_b16 a[232:233], v60 offset:64\n"
      "v_mfma_f32_4x4x4_16b_bf16 v[44:47], a[248:249], a[68:69], v[44:47]\n"
      "ds_read_b64_tr_b16 a[234:235], v60 offset:832\n"
      "v_mfma_f32_4x4x4_16b_bf16 v[48:51], a[248:249], a[70:71], v[48:51]\n"
      "ds_read_b64_tr_b16 a[76:77], v60 offset:12352\n"
      "v_mfma_f32_4x4x4_16b_bf16 v[52:55], a[246:247], a[236:237], v[44:47]\n"
      "ds_read_b64_tr_b16 a[78:79], v60 offset:13120\n"
      "v_mfma_f32_4x4x4_16b_bf16 v[56:59], a[246:247], a[238:239], v[48:51]\n"
      "ds_read_b64_tr_b16 a[228:229], v60 offset:6208\n"
      "v_mfma_f32_4x4x4_16b_bf16 v[44:47], a[250:251], a[236:237], v[44:47]\n"
      "ds_read_b64_tr_b16 a[230:231], v60 offset:6976\n"
      "v_mfma_f32_4x4x4_16b_bf16 v[48:51], a[250:251], a[238:239], v[48:51]\n"
      "ds_read_b128 a[212:215], v1 offset:4160\n"
      "v_mfma_f32_16x16x32_bf16 v[96:99], v[184:187], a[192:195], v[96:99]\n"
      "v_cvt_pk_bf16_f32 v124, v36, v40\n"
      "v_cvt_pk_bf16_f32 v125, v37, v41\n"
      "v_mfma_f32_16x16x32_bf16 v[76:79], v[180:183], a[196:199], v[76:79]\n"
      "v_cvt_pk_bf16_f32 v126, v38, v42\n"
      "v_cvt_pk_bf16_f32 v127, v39, v43\n"
      "v_mfma_f32_16x16x32_bf16 v[80:83], v[172:175], a[196:199], v[80:83]\n"
      "s_nop 0\n"
      "v_cvt_pk_bf16_f32 v120, v28, v32\n"
      "v_cvt_pk_bf16_f32 v121, v29, v33\n"
      "v_mfma_f32_16x16x32_bf16 v[84:87], v[164:167], a[196:199], v[84:87]\n"
      "s_waitcnt lgkmcnt(1)\n"
      "v_cvt_pk_bf16_f32 v122, v30, v34\n"
      "v_cvt_pk_bf16_f32 v123, v31, v35\n"
      "v_mfma_f32_4x4x4_16b_bf16 v[28:31], a[244:245], a[224:225], 0\n"
      "v_mfma_f32_4x4x4_16b_bf16 v[32:35], a[244:245], a[226:227], 0\n"
      "ds_read_b64_tr_b16 a[240:241], v60 offset:128\n"
      "v_mfma_f32_4x4x4_16b_bf16 v[28:31], a[248:249], a[72:73], v[28:31]\n"
      "ds_read_b64_tr_b16 a[242:243], v60 offset:896\n"
      "v_mfma_f32_4x4x4_16b_bf16 v[32:35], a[248:249], a[74:75], v[32:35]\n"
      "ds_read_b64_tr_b16 a[80:81], v60 offset:12416\n"
      "v_mfma_f32_4x4x4_16b_bf16 v[36:39], a[246:247], a[220:221], v[28:31]\n"
      "ds_read_b64_tr_b16 a[82:83], v60 offset:13184\n"
      "v_mfma_f32_4x4x4_16b_bf16 v[40:43], a[246:247], a[222:223], v[32:35]\n"
      "ds_read_b64_tr_b16 a[236:237], v60 offset:6272\n"
      "v_mfma_f32_4x4x4_16b_bf16 v[28:31], a[250:251], a[220:221], v[28:31]\n"
      "ds_read_b64_tr_b16 a[238:239], v60 offset:7040\n"
      "v_mfma_f32_4x4x4_16b_bf16 v[32:35], a[250:251], a[222:223], v[32:35]\n"
      "ds_read_b128 a[192:195], v4 offset:128\n"
      "ds_read_b128 a[196:199], v1 offset:128\n"
      "v_mfma_f32_16x16x32_bf16 v[88:91], v[176:179], a[200:203], v[88:91]\n"
      "v_cvt_pk_bf16_f32 v116, v52, v56\n"
      "v_cvt_pk_bf16_f32 v117, v53, v57\n"
      "v_mfma_f32_16x16x32_bf16 v[92:95], v[168:171], a[200:203], v[92:95]\n"
      "v_cvt_pk_bf16_f32 v118, v54, v58\n"
      "v_cvt_pk_bf16_f32 v119, v55, v59\n"
      "v_mfma_f32_16x16x32_bf16 v[96:99], v[160:163], a[200:203], v[96:99]\n"
      "v_cvt_pk_bf16_f32 v112, v44, v48\n"
      "v_cvt_pk_bf16_f32 v113, v45, v49\n"
      "v_mfma_f32_16x16x32_bf16 v[64:67], a[48:51], a[204:207], v[64:67]\n"
      "s_waitcnt vmcnt(6)\n"
      "v_cvt_pk_bf16_f32 v114, v46, v50\n"
      "v_cvt_pk_bf16_f32 v115, v47, v51\n"
      "s_barrier\n"
      "v_mfma_f32_4x4x4_16b_bf16 v[44:47], a[244:245], a[232:233], 0\n"
      "v_mfma_f32_4x4x4_16b_bf16 v[48:51], a[244:245], a[234:235], 0\n"
      "ds_read_b64_tr_b16 a[224:225], v60 offset:18432\n"
      "v_mfma_f32_4x4x4_16b_bf16 v[44:47], a[248:249], a[76:77], v[44:47]\n"
      "ds_read_b64_tr_b16 a[226:227], v60 offset:19200\n"
      "v_mfma_f32_4x4x4_16b_bf16 v[48:51], a[248:249], a[78:79], v[48:51]\n"
      "ds_read_b64_tr_b16 a[84:85], v61 offset:6400\n"
      "v_mfma_f32_4x4x4_16b_bf16 v[52:55], a[246:247], a[228:229], v[44:47]\n"
      "ds_read_b64_tr_b16 a[86:87], v61 offset:7168\n"
      "v_mfma_f32_4x4x4_16b_bf16 v[56:59], a[246:247], a[230:231], v[48:51]\n"
      "ds_read_b64_tr_b16 a[220:221], v61 offset:256\n"
      "v_mfma_f32_4x4x4_16b_bf16 v[44:47], a[250:251], a[228:229], v[44:47]\n"
      "ds_read_b64_tr_b16 a[222:223], v61 offset:1024\n"
      "v_mfma_f32_4x4x4_16b_bf16 v[48:51], a[250:251], a[230:231], v[48:51]\n"
      "ds_read_b128 a[200:203], v1 offset:4224\n"
      "v_mfma_f32_16x16x32_bf16 v[68:71], a[52:55], a[204:207], v[68:71]\n"
      "v_cvt_pk_bf16_f32 v108, v36, v40\n"
      "v_cvt_pk_bf16_f32 v109, v37, v41\n"
      "v_mfma_f32_16x16x32_bf16 v[72:75], a[56:59], a[204:207], v[72:75]\n"
      "v_cvt_pk_bf16_f32 v110, v38, v42\n"
      "v_cvt_pk_bf16_f32 v111, v39, v43\n"
      "v_mfma_f32_16x16x32_bf16 v[76:79], v[156:159], a[208:211], v[76:79]\n"
      "v_cvt_pk_bf16_f32 v104, v28, v32\n"
      "v_cvt_pk_bf16_f32 v105, v29, v33\n"
      "v_mfma_f32_16x16x32_bf16 v[80:83], v[148:151], a[208:211], v[80:83]\n"
      "s_nop 0\n"
      "s_waitcnt lgkmcnt(1)\n"
      "v_cvt_pk_bf16_f32 v106, v30, v34\n"
      "v_cvt_pk_bf16_f32 v107, v31, v35\n"
      "v_mfma_f32_4x4x4_16b_bf16 v[28:31], a[244:245], a[240:241], 0\n"
      "v_mfma_f32_4x4x4_16b_bf16 v[32:35], a[244:245], a[242:243], 0\n"
      "ds_read_b64_tr_b16 a[232:233], v60 offset:18496\n"
      "v_mfma_f32_4x4x4_16b_bf16 v[28:31], a[248:249], a[80:81], v[28:31]\n"
      "ds_read_b64_tr_b16 a[234:235], v60 offset:19264\n"
      "v_mfma_f32_4x4x4_16b_bf16 v[32:35], a[248:249], a[82:83], v[32:35]\n"
      "ds_read_b64_tr_b16 a[88:89], v61 offset:6464\n"
      "v_mfma_f32_4x4x4_16b_bf16 v[36:39], a[246:247], a[236:237], v[28:31]\n"
      "ds_read_b64_tr_b16 a[90:91], v61 offset:7232\n"
      "v_mfma_f32_4x4x4_16b_bf16 v[40:43], a[246:247], a[238:239], v[32:35]\n"
      "ds_read_b64_tr_b16 a[228:229], v61 offset:320\n"
      "v_mfma_f32_4x4x4_16b_bf16 v[28:31], a[250:251], a[236:237], v[28:31]\n"
      "ds_read_b64_tr_b16 a[230:231], v61 offset:1088\n"
      "v_mfma_f32_4x4x4_16b_bf16 v[32:35], a[250:251], a[238:239], v[32:35]\n"
      "ds_read_b128 a[204:207], v5 offset:0\n"
      "v_mfma_f32_16x16x32_bf16 v[84:87], v[140:143], a[208:211], v[84:87]\n"
      "v_cvt_pk_bf16_f32 v100, v52, v56\n"
      "v_cvt_pk_bf16_f32 v101, v53, v57\n"
      "v_mfma_f32_16x16x32_bf16 v[88:91], v[152:155], a[212:215], v[88:91]\n"
      "v_cvt_pk_bf16_f32 v102, v54, v58\n"
      "v_cvt_pk_bf16_f32 v103, v55, v59\n"
      "v_mfma_f32_16x16x32_bf16 v[92:95], v[144:147], a[212:215], v[92:95]\n"
      "v_cvt_pk_bf16_f32 v44, v44, v48\n"
      "v_cvt_pk_bf16_f32 v45, v45, v49\n"
      "v_mfma_f32_16x16x32_bf16 v[96:99], v[136:139], a[212:215], v[96:99]\n"
      "v_cvt_pk_bf16_f32 v46, v46, v50\n"
      "v_cvt_pk_bf16_f32 v47, v47, v51\n"
      "v_mfma_f32_16x16x32_bf16 v[64:67], a[60:63], a[192:195], v[64:67]\n"
      "v_accvgpr_write_b32 a164, v44\n"
      "v_accvgpr_write_b32 a165, v45\n"
      "v_mfma_f32_16x16x32_bf16 v[68:71], a[64:67], a[192:195], v[68:71]\n"
      "v_accvgpr_write_b32 a166, v46\n"
      "v_accvgpr_write_b32 a167, v47\n"
      "v_mfma_f32_4x4x4_16b_bf16 v[44:47], a[244:245], a[224:225], 0\n"
      "v_mfma_f32_4x4x4_16b_bf16 v[48:51], a[244:245], a[226:227], 0\n"
      "ds_read_b64_tr_b16 a[240:241], v60 offset:18560\n"
      "v_mfma_f32_4x4x4_16b_bf16 v[44:47], a[248:249], a[84:85], v[44:47]\n"
      "ds_read_b64_tr_b16 a[242:243], v60 offset:19328\n"
      "v_mfma_f32_4x4x4_16b_bf16 v[48:51], a[248:249], a[86:87], v[48:51]\n"
      "ds_read_b64_tr_b16 a[92:93], v61 offset:6528\n"
      "v_mfma_f32_4x4x4_16b_bf16 v[52:55], a[246:247], a[220:221], v[44:47]\n"
      "ds_read_b64_tr_b16 a[94:95], v61 offset:7296\n"
      "v_mfma_f32_4x4x4_16b_bf16 v[56:59], a[246:247], a[222:223], v[48:51]\n"
      "ds_read_b64_tr_b16 a[236:237], v61 offset:384\n"
      "v_mfma_f32_4x4x4_16b_bf16 v[44:47], a[250:251], a[220:221], v[44:47]\n"
      "ds_read_b64_tr_b16 a[238:239], v61 offset:1152\n"
      "v_mfma_f32_4x4x4_16b_bf16 v[48:51], a[250:251], a[222:223], v[48:51]\n"
      "ds_read_b128 a[208:211], v2 offset:0\n"
      "ds_read_b128 a[212:215], v2 offset:4096\n"
      "v_cvt_pk_bf16_f32 v36, v36, v40\n"
      "v_cvt_pk_bf16_f32 v37, v37, v41\n"
      "v_cvt_pk_bf16_f32 v38, v38, v42\n"
      "v_cvt_pk_bf16_f32 v39, v39, v43\n"
      "v_mfma_f32_16x16x32_bf16 v[72:75], a[68:71], a[192:195], v[72:75]\n"
      "v_accvgpr_write_b32 a160, v36\n"
      "v_accvgpr_write_b32 a161, v37\n"
      "v_mfma_f32_16x16x32_bf16 v[76:79], v[132:135], a[196:199], v[76:79]\n"
      "v_accvgpr_write_b32 a162, v38\n"
      "v_accvgpr_write_b32 a163, v39\n"
      "v_mfma_f32_16x16x32_bf16 v[80:83], v[124:127], a[196:199], v[80:83]\n"
      "v_cvt_pk_bf16_f32 v28, v28, v32\n"
      "v_cvt_pk_bf16_f32 v29, v29, v33\n"
      "v_mfma_f32_16x16x32_bf16 v[84:87], v[116:119], a[196:199], v[84:87]\n"
      "s_waitcnt lgkmcnt(1)\n"
      "v_cvt_pk_bf16_f32 v30, v30, v34\n"
      "v_cvt_pk_bf16_f32 v31, v31, v35\n"
      "v_mfma_f32_16x16x32_bf16 v[88:91], v[128:131], a[200:203], v[88:91]\n"
      "s_nop 0\n"
      "v_accvgpr_write_b32 a156, v28\n"
      "v_accvgpr_write_b32 a157, v29\n"
      "v_mfma_f32_16x16x32_bf16 v[92:95], v[120:123], a[200:203], v[92:95]\n"
      "s_waitcnt vmcnt(0)\n"
      "v_accvgpr_write_b32 a158, v30\n"
      "v_accvgpr_write_b32 a159, v31\n"
      "s_barrier\n"
      "v_mfma_f32_4x4x4_16b_bf16 v[28:31], a[244:245], a[232:233], 0\n"
      "v_mfma_f32_4x4x4_16b_bf16 v[32:35], a[244:245], a[234:235], 0\n"
      "ds_read_b64_tr_b16 a[224:225], v61 offset:12544\n"
      "v_mfma_f32_4x4x4_16b_bf16 v[28:31], a[248:249], a[88:89], v[28:31]\n"
      "ds_read_b64_tr_b16 a[226:227], v61 offset:13312\n"
      "v_mfma_f32_4x4x4_16b_bf16 v[32:35], a[248:249], a[90:91], v[32:35]\n"
      "ds_read_b64_tr_b16 a[96:97], v62 offset:39680\n"
      "v_mfma_f32_4x4x4_16b_bf16 v[36:39], a[246:247], a[228:229], v[28:31]\n"
      "ds_read_b64_tr_b16 a[98:99], v62 offset:40448\n"
      "v_mfma_f32_4x4x4_16b_bf16 v[40:43], a[246:247], a[230:231], v[32:35]\n"
      "ds_read_b64_tr_b16 a[220:221], v61 offset:18688\n"
      "v_mfma_f32_4x4x4_16b_bf16 v[28:31], a[250:251], a[228:229], v[28:31]\n"
      "ds_read_b64_tr_b16 a[222:223], v61 offset:19456\n"
      "v_mfma_f32_4x4x4_16b_bf16 v[32:35], a[250:251], a[230:231], v[32:35]\n"
      "ds_read_b128 a[192:195], v5 offset:64\n"
      "v_cvt_pk_bf16_f32 v52, v52, v56\n"
      "v_cvt_pk_bf16_f32 v53, v53, v57\n"
      "v_cvt_pk_bf16_f32 v54, v54, v58\n"
      "v_cvt_pk_bf16_f32 v55, v55, v59\n"
      "v_mfma_f32_16x16x32_bf16 v[96:99], v[112:115], a[200:203], v[96:99]\n"
      "v_accvgpr_write_b32 a152, v52\n"
      "v_accvgpr_write_b32 a153, v53\n"
      "v_mfma_f32_16x16x32_bf16 v[64:67], a[72:75], a[204:207], v[64:67]\n"
      "v_accvgpr_write_b32 a154, v54\n"
      "v_accvgpr_write_b32 a155, v55\n"
      "v_mfma_f32_16x16x32_bf16 v[68:71], a[76:79], a[204:207], v[68:71]\n"
      "v_cvt_pk_bf16_f32 v44, v44, v48\n"
      "v_cvt_pk_bf16_f32 v45, v45, v49\n"
      "v_mfma_f32_16x16x32_bf16 v[72:75], a[80:83], a[204:207], v[72:75]\n"
      "v_cvt_pk_bf16_f32 v46, v46, v50\n"
      "v_cvt_pk_bf16_f32 v47, v47, v51\n"
      "v_mfma_f32_16x16x32_bf16 v[76:79], v[108:111], a[208:211], v[76:79]\n"
      "s_nop 0\n"
      "v_accvgpr_write_b32 a148, v44\n"
      "v_accvgpr_write_b32 a149, v45\n"
      "v_mfma_f32_16x16x32_bf16 v[80:83], v[100:103], a[208:211], v[80:83]\n"
      "s_nop 0\n"
      "v_accvgpr_write_b32 a150, v46\n"
      "v_accvgpr_write_b32 a151, v47\n"
      "v_mfma_f32_4x4x4_16b_bf16 v[44:47], a[244:245], a[240:241], 0\n"
      "v_mfma_f32_4x4x4_16b_bf16 v[48:51], a[244:245], a[242:243], 0\n"
      "ds_read_b64_tr_b16 a[232:233], v61 offset:12608\n"
      "v_mfma_f32_4x4x4_16b_bf16 v[44:47], a[248:249], a[92:93], v[44:47]\n"
      "ds_read_b64_tr_b16 a[234:235], v61 offset:13376\n"
      "v_mfma_f32_4x4x4_16b_bf16 v[48:51], a[248:249], a[94:95], v[48:51]\n"
      "ds_read_b64_tr_b16 a[100:101], v62 offset:39744\n"
      "v_mfma_f32_4x4x4_16b_bf16 v[52:55], a[246:247], a[236:237], v[44:47]\n"
      "ds_read_b64_tr_b16 a[102:103], v62 offset:40512\n"
      "v_mfma_f32_4x4x4_16b_bf16 v[56:59], a[246:247], a[238:239], v[48:51]\n"
      "ds_read_b64_tr_b16 a[228:229], v61 offset:18752\n"
      "v_mfma_f32_4x4x4_16b_bf16 v[44:47], a[250:251], a[236:237], v[44:47]\n"
      "ds_read_b64_tr_b16 a[230:231], v61 offset:19520\n"
      "v_mfma_f32_4x4x4_16b_bf16 v[48:51], a[250:251], a[238:239], v[48:51]\n"
      "ds_read_b128 a[196:199], v2 offset:64\n"
      "ds_read_b128 a[200:203], v2 offset:4160\n"
      "v_cvt_pk_bf16_f32 v36, v36, v40\n"
      "v_cvt_pk_bf16_f32 v37, v37, v41\n"
      "v_cvt_pk_bf16_f32 v38, v38, v42\n"
      "v_cvt_pk_bf16_f32 v39, v39, v43\n"
      "v_accvgpr_write_b32 a144, v36\n"
      "v_accvgpr_write_b32 a145, v37\n"
      "v_accvgpr_write_b32 a146, v38\n"
      "v_accvgpr_write_b32 a147, v39\n"
      "v_mfma_f32_16x16x32_bf16 v[84:87], a[160:163], a[208:211], v[84:87]\n"
      "s_waitcnt lgkmcnt(3)\n"
      "v_cvt_pk_bf16_f32 v28, v28, v32\n"
      "v_cvt_pk_bf16_f32 v29, v29, v33\n"
      "v_mfma_f32_16x16x32_bf16 v[88:91], v[104:107], a[212:215], v[88:91]\n"
      "s_nop 0\n"
      "v_cvt_pk_bf16_f32 v30, v30, v34\n"
      "v_cvt_pk_bf16_f32 v31, v31, v35\n"
      "v_mfma_f32_16x16x32_bf16 v[92:95], a[164:167], a[212:215], v[92:95]\n"
      "s_nop 0\n"
      "v_accvgpr_write_b32 a140, v28\n"
      "v_accvgpr_write_b32 a141, v29\n"
      "v_mfma_f32_16x16x32_bf16 v[96:99], a[156:159], a[212:215], v[96:99]\n"
      "s_nop 0\n"
      "v_accvgpr_write_b32 a142, v30\n"
      "v_accvgpr_write_b32 a143, v31\n"
      "v_mfma_f32_4x4x4_16b_bf16 v[28:31], a[244:245], a[224:225], 0\n"
      "v_mfma_f32_4x4x4_16b_bf16 v[32:35], a[244:245], a[226:227], 0\n"
      "ds_read_b64_tr_b16 a[240:241], v61 offset:12672\n"
      "v_mfma_f32_4x4x4_16b_bf16 v[28:31], a[248:249], a[96:97], v[28:31]\n"
      "ds_read_b64_tr_b16 a[242:243], v61 offset:13440\n"
      "v_mfma_f32_4x4x4_16b_bf16 v[32:35], a[248:249], a[98:99], v[32:35]\n"
      "ds_read_b64_tr_b16 a[104:105], v62 offset:39808\n"
      "v_mfma_f32_4x4x4_16b_bf16 v[36:39], a[246:247], a[220:221], v[28:31]\n"
      "ds_read_b64_tr_b16 a[106:107], v62 offset:40576\n"
      "v_mfma_f32_4x4x4_16b_bf16 v[40:43], a[246:247], a[222:223], v[32:35]\n"
      "ds_read_b64_tr_b16 a[236:237], v61 offset:18816\n"
      "v_mfma_f32_4x4x4_16b_bf16 v[28:31], a[250:251], a[220:221], v[28:31]\n"
      "ds_read_b64_tr_b16 a[238:239], v61 offset:19584\n"
      "v_mfma_f32_4x4x4_16b_bf16 v[32:35], a[250:251], a[222:223], v[32:35]\n"
      "v_cvt_pk_bf16_f32 v52, v52, v56\n"
      "v_cvt_pk_bf16_f32 v53, v53, v57\n"
      "v_cvt_pk_bf16_f32 v54, v54, v58\n"
      "v_cvt_pk_bf16_f32 v55, v55, v59\n"
      "v_accvgpr_write_b32 a136, v52\n"
      "v_accvgpr_write_b32 a137, v53\n"
      "v_accvgpr_write_b32 a138, v54\n"
      "v_accvgpr_write_b32 a139, v55\n"
      "v_cvt_pk_bf16_f32 v44, v44, v48\n"
      "v_cvt_pk_bf16_f32 v45, v45, v49\n"
      "v_mfma_f32_16x16x32_bf16 v[64:67], a[84:87], a[192:195], v[64:67]\n"
      "s_nop 0\n"
      "v_cvt_pk_bf16_f32 v46, v46, v50\n"
      "v_cvt_pk_bf16_f32 v47, v47, v51\n"
      "v_mfma_f32_16x16x32_bf16 v[68:71], a[88:91], a[192:195], v[68:71]\n"
      "s_nop 0\n"
      "v_accvgpr_write_b32 a132, v44\n"
      "v_accvgpr_write_b32 a133, v45\n"
      "v_mfma_f32_16x16x32_bf16 v[72:75], a[92:95], a[192:195], v[72:75]\n"
      "s_nop 0\n"
      "v_accvgpr_write_b32 a134, v46\n"
      "v_accvgpr_write_b32 a135, v47\n"
      "v_mfma_f32_4x4x4_16b_bf16 v[44:47], a[244:245], a[232:233], 0\n"
      "v_mfma_f32_4x4x4_16b_bf16 v[48:51], a[244:245], a[234:235], 0\n"
      "ds_read_b128 a[204:207], v5 offset:128\n"
      "v_mfma_f32_4x4x4_16b_bf16 v[44:47], a[248:249], a[100:101], v[44:47]\n"
      "v_mfma_f32_4x4x4_16b_bf16 v[48:51], a[248:249], a[102:103], v[48:51]\n"
      "s_waitcnt lgkmcnt(1)\n"
      "v_mfma_f32_4x4x4_16b_bf16 v[52:55], a[246:247], a[228:229], v[44:47]\n"
      "v_mfma_f32_4x4x4_16b_bf16 v[56:59], a[246:247], a[230:231], v[48:51]\n"
      "v_mfma_f32_4x4x4_16b_bf16 v[44:47], a[250:251], a[228:229], v[44:47]\n"
      "v_mfma_f32_4x4x4_16b_bf16 v[48:51], a[250:251], a[230:231], v[48:51]\n"
      "v_cvt_pk_bf16_f32 v36, v36, v40\n"
      "v_cvt_pk_bf16_f32 v37, v37, v41\n"
      "v_cvt_pk_bf16_f32 v38, v38, v42\n"
      "v_cvt_pk_bf16_f32 v39, v39, v43\n"
      "v_mfma_f32_16x16x32_bf16 v[76:79], a[152:155], a[196:199], v[76:79]\n"
      "v_accvgpr_write_b32 a128, v36\n"
      "v_accvgpr_write_b32 a129, v37\n"
      "v_mfma_f32_16x16x32_bf16 v[80:83], a[144:147], a[196:199], v[80:83]\n"
      "v_accvgpr_write_b32 a130, v38\n"
      "v_accvgpr_write_b32 a131, v39\n"
      "v_mfma_f32_16x16x32_bf16 v[84:87], a[136:139], a[196:199], v[84:87]\n"
      "v_cvt_pk_bf16_f32 v28, v28, v32\n"
      "v_cvt_pk_bf16_f32 v29, v29, v33\n"
      "v_mfma_f32_16x16x32_bf16 v[88:91], a[148:151], a[200:203], v[88:91]\n"
      "v_cvt_pk_bf16_f32 v30, v30, v34\n"
      "v_cvt_pk_bf16_f32 v31, v31, v35\n"
      "v_mfma_f32_16x16x32_bf16 v[92:95], a[140:143], a[200:203], v[92:95]\n"
      "v_accvgpr_write_b32 a124, v28\n"
      "v_accvgpr_write_b32 a125, v29\n"
      "v_mfma_f32_16x16x32_bf16 v[96:99], a[132:135], a[200:203], v[96:99]\n"
      "v_accvgpr_write_b32 a126, v30\n"
      "v_accvgpr_write_b32 a127, v31\n"
      "v_mfma_f32_4x4x4_16b_bf16 v[28:31], a[244:245], a[240:241], 0\n"
      "v_mfma_f32_4x4x4_16b_bf16 v[32:35], a[244:245], a[242:243], 0\n"
      "ds_read_b128 a[208:211], v2 offset:128\n"
      "v_mfma_f32_4x4x4_16b_bf16 v[28:31], a[248:249], a[104:105], v[28:31]\n"
      "ds_read_b128 a[212:215], v2 offset:4224\n"
      "v_mfma_f32_4x4x4_16b_bf16 v[32:35], a[248:249], a[106:107], v[32:35]\n"
      "v_mfma_f32_4x4x4_16b_bf16 v[36:39], a[246:247], a[236:237], v[28:31]\n"
      "s_nop 0\n"
      "s_nop 0\n"
      "v_mfma_f32_4x4x4_16b_bf16 v[40:43], a[246:247], a[238:239], v[32:35]\n"
      "v_mfma_f32_4x4x4_16b_bf16 v[28:31], a[250:251], a[236:237], v[28:31]\n"
      "v_mfma_f32_4x4x4_16b_bf16 v[32:35], a[250:251], a[238:239], v[32:35]\n"
      "v_cvt_pk_bf16_f32 v52, v52, v56\n"
      "v_cvt_pk_bf16_f32 v53, v53, v57\n"
      "v_cvt_pk_bf16_f32 v54, v54, v58\n"
      "v_cvt_pk_bf16_f32 v55, v55, v59\n"
      "v_accvgpr_write_b32 a120, v52\n"
      "v_accvgpr_write_b32 a121, v53\n"
      "v_accvgpr_write_b32 a122, v54\n"
      "v_accvgpr_write_b32 a123, v55\n"
      "v_cvt_pk_bf16_f32 v44, v44, v48\n"
      "v_cvt_pk_bf16_f32 v45, v45, v49\n"
      "v_cvt_pk_bf16_f32 v46, v46, v50\n"
      "v_cvt_pk_bf16_f32 v47, v47, v51\n"
      "v_accvgpr_write_b32 a116, v44\n"
      "v_accvgpr_write_b32 a117, v45\n"
      "v_accvgpr_write_b32 a118, v46\n"
      "v_accvgpr_write_b32 a119, v47\n"
      "s_waitcnt lgkmcnt(0)\n"
      "s_barrier\n"
      "s_mov_b32 m0, s64\n"
      "s_nop 0\n"
      "v_mfma_f32_16x16x32_bf16 v[64:67], a[96:99], a[204:207], v[64:67]\n"
      "ds_read_b128 a[192:195], v3 offset:16896\n"
      "ds_read_b64_tr_b16 a[220:221], v7 offset:16896\n"
      "ds_read_b64_tr_b16 a[222:223], v7 offset:16904\n"
      "v_cvt_pk_bf16_f32 v36, v36, v40\n"
      "v_cvt_pk_bf16_f32 v37, v37, v41\n"
      "v_mfma_f32_16x16x32_bf16 v[68:71], a[100:103], a[204:207], v[68:71]\n"
      "ds_read_b128 a[196:199], v3 offset:16960\n"
      "v_cvt_pk_bf16_f32 v38, v38, v42\n"
      "v_cvt_pk_bf16_f32 v39, v39, v43\n"
      "v_accvgpr_write_b32 a112, v36\n"
      "v_accvgpr_write_b32 a113, v37\n"
      "v_mfma_f32_16x16x32_bf16 v[72:75], a[104:107], a[204:207], v[72:75]\n"
      "v_accvgpr_write_b32 a114, v38\n"
      "v_accvgpr_write_b32 a115, v39\n"
      "v_cvt_pk_bf16_f32 v28, v28, v32\n"
      "v_cvt_pk_bf16_f32 v29, v29, v33\n"
      "v_cvt_pk_bf16_f32 v30, v30, v34\n"
      "v_cvt_pk_bf16_f32 v31, v31, v35\n"
      "v_accvgpr_write_b32 a108, v28\n"
      "v_mfma_f32_16x16x32_bf16 v[76:79], a[128:131], a[208:211], v[76:79]\n"
      "ds_read_b128 a[200:203], v3 offset:17024\n"
      "ds_read_b64_tr_b16 a[224:225], v7 offset:17408\n"
      "ds_read_b64_tr_b16 a[226:227], v7 offset:17416\n"
      "v_accvgpr_write_b32 a109, v29\n"
      "v_accvgpr_write_b32 a110, v30\n"
      "v_accvgpr_write_b32 a111, v31\n"
      "v_mfma_f32_16x16x32_bf16 v[80:83], a[120:123], a[208:211], v[80:83]\n"
      "v_mfma_f32_16x16x32_bf16 v[84:87], a[112:115], a[208:211], v[84:87]\n"
      "s_waitcnt lgkmcnt(3)\n"
      "v_mfma_f32_4x4x4_16b_bf16 v[20:23], a[216:217], a[220:221], 0\n"
      "v_mfma_f32_4x4x4_16b_bf16 v[24:27], a[216:217], a[222:223], 0\n"
      "v_mfma_f32_16x16x32_bf16 v[88:91], a[124:127], a[212:215], v[88:91]\n"
      "s_nop 0\n"
      "ds_read_b128 a[204:207], v4 offset:16896\n"
      "v_mfma_f32_16x16x32_bf16 v[92:95], a[116:119], a[212:215], v[92:95]\n"
      "v_mfma_f32_16x16x32_bf16 v[96:99], a[108:111], a[212:215], v[96:99]\n"
      "ds_read_b64_tr_b16 a[228:229], v7 offset:17920\n"
      "ds_read_b64_tr_b16 a[230:231], v7 offset:17928\n"
      "v_cvt_pk_bf16_f32 v20, v20, v24\n"
      "v_cvt_pk_bf16_f32 v21, v21, v25\n"
      "v_cvt_pk_bf16_f32 v22, v22, v26\n"
      "v_sub_f32 v76, v88, v76\n"
      "v_sub_f32 v77, v89, v77\n"
      "v_cvt_pk_bf16_f32 v23, v23, v27\n"
      "v_mfma_f32_16x16x32_bf16 v[52:55], a[0:3], a[192:195], 0\n"
      "ds_read_b128 a[208:211], v4 offset:16960\n"
      "ds_write_addtid_b32 v20 offset:41216\n"
      "ds_write_addtid_b32 v21 offset:45312\n"
      "ds_write_addtid_b32 v22 offset:49408\n"
      "v_mfma_f32_16x16x32_bf16 v[56:59], a[4:7], a[192:195], 0\n"
      "s_waitcnt lgkmcnt(6)\n"
      "ds_write_addtid_b32 v76 offset:0\n"
      "ds_write_addtid_b32 v77 offset:512\n"
      "ds_write_addtid_b32 v23 offset:53504\n"
      "v_sub_f32 v78, v90, v78\n"
      "v_mfma_f32_4x4x4_16b_bf16 v[28:31], a[216:217], a[224:225], 0\n"
      "v_add_f32 v88, v88, v64\n"
      "v_mfma_f32_4x4x4_16b_bf16 v[32:35], a[216:217], a[226:227], 0\n"
      "ds_write_addtid_b32 v78 offset:1024\n"
      "v_sub_f32 v79, v91, v79\n"
      "v_add_f32 v89, v89, v65\n"
      "v_sub_f32 v80, v92, v80\n"
      "v_mfma_f32_16x16x32_bf16 v[60:63], a[8:11], a[192:195], 0\n"
      "ds_write_addtid_b32 v79 offset:1536\n"
      "ds_write_addtid_b32 v80 offset:2048\n"
      "v_add_f32 v90, v90, v66\n"
      "v_mfma_f32_16x16x32_bf16 v[52:55], a[12:15], a[196:199], v[52:55]\n"
      "ds_read_b128 a[212:215], v4 offset:17024\n"
      "v_sub_f32 v81, v93, v81\n"
      "v_cvt_pk_bf16_f32 v28, v28, v32\n"
      "v_mfma_f32_16x16x32_bf16 v[56:59], a[16:19], a[196:199], v[56:59]\n"
      "ds_write_addtid_b32 v28 offset:41728\n"
      "ds_read_b64_tr_b16 a[232:233], v7 offset:18432\n"
      "ds_write_addtid_b32 v81 offset:2560\n"
      "ds_read_b64_tr_b16 a[234:235], v7 offset:18440\n"
      "v_mfma_f32_16x16x32_bf16 v[60:63], a[20:23], a[196:199], v[60:63]\n"
      "v_add_f32 v91, v91, v67\n"
      "v_sub_f32 v82, v94, v82\n"
      "v_add_f32_e64 v92, v92, v68\n"
      "v_mfma_f32_16x16x32_bf16 v[52:55], a[24:27], a[200:203], v[52:55]\n"
      "s_waitcnt lgkmcnt(5)\n"
      "ds_read_b128 a[192:195], v5 offset:16896\n"
      "ds_write_addtid_b32 v82 offset:3072\n"
      "v_cvt_pk_bf16_f32 v29, v29, v33\n"
      "v_mfma_f32_4x4x4_16b_bf16 v[20:23], a[216:217], a[228:229], 0\n"
      "v_sub_f32 v83, v95, v83\n"
      "v_add_f32_e64 v93, v93, v69\n"
      "v_mfma_f32_4x4x4_16b_bf16 v[24:27], a[216:217], a[230:231], 0\n"
      "ds_write_addtid_b32 v29 offset:45824\n"
      "ds_write_addtid_b32 v83 offset:3584\n"
      "v_sub_f32 v84, v96, v84\n"
      "v_mfma_f32_16x16x32_bf16 v[56:59], a[28:31], a[200:203], v[56:59]\n"
      "ds_write_addtid_b32 v84 offset:4096\n"
      "v_cvt_pk_bf16_f32 v30, v30, v34\n"
      "v_add_f32 v94, v94, v70\n"
      "v_sub_f32 v85, v97, v85\n"
      "v_mfma_f32_16x16x32_bf16 v[60:63], a[32:35], a[200:203], v[60:63]\n"
      "s_nop 0\n"
      "ds_write_addtid_b32 v30 offset:49920\n"
      "ds_write_addtid_b32 v85 offset:4608\n"
      "v_cvt_pk_bf16_f32 v20, v20, v24\n"
      "v_mfma_f32_16x16x32_bf16 v[52:55], a[36:39], a[204:207], v[52:55]\n"
      "ds_read_b128 a[196:199], v5 offset:16960\n"
      "ds_write_addtid_b32 v20 offset:42240\n"
      "v_cvt_pk_bf16_f32 v31, v31, v35\n"
      "v_mfma_f32_16x16x32_bf16 v[56:59], a[40:43], a[204:207], v[56:59]\n"
      "ds_write_addtid_b32 v31 offset:54016\n"
      "v_sub_f32 v86, v98, v86\n"
      "v_add_f32 v95, v95, v71\n"
      "v_mfma_f32_16x16x32_bf16 v[60:63], a[44:47], a[204:207], v[60:63]\n"
      "s_waitcnt lgkmcnt(5)\n"
      "ds_write_addtid_b32 v86 offset:5120\n"
      "v_sub_f32 v87, v99, v87\n"
      "v_cvt_pk_bf16_f32 v21, v21, v25\n"
      "v_add_f32_e64 v96, v96, v72\n"
      "v_mfma_f32_4x4x4_16b_bf16 v[28:31], a[216:217], a[232:233], 0\n"
      "v_cvt_pk_bf16_f32 v22, v22, v26\n"
      "v_add_f32 v97, v97, v73\n"
      "v_mfma_f32_4x4x4_16b_bf16 v[32:35], a[216:217], a[234:235], 0\n"
      "ds_write_addtid_b32 v21 offset:46336\n"
      "ds_write_addtid_b32 v22 offset:50432\n"
      "ds_write_addtid_b32 v87 offset:5632\n"
      "v_mfma_f32_16x16x32_bf16 v[52:55], a[48:51], a[208:211], v[52:55]\n"
      "s_nop 0\n"
      "ds_read_b128 a[200:203], v5 offset:17024\n"
      "v_cvt_pk_bf16_f32 v28, v28, v32\n"
      "v_cvt_pk_bf16_f32 v23, v23, v27\n"
      "v_mfma_f32_16x16x32_bf16 v[56:59], a[52:55], a[208:211], v[56:59]\n"
      "ds_write_addtid_b32 v28 offset:42752\n"
      "ds_write_addtid_b32 v23 offset:54528\n"
      "v_cvt_pk_bf16_f32 v29, v29, v33\n"
      "v_cvt_pk_bf16_f32 v30, v30, v34\n"
      "v_cvt_pk_bf16_f32 v31, v31, v35\n"
      "v_mfma_f32_16x16x32_bf16 v[60:63], a[56:59], a[208:211], v[60:63]\n"
      "ds_write_addtid_b32 v29 offset:46848\n"
      "ds_write_addtid_b32 v30 offset:50944\n"
      "v_mfma_f32_16x16x32_bf16 v[52:55], a[60:63], a[212:215], v[52:55]\n"
      "ds_write_addtid_b32 v31 offset:55040\n"
      "v_add_f32 v98, v98, v74\n"
      "v_add_f32 v99, v99, v75\n"
      "v_fma_f32 v40, 0.5, v76, -v88\n"
      "v_mfma_f32_16x16x32_bf16 v[56:59], a[64:67], a[212:215], v[56:59]\n"
      "v_fma_f32 v41, 0.5, v77, -v89\n"
      "v_fma_f32 v42, 0.5, v78, -v90\n"
      "v_fma_f32 v43, 0.5, v79, -v91\n"
      "v_mfma_f32_16x16x32_bf16 v[60:63], a[68:71], a[212:215], v[60:63]\n"
      "s_waitcnt lgkmcnt(0)\n"
      "v_fma_f32 v44, 0.5, v80, -v92\n"
      "v_fma_f32 v45, 0.5, v81, -v93\n"
      "s_barrier\n"
      "LSloop_%=:\n"
      "v_mfma_f32_16x16x32_bf16 v[52:55], a[72:75], a[192:195], v[52:55]\n"
      "ds_read_b128 a[204:207], v0 offset:16384\n"
      "ds_read2st64_b32 v[28:29], v6 offset0:0 offset1:2\n"
      "v_fma_f32 v46, 0.5, v82, -v94\n"
      "v_mfma_f32_16x16x32_bf16 v[56:59], a[76:79], a[192:195], v[56:59]\n"
      "ds_read_b128 a[208:211], v0 offset:20480\n"
      "ds_read2st64_b32 v[30:31], v6 offset0:4 offset1:6\n"
      "v_mfma_f32_16x16x32_bf16 v[60:63], a[80:83], a[192:195], v[60:63]\n"
      "ds_read_b64_tr_b16 a[220:221], v7 offset:33792\n"
      "v_fma_f32 v47, 0.5, v83, -v95\n"
      "v_mfma_f32_16x16x32_bf16 v[52:55], a[84:87], a[196:199], v[52:55]\n"
      "ds_read_b128 a[212:215], v0 offset:16448\n"
      "v_fma_f32 v48, 0.5, v84, -v96\n"
      "v_mfma_f32_16x16x32_bf16 v[56:59], a[88:91], a[196:199], v[56:59]\n"
      "ds_read_b64_tr_b16 a[222:223], v7 offset:33800\n"
      "ds_read2st64_b32 v[32:33], v6 offset0:8 offset1:10\n"
      "v_fma_f32 v49, 0.5, v85, -v97\n"
      "v_mfma_f32_16x16x32_bf16 v[60:63], a[92:95], a[196:199], v[60:63]\n"
      "ds_read2st64_b32 v[34:35], v6 offset0:12 offset1:14\n"
      "v_fma_f32 v50, 0.5, v86, -v98\n"
      "v_mfma_f32_16x16x32_bf16 v[52:55], a[96:99], a[200:203], v[52:55]\n"
      "ds_read_b128 a[192:195], v0 offset:20544\n"
      "ds_read2st64_b32 v[36:37], v6 offset0:16 offset1:18\n"
      "v_mfma_f32_16x16x32_bf16 v[56:59], a[100:103], a[200:203], v[56:59]\n"
      "ds_read2st64_b32 v[38:39], v6 offset0:20 offset1:22\n"
      "v_fma_f32 v51, 0.5, v87, -v99\n"
      "v_mfma_f32_16x16x32_bf16 v[60:63], a[104:107], a[200:203], v[60:63]\n"
      "s_waitcnt lgkmcnt(8)\n"
      "v_fmac_f32 v28, s46, v64\n"
      "v_fmac_f32 v29, s46, v65\n"
      "v_fmac_f32 v30, s46, v66\n"
      "v_mfma_f32_16x16x32_bf16 v[76:79], v[252:255], a[204:207], v[52:55]\n"
      "s_waitcnt lgkmcnt(5)\n"
      "ds_read_b128 a[196:199], v0 offset:16512\n"
      "v_fmac_f32 v31, s46, v67\n"
      "v_mfma_f32_4x4x4_16b_bf16 v[12:15], a[216:217], a[220:221], 0\n"
      "v_sub_f32 v28, v28, v40\n"
      "v_mfma_f32_4x4x4_16b_bf16 v[16:19], a[216:217], a[222:223], 0\n"
      "v_sub_f32 v29, v29, v41\n"
      "v_sub_f32 v30, v30, v42\n"
      "v_mfma_f32_16x16x32_bf16 v[80:83], v[244:247], a[204:207], v[56:59]\n"
      "v_sub_f32 v31, v31, v43\n"
      "v_fmac_f32 v40, s45, v28\n"
      "v_mfma_f32_16x16x32_bf16 v[84:87], v[236:239], a[204:207], v[60:63]\n"
      "v_fmac_f32 v41, s45, v29\n"
      "v_cvt_pk_bf16_f32 v12, v12, v16\n"
      "v_fmac_f32_e64 v42, s45, v30\n"
      "v_mfma_f32_16x16x32_bf16 v[88:91], v[248:251], a[208:211], v[52:55]\n"
      "ds_read_b128 a[200:203], v0 offset:20608\n"
      "ds_write_addtid_b32 v12 offset:24832\n"
      "v_mfma_f32_16x16x32_bf16 v[92:95], v[240:243], a[208:211], v[56:59]\n"
      "ds_read_b64_tr_b16 a[224:225], v7 offset:34304\n"
      "ds_read_b64_tr_b16 a[226:227], v7 offset:34312\n"
      "v_mfma_f32_16x16x32_bf16 v[96:99], v[232:235], a[208:211], v[60:63]\n"
      "v_fmac_f32_e64 v43, s45, v31\n"
      "v_cvt_pk_bf16_f32 v13, v13, v17\n"
      "v_mfma_f32_16x16x32_bf16 v[76:79], v[228:231], a[212:215], v[76:79]\n"
      "ds_read_b128 a[204:207], v1 offset:16384\n"
      "ds_write_addtid_b32 v13 offset:28928\n"
      "v_fma_f32 v28, s47, v28, v52\n"
      "v_mfma_f32_16x16x32_bf16 v[80:83], v[220:223], a[212:215], v[80:83]\n"
      "v_fma_f32 v29, s47, v29, v53\n"
      "v_cvt_pk_bf16_f32 v14, v14, v18\n"
      "v_mfma_f32_16x16x32_bf16 v[84:87], v[212:215], a[212:215], v[84:87]\n"
      "s_waitcnt lgkmcnt(6)\n"
      "ds_write_addtid_b32 v14 offset:33024\n"
      "v_fma_f32 v30, s47, v30, v54\n"
      "v_mfma_f32_16x16x32_bf16 v[88:91], v[224:227], a[192:195], v[88:91]\n"
      "s_nop 0\n"
      "ds_read_b128 a[208:211], v1 offset:20480\n"
      "v_fma_f32 v31, s47, v31, v55\n"
      "v_mfma_f32_16x16x32_bf16 v[92:95], v[216:219], a[192:195], v[92:95]\n"
      "s_nop 0\n"
      "v_cvt_pk_bf16_f32 v40, v40, v41\n"
      "v_cvt_pk_bf16_f32 v15, v15, v19\n"
      "v_mfma_f32_16x16x32_bf16 v[96:99], v[208:211], a[192:195], v[96:99]\n"
      "s_waitcnt lgkmcnt(4)\n"
      "ds_write_addtid_b32 v40 offset:12288\n"
      "ds_write_addtid_b32 v15 offset:37120\n"
      "v_cvt_pk_bf16_f32 v42, v42, v43\n"
      "v_mfma_f32_4x4x4_16b_bf16 v[20:23], a[216:217], a[224:225], 0\n"
      "v_cvt_pk_bf16_f32 v28, v28, v29\n"
      "v_mfma_f32_4x4x4_16b_bf16 v[24:27], a[216:217], a[226:227], 0\n"
      "ds_write_addtid_b32 v42 offset:12800\n"
      "ds_write_addtid_b32 v28 offset:14336\n"
      "v_mfma_f32_16x16x32_bf16 v[76:79], v[204:207], a[196:199], v[76:79]\n"
      "ds_read_b128 a[212:215], v1 offset:16448\n"
      "v_cvt_pk_bf16_f32 v30, v30, v31\n"
      "v_mfma_f32_16x16x32_bf16 v[80:83], v[196:199], a[196:199], v[80:83]\n"
      "ds_write_addtid_b32 v30 offset:14848\n"
      "v_fmac_f32 v32, s46, v68\n"
      "v_cvt_pk_bf16_f32 v20, v20, v24\n"
      "v_mfma_f32_16x16x32_bf16 v[84:87], v[188:191], a[196:199], v[84:87]\n"
      "ds_write_addtid_b32 v20 offset:25344\n"
      "v_fmac_f32 v33, s46, v69\n"
      "v_mfma_f32_16x16x32_bf16 v[88:91], v[200:203], a[200:203], v[88:91]\n"
      "ds_read_b128 a[192:195], v1 offset:20544\n"
      "ds_read_b64_tr_b16 a[228:229], v7 offset:34816\n"
      "v_mfma_f32_16x16x32_bf16 v[92:95], v[192:195], a[200:203], v[92:95]\n"
      "ds_read_b64_tr_b16 a[230:231], v7 offset:34824\n"
      "v_fmac_f32 v34, s46, v70\n"
      "v_mfma_f32_16x16x32_bf16 v[96:99], v[184:187], a[200:203], v[96:99]\n"
      "s_waitcnt lgkmcnt(4)\n"
      "v_fmac_f32 v35, s46, v71\n"
      "v_cvt_pk_bf16_f32 v21, v21, v25\n"
      "v_mfma_f32_16x16x32_bf16 v[76:79], v[180:183], a[204:207], v[76:79]\n"
      "s_nop 0\n"
      "ds_read_b128 a[196:199], v1 offset:16512\n"
      "ds_write_addtid_b32 v21 offset:29440\n"
      "v_sub_f32 v32, v32, v44\n"
      "v_mfma_f32_16x16x32_bf16 v[80:83], v[172:175], a[204:207], v[80:83]\n"
      "v_sub_f32 v33, v33, v45\n"
      "v_cvt_pk_bf16_f32 v22, v22, v26\n"
      "v_mfma_f32_16x16x32_bf16 v[84:87], v[164:167], a[204:207], v[84:87]\n"
      "ds_write_addtid_b32 v22 offset:33536\n"
      "v_sub_f32 v34, v34, v46\n"
      "v_mfma_f32_16x16x32_bf16 v[88:91], v[176:179], a[208:211], v[88:91]\n"
      "s_nop 0\n"
      "ds_read_b128 a[200:203], v1 offset:20608\n"
      "v_sub_f32 v35, v35, v47\n"
      "v_mfma_f32_16x16x32_bf16 v[92:95], v[168:171], a[208:211], v[92:95]\n"
      "s_waitcnt lgkmcnt(4)\n"
      "v_fmac_f32 v44, s45, v32\n"
      "v_cvt_pk_bf16_f32 v23, v23, v27\n"
      "v_mfma_f32_4x4x4_16b_bf16 v[12:15], a[216:217], a[228:229], 0\n"
      "v_fmac_f32 v45, s45, v33\n"
      "v_mfma_f32_4x4x4_16b_bf16 v[16:19], a[216:217], a[230:231], 0\n"
      "ds_write_addtid_b32 v23 offset:37632\n"
      "v_fmac_f32 v46, s45, v34\n"
      "v_mfma_f32_16x16x32_bf16 v[96:99], v[160:163], a[208:211], v[96:99]\n"
      "v_fmac_f32 v47, s45, v35\n"
      "v_fma_f32 v32, s47, v32, v56\n"
      "v_cvt_pk_bf16_f32 v12, v12, v16\n"
      "v_mfma_f32_16x16x32_bf16 v[76:79], v[156:159], a[212:215], v[76:79]\n"
      "ds_read_b128 a[204:207], v2 offset:16384\n"
      "ds_write_addtid_b32 v12 offset:25856\n"
      "v_mfma_f32_16x16x32_bf16 v[80:83], v[148:151], a[212:215], v[80:83]\n"
      "v_fma_f32 v33, s47, v33, v57\n"
      "v_cvt_pk_bf16_f32 v13, v13, v17\n"
      "v_mfma_f32_16x16x32_bf16 v[84:87], v[140:143], a[212:215], v[84:87]\n"
      "ds_read_b64_tr_b16 a[232:233], v7 offset:35328\n"
      "ds_write_addtid_b32 v13 offset:29952\n"
      "v_fma_f32 v34, s47, v34, v58\n"
      "v_mfma_f32_16x16x32_bf16 v[88:91], v[152:155], a[192:195], v[88:91]\n"
      "ds_read_b128 a[208:211], v2 offset:20480\n"
      "ds_read_b64_tr_b16 a[234:235], v7 offset:35336\n"
      "v_mfma_f32_16x16x32_bf16 v[92:95], v[144:147], a[192:195], v[92:95]\n"
      "s_nop 0\n"
      "v_fma_f32 v35, s47, v35, v59\n"
      "v_cvt_pk_bf16_f32 v44, v44, v45\n"
      "v_mfma_f32_16x16x32_bf16 v[96:99], v[136:139], a[192:195], v[96:99]\n"
      "s_waitcnt lgkmcnt(4)\n"
      "ds_write_addtid_b32 v44 offset:20484\n"
      "v_cvt_pk_bf16_f32 v14, v14, v18\n"
      "v_mfma_f32_16x16x32_bf16 v[76:79], v[132:135], a[196:199], v[76:79]\n"
      "ds_read_b128 a[212:215], v2 offset:16448\n"
      "ds_write_addtid_b32 v14 offset:34048\n"
      "v_cvt_pk_bf16_f32 v46, v46, v47\n"
      "v_mfma_f32_16x16x32_bf16 v[80:83], v[124:127], a[196:199], v[80:83]\n"
      "ds_write_addtid_b32 v46 offset:20996\n"
      "v_cvt_pk_bf16_f32 v32, v32, v33\n"
      "v_mfma_f32_16x16x32_bf16 v[84:87], v[116:119], a[196:199], v[84:87]\n"
      "ds_write_addtid_b32 v32 offset:22532\n"
      "v_cvt_pk_bf16_f32 v34, v34, v35\n"
      "v_mfma_f32_16x16x32_bf16 v[88:91], v[128:131], a[200:203], v[88:91]\n"
      "s_waitcnt lgkmcnt(4)\n"
      "ds_read_b128 a[192:195], v2 offset:20544\n"
      "ds_write_addtid_b32 v34 offset:23044\n"
      "v_fmac_f32 v36, s46, v72\n"
      "v_mfma_f32_4x4x4_16b_bf16 v[20:23], a[216:217], a[232:233], 0\n"
      "v_fmac_f32_e64 v37, s46, v73\n"
      "v_mfma_f32_4x4x4_16b_bf16 v[24:27], a[216:217], a[234:235], 0\n"
      "v_fmac_f32 v38, s46, v74\n"
      "v_cvt_pk_bf16_f32 v15, v15, v19\n"
      "v_mfma_f32_16x16x32_bf16 v[92:95], v[120:123], a[200:203], v[92:95]\n"
      "ds_write_addtid_b32 v15 offset:38144\n"
      "v_fmac_f32 v39, s46, v75\n"
      "v_cvt_pk_bf16_f32 v20, v20, v24\n"
      "v_mfma_f32_16x16x32_bf16 v[96:99], v[112:115], a[200:203], v[96:99]\n"
      "ds_write_addtid_b32 v20 offset:26368\n"
      "v_sub_f32 v36, v36, v48\n"
      "v_mfma_f32_16x16x32_bf16 v[76:79], v[108:111], a[204:207], v[76:79]\n"
      "ds_read_b128 a[196:199], v2 offset:16512\n"
      "v_sub_f32 v37, v37, v49\n"
      "v_mfma_f32_16x16x32_bf16 v[80:83], v[100:103], a[204:207], v[80:83]\n"
      "ds_read_b64_tr_b16 a[220:221], v7 offset:35840\n"
      "ds_read_b64_tr_b16 a[222:223], v7 offset:35848\n"
      "v_mfma_f32_16x16x32_bf16 v[84:87], a[160:163], a[204:207], v[84:87]\n"
      "v_sub_f32_e64 v38, v38, v50\n"
      "v_cvt_pk_bf16_f32 v21, v21, v25\n"
      "v_mfma_f32_16x16x32_bf16 v[88:91], v[104:107], a[208:211], v[88:91]\n"
      "ds_read_b128 a[200:203], v2 offset:20608\n"
      "ds_write_addtid_b32 v21 offset:30464\n"
      "v_sub_f32 v39, v39, v51\n"
      "v_mfma_f32_16x16x32_bf16 v[92:95], a[164:167], a[208:211], v[92:95]\n"
      "s_nop 0\n"
      "v_fmac_f32_e64 v48, s45, v36\n"
      "v_cvt_pk_bf16_f32 v22, v22, v26\n"
      "v_mfma_f32_16x16x32_bf16 v[96:99], a[156:159], a[208:211], v[96:99]\n"
      "s_waitcnt lgkmcnt(4)\n"
      "ds_write_addtid_b32 v22 offset:34560\n"
      "v_fmac_f32 v49, s45, v37\n"
      "v_mfma_f32_16x16x32_bf16 v[76:79], a[152:155], a[212:215], v[76:79]\n"
      "s_nop 0\n"
      "ds_read_b128 a[204:207], v3 offset:33792\n"
      "v_fmac_f32_e64 v50, s45, v38\n"
      "v_mfma_f32_16x16x32_bf16 v[80:83], a[144:147], a[212:215], v[80:83]\n"
      "s_nop 0\n"
      "v_fmac_f32 v51, s45, v39\n"
      "v_cvt_pk_bf16_f32 v23, v23, v27\n"
      "v_mfma_f32_16x16x32_bf16 v[84:87], a[136:139], a[212:215], v[84:87]\n"
      "s_waitcnt lgkmcnt(2)\n"
      "ds_write_addtid_b32 v23 offset:38656\n"
      "v_fma_f32 v36, s47, v36, v60\n"
      "v_mfma_f32_4x4x4_16b_bf16 v[12:15], a[216:217], a[220:221], 0\n"
      "v_fma_f32 v37, s47, v37, v61\n"
      "v_mfma_f32_4x4x4_16b_bf16 v[16:19], a[216:217], a[222:223], 0\n"
      "v_fma_f32 v38, s47, v38, v62\n"
      "v_fma_f32 v39, s47, v39, v63\n"
      "v_mfma_f32_16x16x32_bf16 v[88:91], a[148:151], a[192:195], v[88:91]\n"
      "ds_read_b128 a[208:211], v3 offset:33856\n"
      "v_cvt_pk_bf16_f32 v48, v48, v49\n"
      "v_cvt_pk_bf16_f32 v12, v12, v16\n"
      "v_mfma_f32_16x16x32_bf16 v[92:95], a[140:143], a[192:195], v[92:95]\n"
      "ds_write_addtid_b32 v48 offset:16384\n"
      "ds_write_addtid_b32 v12 offset:26880\n"
      "v_mfma_f32_16x16x32_bf16 v[96:99], a[132:135], a[192:195], v[96:99]\n"
      "v_cvt_pk_bf16_f32 v13, v13, v17\n"
      "v_cvt_pk_bf16_f32 v14, v14, v18\n"
      "v_mfma_f32_16x16x32_bf16 v[76:79], a[128:131], a[196:199], v[76:79]\n"
      "ds_read_b128 a[212:215], v3 offset:33920\n"
      "ds_write_addtid_b32 v13 offset:30976\n"
      "v_mfma_f32_16x16x32_bf16 v[80:83], a[120:123], a[196:199], v[80:83]\n"
      "ds_read_b64_tr_b16 a[224:225], v7 offset:36352\n"
      "ds_write_addtid_b32 v14 offset:35072\n"
      "v_cvt_pk_bf16_f32 v50, v50, v51\n"
      "v_mfma_f32_16x16x32_bf16 v[84:87], a[112:115], a[196:199], v[84:87]\n"
      "ds_write_addtid_b32 v50 offset:16896\n"
      "ds_read_b64_tr_b16 a[226:227], v7 offset:36360\n"
      "v_mfma_f32_16x16x32_bf16 v[88:91], a[124:127], a[200:203], v[88:91]\n"
      "ds_read_b128 a[192:195], v4 offset:33792\n"
      "v_cvt_pk_bf16_f32 v36, v36, v37\n"
      "v_mfma_f32_16x16x32_bf16 v[92:95], a[116:119], a[200:203], v[92:95]\n"
      "ds_write_addtid_b32 v36 offset:18432\n"
      "v_cvt_pk_bf16_f32 v15, v15, v19\n"
      "v_mfma_f32_16x16x32_bf16 v[96:99], a[108:111], a[200:203], v[96:99]\n"
      "s_waitcnt lgkmcnt(6)\n"
      "ds_write_addtid_b32 v15 offset:39168\n"
      "v_sub_f32 v76, v88, v76\n"
      "v_sub_f32_e64 v77, v89, v77\n"
      "v_cvt_pk_bf16_f32 v38, v38, v39\n"
      "v_mfma_f32_16x16x32_bf16 v[64:67], a[0:3], a[204:207], 0\n"
      "s_nop 0\n"
      "ds_read_b128 a[196:199], v4 offset:33856\n"
      "ds_write_addtid_b32 v38 offset:18944\n"
      "v_mfma_f32_16x16x32_bf16 v[68:71], a[4:7], a[204:207], 0\n"
      "s_waitcnt lgkmcnt(5)\n"
      "ds_write_addtid_b32 v76 offset:6144\n"
      "ds_write_addtid_b32 v77 offset:6656\n"
      "v_mfma_f32_4x4x4_16b_bf16 v[20:23], a[216:217], a[224:225], 0\n"
      "v_sub_f32 v78, v90, v78\n"
      "v_mfma_f32_4x4x4_16b_bf16 v[24:27], a[216:217], a[226:227], 0\n"
      "ds_write_addtid_b32 v78 offset:7168\n"
      "v_sub_f32 v79, v91, v79\n"
      "v_mfma_f32_16x16x32_bf16 v[72:75], a[8:11], a[204:207], 0\n"
      "ds_write_addtid_b32 v79 offset:7680\n"
      "v_sub_f32 v80, v92, v80\n"
      "v_sub_f32 v81, v93, v81\n"
      "v_mfma_f32_16x16x32_bf16 v[64:67], a[12:15], a[208:211], v[64:67]\n"
      "ds_read_b128 a[200:203], v4 offset:33920\n"
      "ds_write_addtid_b32 v80 offset:8192\n"
      "v_mfma_f32_16x16x32_bf16 v[68:71], a[16:19], a[208:211], v[68:71]\n"
      "ds_write_addtid_b32 v81 offset:8704\n"
      "v_sub_f32 v82, v94, v82\n"
      "v_mfma_f32_16x16x32_bf16 v[72:75], a[20:23], a[208:211], v[72:75]\n"
      "s_nop 0\n"
      "ds_write_addtid_b32 v82 offset:9216\n"
      "v_cvt_pk_bf16_f32 v20, v20, v24\n"
      "v_mfma_f32_16x16x32_bf16 v[64:67], a[24:27], a[212:215], v[64:67]\n"
      "ds_read_b128 a[204:207], v5 offset:33792\n"
      "ds_write_addtid_b32 v20 offset:27392\n"
      "v_sub_f32 v83, v95, v83\n"
      "v_mfma_f32_16x16x32_bf16 v[68:71], a[28:31], a[212:215], v[68:71]\n"
      "ds_write_addtid_b32 v83 offset:9728\n"
      "v_sub_f32_e64 v84, v96, v84\n"
      "v_mfma_f32_16x16x32_bf16 v[72:75], a[32:35], a[212:215], v[72:75]\n"
      "s_waitcnt lgkmcnt(5)\n"
      "ds_write_addtid_b32 v84 offset:10240\n"
      "v_cvt_pk_bf16_f32 v21, v21, v25\n"
      "v_mfma_f32_16x16x32_bf16 v[64:67], a[36:39], a[192:195], v[64:67]\n"
      "ds_read_b128 a[208:211], v5 offset:33856\n"
      "ds_write_addtid_b32 v21 offset:31488\n"
      "v_mfma_f32_16x16x32_bf16 v[68:71], a[40:43], a[192:195], v[68:71]\n"
      "v_sub_f32 v85, v97, v85\n"
      "v_sub_f32 v86, v98, v86\n"
      "v_cvt_pk_bf16_f32 v22, v22, v26\n"
      "v_mfma_f32_16x16x32_bf16 v[72:75], a[44:47], a[192:195], v[72:75]\n"
      "ds_write_addtid_b32 v22 offset:35584\n"
      "ds_write_addtid_b32 v85 offset:10752\n"
      "v_sub_f32 v87, v99, v87\n"
      "v_mfma_f32_16x16x32_bf16 v[64:67], a[48:51], a[196:199], v[64:67]\n"
      "ds_read_b128 a[212:215], v5 offset:33920\n"
      "ds_write_addtid_b32 v86 offset:11264\n"
      "v_mfma_f32_16x16x32_bf16 v[68:71], a[52:55], a[196:199], v[68:71]\n"
      "s_nop 0\n"
      "ds_write_addtid_b32 v87 offset:11776\n"
      "v_cvt_pk_bf16_f32 v23, v23, v27\n"
      "v_mfma_f32_16x16x32_bf16 v[72:75], a[56:59], a[196:199], v[72:75]\n"
      "ds_write_addtid_b32 v23 offset:39680\n"
      "v_fma_f32 v40, 0.5, v76, -v88\n"
      "v_mfma_f32_16x16x32_bf16 v[64:67], a[60:63], a[200:203], v[64:67]\n"
      "v_fma_f32 v41, 0.5, v77, -v89\n"
      "v_fma_f32 v42, 0.5, v78, -v90\n"
      "v_mfma_f32_16x16x32_bf16 v[68:71], a[64:67], a[200:203], v[68:71]\n"
      "v_fma_f32 v43, 0.5, v79, -v91\n"
      "v_fma_f32 v44, 0.5, v80, -v92\n"
      "v_mfma_f32_16x16x32_bf16 v[72:75], a[68:71], a[200:203], v[72:75]\n"
      "s_waitcnt lgkmcnt(0)\n"
      "v_fma_f32 v45, 0.5, v81, -v93\n"
      "s_barrier\n"
      "LSmid_%=:\n"
      "v_mfma_f32_16x16x32_bf16 v[64:67], a[72:75], a[204:207], v[64:67]\n"
      "ds_read_b128 a[192:195], v0 offset:0\n"
      "ds_read2st64_b32 v[28:29], v6 offset0:24 offset1:26\n"
      "v_fma_f32 v46, 0.5, v82, -v94\n"
      "v_mfma_f32_16x16x32_bf16 v[68:71], a[76:79], a[204:207], v[68:71]\n"
      "ds_read_b128 a[196:199], v0 offset:4096\n"
      "ds_read2st64_b32 v[30:31], v6 offset0:28 offset1:30\n"
      "v_mfma_f32_16x16x32_bf16 v[72:75], a[80:83], a[204:207], v[72:75]\n"
      "ds_read_b64_tr_b16 a[220:221], v7 offset:50688\n"
      "v_fma_f32 v47, 0.5, v83, -v95\n"
      "v_mfma_f32_16x16x32_bf16 v[64:67], a[84:87], a[208:211], v[64:67]\n"
      "ds_read_b128 a[200:203], v0 offset:64\n"
      "v_fma_f32 v48, 0.5, v84, -v96\n"
      "v_mfma_f32_16x16x32_bf16 v[68:71], a[88:91], a[208:211], v[68:71]\n"
      "ds_read_b64_tr_b16 a[222:223], v7 offset:50696\n"
      "ds_read2st64_b32 v[32:33], v6 offset0:32 offset1:34\n"
      "v_fma_f32 v49, 0.5, v85, -v97\n"
      "v_mfma_f32_16x16x32_bf16 v[72:75], a[92:95], a[208:211], v[72:75]\n"
      "ds_read2st64_b32 v[34:35], v6 offset0:36 offset1:38\n"
      "v_fma_f32 v50, 0.5, v86, -v98\n"
      "v_mfma_f32_16x16x32_bf16 v[64:67], a[96:99], a[212:215], v[64:67]\n"
      "ds_read_b128 a[204:207], v0 offset:4160\n"
      "ds_read2st64_b32 v[36:37], v6 offset0:40 offset1:42\n"
      "v_mfma_f32_16x16x32_bf16 v[68:71], a[100:103], a[212:215], v[68:71]\n"
      "ds_read2st64_b32 v[38:39], v6 offset0:44 offset1:46\n"
      "v_fma_f32 v51, 0.5, v87, -v99\n"
      "v_mfma_f32_16x16x32_bf16 v[72:75], a[104:107], a[212:215], v[72:75]\n"
      "s_waitcnt lgkmcnt(8)\n"
      "v_fmac_f32 v28, s46, v52\n"
      "v_fmac_f32 v29, s46, v53\n"
      "v_fmac_f32 v30, s46, v54\n"
      "v_mfma_f32_16x16x32_bf16 v[76:79], v[252:255], a[192:195], v[64:67]\n"
      "s_waitcnt lgkmcnt(5)\n"
      "ds_read_b128 a[208:211], v0 offset:128\n"
      "v_fmac_f32 v31, s46, v55\n"
      "v_mfma_f32_4x4x4_16b_bf16 v[12:15], a[216:217], a[220:221], 0\n"
      "v_sub_f32_e64 v28, v28, v40\n"
      "v_mfma_f32_4x4x4_16b_bf16 v[16:19], a[216:217], a[222:223], 0\n"
      "v_sub_f32 v29, v29, v41\n"
      "v_sub_f32 v30, v30, v42\n"
      "v_mfma_f32_16x16x32_bf16 v[80:83], v[244:247], a[192:195], v[68:71]\n"
      "v_sub_f32 v31, v31, v43\n"
      "v_fmac_f32 v40, s45, v28\n"
      "v_mfma_f32_16x16x32_bf16 v[84:87], v[236:239], a[192:195], v[72:75]\n"
      "v_fmac_f32 v41, s45, v29\n"
      "v_cvt_pk_bf16_f32 v12, v12, v16\n"
      "v_fmac_f32 v42, s45, v30\n"
      "v_mfma_f32_16x16x32_bf16 v[88:91], v[248:251], a[196:199], v[64:67]\n"
      "ds_read_b128 a[212:215], v0 offset:4224\n"
      "ds_write_addtid_b32 v12 offset:41216\n"
      "v_mfma_f32_16x16x32_bf16 v[92:95], v[240:243], a[196:199], v[68:71]\n"
      "ds_read_b64_tr_b16 a[224:225], v7 offset:51200\n"
      "ds_read_b64_tr_b16 a[226:227], v7 offset:51208\n"
      "v_mfma_f32_16x16x32_bf16 v[96:99], v[232:235], a[196:199], v[72:75]\n"
      "v_fmac_f32_e64 v43, s45, v31\n"
      "v_cvt_pk_bf16_f32 v13, v13, v17\n"
      "v_mfma_f32_16x16x32_bf16 v[76:79], v[228:231], a[200:203], v[76:79]\n"
      "s_nop 0\n"
      "ds_read_b128 a[192:195], v1 offset:0\n"
      "ds_write_addtid_b32 v13 offset:45312\n"
      "v_fma_f32 v28, s47, v28, v64\n"
      "v_mfma_f32_16x16x32_bf16 v[80:83], v[220:223], a[200:203], v[80:83]\n"
      "s_nop 0\n"
      "v_fma_f32 v29, s47, v29, v65\n"
      "v_cvt_pk_bf16_f32 v14, v14, v18\n"
      "v_mfma_f32_16x16x32_bf16 v[84:87], v[212:215], a[200:203], v[84:87]\n"
      "s_waitcnt lgkmcnt(6)\n"
      "ds_write_addtid_b32 v14 offset:49408\n"
      "v_fma_f32 v30, s47, v30, v66\n"
      "v_mfma_f32_16x16x32_bf16 v[88:91], v[224:227], a[204:207], v[88:91]\n"
      "s_nop 0\n"
      "ds_read_b128 a[196:199], v1 offset:4096\n"
      "v_fma_f32 v31, s47, v31, v67\n"
      "v_mfma_f32_16x16x32_bf16 v[92:95], v[216:219], a[204:207], v[92:95]\n"
      "s_nop 0\n"
      "v_cvt_pk_bf16_f32 v40, v40, v41\n"
      "v_cvt_pk_bf16_f32 v15, v15, v19\n"
      "v_mfma_f32_16x16x32_bf16 v[96:99], v[208:211], a[204:207], v[96:99]\n"
      "s_waitcnt lgkmcnt(4)\n"
      "ds_write_addtid_b32 v40 offset:13312\n"
      "ds_write_addtid_b32 v15 offset:53504\n"
      "v_cvt_pk_bf16_f32 v42, v42, v43\n"
      "v_mfma_f32_4x4x4_16b_bf16 v[20:23], a[216:217], a[224:225], 0\n"
      "v_cvt_pk_bf16_f32 v28, v28, v29\n"
      "v_mfma_f32_4x4x4_16b_bf16 v[24:27], a[216:217], a[226:227], 0\n"
      "ds_write_addtid_b32 v42 offset:13824\n"
      "ds_write_addtid_b32 v28 offset:15360\n"
      "v_mfma_f32_16x16x32_bf16 v[76:79], v[204:207], a[208:211], v[76:79]\n"
      "ds_read_b128 a[200:203], v1 offset:64\n"
      "v_cvt_pk_bf16_f32 v30, v30, v31\n"
      "v_mfma_f32_16x16x32_bf16 v[80:83], v[196:199], a[208:211], v[80:83]\n"
      "ds_write_addtid_b32 v30 offset:15872\n"
      "v_fmac_f32 v32, s46, v56\n"
      "v_cvt_pk_bf16_f32 v20, v20, v24\n"
      "v_mfma_f32_16x16x32_bf16 v[84:87], v[188:191], a[208:211], v[84:87]\n"
      "ds_write_addtid_b32 v20 offset:41728\n"
      "v_fmac_f32 v33, s46, v57\n"
      "v_mfma_f32_16x16x32_bf16 v[88:91], v[200:203], a[212:215], v[88:91]\n"
      "ds_read_b128 a[204:207], v1 offset:4160\n"
      "ds_read_b64_tr_b16 a[228:229], v7 offset:51712\n"
      "v_mfma_f32_16x16x32_bf16 v[92:95], v[192:195], a[212:215], v[92:95]\n"
      "ds_read_b64_tr_b16 a[230:231], v7 offset:51720\n"
      "v_fmac_f32 v34, s46, v58\n"
      "v_mfma_f32_16x16x32_bf16 v[96:99], v[184:187], a[212:215], v[96:99]\n"
      "s_waitcnt lgkmcnt(4)\n"
      "v_fmac_f32 v35, s46, v59\n"
      "v_cvt_pk_bf16_f32 v21, v21, v25\n"
      "v_mfma_f32_16x16x32_bf16 v[76:79], v[180:183], a[192:195], v[76:79]\n"
      "s_nop 0\n"
      "ds_read_b128 a[208:211], v1 offset:128\n"
      "ds_write_addtid_b32 v21 offset:45824\n"
      "v_sub_f32 v32, v32, v44\n"
      "v_mfma_f32_16x16x32_bf16 v[80:83], v[172:175], a[192:195], v[80:83]\n"
      "v_sub_f32 v33, v33, v45\n"
      "v_cvt_pk_bf16_f32 v22, v22, v26\n"
      "v_mfma_f32_16x16x32_bf16 v[84:87], v[164:167], a[192:195], v[84:87]\n"
      "ds_write_addtid_b32 v22 offset:49920\n"
      "v_sub_f32 v34, v34, v46\n"
      "v_mfma_f32_16x16x32_bf16 v[88:91], v[176:179], a[196:199], v[88:91]\n"
      "s_nop 0\n"
      "ds_read_b128 a[212:215], v1 offset:4224\n"
      "v_sub_f32 v35, v35, v47\n"
      "v_mfma_f32_16x16x32_bf16 v[92:95], v[168:171], a[196:199], v[92:95]\n"
      "s_waitcnt lgkmcnt(4)\n"
      "v_fmac_f32 v44, s45, v32\n"
      "v_cvt_pk_bf16_f32 v23, v23, v27\n"
      "v_mfma_f32_4x4x4_16b_bf16 v[12:15], a[216:217], a[228:229], 0\n"
      "v_fmac_f32 v45, s45, v33\n"
      "v_mfma_f32_4x4x4_16b_bf16 v[16:19], a[216:217], a[230:231], 0\n"
      "ds_write_addtid_b32 v23 offset:54016\n"
      "v_fmac_f32 v46, s45, v34\n"
      "v_mfma_f32_16x16x32_bf16 v[96:99], v[160:163], a[196:199], v[96:99]\n"
      "v_fmac_f32 v47, s45, v35\n"
      "v_fma_f32 v32, s47, v32, v68\n"
      "v_cvt_pk_bf16_f32 v12, v12, v16\n"
      "v_mfma_f32_16x16x32_bf16 v[76:79], v[156:159], a[200:203], v[76:79]\n"
      "ds_read_b128 a[192:195], v2 offset:0\n"
      "ds_write_addtid_b32 v12 offset:42240\n"
      "v_mfma_f32_16x16x32_bf16 v[80:83], v[148:151], a[200:203], v[80:83]\n"
      "v_fma_f32 v33, s47, v33, v69\n"
      "v_cvt_pk_bf16_f32 v13, v13, v17\n"
      "v_mfma_f32_16x16x32_bf16 v[84:87], v[140:143], a[200:203], v[84:87]\n"
      "ds_read_b64_tr_b16 a[232:233], v7 offset:52224\n"
      "ds_write_addtid_b32 v13 offset:46336\n"
      "v_fma_f32 v34, s47, v34, v70\n"
      "v_mfma_f32_16x16x32_bf16 v[88:91], v[152:155], a[204:207], v[88:91]\n"
      "ds_read_b128 a[196:199], v2 offset:4096\n"
      "ds_read_b64_tr_b16 a[234:235], v7 offset:52232\n"
      "v_mfma_f32_16x16x32_bf16 v[92:95], v[144:147], a[204:207], v[92:95]\n"
      "s_nop 0\n"
      "v_fma_f32 v35, s47, v35, v71\n"
      "v_cvt_pk_bf16_f32 v44, v44, v45\n"
      "v_mfma_f32_16x16x32_bf16 v[96:99], v[136:139], a[204:207], v[96:99]\n"
      "s_waitcnt lgkmcnt(4)\n"
      "ds_write_addtid_b32 v44 offset:21508\n"
      "v_cvt_pk_bf16_f32 v14, v14, v18\n"
      "v_mfma_f32_16x16x32_bf16 v[76:79], v[132:135], a[208:211], v[76:79]\n"
      "ds_read_b128 a[200:203], v2 offset:64\n"
      "ds_write_addtid_b32 v14 offset:50432\n"
      "v_cvt_pk_bf16_f32 v46, v46, v47\n"
      "v_mfma_f32_16x16x32_bf16 v[80:83], v[124:127], a[208:211], v[80:83]\n"
      "ds_write_addtid_b32 v46 offset:22020\n"
      "v_cvt_pk_bf16_f32 v32, v32, v33\n"
      "v_mfma_f32_16x16x32_bf16 v[84:87], v[116:119], a[208:211], v[84:87]\n"
      "ds_write_addtid_b32 v32 offset:23556\n"
      "v_cvt_pk_bf16_f32 v34, v34, v35\n"
      "v_mfma_f32_16x16x32_bf16 v[88:91], v[128:131], a[212:215], v[88:91]\n"
      "s_waitcnt lgkmcnt(4)\n"
      "ds_read_b128 a[204:207], v2 offset:4160\n"
      "ds_write_addtid_b32 v34 offset:24068\n"
      "v_fmac_f32 v36, s46, v60\n"
      "v_mfma_f32_4x4x4_16b_bf16 v[20:23], a[216:217], a[232:233], 0\n"
      "v_fmac_f32_e64 v37, s46, v61\n"
      "v_mfma_f32_4x4x4_16b_bf16 v[24:27], a[216:217], a[234:235], 0\n"
      "v_fmac_f32 v38, s46, v62\n"
      "v_cvt_pk_bf16_f32 v15, v15, v19\n"
      "v_mfma_f32_16x16x32_bf16 v[92:95], v[120:123], a[212:215], v[92:95]\n"
      "ds_write_addtid_b32 v15 offset:54528\n"
      "v_fmac_f32 v39, s46, v63\n"
      "v_cvt_pk_bf16_f32 v20, v20, v24\n"
      "v_mfma_f32_16x16x32_bf16 v[96:99], v[112:115], a[212:215], v[96:99]\n"
      "ds_write_addtid_b32 v20 offset:42752\n"
      "v_sub_f32 v36, v36, v48\n"
      "v_mfma_f32_16x16x32_bf16 v[76:79], v[108:111], a[192:195], v[76:79]\n"
      "ds_read_b128 a[208:211], v2 offset:128\n"
      "v_sub_f32 v37, v37, v49\n"
      "v_mfma_f32_16x16x32_bf16 v[80:83], v[100:103], a[192:195], v[80:83]\n"
      "ds_read_b64_tr_b16 a[220:221], v7 offset:52736\n"
      "ds_read_b64_tr_b16 a[222:223], v7 offset:52744\n"
      "v_mfma_f32_16x16x32_bf16 v[84:87], a[160:163], a[192:195], v[84:87]\n"
      "v_sub_f32_e64 v38, v38, v50\n"
      "v_cvt_pk_bf16_f32 v21, v21, v25\n"
      "v_mfma_f32_16x16x32_bf16 v[88:91], v[104:107], a[196:199], v[88:91]\n"
      "ds_read_b128 a[212:215], v2 offset:4224\n"
      "ds_write_addtid_b32 v21 offset:46848\n"
      "v_sub_f32 v39, v39, v51\n"
      "v_mfma_f32_16x16x32_bf16 v[92:95], a[164:167], a[196:199], v[92:95]\n"
      "s_nop 0\n"
      "v_fmac_f32_e64 v48, s45, v36\n"
      "v_cvt_pk_bf16_f32 v22, v22, v26\n"
      "v_mfma_f32_16x16x32_bf16 v[96:99], a[156:159], a[196:199], v[96:99]\n"
      "s_waitcnt lgkmcnt(4)\n"
      "ds_write_addtid_b32 v22 offset:50944\n"
      "v_fmac_f32 v49, s45, v37\n"
      "v_mfma_f32_16x16x32_bf16 v[76:79], a[152:155], a[200:203], v[76:79]\n"
      "s_nop 0\n"
      "ds_read_b128 a[192:195], v3 offset:50688\n"
      "v_fmac_f32_e64 v50, s45, v38\n"
      "v_mfma_f32_16x16x32_bf16 v[80:83], a[144:147], a[200:203], v[80:83]\n"
      "s_nop 0\n"
      "v_fmac_f32 v51, s45, v39\n"
      "v_cvt_pk_bf16_f32 v23, v23, v27\n"
      "v_mfma_f32_16x16x32_bf16 v[84:87], a[136:139], a[200:203], v[84:87]\n"
      "s_waitcnt lgkmcnt(2)\n"
      "ds_write_addtid_b32 v23 offset:55040\n"
      "v_fma_f32 v36, s47, v36, v72\n"
      "v_mfma_f32_4x4x4_16b_bf16 v[12:15], a[216:217], a[220:221], 0\n"
      "v_fma_f32 v37, s47, v37, v73\n"
      "v_mfma_f32_4x4x4_16b_bf16 v[16:19], a[216:217], a[222:223], 0\n"
      "v_fma_f32 v38, s47, v38, v74\n"
      "v_fma_f32 v39, s47, v39, v75\n"
      "v_mfma_f32_16x16x32_bf16 v[88:91], a[148:151], a[204:207], v[88:91]\n"
      "ds_read_b128 a[196:199], v3 offset:50752\n"
      "v_cvt_pk_bf16_f32 v48, v48, v49\n"
      "v_cvt_pk_bf16_f32 v12, v12, v16\n"
      "v_mfma_f32_16x16x32_bf16 v[92:95], a[140:143], a[204:207], v[92:95]\n"
      "ds_write_addtid_b32 v48 offset:17408\n"
      "ds_write_addtid_b32 v12 offset:43264\n"
      "v_mfma_f32_16x16x32_bf16 v[96:99], a[132:135], a[204:207], v[96:99]\n"
      "v_cvt_pk_bf16_f32 v13, v13, v17\n"
      "v_cvt_pk_bf16_f32 v14, v14, v18\n"
      "v_mfma_f32_16x16x32_bf16 v[76:79], a[128:131], a[208:211], v[76:79]\n"
      "ds_read_b128 a[200:203], v3 offset:50816\n"
      "ds_write_addtid_b32 v13 offset:47360\n"
      "v_mfma_f32_16x16x32_bf16 v[80:83], a[120:123], a[208:211], v[80:83]\n"
      "ds_read_b64_tr_b16 a[224:225], v7 offset:53248\n"
      "ds_write_addtid_b32 v14 offset:51456\n"
      "v_cvt_pk_bf16_f32 v50, v50, v51\n"
      "v_mfma_f32_16x16x32_bf16 v[84:87], a[112:115], a[208:211], v[84:87]\n"
      "ds_write_addtid_b32 v50 offset:17920\n"
      "ds_read_b64_tr_b16 a[226:227], v7 offset:53256\n"
      "v_mfma_f32_16x16x32_bf16 v[88:91], a[124:127], a[212:215], v[88:91]\n"
      "ds_read_b128 a[204:207], v4 offset:50688\n"
      "v_cvt_pk_bf16_f32 v36, v36, v37\n"
      "v_mfma_f32_16x16x32_bf16 v[92:95], a[116:119], a[212:215], v[92:95]\n"
      "ds_write_addtid_b32 v36 offset:19456\n"
      "v_cvt_pk_bf16_f32 v15, v15, v19\n"
      "v_mfma_f32_16x16x32_bf16 v[96:99], a[108:111], a[212:215], v[96:99]\n"
      "s_waitcnt lgkmcnt(6)\n"
      "ds_write_addtid_b32 v15 offset:55552\n"
      "v_sub_f32 v76, v88, v76\n"
      "v_sub_f32_e64 v77, v89, v77\n"
      "v_cvt_pk_bf16_f32 v38, v38, v39\n"
      "v_mfma_f32_16x16x32_bf16 v[52:55], a[0:3], a[192:195], 0\n"
      "s_nop 0\n"
      "ds_read_b128 a[208:211], v4 offset:50752\n"
      "ds_write_addtid_b32 v38 offset:19968\n"
      "v_mfma_f32_16x16x32_bf16 v[56:59], a[4:7], a[192:195], 0\n"
      "s_waitcnt lgkmcnt(5)\n"
      "ds_write_addtid_b32 v76 offset:0\n"
      "ds_write_addtid_b32 v77 offset:512\n"
      "v_mfma_f32_4x4x4_16b_bf16 v[20:23], a[216:217], a[224:225], 0\n"
      "v_sub_f32 v78, v90, v78\n"
      "v_mfma_f32_4x4x4_16b_bf16 v[24:27], a[216:217], a[226:227], 0\n"
      "ds_write_addtid_b32 v78 offset:1024\n"
      "v_sub_f32 v79, v91, v79\n"
      "v_mfma_f32_16x16x32_bf16 v[60:63], a[8:11], a[192:195], 0\n"
      "ds_write_addtid_b32 v79 offset:1536\n"
      "v_sub_f32 v80, v92, v80\n"
      "v_sub_f32 v81, v93, v81\n"
      "v_mfma_f32_16x16x32_bf16 v[52:55], a[12:15], a[196:199], v[52:55]\n"
      "ds_read_b128 a[212:215], v4 offset:50816\n"
      "ds_write_addtid_b32 v80 offset:2048\n"
      "v_mfma_f32_16x16x32_bf16 v[56:59], a[16:19], a[196:199], v[56:59]\n"
      "ds_write_addtid_b32 v81 offset:2560\n"
      "v_sub_f32 v82, v94, v82\n"
      "v_mfma_f32_16x16x32_bf16 v[60:63], a[20:23], a[196:199], v[60:63]\n"
      "s_nop 0\n"
      "ds_write_addtid_b32 v82 offset:3072\n"
      "v_cvt_pk_bf16_f32 v20, v20, v24\n"
      "v_mfma_f32_16x16x32_bf16 v[52:55], a[24:27], a[200:203], v[52:55]\n"
      "ds_read_b128 a[192:195], v5 offset:50688\n"
      "ds_write_addtid_b32 v20 offset:43776\n"
      "v_sub_f32 v83, v95, v83\n"
      "v_mfma_f32_16x16x32_bf16 v[56:59], a[28:31], a[200:203], v[56:59]\n"
      "ds_write_addtid_b32 v83 offset:3584\n"
      "v_sub_f32_e64 v84, v96, v84\n"
      "v_mfma_f32_16x16x32_bf16 v[60:63], a[32:35], a[200:203], v[60:63]\n"
      "s_waitcnt lgkmcnt(5)\n"
      "ds_write_addtid_b32 v84 offset:4096\n"
      "v_cvt_pk_bf16_f32 v21, v21, v25\n"
      "v_mfma_f32_16x16x32_bf16 v[52:55], a[36:39], a[204:207], v[52:55]\n"
      "ds_read_b128 a[196:199], v5 offset:50752\n"
      "ds_write_addtid_b32 v21 offset:47872\n"
      "v_mfma_f32_16x16x32_bf16 v[56:59], a[40:43], a[204:207], v[56:59]\n"
      "v_sub_f32 v85, v97, v85\n"
      "v_sub_f32 v86, v98, v86\n"
      "v_cvt_pk_bf16_f32 v22, v22, v26\n"
      "v_mfma_f32_16x16x32_bf16 v[60:63], a[44:47], a[204:207], v[60:63]\n"
      "ds_write_addtid_b32 v22 offset:51968\n"
      "ds_write_addtid_b32 v85 offset:4608\n"
      "v_sub_f32 v87, v99, v87\n"
      "v_mfma_f32_16x16x32_bf16 v[52:55], a[48:51], a[208:211], v[52:55]\n"
      "ds_read_b128 a[200:203], v5 offset:50816\n"
      "ds_write_addtid_b32 v86 offset:5120\n"
      "v_mfma_f32_16x16x32_bf16 v[56:59], a[52:55], a[208:211], v[56:59]\n"
      "s_nop 0\n"
      "ds_write_addtid_b32 v87 offset:5632\n"
      "v_cvt_pk_bf16_f32 v23, v23, v27\n"
      "v_mfma_f32_16x16x32_bf16 v[60:63], a[56:59], a[208:211], v[60:63]\n"
      "ds_write_addtid_b32 v23 offset:56064\n"
      "v_fma_f32 v40, 0.5, v76, -v88\n"
      "v_mfma_f32_16x16x32_bf16 v[52:55], a[60:63], a[212:215], v[52:55]\n"
      "v_fma_f32 v41, 0.5, v77, -v89\n"
      "v_fma_f32 v42, 0.5, v78, -v90\n"
      "v_mfma_f32_16x16x32_bf16 v[56:59], a[64:67], a[212:215], v[56:59]\n"
      "v_fma_f32 v43, 0.5, v79, -v91\n"
      "v_fma_f32 v44, 0.5, v80, -v92\n"
      "v_mfma_f32_16x16x32_bf16 v[60:63], a[68:71], a[212:215], v[60:63]\n"
      "s_waitcnt lgkmcnt(0)\n"
      "v_fma_f32 v45, 0.5, v81, -v93\n"
      "s_barrier\n"
      "v_mfma_f32_16x16x32_bf16 v[52:55], a[72:75], a[192:195], v[52:55]\n"
      "ds_read_b128 a[204:207], v0 offset:16384\n"
      "ds_read2st64_b32 v[28:29], v6 offset0:0 offset1:2\n"
      "v_fma_f32 v46, 0.5, v82, -v94\n"
      "v_mfma_f32_16x16x32_bf16 v[56:59], a[76:79], a[192:195], v[56:59]\n"
      "ds_read_b128 a[208:211], v0 offset:20480\n"
      "ds_read2st64_b32 v[30:31], v6 offset0:4 offset1:6\n"
      "v_mfma_f32_16x16x32_bf16 v[60:63], a[80:83], a[192:195], v[60:63]\n"
      "ds_read_b64_tr_b16 a[220:221], v7 offset:0\n"
      "v_fma_f32 v47, 0.5, v83, -v95\n"
      "v_mfma_f32_16x16x32_bf16 v[52:55], a[84:87], a[196:199], v[52:55]\n"
      "ds_read_b128 a[212:215], v0 offset:16448\n"
      "v_fma_f32 v48, 0.5, v84, -v96\n"
      "v_mfma_f32_16x16x32_bf16 v[56:59], a[88:91], a[196:199], v[56:59]\n"
      "ds_read_b64_tr_b16 a[222:223], v7 offset:8\n"
      "ds_read2st64_b32 v[32:33], v6 offset0:8 offset1:10\n"
      "v_fma_f32 v49, 0.5, v85, -v97\n"
      "v_mfma_f32_16x16x32_bf16 v[60:63], a[92:95], a[196:199], v[60:63]\n"
      "ds_read2st64_b32 v[34:35], v6 offset0:12 offset1:14\n"
      "v_fma_f32 v50, 0.5, v86, -v98\n"
      "v_mfma_f32_16x16x32_bf16 v[52:55], a[96:99], a[200:203], v[52:55]\n"
      "ds_read_b128 a[192:195], v0 offset:20544\n"
      "ds_read2st64_b32 v[36:37], v6 offset0:16 offset1:18\n"
      "v_mfma_f32_16x16x32_bf16 v[56:59], a[100:103], a[200:203], v[56:59]\n"
      "ds_read2st64_b32 v[38:39], v6 offset0:20 offset1:22\n"
      "v_fma_f32 v51, 0.5, v87, -v99\n"
      "v_mfma_f32_16x16x32_bf16 v[60:63], a[104:107], a[200:203], v[60:63]\n"
      "s_waitcnt lgkmcnt(8)\n"
      "v_fmac_f32 v28, s46, v64\n"
      "v_fmac_f32 v29, s46, v65\n"
      "v_fmac_f32 v30, s46, v66\n"
      "v_mfma_f32_16x16x32_bf16 v[76:79], v[252:255], a[204:207], v[52:55]\n"
      "s_waitcnt lgkmcnt(5)\n"
      "ds_read_b128 a[196:199], v0 offset:16512\n"
      "v_fmac_f32 v31, s46, v67\n"
      "v_mfma_f32_4x4x4_16b_bf16 v[12:15], a[216:217], a[220:221], 0\n"
      "v_sub_f32_e64 v28, v28, v40\n"
      "v_mfma_f32_4x4x4_16b_bf16 v[16:19], a[216:217], a[222:223], 0\n"
      "v_sub_f32 v29, v29, v41\n"
      "v_sub_f32 v30, v30, v42\n"
      "v_mfma_f32_16x16x32_bf16 v[80:83], v[244:247], a[204:207], v[56:59]\n"
      "v_sub_f32 v31, v31, v43\n"
      "v_fmac_f32 v40, s45, v28\n"
      "v_mfma_f32_16x16x32_bf16 v[84:87], v[236:239], a[204:207], v[60:63]\n"
      "v_fmac_f32 v41, s45, v29\n"
      "v_cvt_pk_bf16_f32 v12, v12, v16\n"
      "v_fmac_f32 v42, s45, v30\n"
      "v_mfma_f32_16x16x32_bf16 v[88:91], v[248:251], a[208:211], v[52:55]\n"
      "ds_read_b128 a[200:203], v0 offset:20608\n"
      "ds_write_addtid_b32 v12 offset:24832\n"
      "v_mfma_f32_16x16x32_bf16 v[92:95], v[240:243], a[208:211], v[56:59]\n"
      "ds_read_b64_tr_b16 a[224:225], v7 offset:512\n"
      "ds_read_b64_tr_b16 a[226:227], v7 offset:520\n"
      "v_mfma_f32_16x16x32_bf16 v[96:99], v[232:235], a[208:211], v[60:63]\n"
      "v_fmac_f32_e64 v43, s45, v31\n"
      "v_cvt_pk_bf16_f32 v13, v13, v17\n"
      "v_mfma_f32_16x16x32_bf16 v[76:79], v[228:231], a[212:215], v[76:79]\n"
      "s_nop 0\n"
      "ds_read_b128 a[204:207], v1 offset:16384\n"
      "ds_write_addtid_b32 v13 offset:28928\n"
      "v_fma_f32 v28, s47, v28, v52\n"
      "v_mfma_f32_16x16x32_bf16 v[80:83], v[220:223], a[212:215], v[80:83]\n"
      "s_nop 0\n"
      "v_fma_f32 v29, s47, v29, v53\n"
      "v_cvt_pk_bf16_f32 v14, v14, v18\n"
      "v_mfma_f32_16x16x32_bf16 v[84:87], v[212:215], a[212:215], v[84:87]\n"
      "s_waitcnt lgkmcnt(6)\n"
      "ds_write_addtid_b32 v14 offset:33024\n"
      "v_fma_f32 v30, s47, v30, v54\n"
      "v_mfma_f32_16x16x32_bf16 v[88:91], v[224:227], a[192:195], v[88:91]\n"
      "s_nop 0\n"
      "ds_read_b128 a[208:211], v1 offset:20480\n"
      "v_fma_f32 v31, s47, v31, v55\n"
      "v_mfma_f32_16x16x32_bf16 v[92:95], v[216:219], a[192:195], v[92:95]\n"
      "s_nop 0\n"
      "v_cvt_pk_bf16_f32 v40, v40, v41\n"
      "v_cvt_pk_bf16_f32 v15, v15, v19\n"
      "v_mfma_f32_16x16x32_bf16 v[96:99], v[208:211], a[192:195], v[96:99]\n"
      "s_waitcnt lgkmcnt(4)\n"
      "ds_write_addtid_b32 v40 offset:12288\n"
      "ds_write_addtid_b32 v15 offset:37120\n"
      "v_cvt_pk_bf16_f32 v42, v42, v43\n"
      "v_mfma_f32_4x4x4_16b_bf16 v[20:23], a[216:217], a[224:225], 0\n"
      "v_cvt_pk_bf16_f32 v28, v28, v29\n"
      "v_mfma_f32_4x4x4_16b_bf16 v[24:27], a[216:217], a[226:227], 0\n"
      "ds_write_addtid_b32 v42 offset:12800\n"
      "ds_write_addtid_b32 v28 offset:14336\n"
      "v_mfma_f32_16x16x32_bf16 v[76:79], v[204:207], a[196:199], v[76:79]\n"
      "ds_read_b128 a[212:215], v1 offset:16448\n"
      "v_cvt_pk_bf16_f32 v30, v30, v31\n"
      "v_mfma_f32_16x16x32_bf16 v[80:83], v[196:199], a[196:199], v[80:83]\n"
      "ds_write_addtid_b32 v30 offset:14848\n"
      "v_fmac_f32 v32, s46, v68\n"
      "v_cvt_pk_bf16_f32 v20, v20, v24\n"
      "v_mfma_f32_16x16x32_bf16 v[84:87], v[188:191], a[196:199], v[84:87]\n"
      "ds_write_addtid_b32 v20 offset:25344\n"
      "v_fmac_f32 v33, s46, v69\n"
      "v_mfma_f32_16x16x32_bf16 v[88:91], v[200:203], a[200:203], v[88:91]\n"
      "ds_read_b128 a[192:195], v1 offset:20544\n"
      "ds_read_b64_tr_b16 a[228:229], v7 offset:1024\n"
      "v_mfma_f32_16x16x32_bf16 v[92:95], v[192:195], a[200:203], v[92:95]\n"
      "ds_read_b64_tr_b16 a[230:231], v7 offset:1032\n"
      "v_fmac_f32 v34, s46, v70\n"
      "v_mfma_f32_16x16x32_bf16 v[96:99], v[184:187], a[200:203], v[96:99]\n"
      "s_waitcnt lgkmcnt(4)\n"
      "v_fmac_f32 v35, s46, v71\n"
      "v_cvt_pk_bf16_f32 v21, v21, v25\n"
      "v_mfma_f32_16x16x32_bf16 v[76:79], v[180:183], a[204:207], v[76:79]\n"
      "s_nop 0\n"
      "ds_read_b128 a[196:199], v1 offset:16512\n"
      "ds_write_addtid_b32 v21 offset:29440\n"
      "v_sub_f32 v32, v32, v44\n"
      "v_mfma_f32_16x16x32_bf16 v[80:83], v[172:175], a[204:207], v[80:83]\n"
      "v_sub_f32 v33, v33, v45\n"
      "v_cvt_pk_bf16_f32 v22, v22, v26\n"
      "v_mfma_f32_16x16x32_bf16 v[84:87], v[164:167], a[204:207], v[84:87]\n"
      "ds_write_addtid_b32 v22 offset:33536\n"
      "v_sub_f32 v34, v34, v46\n"
      "v_mfma_f32_16x16x32_bf16 v[88:91], v[176:179], a[208:211], v[88:91]\n"
      "s_nop 0\n"
      "ds_read_b128 a[200:203], v1 offset:20608\n"
      "v_sub_f32 v35, v35, v47\n"
      "v_mfma_f32_16x16x32_bf16 v[92:95], v[168:171], a[208:211], v[92:95]\n"
      "s_waitcnt lgkmcnt(4)\n"
      "v_fmac_f32 v44, s45, v32\n"
      "v_cvt_pk_bf16_f32 v23, v23, v27\n"
      "v_mfma_f32_4x4x4_16b_bf16 v[12:15], a[216:217], a[228:229], 0\n"
      "v_fmac_f32 v45, s45, v33\n"
      "v_mfma_f32_4x4x4_16b_bf16 v[16:19], a[216:217], a[230:231], 0\n"
      "ds_write_addtid_b32 v23 offset:37632\n"
      "v_fmac_f32 v46, s45, v34\n"
      "v_mfma_f32_16x16x32_bf16 v[96:99], v[160:163], a[208:211], v[96:99]\n"
      "v_fmac_f32 v47, s45, v35\n"
      "v_fma_f32 v32, s47, v32, v56\n"
      "v_cvt_pk_bf16_f32 v12, v12, v16\n"
      "v_mfma_f32_16x16x32_bf16 v[76:79], v[156:159], a[212:215], v[76:79]\n"
      "ds_read_b128 a[204:207], v2 offset:16384\n"
      "ds_write_addtid_b32 v12 offset:25856\n"
      "v_mfma_f32_16x16x32_bf16 v[80:83], v[148:151], a[212:215], v[80:83]\n"
      "v_fma_f32 v33, s47, v33, v57\n"
      "v_cvt_pk_bf16_f32 v13, v13, v17\n"
      "v_mfma_f32_16x16x32_bf16 v[84:87], v[140:143], a[212:215], v[84:87]\n"
      "ds_read_b64_tr_b16 a[232:233], v7 offset:1536\n"
      "ds_write_addtid_b32 v13 offset:29952\n"
      "v_fma_f32 v34, s47, v34, v58\n"
      "v_mfma_f32_16x16x32_bf16 v[88:91], v[152:155], a[192:195], v[88:91]\n"
      "ds_read_b128 a[208:211], v2 offset:20480\n"
      "ds_read_b64_tr_b16 a[234:235], v7 offset:1544\n"
      "v_mfma_f32_16x16x32_bf16 v[92:95], v[144:147], a[192:195], v[92:95]\n"
      "s_nop 0\n"
      "v_fma_f32 v35, s47, v35, v59\n"
      "v_cvt_pk_bf16_f32 v44, v44, v45\n"
      "v_mfma_f32_16x16x32_bf16 v[96:99], v[136:139], a[192:195], v[96:99]\n"
      "s_waitcnt lgkmcnt(4)\n"
      "ds_write_addtid_b32 v44 offset:20484\n"
      "v_cvt_pk_bf16_f32 v14, v14, v18\n"
      "v_mfma_f32_16x16x32_bf16 v[76:79], v[132:135], a[196:199], v[76:79]\n"
      "ds_read_b128 a[212:215], v2 offset:16448\n"
      "ds_write_addtid_b32 v14 offset:34048\n"
      "v_cvt_pk_bf16_f32 v46, v46, v47\n"
      "v_mfma_f32_16x16x32_bf16 v[80:83], v[124:127], a[196:199], v[80:83]\n"
      "ds_write_addtid_b32 v46 offset:20996\n"
      "v_cvt_pk_bf16_f32 v32, v32, v33\n"
      "v_mfma_f32_16x16x32_bf16 v[84:87], v[116:119], a[196:199], v[84:87]\n"
      "ds_write_addtid_b32 v32 offset:22532\n"
      "v_cvt_pk_bf16_f32 v34, v34, v35\n"
      "v_mfma_f32_16x16x32_bf16 v[88:91], v[128:131], a[200:203], v[88:91]\n"
      "s_waitcnt lgkmcnt(4)\n"
      "ds_read_b128 a[192:195], v2 offset:20544\n"
      "ds_write_addtid_b32 v34 offset:23044\n"
      "v_fmac_f32 v36, s46, v72\n"
      "v_mfma_f32_4x4x4_16b_bf16 v[20:23], a[216:217], a[232:233], 0\n"
      "v_fmac_f32_e64 v37, s46, v73\n"
      "v_mfma_f32_4x4x4_16b_bf16 v[24:27], a[216:217], a[234:235], 0\n"
      "v_fmac_f32 v38, s46, v74\n"
      "v_cvt_pk_bf16_f32 v15, v15, v19\n"
      "v_mfma_f32_16x16x32_bf16 v[92:95], v[120:123], a[200:203], v[92:95]\n"
      "ds_write_addtid_b32 v15 offset:38144\n"
      "v_fmac_f32 v39, s46, v75\n"
      "v_cvt_pk_bf16_f32 v20, v20, v24\n"
      "v_mfma_f32_16x16x32_bf16 v[96:99], v[112:115], a[200:203], v[96:99]\n"
      "ds_write_addtid_b32 v20 offset:26368\n"
      "v_sub_f32 v36, v36, v48\n"
      "v_mfma_f32_16x16x32_bf16 v[76:79], v[108:111], a[204:207], v[76:79]\n"
      "ds_read_b128 a[196:199], v2 offset:16512\n"
      "v_sub_f32 v37, v37, v49\n"
      "v_mfma_f32_16x16x32_bf16 v[80:83], v[100:103], a[204:207], v[80:83]\n"
      "ds_read_b64_tr_b16 a[220:221], v7 offset:2048\n"
      "ds_read_b64_tr_b16 a[222:223], v7 offset:2056\n"
      "v_mfma_f32_16x16x32_bf16 v[84:87], a[160:163], a[204:207], v[84:87]\n"
      "v_sub_f32_e64 v38, v38, v50\n"
      "v_cvt_pk_bf16_f32 v21, v21, v25\n"
      "v_mfma_f32_16x16x32_bf16 v[88:91], v[104:107], a[208:211], v[88:91]\n"
      "ds_read_b128 a[200:203], v2 offset:20608\n"
      "ds_write_addtid_b32 v21 offset:30464\n"
      "v_sub_f32 v39, v39, v51\n"
      "v_mfma_f32_16x16x32_bf16 v[92:95], a[164:167], a[208:211], v[92:95]\n"
      "s_nop 0\n"
      "v_fmac_f32_e64 v48, s45, v36\n"
      "v_cvt_pk_bf16_f32 v22, v22, v26\n"
      "v_mfma_f32_16x16x32_bf16 v[96:99], a[156:159], a[208:211], v[96:99]\n"
      "s_waitcnt lgkmcnt(4)\n"
      "ds_write_addtid_b32 v22 offset:34560\n"
      "v_fmac_f32 v49, s45, v37\n"
      "v_mfma_f32_16x16x32_bf16 v[76:79], a[152:155], a[212:215], v[76:79]\n"
      "s_nop 0\n"
      "ds_read_b128 a[204:207], v3 offset:0\n"
      "v_fmac_f32_e64 v50, s45, v38\n"
      "v_mfma_f32_16x16x32_bf16 v[80:83], a[144:147], a[212:215], v[80:83]\n"
      "s_nop 0\n"
      "v_fmac_f32 v51, s45, v39\n"
      "v_cvt_pk_bf16_f32 v23, v23, v27\n"
      "v_mfma_f32_16x16x32_bf16 v[84:87], a[136:139], a[212:215], v[84:87]\n"
      "s_waitcnt lgkmcnt(2)\n"
      "ds_write_addtid_b32 v23 offset:38656\n"
      "v_fma_f32 v36, s47, v36, v60\n"
      "v_mfma_f32_4x4x4_16b_bf16 v[12:15], a[216:217], a[220:221], 0\n"
      "v_fma_f32 v37, s47, v37, v61\n"
      "v_mfma_f32_4x4x4_16b_bf16 v[16:19], a[216:217], a[222:223], 0\n"
      "v_fma_f32 v38, s47, v38, v62\n"
      "v_fma_f32 v39, s47, v39, v63\n"
      "v_mfma_f32_16x16x32_bf16 v[88:91], a[148:151], a[192:195], v[88:91]\n"
      "ds_read_b128 a[208:211], v3 offset:64\n"
      "v_cvt_pk_bf16_f32 v48, v48, v49\n"
      "v_cvt_pk_bf16_f32 v12, v12, v16\n"
      "v_mfma_f32_16x16x32_bf16 v[92:95], a[140:143], a[192:195], v[92:95]\n"
      "ds_write_addtid_b32 v48 offset:16384\n"
      "ds_write_addtid_b32 v12 offset:26880\n"
      "v_mfma_f32_16x16x32_bf16 v[96:99], a[132:135], a[192:195], v[96:99]\n"
      "v_cvt_pk_bf16_f32 v13, v13, v17\n"
      "v_cvt_pk_bf16_f32 v14, v14, v18\n"
      "v_mfma_f32_16x16x32_bf16 v[76:79], a[128:131], a[196:199], v[76:79]\n"
      "ds_read_b128 a[212:215], v3 offset:128\n"
      "ds_write_addtid_b32 v13 offset:30976\n"
      "v_mfma_f32_16x16x32_bf16 v[80:83], a[120:123], a[196:199], v[80:83]\n"
      "ds_read_b64_tr_b16 a[224:225], v7 offset:2560\n"
      "ds_write_addtid_b32 v14 offset:35072\n"
      "v_cvt_pk_bf16_f32 v50, v50, v51\n"
      "v_mfma_f32_16x16x32_bf16 v[84:87], a[112:115], a[196:199], v[84:87]\n"
      "ds_write_addtid_b32 v50 offset:16896\n"
      "ds_read_b64_tr_b16 a[226:227], v7 offset:2568\n"
      "v_mfma_f32_16x16x32_bf16 v[88:91], a[124:127], a[200:203], v[88:91]\n"
      "ds_read_b128 a[192:195], v4 offset:0\n"
      "v_cvt_pk_bf16_f32 v36, v36, v37\n"
      "v_mfma_f32_16x16x32_bf16 v[92:95], a[116:119], a[200:203], v[92:95]\n"
      "ds_write_addtid_b32 v36 offset:18432\n"
      "v_cvt_pk_bf16_f32 v15, v15, v19\n"
      "v_mfma_f32_16x16x32_bf16 v[96:99], a[108:111], a[200:203], v[96:99]\n"
      "s_waitcnt lgkmcnt(6)\n"
      "ds_write_addtid_b32 v15 offset:39168\n"
      "v_sub_f32 v76, v88, v76\n"
      "v_sub_f32_e64 v77, v89, v77\n"
      "v_cvt_pk_bf16_f32 v38, v38, v39\n"
      "v_mfma_f32_16x16x32_bf16 v[64:67], a[0:3], a[204:207], 0\n"
      "s_nop 0\n"
      "ds_read_b128 a[196:199], v4 offset:64\n"
      "ds_write_addtid_b32 v38 offset:18944\n"
      "v_mfma_f32_16x16x32_bf16 v[68:71], a[4:7], a[204:207], 0\n"
      "s_waitcnt lgkmcnt(5)\n"
      "ds_write_addtid_b32 v76 offset:6144\n"
      "ds_write_addtid_b32 v77 offset:6656\n"
      "v_mfma_f32_4x4x4_16b_bf16 v[20:23], a[216:217], a[224:225], 0\n"
      "v_sub_f32 v78, v90, v78\n"
      "v_mfma_f32_4x4x4_16b_bf16 v[24:27], a[216:217], a[226:227], 0\n"
      "ds_write_addtid_b32 v78 offset:7168\n"
      "v_sub_f32 v79, v91, v79\n"
      "v_mfma_f32_16x16x32_bf16 v[72:75], a[8:11], a[204:207], 0\n"
      "ds_write_addtid_b32 v79 offset:7680\n"
      "v_sub_f32 v80, v92, v80\n"
      "v_sub_f32 v81, v93, v81\n"
      "v_mfma_f32_16x16x32_bf16 v[64:67], a[12:15], a[208:211], v[64:67]\n"
      "ds_read_b128 a[200:203], v4 offset:128\n"
      "ds_write_addtid_b32 v80 offset:8192\n"
      "v_mfma_f32_16x16x32_bf16 v[68:71], a[16:19], a[208:211], v[68:71]\n"
      "ds_write_addtid_b32 v81 offset:8704\n"
      "v_sub_f32 v82, v94, v82\n"
      "v_mfma_f32_16x16x32_bf16 v[72:75], a[20:23], a[208:211], v[72:75]\n"
      "s_nop 0\n"
      "ds_write_addtid_b32 v82 offset:9216\n"
      "v_cvt_pk_bf16_f32 v20, v20, v24\n"
      "v_mfma_f32_16x16x32_bf16 v[64:67], a[24:27], a[212:215], v[64:67]\n"
      "ds_read_b128 a[204:207], v5 offset:0\n"
      "ds_write_addtid_b32 v20 offset:27392\n"
      "v_sub_f32 v83, v95, v83\n"
      "v_mfma_f32_16x16x32_bf16 v[68:71], a[28:31], a[212:215], v[68:71]\n"
      "ds_write_addtid_b32 v83 offset:9728\n"
      "v_sub_f32_e64 v84, v96, v84\n"
      "v_mfma_f32_16x16x32_bf16 v[72:75], a[32:35], a[212:215], v[72:75]\n"
      "s_waitcnt lgkmcnt(5)\n"
      "ds_write_addtid_b32 v84 offset:10240\n"
      "v_cvt_pk_bf16_f32 v21, v21, v25\n"
      "v_mfma_f32_16x16x32_bf16 v[64:67], a[36:39], a[192:195], v[64:67]\n"
      "ds_read_b128 a[208:211], v5 offset:64\n"
      "ds_write_addtid_b32 v21 offset:31488\n"
      "v_mfma_f32_16x16x32_bf16 v[68:71], a[40:43], a[192:195], v[68:71]\n"
      "v_sub_f32 v85, v97, v85\n"
      "v_sub_f32 v86, v98, v86\n"
      "v_cvt_pk_bf16_f32 v22, v22, v26\n"
      "v_mfma_f32_16x16x32_bf16 v[72:75], a[44:47], a[192:195], v[72:75]\n"
      "ds_write_addtid_b32 v22 offset:35584\n"
      "ds_write_addtid_b32 v85 offset:10752\n"
      "v_sub_f32 v87, v99, v87\n"
      "v_mfma_f32_16x16x32_bf16 v[64:67], a[48:51], a[196:199], v[64:67]\n"
      "ds_read_b128 a[212:215], v5 offset:128\n"
      "ds_write_addtid_b32 v86 offset:11264\n"
      "v_mfma_f32_16x16x32_bf16 v[68:71], a[52:55], a[196:199], v[68:71]\n"
      "s_nop 0\n"
      "ds_write_addtid_b32 v87 offset:11776\n"
      "v_cvt_pk_bf16_f32 v23, v23, v27\n"
      "v_mfma_f32_16x16x32_bf16 v[72:75], a[56:59], a[196:199], v[72:75]\n"
      "ds_write_addtid_b32 v23 offset:39680\n"
      "v_fma_f32 v40, 0.5, v76, -v88\n"
      "v_mfma_f32_16x16x32_bf16 v[64:67], a[60:63], a[200:203], v[64:67]\n"
      "v_fma_f32 v41, 0.5, v77, -v89\n"
      "v_fma_f32 v42, 0.5, v78, -v90\n"
      "v_mfma_f32_16x16x32_bf16 v[68:71], a[64:67], a[200:203], v[68:71]\n"
      "v_fma_f32 v43, 0.5, v79, -v91\n"
      "v_fma_f32 v44, 0.5, v80, -v92\n"
      "v_mfma_f32_16x16x32_bf16 v[72:75], a[68:71], a[200:203], v[72:75]\n"
      "s_waitcnt lgkmcnt(0)\n"
      "v_fma_f32 v45, 0.5, v81, -v93\n"
      "s_barrier\n"
      "v_mfma_f32_16x16x32_bf16 v[64:67], a[72:75], a[204:207], v[64:67]\n"
      "ds_read_b128 a[192:195], v0 offset:0\n"
      "ds_read2st64_b32 v[28:29], v6 offset0:24 offset1:26\n"
      "v_fma_f32 v46, 0.5, v82, -v94\n"
      "v_mfma_f32_16x16x32_bf16 v[68:71], a[76:79], a[204:207], v[68:71]\n"
      "ds_read_b128 a[196:199], v0 offset:4096\n"
      "ds_read2st64_b32 v[30:31], v6 offset0:28 offset1:30\n"
      "v_mfma_f32_16x16x32_bf16 v[72:75], a[80:83], a[204:207], v[72:75]\n"
      "ds_read_b64_tr_b16 a[220:221], v7 offset:16896\n"
      "v_fma_f32 v47, 0.5, v83, -v95\n"
      "v_mfma_f32_16x16x32_bf16 v[64:67], a[84:87], a[208:211], v[64:67]\n"
      "ds_read_b128 a[200:203], v0 offset:64\n"
      "v_fma_f32 v48, 0.5, v84, -v96\n"
      "v_mfma_f32_16x16x32_bf16 v[68:71], a[88:91], a[208:211], v[68:71]\n"
      "ds_read_b64_tr_b16 a[222:223], v7 offset:16904\n"
      "ds_read2st64_b32 v[32:33], v6 offset0:32 offset1:34\n"
      "v_fma_f32 v49, 0.5, v85, -v97\n"
      "v_mfma_f32_16x16x32_bf16 v[72:75], a[92:95], a[208:211], v[72:75]\n"
      "ds_read2st64_b32 v[34:35], v6 offset0:36 offset1:38\n"
      "v_fma_f32 v50, 0.5, v86, -v98\n"
      "v_mfma_f32_16x16x32_bf16 v[64:67], a[96:99], a[212:215], v[64:67]\n"
      "ds_read_b128 a[204:207], v0 offset:4160\n"
      "ds_read2st64_b32 v[36:37], v6 offset0:40 offset1:42\n"
      "v_mfma_f32_16x16x32_bf16 v[68:71], a[100:103], a[212:215], v[68:71]\n"
      "ds_read2st64_b32 v[38:39], v6 offset0:44 offset1:46\n"
      "v_fma_f32 v51, 0.5, v87, -v99\n"
      "v_mfma_f32_16x16x32_bf16 v[72:75], a[104:107], a[212:215], v[72:75]\n"
      "s_waitcnt lgkmcnt(8)\n"
      "v_fmac_f32 v28, s46, v52\n"
      "v_fmac_f32 v29, s46, v53\n"
      "v_fmac_f32 v30, s46, v54\n"
      "v_mfma_f32_16x16x32_bf16 v[76:79], v[252:255], a[192:195], v[64:67]\n"
      "s_waitcnt lgkmcnt(5)\n"
      "ds_read_b128 a[208:211], v0 offset:128\n"
      "v_fmac_f32 v31, s46, v55\n"
      "v_mfma_f32_4x4x4_16b_bf16 v[12:15], a[216:217], a[220:221], 0\n"
      "v_sub_f32_e64 v28, v28, v40\n"
      "v_mfma_f32_4x4x4_16b_bf16 v[16:19], a[216:217], a[222:223], 0\n"
      "v_sub_f32 v29, v29, v41\n"
      "v_sub_f32 v30, v30, v42\n"
      "v_mfma_f32_16x16x32_bf16 v[80:83], v[244:247], a[192:195], v[68:71]\n"
      "v_sub_f32 v31, v31, v43\n"
      "v_fmac_f32 v40, s45, v28\n"
      "v_mfma_f32_16x16x32_bf16 v[84:87], v[236:239], a[192:195], v[72:75]\n"
      "v_fmac_f32 v41, s45, v29\n"
      "v_cvt_pk_bf16_f32 v12, v12, v16\n"
      "v_fmac_f32 v42, s45, v30\n"
      "v_mfma_f32_16x16x32_bf16 v[88:91], v[248:251], a[196:199], v[64:67]\n"
      "ds_read_b128 a[212:215], v0 offset:4224\n"
      "ds_write_addtid_b32 v12 offset:41216\n"
      "v_mfma_f32_16x16x32_bf16 v[92:95], v[240:243], a[196:199], v[68:71]\n"
      "ds_read_b64_tr_b16 a[224:225], v7 offset:17408\n"
      "ds_read_b64_tr_b16 a[226:227], v7 offset:17416\n"
      "v_mfma_f32_16x16x32_bf16 v[96:99], v[232:235], a[196:199], v[72:75]\n"
      "v_fmac_f32_e64 v43, s45, v31\n"
      "v_cvt_pk_bf16_f32 v13, v13, v17\n"
      "v_mfma_f32_16x16x32_bf16 v[76:79], v[228:231], a[200:203], v[76:79]\n"
      "s_nop 0\n"
      "ds_read_b128 a[192:195], v1 offset:0\n"
      "ds_write_addtid_b32 v13 offset:45312\n"
      "v_fma_f32 v28, s47, v28, v64\n"
      "v_mfma_f32_16x16x32_bf16 v[80:83], v[220:223], a[200:203], v[80:83]\n"
      "s_nop 0\n"
      "v_fma_f32 v29, s47, v29, v65\n"
      "v_cvt_pk_bf16_f32 v14, v14, v18\n"
      "v_mfma_f32_16x16x32_bf16 v[84:87], v[212:215], a[200:203], v[84:87]\n"
      "s_waitcnt lgkmcnt(6)\n"
      "ds_write_addtid_b32 v14 offset:49408\n"
      "v_fma_f32 v30, s47, v30, v66\n"
      "v_mfma_f32_16x16x32_bf16 v[88:91], v[224:227], a[204:207], v[88:91]\n"
      "s_nop 0\n"
      "ds_read_b128 a[196:199], v1 offset:4096\n"
      "v_fma_f32 v31, s47, v31, v67\n"
      "v_mfma_f32_16x16x32_bf16 v[92:95], v[216:219], a[204:207], v[92:95]\n"
      "s_nop 0\n"
      "v_cvt_pk_bf16_f32 v40, v40, v41\n"
      "v_cvt_pk_bf16_f32 v15, v15, v19\n"
      "v_mfma_f32_16x16x32_bf16 v[96:99], v[208:211], a[204:207], v[96:99]\n"
      "s_waitcnt lgkmcnt(4)\n"
      "ds_write_addtid_b32 v40 offset:13312\n"
      "ds_write_addtid_b32 v15 offset:53504\n"
      "v_cvt_pk_bf16_f32 v42, v42, v43\n"
      "v_mfma_f32_4x4x4_16b_bf16 v[20:23], a[216:217], a[224:225], 0\n"
      "v_cvt_pk_bf16_f32 v28, v28, v29\n"
      "v_mfma_f32_4x4x4_16b_bf16 v[24:27], a[216:217], a[226:227], 0\n"
      "ds_write_addtid_b32 v42 offset:13824\n"
      "ds_write_addtid_b32 v28 offset:15360\n"
      "v_mfma_f32_16x16x32_bf16 v[76:79], v[204:207], a[208:211], v[76:79]\n"
      "ds_read_b128 a[200:203], v1 offset:64\n"
      "v_cvt_pk_bf16_f32 v30, v30, v31\n"
      "v_mfma_f32_16x16x32_bf16 v[80:83], v[196:199], a[208:211], v[80:83]\n"
      "ds_write_addtid_b32 v30 offset:15872\n"
      "v_fmac_f32 v32, s46, v56\n"
      "v_cvt_pk_bf16_f32 v20, v20, v24\n"
      "v_mfma_f32_16x16x32_bf16 v[84:87], v[188:191], a[208:211], v[84:87]\n"
      "ds_write_addtid_b32 v20 offset:41728\n"
      "v_fmac_f32 v33, s46, v57\n"
      "v_mfma_f32_16x16x32_bf16 v[88:91], v[200:203], a[212:215], v[88:91]\n"
      "ds_read_b128 a[204:207], v1 offset:4160\n"
      "ds_read_b64_tr_b16 a[228:229], v7 offset:17920\n"
      "v_mfma_f32_16x16x32_bf16 v[92:95], v[192:195], a[212:215], v[92:95]\n"
      "ds_read_b64_tr_b16 a[230:231], v7 offset:17928\n"
      "v_fmac_f32 v34, s46, v58\n"
      "v_mfma_f32_16x16x32_bf16 v[96:99], v[184:187], a[212:215], v[96:99]\n"
      "s_waitcnt lgkmcnt(4)\n"
      "v_fmac_f32 v35, s46, v59\n"
      "v_cvt_pk_bf16_f32 v21, v21, v25\n"
      "v_mfma_f32_16x16x32_bf16 v[76:79], v[180:183], a[192:195], v[76:79]\n"
      "s_nop 0\n"
      "ds_read_b128 a[208:211], v1 offset:128\n"
      "ds_write_addtid_b32 v21 offset:45824\n"
      "v_sub_f32 v32, v32, v44\n"
      "v_mfma_f32_16x16x32_bf16 v[80:83], v[172:175], a[192:195], v[80:83]\n"
      "v_sub_f32 v33, v33, v45\n"
      "v_cvt_pk_bf16_f32 v22, v22, v26\n"
      "v_mfma_f32_16x16x32_bf16 v[84:87], v[164:167], a[192:195], v[84:87]\n"
      "ds_write_addtid_b32 v22 offset:49920\n"
      "v_sub_f32 v34, v34, v46\n"
      "v_mfma_f32_16x16x32_bf16 v[88:91], v[176:179], a[196:199], v[88:91]\n"
      "s_nop 0\n"
      "ds_read_b128 a[212:215], v1 offset:4224\n"
      "v_sub_f32 v35, v35, v47\n"
      "v_mfma_f32_16x16x32_bf16 v[92:95], v[168:171], a[196:199], v[92:95]\n"
      "s_waitcnt lgkmcnt(4)\n"
      "v_fmac_f32 v44, s45, v32\n"
      "v_cvt_pk_bf16_f32 v23, v23, v27\n"
      "v_mfma_f32_4x4x4_16b_bf16 v[12:15], a[216:217], a[228:229], 0\n"
      "v_fmac_f32 v45, s45, v33\n"
      "v_mfma_f32_4x4x4_16b_bf16 v[16:19], a[216:217], a[230:231], 0\n"
      "ds_write_addtid_b32 v23 offset:54016\n"
      "v_fmac_f32 v46, s45, v34\n"
      "v_mfma_f32_16x16x32_bf16 v[96:99], v[160:163], a[196:199], v[96:99]\n"
      "v_fmac_f32 v47, s45, v35\n"
      "v_fma_f32 v32, s47, v32, v68\n"
      "v_cvt_pk_bf16_f32 v12, v12, v16\n"
      "v_mfma_f32_16x16x32_bf16 v[76:79], v[156:159], a[200:203], v[76:79]\n"
      "ds_read_b128 a[192:195], v2 offset:0\n"
      "ds_write_addtid_b32 v12 offset:42240\n"
      "v_mfma_f32_16x16x32_bf16 v[80:83], v[148:151], a[200:203], v[80:83]\n"
      "v_fma_f32 v33, s47, v33, v69\n"
      "v_cvt_pk_bf16_f32 v13, v13, v17\n"
      "v_mfma_f32_16x16x32_bf16 v[84:87], v[140:143], a[200:203], v[84:87]\n"
      "ds_read_b64_tr_b16 a[232:233], v7 offset:18432\n"
      "ds_write_addtid_b32 v13 offset:46336\n"
      "v_fma_f32 v34, s47, v34, v70\n"
      "v_mfma_f32_16x16x32_bf16 v[88:91], v[152:155], a[204:207], v[88:91]\n"
      "ds_read_b128 a[196:199], v2 offset:4096\n"
      "ds_read_b64_tr_b16 a[234:235], v7 offset:18440\n"
      "v_mfma_f32_16x16x32_bf16 v[92:95], v[144:147], a[204:207], v[92:95]\n"
      "s_nop 0\n"
      "v_fma_f32 v35, s47, v35, v71\n"
      "v_cvt_pk_bf16_f32 v44, v44, v45\n"
      "v_mfma_f32_16x16x32_bf16 v[96:99], v[136:139], a[204:207], v[96:99]\n"
      "s_waitcnt lgkmcnt(4)\n"
      "ds_write_addtid_b32 v44 offset:21508\n"
      "v_cvt_pk_bf16_f32 v14, v14, v18\n"
      "v_mfma_f32_16x16x32_bf16 v[76:79], v[132:135], a[208:211], v[76:79]\n"
      "ds_read_b128 a[200:203], v2 offset:64\n"
      "ds_write_addtid_b32 v14 offset:50432\n"
      "v_cvt_pk_bf16_f32 v46, v46, v47\n"
      "v_mfma_f32_16x16x32_bf16 v[80:83], v[124:127], a[208:211], v[80:83]\n"
      "ds_write_addtid_b32 v46 offset:22020\n"
      "v_cvt_pk_bf16_f32 v32, v32, v33\n"
      "v_mfma_f32_16x16x32_bf16 v[84:87], v[116:119], a[208:211], v[84:87]\n"
      "ds_write_addtid_b32 v32 offset:23556\n"
      "v_cvt_pk_bf16_f32 v34, v34, v35\n"
      "v_mfma_f32_16x16x32_bf16 v[88:91], v[128:131], a[212:215], v[88:91]\n"
      "s_waitcnt lgkmcnt(4)\n"
      "ds_read_b128 a[204:207], v2 offset:4160\n"
      "ds_write_addtid_b32 v34 offset:24068\n"
      "v_fmac_f32 v36, s46, v60\n"
      "v_mfma_f32_4x4x4_16b_bf16 v[20:23], a[216:217], a[232:233], 0\n"
      "v_fmac_f32_e64 v37, s46, v61\n"
      "v_mfma_f32_4x4x4_16b_bf16 v[24:27], a[216:217], a[234:235], 0\n"
      "v_fmac_f32 v38, s46, v62\n"
      "v_cvt_pk_bf16_f32 v15, v15, v19\n"
      "v_mfma_f32_16x16x32_bf16 v[92:95], v[120:123], a[212:215], v[92:95]\n"
      "ds_write_addtid_b32 v15 offset:54528\n"
      "v_fmac_f32 v39, s46, v63\n"
      "v_cvt_pk_bf16_f32 v20, v20, v24\n"
      "v_mfma_f32_16x16x32_bf16 v[96:99], v[112:115], a[212:215], v[96:99]\n"
      "ds_write_addtid_b32 v20 offset:42752\n"
      "v_sub_f32 v36, v36, v48\n"
      "v_mfma_f32_16x16x32_bf16 v[76:79], v[108:111], a[192:195], v[76:79]\n"
      "ds_read_b128 a[208:211], v2 offset:128\n"
      "v_sub_f32 v37, v37, v49\n"
      "v_mfma_f32_16x16x32_bf16 v[80:83], v[100:103], a[192:195], v[80:83]\n"
      "ds_read_b64_tr_b16 a[220:221], v7 offset:18944\n"
      "ds_read_b64_tr_b16 a[222:223], v7 offset:18952\n"
      "v_mfma_f32_16x16x32_bf16 v[84:87], a[160:163], a[192:195], v[84:87]\n"
      "v_sub_f32_e64 v38, v38, v50\n"
      "v_cvt_pk_bf16_f32 v21, v21, v25\n"
      "v_mfma_f32_16x16x32_bf16 v[88:91], v[104:107], a[196:199], v[88:91]\n"
      "ds_read_b128 a[212:215], v2 offset:4224\n"
      "ds_write_addtid_b32 v21 offset:46848\n"
      "v_sub_f32 v39, v39, v51\n"
      "v_mfma_f32_16x16x32_bf16 v[92:95], a[164:167], a[196:199], v[92:95]\n"
      "s_nop 0\n"
      "v_fmac_f32_e64 v48, s45, v36\n"
      "v_cvt_pk_bf16_f32 v22, v22, v26\n"
      "v_mfma_f32_16x16x32_bf16 v[96:99], a[156:159], a[196:199], v[96:99]\n"
      "s_waitcnt lgkmcnt(4)\n"
      "ds_write_addtid_b32 v22 offset:50944\n"
      "v_fmac_f32 v49, s45, v37\n"
      "v_mfma_f32_16x16x32_bf16 v[76:79], a[152:155], a[200:203], v[76:79]\n"
      "s_nop 0\n"
      "ds_read_b128 a[192:195], v3 offset:16896\n"
      "v_fmac_f32_e64 v50, s45, v38\n"
      "v_mfma_f32_16x16x32_bf16 v[80:83], a[144:147], a[200:203], v[80:83]\n"
      "s_nop 0\n"
      "v_fmac_f32 v51, s45, v39\n"
      "v_cvt_pk_bf16_f32 v23, v23, v27\n"
      "v_mfma_f32_16x16x32_bf16 v[84:87], a[136:139], a[200:203], v[84:87]\n"
      "s_waitcnt lgkmcnt(2)\n"
      "ds_write_addtid_b32 v23 offset:55040\n"
      "v_fma_f32 v36, s47, v36, v72\n"
      "v_mfma_f32_4x4x4_16b_bf16 v[12:15], a[216:217], a[220:221], 0\n"
      "v_fma_f32 v37, s47, v37, v73\n"
      "v_mfma_f32_4x4x4_16b_bf16 v[16:19], a[216:217], a[222:223], 0\n"
      "v_fma_f32 v38, s47, v38, v74\n"
      "v_fma_f32 v39, s47, v39, v75\n"
      "v_mfma_f32_16x16x32_bf16 v[88:91], a[148:151], a[204:207], v[88:91]\n"
      "ds_read_b128 a[196:199], v3 offset:16960\n"
      "v_cvt_pk_bf16_f32 v48, v48, v49\n"
      "v_cvt_pk_bf16_f32 v12, v12, v16\n"
      "v_mfma_f32_16x16x32_bf16 v[92:95], a[140:143], a[204:207], v[92:95]\n"
      "ds_write_addtid_b32 v48 offset:17408\n"
      "ds_write_addtid_b32 v12 offset:43264\n"
      "v_mfma_f32_16x16x32_bf16 v[96:99], a[132:135], a[204:207], v[96:99]\n"
      "v_cvt_pk_bf16_f32 v13, v13, v17\n"
      "v_cvt_pk_bf16_f32 v14, v14, v18\n"
      "v_mfma_f32_16x16x32_bf16 v[76:79], a[128:131], a[208:211], v[76:79]\n"
      "ds_read_b128 a[200:203], v3 offset:17024\n"
      "ds_write_addtid_b32 v13 offset:47360\n"
      "v_mfma_f32_16x16x32_bf16 v[80:83], a[120:123], a[208:211], v[80:83]\n"
      "ds_read_b64_tr_b16 a[224:225], v7 offset:19456\n"
      "ds_write_addtid_b32 v14 offset:51456\n"
      "v_cvt_pk_bf16_f32 v50, v50, v51\n"
      "v_mfma_f32_16x16x32_bf16 v[84:87], a[112:115], a[208:211], v[84:87]\n"
      "ds_write_addtid_b32 v50 offset:17920\n"
      "ds_read_b64_tr_b16 a[226:227], v7 offset:19464\n"
      "v_mfma_f32_16x16x32_bf16 v[88:91], a[124:127], a[212:215], v[88:91]\n"
      "ds_read_b128 a[204:207], v4 offset:16896\n"
      "v_cvt_pk_bf16_f32 v36, v36, v37\n"
      "v_mfma_f32_16x16x32_bf16 v[92:95], a[116:119], a[212:215], v[92:95]\n"
      "ds_write_addtid_b32 v36 offset:19456\n"
      "v_cvt_pk_bf16_f32 v15, v15, v19\n"
      "v_mfma_f32_16x16x32_bf16 v[96:99], a[108:111], a[212:215], v[96:99]\n"
      "s_waitcnt lgkmcnt(6)\n"
      "ds_write_addtid_b32 v15 offset:55552\n"
      "v_sub_f32 v76, v88, v76\n"
      "v_sub_f32_e64 v77, v89, v77\n"
      "v_cvt_pk_bf16_f32 v38, v38, v39\n"
      "v_mfma_f32_16x16x32_bf16 v[52:55], a[0:3], a[192:195], 0\n"
      "s_nop 0\n"
      "ds_read_b128 a[208:211], v4 offset:16960\n"
      "ds_write_addtid_b32 v38 offset:19968\n"
      "v_mfma_f32_16x16x32_bf16 v[56:59], a[4:7], a[192:195], 0\n"
      "s_waitcnt lgkmcnt(5)\n"
      "ds_write_addtid_b32 v76 offset:0\n"
      "ds_write_addtid_b32 v77 offset:512\n"
      "v_mfma_f32_4x4x4_16b_bf16 v[20:23], a[216:217], a[224:225], 0\n"
      "v_sub_f32 v78, v90, v78\n"
      "v_mfma_f32_4x4x4_16b_bf16 v[24:27], a[216:217], a[226:227], 0\n"
      "ds_write_addtid_b32 v78 offset:1024\n"
      "v_sub_f32 v79, v91, v79\n"
      "v_mfma_f32_16x16x32_bf16 v[60:63], a[8:11], a[192:195], 0\n"
      "ds_write_addtid_b32 v79 offset:1536\n"
      "v_sub_f32 v80, v92, v80\n"
      "v_sub_f32 v81, v93, v81\n"
      "v_mfma_f32_16x16x32_bf16 v[52:55], a[12:15], a[196:199], v[52:55]\n"
      "ds_read_b128 a[212:215], v4 offset:17024\n"
      "ds_write_addtid_b32 v80 offset:2048\n"
      "v_mfma_f32_16x16x32_bf16 v[56:59], a[16:19], a[196:199], v[56:59]\n"
      "ds_write_addtid_b32 v81 offset:2560\n"
      "v_sub_f32 v82, v94, v82\n"
      "v_mfma_f32_16x16x32_bf16 v[60:63], a[20:23], a[196:199], v[60:63]\n"
      "s_nop 0\n"
      "ds_write_addtid_b32 v82 offset:3072\n"
      "v_cvt_pk_bf16_f32 v20, v20, v24\n"
      "v_mfma_f32_16x16x32_bf16 v[52:55], a[24:27], a[200:203], v[52:55]\n"
      "ds_read_b128 a[192:195], v5 offset:16896\n"
      "ds_write_addtid_b32 v20 offset:43776\n"
      "v_sub_f32 v83, v95, v83\n"
      "v_mfma_f32_16x16x32_bf16 v[56:59], a[28:31], a[200:203], v[56:59]\n"
      "ds_write_addtid_b32 v83 offset:3584\n"
      "v_sub_f32_e64 v84, v96, v84\n"
      "v_mfma_f32_16x16x32_bf16 v[60:63], a[32:35], a[200:203], v[60:63]\n"
      "s_waitcnt lgkmcnt(5)\n"
      "ds_write_addtid_b32 v84 offset:4096\n"
      "v_cvt_pk_bf16_f32 v21, v21, v25\n"
      "v_mfma_f32_16x16x32_bf16 v[52:55], a[36:39], a[204:207], v[52:55]\n"
      "ds_read_b128 a[196:199], v5 offset:16960\n"
      "ds_write_addtid_b32 v21 offset:47872\n"
      "v_mfma_f32_16x16x32_bf16 v[56:59], a[40:43], a[204:207], v[56:59]\n"
      "v_sub_f32 v85, v97, v85\n"
      "v_sub_f32 v86, v98, v86\n"
      "v_cvt_pk_bf16_f32 v22, v22, v26\n"
      "v_mfma_f32_16x16x32_bf16 v[60:63], a[44:47], a[204:207], v[60:63]\n"
      "ds_write_addtid_b32 v22 offset:51968\n"
      "ds_write_addtid_b32 v85 offset:4608\n"
      "v_sub_f32 v87, v99, v87\n"
      "v_mfma_f32_16x16x32_bf16 v[52:55], a[48:51], a[208:211], v[52:55]\n"
      "ds_read_b128 a[200:203], v5 offset:17024\n"
      "ds_write_addtid_b32 v86 offset:5120\n"
      "v_mfma_f32_16x16x32_bf16 v[56:59], a[52:55], a[208:211], v[56:59]\n"
      "s_nop 0\n"
      "ds_write_addtid_b32 v87 offset:5632\n"
      "v_cvt_pk_bf16_f32 v23, v23, v27\n"
      "v_mfma_f32_16x16x32_bf16 v[60:63], a[56:59], a[208:211], v[60:63]\n"
      "ds_write_addtid_b32 v23 offset:56064\n"
      "v_fma_f32 v40, 0.5, v76, -v88\n"
      "v_mfma_f32_16x16x32_bf16 v[52:55], a[60:63], a[212:215], v[52:55]\n"
      "v_fma_f32 v41, 0.5, v77, -v89\n"
      "v_fma_f32 v42, 0.5, v78, -v90\n"
      "v_mfma_f32_16x16x32_bf16 v[56:59], a[64:67], a[212:215], v[56:59]\n"
      "s_nop 0\n"
      "v_fma_f32 v43, 0.5, v79, -v91\n"
      "v_fma_f32 v44, 0.5, v80, -v92\n"
      "v_mfma_f32_16x16x32_bf16 v[60:63], a[68:71], a[212:215], v[60:63]\n"
      "s_waitcnt lgkmcnt(0)\n"
      "v_fma_f32 v45, 0.5, v81, -v93\n"
      "s_barrier\n"
      "s_add_u32 s55, s55, 2\n"
      "s_cmp_lt_u32 s55, 6\n"
      "s_cbranch_scc1 LSloop_%=\n"
      "LSlast_%=:\n"
      "v_mfma_f32_16x16x32_bf16 v[52:55], a[72:75], a[192:195], v[52:55]\n"
      "s_add_u32 s59, s62, 8847360\n"
      "ds_read_b128 a[204:207], v0 offset:16384\n"
      "ds_read2st64_b32 v[28:29], v6 offset0:0 offset1:2\n"
      "v_fma_f32 v46, 0.5, v82, -v94\n"
      "v_mfma_f32_16x16x32_bf16 v[56:59], a[76:79], a[192:195], v[56:59]\n"
      "ds_read_b128 a[208:211], v0 offset:20480\n"
      "ds_read2st64_b32 v[30:31], v6 offset0:4 offset1:6\n"
      "v_mfma_f32_16x16x32_bf16 v[60:63], a[80:83], a[192:195], v[60:63]\n"
      "ds_read_b64_tr_b16 a[220:221], v7 offset:33792\n"
      "v_fma_f32 v47, 0.5, v83, -v95\n"
      "v_mfma_f32_16x16x32_bf16 v[52:55], a[84:87], a[196:199], v[52:55]\n"
      "ds_read_b128 a[212:215], v0 offset:16448\n"
      "v_fma_f32 v48, 0.5, v84, -v96\n"
      "v_mfma_f32_16x16x32_bf16 v[56:59], a[88:91], a[196:199], v[56:59]\n"
      "ds_read_b64_tr_b16 a[222:223], v7 offset:33800\n"
      "ds_read2st64_b32 v[32:33], v6 offset0:8 offset1:10\n"
      "v_fma_f32 v49, 0.5, v85, -v97\n"
      "v_mfma_f32_16x16x32_bf16 v[60:63], a[92:95], a[196:199], v[60:63]\n"
      "ds_read2st64_b32 v[34:35], v6 offset0:12 offset1:14\n"
      "v_fma_f32 v50, 0.5, v86, -v98\n"
      "v_mfma_f32_16x16x32_bf16 v[52:55], a[96:99], a[200:203], v[52:55]\n"
      "ds_read_b128 a[192:195], v0 offset:20544\n"
      "v_fma_f32 v51, 0.5, v87, -v99\n"
      "v_mfma_f32_16x16x32_bf16 v[56:59], a[100:103], a[200:203], v[56:59]\n"
      "ds_read2st64_b32 v[36:37], v6 offset0:16 offset1:18\n"
      "v_mfma_f32_16x16x32_bf16 v[60:63], a[104:107], a[200:203], v[60:63]\n"
      "s_waitcnt lgkmcnt(7)\n"
      "ds_read2st64_b32 v[38:39], v6 offset0:20 offset1:22\n"
      "v_fmac_f32 v28, s46, v64\n"
      "v_fmac_f32 v29, s46, v65\n"
      "v_fmac_f32_e64 v30, s46, v66\n"
      "v_mfma_f32_16x16x32_bf16 v[76:79], v[252:255], a[204:207], v[52:55]\n"
      "s_waitcnt lgkmcnt(5)\n"
      "ds_read_b128 a[196:199], v0 offset:16512\n"
      "v_fmac_f32 v31, s46, v67\n"
      "v_mfma_f32_4x4x4_16b_bf16 v[12:15], a[216:217], a[220:221], 0\n"
      "v_sub_f32 v28, v28, v40\n"
      "v_mfma_f32_4x4x4_16b_bf16 v[16:19], a[216:217], a[222:223], 0\n"
      "ds_read2st64_b32 a[240:241], v8 offset0:4 offset1:6\n"
      "ds_read2st64_b32 a[242:243], v9 offset0:4 offset1:6\n"
      "v_sub_f32 v29, v29, v41\n"
      "v_mfma_f32_16x16x32_bf16 v[80:83], v[244:247], a[204:207], v[56:59]\n"
      "v_sub_f32 v30, v30, v42\n"
      "v_sub_f32 v31, v31, v43\n"
      "v_mfma_f32_16x16x32_bf16 v[84:87], v[236:239], a[204:207], v[60:63]\n"
      "v_fmac_f32 v40, s45, v28\n"
      "v_fmac_f32 v41, s45, v29\n"
      "v_mfma_f32_16x16x32_bf16 v[88:91], v[248:251], a[208:211], v[52:55]\n"
      "ds_read_b128 a[200:203], v0 offset:20608\n"
      "v_cvt_pk_bf16_f32 v12, v12, v16\n"
      "v_mfma_f32_16x16x32_bf16 v[92:95], v[240:243], a[208:211], v[56:59]\n"
      "s_nop 0\n"
      "ds_write_addtid_b32 v12 offset:24832\n"
      "ds_read_b64_tr_b16 a[224:225], v7 offset:34304\n"
      "v_fmac_f32 v42, s45, v30\n"
      "v_mfma_f32_16x16x32_bf16 v[96:99], v[232:235], a[208:211], v[60:63]\n"
      "ds_read_b64_tr_b16 a[226:227], v7 offset:34312\n"
      "v_fmac_f32 v43, s45, v31\n"
      "v_mfma_f32_16x16x32_bf16 v[76:79], v[228:231], a[212:215], v[76:79]\n"
      "ds_read_b128 a[204:207], v1 offset:16384\n"
      "v_cvt_pk_bf16_f32 v13, v13, v17\n"
      "v_mfma_f32_16x16x32_bf16 v[80:83], v[220:223], a[212:215], v[80:83]\n"
      "ds_write_addtid_b32 v13 offset:28928\n"
      "v_fma_f32 v28, s47, v28, v52\n"
      "v_mfma_f32_16x16x32_bf16 v[84:87], v[212:215], a[212:215], v[84:87]\n"
      "s_waitcnt lgkmcnt(6)\n"
      "v_fma_f32 v29, s47, v29, v53\n"
      "v_cvt_pk_bf16_f32 v14, v14, v18\n"
      "v_mfma_f32_16x16x32_bf16 v[88:91], v[224:227], a[192:195], v[88:91]\n"
      "s_nop 0\n"
      "ds_read_b128 a[208:211], v1 offset:20480\n"
      "ds_write_addtid_b32 v14 offset:33024\n"
      "v_fma_f32 v30, s47, v30, v54\n"
      "v_mfma_f32_16x16x32_bf16 v[92:95], v[216:219], a[192:195], v[92:95]\n"
      "s_nop 0\n"
      "v_fma_f32 v31, s47, v31, v55\n"
      "v_cvt_pk_bf16_f32 v15, v15, v19\n"
      "v_mfma_f32_16x16x32_bf16 v[96:99], v[208:211], a[192:195], v[96:99]\n"
      "s_waitcnt lgkmcnt(2)\n"
      "ds_write_addtid_b32 v15 offset:37120\n"
      "v_cvt_pk_bf16_f32 v40, v40, v41\n"
      "v_mfma_f32_4x4x4_16b_bf16 v[20:23], a[216:217], a[224:225], 0\n"
      "v_cvt_pk_bf16_f32 v42, v42, v43\n"
      "v_mfma_f32_4x4x4_16b_bf16 v[24:27], a[216:217], a[226:227], 0\n"
      "ds_write_addtid_b32 v40 offset:12288\n"
      "ds_write_addtid_b32 v42 offset:12800\n"
      "v_cvt_pk_bf16_f32 v28, v28, v29\n"
      "v_mfma_f32_16x16x32_bf16 v[76:79], v[204:207], a[196:199], v[76:79]\n"
      "ds_read_b128 a[212:215], v1 offset:16448\n"
      "ds_write_addtid_b32 v28 offset:14336\n"
      "v_cvt_pk_bf16_f32 v20, v20, v24\n"
      "v_mfma_f32_16x16x32_bf16 v[80:83], v[196:199], a[196:199], v[80:83]\n"
      "ds_write_addtid_b32 v20 offset:25344\n"
      "v_cvt_pk_bf16_f32 v30, v30, v31\n"
      "v_mfma_f32_16x16x32_bf16 v[84:87], v[188:191], a[196:199], v[84:87]\n"
      "buffer_store_dwordx4 a[240:243], v11, s[8:11], s59 offen offset:256 sc1\n"
      "v_mfma_f32_16x16x32_bf16 v[88:91], v[200:203], a[200:203], v[88:91]\n"
      "ds_read_b128 a[192:195], v1 offset:20544\n"
      "ds_write_addtid_b32 v30 offset:14848\n"
      "v_mfma_f32_16x16x32_bf16 v[92:95], v[192:195], a[200:203], v[92:95]\n"
      "ds_read_b64_tr_b16 a[228:229], v7 offset:34816\n"
      "ds_read_b64_tr_b16 a[230:231], v7 offset:34824\n"
      "v_fmac_f32 v32, s46, v68\n"
      "v_mfma_f32_16x16x32_bf16 v[96:99], v[184:187], a[200:203], v[96:99]\n"
      "v_fmac_f32 v33, s46, v69\n"
      "v_cvt_pk_bf16_f32 v21, v21, v25\n"
      "v_mfma_f32_16x16x32_bf16 v[76:79], v[180:183], a[204:207], v[76:79]\n"
      "ds_read_b128 a[196:199], v1 offset:16512\n"
      "ds_write_addtid_b32 v21 offset:29440\n"
      "v_mfma_f32_16x16x32_bf16 v[80:83], v[172:175], a[204:207], v[80:83]\n"
      "v_fmac_f32 v34, s46, v70\n"
      "v_fmac_f32 v35, s46, v71\n"
      "v_mfma_f32_16x16x32_bf16 v[84:87], v[164:167], a[204:207], v[84:87]\n"
      "s_waitcnt lgkmcnt(6)\n"
      "v_sub_f32 v32, v32, v44\n"
      "v_cvt_pk_bf16_f32 v22, v22, v26\n"
      "v_mfma_f32_16x16x32_bf16 v[88:91], v[176:179], a[208:211], v[88:91]\n"
      "ds_read_b128 a[200:203], v1 offset:20608\n"
      "ds_write_addtid_b32 v22 offset:33536\n"
      "v_sub_f32 v33, v33, v45\n"
      "v_mfma_f32_16x16x32_bf16 v[92:95], v[168:171], a[208:211], v[92:95]\n"
      "s_waitcnt lgkmcnt(4)\n"
      "v_sub_f32_e64 v34, v34, v46\n"
      "v_cvt_pk_bf16_f32 v23, v23, v27\n"
      "v_mfma_f32_4x4x4_16b_bf16 v[12:15], a[216:217], a[228:229], 0\n"
      "v_sub_f32 v35, v35, v47\n"
      "v_mfma_f32_4x4x4_16b_bf16 v[16:19], a[216:217], a[230:231], 0\n"
      "ds_write_addtid_b32 v23 offset:37632\n"
      "v_fmac_f32 v44, s45, v32\n"
      "v_mfma_f32_16x16x32_bf16 v[96:99], v[160:163], a[208:211], v[96:99]\n"
      "v_fmac_f32 v45, s45, v33\n"
      "v_fmac_f32 v46, s45, v34\n"
      "v_cvt_pk_bf16_f32 v12, v12, v16\n"
      "v_mfma_f32_16x16x32_bf16 v[76:79], v[156:159], a[212:215], v[76:79]\n"
      "ds_read_b128 a[204:207], v2 offset:16384\n"
      "ds_write_addtid_b32 v12 offset:25856\n"
      "v_mfma_f32_16x16x32_bf16 v[80:83], v[148:151], a[212:215], v[80:83]\n"
      "v_fmac_f32 v47, s45, v35\n"
      "v_cvt_pk_bf16_f32 v13, v13, v17\n"
      "v_mfma_f32_16x16x32_bf16 v[84:87], v[140:143], a[212:215], v[84:87]\n"
      "s_nop 0\n"
      "ds_read_b64_tr_b16 a[232:233], v7 offset:35328\n"
      "ds_write_addtid_b32 v13 offset:29952\n"
      "v_fma_f32 v32, s47, v32, v56\n"
      "v_mfma_f32_16x16x32_bf16 v[88:91], v[152:155], a[192:195], v[88:91]\n"
      "ds_read_b128 a[208:211], v2 offset:20480\n"
      "ds_read_b64_tr_b16 a[234:235], v7 offset:35336\n"
      "v_mfma_f32_16x16x32_bf16 v[92:95], v[144:147], a[192:195], v[92:95]\n"
      "v_fma_f32 v33, s47, v33, v57\n"
      "v_fma_f32 v34, s47, v34, v58\n"
      "v_mfma_f32_16x16x32_bf16 v[96:99], v[136:139], a[192:195], v[96:99]\n"
      "s_waitcnt lgkmcnt(4)\n"
      "v_fma_f32 v35, s47, v35, v59\n"
      "v_cvt_pk_bf16_f32 v14, v14, v18\n"
      "v_mfma_f32_16x16x32_bf16 v[76:79], v[132:135], a[196:199], v[76:79]\n"
      "ds_read_b128 a[212:215], v2 offset:16448\n"
      "ds_write_addtid_b32 v14 offset:34048\n"
      "v_mfma_f32_16x16x32_bf16 v[80:83], v[124:127], a[196:199], v[80:83]\n"
      "s_nop 0\n"
      "v_cvt_pk_bf16_f32 v44, v44, v45\n"
      "v_cvt_pk_bf16_f32 v46, v46, v47\n"
      "v_mfma_f32_16x16x32_bf16 v[84:87], v[116:119], a[196:199], v[84:87]\n"
      "s_nop 0\n"
      "ds_write_addtid_b32 v44 offset:20484\n"
      "ds_write_addtid_b32 v46 offset:20996\n"
      "v_cvt_pk_bf16_f32 v32, v32, v33\n"
      "v_mfma_f32_16x16x32_bf16 v[88:91], v[128:131], a[200:203], v[88:91]\n"
      "s_waitcnt lgkmcnt(4)\n"
      "ds_read_b128 a[192:195], v2 offset:20544\n"
      "ds_write_addtid_b32 v32 offset:22532\n"
      "v_mfma_f32_4x4x4_16b_bf16 v[20:23], a[216:217], a[232:233], 0\n"
      "v_cvt_pk_bf16_f32 v34, v34, v35\n"
      "v_cvt_pk_bf16_f32 v15, v15, v19\n"
      "v_mfma_f32_4x4x4_16b_bf16 v[24:27], a[216:217], a[234:235], 0\n"
      "ds_write_addtid_b32 v34 offset:23044\n"
      "ds_write_addtid_b32 v15 offset:38144\n"
      "v_fmac_f32_e64 v36, s46, v72\n"
      "v_mfma_f32_16x16x32_bf16 v[92:95], v[120:123], a[200:203], v[92:95]\n"
      "v_fmac_f32 v37, s46, v73\n"
      "v_cvt_pk_bf16_f32 v20, v20, v24\n"
      "v_mfma_f32_16x16x32_bf16 v[96:99], v[112:115], a[200:203], v[96:99]\n"
      "ds_write_addtid_b32 v20 offset:26368\n"
      "v_fmac_f32 v38, s46, v74\n"
      "v_mfma_f32_16x16x32_bf16 v[76:79], v[108:111], a[204:207], v[76:79]\n"
      "ds_read_b128 a[196:199], v2 offset:16512\n"
      "v_fmac_f32_e64 v39, s46, v75\n"
      "v_mfma_f32_16x16x32_bf16 v[80:83], v[100:103], a[204:207], v[80:83]\n"
      "ds_read_b64_tr_b16 a[220:221], v7 offset:35840\n"
      "ds_read_b64_tr_b16 a[222:223], v7 offset:35848\n"
      "v_sub_f32 v36, v36, v48\n"
      "v_mfma_f32_16x16x32_bf16 v[84:87], a[160:163], a[204:207], v[84:87]\n"
      "v_sub_f32 v37, v37, v49\n"
      "v_cvt_pk_bf16_f32 v21, v21, v25\n"
      "v_mfma_f32_16x16x32_bf16 v[88:91], v[104:107], a[208:211], v[88:91]\n"
      "ds_read_b128 a[200:203], v2 offset:20608\n"
      "ds_write_addtid_b32 v21 offset:30464\n"
      "v_mfma_f32_16x16x32_bf16 v[92:95], a[164:167], a[208:211], v[92:95]\n"
      "v_sub_f32 v38, v38, v50\n"
      "v_sub_f32 v39, v39, v51\n"
      "v_mfma_f32_16x16x32_bf16 v[96:99], a[156:159], a[208:211], v[96:99]\n"
      "s_waitcnt lgkmcnt(3)\n"
      "v_fmac_f32 v48, s45, v36\n"
      "v_cvt_pk_bf16_f32 v22, v22, v26\n"
      "v_mfma_f32_16x16x32_bf16 v[76:79], a[152:155], a[212:215], v[76:79]\n"
      "ds_read_b128 a[204:207], v3 offset:33792\n"
      "ds_write_addtid_b32 v22 offset:34560\n"
      "v_fmac_f32 v49, s45, v37\n"
      "v_mfma_f32_16x16x32_bf16 v[80:83], a[144:147], a[212:215], v[80:83]\n"
      "v_fmac_f32 v50, s45, v38\n"
      "v_cvt_pk_bf16_f32 v23, v23, v27\n"
      "v_mfma_f32_16x16x32_bf16 v[84:87], a[136:139], a[212:215], v[84:87]\n"
      "s_waitcnt lgkmcnt(2)\n"
      "ds_write_addtid_b32 v23 offset:38656\n"
      "v_fmac_f32 v51, s45, v39\n"
      "v_mfma_f32_4x4x4_16b_bf16 v[12:15], a[216:217], a[220:221], 0\n"
      "v_fma_f32 v36, s47, v36, v60\n"
      "v_mfma_f32_4x4x4_16b_bf16 v[16:19], a[216:217], a[222:223], 0\n"
      "v_fma_f32 v37, s47, v37, v61\n"
      "v_fma_f32 v38, s47, v38, v62\n"
      "v_mfma_f32_16x16x32_bf16 v[88:91], a[148:151], a[192:195], v[88:91]\n"
      "ds_read_b128 a[208:211], v3 offset:33856\n"
      "v_fma_f32 v39, s47, v39, v63\n"
      "v_cvt_pk_bf16_f32 v12, v12, v16\n"
      "v_mfma_f32_16x16x32_bf16 v[92:95], a[140:143], a[192:195], v[92:95]\n"
      "ds_write_addtid_b32 v12 offset:26880\n"
      "v_cvt_pk_bf16_f32 v13, v13, v17\n"
      "v_mfma_f32_16x16x32_bf16 v[96:99], a[132:135], a[192:195], v[96:99]\n"
      "ds_write_addtid_b32 v13 offset:30976\n"
      "v_cvt_pk_bf16_f32 v48, v48, v49\n"
      "v_mfma_f32_16x16x32_bf16 v[76:79], a[128:131], a[196:199], v[76:79]\n"
      "ds_read_b128 a[212:215], v3 offset:33920\n"
      "ds_write_addtid_b32 v48 offset:16384\n"
      "v_mfma_f32_16x16x32_bf16 v[80:83], a[120:123], a[196:199], v[80:83]\n"
      "ds_read_b64_tr_b16 a[224:225], v7 offset:36352\n"
      "ds_read_b64_tr_b16 a[226:227], v7 offset:36360\n"
      "v_cvt_pk_bf16_f32 v14, v14, v18\n"
      "v_mfma_f32_16x16x32_bf16 v[84:87], a[112:115], a[196:199], v[84:87]\n"
      "ds_write_addtid_b32 v14 offset:35072\n"
      "v_cvt_pk_bf16_f32 v50, v50, v51\n"
      "v_mfma_f32_16x16x32_bf16 v[88:91], a[124:127], a[200:203], v[88:91]\n"
      "ds_read_b128 a[192:195], v4 offset:33792\n"
      "ds_write_addtid_b32 v50 offset:16896\n"
      "v_mfma_f32_16x16x32_bf16 v[92:95], a[116:119], a[200:203], v[92:95]\n"
      "s_nop 0\n"
      "v_cvt_pk_bf16_f32 v36, v36, v37\n"
      "v_cvt_pk_bf16_f32 v15, v15, v19\n"
      "v_mfma_f32_16x16x32_bf16 v[96:99], a[108:111], a[200:203], v[96:99]\n"
      "s_waitcnt lgkmcnt(5)\n"
      "ds_write_addtid_b32 v36 offset:18432\n"
      "ds_write_addtid_b32 v15 offset:39168\n"
      "v_sub_f32 v76, v88, v76\n"
      "v_sub_f32 v77, v89, v77\n"
      "v_mfma_f32_16x16x32_bf16 v[64:67], a[0:3], a[204:207], 0\n"
      "s_nop 0\n"
      "ds_read_b128 a[196:199], v4 offset:33856\n"
      "ds_write_addtid_b32 v76 offset:6144\n"
      "v_mfma_f32_16x16x32_bf16 v[68:71], a[4:7], a[204:207], 0\n"
      "s_waitcnt lgkmcnt(6)\n"
      "ds_write_addtid_b32 v77 offset:6656\n"
      "v_sub_f32 v78, v90, v78\n"
      "v_mfma_f32_4x4x4_16b_bf16 v[20:23], a[216:217], a[224:225], 0\n"
      "v_sub_f32 v79, v91, v79\n"
      "v_cvt_pk_bf16_f32 v38, v38, v39\n"
      "v_mfma_f32_4x4x4_16b_bf16 v[24:27], a[216:217], a[226:227], 0\n"
      "ds_write_addtid_b32 v38 offset:18944\n"
      "ds_write_addtid_b32 v78 offset:7168\n"
      "v_mfma_f32_16x16x32_bf16 v[72:75], a[8:11], a[204:207], 0\n"
      "ds_write_addtid_b32 v79 offset:7680\n"
      "v_sub_f32 v80, v92, v80\n"
      "v_mfma_f32_16x16x32_bf16 v[64:67], a[12:15], a[208:211], v[64:67]\n"
      "ds_read_b128 a[200:203], v4 offset:33920\n"
      "ds_write_addtid_b32 v80 offset:8192\n"
      "v_sub_f32 v81, v93, v81\n"
      "v_mfma_f32_16x16x32_bf16 v[68:71], a[16:19], a[208:211], v[68:71]\n"
      "ds_write_addtid_b32 v81 offset:8704\n"
      "v_sub_f32_e64 v82, v94, v82\n"
      "v_mfma_f32_16x16x32_bf16 v[72:75], a[20:23], a[208:211], v[72:75]\n"
      "ds_write_addtid_b32 v82 offset:9216\n"
      "v_cvt_pk_bf16_f32 v20, v20, v24\n"
      "v_mfma_f32_16x16x32_bf16 v[64:67], a[24:27], a[212:215], v[64:67]\n"
      "ds_read_b128 a[204:207], v5 offset:33792\n"
      "ds_write_addtid_b32 v20 offset:27392\n"
      "v_mfma_f32_16x16x32_bf16 v[68:71], a[28:31], a[212:215], v[68:71]\n"
      "v_sub_f32 v83, v95, v83\n"
      "v_sub_f32 v84, v96, v84\n"
      "v_cvt_pk_bf16_f32 v21, v21, v25\n"
      "v_mfma_f32_16x16x32_bf16 v[72:75], a[32:35], a[212:215], v[72:75]\n"
      "s_waitcnt lgkmcnt(4)\n"
      "ds_write_addtid_b32 v21 offset:31488\n"
      "ds_write_addtid_b32 v83 offset:9728\n"
      "v_mfma_f32_16x16x32_bf16 v[64:67], a[36:39], a[192:195], v[64:67]\n"
      "s_nop 0\n"
      "ds_read_b128 a[208:211], v5 offset:33856\n"
      "ds_write_addtid_b32 v84 offset:10240\n"
      "v_mfma_f32_16x16x32_bf16 v[68:71], a[40:43], a[192:195], v[68:71]\n"
      "v_sub_f32 v85, v97, v85\n"
      "v_sub_f32 v86, v98, v86\n"
      "v_cvt_pk_bf16_f32 v22, v22, v26\n"
      "v_mfma_f32_16x16x32_bf16 v[72:75], a[44:47], a[192:195], v[72:75]\n"
      "ds_write_addtid_b32 v22 offset:35584\n"
      "ds_write_addtid_b32 v85 offset:10752\n"
      "v_sub_f32 v87, v99, v87\n"
      "v_mfma_f32_16x16x32_bf16 v[64:67], a[48:51], a[196:199], v[64:67]\n"
      "s_nop 0\n"
      "ds_read_b128 a[212:215], v5 offset:33920\n"
      "ds_write_addtid_b32 v86 offset:11264\n"
      "v_mfma_f32_16x16x32_bf16 v[68:71], a[52:55], a[196:199], v[68:71]\n"
      "ds_write_addtid_b32 v87 offset:11776\n"
      "v_cvt_pk_bf16_f32 v23, v23, v27\n"
      "v_mfma_f32_16x16x32_bf16 v[72:75], a[56:59], a[196:199], v[72:75]\n"
      "ds_write_addtid_b32 v23 offset:39680\n"
      "v_fma_f32 v40, 0.5, v76, -v88\n"
      "v_mfma_f32_16x16x32_bf16 v[64:67], a[60:63], a[200:203], v[64:67]\n"
      "v_fma_f32 v41, 0.5, v77, -v89\n"
      "v_fma_f32 v42, 0.5, v78, -v90\n"
      "v_mfma_f32_16x16x32_bf16 v[68:71], a[64:67], a[200:203], v[68:71]\n"
      "s_nop 0\n"
      "v_fma_f32 v43, 0.5, v79, -v91\n"
      "v_fma_f32 v44, 0.5, v80, -v92\n"
      "v_mfma_f32_16x16x32_bf16 v[72:75], a[68:71], a[200:203], v[72:75]\n"
      "s_waitcnt lgkmcnt(0)\n"
      "v_fma_f32 v45, 0.5, v81, -v93\n"
      "s_barrier\n"
      "v_mfma_f32_16x16x32_bf16 v[64:67], a[72:75], a[204:207], v[64:67]\n"
      "s_add_u32 s59, s62, 9633792\n"
      "ds_read_b128 a[192:195], v0 offset:0\n"
      "ds_read2st64_b32 v[28:29], v6 offset0:24 offset1:26\n"
      "v_fma_f32 v46, 0.5, v82, -v94\n"
      "v_mfma_f32_16x16x32_bf16 v[68:71], a[76:79], a[204:207], v[68:71]\n"
      "s_nop 0\n"
      "ds_read_b128 a[196:199], v0 offset:4096\n"
      "ds_read2st64_b32 v[30:31], v6 offset0:28 offset1:30\n"
      "v_mfma_f32_16x16x32_bf16 v[72:75], a[80:83], a[204:207], v[72:75]\n"
      "ds_read_b64_tr_b16 a[220:221], v7 offset:50688\n"
      "v_fma_f32 v47, 0.5, v83, -v95\n"
      "v_mfma_f32_16x16x32_bf16 v[64:67], a[84:87], a[208:211], v[64:67]\n"
      "ds_read_b128 a[200:203], v0 offset:64\n"
      "v_fma_f32 v48, 0.5, v84, -v96\n"
      "v_mfma_f32_16x16x32_bf16 v[68:71], a[88:91], a[208:211], v[68:71]\n"
      "ds_read_b64_tr_b16 a[222:223], v7 offset:50696\n"
      "ds_read2st64_b32 v[32:33], v6 offset0:32 offset1:34\n"
      "v_fma_f32 v49, 0.5, v85, -v97\n"
      "v_mfma_f32_16x16x32_bf16 v[72:75], a[92:95], a[208:211], v[72:75]\n"
      "ds_read2st64_b32 v[34:35], v6 offset0:36 offset1:38\n"
      "v_fma_f32 v50, 0.5, v86, -v98\n"
      "v_mfma_f32_16x16x32_bf16 v[64:67], a[96:99], a[212:215], v[64:67]\n"
      "ds_read_b128 a[204:207], v0 offset:4160\n"
      "v_fma_f32 v51, 0.5, v87, -v99\n"
      "v_mfma_f32_16x16x32_bf16 v[68:71], a[100:103], a[212:215], v[68:71]\n"
      "ds_read2st64_b32 v[36:37], v6 offset0:40 offset1:42\n"
      "v_mfma_f32_16x16x32_bf16 v[72:75], a[104:107], a[212:215], v[72:75]\n"
      "s_waitcnt lgkmcnt(7)\n"
      "ds_read2st64_b32 v[38:39], v6 offset0:44 offset1:46\n"
      "v_fmac_f32 v28, s46, v52\n"
      "v_fmac_f32 v29, s46, v53\n"
      "v_fmac_f32 v30, s46, v54\n"
      "v_mfma_f32_16x16x32_bf16 v[76:79], v[252:255], a[192:195], v[64:67]\n"
      "s_waitcnt lgkmcnt(5)\n"
      "ds_read_b128 a[208:211], v0 offset:128\n"
      "v_fmac_f32 v31, s46, v55\n"
      "v_mfma_f32_4x4x4_16b_bf16 v[12:15], a[216:217], a[220:221], 0\n"
      "v_sub_f32 v28, v28, v40\n"
      "v_mfma_f32_4x4x4_16b_bf16 v[16:19], a[216:217], a[222:223], 0\n"
      "ds_read2st64_b32 a[240:241], v8 offset0:0 offset1:2\n"
      "ds_read2st64_b32 a[242:243], v9 offset0:0 offset1:2\n"
      "v_sub_f32 v29, v29, v41\n"
      "v_mfma_f32_16x16x32_bf16 v[80:83], v[244:247], a[192:195], v[68:71]\n"
      "v_sub_f32 v30, v30, v42\n"
      "v_sub_f32 v31, v31, v43\n"
      "v_mfma_f32_16x16x32_bf16 v[84:87], v[236:239], a[192:195], v[72:75]\n"
      "v_fmac_f32 v40, s45, v28\n"
      "v_fmac_f32 v41, s45, v29\n"
      "v_mfma_f32_16x16x32_bf16 v[88:91], v[248:251], a[196:199], v[64:67]\n"
      "ds_read_b128 a[212:215], v0 offset:4224\n"
      "v_cvt_pk_bf16_f32 v12, v12, v16\n"
      "v_mfma_f32_16x16x32_bf16 v[92:95], v[240:243], a[196:199], v[68:71]\n"
      "ds_write_addtid_b32 v12 offset:41216\n"
      "ds_read_b64_tr_b16 a[224:225], v7 offset:51200\n"
      "v_fmac_f32 v42, s45, v30\n"
      "v_mfma_f32_16x16x32_bf16 v[96:99], v[232:235], a[196:199], v[72:75]\n"
      "ds_read_b64_tr_b16 a[226:227], v7 offset:51208\n"
      "v_fmac_f32 v43, s45, v31\n"
      "v_mfma_f32_16x16x32_bf16 v[76:79], v[228:231], a[200:203], v[76:79]\n"
      "ds_read_b128 a[192:195], v1 offset:0\n"
      "v_cvt_pk_bf16_f32 v13, v13, v17\n"
      "v_mfma_f32_16x16x32_bf16 v[80:83], v[220:223], a[200:203], v[80:83]\n"
      "ds_write_addtid_b32 v13 offset:45312\n"
      "v_fma_f32 v28, s47, v28, v64\n"
      "v_mfma_f32_16x16x32_bf16 v[84:87], v[212:215], a[200:203], v[84:87]\n"
      "s_waitcnt lgkmcnt(6)\n"
      "v_fma_f32 v29, s47, v29, v65\n"
      "v_cvt_pk_bf16_f32 v14, v14, v18\n"
      "v_mfma_f32_16x16x32_bf16 v[88:91], v[224:227], a[204:207], v[88:91]\n"
      "s_nop 0\n"
      "ds_read_b128 a[196:199], v1 offset:4096\n"
      "ds_write_addtid_b32 v14 offset:49408\n"
      "v_fma_f32 v30, s47, v30, v66\n"
      "v_mfma_f32_16x16x32_bf16 v[92:95], v[216:219], a[204:207], v[92:95]\n"
      "s_nop 0\n"
      "v_fma_f32 v31, s47, v31, v67\n"
      "v_cvt_pk_bf16_f32 v15, v15, v19\n"
      "v_mfma_f32_16x16x32_bf16 v[96:99], v[208:211], a[204:207], v[96:99]\n"
      "s_waitcnt lgkmcnt(2)\n"
      "ds_write_addtid_b32 v15 offset:53504\n"
      "v_cvt_pk_bf16_f32 v40, v40, v41\n"
      "v_mfma_f32_4x4x4_16b_bf16 v[20:23], a[216:217], a[224:225], 0\n"
      "v_cvt_pk_bf16_f32 v42, v42, v43\n"
      "v_mfma_f32_4x4x4_16b_bf16 v[24:27], a[216:217], a[226:227], 0\n"
      "ds_write_addtid_b32 v40 offset:13312\n"
      "ds_write_addtid_b32 v42 offset:13824\n"
      "v_cvt_pk_bf16_f32 v28, v28, v29\n"
      "v_mfma_f32_16x16x32_bf16 v[76:79], v[204:207], a[208:211], v[76:79]\n"
      "ds_read_b128 a[200:203], v1 offset:64\n"
      "ds_write_addtid_b32 v28 offset:15360\n"
      "v_cvt_pk_bf16_f32 v20, v20, v24\n"
      "v_mfma_f32_16x16x32_bf16 v[80:83], v[196:199], a[208:211], v[80:83]\n"
      "ds_write_addtid_b32 v20 offset:41728\n"
      "v_cvt_pk_bf16_f32 v30, v30, v31\n"
      "v_mfma_f32_16x16x32_bf16 v[84:87], v[188:191], a[208:211], v[84:87]\n"
      "buffer_store_dwordx4 a[240:243], v11, s[8:11], s59 offen offset:256 sc1\n"
      "v_mfma_f32_16x16x32_bf16 v[88:91], v[200:203], a[212:215], v[88:91]\n"
      "ds_read_b128 a[204:207], v1 offset:4160\n"
      "ds_write_addtid_b32 v30 offset:15872\n"
      "v_mfma_f32_16x16x32_bf16 v[92:95], v[192:195], a[212:215], v[92:95]\n"
      "ds_read_b64_tr_b16 a[228:229], v7 offset:51712\n"
      "ds_read_b64_tr_b16 a[230:231], v7 offset:51720\n"
      "v_fmac_f32_e64 v32, s46, v56\n"
      "v_mfma_f32_16x16x32_bf16 v[96:99], v[184:187], a[212:215], v[96:99]\n"
      "v_fmac_f32 v33, s46, v57\n"
      "v_cvt_pk_bf16_f32 v21, v21, v25\n"
      "v_mfma_f32_16x16x32_bf16 v[76:79], v[180:183], a[192:195], v[76:79]\n"
      "ds_read_b128 a[208:211], v1 offset:128\n"
      "ds_write_addtid_b32 v21 offset:45824\n"
      "v_mfma_f32_16x16x32_bf16 v[80:83], v[172:175], a[192:195], v[80:83]\n"
      "v_fmac_f32 v34, s46, v58\n"
      "v_fmac_f32_e64 v35, s46, v59\n"
      "v_mfma_f32_16x16x32_bf16 v[84:87], v[164:167], a[192:195], v[84:87]\n"
      "s_waitcnt lgkmcnt(6)\n"
      "v_sub_f32 v32, v32, v44\n"
      "v_cvt_pk_bf16_f32 v22, v22, v26\n"
      "v_mfma_f32_16x16x32_bf16 v[88:91], v[176:179], a[196:199], v[88:91]\n"
      "ds_read_b128 a[212:215], v1 offset:4224\n"
      "ds_write_addtid_b32 v22 offset:49920\n"
      "v_sub_f32 v33, v33, v45\n"
      "v_mfma_f32_16x16x32_bf16 v[92:95], v[168:171], a[196:199], v[92:95]\n"
      "s_waitcnt lgkmcnt(4)\n"
      "v_sub_f32 v34, v34, v46\n"
      "v_cvt_pk_bf16_f32 v23, v23, v27\n"
      "v_mfma_f32_4x4x4_16b_bf16 v[12:15], a[216:217], a[228:229], 0\n"
      "v_sub_f32 v35, v35, v47\n"
      "v_mfma_f32_4x4x4_16b_bf16 v[16:19], a[216:217], a[230:231], 0\n"
      "ds_write_addtid_b32 v23 offset:54016\n"
      "v_fmac_f32 v44, s45, v32\n"
      "v_mfma_f32_16x16x32_bf16 v[96:99], v[160:163], a[196:199], v[96:99]\n"
      "s_nop 0\n"
      "v_fmac_f32_e64 v45, s45, v33\n"
      "v_fmac_f32_e64 v46, s45, v34\n"
      "v_cvt_pk_bf16_f32 v12, v12, v16\n"
      "v_mfma_f32_16x16x32_bf16 v[76:79], v[156:159], a[200:203], v[76:79]\n"
      "s_nop 0\n"
      "ds_read_b128 a[192:195], v2 offset:0\n"
      "ds_write_addtid_b32 v12 offset:42240\n"
      "v_mfma_f32_16x16x32_bf16 v[80:83], v[148:151], a[200:203], v[80:83]\n"
      "s_nop 0\n"
      "v_fmac_f32_e64 v47, s45, v35\n"
      "v_cvt_pk_bf16_f32 v13, v13, v17\n"
      "v_mfma_f32_16x16x32_bf16 v[84:87], v[140:143], a[200:203], v[84:87]\n"
      "s_nop 0\n"
      "ds_read_b64_tr_b16 a[232:233], v7 offset:52224\n"
      "ds_write_addtid_b32 v13 offset:46336\n"
      "v_fma_f32 v32, s47, v32, v68\n"
      "v_mfma_f32_16x16x32_bf16 v[88:91], v[152:155], a[204:207], v[88:91]\n"
      "s_nop 0\n"
      "ds_read_b128 a[196:199], v2 offset:4096\n"
      "ds_read_b64_tr_b16 a[234:235], v7 offset:52232\n"
      "v_mfma_f32_16x16x32_bf16 v[92:95], v[144:147], a[204:207], v[92:95]\n"
      "s_nop 0\n"
      "v_fma_f32 v33, s47, v33, v69\n"
      "v_fma_f32 v34, s47, v34, v70\n"
      "v_mfma_f32_16x16x32_bf16 v[96:99], v[136:139], a[204:207], v[96:99]\n"
      "s_waitcnt lgkmcnt(4)\n"
      "v_fma_f32 v35, s47, v35, v71\n"
      "v_cvt_pk_bf16_f32 v14, v14, v18\n"
      "v_mfma_f32_16x16x32_bf16 v[76:79], v[132:135], a[208:211], v[76:79]\n"
      "s_nop 0\n"
      "ds_read_b128 a[200:203], v2 offset:64\n"
      "ds_write_addtid_b32 v14 offset:50432\n"
      "v_mfma_f32_16x16x32_bf16 v[80:83], v[124:127], a[208:211], v[80:83]\n"
      "s_nop 0\n"
      "v_cvt_pk_bf16_f32 v44, v44, v45\n"
      "v_cvt_pk_bf16_f32 v46, v46, v47\n"
      "v_mfma_f32_16x16x32_bf16 v[84:87], v[116:119], a[208:211], v[84:87]\n"
      "s_nop 0\n"
      "ds_write_addtid_b32 v44 offset:21508\n"
      "ds_write_addtid_b32 v46 offset:22020\n"
      "v_cvt_pk_bf16_f32 v32, v32, v33\n"
      "v_mfma_f32_16x16x32_bf16 v[88:91], v[128:131], a[212:215], v[88:91]\n"
      "s_waitcnt lgkmcnt(4)\n"
      "ds_read_b128 a[204:207], v2 offset:4160\n"
      "ds_write_addtid_b32 v32 offset:23556\n"
      "v_mfma_f32_4x4x4_16b_bf16 v[20:23], a[216:217], a[232:233], 0\n"
      "v_cvt_pk_bf16_f32 v34, v34, v35\n"
      "v_cvt_pk_bf16_f32 v15, v15, v19\n"
      "v_mfma_f32_4x4x4_16b_bf16 v[24:27], a[216:217], a[234:235], 0\n"
      "ds_write_addtid_b32 v34 offset:24068\n"
      "ds_write_addtid_b32 v15 offset:54528\n"
      "v_fmac_f32 v36, s46, v60\n"
      "v_mfma_f32_16x16x32_bf16 v[92:95], v[120:123], a[212:215], v[92:95]\n"
      "v_fmac_f32 v37, s46, v61\n"
      "v_cvt_pk_bf16_f32 v20, v20, v24\n"
      "v_mfma_f32_16x16x32_bf16 v[96:99], v[112:115], a[212:215], v[96:99]\n"
      "ds_write_addtid_b32 v20 offset:42752\n"
      "v_fmac_f32 v38, s46, v62\n"
      "v_mfma_f32_16x16x32_bf16 v[76:79], v[108:111], a[192:195], v[76:79]\n"
      "ds_read_b128 a[208:211], v2 offset:128\n"
      "v_fmac_f32 v39, s46, v63\n"
      "v_mfma_f32_16x16x32_bf16 v[80:83], v[100:103], a[192:195], v[80:83]\n"
      "ds_read_b64_tr_b16 a[220:221], v7 offset:52736\n"
      "ds_read_b64_tr_b16 a[222:223], v7 offset:52744\n"
      "v_sub_f32 v36, v36, v48\n"
      "v_mfma_f32_16x16x32_bf16 v[84:87], a[160:163], a[192:195], v[84:87]\n"
      "v_sub_f32 v37, v37, v49\n"
      "v_cvt_pk_bf16_f32 v21, v21, v25\n"
      "v_mfma_f32_16x16x32_bf16 v[88:91], v[104:107], a[196:199], v[88:91]\n"
      "ds_read_b128 a[212:215], v2 offset:4224\n"
      "ds_write_addtid_b32 v21 offset:46848\n"
      "v_mfma_f32_16x16x32_bf16 v[92:95], a[164:167], a[196:199], v[92:95]\n"
      "v_sub_f32 v38, v38, v50\n"
      "v_sub_f32 v39, v39, v51\n"
      "v_mfma_f32_16x16x32_bf16 v[96:99], a[156:159], a[196:199], v[96:99]\n"
      "s_waitcnt lgkmcnt(3)\n"
      "v_fmac_f32 v48, s45, v36\n"
      "v_cvt_pk_bf16_f32 v22, v22, v26\n"
      "v_mfma_f32_16x16x32_bf16 v[76:79], a[152:155], a[200:203], v[76:79]\n"
      "ds_read_b128 a[192:195], v3 offset:50688\n"
      "ds_write_addtid_b32 v22 offset:50944\n"
      "v_fmac_f32 v49, s45, v37\n"
      "v_mfma_f32_16x16x32_bf16 v[80:83], a[144:147], a[200:203], v[80:83]\n"
      "v_fmac_f32 v50, s45, v38\n"
      "v_cvt_pk_bf16_f32 v23, v23, v27\n"
      "v_mfma_f32_16x16x32_bf16 v[84:87], a[136:139], a[200:203], v[84:87]\n"
      "s_waitcnt lgkmcnt(2)\n"
      "ds_write_addtid_b32 v23 offset:55040\n"
      "v_fmac_f32 v51, s45, v39\n"
      "v_mfma_f32_4x4x4_16b_bf16 v[12:15], a[216:217], a[220:221], 0\n"
      "v_fma_f32 v36, s47, v36, v72\n"
      "v_mfma_f32_4x4x4_16b_bf16 v[16:19], a[216:217], a[222:223], 0\n"
      "v_fma_f32 v37, s47, v37, v73\n"
      "v_fma_f32 v38, s47, v38, v74\n"
      "v_mfma_f32_16x16x32_bf16 v[88:91], a[148:151], a[204:207], v[88:91]\n"
      "ds_read_b128 a[196:199], v3 offset:50752\n"
      "v_fma_f32 v39, s47, v39, v75\n"
      "v_cvt_pk_bf16_f32 v12, v12, v16\n"
      "v_mfma_f32_16x16x32_bf16 v[92:95], a[140:143], a[204:207], v[92:95]\n"
      "ds_write_addtid_b32 v12 offset:43264\n"
      "v_cvt_pk_bf16_f32 v13, v13, v17\n"
      "v_mfma_f32_16x16x32_bf16 v[96:99], a[132:135], a[204:207], v[96:99]\n"
      "ds_write_addtid_b32 v13 offset:47360\n"
      "v_cvt_pk_bf16_f32 v48, v48, v49\n"
      "v_mfma_f32_16x16x32_bf16 v[76:79], a[128:131], a[208:211], v[76:79]\n"
      "ds_read_b128 a[200:203], v3 offset:50816\n"
      "ds_write_addtid_b32 v48 offset:17408\n"
      "v_mfma_f32_16x16x32_bf16 v[80:83], a[120:123], a[208:211], v[80:83]\n"
      "ds_read_b64_tr_b16 a[224:225], v7 offset:53248\n"
      "ds_read_b64_tr_b16 a[226:227], v7 offset:53256\n"
      "v_cvt_pk_bf16_f32 v14, v14, v18\n"
      "v_mfma_f32_16x16x32_bf16 v[84:87], a[112:115], a[208:211], v[84:87]\n"
      "ds_write_addtid_b32 v14 offset:51456\n"
      "v_cvt_pk_bf16_f32 v50, v50, v51\n"
      "v_mfma_f32_16x16x32_bf16 v[88:91], a[124:127], a[212:215], v[88:91]\n"
      "ds_read_b128 a[204:207], v4 offset:50688\n"
      "ds_write_addtid_b32 v50 offset:17920\n"
      "v_mfma_f32_16x16x32_bf16 v[92:95], a[116:119], a[212:215], v[92:95]\n"
      "v_cvt_pk_bf16_f32 v36, v36, v37\n"
      "v_cvt_pk_bf16_f32 v15, v15, v19\n"
      "v_mfma_f32_16x16x32_bf16 v[96:99], a[108:111], a[212:215], v[96:99]\n"
      "s_waitcnt lgkmcnt(5)\n"
      "ds_write_addtid_b32 v36 offset:19456\n"
      "ds_write_addtid_b32 v15 offset:55552\n"
      "v_sub_f32 v76, v88, v76\n"
      "v_sub_f32_e64 v77, v89, v77\n"
      "v_mfma_f32_16x16x32_bf16 v[52:55], a[0:3], a[192:195], 0\n"
      "ds_read_b128 a[208:211], v4 offset:50752\n"
      "ds_write_addtid_b32 v76 offset:0\n"
      "v_mfma_f32_16x16x32_bf16 v[56:59], a[4:7], a[192:195], 0\n"
      "s_waitcnt lgkmcnt(6)\n"
      "ds_write_addtid_b32 v77 offset:512\n"
      "v_sub_f32 v78, v90, v78\n"
      "v_mfma_f32_4x4x4_16b_bf16 v[20:23], a[216:217], a[224:225], 0\n"
      "v_sub_f32_e64 v79, v91, v79\n"
      "v_cvt_pk_bf16_f32 v38, v38, v39\n"
      "v_mfma_f32_4x4x4_16b_bf16 v[24:27], a[216:217], a[226:227], 0\n"
      "ds_write_addtid_b32 v38 offset:19968\n"
      "ds_write_addtid_b32 v78 offset:1024\n"
      "v_mfma_f32_16x16x32_bf16 v[60:63], a[8:11], a[192:195], 0\n"
      "ds_write_addtid_b32 v79 offset:1536\n"
      "v_sub_f32 v80, v92, v80\n"
      "v_mfma_f32_16x16x32_bf16 v[52:55], a[12:15], a[196:199], v[52:55]\n"
      "s_nop 0\n"
      "ds_read_b128 a[212:215], v4 offset:50816\n"
      "ds_write_addtid_b32 v80 offset:2048\n"
      "v_sub_f32 v81, v93, v81\n"
      "v_mfma_f32_16x16x32_bf16 v[56:59], a[16:19], a[196:199], v[56:59]\n"
      "ds_write_addtid_b32 v81 offset:2560\n"
      "v_sub_f32 v82, v94, v82\n"
      "v_mfma_f32_16x16x32_bf16 v[60:63], a[20:23], a[196:199], v[60:63]\n"
      "ds_write_addtid_b32 v82 offset:3072\n"
      "v_cvt_pk_bf16_f32 v20, v20, v24\n"
      "v_mfma_f32_16x16x32_bf16 v[52:55], a[24:27], a[200:203], v[52:55]\n"
      "ds_read_b128 a[192:195], v5 offset:50688\n"
      "ds_write_addtid_b32 v20 offset:43776\n"
      "v_mfma_f32_16x16x32_bf16 v[56:59], a[28:31], a[200:203], v[56:59]\n"
      "v_sub_f32 v83, v95, v83\n"
      "v_sub_f32_e64 v84, v96, v84\n"
      "v_cvt_pk_bf16_f32 v21, v21, v25\n"
      "v_mfma_f32_16x16x32_bf16 v[60:63], a[32:35], a[200:203], v[60:63]\n"
      "s_waitcnt lgkmcnt(4)\n"
      "ds_write_addtid_b32 v21 offset:47872\n"
      "ds_write_addtid_b32 v83 offset:3584\n"
      "v_mfma_f32_16x16x32_bf16 v[52:55], a[36:39], a[204:207], v[52:55]\n"
      "ds_read_b128 a[196:199], v5 offset:50752\n"
      "ds_write_addtid_b32 v84 offset:4096\n"
      "v_mfma_f32_16x16x32_bf16 v[56:59], a[40:43], a[204:207], v[56:59]\n"
      "v_sub_f32 v85, v97, v85\n"
      "v_sub_f32 v86, v98, v86\n"
      "v_cvt_pk_bf16_f32 v22, v22, v26\n"
      "v_mfma_f32_16x16x32_bf16 v[60:63], a[44:47], a[204:207], v[60:63]\n"
      "ds_write_addtid_b32 v22 offset:51968\n"
      "ds_write_addtid_b32 v85 offset:4608\n"
      "v_sub_f32 v87, v99, v87\n"
      "v_mfma_f32_16x16x32_bf16 v[52:55], a[48:51], a[208:211], v[52:55]\n"
      "ds_read_b128 a[200:203], v5 offset:50816\n"
      "ds_write_addtid_b32 v86 offset:5120\n"
      "v_mfma_f32_16x16x32_bf16 v[56:59], a[52:55], a[208:211], v[56:59]\n"
      "s_nop 0\n"
      "ds_write_addtid_b32 v87 offset:5632\n"
      "v_cvt_pk_bf16_f32 v23, v23, v27\n"
      "v_mfma_f32_16x16x32_bf16 v[60:63], a[56:59], a[208:211], v[60:63]\n"
      "ds_write_addtid_b32 v23 offset:56064\n"
      "v_fma_f32 v40, 0.5, v76, -v88\n"
      "v_mfma_f32_16x16x32_bf16 v[52:55], a[60:63], a[212:215], v[52:55]\n"
      "v_fma_f32 v41, 0.5, v77, -v89\n"
      "v_fma_f32 v42, 0.5, v78, -v90\n"
      "v_mfma_f32_16x16x32_bf16 v[56:59], a[64:67], a[212:215], v[56:59]\n"
      "v_fma_f32 v43, 0.5, v79, -v91\n"
      "v_fma_f32 v44, 0.5, v80, -v92\n"
      "v_mfma_f32_16x16x32_bf16 v[60:63], a[68:71], a[212:215], v[60:63]\n"
      "s_waitcnt lgkmcnt(0)\n"
      "v_fma_f32 v45, 0.5, v81, -v93\n"
      "s_barrier\n"
      "v_mfma_f32_16x16x32_bf16 v[52:55], a[72:75], a[192:195], v[52:55]\n"
      "s_add_u32 s59, s62, 10420224\n"
      "ds_read_b128 a[204:207], v0 offset:16384\n"
      "ds_read2st64_b32 v[28:29], v6 offset0:0 offset1:2\n"
      "v_fma_f32 v46, 0.5, v82, -v94\n"
      "v_mfma_f32_16x16x32_bf16 v[56:59], a[76:79], a[192:195], v[56:59]\n"
      "ds_read_b128 a[208:211], v0 offset:20480\n"
      "ds_read2st64_b32 v[30:31], v6 offset0:4 offset1:6\n"
      "v_mfma_f32_16x16x32_bf16 v[60:63], a[80:83], a[192:195], v[60:63]\n"
      "ds_read2st64_b32 v[32:33], v6 offset0:8 offset1:10\n"
      "v_fma_f32 v47, 0.5, v83, -v95\n"
      "v_mfma_f32_16x16x32_bf16 v[52:55], a[84:87], a[196:199], v[52:55]\n"
      "ds_read_b128 a[212:215], v0 offset:16448\n"
      "v_fma_f32 v48, 0.5, v84, -v96\n"
      "v_mfma_f32_16x16x32_bf16 v[56:59], a[88:91], a[196:199], v[56:59]\n"
      "ds_read2st64_b32 v[34:35], v6 offset0:12 offset1:14\n"
      "ds_read2st64_b32 v[36:37], v6 offset0:16 offset1:18\n"
      "v_mfma_f32_16x16x32_bf16 v[60:63], a[92:95], a[196:199], v[60:63]\n"
      "s_nop 0\n"
      "v_bfe_u32 v16, v11, 6, 1\n"
      "v_mul_i32_i24 v17, 12296, v16\n"
      "s_mov_b32 s50, -8200\n"
      "v_add3_u32 v12, v8, v17, s50\n"
      "v_add_u32 v14, 128, v12\n"
      "v_mul_i32_i24 v17, 8, v16\n"
      "s_mov_b32 s50, -4104\n"
      "v_add3_u32 v13, v8, v17, s50\n"
      "v_add_u32 v15, 128, v13\n"
      "v_mfma_f32_16x16x32_bf16 v[52:55], a[96:99], a[200:203], v[52:55]\n"
      "ds_read_b128 a[192:195], v0 offset:20544\n"
      "ds_read2st64_b32 a[232:233], v12 offset0:4 offset1:6\n"
      "v_mfma_f32_16x16x32_bf16 v[56:59], a[100:103], a[200:203], v[56:59]\n"
      "ds_read2st64_b32 a[234:235], v14 offset0:4 offset1:6\n"
      "ds_read2st64_b32 v[38:39], v6 offset0:20 offset1:22\n"
      "v_fma_f32 v49, 0.5, v85, -v97\n"
      "v_fma_f32 v50, 0.5, v86, -v98\n"
      "v_fma_f32 v51, 0.5, v87, -v99\n"
      "v_mfma_f32_16x16x32_bf16 v[60:63], a[104:107], a[200:203], v[60:63]\n"
      "s_waitcnt lgkmcnt(6)\n"
      "v_fmac_f32 v28, s46, v64\n"
      "v_fmac_f32 v29, s46, v65\n"
      "v_mfma_f32_16x16x32_bf16 v[76:79], v[252:255], a[204:207], v[52:55]\n"
      "ds_read_b128 a[196:199], v0 offset:16512\n"
      "v_fmac_f32 v30, s46, v66\n"
      "v_mfma_f32_16x16x32_bf16 v[80:83], v[244:247], a[204:207], v[56:59]\n"
      "v_fmac_f32 v31, s46, v67\n"
      "v_sub_f32 v28, v28, v40\n"
      "v_mfma_f32_16x16x32_bf16 v[84:87], v[236:239], a[204:207], v[60:63]\n"
      "v_sub_f32 v29, v29, v41\n"
      "v_sub_f32 v30, v30, v42\n"
      "v_mfma_f32_16x16x32_bf16 v[88:91], v[248:251], a[208:211], v[52:55]\n"
      "ds_read_b128 a[200:203], v0 offset:20608\n"
      "v_sub_f32 v31, v31, v43\n"
      "v_mfma_f32_16x16x32_bf16 v[92:95], v[240:243], a[208:211], v[56:59]\n"
      "v_fmac_f32 v40, s45, v28\n"
      "v_fmac_f32_e64 v41, s45, v29\n"
      "v_mfma_f32_16x16x32_bf16 v[96:99], v[232:235], a[208:211], v[60:63]\n"
      "v_fmac_f32 v42, s45, v30\n"
      "v_fmac_f32 v43, s45, v31\n"
      "v_mfma_f32_16x16x32_bf16 v[76:79], v[228:231], a[212:215], v[76:79]\n"
      "ds_read_b128 a[204:207], v1 offset:16384\n"
      "v_fma_f32 v28, s47, v28, v52\n"
      "v_mfma_f32_16x16x32_bf16 v[80:83], v[220:223], a[212:215], v[80:83]\n"
      "v_fma_f32 v29, s47, v29, v53\n"
      "v_mfma_f32_16x16x32_bf16 v[84:87], v[212:215], a[212:215], v[84:87]\n"
      "s_waitcnt lgkmcnt(1)\n"
      "v_fma_f32 v30, s47, v30, v54\n"
      "v_mfma_f32_16x16x32_bf16 v[88:91], v[224:227], a[192:195], v[88:91]\n"
      "ds_read_b128 a[208:211], v1 offset:20480\n"
      "v_fma_f32 v31, s47, v31, v55\n"
      "v_mfma_f32_16x16x32_bf16 v[92:95], v[216:219], a[192:195], v[92:95]\n"
      "v_cvt_pk_bf16_f32 v40, v40, v41\n"
      "v_mfma_f32_16x16x32_bf16 v[96:99], v[208:211], a[192:195], v[96:99]\n"
      "s_nop 0\n"
      "buffer_store_dwordx4 a[232:235], v11, s[8:11], s59 offen offset:0 sc1\n"
      "v_mfma_f32_16x16x32_bf16 v[76:79], v[204:207], a[196:199], v[76:79]\n"
      "ds_read_b128 a[212:215], v1 offset:16448\n"
      "ds_write_addtid_b32 v40 offset:12288\n"
      "v_mfma_f32_16x16x32_bf16 v[80:83], v[196:199], a[196:199], v[80:83]\n"
      "ds_read2st64_b32 a[236:237], v13 offset0:4 offset1:6\n"
      "ds_read2st64_b32 a[238:239], v15 offset0:4 offset1:6\n"
      "v_mfma_f32_16x16x32_bf16 v[84:87], v[188:191], a[196:199], v[84:87]\n"
      "v_cvt_pk_bf16_f32 v42, v42, v43\n"
      "v_cvt_pk_bf16_f32 v28, v28, v29\n"
      "v_mfma_f32_16x16x32_bf16 v[88:91], v[200:203], a[200:203], v[88:91]\n"
      "ds_read_b128 a[192:195], v1 offset:20544\n"
      "ds_write_addtid_b32 v42 offset:12800\n"
      "v_mfma_f32_16x16x32_bf16 v[92:95], v[192:195], a[200:203], v[92:95]\n"
      "ds_write_addtid_b32 v28 offset:14336\n"
      "v_cvt_pk_bf16_f32 v30, v30, v31\n"
      "v_mfma_f32_16x16x32_bf16 v[96:99], v[184:187], a[200:203], v[96:99]\n"
      "s_waitcnt lgkmcnt(6)\n"
      "ds_write_addtid_b32 v30 offset:14848\n"
      "v_fmac_f32 v32, s46, v68\n"
      "v_mfma_f32_16x16x32_bf16 v[76:79], v[180:183], a[204:207], v[76:79]\n"
      "ds_read_b128 a[196:199], v1 offset:16512\n"
      "v_fmac_f32 v33, s46, v69\n"
      "v_mfma_f32_16x16x32_bf16 v[80:83], v[172:175], a[204:207], v[80:83]\n"
      "v_fmac_f32 v34, s46, v70\n"
      "v_fmac_f32_e64 v35, s46, v71\n"
      "v_mfma_f32_16x16x32_bf16 v[84:87], v[164:167], a[204:207], v[84:87]\n"
      "v_sub_f32 v32, v32, v44\n"
      "v_sub_f32 v33, v33, v45\n"
      "v_mfma_f32_16x16x32_bf16 v[88:91], v[176:179], a[208:211], v[88:91]\n"
      "ds_read_b128 a[200:203], v1 offset:20608\n"
      "v_sub_f32 v34, v34, v46\n"
      "v_mfma_f32_16x16x32_bf16 v[92:95], v[168:171], a[208:211], v[92:95]\n"
      "v_sub_f32 v35, v35, v47\n"
      "v_fmac_f32 v44, s45, v32\n"
      "v_mfma_f32_16x16x32_bf16 v[96:99], v[160:163], a[208:211], v[96:99]\n"
      "v_fmac_f32 v45, s45, v33\n"
      "v_fmac_f32 v46, s45, v34\n"
      "v_mfma_f32_16x16x32_bf16 v[76:79], v[156:159], a[212:215], v[76:79]\n"
      "ds_read_b128 a[204:207], v2 offset:16384\n"
      "v_fmac_f32 v47, s45, v35\n"
      "v_mfma_f32_16x16x32_bf16 v[80:83], v[148:151], a[212:215], v[80:83]\n"
      "s_nop 0\n"
      "v_fma_f32 v32, s47, v32, v56\n"
      "v_fma_f32 v33, s47, v33, v57\n"
      "v_mfma_f32_16x16x32_bf16 v[84:87], v[140:143], a[212:215], v[84:87]\n"
      "s_waitcnt lgkmcnt(1)\n"
      "v_fma_f32 v34, s47, v34, v58\n"
      "v_fma_f32 v35, s47, v35, v59\n"
      "v_mfma_f32_16x16x32_bf16 v[88:91], v[152:155], a[192:195], v[88:91]\n"
      "buffer_store_dwordx4 a[236:239], v11, s[8:11], s59 offen offset:128 sc1\n"
      "ds_read_b128 a[208:211], v2 offset:20480\n"
      "v_mfma_f32_16x16x32_bf16 v[92:95], v[144:147], a[192:195], v[92:95]\n"
      "v_cvt_pk_bf16_f32 v44, v44, v45\n"
      "v_cvt_pk_bf16_f32 v46, v46, v47\n"
      "v_mfma_f32_16x16x32_bf16 v[96:99], v[136:139], a[192:195], v[96:99]\n"
      "ds_write_addtid_b32 v44 offset:20484\n"
      "ds_read2st64_b32 a[240:241], v8 offset0:4 offset1:6\n"
      "v_mfma_f32_16x16x32_bf16 v[76:79], v[132:135], a[196:199], v[76:79]\n"
      "ds_read_b128 a[212:215], v2 offset:16448\n"
      "ds_write_addtid_b32 v46 offset:20996\n"
      "v_mfma_f32_16x16x32_bf16 v[80:83], v[124:127], a[196:199], v[80:83]\n"
      "ds_read2st64_b32 a[242:243], v9 offset0:4 offset1:6\n"
      "v_cvt_pk_bf16_f32 v32, v32, v33\n"
      "v_mfma_f32_16x16x32_bf16 v[84:87], v[116:119], a[196:199], v[84:87]\n"
      "ds_write_addtid_b32 v32 offset:22532\n"
      "v_cvt_pk_bf16_f32 v34, v34, v35\n"
      "v_mfma_f32_16x16x32_bf16 v[88:91], v[128:131], a[200:203], v[88:91]\n"
      "ds_read_b128 a[192:195], v2 offset:20544\n"
      "ds_write_addtid_b32 v34 offset:23044\n"
      "v_mfma_f32_16x16x32_bf16 v[92:95], v[120:123], a[200:203], v[92:95]\n"
      "v_fmac_f32 v36, s46, v72\n"
      "v_fmac_f32 v37, s46, v73\n"
      "v_mfma_f32_16x16x32_bf16 v[96:99], v[112:115], a[200:203], v[96:99]\n"
      "s_waitcnt lgkmcnt(5)\n"
      "v_fmac_f32 v38, s46, v74\n"
      "v_fmac_f32 v39, s46, v75\n"
      "v_mfma_f32_16x16x32_bf16 v[76:79], v[108:111], a[204:207], v[76:79]\n"
      "ds_read_b128 a[196:199], v2 offset:16512\n"
      "v_sub_f32 v36, v36, v48\n"
      "v_mfma_f32_16x16x32_bf16 v[80:83], v[100:103], a[204:207], v[80:83]\n"
      "v_sub_f32 v37, v37, v49\n"
      "v_sub_f32 v38, v38, v50\n"
      "v_mfma_f32_16x16x32_bf16 v[84:87], a[160:163], a[204:207], v[84:87]\n"
      "v_sub_f32 v39, v39, v51\n"
      "v_fmac_f32 v48, s45, v36\n"
      "v_mfma_f32_16x16x32_bf16 v[88:91], v[104:107], a[208:211], v[88:91]\n"
      "ds_read_b128 a[200:203], v2 offset:20608\n"
      "v_fmac_f32 v49, s45, v37\n"
      "v_mfma_f32_16x16x32_bf16 v[92:95], a[164:167], a[208:211], v[92:95]\n"
      "v_fmac_f32 v50, s45, v38\n"
      "v_fmac_f32 v51, s45, v39\n"
      "v_mfma_f32_16x16x32_bf16 v[96:99], a[156:159], a[208:211], v[96:99]\n"
      "s_nop 0\n"
      "v_fma_f32 v36, s47, v36, v60\n"
      "v_fma_f32 v37, s47, v37, v61\n"
      "v_mfma_f32_16x16x32_bf16 v[76:79], a[152:155], a[212:215], v[76:79]\n"
      "s_waitcnt lgkmcnt(1)\n"
      "ds_read_b128 a[204:207], v3 offset:0\n"
      "v_fma_f32 v38, s47, v38, v62\n"
      "v_mfma_f32_16x16x32_bf16 v[80:83], a[144:147], a[212:215], v[80:83]\n"
      "s_nop 0\n"
      "buffer_store_dwordx4 a[240:243], v11, s[8:11], s59 offen offset:256 sc1\n"
      "v_mfma_f32_16x16x32_bf16 v[84:87], a[136:139], a[212:215], v[84:87]\n"
      "v_fma_f32 v39, s47, v39, v63\n"
      "v_cvt_pk_bf16_f32 v48, v48, v49\n"
      "v_mfma_f32_16x16x32_bf16 v[88:91], a[148:151], a[192:195], v[88:91]\n"
      "ds_read_b128 a[208:211], v3 offset:64\n"
      "ds_write_addtid_b32 v48 offset:16384\n"
      "v_mfma_f32_16x16x32_bf16 v[92:95], a[140:143], a[192:195], v[92:95]\n"
      "v_cvt_pk_bf16_f32 v50, v50, v51\n"
      "v_cvt_pk_bf16_f32 v36, v36, v37\n"
      "v_mfma_f32_16x16x32_bf16 v[96:99], a[132:135], a[192:195], v[96:99]\n"
      "ds_write_addtid_b32 v50 offset:16896\n"
      "ds_write_addtid_b32 v36 offset:18432\n"
      "v_mfma_f32_16x16x32_bf16 v[76:79], a[128:131], a[196:199], v[76:79]\n"
      "ds_read_b128 a[212:215], v3 offset:128\n"
      "v_cvt_pk_bf16_f32 v38, v38, v39\n"
      "v_mfma_f32_16x16x32_bf16 v[80:83], a[120:123], a[196:199], v[80:83]\n"
      "ds_write_addtid_b32 v38 offset:18944\n"
      "v_mfma_f32_16x16x32_bf16 v[84:87], a[112:115], a[196:199], v[84:87]\n"
      "s_waitcnt lgkmcnt(4)\n"
      "v_mfma_f32_16x16x32_bf16 v[88:91], a[124:127], a[200:203], v[88:91]\n"
      "ds_read_b128 a[192:195], v4 offset:0\n"
      "v_mfma_f32_16x16x32_bf16 v[92:95], a[116:119], a[200:203], v[92:95]\n"
      "v_mfma_f32_16x16x32_bf16 v[96:99], a[108:111], a[200:203], v[96:99]\n"
      "v_mfma_f32_16x16x32_bf16 v[64:67], a[0:3], a[204:207], 0\n"
      "s_nop 0\n"
      "ds_read_b128 a[196:199], v4 offset:64\n"
      "v_mfma_f32_16x16x32_bf16 v[68:71], a[4:7], a[204:207], 0\n"
      "v_mfma_f32_16x16x32_bf16 v[72:75], a[8:11], a[204:207], 0\n"
      "v_mfma_f32_16x16x32_bf16 v[64:67], a[12:15], a[208:211], v[64:67]\n"
      "ds_read_b128 a[200:203], v4 offset:128\n"
      "v_sub_f32 v76, v88, v76\n"
      "v_sub_f32 v77, v89, v77\n"
      "v_sub_f32 v78, v90, v78\n"
      "v_sub_f32 v79, v91, v79\n"
      "v_sub_f32 v80, v92, v80\n"
      "v_sub_f32 v81, v93, v81\n"
      "v_mfma_f32_16x16x32_bf16 v[68:71], a[16:19], a[208:211], v[68:71]\n"
      "ds_write_addtid_b32 v76 offset:6144\n"
      "ds_write_addtid_b32 v77 offset:6656\n"
      "ds_write_addtid_b32 v78 offset:7168\n"
      "v_sub_f32 v82, v94, v82\n"
      "v_sub_f32 v83, v95, v83\n"
      "v_sub_f32 v84, v96, v84\n"
      "v_sub_f32_e64 v85, v97, v85\n"
      "v_mfma_f32_16x16x32_bf16 v[72:75], a[20:23], a[208:211], v[72:75]\n"
      "s_waitcnt lgkmcnt(3)\n"
      "ds_write_addtid_b32 v79 offset:7680\n"
      "ds_write_addtid_b32 v80 offset:8192\n"
      "ds_write_addtid_b32 v81 offset:8704\n"
      "v_sub_f32 v86, v98, v86\n"
      "v_sub_f32 v87, v99, v87\n"
      "v_mfma_f32_16x16x32_bf16 v[64:67], a[24:27], a[212:215], v[64:67]\n"
      "ds_write_addtid_b32 v82 offset:9216\n"
      "ds_write_addtid_b32 v83 offset:9728\n"
      "ds_write_addtid_b32 v84 offset:10240\n"
      "v_mfma_f32_16x16x32_bf16 v[68:71], a[28:31], a[212:215], v[68:71]\n"
      "ds_write_addtid_b32 v85 offset:10752\n"
      "ds_write_addtid_b32 v86 offset:11264\n"
      "ds_write_addtid_b32 v87 offset:11776\n"
      "v_mfma_f32_16x16x32_bf16 v[72:75], a[32:35], a[212:215], v[72:75]\n"
      "s_waitcnt lgkmcnt(0)\n"
      "s_barrier\n"
      "v_mfma_f32_16x16x32_bf16 v[64:67], a[36:39], a[192:195], v[64:67]\n"
      "s_add_u32 s59, s59, 786432\n"
      "ds_read_b128 a[204:207], v5 offset:0\n"
      "ds_read2st64_b32 v[28:29], v6 offset0:24 offset1:26\n"
      "v_mfma_f32_16x16x32_bf16 v[68:71], a[40:43], a[192:195], v[68:71]\n"
      "ds_read2st64_b32 v[30:31], v6 offset0:28 offset1:30\n"
      "ds_read2st64_b32 v[32:33], v6 offset0:32 offset1:34\n"
      "v_mfma_f32_16x16x32_bf16 v[72:75], a[44:47], a[192:195], v[72:75]\n"
      "ds_read2st64_b32 v[34:35], v6 offset0:36 offset1:38\n"
      "ds_read2st64_b32 v[36:37], v6 offset0:40 offset1:42\n"
      "v_mfma_f32_16x16x32_bf16 v[64:67], a[48:51], a[196:199], v[64:67]\n"
      "ds_read_b128 a[208:211], v5 offset:64\n"
      "ds_read2st64_b32 v[38:39], v6 offset0:44 offset1:46\n"
      "v_mfma_f32_16x16x32_bf16 v[68:71], a[52:55], a[196:199], v[68:71]\n"
      "v_fma_f32 v40, 0.5, v76, -v88\n"
      "v_fma_f32 v41, 0.5, v77, -v89\n"
      "v_mfma_f32_16x16x32_bf16 v[72:75], a[56:59], a[196:199], v[72:75]\n"
      "ds_read2st64_b32 a[232:233], v12 offset0:0 offset1:2\n"
      "ds_read2st64_b32 a[234:235], v14 offset0:0 offset1:2\n"
      "v_mfma_f32_16x16x32_bf16 v[64:67], a[60:63], a[200:203], v[64:67]\n"
      "ds_read_b128 a[212:215], v5 offset:128\n"
      "v_fma_f32 v42, 0.5, v78, -v90\n"
      "v_mfma_f32_16x16x32_bf16 v[68:71], a[64:67], a[200:203], v[68:71]\n"
      "ds_read2st64_b32 a[236:237], v13 offset0:0 offset1:2\n"
      "ds_read2st64_b32 a[238:239], v15 offset0:0 offset1:2\n"
      "v_mfma_f32_16x16x32_bf16 v[72:75], a[68:71], a[200:203], v[72:75]\n"
      "s_waitcnt lgkmcnt(9)\n"
      "v_fma_f32 v43, 0.5, v79, -v91\n"
      "v_fma_f32 v44, 0.5, v80, -v92\n"
      "v_mfma_f32_16x16x32_bf16 v[64:67], a[72:75], a[204:207], v[64:67]\n"
      "s_waitcnt lgkmcnt(3)\n"
      "buffer_store_dwordx4 a[232:235], v11, s[8:11], s59 offen offset:0 sc1\n"
      "v_mfma_f32_16x16x32_bf16 v[68:71], a[76:79], a[204:207], v[68:71]\n"
      "v_fma_f32 v45, 0.5, v81, -v93\n"
      "v_fma_f32 v46, 0.5, v82, -v94\n"
      "v_mfma_f32_16x16x32_bf16 v[72:75], a[80:83], a[204:207], v[72:75]\n"
      "ds_read2st64_b32 a[240:241], v8 offset0:0 offset1:2\n"
      "ds_read2st64_b32 a[242:243], v9 offset0:0 offset1:2\n"
      "v_mfma_f32_16x16x32_bf16 v[64:67], a[84:87], a[208:211], v[64:67]\n"
      "s_waitcnt lgkmcnt(2)\n"
      "buffer_store_dwordx4 a[236:239], v11, s[8:11], s59 offen offset:128 sc1\n"
      "v_mfma_f32_16x16x32_bf16 v[68:71], a[88:91], a[208:211], v[68:71]\n"
      "s_nop 0\n"
      "v_fma_f32 v47, 0.5, v83, -v95\n"
      "v_fma_f32 v48, 0.5, v84, -v96\n"
      "v_mfma_f32_16x16x32_bf16 v[72:75], a[92:95], a[208:211], v[72:75]\n"
      "v_fma_f32 v49, 0.5, v85, -v97\n"
      "v_fma_f32 v50, 0.5, v86, -v98\n"
      "v_mfma_f32_16x16x32_bf16 v[64:67], a[96:99], a[212:215], v[64:67]\n"
      "s_waitcnt lgkmcnt(0)\n"
      "buffer_store_dwordx4 a[240:243], v11, s[8:11], s59 offen offset:256 sc1\n"
      "v_mfma_f32_16x16x32_bf16 v[68:71], a[100:103], a[212:215], v[68:71]\n"
      "s_nop 0\n"
      "v_fma_f32 v51, 0.5, v87, -v99\n"
      "v_fmac_f32 v28, s46, v52\n"
      "v_mfma_f32_16x16x32_bf16 v[72:75], a[104:107], a[212:215], v[72:75]\n"
      "v_fmac_f32 v29, s46, v53\n"
      "v_fmac_f32 v30, s46, v54\n"
      "v_fmac_f32 v31, s46, v55\n"
      "v_sub_f32 v28, v28, v40\n"
      "v_sub_f32 v29, v29, v41\n"
      "v_sub_f32 v30, v30, v42\n"
      "v_sub_f32 v31, v31, v43\n"
      "v_fmac_f32 v40, s45, v28\n"
      "v_fmac_f32 v41, s45, v29\n"
      "v_pk_fma_f32 v[42:43], v[30:31], s[44:45], v[42:43] op_sel:[0,1,0] op_sel_hi:[1,1,1]\n"
      "v_cvt_pk_bf16_f32 v40, v40, v41\n"
      "v_cvt_pk_bf16_f32 v42, v42, v43\n"
      "v_pk_fma_f32 v[32:33], v[56:57], s[46:47], v[32:33] op_sel:[0,0,0] op_sel_hi:[1,0,1]\n"
      "v_pk_fma_f32 v[34:35], v[58:59], s[46:47], v[34:35] op_sel:[0,0,0] op_sel_hi:[1,0,1]\n"
      "v_pk_add_f32 v[32:33], v[32:33], v[44:45] neg_lo:[0,1] neg_hi:[0,1]\n"
      "v_pk_add_f32 v[34:35], v[34:35], v[46:47] neg_lo:[0,1] neg_hi:[0,1]\n"
      "v_pk_fma_f32 v[44:45], v[32:33], s[44:45], v[44:45] op_sel:[0,1,0] op_sel_hi:[1,1,1]\n"
      "v_pk_fma_f32 v[46:47], v[34:35], s[44:45], v[46:47] op_sel:[0,1,0] op_sel_hi:[1,1,1]\n"
      "v_cvt_pk_bf16_f32 v44, v44, v45\n"
      "v_cvt_pk_bf16_f32 v46, v46, v47\n"
      "v_pk_fma_f32 v[36:37], v[60:61], s[46:47], v[36:37] op_sel:[0,0,0] op_sel_hi:[1,0,1]\n"
      "v_pk_fma_f32 v[38:39], v[62:63], s[46:47], v[38:39] op_sel:[0,0,0] op_sel_hi:[1,0,1]\n"
      "v_pk_add_f32 v[36:37], v[36:37], v[48:49] neg_lo:[0,1] neg_hi:[0,1]\n"
      "v_pk_add_f32 v[38:39], v[38:39], v[50:51] neg_lo:[0,1] neg_hi:[0,1]\n"
      "v_pk_fma_f32 v[48:49], v[36:37], s[44:45], v[48:49] op_sel:[0,1,0] op_sel_hi:[1,1,1]\n"
      "v_pk_fma_f32 v[50:51], v[38:39], s[44:45], v[50:51] op_sel:[0,1,0] op_sel_hi:[1,1,1]\n"
      "v_cvt_pk_bf16_f32 v48, v48, v49\n"
      "v_cvt_pk_bf16_f32 v50, v50, v51\n"
      "ds_write_addtid_b32 v40 offset:13312\n"
      "ds_write_addtid_b32 v42 offset:13824\n"
      "ds_write_addtid_b32 v44 offset:21508\n"
      "ds_write_addtid_b32 v46 offset:22020\n"
      "ds_write_addtid_b32 v48 offset:17408\n"
      "ds_write_addtid_b32 v50 offset:17920\n"
      "v_pk_fma_f32 v[28:29], v[28:29], s[46:47], v[64:65] op_sel:[0,1,0] op_sel_hi:[1,1,1]\n"
      "v_pk_fma_f32 v[30:31], v[30:31], s[46:47], v[66:67] op_sel:[0,1,0] op_sel_hi:[1,1,1]\n"
      "v_cvt_pk_bf16_f32 v28, v28, v29\n"
      "v_cvt_pk_bf16_f32 v30, v30, v31\n"
      "v_pk_fma_f32 v[32:33], v[32:33], s[46:47], v[68:69] op_sel:[0,1,0] op_sel_hi:[1,1,1]\n"
      "v_pk_fma_f32 v[34:35], v[34:35], s[46:47], v[70:71] op_sel:[0,1,0] op_sel_hi:[1,1,1]\n"
      "v_cvt_pk_bf16_f32 v32, v32, v33\n"
      "v_cvt_pk_bf16_f32 v34, v34, v35\n"
      "v_pk_fma_f32 v[36:37], v[36:37], s[46:47], v[72:73] op_sel:[0,1,0] op_sel_hi:[1,1,1]\n"
      "v_pk_fma_f32 v[38:39], v[38:39], s[46:47], v[74:75] op_sel:[0,1,0] op_sel_hi:[1,1,1]\n"
      "v_cvt_pk_bf16_f32 v36, v36, v37\n"
      "v_cvt_pk_bf16_f32 v38, v38, v39\n"
      "ds_write_addtid_b32 v28 offset:15360\n"
      "ds_write_addtid_b32 v30 offset:15872\n"
      "ds_write_addtid_b32 v32 offset:23556\n"
      "ds_write_addtid_b32 v34 offset:24068\n"
      "ds_write_addtid_b32 v36 offset:19456\n"
      "ds_write_addtid_b32 v38 offset:19968\n"
      "s_waitcnt lgkmcnt(0)\n"
      "s_barrier\n"
      "ds_read2st64_b32 a[232:233], v12 offset0:4 offset1:6\n"
      "ds_read2st64_b32 a[234:235], v14 offset0:4 offset1:6\n"
      "ds_read2st64_b32 a[236:237], v13 offset0:4 offset1:6\n"
      "ds_read2st64_b32 a[238:239], v15 offset0:4 offset1:6\n"
      "ds_read2st64_b32 a[240:241], v8 offset0:4 offset1:6\n"
      "ds_read2st64_b32 a[242:243], v9 offset0:4 offset1:6\n"
      "s_add_u32 s59, s59, 786432\n"
      "s_waitcnt lgkmcnt(4)\n"
      "buffer_store_dwordx4 a[232:235], v11, s[8:11], s59 offen offset:0 sc1\n"
      "s_waitcnt lgkmcnt(2)\n"
      "buffer_store_dwordx4 a[236:239], v11, s[8:11], s59 offen offset:128 sc1\n"
      "s_waitcnt lgkmcnt(0)\n"
      "buffer_store_dwordx4 a[240:243], v11, s[8:11], s59 offen offset:256 sc1\n"
      "LSend_%=:\n"
      "Lfin_%=:\n"
      "s_waitcnt vmcnt(0) lgkmcnt(0)\n"
      "s_endpgm\n"
      ".rept 512\n"
      "s_nop 0\n"
      ".endr\n"
      ".Lwg_end:\n"
      : "+{v0}"(tid)
      : "{s[0:1]}"(__builtin_amdgcn_kernarg_segment_ptr()), "{s2}"(blockIdx.x)
      : "v1", "v2", "v3", "v4", "v5", "v6", "v7", "v8", "v9", "v10", "v11", "v12", "v13", "v14", "v15", "v16", "v17", "v18", "v19", "v20", "v21", "v22", "v23", "v24", "v25", "v26", "v27", "v28", "v29", "v30", "v31", "v32", "v33", "v34", "v35", "v36", "v37", "v38", "v39", "v40", "v41", "v42", "v43", "v44", "v45", "v46", "v47", "v48", "v49", "v50", "v51", "v52", "v53", "v54", "v55", "v56", "v57", "v58", "v59", "v60", "v61", "v62", "v63", "v64", "v65", "v66", "v67", "v68", "v69", "v70", "v71", "v72", "v73", "v74", "v75", "v76", "v77", "v78", "v79", "v80", "v81", "v82", "v83", "v84", "v85", "v86", "v87", "v88", "v89", "v90", "v91", "v92", "v93", "v94", "v95", "v96", "v97", "v98", "v99", "v100", "v101", "v102", "v103", "v104", "v105", "v106", "v107", "v108", "v109", "v110", "v111", "v112", "v113", "v114", "v115", "v116", "v117", "v118", "v119", "v120", "v121", "v122", "v123", "v124", "v125", "v126", "v127", "v128", "v129", "v130", "v131", "v132", "v133", "v134", "v135", "v136", "v137", "v138", "v139", "v140", "v141", "v142", "v143", "v144", "v145", "v146", "v147", "v148", "v149", "v150", "v151", "v152", "v153", "v154", "v155", "v156", "v157", "v158", "v159", "v160", "v161", "v162", "v163", "v164", "v165", "v166", "v167", "v168", "v169", "v170", "v171", "v172", "v173", "v174", "v175", "v176", "v177", "v178", "v179", "v180", "v181", "v182", "v183", "v184", "v185", "v186", "v187", "v188", "v189", "v190", "v191", "v192", "v193", "v194", "v195", "v196", "v197", "v198", "v199", "v200", "v201", "v202", "v203", "v204", "v205", "v206", "v207", "v208", "v209", "v210", "v211", "v212", "v213", "v214", "v215", "v216", "v217", "v218", "v219", "v220", "v221", "v222", "v223", "v224", "v225", "v226", "v227", "v228", "v229", "v230", "v231", "v232", "v233", "v234", "v235", "v236", "v237", "v238", "v239", "v240", "v241", "v242", "v243", "v244", "v245", "v246", "v247", "v248", "v249", "v250", "v251", "v252", "v253", "v254", "v255", "a0", "a1", "a2", "a3", "a4", "a5", "a6", "a7", "a8", "a9", "a10", "a11", "a12", "a13", "a14", "a15", "a16", "a17", "a18", "a19", "a20", "a21", "a22", "a23", "a24", "a25", "a26", "a27", "a28", "a29", "a30", "a31", "a32", "a33", "a34", "a35", "a36", "a37", "a38", "a39", "a40", "a41", "a42", "a43", "a44", "a45", "a46", "a47", "a48", "a49", "a50", "a51", "a52", "a53", "a54", "a55", "a56", "a57", "a58", "a59", "a60", "a61", "a62", "a63", "a64", "a65", "a66", "a67", "a68", "a69", "a70", "a71", "a72", "a73", "a74", "a75", "a76", "a77", "a78", "a79", "a80", "a81", "a82", "a83", "a84", "a85", "a86", "a87", "a88", "a89", "a90", "a91", "a92", "a93", "a94", "a95", "a96", "a97", "a98", "a99", "a100", "a101", "a102", "a103", "a104", "a105", "a106", "a107", "a108", "a109", "a110", "a111", "a112", "a113", "a114", "a115", "a116", "a117", "a118", "a119", "a120", "a121", "a122", "a123", "a124", "a125", "a126", "a127", "a128", "a129", "a130", "a131", "a132", "a133", "a134", "a135", "a136", "a137", "a138", "a139", "a140", "a141", "a142", "a143", "a144", "a145", "a146", "a147", "a148", "a149", "a150", "a151", "a152", "a153", "a154", "a155", "a156", "a157", "a158", "a159", "a160", "a161", "a162", "a163", "a164", "a165", "a166", "a167", "a168", "a169", "a170", "a171", "a172", "a173", "a174", "a175", "a176", "a177", "a178", "a179", "a180", "a181", "a182", "a183", "a184", "a185", "a186", "a187", "a188", "a189", "a190", "a191", "a192", "a193", "a194", "a195", "a196", "a197", "a198", "a199", "a200", "a201", "a202", "a203", "a204", "a205", "a206", "a207", "a208", "a209", "a210", "a211", "a212", "a213", "a214", "a215", "a216", "a217", "a218", "a219", "a220", "a221", "a222", "a223", "a224", "a225", "a226", "a227", "a228", "a229", "a230", "a231", "a232", "a233", "a234", "a235", "a236", "a237", "a238", "a239", "a240", "a241", "a242", "a243", "a244", "a245", "a246", "a247", "a248", "a249", "a250", "a251", "a252", "a253", "a254", "a255", "s3", "s4", "s5", "s6", "s7", "s8", "s9", "s10", "s11", "s12", "s13", "s14", "s15", "s16", "s17", "s18", "s19", "s20", "s21", "s22", "s23", "s24", "s25", "s26", "s27", "s28", "s29", "s30", "s31", "s33", "s34", "s35", "s36", "s37", "s38", "s39", "s40", "s41", "s42", "s43", "s44", "s45", "s46", "s47", "s48", "s49", "s50", "s51", "s52", "s53", "s54", "s55", "s56", "s57", "s58", "s59", "s60", "s61", "s62", "s63", "s64", "s65", "s66", "s67", "s68", "s69", "s70", "s71", "s72", "s73", "s74", "s75", "s76", "s77", "s78", "s79", "s80", "s81", "s82", "s83", "s84", "s85", "s86", "s87", "s88", "s89", "s90", "s91", "s92", "s93", "s94", "s95", "s96", "s97", "s98", "s99", "scc", "vcc", "memory");
  asm volatile("" :: "v"((unsigned)(uintptr_t)smem) : "memory");
}

extern "C" void kernel_launch(void* const* d_in, const int* in_sizes, int n_in, void* d_out,
                              int out_size, void* d_ws, size_t ws_size, hipStream_t stream) {
  conv_kernel<<<256, 256, 0, stream>>>((const char*)d_in[0], (const char*)d_in[1], (char*)d_out, (char*)d_ws);
}
```

```cpp
#include <hip/hip_runtime.h>
#include <hip/hip_bf16.h>
#include <stdint.h>

__global__ void __launch_bounds__(256, 1)
conv_kernel(const char* __restrict__ x, const char* __restrict__ w, char* __restrict__ out, char* __restrict__ dbg) {
  __shared__ __attribute__((aligned(1024))) char smem[163840];
  unsigned tid = threadIdx.x;
  asm volatile(
      "s_load_dwordx2 s[12:13], s[0:1], 8\n"
      "s_load_dwordx2 s[4:5], s[0:1], 0\n"
      "s_load_dwordx2 s[8:9], s[0:1], 16\n"
      "v_readfirstlane_b32 s16, v0\n"
      "s_getpc_b64 s[26:27]\n"
      ".Lwg_pc:\n"
      "v_and_b32 v37, 3, v0\n"
      "s_lshr_b32 s23, s2, 3\n"
      "s_lshl_b32 s28, s16, 3\n"
      "v_lshlrev_b32 v40, 7, v37\n"
      "s_lshl_b32 s29, s23, 11\n"
      "s_mov_b32 m0, 150016\n"
      "s_add_u32 s28, s28, s29\n"
      "v_and_b32 v36, 63, v0\n"
      "v_add_u32 v40, s28, v40\n"
      "s_lshr_b32 s16, s16, 6\n"
      "v_min_u32 v40, .Lwg_end-.Lwg_pc-4, v40\n"
      "s_and_b32 s18, s16, 1\n"
      "global_load_lds_dword v40, s[26:27]\n"
      "v_lshlrev_b32 v17, 4, v36\n"
      "s_lshr_b32 s17, s16, 1\n"
      "s_bfe_u32 s28, s2, 0x20001\n"
      "s_mul_i32 s19, s28, 12582912\n"
      "s_and_b32 s28, s23, 1\n"
      "s_mul_i32 s20, s28, 98304\n"
      "s_lshr_b32 s28, s23, 1\n"
      "s_mul_i32 s29, s28, 3072\n"
      "s_bitcmp1_b32 s2, 0\n"
      "s_cselect_b32 s28, 49152, 0\n"
      "s_add_u32 s21, s20, s29\n"
      "s_add_u32 s21, s21, s28\n"
      "s_add_u32 s6, s19, 12783616\n"
      "s_min_u32 s6, s6, 50335744\n"
      "s_mov_b32 s7, 0x20000\n"
      "s_mov_b32 s10, 50331648\n"
      "s_mov_b32 s11, 0x20000\n"
      "s_mov_b32 s14, 165888\n"
      "s_mov_b32 s15, 0x20000\n"
      "s_mul_i32 s63, s18, 3072\n"
      "s_add_u32 s54, s19, s20\n"
      "s_sub_u32 s54, s54, 196608\n"
      "s_lshl_b32 s64, s18, 8\n"
      "s_mul_i32 s28, s17, 24832\n"
      "s_add_u32 s64, s64, s28\n"
      "s_mov_b32 s55, 0\n"
      "v_bitop3_b32 v18, v36, v17, 32 bitop3:0x6c\n"
      "v_xor_b32 v19, 32, v17\n"
      "v_bfe_u32 v38, v36, 2, 2\n"
      "v_lshrrev_b32 v39, 4, v36\n"
      "s_cmp_eq_u32 s17, 1\n"
      "s_cbranch_scc1 LBD_%=\n"
      "v_lshlrev_b32 v40, 3, v17\n"
      "s_mul_i32 s53, s18, 2688\n"
      "s_mul_i32 s34, s23, 5184\n"
      "v_min_u32 v40, 2560, v40\n"
      "s_add_u32 s53, s53, s34\n"
      "v_lshlrev_b32 v43, 4, v37\n"
      "s_mul_i32 s26, s17, 0x8080\n"
      "s_sub_u32 s26, 0xbf80, s26\n"
      "s_mov_b32 s27, 0\n"
      "s_lshl_b32 s34, s17, 7\n"
      "s_mov_b32 s29, 0\n"
      "s_sub_u32 s28, s26, s34\n"
      "s_mov_b32 s31, 0\n"
      "s_sub_u32 s30, s28, s34\n"
      "v_lshlrev_b64 v[44:45], v43, s[26:27]\n"
      "v_lshlrev_b64 v[46:47], v43, s[28:29]\n"
      "s_xor_b32 s28, s28, 0x8000\n"
      "v_lshlrev_b64 v[48:49], v43, s[30:31]\n"
      "v_lshlrev_b64 v[50:51], v43, s[28:29]\n"
      "v_accvgpr_write_b32 a244, v44\n"
      "v_accvgpr_write_b32 a245, v45\n"
      "v_accvgpr_write_b32 a246, v46\n"
      "v_accvgpr_write_b32 a247, v47\n"
      "v_accvgpr_write_b32 a248, v48\n"
      "v_accvgpr_write_b32 a249, v49\n"
      "v_accvgpr_write_b32 a250, v50\n"
      "v_accvgpr_write_b32 a251, v51\n"
      "s_waitcnt lgkmcnt(0)\n"
      "s_and_b32 s13, s13, 0xffff\n"
      "buffer_load_dword v40, s[12:15], s53 offen lds\n"
      "s_add_u32 m0, s63, 30976\n"
      "s_add_u32 s52, s63, 55296\n"
      "buffer_load_dwordx4 v17, s[12:15], s52 offen offset:0 lds\n"
      "buffer_load_dwordx4 v18, s[12:15], s52 offen offset:1024 lds\n"
      "buffer_load_dwordx4 v19, s[12:15], s52 offen offset:2048 lds\n"
      "s_add_u32 m0, s63, 140800\n"
      "s_add_u32 s52, s63, 110592\n"
      "buffer_load_dwordx4 v17, s[12:15], s52 offen offset:0 lds\n"
      "buffer_load_dwordx4 v18, s[12:15], s52 offen offset:1024 lds\n"
      "buffer_load_dwordx4 v19, s[12:15], s52 offen offset:2048 lds\n"
      "s_branch LS_%=\n"
      "LBD_%=:\n"
      "s_sub_u32 s26, s21, s20\n"
      "s_lshl_b32 s37, s18, 11\n"
      "s_add_u32 s26, s26, s37\n"
      "s_add_u32 s27, s26, 3712\n"
      "v_add_u32 v44, s27, v17\n"
      "s_cmp_eq_u32 s18, 1\n"
      "s_cselect_b32 s28, 16, 48\n"
      "s_cselect_b32 s29, 0, 32\n"
      "s_cselect_b32 s27, 48, 64\n"
      "v_cmp_le_u32 vcc, s28, v36\n"
      "v_cmp_gt_u32 s[30:31], s29, v36\n"
      "v_xor_b32 v45, 32, v44\n"
      "s_cmp_eq_u32 s26, 0\n"
      "s_cselect_b32 s28, 24, 0\n"
      "v_cndmask_b32 v47, v44, v45, vcc\n"
      "v_cndmask_b32 v46, v44, v45, s[30:31]\n"
      "v_cmp_le_u32 vcc, s28, v36\n"
      "v_cmp_gt_u32 s[30:31], s27, v36\n"
      "s_cmp_eq_u32 s26, 97280\n"
      "s_cselect_b32 s27, 24, 64\n"
      "v_cmp_gt_u32 s[34:35], s27, v36\n"
      "v_subrev_u32 v45, 256, v44\n"
      "v_cndmask_b32 v8, 2.0, v47, vcc\n"
      "v_cndmask_b32 v48, v45, v46, s[30:31]\n"
      "v_cndmask_b32 v9, 2.0, v48, s[34:35]\n"
      "s_add_u32 s34, s19, s21\n"
      "s_cmp_eq_u32 s19, 0\n"
      "s_cselect_b32 s36, 0x80000000, s54\n"
      "s_waitcnt lgkmcnt(0)\n"
      "s_load_dword s44, s[4:5], s34\n"
      "s_and_b32 s13, s13, 0xffff\n"
      "s_add_u32 m0, s63, 24832\n"
      "s_add_u32 s52, s63, 0\n"
      "buffer_load_dwordx4 v17, s[12:15], s52 offen offset:0 lds\n"
      "buffer_load_dwordx4 v18, s[12:15], s52 offen offset:1024 lds\n"
      "buffer_load_dwordx4 v19, s[12:15], s52 offen offset:2048 lds\n"
      "s_add_u32 s4, s4, -4096\n"
      "s_addc_u32 s5, s5, -1\n"
      "s_and_b32 s5, s5, 0xffff\n"
      "s_add_u32 s53, s54, 196608\n"
      "s_add_u32 m0, s37, 82432\n"
      "s_nop 0\n"
      "buffer_load_dwordx4 v8, s[4:7], s53 offen offset:0 nt lds\n"
      "buffer_load_dwordx4 v9, s[4:7], s53 offen offset:1024 nt lds\n"
      "s_add_u32 s53, s54, 393216\n"
      "s_add_u32 m0, s37, 86536\n"
      "s_nop 0\n"
      "buffer_load_dwordx4 v8, s[4:7], s53 offen offset:0 nt lds\n"
      "buffer_load_dwordx4 v9, s[4:7], s53 offen offset:1024 nt lds\n"
      "s_add_u32 s53, s54, 589824\n"
      "s_add_u32 m0, s37, 90760\n"
      "s_nop 0\n"
      "buffer_load_dwordx4 v8, s[4:7], s53 offen offset:0 nt lds\n"
      "buffer_load_dwordx4 v9, s[4:7], s53 offen offset:1024 nt lds\n"
      "s_add_u32 s53, s54, 786432\n"
      "s_add_u32 m0, s37, 95104\n"
      "s_nop 0\n"
      "buffer_load_dwordx4 v8, s[4:7], s53 offen offset:0 nt lds\n"
      "buffer_load_dwordx4 v9, s[4:7], s53 offen offset:1024 nt lds\n"
      "s_add_u32 s53, s36, 0\n"
      "s_add_u32 m0, s37, 150272\n"
      "s_nop 0\n"
      "buffer_load_dwordx4 v8, s[4:7], s53 offen offset:0 nt lds\n"
      "buffer_load_dwordx4 v9, s[4:7], s53 offen offset:1024 nt lds\n"
      "v_lshlrev_b32 v43, 4, v37\n"
      "v_lshlrev_b32 v44, 2, v37\n"
      "v_and_b32 v43, 16, v43\n"
      "v_and_or_b32 v43, v44, 8, v43\n"
      "v_mul_u32_u24 v44, 192, v38\n"
      "v_mul_u32_u24 v45, 1536, v39\n"
      "s_lshl_b32 s27, s18, 5\n"
      "v_lshlrev_b32 v46, 1, v36\n"
      "v_add3_u32 v43, v43, v44, v45\n"
      "v_bitop3_b32 v46, v46, s27, 32 bitop3:0x6c\n"
      "v_add_u32 v60, v43, v46\n"
      "v_add_u32 v61, 57344, v60\n"
      "v_add_u32 v62, 114688, v60\n"
      "v_lshlrev_b32 v43, 4, v37\n"
      "s_mul_i32 s26, s17, 0x8080\n"
      "s_sub_u32 s26, 0xbf80, s26\n"
      "s_mov_b32 s27, 0\n"
      "s_lshl_b32 s34, s17, 7\n"
      "s_mov_b32 s29, 0\n"
      "s_sub_u32 s28, s26, s34\n"
      "s_mov_b32 s31, 0\n"
      "s_sub_u32 s30, s28, s34\n"
      "v_lshlrev_b64 v[44:45], v43, s[26:27]\n"
      "v_lshlrev_b64 v[46:47], v43, s[28:29]\n"
      "s_xor_b32 s28, s28, 0x8000\n"
      "v_lshlrev_b64 v[48:49], v43, s[30:31]\n"
      "v_lshlrev_b64 v[50:51], v43, s[28:29]\n"
      "v_accvgpr_write_b32 a244, v44\n"
      "v_accvgpr_write_b32 a245, v45\n"
      "v_accvgpr_write_b32 a246, v46\n"
      "v_accvgpr_write_b32 a247, v47\n"
      "v_accvgpr_write_b32 a248, v48\n"
      "v_accvgpr_write_b32 a249, v49\n"
      "v_accvgpr_write_b32 a250, v50\n"
      "v_accvgpr_write_b32 a251, v51\n"
      "s_lshl_b32 s26, s18, 3\n"
      "v_and_b32 v42, 15, v36\n"
      "v_xor_b32 v42, s26, v42\n"
      "s_lshl_b32 s26, s17, 13\n"
      "s_add_u32 s26, s26, 24832\n"
      "s_mul_i32 s27, s17, 12672\n"
      "v_mul_u32_u24 v44, 192, v42\n"
      "v_lshlrev_b32 v43, 3, v42\n"
      "v_and_b32 v45, 48, v36\n"
      "s_add_u32 s27, s27, 82432\n"
      "v_bitop3_b32 v46, v43, v45, 32 bitop3:0x6c\n"
      "v_add3_u32 v0, v46, v44, s26\n"
      "v_add3_u32 v3, v46, v44, s27\n"
      "v_add_u32 v43, 16, v43\n"
      "v_add_u32 v44, 384, v44\n"
      "v_bitop3_b32 v46, v43, v45, 32 bitop3:0x6c\n"
      "v_add3_u32 v1, v46, v44, s26\n"
      "v_add3_u32 v4, v46, v44, s27\n"
      "v_add_u32 v43, 16, v43\n"
      "v_add_u32 v44, 384, v44\n"
      "v_bitop3_b32 v46, v43, v45, 32 bitop3:0x6c\n"
      "v_add3_u32 v2, v46, v44, s26\n"
      "v_add3_u32 v5, v46, v44, s27\n"
      "v_lshlrev_b32 v43, 31, v36\n"
      "v_and_b32 v44, 2, v36\n"
      "v_or_b32 v43, 0x3f803f80, v43\n"
      "v_mul_u32_u24 v45, 0xc00080, v44\n"
      "v_lshl_add_u32 v44, v44, 22, v43\n"
      "v_add_u32 v45, v45, v43\n"
      "v_accvgpr_write_b32 a216, v44\n"
      "v_accvgpr_write_b32 a217, v45\n"
      "v_lshlrev_b32 v43, 3, v38\n"
      "s_mov_b32 s26, 806420736\n"
      "v_bfe_u32 v43, s26, v43, 8\n"
      "v_lshlrev_b32 v44, 12, v38\n"
      "s_lshl_b32 s28, s18, 8\n"
      "s_add_u32 s28, s28, 82432\n"
      "v_lshl_add_u32 v43, v43, 3, v44\n"
      "v_lshl_add_u32 v44, v39, 2, v37\n"
      "v_lshl_add_u32 v43, v44, 4, v43\n"
      "v_add_u32 v7, s28, v43\n"
      "s_mul_i32 s26, s17, 70144\n"
      "s_lshl_b32 s27, s18, 8\n"
      "s_sub_u32 s26, 70144, s26\n"
      "s_add_u32 s26, s26, s27\n"
      "v_lshl_add_u32 v6, v36, 2, s26\n"
      "v_lshrrev_b32 v43, 3, v36\n"
      "v_and_b32 v44, 7, v36\n"
      "v_mul_u32_u24 v45, 384, v43\n"
      "v_lshl_add_u32 v16, v44, 4, v45\n"
      "v_and_b32 v44, 2, v36\n"
      "v_lshlrev_b32 v43, 3, v43\n"
      "v_lshlrev_b32 v45, 4, v44\n"
      "v_xor_b32 v43, v43, v45\n"
      "v_lshl_add_u32 v43, v44, 7, v43\n"
      "v_and_b32 v44, 1, v36\n"
      "s_lshl_b32 s26, s18, 11\n"
      "v_lshl_add_u32 v43, v44, 6, v43\n"
      "s_add_u32 s26, s26, 12288\n"
      "v_bfe_u32 v44, v36, 2, 1\n"
      "v_add_u32 v43, s26, v43\n"
      "v_mul_i32_i24 v45, 8196, v44\n"
      "s_mov_b32 s27, 0\n"
      "v_add3_u32 v10, v43, v45, s27\n"
      "v_add_u32 v13, 128, v10\n"
      "v_mul_i32_i24 v45, -4092, v44\n"
      "s_mov_b32 s27, 4096\n"
      "v_add3_u32 v11, v43, v45, s27\n"
      "v_add_u32 v14, 128, v11\n"
      "v_mul_i32_i24 v45, -4100, v44\n"
      "s_mov_b32 s27, 8200\n"
      "v_add3_u32 v12, v43, v45, s27\n"
      "v_add_u32 v15, 128, v12\n"
      "s_mul_i32 s62, s18, 393216\n"
      "s_add_u32 s62, s62, s19\n"
      "s_add_u32 s62, s62, s21\n"
      "s_mov_b32 s40, 0x80000000\n"
      "s_and_b32 s9, s9, 0xffff\n"
      "s_mov_b32 s45, 0xbf2aaaab\n"
      "s_mov_b32 s46, 0xbe800000\n"
      "s_mov_b32 s47, 0x3f2aaaab\n"
      "s_mov_b32 s48, 0xc0c00000\n"
      "s_mov_b32 s49, 0x3f000000\n"
      "s_add_u32 m0, s63, 0\n"
      "s_add_u32 s52, s63, 6144\n"
      "buffer_load_dwordx4 v17, s[12:15], s52 offen offset:0 lds\n"
      "buffer_load_dwordx4 v18, s[12:15], s52 offen offset:1024 lds\n"
      "buffer_load_dwordx4 v19, s[12:15], s52 offen offset:2048 lds\n"
      "LDpro_%=:\n"
      "s_waitcnt vmcnt(13)\n"
      "s_barrier\n"
      "ds_read_b64_tr_b16 a[0:1], v60 offset:24832\n"
      "ds_read_b64_tr_b16 a[2:3], v60 offset:25600\n"
      "ds_read_b64_tr_b16 a[224:225], v62 offset:26112\n"
      "ds_read_b64_tr_b16 a[226:227], v62 offset:26880\n"
      "ds_read_b64_tr_b16 a[220:221], v60 offset:30976\n"
      "ds_read_b64_tr_b16 a[222:223], v60 offset:31744\n"
      "ds_read_b64_tr_b16 a[4:5], v60 offset:24896\n"
      "ds_read_b64_tr_b16 a[6:7], v60 offset:25664\n"
      "ds_read_b64_tr_b16 a[232:233], v62 offset:26176\n"
      "ds_read_b64_tr_b16 a[234:235], v62 offset:26944\n"
      "ds_read_b64_tr_b16 a[228:229], v60 offset:31040\n"
      "ds_read_b64_tr_b16 a[230:231], v60 offset:31808\n"
      "s_add_u32 m0, s63, 63744\n"
      "s_add_u32 s52, s63, 122880\n"
      "s_waitcnt lgkmcnt(10)\n"
      "v_mfma_f32_4x4x4_16b_bf16 v[28:31], a[244:245], a[0:1], 0\n"
      "v_mfma_f32_4x4x4_16b_bf16 v[32:35], a[244:245], a[2:3], 0\n"
      "buffer_load_dwordx4 v17, s[12:15], s52 offen offset:0 lds\n"
      "s_waitcnt lgkmcnt(8)\n"
      "v_mfma_f32_4x4x4_16b_bf16 v[28:31], a[248:249], a[224:225], v[28:31]\n"
      "ds_read_b64_tr_b16 a[8:9], v60 offset:24960\n"
      "v_mfma_f32_4x4x4_16b_bf16 v[32:35], a[248:249], a[226:227], v[32:35]\n"
      "buffer_load_dwordx4 v18, s[12:15], s52 offen offset:1024 lds\n"
      "s_waitcnt lgkmcnt(7)\n"
      "v_mfma_f32_4x4x4_16b_bf16 v[36:39], a[246:247], a[220:221], v[28:31]\n"
      "ds_read_b64_tr_b16 a[10:11], v60 offset:25728\n"
      "v_mfma_f32_4x4x4_16b_bf16 v[40:43], a[246:247], a[222:223], v[32:35]\n"
      "buffer_load_dwordx4 v19, s[12:15], s52 offen offset:2048 lds\n"
      "v_mfma_f32_4x4x4_16b_bf16 v[28:31], a[250:251], a[220:221], v[28:31]\n"
      "ds_read_b64_tr_b16 a[240:241], v62 offset:26240\n"
      "v_mfma_f32_4x4x4_16b_bf16 v[32:35], a[250:251], a[222:223], v[32:35]\n"
      "ds_read_b64_tr_b16 a[242:243], v62 offset:27008\n"
      "ds_read_b64_tr_b16 a[236:237], v60 offset:31104\n"
      "ds_read_b64_tr_b16 a[238:239], v60 offset:31872\n"
      "s_add_u32 m0, s63, 154368\n"
      "s_add_u32 s52, s63, 129024\n"
      "s_nop 0\n"
      "s_waitcnt lgkmcnt(5)\n"
      "v_mfma_f32_4x4x4_16b_bf16 v[44:47], a[244:245], a[4:5], 0\n"
      "v_mfma_f32_4x4x4_16b_bf16 v[48:51], a[244:245], a[6:7], 0\n"
      "buffer_load_dwordx4 v17, s[12:15], s52 offen offset:0 lds\n"
      "v_mfma_f32_4x4x4_16b_bf16 v[44:47], a[248:249], a[232:233], v[44:47]\n"
      "buffer_load_dwordx4 v18, s[12:15], s52 offen offset:1024 lds\n"
      "v_mfma_f32_4x4x4_16b_bf16 v[48:51], a[248:249], a[234:235], v[48:51]\n"
      "buffer_load_dwordx4 v19, s[12:15], s52 offen offset:2048 lds\n"
      "v_mfma_f32_4x4x4_16b_bf16 v[52:55], a[246:247], a[228:229], v[44:47]\n"
      "v_mfma_f32_4x4x4_16b_bf16 v[56:59], a[246:247], a[230:231], v[48:51]\n"
      "v_mfma_f32_4x4x4_16b_bf16 v[44:47], a[250:251], a[228:229], v[44:47]\n"
      "v_mfma_f32_4x4x4_16b_bf16 v[48:51], a[250:251], a[230:231], v[48:51]\n"
      "v_cvt_pk_bf16_f32 v252, v36, v40\n"
      "v_cvt_pk_bf16_f32 v253, v37, v41\n"
      "v_cvt_pk_bf16_f32 v254, v38, v42\n"
      "v_cvt_pk_bf16_f32 v255, v39, v43\n"
      "v_cvt_pk_bf16_f32 v248, v28, v32\n"
      "v_cvt_pk_bf16_f32 v249, v29, v33\n"
      "v_cvt_pk_bf16_f32 v250, v30, v34\n"
      "v_cvt_pk_bf16_f32 v251, v31, v35\n"
      "s_nop 0\n"
      "s_waitcnt lgkmcnt(0)\n"
      "v_mfma_f32_4x4x4_16b_bf16 v[28:31], a[244:245], a[8:9], 0\n"
      "v_mfma_f32_4x4x4_16b_bf16 v[32:35], a[244:245], a[10:11], 0\n"
      "s_nop 0\n"
      "v_mfma_f32_4x4x4_16b_bf16 v[28:31], a[248:249], a[240:241], v[28:31]\n"
      "v_mfma_f32_4x4x4_16b_bf16 v[32:35], a[248:249], a[242:243], v[32:35]\n"
      "s_nop 0\n"
      "v_mfma_f32_4x4x4_16b_bf16 v[36:39], a[246:247], a[236:237], v[28:31]\n"
      "v_mfma_f32_4x4x4_16b_bf16 v[40:43], a[246:247], a[238:239], v[32:35]\n"
      "v_mfma_f32_4x4x4_16b_bf16 v[28:31], a[250:251], a[236:237], v[28:31]\n"
      "v_mfma_f32_4x4x4_16b_bf16 v[32:35], a[250:251], a[238:239], v[32:35]\n"
      "v_cvt_pk_bf16_f32 v244, v52, v56\n"
      "v_cvt_pk_bf16_f32 v245, v53, v57\n"
      "v_cvt_pk_bf16_f32 v246, v54, v58\n"
      "v_cvt_pk_bf16_f32 v247, v55, v59\n"
      "v_cvt_pk_bf16_f32 v240, v44, v48\n"
      "v_cvt_pk_bf16_f32 v241, v45, v49\n"
      "v_cvt_pk_bf16_f32 v242, v46, v50\n"
      "v_cvt_pk_bf16_f32 v243, v47, v51\n"
      "s_waitcnt vmcnt(6)\n"
      "s_barrier\n"
      "ds_read_b64_tr_b16 a[12:13], v60 offset:0\n"
      "ds_read_b64_tr_b16 a[14:15], v60 offset:768\n"
      "ds_read_b64_tr_b16 a[224:225], v60 offset:12288\n"
      "ds_read_b64_tr_b16 a[226:227], v60 offset:13056\n"
      "ds_read_b64_tr_b16 a[220:221], v60 offset:6144\n"
      "ds_read_b64_tr_b16 a[222:223], v60 offset:6912\n"
      "s_add_u32 m0, s63, 128512\n"
      "s_add_u32 s52, s63, 135168\n"
      "s_waitcnt lgkmcnt(4)\n"
      "v_mfma_f32_4x4x4_16b_bf16 v[44:47], a[244:245], a[12:13], 0\n"
      "v_mfma_f32_4x4x4_16b_bf16 v[48:51], a[244:245], a[14:15], 0\n"
      "buffer_load_dwordx4 v17, s[12:15], s52 offen offset:0 lds\n"
      "s_waitcnt lgkmcnt(2)\n"
      "v_mfma_f32_4x4x4_16b_bf16 v[44:47], a[248:249], a[224:225], v[44:47]\n"
      "ds_read_b64_tr_b16 a[16:17], v60 offset:64\n"
      "v_mfma_f32_4x4x4_16b_bf16 v[48:51], a[248:249], a[226:227], v[48:51]\n"
      "buffer_load_dwordx4 v18, s[12:15], s52 offen offset:1024 lds\n"
      "s_nop 0\n"
      "s_waitcnt lgkmcnt(1)\n"
      "v_mfma_f32_4x4x4_16b_bf16 v[52:55], a[246:247], a[220:221], v[44:47]\n"
      "ds_read_b64_tr_b16 a[18:19], v60 offset:832\n"
      "v_mfma_f32_4x4x4_16b_bf16 v[56:59], a[246:247], a[222:223], v[48:51]\n"
      "buffer_load_dwordx4 v19, s[12:15], s52 offen offset:2048 lds\n"
      "v_mfma_f32_4x4x4_16b_bf16 v[44:47], a[250:251], a[220:221], v[44:47]\n"
      "ds_read_b64_tr_b16 a[232:233], v60 offset:12352\n"
      "v_mfma_f32_4x4x4_16b_bf16 v[48:51], a[250:251], a[222:223], v[48:51]\n"
      "ds_read_b64_tr_b16 a[234:235], v60 offset:13120\n"
      "ds_read_b64_tr_b16 a[228:229], v60 offset:6208\n"
      "ds_read_b64_tr_b16 a[230:231], v60 offset:6976\n"
      "ds_read_b64_tr_b16 a[20:21], v60 offset:128\n"
      "ds_read_b64_tr_b16 a[22:23], v60 offset:896\n"
      "ds_read_b64_tr_b16 a[240:241], v60 offset:12416\n"
      "ds_read_b64_tr_b16 a[242:243], v60 offset:13184\n"
      "ds_read_b64_tr_b16 a[236:237], v60 offset:6272\n"
      "ds_read_b64_tr_b16 a[238:239], v60 offset:7040\n"
      "ds_read_b64_tr_b16 a[168:169], v7 offset:2048\n"
      "ds_read_b64_tr_b16 a[170:171], v7 offset:2056\n"
      "ds_read_b128 a[204:207], v3 offset:55168\n"
      "v_cvt_pk_bf16_f32 v236, v36, v40\n"
      "v_cvt_pk_bf16_f32 v237, v37, v41\n"
      "v_cvt_pk_bf16_f32 v238, v38, v42\n"
      "v_cvt_pk_bf16_f32 v239, v39, v43\n"
      "v_cvt_pk_bf16_f32 v232, v28, v32\n"
      "v_cvt_pk_bf16_f32 v233, v29, v33\n"
      "v_cvt_pk_bf16_f32 v234, v30, v34\n"
      "v_cvt_pk_bf16_f32 v235, v31, v35\n"
      "s_waitcnt vmcnt(6)\n"
      "s_barrier\n"
      "s_nop 0\n"
      "s_waitcnt lgkmcnt(8)\n"
      "v_mfma_f32_4x4x4_16b_bf16 v[28:31], a[244:245], a[16:17], 0\n"
      "v_mfma_f32_4x4x4_16b_bf16 v[32:35], a[244:245], a[18:19], 0\n"
      "ds_read_b64_tr_b16 a[24:25], v60 offset:18432\n"
      "v_mfma_f32_4x4x4_16b_bf16 v[28:31], a[248:249], a[232:233], v[28:31]\n"
      "ds_read_b64_tr_b16 a[26:27], v60 offset:19200\n"
      "v_mfma_f32_4x4x4_16b_bf16 v[32:35], a[248:249], a[234:235], v[32:35]\n"
      "ds_read_b64_tr_b16 a[224:225], v61 offset:6400\n"
      "v_mfma_f32_4x4x4_16b_bf16 v[36:39], a[246:247], a[228:229], v[28:31]\n"
      "ds_read_b64_tr_b16 a[226:227], v61 offset:7168\n"
      "v_mfma_f32_4x4x4_16b_bf16 v[40:43], a[246:247], a[230:231], v[32:35]\n"
      "ds_read_b64_tr_b16 a[220:221], v61 offset:256\n"
      "v_mfma_f32_4x4x4_16b_bf16 v[28:31], a[250:251], a[228:229], v[28:31]\n"
      "ds_read_b64_tr_b16 a[222:223], v61 offset:1024\n"
      "v_mfma_f32_4x4x4_16b_bf16 v[32:35], a[250:251], a[230:231], v[32:35]\n"
      "ds_read_b64_tr_b16 a[172:173], v7 offset:2560\n"
      "s_nop 0\n"
      "s_waitcnt lgkmcnt(7)\n"
      "v_mfma_f32_4x4x4_16b_bf16 v[20:23], a[216:217], a[168:169], 0\n"
      "ds_read_b64_tr_b16 a[174:175], v7 offset:2568\n"
      "v_mfma_f32_4x4x4_16b_bf16 v[24:27], a[216:217], a[170:171], 0\n"
      "ds_read_b128 a[208:211], v3 offset:55232\n"
      "v_cvt_pk_bf16_f32 v228, v52, v56\n"
      "v_cvt_pk_bf16_f32 v229, v53, v57\n"
      "v_cvt_pk_bf16_f32 v230, v54, v58\n"
      "v_cvt_pk_bf16_f32 v231, v55, v59\n"
      "v_cvt_pk_bf16_f32 v224, v44, v48\n"
      "v_cvt_pk_bf16_f32 v225, v45, v49\n"
      "v_cvt_pk_bf16_f32 v226, v46, v50\n"
      "v_cvt_pk_bf16_f32 v227, v47, v51\n"
      "s_add_u32 m0, s63, 134656\n"
      "s_add_u32 s52, s63, 141312\n"
      "v_mfma_f32_4x4x4_16b_bf16 v[44:47], a[244:245], a[20:21], 0\n"
      "v_mfma_f32_4x4x4_16b_bf16 v[48:51], a[244:245], a[22:23], 0\n"
      "buffer_load_dwordx4 v17, s[12:15], s52 offen offset:0 lds\n"
      "v_mfma_f32_4x4x4_16b_bf16 v[44:47], a[248:249], a[240:241], v[44:47]\n"
      "ds_read_b64_tr_b16 a[28:29], v60 offset:18496\n"
      "v_mfma_f32_4x4x4_16b_bf16 v[48:51], a[248:249], a[242:243], v[48:51]\n"
      "buffer_load_dwordx4 v18, s[12:15], s52 offen offset:1024 lds\n"
      "v_mfma_f32_4x4x4_16b_bf16 v[52:55], a[246:247], a[236:237], v[44:47]\n"
      "ds_read_b64_tr_b16 a[30:31], v60 offset:19264\n"
      "v_mfma_f32_4x4x4_16b_bf16 v[56:59], a[246:247], a[238:239], v[48:51]\n"
      "buffer_load_dwordx4 v19, s[12:15], s52 offen offset:2048 lds\n"
      "v_mfma_f32_4x4x4_16b_bf16 v[44:47], a[250:251], a[236:237], v[44:47]\n"
      "ds_read_b64_tr_b16 a[232:233], v61 offset:6464\n"
      "v_mfma_f32_4x4x4_16b_bf16 v[48:51], a[250:251], a[238:239], v[48:51]\n"
      "s_add_u32 s53, s54, 983040\n"
      "s_nop 0\n"
      "s_waitcnt lgkmcnt(3)\n"
      "v_mfma_f32_4x4x4_16b_bf16 v[100:103], a[216:217], a[172:173], 0\n"
      "ds_read_b64_tr_b16 a[234:235], v61 offset:7232\n"
      "v_mfma_f32_4x4x4_16b_bf16 v[104:107], a[216:217], a[174:175], 0\n"
      "s_add_u32 m0, s37, 99328\n"
      "ds_read_b64_tr_b16 a[228:229], v61 offset:320\n"
      "buffer_load_dwordx4 v8, s[4:7], s53 offen offset:0 nt lds\n"
      "buffer_load_dwordx4 v9, s[4:7], s53 offen offset:1024 nt lds\n"
      "ds_read_b64_tr_b16 a[230:231], v61 offset:1088\n"
      "s_add_u32 s53, s54, 1179648\n"
      "ds_read_b64_tr_b16 a[176:177], v7 offset:3072\n"
      "s_add_u32 m0, s37, 103432\n"
      "ds_read_b64_tr_b16 a[178:179], v7 offset:3080\n"
      "buffer_load_dwordx4 v8, s[4:7], s53 offen offset:0 nt lds\n"
      "buffer_load_dwordx4 v9, s[4:7], s53 offen offset:1024 nt lds\n"
      "v_cvt_pk_bf16_f32 v220, v36, v40\n"
      "v_cvt_pk_bf16_f32 v221, v37, v41\n"
      "v_cvt_pk_bf16_f32 v222, v38, v42\n"
      "v_cvt_pk_bf16_f32 v223, v39, v43\n"
      "v_cvt_pk_bf16_f32 v216, v28, v32\n"
      "v_cvt_pk_bf16_f32 v217, v29, v33\n"
      "v_mfma_f32_16x16x32_bf16 v[64:67], a[0:3], a[204:207], 0\n"
      "v_cvt_pk_bf16_f32 v218, v30, v34\n"
      "v_cvt_pk_bf16_f32 v219, v31, v35\n"
      "v_mfma_f32_16x16x32_bf16 v[68:71], a[4:7], a[204:207], 0\n"
      "s_add_u32 m0, s37, 107656\n"
      "v_cvt_pk_bf16_f32 v20, v20, v24\n"
      "v_cvt_pk_bf16_f32 v21, v21, v25\n"
      "v_mfma_f32_16x16x32_bf16 v[72:75], a[8:11], a[204:207], 0\n"
      "s_add_u32 s53, s54, 1376256\n"
      "v_cvt_pk_bf16_f32 v22, v22, v26\n"
      "v_cvt_pk_bf16_f32 v23, v23, v27\n"
      "v_mfma_f32_4x4x4_16b_bf16 v[28:31], a[244:245], a[24:25], 0\n"
      "v_mfma_f32_4x4x4_16b_bf16 v[32:35], a[244:245], a[26:27], 0\n"
      "buffer_load_dwordx4 v8, s[4:7], s53 offen offset:0 nt lds\n"
      "buffer_load_dwordx4 v9, s[4:7], s53 offen offset:1024 nt lds\n"
      "v_mfma_f32_4x4x4_16b_bf16 v[28:31], a[248:249], a[224:225], v[28:31]\n"
      "ds_read_b64_tr_b16 a[32:33], v60 offset:18560\n"
      "v_mfma_f32_4x4x4_16b_bf16 v[32:35], a[248:249], a[226:227], v[32:35]\n"
      "s_add_u32 s53, s54, 1572864\n"
      "v_mfma_f32_4x4x4_16b_bf16 v[36:39], a[246:247], a[220:221], v[28:31]\n"
      "ds_read_b64_tr_b16 a[34:35], v60 offset:19328\n"
      "v_mfma_f32_4x4x4_16b_bf16 v[40:43], a[246:247], a[222:223], v[32:35]\n"
      "s_add_u32 m0, s37, 112000\n"
      "v_mfma_f32_4x4x4_16b_bf16 v[28:31], a[250:251], a[220:221], v[28:31]\n"
      "ds_read_b64_tr_b16 a[240:241], v61 offset:6528\n"
      "v_mfma_f32_4x4x4_16b_bf16 v[32:35], a[250:251], a[222:223], v[32:35]\n"
      "buffer_load_dwordx4 v8, s[4:7], s53 offen offset:0 nt lds\n"
      "buffer_load_dwordx4 v9, s[4:7], s53 offen offset:1024 nt lds\n"
      "s_nop 0\n"
      "s_waitcnt lgkmcnt(3)\n"
      "v_mfma_f32_4x4x4_16b_bf16 v[108:111], a[216:217], a[176:177], 0\n"
      "ds_read_b64_tr_b16 a[242:243], v61 offset:7296\n"
      "v_mfma_f32_4x4x4_16b_bf16 v[112:115], a[216:217], a[178:179], 0\n"
      "ds_read_b64_tr_b16 a[236:237], v61 offset:384\n"
      "ds_read_b64_tr_b16 a[238:239], v61 offset:1152\n"
      "ds_read_b64_tr_b16 a[180:181], v7 offset:3584\n"
      "ds_read_b64_tr_b16 a[182:183], v7 offset:3592\n"
      "s_mov_b32 m0, s64\n"
      "s_nop 0\n"
      "ds_write_addtid_b32 v20 offset:2048\n"
      "ds_write_addtid_b32 v21 offset:6144\n"
      "ds_write_addtid_b32 v22 offset:10240\n"
      "ds_write_addtid_b32 v23 offset:14336\n"
      "v_cvt_pk_bf16_f32 v212, v52, v56\n"
      "v_cvt_pk_bf16_f32 v213, v53, v57\n"
      "v_cvt_pk_bf16_f32 v214, v54, v58\n"
      "v_cvt_pk_bf16_f32 v215, v55, v59\n"
      "v_cvt_pk_bf16_f32 v208, v44, v48\n"
      "v_cvt_pk_bf16_f32 v209, v45, v49\n"
      "v_mfma_f32_16x16x32_bf16 v[64:67], a[12:15], a[208:211], v[64:67]\n"
      "v_cvt_pk_bf16_f32 v210, v46, v50\n"
      "v_cvt_pk_bf16_f32 v211, v47, v51\n"
      "v_mfma_f32_16x16x32_bf16 v[68:71], a[16:19], a[208:211], v[68:71]\n"
      "v_cvt_pk_bf16_f32 v100, v100, v104\n"
      "v_cvt_pk_bf16_f32 v101, v101, v105\n"
      "v_mfma_f32_16x16x32_bf16 v[72:75], a[20:23], a[208:211], v[72:75]\n"
      "s_waitcnt vmcnt(14)\n"
      "v_cvt_pk_bf16_f32 v102, v102, v106\n"
      "v_cvt_pk_bf16_f32 v103, v103, v107\n"
      "s_barrier\n"
      "v_mfma_f32_4x4x4_16b_bf16 v[44:47], a[244:245], a[28:29], 0\n"
      "v_mfma_f32_4x4x4_16b_bf16 v[48:51], a[244:245], a[30:31], 0\n"
      "ds_read_b64_tr_b16 a[36:37], v61 offset:12544\n"
      "v_mfma_f32_4x4x4_16b_bf16 v[44:47], a[248:249], a[232:233], v[44:47]\n"
      "ds_read_b64_tr_b16 a[38:39], v61 offset:13312\n"
      "v_mfma_f32_4x4x4_16b_bf16 v[48:51], a[248:249], a[234:235], v[48:51]\n"
      "ds_read_b64_tr_b16 a[224:225], v62 offset:39680\n"
      "v_mfma_f32_4x4x4_16b_bf16 v[52:55], a[246:247], a[228:229], v[44:47]\n"
      "ds_read_b64_tr_b16 a[226:227], v62 offset:40448\n"
      "v_mfma_f32_4x4x4_16b_bf16 v[56:59], a[246:247], a[230:231], v[48:51]\n"
      "ds_read_b64_tr_b16 a[220:221], v61 offset:18688\n"
      "v_mfma_f32_4x4x4_16b_bf16 v[44:47], a[250:251], a[228:229], v[44:47]\n"
      "ds_read_b64_tr_b16 a[222:223], v61 offset:19456\n"
      "v_mfma_f32_4x4x4_16b_bf16 v[48:51], a[250:251], a[230:231], v[48:51]\n"
      "ds_write_addtid_b32 v100 offset:2560\n"
      "s_nop 0\n"
      "s_waitcnt lgkmcnt(7)\n"
      "v_mfma_f32_4x4x4_16b_bf16 v[116:119], a[216:217], a[180:181], 0\n"
      "ds_write_addtid_b32 v101 offset:6656\n"
      "v_mfma_f32_4x4x4_16b_bf16 v[120:123], a[216:217], a[182:183], 0\n"
      "ds_write_addtid_b32 v102 offset:10752\n"
      "ds_write_addtid_b32 v103 offset:14848\n"
      "ds_read_b128 a[212:215], v3 offset:55296\n"
      "v_cvt_pk_bf16_f32 v204, v36, v40\n"
      "v_cvt_pk_bf16_f32 v205, v37, v41\n"
      "v_cvt_pk_bf16_f32 v206, v38, v42\n"
      "v_cvt_pk_bf16_f32 v207, v39, v43\n"
      "v_cvt_pk_bf16_f32 v200, v28, v32\n"
      "v_cvt_pk_bf16_f32 v201, v29, v33\n"
      "v_cvt_pk_bf16_f32 v202, v30, v34\n"
      "v_cvt_pk_bf16_f32 v203, v31, v35\n"
      "v_cvt_pk_bf16_f32 v108, v108, v112\n"
      "v_cvt_pk_bf16_f32 v109, v109, v113\n"
      "v_cvt_pk_bf16_f32 v110, v110, v114\n"
      "v_cvt_pk_bf16_f32 v111, v111, v115\n"
      "v_mfma_f32_4x4x4_16b_bf16 v[28:31], a[244:245], a[32:33], 0\n"
      "v_mfma_f32_4x4x4_16b_bf16 v[32:35], a[244:245], a[34:35], 0\n"
      "ds_read_b64_tr_b16 a[40:41], v61 offset:12608\n"
      "v_mfma_f32_4x4x4_16b_bf16 v[28:31], a[248:249], a[240:241], v[28:31]\n"
      "ds_read_b64_tr_b16 a[42:43], v61 offset:13376\n"
      "v_mfma_f32_4x4x4_16b_bf16 v[32:35], a[248:249], a[242:243], v[32:35]\n"
      "ds_read_b64_tr_b16 a[232:233], v62 offset:39744\n"
      "v_mfma_f32_4x4x4_16b_bf16 v[36:39], a[246:247], a[236:237], v[28:31]\n"
      "ds_read_b64_tr_b16 a[234:235], v62 offset:40512\n"
      "v_mfma_f32_4x4x4_16b_bf16 v[40:43], a[246:247], a[238:239], v[32:35]\n"
      "ds_read_b64_tr_b16 a[228:229], v61 offset:18752\n"
      "v_mfma_f32_4x4x4_16b_bf16 v[28:31], a[250:251], a[236:237], v[28:31]\n"
      "ds_read_b64_tr_b16 a[230:231], v61 offset:19520\n"
      "v_mfma_f32_4x4x4_16b_bf16 v[32:35], a[250:251], a[238:239], v[32:35]\n"
      "ds_write_addtid_b32 v108 offset:3072\n"
      "ds_write_addtid_b32 v109 offset:7168\n"
      "ds_write_addtid_b32 v110 offset:11264\n"
      "ds_write_addtid_b32 v111 offset:15360\n"
      "v_cvt_pk_bf16_f32 v196, v52, v56\n"
      "v_cvt_pk_bf16_f32 v197, v53, v57\n"
      "v_cvt_pk_bf16_f32 v198, v54, v58\n"
      "v_cvt_pk_bf16_f32 v199, v55, v59\n"
      "v_cvt_pk_bf16_f32 v192, v44, v48\n"
      "v_cvt_pk_bf16_f32 v193, v45, v49\n"
      "v_cvt_pk_bf16_f32 v194, v46, v50\n"
      "v_cvt_pk_bf16_f32 v195, v47, v51\n"
      "v_cvt_pk_bf16_f32 v116, v116, v120\n"
      "v_cvt_pk_bf16_f32 v117, v117, v121\n"
      "v_cvt_pk_bf16_f32 v118, v118, v122\n"
      "v_cvt_pk_bf16_f32 v119, v119, v123\n"
      "s_add_u32 m0, s63, 12288\n"
      "s_add_u32 s52, s63, 147456\n"
      "s_nop 0\n"
      "s_waitcnt lgkmcnt(3)\n"
      "v_mfma_f32_4x4x4_16b_bf16 v[44:47], a[244:245], a[36:37], 0\n"
      "v_mfma_f32_4x4x4_16b_bf16 v[48:51], a[244:245], a[38:39], 0\n"
      "buffer_load_dwordx4 v17, s[12:15], s52 offen offset:0 lds\n"
      "v_mfma_f32_4x4x4_16b_bf16 v[44:47], a[248:249], a[224:225], v[44:47]\n"
      "ds_read_b64_tr_b16 a[44:45], v61 offset:12672\n"
      "v_mfma_f32_4x4x4_16b_bf16 v[48:51], a[248:249], a[226:227], v[48:51]\n"
      "buffer_load_dwordx4 v18, s[12:15], s52 offen offset:1024 lds\n"
      "v_mfma_f32_4x4x4_16b_bf16 v[52:55], a[246:247], a[220:221], v[44:47]\n"
      "ds_read_b64_tr_b16 a[46:47], v61 offset:13440\n"
      "v_mfma_f32_4x4x4_16b_bf16 v[56:59], a[246:247], a[222:223], v[48:51]\n"
      "buffer_load_dwordx4 v19, s[12:15], s52 offen offset:2048 lds\n"
      "v_mfma_f32_4x4x4_16b_bf16 v[44:47], a[250:251], a[220:221], v[44:47]\n"
      "ds_read_b64_tr_b16 a[240:241], v62 offset:39808\n"
      "v_mfma_f32_4x4x4_16b_bf16 v[48:51], a[250:251], a[222:223], v[48:51]\n"
      "ds_read_b64_tr_b16 a[242:243], v62 offset:40576\n"
      "ds_read_b64_tr_b16 a[236:237], v61 offset:18816\n"
      "ds_read_b64_tr_b16 a[238:239], v61 offset:19584\n"
      "s_mov_b32 m0, s64\n"
      "s_nop 0\n"
      "ds_write_addtid_b32 v116 offset:3584\n"
      "ds_write_addtid_b32 v117 offset:7680\n"
      "ds_write_addtid_b32 v118 offset:11776\n"
      "ds_write_addtid_b32 v119 offset:15872\n"
      "v_cvt_pk_bf16_f32 v188, v36, v40\n"
      "v_cvt_pk_bf16_f32 v189, v37, v41\n"
      "v_mfma_f32_16x16x32_bf16 v[64:67], a[24:27], a[212:215], v[64:67]\n"
      "v_cvt_pk_bf16_f32 v190, v38, v42\n"
      "v_cvt_pk_bf16_f32 v191, v39, v43\n"
      "v_mfma_f32_16x16x32_bf16 v[68:71], a[28:31], a[212:215], v[68:71]\n"
      "s_waitcnt vmcnt(14)\n"
      "v_cvt_pk_bf16_f32 v184, v28, v32\n"
      "v_cvt_pk_bf16_f32 v185, v29, v33\n"
      "v_mfma_f32_16x16x32_bf16 v[72:75], a[32:35], a[212:215], v[72:75]\n"
      "s_waitcnt lgkmcnt(0)\n"
      "v_cvt_pk_bf16_f32 v186, v30, v34\n"
      "v_cvt_pk_bf16_f32 v187, v31, v35\n"
      "s_nop 0\n"
      "s_barrier\n"
      "v_mfma_f32_4x4x4_16b_bf16 v[28:31], a[244:245], a[40:41], 0\n"
      "v_mfma_f32_4x4x4_16b_bf16 v[32:35], a[244:245], a[42:43], 0\n"
      "ds_read_b64_tr_b16 a[48:49], v62 offset:1536\n"
      "v_mfma_f32_4x4x4_16b_bf16 v[28:31], a[248:249], a[232:233], v[28:31]\n"
      "ds_read_b64_tr_b16 a[50:51], v62 offset:2304\n"
      "v_mfma_f32_4x4x4_16b_bf16 v[32:35], a[248:249], a[234:235], v[32:35]\n"
      "ds_read_b64_tr_b16 a[224:225], v62 offset:13824\n"
      "v_mfma_f32_4x4x4_16b_bf16 v[36:39], a[246:247], a[228:229], v[28:31]\n"
      "ds_read_b64_tr_b16 a[226:227], v62 offset:14592\n"
      "v_mfma_f32_4x4x4_16b_bf16 v[40:43], a[246:247], a[230:231], v[32:35]\n"
      "ds_read_b64_tr_b16 a[220:221], v62 offset:7680\n"
      "v_mfma_f32_4x4x4_16b_bf16 v[28:31], a[250:251], a[228:229], v[28:31]\n"
      "ds_read_b64_tr_b16 a[222:223], v62 offset:8448\n"
      "v_mfma_f32_4x4x4_16b_bf16 v[32:35], a[250:251], a[230:231], v[32:35]\n"
      "ds_read_b128 a[192:195], v4 offset:55168\n"
      "ds_read_b128 a[196:199], v0 offset:0\n"
      "ds_read_b128 a[200:203], v0 offset:4096\n"
      "v_cvt_pk_bf16_f32 v180, v52, v56\n"
      "v_cvt_pk_bf16_f32 v181, v53, v57\n"
      "v_cvt_pk_bf16_f32 v182, v54, v58\n"
      "v_cvt_pk_bf16_f32 v183, v55, v59\n"
      "v_cvt_pk_bf16_f32 v176, v44, v48\n"
      "v_cvt_pk_bf16_f32 v177, v45, v49\n"
      "v_cvt_pk_bf16_f32 v178, v46, v50\n"
      "v_cvt_pk_bf16_f32 v179, v47, v51\n"
      "v_mfma_f32_4x4x4_16b_bf16 v[44:47], a[244:245], a[44:45], 0\n"
      "v_mfma_f32_4x4x4_16b_bf16 v[48:51], a[244:245], a[46:47], 0\n"
      "ds_read_b64_tr_b16 a[52:53], v62 offset:1600\n"
      "v_mfma_f32_4x4x4_16b_bf16 v[44:47], a[248:249], a[240:241], v[44:47]\n"
      "ds_read_b64_tr_b16 a[54:55], v62 offset:2368\n"
      "v_mfma_f32_4x4x4_16b_bf16 v[48:51], a[248:249], a[242:243], v[48:51]\n"
      "ds_read_b64_tr_b16 a[232:233], v62 offset:13888\n"
      "v_mfma_f32_4x4x4_16b_bf16 v[52:55], a[246:247], a[236:237], v[44:47]\n"
      "ds_read_b64_tr_b16 a[234:235], v62 offset:14656\n"
      "v_mfma_f32_4x4x4_16b_bf16 v[56:59], a[246:247], a[238:239], v[48:51]\n"
      "ds_read_b64_tr_b16 a[228:229], v62 offset:7744\n"
      "v_mfma_f32_4x4x4_16b_bf16 v[44:47], a[250:251], a[236:237], v[44:47]\n"
      "ds_read_b64_tr_b16 a[230:231], v62 offset:8512\n"
      "v_mfma_f32_4x4x4_16b_bf16 v[48:51], a[250:251], a[238:239], v[48:51]\n"
      "ds_read_b128 a[204:207], v0 offset:64\n"
      "ds_read_b128 a[208:211], v0 offset:4160\n"
      "ds_read_b128 a[212:215], v0 offset:128\n"
      "v_cvt_pk_bf16_f32 v172, v36, v40\n"
      "v_cvt_pk_bf16_f32 v173, v37, v41\n"
      "v_cvt_pk_bf16_f32 v174, v38, v42\n"
      "v_cvt_pk_bf16_f32 v175, v39, v43\n"
      "v_cvt_pk_bf16_f32 v168, v28, v32\n"
      "v_cvt_pk_bf16_f32 v169, v29, v33\n"
      "v_cvt_pk_bf16_f32 v170, v30, v34\n"
      "v_cvt_pk_bf16_f32 v171, v31, v35\n"
      "s_add_u32 m0, s63, 63744\n"
      "s_add_u32 s52, s63, 153600\n"
      "s_nop 0\n"
      "s_waitcnt lgkmcnt(4)\n"
      "v_mfma_f32_4x4x4_16b_bf16 v[28:31], a[244:245], a[48:49], 0\n"
      "v_mfma_f32_4x4x4_16b_bf16 v[32:35], a[244:245], a[50:51], 0\n"
      "buffer_load_dwordx4 v17, s[12:15], s52 offen offset:0 lds\n"
      "v_mfma_f32_4x4x4_16b_bf16 v[28:31], a[248:249], a[224:225], v[28:31]\n"
      "ds_read_b64_tr_b16 a[56:57], v62 offset:1664\n"
      "v_mfma_f32_4x4x4_16b_bf16 v[32:35], a[248:249], a[226:227], v[32:35]\n"
      "buffer_load_dwordx4 v18, s[12:15], s52 offen offset:1024 lds\n"
      "v_mfma_f32_4x4x4_16b_bf16 v[36:39], a[246:247], a[220:221], v[28:31]\n"
      "ds_read_b64_tr_b16 a[58:59], v62 offset:2432\n"
      "v_mfma_f32_4x4x4_16b_bf16 v[40:43], a[246:247], a[222:223], v[32:35]\n"
      "buffer_load_dwordx4 v19, s[12:15], s52 offen offset:2048 lds\n"
      "v_mfma_f32_4x4x4_16b_bf16 v[28:31], a[250:251], a[220:221], v[28:31]\n"
      "ds_read_b64_tr_b16 a[240:241], v62 offset:13952\n"
      "v_mfma_f32_4x4x4_16b_bf16 v[32:35], a[250:251], a[222:223], v[32:35]\n"
      "ds_read_b64_tr_b16 a[242:243], v62 offset:14720\n"
      "ds_read_b64_tr_b16 a[236:237], v62 offset:7808\n"
      "ds_read_b64_tr_b16 a[238:239], v62 offset:8576\n"
      "v_mfma_f32_16x16x32_bf16 v[64:67], a[36:39], a[192:195], v[64:67]\n"
      "v_cvt_pk_bf16_f32 v164, v52, v56\n"
      "v_cvt_pk_bf16_f32 v165, v53, v57\n"
      "v_mfma_f32_16x16x32_bf16 v[68:71], a[40:43], a[192:195], v[68:71]\n"
      "v_cvt_pk_bf16_f32 v166, v54, v58\n"
      "v_cvt_pk_bf16_f32 v167, v55, v59\n"
      "v_mfma_f32_16x16x32_bf16 v[72:75], a[44:47], a[192:195], v[72:75]\n"
      "v_cvt_pk_bf16_f32 v160, v44, v48\n"
      "v_cvt_pk_bf16_f32 v161, v45, v49\n"
      "v_mfma_f32_16x16x32_bf16 v[76:79], v[252:255], a[196:199], 0\n"
      "s_waitcnt vmcnt(14)\n"
      "v_cvt_pk_bf16_f32 v162, v46, v50\n"
      "v_cvt_pk_bf16_f32 v163, v47, v51\n"
      "s_barrier\n"
      "s_add_u32 m0, s63, 154368\n"
      "s_add_u32 s52, s63, 159744\n"
      "v_mfma_f32_4x4x4_16b_bf16 v[44:47], a[244:245], a[52:53], 0\n"
      "v_mfma_f32_4x4x4_16b_bf16 v[48:51], a[244:245], a[54:55], 0\n"
      "buffer_load_dwordx4 v17, s[12:15], s52 offen offset:0 lds\n"
      "v_mfma_f32_4x4x4_16b_bf16 v[44:47], a[248:249], a[232:233], v[44:47]\n"
      "ds_read_b64_tr_b16 a[60:61], v60 offset:41216\n"
      "v_mfma_f32_4x4x4_16b_bf16 v[48:51], a[248:249], a[234:235], v[48:51]\n"
      "buffer_load_dwordx4 v18, s[12:15], s52 offen offset:1024 lds\n"
      "s_nop 0\n"
      "s_waitcnt lgkmcnt(1)\n"
      "v_mfma_f32_4x4x4_16b_bf16 v[52:55], a[246:247], a[228:229], v[44:47]\n"
      "ds_read_b64_tr_b16 a[62:63], v60 offset:41984\n"
      "v_mfma_f32_4x4x4_16b_bf16 v[56:59], a[246:247], a[230:231], v[48:51]\n"
      "buffer_load_dwordx4 v19, s[12:15], s52 offen offset:2048 lds\n"
      "v_mfma_f32_4x4x4_16b_bf16 v[44:47], a[250:251], a[228:229], v[44:47]\n"
      "ds_read_b64_tr_b16 a[224:225], v62 offset:19968\n"
      "v_mfma_f32_4x4x4_16b_bf16 v[48:51], a[250:251], a[230:231], v[48:51]\n"
      "ds_read_b64_tr_b16 a[226:227], v62 offset:20736\n"
      "ds_read_b64_tr_b16 a[220:221], v60 offset:47360\n"
      "ds_read_b64_tr_b16 a[222:223], v60 offset:48128\n"
      "ds_read_b128 a[192:195], v0 offset:4224\n"
      "v_mfma_f32_16x16x32_bf16 v[80:83], v[244:247], a[196:199], 0\n"
      "v_cvt_pk_bf16_f32 v156, v36, v40\n"
      "v_cvt_pk_bf16_f32 v157, v37, v41\n"
      "v_mfma_f32_16x16x32_bf16 v[84:87], v[236:239], a[196:199], 0\n"
      "v_cvt_pk_bf16_f32 v158, v38, v42\n"
      "v_cvt_pk_bf16_f32 v159, v39, v43\n"
      "v_mfma_f32_16x16x32_bf16 v[88:91], v[248:251], a[200:203], 0\n"
      "v_cvt_pk_bf16_f32 v152, v28, v32\n"
      "v_cvt_pk_bf16_f32 v153, v29, v33\n"
      "v_mfma_f32_16x16x32_bf16 v[92:95], v[240:243], a[200:203], 0\n"
      "v_cvt_pk_bf16_f32 v154, v30, v34\n"
      "v_cvt_pk_bf16_f32 v155, v31, v35\n"
      "v_mfma_f32_4x4x4_16b_bf16 v[28:31], a[244:245], a[56:57], 0\n"
      "v_mfma_f32_4x4x4_16b_bf16 v[32:35], a[244:245], a[58:59], 0\n"
      "ds_read_b64_tr_b16 a[64:65], v60 offset:41280\n"
      "v_mfma_f32_4x4x4_16b_bf16 v[28:31], a[248:249], a[240:241], v[28:31]\n"
      "ds_read_b64_tr_b16 a[66:67], v60 offset:42048\n"
      "v_mfma_f32_4x4x4_16b_bf16 v[32:35], a[248:249], a[242:243], v[32:35]\n"
      "ds_read_b64_tr_b16 a[232:233], v62 offset:20032\n"
      "v_mfma_f32_4x4x4_16b_bf16 v[36:39], a[246:247], a[236:237], v[28:31]\n"
      "ds_read_b64_tr_b16 a[234:235], v62 offset:20800\n"
      "v_mfma_f32_4x4x4_16b_bf16 v[40:43], a[246:247], a[238:239], v[32:35]\n"
      "ds_read_b64_tr_b16 a[228:229], v60 offset:47424\n"
      "v_mfma_f32_4x4x4_16b_bf16 v[28:31], a[250:251], a[236:237], v[28:31]\n"
      "ds_read_b64_tr_b16 a[230:231], v60 offset:48192\n"
      "v_mfma_f32_4x4x4_16b_bf16 v[32:35], a[250:251], a[238:239], v[32:35]\n"
      "ds_read_b128 a[196:199], v1 offset:0\n"
      "v_mfma_f32_16x16x32_bf16 v[96:99], v[232:235], a[200:203], 0\n"
      "v_cvt_pk_bf16_f32 v148, v52, v56\n"
      "v_cvt_pk_bf16_f32 v149, v53, v57\n"
      "v_mfma_f32_16x16x32_bf16 v[76:79], v[228:231], a[204:207], v[76:79]\n"
      "v_cvt_pk_bf16_f32 v150, v54, v58\n"
      "v_cvt_pk_bf16_f32 v151, v55, v59\n"
      "v_mfma_f32_16x16x32_bf16 v[80:83], v[220:223], a[204:207], v[80:83]\n"
      "s_nop 0\n"
      "v_cvt_pk_bf16_f32 v144, v44, v48\n"
      "v_cvt_pk_bf16_f32 v145, v45, v49\n"
      "v_mfma_f32_16x16x32_bf16 v[84:87], v[212:215], a[204:207], v[84:87]\n"
      "s_waitcnt lgkmcnt(1)\n"
      "v_cvt_pk_bf16_f32 v146, v46, v50\n"
      "v_cvt_pk_bf16_f32 v147, v47, v51\n"
      "v_mfma_f32_4x4x4_16b_bf16 v[44:47], a[244:245], a[60:61], 0\n"
      "v_mfma_f32_4x4x4_16b_bf16 v[48:51], a[244:245], a[62:63], 0\n"
      "ds_read_b64_tr_b16 a[68:69], v60 offset:41344\n"
      "v_mfma_f32_4x4x4_16b_bf16 v[44:47], a[248:249], a[224:225], v[44:47]\n"
      "ds_read_b64_tr_b16 a[70:71], v60 offset:42112\n"
      "v_mfma_f32_4x4x4_16b_bf16 v[48:51], a[248:249], a[226:227], v[48:51]\n"
      "ds_read_b64_tr_b16 a[240:241], v62 offset:20096\n"
      "v_mfma_f32_4x4x4_16b_bf16 v[52:55], a[246:247], a[220:221], v[44:47]\n"
      "ds_read_b64_tr_b16 a[242:243], v62 offset:20864\n"
      "v_mfma_f32_4x4x4_16b_bf16 v[56:59], a[246:247], a[222:223], v[48:51]\n"
      "ds_read_b64_tr_b16 a[236:237], v60 offset:47488\n"
      "v_mfma_f32_4x4x4_16b_bf16 v[44:47], a[250:251], a[220:221], v[44:47]\n"
      "ds_read_b64_tr_b16 a[238:239], v60 offset:48256\n"
      "v_mfma_f32_4x4x4_16b_bf16 v[48:51], a[250:251], a[222:223], v[48:51]\n"
      "ds_read_b128 a[200:203], v1 offset:4096\n"
      "ds_read_b128 a[204:207], v4 offset:55232\n"
      "v_mfma_f32_16x16x32_bf16 v[88:91], v[224:227], a[208:211], v[88:91]\n"
      "v_cvt_pk_bf16_f32 v140, v36, v40\n"
      "v_cvt_pk_bf16_f32 v141, v37, v41\n"
      "v_mfma_f32_16x16x32_bf16 v[92:95], v[216:219], a[208:211], v[92:95]\n"
      "v_cvt_pk_bf16_f32 v142, v38, v42\n"
      "v_cvt_pk_bf16_f32 v143, v39, v43\n"
      "v_mfma_f32_16x16x32_bf16 v[96:99], v[208:211], a[208:211], v[96:99]\n"
      "v_cvt_pk_bf16_f32 v136, v28, v32\n"
      "v_cvt_pk_bf16_f32 v137, v29, v33\n"
      "v_mfma_f32_16x16x32_bf16 v[76:79], v[204:207], a[212:215], v[76:79]\n"
      "s_waitcnt vmcnt(6)\n"
      "v_cvt_pk_bf16_f32 v138, v30, v34\n"
      "v_cvt_pk_bf16_f32 v139, v31, v35\n"
      "s_barrier\n"
      "s_add_u32 s53, s54, 1769472\n"
      "s_add_u32 m0, s37, 116224\n"
      "v_mfma_f32_4x4x4_16b_bf16 v[28:31], a[244:245], a[64:65], 0\n"
      "v_mfma_f32_4x4x4_16b_bf16 v[32:35], a[244:245], a[66:67], 0\n"
      "buffer_load_dwordx4 v8, s[4:7], s53 offen offset:0 nt lds\n"
      "buffer_load_dwordx4 v9, s[4:7], s53 offen offset:1024 nt lds\n"
      "v_mfma_f32_4x4x4_16b_bf16 v[28:31], a[248:249], a[232:233], v[28:31]\n"
      "ds_read_b64_tr_b16 a[72:73], v60 offset:0\n"
      "v_mfma_f32_4x4x4_16b_bf16 v[32:35], a[248:249], a[234:235], v[32:35]\n"
      "ds_read_b64_tr_b16 a[74:75], v60 offset:768\n"
      "v_mfma_f32_4x4x4_16b_bf16 v[36:39], a[246:247], a[228:229], v[28:31]\n"
      "ds_read_b64_tr_b16 a[224:225], v60 offset:12288\n"
      "v_mfma_f32_4x4x4_16b_bf16 v[40:43], a[246:247], a[230:231], v[32:35]\n"
      "ds_read_b64_tr_b16 a[226:227], v60 offset:13056\n"
      "v_mfma_f32_4x4x4_16b_bf16 v[28:31], a[250:251], a[228:229], v[28:31]\n"
      "ds_read_b64_tr_b16 a[220:221], v60 offset:6144\n"
      "v_mfma_f32_4x4x4_16b_bf16 v[32:35], a[250:251], a[230:231], v[32:35]\n"
      "ds_read_b64_tr_b16 a[222:223], v60 offset:6912\n"
      "ds_read_b128 a[208:211], v1 offset:64\n"
      "v_mfma_f32_16x16x32_bf16 v[80:83], v[196:199], a[212:215], v[80:83]\n"
      "v_cvt_pk_bf16_f32 v132, v52, v56\n"
      "v_cvt_pk_bf16_f32 v133, v53, v57\n"
      "v_mfma_f32_16x16x32_bf16 v[84:87], v[188:191], a[212:215], v[84:87]\n"
      "v_cvt_pk_bf16_f32 v134, v54, v58\n"
      "v_cvt_pk_bf16_f32 v135, v55, v59\n"
      "v_mfma_f32_16x16x32_bf16 v[88:91], v[200:203], a[192:195], v[88:91]\n"
      "s_add_u32 m0, s37, 120328\n"
      "v_cvt_pk_bf16_f32 v128, v44, v48\n"
      "v_cvt_pk_bf16_f32 v129, v45, v49\n"
      "v_mfma_f32_16x16x32_bf16 v[92:95], v[192:195], a[192:195], v[92:95]\n"
      "s_add_u32 s53, s54, 1966080\n"
      "v_cvt_pk_bf16_f32 v130, v46, v50\n"
      "v_cvt_pk_bf16_f32 v131, v47, v51\n"
      "s_nop 0\n"
      "s_waitcnt lgkmcnt(1)\n"
      "v_mfma_f32_4x4x4_16b_bf16 v[44:47], a[244:245], a[68:69], 0\n"
      "v_mfma_f32_4x4x4_16b_bf16 v[48:51], a[244:245], a[70:71], 0\n"
      "buffer_load_dwordx4 v8, s[4:7], s53 offen offset:0 nt lds\n"
      "buffer_load_dwordx4 v9, s[4:7], s53 offen offset:1024 nt lds\n"
      "v_mfma_f32_4x4x4_16b_bf16 v[44:47], a[248:249], a[240:241], v[44:47]\n"
      "ds_read_b64_tr_b16 a[76:77], v60 offset:64\n"
      "v_mfma_f32_4x4x4_16b_bf16 v[48:51], a[248:249], a[242:243], v[48:51]\n"
      "ds_read_b64_tr_b16 a[78:79], v60 offset:832\n"
      "v_mfma_f32_4x4x4_16b_bf16 v[52:55], a[246:247], a[236:237], v[44:47]\n"
      "ds_read_b64_tr_b16 a[232:233], v60 offset:12352\n"
      "v_mfma_f32_4x4x4_16b_bf16 v[56:59], a[246:247], a[238:239], v[48:51]\n"
      "ds_read_b64_tr_b16 a[234:235], v60 offset:13120\n"
      "v_mfma_f32_4x4x4_16b_bf16 v[44:47], a[250:251], a[236:237], v[44:47]\n"
      "ds_read_b64_tr_b16 a[228:229], v60 offset:6208\n"
      "v_mfma_f32_4x4x4_16b_bf16 v[48:51], a[250:251], a[238:239], v[48:51]\n"
      "ds_read_b64_tr_b16 a[230:231], v60 offset:6976\n"
      "ds_read_b128 a[212:215], v1 offset:4160\n"
      "v_mfma_f32_16x16x32_bf16 v[96:99], v[184:187], a[192:195], v[96:99]\n"
      "s_nop 0\n"
      "v_cvt_pk_bf16_f32 v124, v36, v40\n"
      "v_cvt_pk_bf16_f32 v125, v37, v41\n"
      "v_mfma_f32_16x16x32_bf16 v[76:79], v[180:183], a[196:199], v[76:79]\n"
      "s_nop 0\n"
      "v_cvt_pk_bf16_f32 v126, v38, v42\n"
      "v_cvt_pk_bf16_f32 v127, v39, v43\n"
      "v_mfma_f32_16x16x32_bf16 v[80:83], v[172:175], a[196:199], v[80:83]\n"
      "s_add_u32 m0, s37, 124552\n"
      "v_cvt_pk_bf16_f32 v120, v28, v32\n"
      "v_cvt_pk_bf16_f32 v121, v29, v33\n"
      "v_mfma_f32_16x16x32_bf16 v[84:87], v[164:167], a[196:199], v[84:87]\n"
      "s_add_u32 s53, s54, 2162688\n"
      "v_cvt_pk_bf16_f32 v122, v30, v34\n"
      "v_cvt_pk_bf16_f32 v123, v31, v35\n"
      "v_mfma_f32_4x4x4_16b_bf16 v[28:31], a[244:245], a[72:73], 0\n"
      "v_mfma_f32_4x4x4_16b_bf16 v[32:35], a[244:245], a[74:75], 0\n"
      "buffer_load_dwordx4 v8, s[4:7], s53 offen offset:0 nt lds\n"
      "buffer_load_dwordx4 v9, s[4:7], s53 offen offset:1024 nt lds\n"
      "v_mfma_f32_4x4x4_16b_bf16 v[28:31], a[248:249], a[224:225], v[28:31]\n"
      "ds_read_b64_tr_b16 a[80:81], v60 offset:128\n"
      "v_mfma_f32_4x4x4_16b_bf16 v[32:35], a[248:249], a[226:227], v[32:35]\n"
      "ds_read_b64_tr_b16 a[82:83], v60 offset:896\n"
      "v_mfma_f32_4x4x4_16b_bf16 v[36:39], a[246:247], a[220:221], v[28:31]\n"
      "ds_read_b64_tr_b16 a[240:241], v60 offset:12416\n"
      "v_mfma_f32_4x4x4_16b_bf16 v[40:43], a[246:247], a[222:223], v[32:35]\n"
      "ds_read_b64_tr_b16 a[242:243], v60 offset:13184\n"
      "v_mfma_f32_4x4x4_16b_bf16 v[28:31], a[250:251], a[220:221], v[28:31]\n"
      "ds_read_b64_tr_b16 a[236:237], v60 offset:6272\n"
      "v_mfma_f32_4x4x4_16b_bf16 v[32:35], a[250:251], a[222:223], v[32:35]\n"
      "ds_read_b64_tr_b16 a[238:239], v60 offset:7040\n"
      "ds_read_b128 a[192:195], v4 offset:55296\n"
      "ds_read_b128 a[196:199], v1 offset:128\n"
      "v_mfma_f32_16x16x32_bf16 v[88:91], v[176:179], a[200:203], v[88:91]\n"
      "s_nop 0\n"
      "v_cvt_pk_bf16_f32 v116, v52, v56\n"
      "v_cvt_pk_bf16_f32 v117, v53, v57\n"
      "v_mfma_f32_16x16x32_bf16 v[92:95], v[168:171], a[200:203], v[92:95]\n"
      "s_nop 0\n"
      "v_cvt_pk_bf16_f32 v118, v54, v58\n"
      "v_cvt_pk_bf16_f32 v119, v55, v59\n"
      "v_mfma_f32_16x16x32_bf16 v[96:99], v[160:163], a[200:203], v[96:99]\n"
      "s_nop 0\n"
      "v_cvt_pk_bf16_f32 v112, v44, v48\n"
      "v_cvt_pk_bf16_f32 v113, v45, v49\n"
      "v_mfma_f32_16x16x32_bf16 v[64:67], a[48:51], a[204:207], v[64:67]\n"
      "s_waitcnt vmcnt(9)\n"
      "v_cvt_pk_bf16_f32 v114, v46, v50\n"
      "v_cvt_pk_bf16_f32 v115, v47, v51\n"
      "s_barrier\n"
      "s_add_u32 s53, s54, 2359296\n"
      "s_add_u32 m0, s37, 128896\n"
      "s_waitcnt lgkmcnt(0)\n"
      "v_mfma_f32_4x4x4_16b_bf16 v[44:47], a[244:245], a[76:77], 0\n"
      "v_mfma_f32_4x4x4_16b_bf16 v[48:51], a[244:245], a[78:79], 0\n"
      "buffer_load_dwordx4 v8, s[4:7], s53 offen offset:0 nt lds\n"
      "buffer_load_dwordx4 v9, s[4:7], s53 offen offset:1024 nt lds\n"
      "v_mfma_f32_4x4x4_16b_bf16 v[44:47], a[248:249], a[232:233], v[44:47]\n"
      "ds_read_b64_tr_b16 a[84:85], v60 offset:18432\n"
      "v_mfma_f32_4x4x4_16b_bf16 v[48:51], a[248:249], a[234:235], v[48:51]\n"
      "ds_read_b64_tr_b16 a[86:87], v60 offset:19200\n"
      "v_mfma_f32_4x4x4_16b_bf16 v[52:55], a[246:247], a[228:229], v[44:47]\n"
      "ds_read_b64_tr_b16 a[224:225], v61 offset:6400\n"
      "v_mfma_f32_4x4x4_16b_bf16 v[56:59], a[246:247], a[230:231], v[48:51]\n"
      "ds_read_b64_tr_b16 a[226:227], v61 offset:7168\n"
      "v_mfma_f32_4x4x4_16b_bf16 v[44:47], a[250:251], a[228:229], v[44:47]\n"
      "ds_read_b64_tr_b16 a[220:221], v61 offset:256\n"
      "v_mfma_f32_4x4x4_16b_bf16 v[48:51], a[250:251], a[230:231], v[48:51]\n"
      "ds_read_b64_tr_b16 a[222:223], v61 offset:1024\n"
      "ds_read_b128 a[200:203], v1 offset:4224\n"
      "v_mfma_f32_16x16x32_bf16 v[68:71], a[52:55], a[204:207], v[68:71]\n"
      "v_cvt_pk_bf16_f32 v108, v36, v40\n"
      "v_cvt_pk_bf16_f32 v109, v37, v41\n"
      "v_mfma_f32_16x16x32_bf16 v[72:75], a[56:59], a[204:207], v[72:75]\n"
      "v_cvt_pk_bf16_f32 v110, v38, v42\n"
      "v_cvt_pk_bf16_f32 v111, v39, v43\n"
      "v_mfma_f32_16x16x32_bf16 v[76:79], v[156:159], a[208:211], v[76:79]\n"
      "s_add_u32 m0, s37, 133120\n"
      "v_cvt_pk_bf16_f32 v104, v28, v32\n"
      "v_cvt_pk_bf16_f32 v105, v29, v33\n"
      "v_mfma_f32_16x16x32_bf16 v[80:83], v[148:151], a[208:211], v[80:83]\n"
      "s_add_u32 s53, s54, 2555904\n"
      "v_cvt_pk_bf16_f32 v106, v30, v34\n"
      "v_cvt_pk_bf16_f32 v107, v31, v35\n"
      "v_mfma_f32_4x4x4_16b_bf16 v[28:31], a[244:245], a[80:81], 0\n"
      "v_mfma_f32_4x4x4_16b_bf16 v[32:35], a[244:245], a[82:83], 0\n"
      "buffer_load_dwordx4 v8, s[4:7], s53 offen offset:0 nt lds\n"
      "buffer_load_dwordx4 v9, s[4:7], s53 offen offset:1024 nt lds\n"
      "v_mfma_f32_4x4x4_16b_bf16 v[28:31], a[248:249], a[240:241], v[28:31]\n"
      "ds_read_b64_tr_b16 a[88:89], v60 offset:18496\n"
      "v_mfma_f32_4x4x4_16b_bf16 v[32:35], a[248:249], a[242:243], v[32:35]\n"
      "ds_read_b64_tr_b16 a[90:91], v60 offset:19264\n"
      "v_mfma_f32_4x4x4_16b_bf16 v[36:39], a[246:247], a[236:237], v[28:31]\n"
      "ds_read_b64_tr_b16 a[232:233], v61 offset:6464\n"
      "v_mfma_f32_4x4x4_16b_bf16 v[40:43], a[246:247], a[238:239], v[32:35]\n"
      "ds_read_b64_tr_b16 a[234:235], v61 offset:7232\n"
      "v_mfma_f32_4x4x4_16b_bf16 v[28:31], a[250:251], a[236:237], v[28:31]\n"
      "ds_read_b64_tr_b16 a[228:229], v61 offset:320\n"
      "v_mfma_f32_4x4x4_16b_bf16 v[32:35], a[250:251], a[238:239], v[32:35]\n"
      "ds_read_b64_tr_b16 a[230:231], v61 offset:1088\n"
      "ds_read_b128 a[204:207], v5 offset:55168\n"
      "v_mfma_f32_16x16x32_bf16 v[84:87], v[140:143], a[208:211], v[84:87]\n"
      "v_cvt_pk_bf16_f32 v100, v52, v56\n"
      "v_cvt_pk_bf16_f32 v101, v53, v57\n"
      "v_mfma_f32_16x16x32_bf16 v[88:91], v[152:155], a[212:215], v[88:91]\n"
      "v_cvt_pk_bf16_f32 v102, v54, v58\n"
      "v_cvt_pk_bf16_f32 v103, v55, v59\n"
      "v_mfma_f32_16x16x32_bf16 v[92:95], v[144:147], a[212:215], v[92:95]\n"
      "v_cvt_pk_bf16_f32 v44, v44, v48\n"
      "v_cvt_pk_bf16_f32 v45, v45, v49\n"
      "v_mfma_f32_16x16x32_bf16 v[96:99], v[136:139], a[212:215], v[96:99]\n"
      "v_cvt_pk_bf16_f32 v46, v46, v50\n"
      "v_cvt_pk_bf16_f32 v47, v47, v51\n"
      "v_mfma_f32_16x16x32_bf16 v[64:67], a[60:63], a[192:195], v[64:67]\n"
      "s_add_u32 m0, s37, 137224\n"
      "v_accvgpr_write_b32 a164, v44\n"
      "v_accvgpr_write_b32 a165, v45\n"
      "v_mfma_f32_16x16x32_bf16 v[68:71], a[64:67], a[192:195], v[68:71]\n"
      "s_add_u32 s53, s54, 2752512\n"
      "v_accvgpr_write_b32 a166, v46\n"
      "v_accvgpr_write_b32 a167, v47\n"
      "s_nop 0\n"
      "s_waitcnt lgkmcnt(0)\n"
      "v_mfma_f32_4x4x4_16b_bf16 v[44:47], a[244:245], a[84:85], 0\n"
      "v_mfma_f32_4x4x4_16b_bf16 v[48:51], a[244:245], a[86:87], 0\n"
      "buffer_load_dwordx4 v8, s[4:7], s53 offen offset:0 nt lds\n"
      "buffer_load_dwordx4 v9, s[4:7], s53 offen offset:1024 nt lds\n"
      "v_mfma_f32_4x4x4_16b_bf16 v[44:47], a[248:249], a[224:225], v[44:47]\n"
      "ds_read_b64_tr_b16 a[92:93], v60 offset:18560\n"
      "v_mfma_f32_4x4x4_16b_bf16 v[48:51], a[248:249], a[226:227], v[48:51]\n"
      "ds_read_b64_tr_b16 a[94:95], v60 offset:19328\n"
      "v_mfma_f32_4x4x4_16b_bf16 v[52:55], a[246:247], a[220:221], v[44:47]\n"
      "ds_read_b64_tr_b16 a[240:241], v61 offset:6528\n"
      "v_mfma_f32_4x4x4_16b_bf16 v[56:59], a[246:247], a[222:223], v[48:51]\n"
      "ds_read_b64_tr_b16 a[242:243], v61 offset:7296\n"
      "v_mfma_f32_4x4x4_16b_bf16 v[44:47], a[250:251], a[220:221], v[44:47]\n"
      "ds_read_b64_tr_b16 a[236:237], v61 offset:384\n"
      "v_mfma_f32_4x4x4_16b_bf16 v[48:51], a[250:251], a[222:223], v[48:51]\n"
      "ds_read_b64_tr_b16 a[238:239], v61 offset:1152\n"
      "ds_read_b128 a[208:211], v2 offset:0\n"
      "ds_read_b128 a[212:215], v2 offset:4096\n"
      "v_cvt_pk_bf16_f32 v36, v36, v40\n"
      "v_cvt_pk_bf16_f32 v37, v37, v41\n"
      "v_cvt_pk_bf16_f32 v38, v38, v42\n"
      "v_cvt_pk_bf16_f32 v39, v39, v43\n"
      "v_mfma_f32_16x16x32_bf16 v[72:75], a[68:71], a[192:195], v[72:75]\n"
      "v_accvgpr_write_b32 a160, v36\n"
      "v_accvgpr_write_b32 a161, v37\n"
      "v_mfma_f32_16x16x32_bf16 v[76:79], v[132:135], a[196:199], v[76:79]\n"
      "s_nop 0\n"
      "v_accvgpr_write_b32 a162, v38\n"
      "v_accvgpr_write_b32 a163, v39\n"
      "v_mfma_f32_16x16x32_bf16 v[80:83], v[124:127], a[196:199], v[80:83]\n"
      "s_nop 0\n"
      "v_cvt_pk_bf16_f32 v28, v28, v32\n"
      "v_cvt_pk_bf16_f32 v29, v29, v33\n"
      "v_mfma_f32_16x16x32_bf16 v[84:87], v[116:119], a[196:199], v[84:87]\n"
      "s_nop 0\n"
      "v_cvt_pk_bf16_f32 v30, v30, v34\n"
      "v_cvt_pk_bf16_f32 v31, v31, v35\n"
      "v_mfma_f32_16x16x32_bf16 v[88:91], v[128:131], a[200:203], v[88:91]\n"
      "s_nop 0\n"
      "v_accvgpr_write_b32 a156, v28\n"
      "v_accvgpr_write_b32 a157, v29\n"
      "v_mfma_f32_16x16x32_bf16 v[92:95], v[120:123], a[200:203], v[92:95]\n"
      "s_waitcnt vmcnt(12)\n"
      "v_accvgpr_write_b32 a158, v30\n"
      "v_accvgpr_write_b32 a159, v31\n"
      "s_barrier\n"
      "s_add_u32 s53, s54, 2949120\n"
      "s_add_u32 m0, s37, 141448\n"
      "v_mfma_f32_4x4x4_16b_bf16 v[28:31], a[244:245], a[88:89], 0\n"
      "v_mfma_f32_4x4x4_16b_bf16 v[32:35], a[244:245], a[90:91], 0\n"
      "buffer_load_dwordx4 v8, s[4:7], s53 offen offset:0 nt lds\n"
      "buffer_load_dwordx4 v9, s[4:7], s53 offen offset:1024 nt lds\n"
      "v_mfma_f32_4x4x4_16b_bf16 v[28:31], a[248:249], a[232:233], v[28:31]\n"
      "ds_read_b64_tr_b16 a[96:97], v61 offset:12544\n"
      "v_mfma_f32_4x4x4_16b_bf16 v[32:35], a[248:249], a[234:235], v[32:35]\n"
      "ds_read_b64_tr_b16 a[98:99], v61 offset:13312\n"
      "v_mfma_f32_4x4x4_16b_bf16 v[36:39], a[246:247], a[228:229], v[28:31]\n"
      "ds_read_b64_tr_b16 a[224:225], v62 offset:39680\n"
      "v_mfma_f32_4x4x4_16b_bf16 v[40:43], a[246:247], a[230:231], v[32:35]\n"
      "ds_read_b64_tr_b16 a[226:227], v62 offset:40448\n"
      "v_mfma_f32_4x4x4_16b_bf16 v[28:31], a[250:251], a[228:229], v[28:31]\n"
      "ds_read_b64_tr_b16 a[220:221], v61 offset:18688\n"
      "v_mfma_f32_4x4x4_16b_bf16 v[32:35], a[250:251], a[230:231], v[32:35]\n"
      "ds_read_b64_tr_b16 a[222:223], v61 offset:19456\n"
      "ds_read_b128 a[192:195], v5 offset:55232\n"
      "v_cvt_pk_bf16_f32 v52, v52, v56\n"
      "v_cvt_pk_bf16_f32 v53, v53, v57\n"
      "v_cvt_pk_bf16_f32 v54, v54, v58\n"
      "v_cvt_pk_bf16_f32 v55, v55, v59\n"
      "v_mfma_f32_16x16x32_bf16 v[96:99], v[112:115], a[200:203], v[96:99]\n"
      "s_nop 0\n"
      "v_accvgpr_write_b32 a152, v52\n"
      "v_accvgpr_write_b32 a153, v53\n"
      "v_mfma_f32_16x16x32_bf16 v[64:67], a[72:75], a[204:207], v[64:67]\n"
      "s_nop 0\n"
      "v_accvgpr_write_b32 a154, v54\n"
      "v_accvgpr_write_b32 a155, v55\n"
      "v_mfma_f32_16x16x32_bf16 v[68:71], a[76:79], a[204:207], v[68:71]\n"
      "s_nop 0\n"
      "v_cvt_pk_bf16_f32 v44, v44, v48\n"
      "v_cvt_pk_bf16_f32 v45, v45, v49\n"
      "v_mfma_f32_16x16x32_bf16 v[72:75], a[80:83], a[204:207], v[72:75]\n"
      "s_waitcnt lgkmcnt(0)\n"
      "v_cvt_pk_bf16_f32 v46, v46, v50\n"
      "v_cvt_pk_bf16_f32 v47, v47, v51\n"
      "v_mfma_f32_16x16x32_bf16 v[76:79], v[108:111], a[208:211], v[76:79]\n"
      "s_add_u32 m0, s37, 145792\n"
      "v_accvgpr_write_b32 a148, v44\n"
      "v_accvgpr_write_b32 a149, v45\n"
      "v_mfma_f32_16x16x32_bf16 v[80:83], v[100:103], a[208:211], v[80:83]\n"
      "s_add_u32 s53, s54, 3145728\n"
      "v_accvgpr_write_b32 a150, v46\n"
      "v_accvgpr_write_b32 a151, v47\n"
      "v_mfma_f32_4x4x4_16b_bf16 v[44:47], a[244:245], a[92:93], 0\n"
      "v_mfma_f32_4x4x4_16b_bf16 v[48:51], a[244:245], a[94:95], 0\n"
      "buffer_load_dwordx4 v8, s[4:7], s53 offen offset:0 nt lds\n"
      "buffer_load_dwordx4 v9, s[4:7], s53 offen offset:1024 nt lds\n"
      "v_mfma_f32_4x4x4_16b_bf16 v[44:47], a[248:249], a[240:241], v[44:47]\n"
      "ds_read_b64_tr_b16 a[100:101], v61 offset:12608\n"
      "v_mfma_f32_4x4x4_16b_bf16 v[48:51], a[248:249], a[242:243], v[48:51]\n"
      "ds_read_b64_tr_b16 a[102:103], v61 offset:13376\n"
      "v_mfma_f32_4x4x4_16b_bf16 v[52:55], a[246:247], a[236:237], v[44:47]\n"
      "ds_read_b64_tr_b16 a[232:233], v62 offset:39744\n"
      "v_mfma_f32_4x4x4_16b_bf16 v[56:59], a[246:247], a[238:239], v[48:51]\n"
      "ds_read_b64_tr_b16 a[234:235], v62 offset:40512\n"
      "v_mfma_f32_4x4x4_16b_bf16 v[44:47], a[250:251], a[236:237], v[44:47]\n"
      "ds_read_b64_tr_b16 a[228:229], v61 offset:18752\n"
      "v_mfma_f32_4x4x4_16b_bf16 v[48:51], a[250:251], a[238:239], v[48:51]\n"
      "ds_read_b64_tr_b16 a[230:231], v61 offset:19520\n"
      "ds_read_b128 a[196:199], v2 offset:64\n"
      "ds_read_b128 a[200:203], v2 offset:4160\n"
      "v_cvt_pk_bf16_f32 v36, v36, v40\n"
      "v_cvt_pk_bf16_f32 v37, v37, v41\n"
      "v_cvt_pk_bf16_f32 v38, v38, v42\n"
      "v_cvt_pk_bf16_f32 v39, v39, v43\n"
      "v_accvgpr_write_b32 a144, v36\n"
      "v_accvgpr_write_b32 a145, v37\n"
      "v_accvgpr_write_b32 a146, v38\n"
      "v_accvgpr_write_b32 a147, v39\n"
      "v_mfma_f32_16x16x32_bf16 v[84:87], a[160:163], a[208:211], v[84:87]\n"
      "s_nop 0\n"
      "v_cvt_pk_bf16_f32 v28, v28, v32\n"
      "v_cvt_pk_bf16_f32 v29, v29, v33\n"
      "v_mfma_f32_16x16x32_bf16 v[88:91], v[104:107], a[212:215], v[88:91]\n"
      "s_nop 0\n"
      "v_cvt_pk_bf16_f32 v30, v30, v34\n"
      "v_cvt_pk_bf16_f32 v31, v31, v35\n"
      "v_mfma_f32_16x16x32_bf16 v[92:95], a[164:167], a[212:215], v[92:95]\n"
      "s_nop 0\n"
      "v_accvgpr_write_b32 a140, v28\n"
      "v_accvgpr_write_b32 a141, v29\n"
      "v_mfma_f32_16x16x32_bf16 v[96:99], a[156:159], a[212:215], v[96:99]\n"
      "s_nop 0\n"
      "v_accvgpr_write_b32 a142, v30\n"
      "v_accvgpr_write_b32 a143, v31\n"
      "v_mfma_f32_4x4x4_16b_bf16 v[28:31], a[244:245], a[96:97], 0\n"
      "v_mfma_f32_4x4x4_16b_bf16 v[32:35], a[244:245], a[98:99], 0\n"
      "ds_read_b64_tr_b16 a[104:105], v61 offset:12672\n"
      "v_mfma_f32_4x4x4_16b_bf16 v[28:31], a[248:249], a[224:225], v[28:31]\n"
      "ds_read_b64_tr_b16 a[106:107], v61 offset:13440\n"
      "v_mfma_f32_4x4x4_16b_bf16 v[32:35], a[248:249], a[226:227], v[32:35]\n"
      "ds_read_b64_tr_b16 a[240:241], v62 offset:39808\n"
      "v_mfma_f32_4x4x4_16b_bf16 v[36:39], a[246:247], a[220:221], v[28:31]\n"
      "ds_read_b64_tr_b16 a[242:243], v62 offset:40576\n"
      "v_mfma_f32_4x4x4_16b_bf16 v[40:43], a[246:247], a[222:223], v[32:35]\n"
      "ds_read_b64_tr_b16 a[236:237], v61 offset:18816\n"
      "v_mfma_f32_4x4x4_16b_bf16 v[28:31], a[250:251], a[220:221], v[28:31]\n"
      "ds_read_b64_tr_b16 a[238:239], v61 offset:19584\n"
      "v_mfma_f32_4x4x4_16b_bf16 v[32:35], a[250:251], a[222:223], v[32:35]\n"
      "v_cvt_pk_bf16_f32 v52, v52, v56\n"
      "v_cvt_pk_bf16_f32 v53, v53, v57\n"
      "v_cvt_pk_bf16_f32 v54, v54, v58\n"
      "v_cvt_pk_bf16_f32 v55, v55, v59\n"
      "v_accvgpr_write_b32 a136, v52\n"
      "v_accvgpr_write_b32 a137, v53\n"
      "v_accvgpr_write_b32 a138, v54\n"
      "v_accvgpr_write_b32 a139, v55\n"
      "v_cvt_pk_bf16_f32 v44, v44, v48\n"
      "v_cvt_pk_bf16_f32 v45, v45, v49\n"
      "v_mfma_f32_16x16x32_bf16 v[64:67], a[84:87], a[192:195], v[64:67]\n"
      "s_nop 0\n"
      "v_cvt_pk_bf16_f32 v46, v46, v50\n"
      "v_cvt_pk_bf16_f32 v47, v47, v51\n"
      "v_mfma_f32_16x16x32_bf16 v[68:71], a[88:91], a[192:195], v[68:71]\n"
      "s_nop 0\n"
      "v_accvgpr_write_b32 a132, v44\n"
      "v_accvgpr_write_b32 a133, v45\n"
      "v_mfma_f32_16x16x32_bf16 v[72:75], a[92:95], a[192:195], v[72:75]\n"
      "s_waitcnt lgkmcnt(0)\n"
      "v_accvgpr_write_b32 a134, v46\n"
      "v_accvgpr_write_b32 a135, v47\n"
      "v_mfma_f32_4x4x4_16b_bf16 v[44:47], a[244:245], a[100:101], 0\n"
      "v_mfma_f32_4x4x4_16b_bf16 v[48:51], a[244:245], a[102:103], 0\n"
      "ds_read_b128 a[204:207], v5 offset:55296\n"
      "v_mfma_f32_4x4x4_16b_bf16 v[44:47], a[248:249], a[232:233], v[44:47]\n"
      "v_mfma_f32_4x4x4_16b_bf16 v[48:51], a[248:249], a[234:235], v[48:51]\n"
      "s_nop 0\n"
      "v_mfma_f32_4x4x4_16b_bf16 v[52:55], a[246:247], a[228:229], v[44:47]\n"
      "v_mfma_f32_4x4x4_16b_bf16 v[56:59], a[246:247], a[230:231], v[48:51]\n"
      "v_mfma_f32_4x4x4_16b_bf16 v[44:47], a[250:251], a[228:229], v[44:47]\n"
      "v_mfma_f32_4x4x4_16b_bf16 v[48:51], a[250:251], a[230:231], v[48:51]\n"
      "v_cvt_pk_bf16_f32 v36, v36, v40\n"
      "v_cvt_pk_bf16_f32 v37, v37, v41\n"
      "v_cvt_pk_bf16_f32 v38, v38, v42\n"
      "v_cvt_pk_bf16_f32 v39, v39, v43\n"
      "v_mfma_f32_16x16x32_bf16 v[76:79], a[152:155], a[196:199], v[76:79]\n"
      "v_accvgpr_write_b32 a128, v36\n"
      "v_accvgpr_write_b32 a129, v37\n"
      "v_mfma_f32_16x16x32_bf16 v[80:83], a[144:147], a[196:199], v[80:83]\n"
      "v_accvgpr_write_b32 a130, v38\n"
      "v_accvgpr_write_b32 a131, v39\n"
      "v_mfma_f32_16x16x32_bf16 v[84:87], a[136:139], a[196:199], v[84:87]\n"
      "v_cvt_pk_bf16_f32 v28, v28, v32\n"
      "v_cvt_pk_bf16_f32 v29, v29, v33\n"
      "v_mfma_f32_16x16x32_bf16 v[88:91], a[148:151], a[200:203], v[88:91]\n"
      "v_cvt_pk_bf16_f32 v30, v30, v34\n"
      "v_cvt_pk_bf16_f32 v31, v31, v35\n"
      "v_mfma_f32_16x16x32_bf16 v[92:95], a[140:143], a[200:203], v[92:95]\n"
      "v_accvgpr_write_b32 a124, v28\n"
      "v_accvgpr_write_b32 a125, v29\n"
      "v_mfma_f32_16x16x32_bf16 v[96:99], a[132:135], a[200:203], v[96:99]\n"
      "v_accvgpr_write_b32 a126, v30\n"
      "v_accvgpr_write_b32 a127, v31\n"
      "v_mfma_f32_4x4x4_16b_bf16 v[28:31], a[244:245], a[104:105], 0\n"
      "v_mfma_f32_4x4x4_16b_bf16 v[32:35], a[244:245], a[106:107], 0\n"
      "ds_read_b128 a[208:211], v2 offset:128\n"
      "v_mfma_f32_4x4x4_16b_bf16 v[28:31], a[248:249], a[240:241], v[28:31]\n"
      "ds_read_b128 a[212:215], v2 offset:4224\n"
      "v_mfma_f32_4x4x4_16b_bf16 v[32:35], a[248:249], a[242:243], v[32:35]\n"
      "v_mfma_f32_4x4x4_16b_bf16 v[36:39], a[246:247], a[236:237], v[28:31]\n"
      "s_nop 0\n"
      "s_nop 0\n"
      "v_mfma_f32_4x4x4_16b_bf16 v[40:43], a[246:247], a[238:239], v[32:35]\n"
      "v_mfma_f32_4x4x4_16b_bf16 v[28:31], a[250:251], a[236:237], v[28:31]\n"
      "v_mfma_f32_4x4x4_16b_bf16 v[32:35], a[250:251], a[238:239], v[32:35]\n"
      "v_cvt_pk_bf16_f32 v52, v52, v56\n"
      "v_cvt_pk_bf16_f32 v53, v53, v57\n"
      "v_cvt_pk_bf16_f32 v54, v54, v58\n"
      "v_cvt_pk_bf16_f32 v55, v55, v59\n"
      "v_accvgpr_write_b32 a120, v52\n"
      "v_accvgpr_write_b32 a121, v53\n"
      "v_accvgpr_write_b32 a122, v54\n"
      "v_accvgpr_write_b32 a123, v55\n"
      "v_cvt_pk_bf16_f32 v44, v44, v48\n"
      "v_cvt_pk_bf16_f32 v45, v45, v49\n"
      "v_cvt_pk_bf16_f32 v46, v46, v50\n"
      "v_cvt_pk_bf16_f32 v47, v47, v51\n"
      "v_accvgpr_write_b32 a116, v44\n"
      "v_accvgpr_write_b32 a117, v45\n"
      "v_accvgpr_write_b32 a118, v46\n"
      "v_accvgpr_write_b32 a119, v47\n"
      "s_waitcnt lgkmcnt(0)\n"
      "s_barrier\n"
      "s_mov_b32 m0, s64\n"
      "s_nop 0\n"
      "v_mfma_f32_16x16x32_bf16 v[64:67], a[96:99], a[204:207], v[64:67]\n"
      "s_add_u32 s53, s54, 3342336\n"
      "ds_read_b128 a[192:195], v3 offset:0\n"
      "ds_read_b64_tr_b16 a[220:221], v7 offset:18944\n"
      "ds_read_b64_tr_b16 a[222:223], v7 offset:18952\n"
      "v_cvt_pk_bf16_f32 v36, v36, v40\n"
      "v_cvt_pk_bf16_f32 v37, v37, v41\n"
      "v_cvt_pk_bf16_f32 v38, v38, v42\n"
      "v_cvt_pk_bf16_f32 v39, v39, v43\n"
      "v_mfma_f32_16x16x32_bf16 v[68:71], a[100:103], a[204:207], v[68:71]\n"
      "s_add_u32 m0, s37, 82432\n"
      "ds_read_b128 a[196:199], v3 offset:64\n"
      "v_accvgpr_write_b32 a112, v36\n"
      "v_accvgpr_write_b32 a113, v37\n"
      "v_accvgpr_write_b32 a114, v38\n"
      "v_accvgpr_write_b32 a115, v39\n"
      "v_cvt_pk_bf16_f32 v28, v28, v32\n"
      "v_cvt_pk_bf16_f32 v29, v29, v33\n"
      "v_mfma_f32_16x16x32_bf16 v[72:75], a[104:107], a[204:207], v[72:75]\n"
      "s_nop 0\n"
      "buffer_load_dwordx4 v8, s[4:7], s53 offen offset:0 nt lds\n"
      "buffer_load_dwordx4 v9, s[4:7], s53 offen offset:1024 nt lds\n"
      "v_cvt_pk_bf16_f32 v30, v30, v34\n"
      "v_mfma_f32_16x16x32_bf16 v[76:79], a[128:131], a[208:211], v[76:79]\n"
      "s_mov_b32 m0, s64\n"
      "ds_read_b128 a[200:203], v3 offset:128\n"
      "ds_read_b64_tr_b16 a[224:225], v7 offset:19456\n"
      "ds_read_b64_tr_b16 a[226:227], v7 offset:19464\n"
      "v_cvt_pk_bf16_f32 v31, v31, v35\n"
      "v_accvgpr_write_b32 a108, v28\n"
      "v_accvgpr_write_b32 a109, v29\n"
      "v_accvgpr_write_b32 a110, v30\n"
      "v_accvgpr_write_b32 a111, v31\n"
      "v_mfma_f32_16x16x32_bf16 v[80:83], a[120:123], a[208:211], v[80:83]\n"
      "s_nop 0\n"
      "buffer_load_dword a252, v8, s[4:7], s40 offen\n"
      "v_mfma_f32_16x16x32_bf16 v[84:87], a[112:115], a[208:211], v[84:87]\n"
      "s_waitcnt lgkmcnt(3)\n"
      "buffer_load_dword a252, v8, s[4:7], s40 offen\n"
      "buffer_load_dword a252, v8, s[4:7], s40 offen\n"
      "v_mfma_f32_4x4x4_16b_bf16 v[20:23], a[216:217], a[220:221], 0\n"
      "buffer_load_dword a252, v8, s[4:7], s40 offen\n"
      "v_mfma_f32_4x4x4_16b_bf16 v[24:27], a[216:217], a[222:223], 0\n"
      "buffer_load_dword a252, v8, s[4:7], s40 offen\n"
      "v_mfma_f32_16x16x32_bf16 v[88:91], a[124:127], a[212:215], v[88:91]\n"
      "s_add_u32 s53, s54, 3538944\n"
      "ds_read_b128 a[204:207], v4 offset:0\n"
      "v_mfma_f32_16x16x32_bf16 v[92:95], a[116:119], a[212:215], v[92:95]\n"
      "s_add_u32 m0, s37, 86536\n"
      "v_cvt_pk_bf16_f32 v20, v20, v24\n"
      "v_cvt_pk_bf16_f32 v21, v21, v25\n"
      "v_cvt_pk_bf16_f32 v22, v22, v26\n"
      "v_mfma_f32_16x16x32_bf16 v[96:99], a[108:111], a[212:215], v[96:99]\n"
      "s_nop 0\n"
      "buffer_load_dwordx4 v8, s[4:7], s53 offen offset:0 nt lds\n"
      "buffer_load_dwordx4 v9, s[4:7], s53 offen offset:1024 nt lds\n"
      "ds_read_b64_tr_b16 a[228:229], v7 offset:19968\n"
      "ds_read_b64_tr_b16 a[230:231], v7 offset:19976\n"
      "v_mfma_f32_16x16x32_bf16 v[52:55], a[0:3], a[192:195], 0\n"
      "s_mov_b32 m0, s64\n"
      "ds_read_b128 a[208:211], v4 offset:64\n"
      "ds_write_addtid_b32 v20 offset:18432\n"
      "ds_write_addtid_b32 v21 offset:22528\n"
      "v_mfma_f32_16x16x32_bf16 v[56:59], a[4:7], a[192:195], 0\n"
      "s_waitcnt lgkmcnt(5)\n"
      "ds_write_addtid_b32 v22 offset:26624\n"
      "v_add_f32 v76, v76, v88\n"
      "v_add_f32 v77, v77, v89\n"
      "v_mfma_f32_4x4x4_16b_bf16 v[28:31], a[216:217], a[224:225], 0\n"
      "v_add_f32 v78, v78, v90\n"
      "v_mfma_f32_4x4x4_16b_bf16 v[32:35], a[216:217], a[226:227], 0\n"
      "ds_write_addtid_b32 v76 offset:45312\n"
      "ds_write_addtid_b32 v77 offset:45824\n"
      "ds_write_addtid_b32 v78 offset:46336\n"
      "v_add_f32 v79, v79, v91\n"
      "v_mfma_f32_16x16x32_bf16 v[60:63], a[8:11], a[192:195], 0\n"
      "s_nop 0\n"
      "ds_write_addtid_b32 v79 offset:46848\n"
      "v_cvt_pk_bf16_f32 v23, v23, v27\n"
      "v_cvt_pk_bf16_f32 v28, v28, v32\n"
      "v_mfma_f32_16x16x32_bf16 v[52:55], a[12:15], a[196:199], v[52:55]\n"
      "s_add_u32 s53, s54, 3735552\n"
      "ds_read_b128 a[212:215], v4 offset:128\n"
      "ds_write_addtid_b32 v28 offset:18944\n"
      "ds_write_addtid_b32 v23 offset:30720\n"
      "v_mfma_f32_16x16x32_bf16 v[56:59], a[16:19], a[196:199], v[56:59]\n"
      "s_add_u32 m0, s37, 90760\n"
      "ds_read_b64_tr_b16 a[232:233], v7 offset:20480\n"
      "ds_read_b64_tr_b16 a[234:235], v7 offset:20488\n"
      "v_add_f32_e64 v80, v80, v92\n"
      "v_mfma_f32_16x16x32_bf16 v[60:63], a[20:23], a[196:199], v[60:63]\n"
      "s_waitcnt lgkmcnt(6)\n"
      "buffer_load_dwordx4 v8, s[4:7], s53 offen offset:0 nt lds\n"
      "buffer_load_dwordx4 v9, s[4:7], s53 offen offset:1024 nt lds\n"
      "v_mfma_f32_16x16x32_bf16 v[52:55], a[24:27], a[200:203], v[52:55]\n"
      "s_mov_b32 m0, s64\n"
      "ds_read_b128 a[192:195], v5 offset:0\n"
      "ds_write_addtid_b32 v80 offset:47360\n"
      "v_add_f32 v81, v81, v93\n"
      "v_mfma_f32_4x4x4_16b_bf16 v[20:23], a[216:217], a[228:229], 0\n"
      "v_cvt_pk_bf16_f32 v29, v29, v33\n"
      "v_add_f32 v82, v82, v94\n"
      "v_mfma_f32_4x4x4_16b_bf16 v[24:27], a[216:217], a[230:231], 0\n"
      "ds_write_addtid_b32 v29 offset:23040\n"
      "ds_write_addtid_b32 v81 offset:47872\n"
      "ds_write_addtid_b32 v82 offset:48384\n"
      "v_mfma_f32_16x16x32_bf16 v[56:59], a[28:31], a[200:203], v[56:59]\n"
      "v_add_f32 v83, v83, v95\n"
      "v_cvt_pk_bf16_f32 v30, v30, v34\n"
      "v_add_f32 v84, v84, v96\n"
      "v_mfma_f32_16x16x32_bf16 v[60:63], a[32:35], a[200:203], v[60:63]\n"
      "ds_write_addtid_b32 v30 offset:27136\n"
      "ds_write_addtid_b32 v83 offset:48896\n"
      "ds_write_addtid_b32 v84 offset:49408\n"
      "v_mfma_f32_16x16x32_bf16 v[52:55], a[36:39], a[204:207], v[52:55]\n"
      "ds_read_b128 a[196:199], v5 offset:64\n"
      "v_cvt_pk_bf16_f32 v31, v31, v35\n"
      "v_cvt_pk_bf16_f32 v20, v20, v24\n"
      "v_mfma_f32_16x16x32_bf16 v[56:59], a[40:43], a[204:207], v[56:59]\n"
      "ds_write_addtid_b32 v20 offset:19456\n"
      "ds_write_addtid_b32 v31 offset:31232\n"
      "v_add_f32_e64 v85, v85, v97\n"
      "v_mfma_f32_16x16x32_bf16 v[60:63], a[44:47], a[204:207], v[60:63]\n"
      "s_waitcnt lgkmcnt(6)\n"
      "ds_write_addtid_b32 v85 offset:49920\n"
      "v_add_f32 v86, v86, v98\n"
      "v_add_f32_e64 v87, v87, v99\n"
      "v_mfma_f32_4x4x4_16b_bf16 v[28:31], a[216:217], a[232:233], 0\n"
      "v_cvt_pk_bf16_f32 v21, v21, v25\n"
      "v_cvt_pk_bf16_f32 v22, v22, v26\n"
      "v_mfma_f32_4x4x4_16b_bf16 v[32:35], a[216:217], a[234:235], 0\n"
      "ds_write_addtid_b32 v21 offset:23552\n"
      "ds_write_addtid_b32 v86 offset:50432\n"
      "ds_write_addtid_b32 v22 offset:27648\n"
      "v_mfma_f32_16x16x32_bf16 v[52:55], a[48:51], a[208:211], v[52:55]\n"
      "ds_read_b128 a[200:203], v5 offset:128\n"
      "ds_write_addtid_b32 v87 offset:50944\n"
      "v_cvt_pk_bf16_f32 v28, v28, v32\n"
      "v_mfma_f32_16x16x32_bf16 v[56:59], a[52:55], a[208:211], v[56:59]\n"
      "s_nop 0\n"
      "ds_write_addtid_b32 v28 offset:19968\n"
      "v_cvt_pk_bf16_f32 v29, v29, v33\n"
      "v_cvt_pk_bf16_f32 v30, v30, v34\n"
      "v_cvt_pk_bf16_f32 v31, v31, v35\n"
      "v_cvt_pk_bf16_f32 v23, v23, v27\n"
      "v_mfma_f32_16x16x32_bf16 v[60:63], a[56:59], a[208:211], v[60:63]\n"
      "s_nop 0\n"
      "ds_write_addtid_b32 v29 offset:24064\n"
      "ds_write_addtid_b32 v23 offset:31744\n"
      "v_mfma_f32_16x16x32_bf16 v[52:55], a[60:63], a[212:215], v[52:55]\n"
      "s_nop 0\n"
      "ds_write_addtid_b32 v30 offset:28160\n"
      "ds_write_addtid_b32 v31 offset:32256\n"
      "v_fma_f32 v40, -2.0, v88, v76\n"
      "v_mfma_f32_16x16x32_bf16 v[56:59], a[64:67], a[212:215], v[56:59]\n"
      "s_waitcnt vmcnt(19)\n"
      "v_fma_f32 v41, -2.0, v89, v77\n"
      "v_fma_f32 v42, -2.0, v90, v78\n"
      "v_fma_f32 v43, -2.0, v91, v79\n"
      "v_mfma_f32_16x16x32_bf16 v[60:63], a[68:71], a[212:215], v[60:63]\n"
      "s_waitcnt lgkmcnt(0)\n"
      "v_fma_f32 v44, -2.0, v92, v80\n"
      "v_fma_f32 v45, -2.0, v93, v81\n"
      "s_barrier\n"
      "s_add_u32 s54, s54, 3735552\n"
      "LDloop_%=:\n"
      "v_mfma_f32_16x16x32_bf16 v[52:55], a[72:75], a[192:195], v[52:55]\n"
      "s_add_u32 s58, s40, 0\n"
      "ds_read_b128 a[204:207], v0 offset:16384\n"
      "ds_read2st64_b32 a[220:221], v10 offset0:181 offset1:183\n"
      "ds_read2st64_b32 a[222:223], v13 offset0:181 offset1:183\n"
      "ds_read2st64_b32 a[224:225], v11 offset0:181 offset1:183\n"
      "v_mfma_f32_16x16x32_bf16 v[56:59], a[76:79], a[192:195], v[56:59]\n"
      "s_mov_b32 s40, s62\n"
      "ds_read_b128 a[208:211], v0 offset:20480\n"
      "v_fma_f32 v46, -2.0, v94, v82\n"
      "v_mfma_f32_16x16x32_bf16 v[60:63], a[80:83], a[192:195], v[60:63]\n"
      "s_add_u32 s62, s62, 786432\n"
      "ds_read2st64_b32 v[28:29], v6 offset0:0 offset1:2\n"
      "ds_read2st64_b32 a[226:227], v14 offset0:181 offset1:183\n"
      "v_mfma_f32_16x16x32_bf16 v[52:55], a[84:87], a[196:199], v[52:55]\n"
      "s_add_u32 s59, s58, 196608\n"
      "ds_read_b128 a[212:215], v0 offset:16448\n"
      "v_fma_f32 v47, -2.0, v95, v83\n"
      "v_fma_f32 v48, -2.0, v96, v84\n"
      "v_mfma_f32_16x16x32_bf16 v[56:59], a[88:91], a[196:199], v[56:59]\n"
      "s_nop 0\n"
      "s_waitcnt lgkmcnt(5)\n"
      "ds_read2st64_b32 v[30:31], v6 offset0:4 offset1:6\n"
      "v_fma_f32 v49, -2.0, v97, v85\n"
      "v_mfma_f32_16x16x32_bf16 v[60:63], a[92:95], a[196:199], v[60:63]\n"
      "s_add_u32 s54, s54, 196608\n"
      "v_fma_f32 v50, -2.0, v98, v86\n"
      "v_fma_f32 v51, -2.0, v99, v87\n"
      "v_mfma_f32_16x16x32_bf16 v[52:55], a[96:99], a[200:203], v[52:55]\n"
      "s_add_u32 m0, s37, 95104\n"
      "buffer_store_dwordx4 a[220:223], v16, s[8:11], s58 offen offset:0 sc1\n"
      "ds_read_b128 a[192:195], v0 offset:20544\n"
      "v_mfma_f32_16x16x32_bf16 v[56:59], a[100:103], a[200:203], v[56:59]\n"
      "buffer_load_dwordx4 v8, s[4:7], s54 offen offset:0 nt lds\n"
      "buffer_load_dwordx4 v9, s[4:7], s54 offen offset:1024 nt lds\n"
      "ds_read2st64_b32 v[32:33], v6 offset0:8 offset1:10\n"
      "v_mfma_f32_16x16x32_bf16 v[60:63], a[104:107], a[200:203], v[60:63]\n"
      "s_mov_b32 m0, s64\n"
      "ds_read2st64_b32 v[34:35], v6 offset0:12 offset1:14\n"
      "ds_read2st64_b32 v[36:37], v6 offset0:16 offset1:18\n"
      "ds_read2st64_b32 v[38:39], v6 offset0:20 offset1:22\n"
      "v_mfma_f32_16x16x32_bf16 v[76:79], v[252:255], a[204:207], 0\n"
      "s_nop 0\n"
      "ds_read_b128 a[196:199], v0 offset:16512\n"
      "v_mfma_f32_16x16x32_bf16 v[80:83], v[244:247], a[204:207], 0\n"
      "ds_read_b64_tr_b16 a[244:245], v7 offset:36864\n"
      "ds_read2st64_b32 a[228:229], v12 offset0:181 offset1:183\n"
      "ds_read_b64_tr_b16 a[246:247], v7 offset:36872\n"
      "v_mfma_f32_16x16x32_bf16 v[84:87], v[236:239], a[204:207], 0\n"
      "s_waitcnt lgkmcnt(9)\n"
      "ds_read2st64_b32 a[230:231], v15 offset0:181 offset1:183\n"
      "v_fmac_f32 v40, s46, v28\n"
      "v_fmac_f32_e64 v41, s46, v29\n"
      "v_mfma_f32_16x16x32_bf16 v[88:91], v[248:251], a[208:211], 0\n"
      "ds_read_b128 a[200:203], v0 offset:20608\n"
      "v_fmac_f32 v42, s46, v30\n"
      "v_mfma_f32_16x16x32_bf16 v[92:95], v[240:243], a[208:211], 0\n"
      "buffer_store_dwordx4 a[224:227], v16, s[8:11], s58 offen offset:128 sc1\n"
      "v_mfma_f32_16x16x32_bf16 v[96:99], v[232:235], a[208:211], 0\n"
      "v_fmac_f32 v43, s46, v31\n"
      "v_fma_f32 v28, 0.5, v28, v64\n"
      "v_fma_f32 v29, 0.5, v29, v65\n"
      "v_mfma_f32_16x16x32_bf16 v[76:79], v[228:231], a[212:215], v[76:79]\n"
      "ds_read_b128 a[204:207], v1 offset:16384\n"
      "v_fma_f32 v30, 0.5, v30, v66\n"
      "v_mfma_f32_16x16x32_bf16 v[80:83], v[220:223], a[212:215], v[80:83]\n"
      "v_fma_f32 v31, 0.5, v31, v67\n"
      "v_fmac_f32 v40, s48, v52\n"
      "v_fmac_f32 v41, s48, v53\n"
      "v_mfma_f32_16x16x32_bf16 v[84:87], v[212:215], a[212:215], v[84:87]\n"
      "s_waitcnt lgkmcnt(1)\n"
      "v_fmac_f32 v42, s48, v54\n"
      "v_fmac_f32 v43, s48, v55\n"
      "v_mfma_f32_4x4x4_16b_bf16 v[20:23], a[216:217], a[244:245], 0\n"
      "v_fmac_f32 v28, s45, v40\n"
      "v_mfma_f32_4x4x4_16b_bf16 v[24:27], a[216:217], a[246:247], 0\n"
      "v_fmac_f32 v29, s45, v41\n"
      "v_fmac_f32 v30, s45, v42\n"
      "v_mfma_f32_16x16x32_bf16 v[88:91], v[224:227], a[192:195], v[88:91]\n"
      "ds_read_b128 a[208:211], v1 offset:20480\n"
      "v_fmac_f32_e64 v31, s45, v43\n"
      "v_cvt_pk_bf16_f32 v20, v20, v24\n"
      "v_mfma_f32_16x16x32_bf16 v[92:95], v[216:219], a[192:195], v[92:95]\n"
      "ds_write_addtid_b32 v20 offset:3072\n"
      "v_fma_f32 v40, s47, v40, -v52\n"
      "v_mfma_f32_16x16x32_bf16 v[96:99], v[208:211], a[192:195], v[96:99]\n"
      "s_add_u32 s54, s54, 196608\n"
      "v_cvt_pk_bf16_f32 v21, v21, v25\n"
      "v_fma_f32 v41, s47, v41, -v53\n"
      "v_mfma_f32_16x16x32_bf16 v[76:79], v[204:207], a[196:199], v[76:79]\n"
      "s_add_u32 m0, s37, 99328\n"
      "ds_read_b128 a[212:215], v1 offset:16448\n"
      "v_fma_f32 v42, s47, v42, -v54\n"
      "v_mfma_f32_16x16x32_bf16 v[80:83], v[196:199], a[196:199], v[80:83]\n"
      "s_nop 0\n"
      "buffer_load_dwordx4 v8, s[4:7], s54 offen offset:0 nt lds\n"
      "buffer_load_dwordx4 v9, s[4:7], s54 offen offset:1024 nt lds\n"
      "v_mfma_f32_16x16x32_bf16 v[84:87], v[188:191], a[196:199], v[84:87]\n"
      "s_mov_b32 m0, s64\n"
      "v_fma_f32 v43, s47, v43, -v55\n"
      "v_cvt_pk_bf16_f32 v22, v22, v26\n"
      "v_mfma_f32_16x16x32_bf16 v[88:91], v[200:203], a[200:203], v[88:91]\n"
      "buffer_store_dwordx4 a[228:231], v16, s[8:11], s58 offen offset:256 sc1\n"
      "ds_read_b128 a[192:195], v1 offset:20544\n"
      "v_mfma_f32_16x16x32_bf16 v[92:95], v[192:195], a[200:203], v[92:95]\n"
      "ds_write_addtid_b32 v21 offset:7168\n"
      "ds_read2st64_b32 a[232:233], v10 offset0:4 offset1:6\n"
      "ds_write_addtid_b32 v22 offset:11264\n"
      "v_cvt_pk_bf16_f32 v28, v28, v29\n"
      "v_mfma_f32_16x16x32_bf16 v[96:99], v[184:187], a[200:203], v[96:99]\n"
      "s_waitcnt lgkmcnt(4)\n"
      "ds_write_addtid_b32 v28 offset:32768\n"
      "ds_read2st64_b32 a[234:235], v13 offset0:4 offset1:6\n"
      "v_cvt_pk_bf16_f32 v30, v30, v31\n"
      "v_mfma_f32_16x16x32_bf16 v[76:79], v[180:183], a[204:207], v[76:79]\n"
      "s_nop 0\n"
      "ds_read_b128 a[196:199], v1 offset:16512\n"
      "ds_write_addtid_b32 v30 offset:33280\n"
      "v_mfma_f32_16x16x32_bf16 v[80:83], v[172:175], a[204:207], v[80:83]\n"
      "v_cvt_pk_bf16_f32 v40, v40, v41\n"
      "v_cvt_pk_bf16_f32 v23, v23, v27\n"
      "v_cvt_pk_bf16_f32 v42, v42, v43\n"
      "v_mfma_f32_16x16x32_bf16 v[84:87], v[164:167], a[204:207], v[84:87]\n"
      "ds_write_addtid_b32 v40 offset:34816\n"
      "ds_write_addtid_b32 v42 offset:35328\n"
      "ds_write_addtid_b32 v23 offset:15360\n"
      "v_mfma_f32_16x16x32_bf16 v[88:91], v[176:179], a[208:211], v[88:91]\n"
      "ds_read_b128 a[200:203], v1 offset:20608\n"
      "v_fmac_f32 v44, s46, v32\n"
      "v_mfma_f32_16x16x32_bf16 v[92:95], v[168:171], a[208:211], v[92:95]\n"
      "v_fmac_f32 v45, s46, v33\n"
      "v_fmac_f32 v46, s46, v34\n"
      "v_mfma_f32_16x16x32_bf16 v[96:99], v[160:163], a[208:211], v[96:99]\n"
      "v_fmac_f32 v47, s46, v35\n"
      "v_fma_f32 v32, 0.5, v32, v68\n"
      "v_mfma_f32_16x16x32_bf16 v[76:79], v[156:159], a[212:215], v[76:79]\n"
      "ds_read_b128 a[204:207], v2 offset:16384\n"
      "v_fma_f32 v33, 0.5, v33, v69\n"
      "v_mfma_f32_16x16x32_bf16 v[80:83], v[148:151], a[212:215], v[80:83]\n"
      "v_fma_f32 v34, 0.5, v34, v70\n"
      "v_fma_f32 v35, 0.5, v35, v71\n"
      "v_mfma_f32_16x16x32_bf16 v[84:87], v[140:143], a[212:215], v[84:87]\n"
      "s_waitcnt lgkmcnt(1)\n"
      "v_fmac_f32 v44, s48, v56\n"
      "v_fmac_f32 v45, s48, v57\n"
      "v_fmac_f32 v46, s48, v58\n"
      "v_mfma_f32_16x16x32_bf16 v[88:91], v[152:155], a[192:195], v[88:91]\n"
      "ds_read_b128 a[208:211], v2 offset:20480\n"
      "v_fmac_f32 v47, s48, v59\n"
      "v_mfma_f32_16x16x32_bf16 v[92:95], v[144:147], a[192:195], v[92:95]\n"
      "v_fmac_f32 v32, s45, v44\n"
      "v_fmac_f32 v33, s45, v45\n"
      "v_mfma_f32_16x16x32_bf16 v[96:99], v[136:139], a[192:195], v[96:99]\n"
      "s_nop 0\n"
      "buffer_store_dwordx4 a[232:235], v16, s[8:11], s59 offen offset:0 sc1\n"
      "ds_read_b64_tr_b16 a[248:249], v7 offset:37376\n"
      "v_mfma_f32_16x16x32_bf16 v[76:79], v[132:135], a[196:199], v[76:79]\n"
      "ds_read_b128 a[212:215], v2 offset:16448\n"
      "ds_read_b64_tr_b16 a[250:251], v7 offset:37384\n"
      "v_fmac_f32 v34, s45, v46\n"
      "v_mfma_f32_16x16x32_bf16 v[80:83], v[124:127], a[196:199], v[80:83]\n"
      "v_fmac_f32 v35, s45, v47\n"
      "v_fma_f32 v44, s47, v44, -v56\n"
      "v_mfma_f32_16x16x32_bf16 v[84:87], v[116:119], a[196:199], v[84:87]\n"
      "v_fma_f32 v45, s47, v45, -v57\n"
      "v_fma_f32 v46, s47, v46, -v58\n"
      "v_mfma_f32_16x16x32_bf16 v[88:91], v[128:131], a[200:203], v[88:91]\n"
      "ds_read_b128 a[192:195], v2 offset:20544\n"
      "v_fma_f32 v47, s47, v47, -v59\n"
      "v_mfma_f32_16x16x32_bf16 v[92:95], v[120:123], a[200:203], v[92:95]\n"
      "v_cvt_pk_bf16_f32 v32, v32, v33\n"
      "v_cvt_pk_bf16_f32 v34, v34, v35\n"
      "v_mfma_f32_16x16x32_bf16 v[96:99], v[112:115], a[200:203], v[96:99]\n"
      "s_waitcnt lgkmcnt(1)\n"
      "ds_write_addtid_b32 v32 offset:40964\n"
      "ds_write_addtid_b32 v34 offset:41476\n"
      "v_cvt_pk_bf16_f32 v44, v44, v45\n"
      "v_mfma_f32_16x16x32_bf16 v[76:79], v[108:111], a[204:207], v[76:79]\n"
      "s_nop 0\n"
      "ds_read_b128 a[196:199], v2 offset:16512\n"
      "ds_write_addtid_b32 v44 offset:43012\n"
      "v_cvt_pk_bf16_f32 v46, v46, v47\n"
      "v_mfma_f32_4x4x4_16b_bf16 v[20:23], a[216:217], a[248:249], 0\n"
      "v_fmac_f32 v48, s46, v36\n"
      "v_mfma_f32_4x4x4_16b_bf16 v[24:27], a[216:217], a[250:251], 0\n"
      "ds_write_addtid_b32 v46 offset:43524\n"
      "v_fmac_f32 v49, s46, v37\n"
      "v_fmac_f32 v50, s46, v38\n"
      "v_mfma_f32_16x16x32_bf16 v[80:83], v[100:103], a[204:207], v[80:83]\n"
      "v_fmac_f32 v51, s46, v39\n"
      "v_cvt_pk_bf16_f32 v20, v20, v24\n"
      "v_fma_f32 v36, 0.5, v36, v72\n"
      "v_mfma_f32_16x16x32_bf16 v[84:87], a[160:163], a[204:207], v[84:87]\n"
      "s_add_u32 s54, s54, 196608\n"
      "ds_write_addtid_b32 v20 offset:3584\n"
      "v_fma_f32 v37, 0.5, v37, v73\n"
      "v_mfma_f32_16x16x32_bf16 v[88:91], v[104:107], a[208:211], v[88:91]\n"
      "s_add_u32 m0, s37, 103432\n"
      "ds_read_b128 a[200:203], v2 offset:20608\n"
      "ds_read2st64_b32 a[236:237], v11 offset0:4 offset1:6\n"
      "ds_read2st64_b32 a[238:239], v14 offset0:4 offset1:6\n"
      "v_mfma_f32_16x16x32_bf16 v[92:95], a[164:167], a[208:211], v[92:95]\n"
      "s_nop 0\n"
      "buffer_load_dwordx4 v8, s[4:7], s54 offen offset:0 nt lds\n"
      "buffer_load_dwordx4 v9, s[4:7], s54 offen offset:1024 nt lds\n"
      "v_mfma_f32_16x16x32_bf16 v[96:99], a[156:159], a[208:211], v[96:99]\n"
      "s_mov_b32 m0, s64\n"
      "v_fma_f32 v38, 0.5, v38, v74\n"
      "v_fma_f32 v39, 0.5, v39, v75\n"
      "v_cvt_pk_bf16_f32 v21, v21, v25\n"
      "v_mfma_f32_16x16x32_bf16 v[76:79], a[152:155], a[212:215], v[76:79]\n"
      "ds_read_b128 a[204:207], v3 offset:16896\n"
      "ds_write_addtid_b32 v21 offset:7680\n"
      "v_mfma_f32_16x16x32_bf16 v[80:83], a[144:147], a[212:215], v[80:83]\n"
      "v_fmac_f32 v48, s48, v60\n"
      "v_fmac_f32 v49, s48, v61\n"
      "v_cvt_pk_bf16_f32 v22, v22, v26\n"
      "v_mfma_f32_16x16x32_bf16 v[84:87], a[136:139], a[212:215], v[84:87]\n"
      "s_waitcnt lgkmcnt(2)\n"
      "ds_write_addtid_b32 v22 offset:11776\n"
      "v_fmac_f32 v50, s48, v62\n"
      "v_mfma_f32_16x16x32_bf16 v[88:91], a[148:151], a[192:195], v[88:91]\n"
      "ds_read_b128 a[208:211], v3 offset:16960\n"
      "v_fmac_f32 v51, s48, v63\n"
      "v_mfma_f32_16x16x32_bf16 v[92:95], a[140:143], a[192:195], v[92:95]\n"
      "v_fmac_f32 v36, s45, v48\n"
      "v_cvt_pk_bf16_f32 v23, v23, v27\n"
      "v_mfma_f32_16x16x32_bf16 v[96:99], a[132:135], a[192:195], v[96:99]\n"
      "ds_write_addtid_b32 v23 offset:15872\n"
      "ds_read2st64_b32 a[240:241], v12 offset0:4 offset1:6\n"
      "ds_read2st64_b32 a[242:243], v15 offset0:4 offset1:6\n"
      "v_mfma_f32_16x16x32_bf16 v[76:79], a[128:131], a[196:199], v[76:79]\n"
      "ds_read_b128 a[212:215], v3 offset:17024\n"
      "v_fmac_f32 v37, s45, v49\n"
      "v_fmac_f32 v38, s45, v50\n"
      "v_mfma_f32_16x16x32_bf16 v[80:83], a[120:123], a[196:199], v[80:83]\n"
      "s_nop 0\n"
      "v_fmac_f32_e64 v39, s45, v51\n"
      "v_fma_f32 v48, s47, v48, -v60\n"
      "v_mfma_f32_16x16x32_bf16 v[84:87], a[112:115], a[196:199], v[84:87]\n"
      "s_nop 0\n"
      "v_fma_f32 v49, s47, v49, -v61\n"
      "v_fma_f32 v50, s47, v50, -v62\n"
      "v_mfma_f32_16x16x32_bf16 v[88:91], a[124:127], a[200:203], v[88:91]\n"
      "s_add_u32 s54, s54, 196608\n"
      "ds_read_b128 a[192:195], v4 offset:16896\n"
      "v_fma_f32 v51, s47, v51, -v63\n"
      "v_mfma_f32_16x16x32_bf16 v[92:95], a[116:119], a[200:203], v[92:95]\n"
      "s_add_u32 m0, s37, 107656\n"
      "v_cvt_pk_bf16_f32 v36, v36, v37\n"
      "v_cvt_pk_bf16_f32 v38, v38, v39\n"
      "v_mfma_f32_16x16x32_bf16 v[96:99], a[108:111], a[200:203], v[96:99]\n"
      "s_waitcnt lgkmcnt(1)\n"
      "buffer_store_dwordx4 a[236:239], v16, s[8:11], s59 offen offset:128 sc1\n"
      "buffer_load_dwordx4 v8, s[4:7], s54 offen offset:0 nt lds\n"
      "buffer_load_dwordx4 v9, s[4:7], s54 offen offset:1024 nt lds\n"
      "v_mfma_f32_16x16x32_bf16 v[64:67], a[0:3], a[204:207], 0\n"
      "s_mov_b32 m0, s64\n"
      "ds_read_b128 a[196:199], v4 offset:16960\n"
      "ds_write_addtid_b32 v36 offset:36864\n"
      "ds_write_addtid_b32 v38 offset:37376\n"
      "v_add_f32 v76, v76, v88\n"
      "v_mfma_f32_16x16x32_bf16 v[68:71], a[4:7], a[204:207], 0\n"
      "ds_write_addtid_b32 v76 offset:51456\n"
      "v_add_f32 v77, v77, v89\n"
      "v_mfma_f32_16x16x32_bf16 v[72:75], a[8:11], a[204:207], 0\n"
      "ds_write_addtid_b32 v77 offset:51968\n"
      "v_add_f32 v78, v78, v90\n"
      "v_add_f32 v79, v79, v91\n"
      "v_mfma_f32_16x16x32_bf16 v[64:67], a[12:15], a[208:211], v[64:67]\n"
      "ds_read_b128 a[200:203], v4 offset:17024\n"
      "ds_write_addtid_b32 v78 offset:52480\n"
      "v_mfma_f32_16x16x32_bf16 v[68:71], a[16:19], a[208:211], v[68:71]\n"
      "ds_write_addtid_b32 v79 offset:52992\n"
      "v_add_f32 v80, v80, v92\n"
      "v_cvt_pk_bf16_f32 v48, v48, v49\n"
      "v_mfma_f32_16x16x32_bf16 v[72:75], a[20:23], a[208:211], v[72:75]\n"
      "s_nop 0\n"
      "ds_write_addtid_b32 v48 offset:38912\n"
      "ds_write_addtid_b32 v80 offset:53504\n"
      "v_mfma_f32_16x16x32_bf16 v[64:67], a[24:27], a[212:215], v[64:67]\n"
      "ds_read_b128 a[204:207], v5 offset:16896\n"
      "v_add_f32 v81, v81, v93\n"
      "v_mfma_f32_16x16x32_bf16 v[68:71], a[28:31], a[212:215], v[68:71]\n"
      "buffer_store_dwordx4 a[240:243], v16, s[8:11], s59 offen offset:256 sc1\n"
      "ds_write_addtid_b32 v81 offset:54016\n"
      "v_mfma_f32_16x16x32_bf16 v[72:75], a[32:35], a[212:215], v[72:75]\n"
      "s_waitcnt lgkmcnt(4)\n"
      "v_add_f32 v82, v82, v94\n"
      "v_add_f32 v83, v83, v95\n"
      "v_mfma_f32_16x16x32_bf16 v[64:67], a[36:39], a[192:195], v[64:67]\n"
      "ds_read_b128 a[208:211], v5 offset:16960\n"
      "ds_write_addtid_b32 v82 offset:54528\n"
      "ds_write_addtid_b32 v83 offset:55040\n"
      "v_cvt_pk_bf16_f32 v50, v50, v51\n"
      "v_mfma_f32_16x16x32_bf16 v[68:71], a[40:43], a[192:195], v[68:71]\n"
      "ds_write_addtid_b32 v50 offset:39424\n"
      "v_add_f32_e64 v84, v84, v96\n"
      "v_mfma_f32_16x16x32_bf16 v[72:75], a[44:47], a[192:195], v[72:75]\n"
      "ds_write_addtid_b32 v84 offset:55552\n"
      "v_add_f32 v85, v85, v97\n"
      "v_add_f32_e64 v86, v86, v98\n"
      "v_add_f32_e64 v87, v87, v99\n"
      "v_mfma_f32_16x16x32_bf16 v[64:67], a[48:51], a[196:199], v[64:67]\n"
      "s_nop 0\n"
      "ds_read_b128 a[212:215], v5 offset:17024\n"
      "ds_write_addtid_b32 v85 offset:56064\n"
      "v_mfma_f32_16x16x32_bf16 v[68:71], a[52:55], a[196:199], v[68:71]\n"
      "s_nop 0\n"
      "ds_write_addtid_b32 v86 offset:56576\n"
      "ds_write_addtid_b32 v87 offset:57088\n"
      "v_mfma_f32_16x16x32_bf16 v[72:75], a[56:59], a[196:199], v[72:75]\n"
      "s_nop 0\n"
      "v_fma_f32 v40, -2.0, v88, v76\n"
      "v_fma_f32 v41, -2.0, v89, v77\n"
      "v_mfma_f32_16x16x32_bf16 v[64:67], a[60:63], a[200:203], v[64:67]\n"
      "s_nop 0\n"
      "v_fma_f32 v42, -2.0, v90, v78\n"
      "v_fma_f32 v43, -2.0, v91, v79\n"
      "v_mfma_f32_16x16x32_bf16 v[68:71], a[64:67], a[200:203], v[68:71]\n"
      "s_waitcnt vmcnt(25)\n"
      "v_fma_f32 v44, -2.0, v92, v80\n"
      "v_fma_f32 v45, -2.0, v93, v81\n"
      "v_mfma_f32_16x16x32_bf16 v[72:75], a[68:71], a[200:203], v[72:75]\n"
      "s_waitcnt lgkmcnt(0)\n"
      "s_barrier\n"
      "LDmid_%=:\n"
      "v_mfma_f32_16x16x32_bf16 v[64:67], a[72:75], a[204:207], v[64:67]\n"
      "s_add_u32 s58, s40, 0\n"
      "ds_read_b128 a[192:195], v0 offset:0\n"
      "ds_read2st64_b32 a[220:221], v10 offset0:177 offset1:179\n"
      "ds_read2st64_b32 a[222:223], v13 offset0:177 offset1:179\n"
      "ds_read2st64_b32 a[224:225], v11 offset0:177 offset1:179\n"
      "v_mfma_f32_16x16x32_bf16 v[68:71], a[76:79], a[204:207], v[68:71]\n"
      "s_mov_b32 s40, s62\n"
      "ds_read_b128 a[196:199], v0 offset:4096\n"
      "v_fma_f32 v46, -2.0, v94, v82\n"
      "v_mfma_f32_16x16x32_bf16 v[72:75], a[80:83], a[204:207], v[72:75]\n"
      "s_add_u32 s62, s62, 786432\n"
      "ds_read2st64_b32 v[28:29], v6 offset0:24 offset1:26\n"
      "ds_read2st64_b32 a[226:227], v14 offset0:177 offset1:179\n"
      "v_mfma_f32_16x16x32_bf16 v[64:67], a[84:87], a[208:211], v[64:67]\n"
      "s_add_u32 s59, s58, 196608\n"
      "ds_read_b128 a[200:203], v0 offset:64\n"
      "v_fma_f32 v47, -2.0, v95, v83\n"
      "v_fma_f32 v48, -2.0, v96, v84\n"
      "v_mfma_f32_16x16x32_bf16 v[68:71], a[88:91], a[208:211], v[68:71]\n"
      "s_nop 0\n"
      "s_waitcnt lgkmcnt(5)\n"
      "ds_read2st64_b32 v[30:31], v6 offset0:28 offset1:30\n"
      "v_fma_f32 v49, -2.0, v97, v85\n"
      "v_mfma_f32_16x16x32_bf16 v[72:75], a[92:95], a[208:211], v[72:75]\n"
      "s_add_u32 s54, s54, 196608\n"
      "v_fma_f32 v50, -2.0, v98, v86\n"
      "v_fma_f32 v51, -2.0, v99, v87\n"
      "v_mfma_f32_16x16x32_bf16 v[64:67], a[96:99], a[212:215], v[64:67]\n"
      "s_add_u32 m0, s37, 112000\n"
      "buffer_store_dwordx4 a[220:223], v16, s[8:11], s58 offen offset:0 sc1\n"
      "ds_read_b128 a[204:207], v0 offset:4160\n"
      "v_mfma_f32_16x16x32_bf16 v[68:71], a[100:103], a[212:215], v[68:71]\n"
      "buffer_load_dwordx4 v8, s[4:7], s54 offen offset:0 nt lds\n"
      "buffer_load_dwordx4 v9, s[4:7], s54 offen offset:1024 nt lds\n"
      "ds_read2st64_b32 v[32:33], v6 offset0:32 offset1:34\n"
      "v_mfma_f32_16x16x32_bf16 v[72:75], a[104:107], a[212:215], v[72:75]\n"
      "s_mov_b32 m0, s64\n"
      "ds_read2st64_b32 v[34:35], v6 offset0:36 offset1:38\n"
      "ds_read2st64_b32 v[36:37], v6 offset0:40 offset1:42\n"
      "ds_read2st64_b32 v[38:39], v6 offset0:44 offset1:46\n"
      "v_mfma_f32_16x16x32_bf16 v[76:79], v[252:255], a[192:195], 0\n"
      "s_nop 0\n"
      "ds_read_b128 a[208:211], v0 offset:128\n"
      "v_mfma_f32_16x16x32_bf16 v[80:83], v[244:247], a[192:195], 0\n"
      "ds_read_b64_tr_b16 a[244:245], v7 offset:53760\n"
      "ds_read2st64_b32 a[228:229], v12 offset0:177 offset1:179\n"
      "ds_read_b64_tr_b16 a[246:247], v7 offset:53768\n"
      "v_mfma_f32_16x16x32_bf16 v[84:87], v[236:239], a[192:195], 0\n"
      "s_waitcnt lgkmcnt(9)\n"
      "ds_read2st64_b32 a[230:231], v15 offset0:177 offset1:179\n"
      "v_fmac_f32 v40, s46, v28\n"
      "v_fmac_f32 v41, s46, v29\n"
      "v_mfma_f32_16x16x32_bf16 v[88:91], v[248:251], a[196:199], 0\n"
      "ds_read_b128 a[212:215], v0 offset:4224\n"
      "v_fmac_f32 v42, s46, v30\n"
      "v_mfma_f32_16x16x32_bf16 v[92:95], v[240:243], a[196:199], 0\n"
      "buffer_store_dwordx4 a[224:227], v16, s[8:11], s58 offen offset:128 sc1\n"
      "v_mfma_f32_16x16x32_bf16 v[96:99], v[232:235], a[196:199], 0\n"
      "v_fmac_f32_e64 v43, s46, v31\n"
      "v_fma_f32 v28, 0.5, v28, v52\n"
      "v_fma_f32 v29, 0.5, v29, v53\n"
      "v_mfma_f32_16x16x32_bf16 v[76:79], v[228:231], a[200:203], v[76:79]\n"
      "ds_read_b128 a[192:195], v1 offset:0\n"
      "v_fma_f32 v30, 0.5, v30, v54\n"
      "v_mfma_f32_16x16x32_bf16 v[80:83], v[220:223], a[200:203], v[80:83]\n"
      "v_fma_f32 v31, 0.5, v31, v55\n"
      "v_fmac_f32 v40, s48, v64\n"
      "v_fmac_f32 v41, s48, v65\n"
      "v_mfma_f32_16x16x32_bf16 v[84:87], v[212:215], a[200:203], v[84:87]\n"
      "s_waitcnt lgkmcnt(1)\n"
      "v_fmac_f32 v42, s48, v66\n"
      "v_fmac_f32 v43, s48, v67\n"
      "v_mfma_f32_4x4x4_16b_bf16 v[20:23], a[216:217], a[244:245], 0\n"
      "v_fmac_f32 v28, s45, v40\n"
      "v_mfma_f32_4x4x4_16b_bf16 v[24:27], a[216:217], a[246:247], 0\n"
      "v_fmac_f32 v29, s45, v41\n"
      "v_fmac_f32_e64 v30, s45, v42\n"
      "v_mfma_f32_16x16x32_bf16 v[88:91], v[224:227], a[204:207], v[88:91]\n"
      "s_nop 0\n"
      "ds_read_b128 a[196:199], v1 offset:4096\n"
      "v_fmac_f32 v31, s45, v43\n"
      "v_cvt_pk_bf16_f32 v20, v20, v24\n"
      "v_mfma_f32_16x16x32_bf16 v[92:95], v[216:219], a[204:207], v[92:95]\n"
      "s_nop 0\n"
      "ds_write_addtid_b32 v20 offset:19456\n"
      "v_fma_f32 v40, s47, v40, -v64\n"
      "v_mfma_f32_16x16x32_bf16 v[96:99], v[208:211], a[204:207], v[96:99]\n"
      "s_add_u32 s54, s54, 196608\n"
      "v_cvt_pk_bf16_f32 v21, v21, v25\n"
      "v_fma_f32 v41, s47, v41, -v65\n"
      "v_mfma_f32_16x16x32_bf16 v[76:79], v[204:207], a[208:211], v[76:79]\n"
      "s_add_u32 m0, s37, 116224\n"
      "ds_read_b128 a[200:203], v1 offset:64\n"
      "v_fma_f32 v42, s47, v42, -v66\n"
      "v_mfma_f32_16x16x32_bf16 v[80:83], v[196:199], a[208:211], v[80:83]\n"
      "buffer_load_dwordx4 v8, s[4:7], s54 offen offset:0 nt lds\n"
      "buffer_load_dwordx4 v9, s[4:7], s54 offen offset:1024 nt lds\n"
      "v_mfma_f32_16x16x32_bf16 v[84:87], v[188:191], a[208:211], v[84:87]\n"
      "s_mov_b32 m0, s64\n"
      "v_fma_f32 v43, s47, v43, -v67\n"
      "v_cvt_pk_bf16_f32 v22, v22, v26\n"
      "v_mfma_f32_16x16x32_bf16 v[88:91], v[200:203], a[212:215], v[88:91]\n"
      "s_nop 0\n"
      "buffer_store_dwordx4 a[228:231], v16, s[8:11], s58 offen offset:256 sc1\n"
      "ds_read_b128 a[204:207], v1 offset:4160\n"
      "v_mfma_f32_16x16x32_bf16 v[92:95], v[192:195], a[212:215], v[92:95]\n"
      "s_nop 0\n"
      "ds_write_addtid_b32 v21 offset:23552\n"
      "ds_read2st64_b32 a[232:233], v10 offset0:0 offset1:2\n"
      "ds_write_addtid_b32 v22 offset:27648\n"
      "v_cvt_pk_bf16_f32 v28, v28, v29\n"
      "v_mfma_f32_16x16x32_bf16 v[96:99], v[184:187], a[212:215], v[96:99]\n"
      "s_waitcnt lgkmcnt(4)\n"
      "ds_write_addtid_b32 v28 offset:33792\n"
      "ds_read2st64_b32 a[234:235], v13 offset0:0 offset1:2\n"
      "v_cvt_pk_bf16_f32 v30, v30, v31\n"
      "v_mfma_f32_16x16x32_bf16 v[76:79], v[180:183], a[192:195], v[76:79]\n"
      "ds_read_b128 a[208:211], v1 offset:128\n"
      "ds_write_addtid_b32 v30 offset:34304\n"
      "v_mfma_f32_16x16x32_bf16 v[80:83], v[172:175], a[192:195], v[80:83]\n"
      "v_cvt_pk_bf16_f32 v40, v40, v41\n"
      "v_cvt_pk_bf16_f32 v23, v23, v27\n"
      "v_cvt_pk_bf16_f32 v42, v42, v43\n"
      "v_mfma_f32_16x16x32_bf16 v[84:87], v[164:167], a[192:195], v[84:87]\n"
      "ds_write_addtid_b32 v40 offset:35840\n"
      "ds_write_addtid_b32 v42 offset:36352\n"
      "ds_write_addtid_b32 v23 offset:31744\n"
      "v_mfma_f32_16x16x32_bf16 v[88:91], v[176:179], a[196:199], v[88:91]\n"
      "ds_read_b128 a[212:215], v1 offset:4224\n"
      "v_fmac_f32 v44, s46, v32\n"
      "v_mfma_f32_16x16x32_bf16 v[92:95], v[168:171], a[196:199], v[92:95]\n"
      "v_fmac_f32 v45, s46, v33\n"
      "v_fmac_f32 v46, s46, v34\n"
      "v_mfma_f32_16x16x32_bf16 v[96:99], v[160:163], a[196:199], v[96:99]\n"
      "v_fmac_f32 v47, s46, v35\n"
      "v_fma_f32 v32, 0.5, v32, v56\n"
      "v_mfma_f32_16x16x32_bf16 v[76:79], v[156:159], a[200:203], v[76:79]\n"
      "ds_read_b128 a[192:195], v2 offset:0\n"
      "v_fma_f32 v33, 0.5, v33, v57\n"
      "v_mfma_f32_16x16x32_bf16 v[80:83], v[148:151], a[200:203], v[80:83]\n"
      "v_fma_f32 v34, 0.5, v34, v58\n"
      "v_fma_f32 v35, 0.5, v35, v59\n"
      "v_mfma_f32_16x16x32_bf16 v[84:87], v[140:143], a[200:203], v[84:87]\n"
      "s_waitcnt lgkmcnt(1)\n"
      "v_fmac_f32 v44, s48, v68\n"
      "v_fmac_f32 v45, s48, v69\n"
      "v_fmac_f32 v46, s48, v70\n"
      "v_mfma_f32_16x16x32_bf16 v[88:91], v[152:155], a[204:207], v[88:91]\n"
      "ds_read_b128 a[196:199], v2 offset:4096\n"
      "v_fmac_f32 v47, s48, v71\n"
      "v_mfma_f32_16x16x32_bf16 v[92:95], v[144:147], a[204:207], v[92:95]\n"
      "v_fmac_f32 v32, s45, v44\n"
      "v_fmac_f32 v33, s45, v45\n"
      "v_mfma_f32_16x16x32_bf16 v[96:99], v[136:139], a[204:207], v[96:99]\n"
      "buffer_store_dwordx4 a[232:235], v16, s[8:11], s59 offen offset:0 sc1\n"
      "ds_read_b64_tr_b16 a[248:249], v7 offset:54272\n"
      "v_mfma_f32_16x16x32_bf16 v[76:79], v[132:135], a[208:211], v[76:79]\n"
      "ds_read_b128 a[200:203], v2 offset:64\n"
      "ds_read_b64_tr_b16 a[250:251], v7 offset:54280\n"
      "v_fmac_f32 v34, s45, v46\n"
      "v_mfma_f32_16x16x32_bf16 v[80:83], v[124:127], a[208:211], v[80:83]\n"
      "v_fmac_f32 v35, s45, v47\n"
      "v_fma_f32 v44, s47, v44, -v68\n"
      "v_mfma_f32_16x16x32_bf16 v[84:87], v[116:119], a[208:211], v[84:87]\n"
      "v_fma_f32 v45, s47, v45, -v69\n"
      "v_fma_f32 v46, s47, v46, -v70\n"
      "v_mfma_f32_16x16x32_bf16 v[88:91], v[128:131], a[212:215], v[88:91]\n"
      "s_nop 0\n"
      "ds_read_b128 a[204:207], v2 offset:4160\n"
      "v_fma_f32 v47, s47, v47, -v71\n"
      "v_mfma_f32_16x16x32_bf16 v[92:95], v[120:123], a[212:215], v[92:95]\n"
      "s_nop 0\n"
      "v_cvt_pk_bf16_f32 v32, v32, v33\n"
      "v_cvt_pk_bf16_f32 v34, v34, v35\n"
      "v_mfma_f32_16x16x32_bf16 v[96:99], v[112:115], a[212:215], v[96:99]\n"
      "s_waitcnt lgkmcnt(1)\n"
      "ds_write_addtid_b32 v32 offset:41988\n"
      "ds_write_addtid_b32 v34 offset:42500\n"
      "v_cvt_pk_bf16_f32 v44, v44, v45\n"
      "v_mfma_f32_16x16x32_bf16 v[76:79], v[108:111], a[192:195], v[76:79]\n"
      "ds_read_b128 a[208:211], v2 offset:128\n"
      "ds_write_addtid_b32 v44 offset:44036\n"
      "v_cvt_pk_bf16_f32 v46, v46, v47\n"
      "v_mfma_f32_4x4x4_16b_bf16 v[20:23], a[216:217], a[248:249], 0\n"
      "v_fmac_f32 v48, s46, v36\n"
      "v_mfma_f32_4x4x4_16b_bf16 v[24:27], a[216:217], a[250:251], 0\n"
      "ds_write_addtid_b32 v46 offset:44548\n"
      "v_fmac_f32 v49, s46, v37\n"
      "v_fmac_f32 v50, s46, v38\n"
      "v_mfma_f32_16x16x32_bf16 v[80:83], v[100:103], a[192:195], v[80:83]\n"
      "v_fmac_f32 v51, s46, v39\n"
      "v_cvt_pk_bf16_f32 v20, v20, v24\n"
      "v_fma_f32 v36, 0.5, v36, v60\n"
      "v_mfma_f32_16x16x32_bf16 v[84:87], a[160:163], a[192:195], v[84:87]\n"
      "s_add_u32 s54, s54, 196608\n"
      "ds_write_addtid_b32 v20 offset:19968\n"
      "v_fma_f32 v37, 0.5, v37, v61\n"
      "v_mfma_f32_16x16x32_bf16 v[88:91], v[104:107], a[196:199], v[88:91]\n"
      "s_add_u32 m0, s37, 120328\n"
      "ds_read_b128 a[212:215], v2 offset:4224\n"
      "ds_read2st64_b32 a[236:237], v11 offset0:0 offset1:2\n"
      "ds_read2st64_b32 a[238:239], v14 offset0:0 offset1:2\n"
      "v_mfma_f32_16x16x32_bf16 v[92:95], a[164:167], a[196:199], v[92:95]\n"
      "buffer_load_dwordx4 v8, s[4:7], s54 offen offset:0 nt lds\n"
      "buffer_load_dwordx4 v9, s[4:7], s54 offen offset:1024 nt lds\n"
      "v_mfma_f32_16x16x32_bf16 v[96:99], a[156:159], a[196:199], v[96:99]\n"
      "s_mov_b32 m0, s64\n"
      "v_fma_f32 v38, 0.5, v38, v62\n"
      "v_fma_f32 v39, 0.5, v39, v63\n"
      "v_cvt_pk_bf16_f32 v21, v21, v25\n"
      "v_mfma_f32_16x16x32_bf16 v[76:79], a[152:155], a[200:203], v[76:79]\n"
      "s_nop 0\n"
      "ds_read_b128 a[192:195], v3 offset:33792\n"
      "ds_write_addtid_b32 v21 offset:24064\n"
      "v_mfma_f32_16x16x32_bf16 v[80:83], a[144:147], a[200:203], v[80:83]\n"
      "v_fmac_f32 v48, s48, v72\n"
      "v_fmac_f32 v49, s48, v73\n"
      "v_cvt_pk_bf16_f32 v22, v22, v26\n"
      "v_mfma_f32_16x16x32_bf16 v[84:87], a[136:139], a[200:203], v[84:87]\n"
      "s_waitcnt lgkmcnt(2)\n"
      "ds_write_addtid_b32 v22 offset:28160\n"
      "v_fmac_f32 v50, s48, v74\n"
      "v_mfma_f32_16x16x32_bf16 v[88:91], a[148:151], a[204:207], v[88:91]\n"
      "ds_read_b128 a[196:199], v3 offset:33856\n"
      "v_fmac_f32 v51, s48, v75\n"
      "v_mfma_f32_16x16x32_bf16 v[92:95], a[140:143], a[204:207], v[92:95]\n"
      "v_fmac_f32 v36, s45, v48\n"
      "v_cvt_pk_bf16_f32 v23, v23, v27\n"
      "v_mfma_f32_16x16x32_bf16 v[96:99], a[132:135], a[204:207], v[96:99]\n"
      "ds_write_addtid_b32 v23 offset:32256\n"
      "ds_read2st64_b32 a[240:241], v12 offset0:0 offset1:2\n"
      "ds_read2st64_b32 a[242:243], v15 offset0:0 offset1:2\n"
      "v_mfma_f32_16x16x32_bf16 v[76:79], a[128:131], a[208:211], v[76:79]\n"
      "ds_read_b128 a[200:203], v3 offset:33920\n"
      "v_fmac_f32 v37, s45, v49\n"
      "v_fmac_f32 v38, s45, v50\n"
      "v_mfma_f32_16x16x32_bf16 v[80:83], a[120:123], a[208:211], v[80:83]\n"
      "v_fmac_f32 v39, s45, v51\n"
      "v_fma_f32 v48, s47, v48, -v72\n"
      "v_mfma_f32_16x16x32_bf16 v[84:87], a[112:115], a[208:211], v[84:87]\n"
      "s_nop 0\n"
      "v_fma_f32 v49, s47, v49, -v73\n"
      "v_fma_f32 v50, s47, v50, -v74\n"
      "v_mfma_f32_16x16x32_bf16 v[88:91], a[124:127], a[212:215], v[88:91]\n"
      "s_add_u32 s54, s54, 196608\n"
      "ds_read_b128 a[204:207], v4 offset:33792\n"
      "v_fma_f32 v51, s47, v51, -v75\n"
      "v_mfma_f32_16x16x32_bf16 v[92:95], a[116:119], a[212:215], v[92:95]\n"
      "s_add_u32 m0, s37, 124552\n"
      "v_cvt_pk_bf16_f32 v36, v36, v37\n"
      "v_cvt_pk_bf16_f32 v38, v38, v39\n"
      "v_mfma_f32_16x16x32_bf16 v[96:99], a[108:111], a[212:215], v[96:99]\n"
      "s_waitcnt lgkmcnt(1)\n"
      "buffer_store_dwordx4 a[236:239], v16, s[8:11], s59 offen offset:128 sc1\n"
      "buffer_load_dwordx4 v8, s[4:7], s54 offen offset:0 nt lds\n"
      "buffer_load_dwordx4 v9, s[4:7], s54 offen offset:1024 nt lds\n"
      "v_mfma_f32_16x16x32_bf16 v[52:55], a[0:3], a[192:195], 0\n"
      "s_mov_b32 m0, s64\n"
      "ds_read_b128 a[208:211], v4 offset:33856\n"
      "ds_write_addtid_b32 v36 offset:37888\n"
      "ds_write_addtid_b32 v38 offset:38400\n"
      "v_add_f32 v76, v76, v88\n"
      "v_mfma_f32_16x16x32_bf16 v[56:59], a[4:7], a[192:195], 0\n"
      "ds_write_addtid_b32 v76 offset:45312\n"
      "v_add_f32 v77, v77, v89\n"
      "v_mfma_f32_16x16x32_bf16 v[60:63], a[8:11], a[192:195], 0\n"
      "ds_write_addtid_b32 v77 offset:45824\n"
      "v_add_f32 v78, v78, v90\n"
      "v_add_f32 v79, v79, v91\n"
      "v_mfma_f32_16x16x32_bf16 v[52:55], a[12:15], a[196:199], v[52:55]\n"
      "ds_read_b128 a[212:215], v4 offset:33920\n"
      "ds_write_addtid_b32 v78 offset:46336\n"
      "v_mfma_f32_16x16x32_bf16 v[56:59], a[16:19], a[196:199], v[56:59]\n"
      "ds_write_addtid_b32 v79 offset:46848\n"
      "v_add_f32 v80, v80, v92\n"
      "v_cvt_pk_bf16_f32 v48, v48, v49\n"
      "v_mfma_f32_16x16x32_bf16 v[60:63], a[20:23], a[196:199], v[60:63]\n"
      "s_nop 0\n"
      "ds_write_addtid_b32 v48 offset:39936\n"
      "ds_write_addtid_b32 v80 offset:47360\n"
      "v_mfma_f32_16x16x32_bf16 v[52:55], a[24:27], a[200:203], v[52:55]\n"
      "ds_read_b128 a[192:195], v5 offset:33792\n"
      "v_add_f32 v81, v81, v93\n"
      "v_mfma_f32_16x16x32_bf16 v[56:59], a[28:31], a[200:203], v[56:59]\n"
      "buffer_store_dwordx4 a[240:243], v16, s[8:11], s59 offen offset:256 sc1\n"
      "ds_write_addtid_b32 v81 offset:47872\n"
      "v_mfma_f32_16x16x32_bf16 v[60:63], a[32:35], a[200:203], v[60:63]\n"
      "s_waitcnt lgkmcnt(4)\n"
      "v_add_f32 v82, v82, v94\n"
      "v_add_f32 v83, v83, v95\n"
      "v_mfma_f32_16x16x32_bf16 v[52:55], a[36:39], a[204:207], v[52:55]\n"
      "ds_read_b128 a[196:199], v5 offset:33856\n"
      "ds_write_addtid_b32 v82 offset:48384\n"
      "ds_write_addtid_b32 v83 offset:48896\n"
      "v_cvt_pk_bf16_f32 v50, v50, v51\n"
      "v_mfma_f32_16x16x32_bf16 v[56:59], a[40:43], a[204:207], v[56:59]\n"
      "ds_write_addtid_b32 v50 offset:40448\n"
      "v_add_f32 v84, v84, v96\n"
      "v_mfma_f32_16x16x32_bf16 v[60:63], a[44:47], a[204:207], v[60:63]\n"
      "ds_write_addtid_b32 v84 offset:49408\n"
      "v_add_f32 v85, v85, v97\n"
      "v_add_f32 v86, v86, v98\n"
      "v_add_f32_e64 v87, v87, v99\n"
      "v_mfma_f32_16x16x32_bf16 v[52:55], a[48:51], a[208:211], v[52:55]\n"
      "s_nop 0\n"
      "ds_read_b128 a[200:203], v5 offset:33920\n"
      "ds_write_addtid_b32 v85 offset:49920\n"
      "v_mfma_f32_16x16x32_bf16 v[56:59], a[52:55], a[208:211], v[56:59]\n"
      "s_nop 0\n"
      "ds_write_addtid_b32 v86 offset:50432\n"
      "ds_write_addtid_b32 v87 offset:50944\n"
      "v_mfma_f32_16x16x32_bf16 v[60:63], a[56:59], a[208:211], v[60:63]\n"
      "s_nop 0\n"
      "v_fma_f32 v40, -2.0, v88, v76\n"
      "v_fma_f32 v41, -2.0, v89, v77\n"
      "v_mfma_f32_16x16x32_bf16 v[52:55], a[60:63], a[212:215], v[52:55]\n"
      "s_nop 0\n"
      "v_fma_f32 v42, -2.0, v90, v78\n"
      "v_fma_f32 v43, -2.0, v91, v79\n"
      "v_mfma_f32_16x16x32_bf16 v[56:59], a[64:67], a[212:215], v[56:59]\n"
      "s_waitcnt vmcnt(25)\n"
      "v_fma_f32 v44, -2.0, v92, v80\n"
      "v_fma_f32 v45, -2.0, v93, v81\n"
      "v_mfma_f32_16x16x32_bf16 v[60:63], a[68:71], a[212:215], v[60:63]\n"
      "s_waitcnt lgkmcnt(0)\n"
      "s_barrier\n"
      "v_mfma_f32_16x16x32_bf16 v[52:55], a[72:75], a[192:195], v[52:55]\n"
      "s_add_u32 s58, s40, 0\n"
      "ds_read_b128 a[204:207], v0 offset:16384\n"
      "ds_read2st64_b32 a[220:221], v10 offset0:181 offset1:183\n"
      "ds_read2st64_b32 a[222:223], v13 offset0:181 offset1:183\n"
      "ds_read2st64_b32 a[224:225], v11 offset0:181 offset1:183\n"
      "v_mfma_f32_16x16x32_bf16 v[56:59], a[76:79], a[192:195], v[56:59]\n"
      "s_mov_b32 s40, s62\n"
      "ds_read_b128 a[208:211], v0 offset:20480\n"
      "v_fma_f32 v46, -2.0, v94, v82\n"
      "v_mfma_f32_16x16x32_bf16 v[60:63], a[80:83], a[192:195], v[60:63]\n"
      "s_add_u32 s62, s62, 786432\n"
      "ds_read2st64_b32 v[28:29], v6 offset0:0 offset1:2\n"
      "ds_read2st64_b32 a[226:227], v14 offset0:181 offset1:183\n"
      "v_mfma_f32_16x16x32_bf16 v[52:55], a[84:87], a[196:199], v[52:55]\n"
      "s_add_u32 s59, s58, 196608\n"
      "ds_read_b128 a[212:215], v0 offset:16448\n"
      "v_fma_f32 v47, -2.0, v95, v83\n"
      "v_fma_f32 v48, -2.0, v96, v84\n"
      "v_mfma_f32_16x16x32_bf16 v[56:59], a[88:91], a[196:199], v[56:59]\n"
      "s_nop 0\n"
      "s_waitcnt lgkmcnt(5)\n"
      "ds_read2st64_b32 v[30:31], v6 offset0:4 offset1:6\n"
      "v_fma_f32 v49, -2.0, v97, v85\n"
      "v_mfma_f32_16x16x32_bf16 v[60:63], a[92:95], a[196:199], v[60:63]\n"
      "s_add_u32 s54, s54, 196608\n"
      "v_fma_f32 v50, -2.0, v98, v86\n"
      "v_fma_f32 v51, -2.0, v99, v87\n"
      "v_mfma_f32_16x16x32_bf16 v[52:55], a[96:99], a[200:203], v[52:55]\n"
      "s_add_u32 m0, s37, 128896\n"
      "buffer_store_dwordx4 a[220:223], v16, s[8:11], s58 offen offset:0 sc1\n"
      "ds_read_b128 a[192:195], v0 offset:20544\n"
      "v_mfma_f32_16x16x32_bf16 v[56:59], a[100:103], a[200:203], v[56:59]\n"
      "buffer_load_dwordx4 v8, s[4:7], s54 offen offset:0 nt lds\n"
      "buffer_load_dwordx4 v9, s[4:7], s54 offen offset:1024 nt lds\n"
      "ds_read2st64_b32 v[32:33], v6 offset0:8 offset1:10\n"
      "v_mfma_f32_16x16x32_bf16 v[60:63], a[104:107], a[200:203], v[60:63]\n"
      "s_mov_b32 m0, s64\n"
      "ds_read2st64_b32 v[34:35], v6 offset0:12 offset1:14\n"
      "ds_read2st64_b32 v[36:37], v6 offset0:16 offset1:18\n"
      "ds_read2st64_b32 v[38:39], v6 offset0:20 offset1:22\n"
      "v_mfma_f32_16x16x32_bf16 v[76:79], v[252:255], a[204:207], 0\n"
      "s_nop 0\n"
      "ds_read_b128 a[196:199], v0 offset:16512\n"
      "v_mfma_f32_16x16x32_bf16 v[80:83], v[244:247], a[204:207], 0\n"
      "ds_read_b64_tr_b16 a[244:245], v7 offset:3072\n"
      "ds_read2st64_b32 a[228:229], v12 offset0:181 offset1:183\n"
      "ds_read_b64_tr_b16 a[246:247], v7 offset:3080\n"
      "v_mfma_f32_16x16x32_bf16 v[84:87], v[236:239], a[204:207], 0\n"
      "s_waitcnt lgkmcnt(9)\n"
      "ds_read2st64_b32 a[230:231], v15 offset0:181 offset1:183\n"
      "v_fmac_f32 v40, s46, v28\n"
      "v_fmac_f32 v41, s46, v29\n"
      "v_mfma_f32_16x16x32_bf16 v[88:91], v[248:251], a[208:211], 0\n"
      "ds_read_b128 a[200:203], v0 offset:20608\n"
      "v_fmac_f32 v42, s46, v30\n"
      "v_mfma_f32_16x16x32_bf16 v[92:95], v[240:243], a[208:211], 0\n"
      "buffer_store_dwordx4 a[224:227], v16, s[8:11], s58 offen offset:128 sc1\n"
      "v_mfma_f32_16x16x32_bf16 v[96:99], v[232:235], a[208:211], 0\n"
      "v_fmac_f32_e64 v43, s46, v31\n"
      "v_fma_f32 v28, 0.5, v28, v64\n"
      "v_fma_f32 v29, 0.5, v29, v65\n"
      "v_mfma_f32_16x16x32_bf16 v[76:79], v[228:231], a[212:215], v[76:79]\n"
      "ds_read_b128 a[204:207], v1 offset:16384\n"
      "v_fma_f32 v30, 0.5, v30, v66\n"
      "v_mfma_f32_16x16x32_bf16 v[80:83], v[220:223], a[212:215], v[80:83]\n"
      "v_fma_f32 v31, 0.5, v31, v67\n"
      "v_fmac_f32 v40, s48, v52\n"
      "v_fmac_f32 v41, s48, v53\n"
      "v_mfma_f32_16x16x32_bf16 v[84:87], v[212:215], a[212:215], v[84:87]\n"
      "s_waitcnt lgkmcnt(1)\n"
      "v_fmac_f32 v42, s48, v54\n"
      "v_fmac_f32 v43, s48, v55\n"
      "v_mfma_f32_4x4x4_16b_bf16 v[20:23], a[216:217], a[244:245], 0\n"
      "v_fmac_f32 v28, s45, v40\n"
      "v_mfma_f32_4x4x4_16b_bf16 v[24:27], a[216:217], a[246:247], 0\n"
      "v_fmac_f32 v29, s45, v41\n"
      "v_fmac_f32_e64 v30, s45, v42\n"
      "v_mfma_f32_16x16x32_bf16 v[88:91], v[224:227], a[192:195], v[88:91]\n"
      "s_nop 0\n"
      "ds_read_b128 a[208:211], v1 offset:20480\n"
      "v_fmac_f32 v31, s45, v43\n"
      "v_cvt_pk_bf16_f32 v20, v20, v24\n"
      "v_mfma_f32_16x16x32_bf16 v[92:95], v[216:219], a[192:195], v[92:95]\n"
      "s_nop 0\n"
      "ds_write_addtid_b32 v20 offset:3072\n"
      "v_fma_f32 v40, s47, v40, -v52\n"
      "v_mfma_f32_16x16x32_bf16 v[96:99], v[208:211], a[192:195], v[96:99]\n"
      "s_add_u32 s54, s54, 196608\n"
      "v_cvt_pk_bf16_f32 v21, v21, v25\n"
      "v_fma_f32 v41, s47, v41, -v53\n"
      "v_mfma_f32_16x16x32_bf16 v[76:79], v[204:207], a[196:199], v[76:79]\n"
      "s_add_u32 m0, s37, 133120\n"
      "ds_read_b128 a[212:215], v1 offset:16448\n"
      "v_fma_f32 v42, s47, v42, -v54\n"
      "v_mfma_f32_16x16x32_bf16 v[80:83], v[196:199], a[196:199], v[80:83]\n"
      "buffer_load_dwordx4 v8, s[4:7], s54 offen offset:0 nt lds\n"
      "buffer_load_dwordx4 v9, s[4:7], s54 offen offset:1024 nt lds\n"
      "v_mfma_f32_16x16x32_bf16 v[84:87], v[188:191], a[196:199], v[84:87]\n"
      "s_mov_b32 m0, s64\n"
      "v_fma_f32 v43, s47, v43, -v55\n"
      "v_cvt_pk_bf16_f32 v22, v22, v26\n"
      "v_mfma_f32_16x16x32_bf16 v[88:91], v[200:203], a[200:203], v[88:91]\n"
      "s_nop 0\n"
      "buffer_store_dwordx4 a[228:231], v16, s[8:11], s58 offen offset:256 sc1\n"
      "ds_read_b128 a[192:195], v1 offset:20544\n"
      "v_mfma_f32_16x16x32_bf16 v[92:95], v[192:195], a[200:203], v[92:95]\n"
      "s_nop 0\n"
      "ds_write_addtid_b32 v21 offset:7168\n"
      "ds_read2st64_b32 a[232:233], v10 offset0:4 offset1:6\n"
      "ds_write_addtid_b32 v22 offset:11264\n"
      "v_cvt_pk_bf16_f32 v28, v28, v29\n"
      "v_mfma_f32_16x16x32_bf16 v[96:99], v[184:187], a[200:203], v[96:99]\n"
      "s_waitcnt lgkmcnt(4)\n"
      "ds_write_addtid_b32 v28 offset:32768\n"
      "ds_read2st64_b32 a[234:235], v13 offset0:4 offset1:6\n"
      "v_cvt_pk_bf16_f32 v30, v30, v31\n"
      "v_mfma_f32_16x16x32_bf16 v[76:79], v[180:183], a[204:207], v[76:79]\n"
      "ds_read_b128 a[196:199], v1 offset:16512\n"
      "ds_write_addtid_b32 v30 offset:33280\n"
      "v_mfma_f32_16x16x32_bf16 v[80:83], v[172:175], a[204:207], v[80:83]\n"
      "v_cvt_pk_bf16_f32 v40, v40, v41\n"
      "v_cvt_pk_bf16_f32 v23, v23, v27\n"
      "v_cvt_pk_bf16_f32 v42, v42, v43\n"
      "v_mfma_f32_16x16x32_bf16 v[84:87], v[164:167], a[204:207], v[84:87]\n"
      "ds_write_addtid_b32 v40 offset:34816\n"
      "ds_write_addtid_b32 v42 offset:35328\n"
      "ds_write_addtid_b32 v23 offset:15360\n"
      "v_mfma_f32_16x16x32_bf16 v[88:91], v[176:179], a[208:211], v[88:91]\n"
      "ds_read_b128 a[200:203], v1 offset:20608\n"
      "v_fmac_f32 v44, s46, v32\n"
      "v_mfma_f32_16x16x32_bf16 v[92:95], v[168:171], a[208:211], v[92:95]\n"
      "v_fmac_f32 v45, s46, v33\n"
      "v_fmac_f32 v46, s46, v34\n"
      "v_mfma_f32_16x16x32_bf16 v[96:99], v[160:163], a[208:211], v[96:99]\n"
      "v_fmac_f32 v47, s46, v35\n"
      "v_fma_f32 v32, 0.5, v32, v68\n"
      "v_mfma_f32_16x16x32_bf16 v[76:79], v[156:159], a[212:215], v[76:79]\n"
      "ds_read_b128 a[204:207], v2 offset:16384\n"
      "v_fma_f32 v33, 0.5, v33, v69\n"
      "v_mfma_f32_16x16x32_bf16 v[80:83], v[148:151], a[212:215], v[80:83]\n"
      "v_fma_f32 v34, 0.5, v34, v70\n"
      "v_fma_f32 v35, 0.5, v35, v71\n"
      "v_mfma_f32_16x16x32_bf16 v[84:87], v[140:143], a[212:215], v[84:87]\n"
      "s_waitcnt lgkmcnt(1)\n"
      "v_fmac_f32 v44, s48, v56\n"
      "v_fmac_f32 v45, s48, v57\n"
      "v_fmac_f32 v46, s48, v58\n"
      "v_mfma_f32_16x16x32_bf16 v[88:91], v[152:155], a[192:195], v[88:91]\n"
      "ds_read_b128 a[208:211], v2 offset:20480\n"
      "v_fmac_f32 v47, s48, v59\n"
      "v_mfma_f32_16x16x32_bf16 v[92:95], v[144:147], a[192:195], v[92:95]\n"
      "v_fmac_f32 v32, s45, v44\n"
      "v_fmac_f32 v33, s45, v45\n"
      "v_mfma_f32_16x16x32_bf16 v[96:99], v[136:139], a[192:195], v[96:99]\n"
      "buffer_store_dwordx4 a[232:235], v16, s[8:11], s59 offen offset:0 sc1\n"
      "ds_read_b64_tr_b16 a[248:249], v7 offset:3584\n"
      "v_mfma_f32_16x16x32_bf16 v[76:79], v[132:135], a[196:199], v[76:79]\n"
      "ds_read_b128 a[212:215], v2 offset:16448\n"
      "ds_read_b64_tr_b16 a[250:251], v7 offset:3592\n"
      "v_fmac_f32 v34, s45, v46\n"
      "v_mfma_f32_16x16x32_bf16 v[80:83], v[124:127], a[196:199], v[80:83]\n"
      "v_fmac_f32 v35, s45, v47\n"
      "v_fma_f32 v44, s47, v44, -v56\n"
      "v_mfma_f32_16x16x32_bf16 v[84:87], v[116:119], a[196:199], v[84:87]\n"
      "v_fma_f32 v45, s47, v45, -v57\n"
      "v_fma_f32 v46, s47, v46, -v58\n"
      "v_mfma_f32_16x16x32_bf16 v[88:91], v[128:131], a[200:203], v[88:91]\n"
      "s_nop 0\n"
      "ds_read_b128 a[192:195], v2 offset:20544\n"
      "v_fma_f32 v47, s47, v47, -v59\n"
      "v_mfma_f32_16x16x32_bf16 v[92:95], v[120:123], a[200:203], v[92:95]\n"
      "s_nop 0\n"
      "v_cvt_pk_bf16_f32 v32, v32, v33\n"
      "v_cvt_pk_bf16_f32 v34, v34, v35\n"
      "v_mfma_f32_16x16x32_bf16 v[96:99], v[112:115], a[200:203], v[96:99]\n"
      "s_waitcnt lgkmcnt(1)\n"
      "ds_write_addtid_b32 v32 offset:40964\n"
      "ds_write_addtid_b32 v34 offset:41476\n"
      "v_cvt_pk_bf16_f32 v44, v44, v45\n"
      "v_mfma_f32_16x16x32_bf16 v[76:79], v[108:111], a[204:207], v[76:79]\n"
      "ds_read_b128 a[196:199], v2 offset:16512\n"
      "ds_write_addtid_b32 v44 offset:43012\n"
      "v_cvt_pk_bf16_f32 v46, v46, v47\n"
      "v_mfma_f32_4x4x4_16b_bf16 v[20:23], a[216:217], a[248:249], 0\n"
      "v_fmac_f32 v48, s46, v36\n"
      "v_mfma_f32_4x4x4_16b_bf16 v[24:27], a[216:217], a[250:251], 0\n"
      "ds_write_addtid_b32 v46 offset:43524\n"
      "v_fmac_f32 v49, s46, v37\n"
      "v_fmac_f32 v50, s46, v38\n"
      "v_mfma_f32_16x16x32_bf16 v[80:83], v[100:103], a[204:207], v[80:83]\n"
      "v_fmac_f32 v51, s46, v39\n"
      "v_cvt_pk_bf16_f32 v20, v20, v24\n"
      "v_fma_f32 v36, 0.5, v36, v72\n"
      "v_mfma_f32_16x16x32_bf16 v[84:87], a[160:163], a[204:207], v[84:87]\n"
      "s_add_u32 s54, s54, 196608\n"
      "ds_write_addtid_b32 v20 offset:3584\n"
      "v_fma_f32 v37, 0.5, v37, v73\n"
      "v_mfma_f32_16x16x32_bf16 v[88:91], v[104:107], a[208:211], v[88:91]\n"
      "s_add_u32 m0, s37, 137224\n"
      "ds_read_b128 a[200:203], v2 offset:20608\n"
      "ds_read2st64_b32 a[236:237], v11 offset0:4 offset1:6\n"
      "ds_read2st64_b32 a[238:239], v14 offset0:4 offset1:6\n"
      "v_mfma_f32_16x16x32_bf16 v[92:95], a[164:167], a[208:211], v[92:95]\n"
      "buffer_load_dwordx4 v8, s[4:7], s54 offen offset:0 nt lds\n"
      "buffer_load_dwordx4 v9, s[4:7], s54 offen offset:1024 nt lds\n"
      "v_mfma_f32_16x16x32_bf16 v[96:99], a[156:159], a[208:211], v[96:99]\n"
      "s_mov_b32 m0, s64\n"
      "v_fma_f32 v38, 0.5, v38, v74\n"
      "v_fma_f32 v39, 0.5, v39, v75\n"
      "v_cvt_pk_bf16_f32 v21, v21, v25\n"
      "v_mfma_f32_16x16x32_bf16 v[76:79], a[152:155], a[212:215], v[76:79]\n"
      "s_nop 0\n"
      "ds_read_b128 a[204:207], v3 offset:50688\n"
      "ds_write_addtid_b32 v21 offset:7680\n"
      "v_mfma_f32_16x16x32_bf16 v[80:83], a[144:147], a[212:215], v[80:83]\n"
      "v_fmac_f32 v48, s48, v60\n"
      "v_fmac_f32 v49, s48, v61\n"
      "v_cvt_pk_bf16_f32 v22, v22, v26\n"
      "v_mfma_f32_16x16x32_bf16 v[84:87], a[136:139], a[212:215], v[84:87]\n"
      "s_waitcnt lgkmcnt(2)\n"
      "ds_write_addtid_b32 v22 offset:11776\n"
      "v_fmac_f32 v50, s48, v62\n"
      "v_mfma_f32_16x16x32_bf16 v[88:91], a[148:151], a[192:195], v[88:91]\n"
      "ds_read_b128 a[208:211], v3 offset:50752\n"
      "v_fmac_f32 v51, s48, v63\n"
      "v_mfma_f32_16x16x32_bf16 v[92:95], a[140:143], a[192:195], v[92:95]\n"
      "v_fmac_f32 v36, s45, v48\n"
      "v_cvt_pk_bf16_f32 v23, v23, v27\n"
      "v_mfma_f32_16x16x32_bf16 v[96:99], a[132:135], a[192:195], v[96:99]\n"
      "ds_write_addtid_b32 v23 offset:15872\n"
      "ds_read2st64_b32 a[240:241], v12 offset0:4 offset1:6\n"
      "ds_read2st64_b32 a[242:243], v15 offset0:4 offset1:6\n"
      "v_mfma_f32_16x16x32_bf16 v[76:79], a[128:131], a[196:199], v[76:79]\n"
      "ds_read_b128 a[212:215], v3 offset:50816\n"
      "v_fmac_f32 v37, s45, v49\n"
      "v_fmac_f32 v38, s45, v50\n"
      "v_mfma_f32_16x16x32_bf16 v[80:83], a[120:123], a[196:199], v[80:83]\n"
      "v_fmac_f32 v39, s45, v51\n"
      "v_fma_f32 v48, s47, v48, -v60\n"
      "v_mfma_f32_16x16x32_bf16 v[84:87], a[112:115], a[196:199], v[84:87]\n"
      "s_nop 0\n"
      "v_fma_f32 v49, s47, v49, -v61\n"
      "v_fma_f32 v50, s47, v50, -v62\n"
      "v_mfma_f32_16x16x32_bf16 v[88:91], a[124:127], a[200:203], v[88:91]\n"
      "s_add_u32 s54, s54, 196608\n"
      "ds_read_b128 a[192:195], v4 offset:50688\n"
      "v_fma_f32 v51, s47, v51, -v63\n"
      "v_mfma_f32_16x16x32_bf16 v[92:95], a[116:119], a[200:203], v[92:95]\n"
      "s_add_u32 m0, s37, 141448\n"
      "v_cvt_pk_bf16_f32 v36, v36, v37\n"
      "v_cvt_pk_bf16_f32 v38, v38, v39\n"
      "v_mfma_f32_16x16x32_bf16 v[96:99], a[108:111], a[200:203], v[96:99]\n"
      "s_waitcnt lgkmcnt(1)\n"
      "buffer_store_dwordx4 a[236:239], v16, s[8:11], s59 offen offset:128 sc1\n"
      "buffer_load_dwordx4 v8, s[4:7], s54 offen offset:0 nt lds\n"
      "buffer_load_dwordx4 v9, s[4:7], s54 offen offset:1024 nt lds\n"
      "v_mfma_f32_16x16x32_bf16 v[64:67], a[0:3], a[204:207], 0\n"
      "s_mov_b32 m0, s64\n"
      "ds_read_b128 a[196:199], v4 offset:50752\n"
      "ds_write_addtid_b32 v36 offset:36864\n"
      "ds_write_addtid_b32 v38 offset:37376\n"
      "v_add_f32 v76, v76, v88\n"
      "v_mfma_f32_16x16x32_bf16 v[68:71], a[4:7], a[204:207], 0\n"
      "ds_write_addtid_b32 v76 offset:51456\n"
      "v_add_f32 v77, v77, v89\n"
      "v_mfma_f32_16x16x32_bf16 v[72:75], a[8:11], a[204:207], 0\n"
      "ds_write_addtid_b32 v77 offset:51968\n"
      "v_add_f32 v78, v78, v90\n"
      "v_add_f32 v79, v79, v91\n"
      "v_mfma_f32_16x16x32_bf16 v[64:67], a[12:15], a[208:211], v[64:67]\n"
      "ds_read_b128 a[200:203], v4 offset:50816\n"
      "ds_write_addtid_b32 v78 offset:52480\n"
      "v_mfma_f32_16x16x32_bf16 v[68:71], a[16:19], a[208:211], v[68:71]\n"
      "ds_write_addtid_b32 v79 offset:52992\n"
      "v_add_f32 v80, v80, v92\n"
      "v_cvt_pk_bf16_f32 v48, v48, v49\n"
      "v_mfma_f32_16x16x32_bf16 v[72:75], a[20:23], a[208:211], v[72:75]\n"
      "s_nop 0\n"
      "ds_write_addtid_b32 v48 offset:38912\n"
      "ds_write_addtid_b32 v80 offset:53504\n"
      "v_mfma_f32_16x16x32_bf16 v[64:67], a[24:27], a[212:215], v[64:67]\n"
      "ds_read_b128 a[204:207], v5 offset:50688\n"
      "v_add_f32 v81, v81, v93\n"
      "v_mfma_f32_16x16x32_bf16 v[68:71], a[28:31], a[212:215], v[68:71]\n"
      "buffer_store_dwordx4 a[240:243], v16, s[8:11], s59 offen offset:256 sc1\n"
      "ds_write_addtid_b32 v81 offset:54016\n"
      "v_mfma_f32_16x16x32_bf16 v[72:75], a[32:35], a[212:215], v[72:75]\n"
      "s_waitcnt lgkmcnt(4)\n"
      "v_add_f32 v82, v82, v94\n"
      "v_add_f32 v83, v83, v95\n"
      "v_mfma_f32_16x16x32_bf16 v[64:67], a[36:39], a[192:195], v[64:67]\n"
      "ds_read_b128 a[208:211], v5 offset:50752\n"
      "ds_write_addtid_b32 v82 offset:54528\n"
      "ds_write_addtid_b32 v83 offset:55040\n"
      "v_cvt_pk_bf16_f32 v50, v50, v51\n"
      "v_mfma_f32_16x16x32_bf16 v[68:71], a[40:43], a[192:195], v[68:71]\n"
      "ds_write_addtid_b32 v50 offset:39424\n"
      "v_add_f32 v84, v84, v96\n"
      "v_mfma_f32_16x16x32_bf16 v[72:75], a[44:47], a[192:195], v[72:75]\n"
      "ds_write_addtid_b32 v84 offset:55552\n"
      "v_add_f32 v85, v85, v97\n"
      "v_add_f32 v86, v86, v98\n"
      "v_add_f32_e64 v87, v87, v99\n"
      "v_mfma_f32_16x16x32_bf16 v[64:67], a[48:51], a[196:199], v[64:67]\n"
      "s_nop 0\n"
      "ds_read_b128 a[212:215], v5 offset:50816\n"
      "ds_write_addtid_b32 v85 offset:56064\n"
      "v_mfma_f32_16x16x32_bf16 v[68:71], a[52:55], a[196:199], v[68:71]\n"
      "s_nop 0\n"
      "ds_write_addtid_b32 v86 offset:56576\n"
      "ds_write_addtid_b32 v87 offset:57088\n"
      "v_mfma_f32_16x16x32_bf16 v[72:75], a[56:59], a[196:199], v[72:75]\n"
      "s_nop 0\n"
      "v_fma_f32 v40, -2.0, v88, v76\n"
      "v_fma_f32 v41, -2.0, v89, v77\n"
      "v_mfma_f32_16x16x32_bf16 v[64:67], a[60:63], a[200:203], v[64:67]\n"
      "s_nop 0\n"
      "v_fma_f32 v42, -2.0, v90, v78\n"
      "v_fma_f32 v43, -2.0, v91, v79\n"
      "v_mfma_f32_16x16x32_bf16 v[68:71], a[64:67], a[200:203], v[68:71]\n"
      "s_waitcnt vmcnt(25)\n"
      "v_fma_f32 v44, -2.0, v92, v80\n"
      "v_fma_f32 v45, -2.0, v93, v81\n"
      "v_mfma_f32_16x16x32_bf16 v[72:75], a[68:71], a[200:203], v[72:75]\n"
      "s_waitcnt lgkmcnt(0)\n"
      "s_barrier\n"
      "v_mfma_f32_16x16x32_bf16 v[64:67], a[72:75], a[204:207], v[64:67]\n"
      "s_add_u32 s58, s40, 0\n"
      "ds_read_b128 a[192:195], v0 offset:0\n"
      "ds_read2st64_b32 a[220:221], v10 offset0:177 offset1:179\n"
      "ds_read2st64_b32 a[222:223], v13 offset0:177 offset1:179\n"
      "ds_read2st64_b32 a[224:225], v11 offset0:177 offset1:179\n"
      "v_mfma_f32_16x16x32_bf16 v[68:71], a[76:79], a[204:207], v[68:71]\n"
      "s_mov_b32 s40, s62\n"
      "ds_read_b128 a[196:199], v0 offset:4096\n"
      "v_fma_f32 v46, -2.0, v94, v82\n"
      "v_mfma_f32_16x16x32_bf16 v[72:75], a[80:83], a[204:207], v[72:75]\n"
      "s_add_u32 s62, s62, 786432\n"
      "ds_read2st64_b32 v[28:29], v6 offset0:24 offset1:26\n"
      "ds_read2st64_b32 a[226:227], v14 offset0:177 offset1:179\n"
      "v_mfma_f32_16x16x32_bf16 v[64:67], a[84:87], a[208:211], v[64:67]\n"
      "s_add_u32 s59, s58, 196608\n"
      "ds_read_b128 a[200:203], v0 offset:64\n"
      "v_fma_f32 v47, -2.0, v95, v83\n"
      "v_fma_f32 v48, -2.0, v96, v84\n"
      "v_mfma_f32_16x16x32_bf16 v[68:71], a[88:91], a[208:211], v[68:71]\n"
      "s_nop 0\n"
      "s_waitcnt lgkmcnt(5)\n"
      "ds_read2st64_b32 v[30:31], v6 offset0:28 offset1:30\n"
      "v_fma_f32 v49, -2.0, v97, v85\n"
      "v_mfma_f32_16x16x32_bf16 v[72:75], a[92:95], a[208:211], v[72:75]\n"
      "s_add_u32 s54, s54, 196608\n"
      "v_fma_f32 v50, -2.0, v98, v86\n"
      "v_fma_f32 v51, -2.0, v99, v87\n"
      "v_mfma_f32_16x16x32_bf16 v[64:67], a[96:99], a[212:215], v[64:67]\n"
      "s_add_u32 m0, s37, 145792\n"
      "buffer_store_dwordx4 a[220:223], v16, s[8:11], s58 offen offset:0 sc1\n"
      "ds_read_b128 a[204:207], v0 offset:4160\n"
      "v_mfma_f32_16x16x32_bf16 v[68:71], a[100:103], a[212:215], v[68:71]\n"
      "buffer_load_dwordx4 v8, s[4:7], s54 offen offset:0 nt lds\n"
      "buffer_load_dwordx4 v9, s[4:7], s54 offen offset:1024 nt lds\n"
      "ds_read2st64_b32 v[32:33], v6 offset0:32 offset1:34\n"
      "v_mfma_f32_16x16x32_bf16 v[72:75], a[104:107], a[212:215], v[72:75]\n"
      "s_mov_b32 m0, s64\n"
      "ds_read2st64_b32 v[34:35], v6 offset0:36 offset1:38\n"
      "ds_read2st64_b32 v[36:37], v6 offset0:40 offset1:42\n"
      "ds_read2st64_b32 v[38:39], v6 offset0:44 offset1:46\n"
      "v_mfma_f32_16x16x32_bf16 v[76:79], v[252:255], a[192:195], 0\n"
      "s_nop 0\n"
      "ds_read_b128 a[208:211], v0 offset:128\n"
      "v_mfma_f32_16x16x32_bf16 v[80:83], v[244:247], a[192:195], 0\n"
      "ds_read_b64_tr_b16 a[244:245], v7 offset:19968\n"
      "ds_read2st64_b32 a[228:229], v12 offset0:177 offset1:179\n"
      "ds_read_b64_tr_b16 a[246:247], v7 offset:19976\n"
      "v_mfma_f32_16x16x32_bf16 v[84:87], v[236:239], a[192:195], 0\n"
      "s_waitcnt lgkmcnt(9)\n"
      "ds_read2st64_b32 a[230:231], v15 offset0:177 offset1:179\n"
      "v_fmac_f32 v40, s46, v28\n"
      "v_fmac_f32 v41, s46, v29\n"
      "v_mfma_f32_16x16x32_bf16 v[88:91], v[248:251], a[196:199], 0\n"
      "ds_read_b128 a[212:215], v0 offset:4224\n"
      "v_fmac_f32 v42, s46, v30\n"
      "v_mfma_f32_16x16x32_bf16 v[92:95], v[240:243], a[196:199], 0\n"
      "buffer_store_dwordx4 a[224:227], v16, s[8:11], s58 offen offset:128 sc1\n"
      "v_mfma_f32_16x16x32_bf16 v[96:99], v[232:235], a[196:199], 0\n"
      "v_fmac_f32_e64 v43, s46, v31\n"
      "v_fma_f32 v28, 0.5, v28, v52\n"
      "v_fma_f32 v29, 0.5, v29, v53\n"
      "v_mfma_f32_16x16x32_bf16 v[76:79], v[228:231], a[200:203], v[76:79]\n"
      "ds_read_b128 a[192:195], v1 offset:0\n"
      "v_fma_f32 v30, 0.5, v30, v54\n"
      "v_mfma_f32_16x16x32_bf16 v[80:83], v[220:223], a[200:203], v[80:83]\n"
      "v_fma_f32 v31, 0.5, v31, v55\n"
      "v_fmac_f32 v40, s48, v64\n"
      "v_fmac_f32 v41, s48, v65\n"
      "v_mfma_f32_16x16x32_bf16 v[84:87], v[212:215], a[200:203], v[84:87]\n"
      "s_waitcnt lgkmcnt(1)\n"
      "v_fmac_f32 v42, s48, v66\n"
      "v_fmac_f32 v43, s48, v67\n"
      "v_mfma_f32_4x4x4_16b_bf16 v[20:23], a[216:217], a[244:245], 0\n"
      "v_fmac_f32 v28, s45, v40\n"
      "v_mfma_f32_4x4x4_16b_bf16 v[24:27], a[216:217], a[246:247], 0\n"
      "v_fmac_f32 v29, s45, v41\n"
      "v_fmac_f32_e64 v30, s45, v42\n"
      "v_mfma_f32_16x16x32_bf16 v[88:91], v[224:227], a[204:207], v[88:91]\n"
      "s_nop 0\n"
      "ds_read_b128 a[196:199], v1 offset:4096\n"
      "v_fmac_f32 v31, s45, v43\n"
      "v_cvt_pk_bf16_f32 v20, v20, v24\n"
      "v_mfma_f32_16x16x32_bf16 v[92:95], v[216:219], a[204:207], v[92:95]\n"
      "s_nop 0\n"
      "ds_write_addtid_b32 v20 offset:19456\n"
      "v_fma_f32 v40, s47, v40, -v64\n"
      "v_mfma_f32_16x16x32_bf16 v[96:99], v[208:211], a[204:207], v[96:99]\n"
      "s_add_u32 s54, s54, 196608\n"
      "v_cvt_pk_bf16_f32 v21, v21, v25\n"
      "v_fma_f32 v41, s47, v41, -v65\n"
      "v_mfma_f32_16x16x32_bf16 v[76:79], v[204:207], a[208:211], v[76:79]\n"
      "s_add_u32 m0, s37, 82432\n"
      "ds_read_b128 a[200:203], v1 offset:64\n"
      "v_fma_f32 v42, s47, v42, -v66\n"
      "v_mfma_f32_16x16x32_bf16 v[80:83], v[196:199], a[208:211], v[80:83]\n"
      "buffer_load_dwordx4 v8, s[4:7], s54 offen offset:0 nt lds\n"
      "buffer_load_dwordx4 v9, s[4:7], s54 offen offset:1024 nt lds\n"
      "v_mfma_f32_16x16x32_bf16 v[84:87], v[188:191], a[208:211], v[84:87]\n"
      "s_mov_b32 m0, s64\n"
      "v_fma_f32 v43, s47, v43, -v67\n"
      "v_cvt_pk_bf16_f32 v22, v22, v26\n"
      "v_mfma_f32_16x16x32_bf16 v[88:91], v[200:203], a[212:215], v[88:91]\n"
      "s_nop 0\n"
      "buffer_store_dwordx4 a[228:231], v16, s[8:11], s58 offen offset:256 sc1\n"
      "ds_read_b128 a[204:207], v1 offset:4160\n"
      "v_mfma_f32_16x16x32_bf16 v[92:95], v[192:195], a[212:215], v[92:95]\n"
      "s_nop 0\n"
      "ds_write_addtid_b32 v21 offset:23552\n"
      "ds_read2st64_b32 a[232:233], v10 offset0:0 offset1:2\n"
      "ds_write_addtid_b32 v22 offset:27648\n"
      "v_cvt_pk_bf16_f32 v28, v28, v29\n"
      "v_mfma_f32_16x16x32_bf16 v[96:99], v[184:187], a[212:215], v[96:99]\n"
      "s_waitcnt lgkmcnt(4)\n"
      "ds_write_addtid_b32 v28 offset:33792\n"
      "ds_read2st64_b32 a[234:235], v13 offset0:0 offset1:2\n"
      "v_cvt_pk_bf16_f32 v30, v30, v31\n"
      "v_mfma_f32_16x16x32_bf16 v[76:79], v[180:183], a[192:195], v[76:79]\n"
      "ds_read_b128 a[208:211], v1 offset:128\n"
      "ds_write_addtid_b32 v30 offset:34304\n"
      "v_mfma_f32_16x16x32_bf16 v[80:83], v[172:175], a[192:195], v[80:83]\n"
      "v_cvt_pk_bf16_f32 v40, v40, v41\n"
      "v_cvt_pk_bf16_f32 v23, v23, v27\n"
      "v_cvt_pk_bf16_f32 v42, v42, v43\n"
      "v_mfma_f32_16x16x32_bf16 v[84:87], v[164:167], a[192:195], v[84:87]\n"
      "ds_write_addtid_b32 v40 offset:35840\n"
      "ds_write_addtid_b32 v42 offset:36352\n"
      "ds_write_addtid_b32 v23 offset:31744\n"
      "v_mfma_f32_16x16x32_bf16 v[88:91], v[176:179], a[196:199], v[88:91]\n"
      "ds_read_b128 a[212:215], v1 offset:4224\n"
      "v_fmac_f32 v44, s46, v32\n"
      "v_mfma_f32_16x16x32_bf16 v[92:95], v[168:171], a[196:199], v[92:95]\n"
      "v_fmac_f32 v45, s46, v33\n"
      "v_fmac_f32 v46, s46, v34\n"
      "v_mfma_f32_16x16x32_bf16 v[96:99], v[160:163], a[196:199], v[96:99]\n"
      "v_fmac_f32 v47, s46, v35\n"
      "v_fma_f32 v32, 0.5, v32, v56\n"
      "v_mfma_f32_16x16x32_bf16 v[76:79], v[156:159], a[200:203], v[76:79]\n"
      "ds_read_b128 a[192:195], v2 offset:0\n"
      "v_fma_f32 v33, 0.5, v33, v57\n"
      "v_mfma_f32_16x16x32_bf16 v[80:83], v[148:151], a[200:203], v[80:83]\n"
      "v_fma_f32 v34, 0.5, v34, v58\n"
      "v_fma_f32 v35, 0.5, v35, v59\n"
      "v_mfma_f32_16x16x32_bf16 v[84:87], v[140:143], a[200:203], v[84:87]\n"
      "s_waitcnt lgkmcnt(1)\n"
      "v_fmac_f32 v44, s48, v68\n"
      "v_fmac_f32 v45, s48, v69\n"
      "v_fmac_f32 v46, s48, v70\n"
      "v_mfma_f32_16x16x32_bf16 v[88:91], v[152:155], a[204:207], v[88:91]\n"
      "ds_read_b128 a[196:199], v2 offset:4096\n"
      "v_fmac_f32 v47, s48, v71\n"
      "v_mfma_f32_16x16x32_bf16 v[92:95], v[144:147], a[204:207], v[92:95]\n"
      "v_fmac_f32 v32, s45, v44\n"
      "v_fmac_f32 v33, s45, v45\n"
      "v_mfma_f32_16x16x32_bf16 v[96:99], v[136:139], a[204:207], v[96:99]\n"
      "buffer_store_dwordx4 a[232:235], v16, s[8:11], s59 offen offset:0 sc1\n"
      "ds_read_b64_tr_b16 a[248:249], v7 offset:20480\n"
      "v_mfma_f32_16x16x32_bf16 v[76:79], v[132:135], a[208:211], v[76:79]\n"
      "ds_read_b128 a[200:203], v2 offset:64\n"
      "ds_read_b64_tr_b16 a[250:251], v7 offset:20488\n"
      "v_fmac_f32 v34, s45, v46\n"
      "v_mfma_f32_16x16x32_bf16 v[80:83], v[124:127], a[208:211], v[80:83]\n"
      "v_fmac_f32 v35, s45, v47\n"
      "v_fma_f32 v44, s47, v44, -v68\n"
      "v_mfma_f32_16x16x32_bf16 v[84:87], v[116:119], a[208:211], v[84:87]\n"
      "v_fma_f32 v45, s47, v45, -v69\n"
      "v_fma_f32 v46, s47, v46, -v70\n"
      "v_mfma_f32_16x16x32_bf16 v[88:91], v[128:131], a[212:215], v[88:91]\n"
      "s_nop 0\n"
      "ds_read_b128 a[204:207], v2 offset:4160\n"
      "v_fma_f32 v47, s47, v47, -v71\n"
      "v_mfma_f32_16x16x32_bf16 v[92:95], v[120:123], a[212:215], v[92:95]\n"
      "s_nop 0\n"
      "v_cvt_pk_bf16_f32 v32, v32, v33\n"
      "v_cvt_pk_bf16_f32 v34, v34, v35\n"
      "v_mfma_f32_16x16x32_bf16 v[96:99], v[112:115], a[212:215], v[96:99]\n"
      "s_waitcnt lgkmcnt(1)\n"
      "ds_write_addtid_b32 v32 offset:41988\n"
      "ds_write_addtid_b32 v34 offset:42500\n"
      "v_cvt_pk_bf16_f32 v44, v44, v45\n"
      "v_mfma_f32_16x16x32_bf16 v[76:79], v[108:111], a[192:195], v[76:79]\n"
      "ds_read_b128 a[208:211], v2 offset:128\n"
      "ds_write_addtid_b32 v44 offset:44036\n"
      "v_cvt_pk_bf16_f32 v46, v46, v47\n"
      "v_mfma_f32_4x4x4_16b_bf16 v[20:23], a[216:217], a[248:249], 0\n"
      "v_fmac_f32 v48, s46, v36\n"
      "v_mfma_f32_4x4x4_16b_bf16 v[24:27], a[216:217], a[250:251], 0\n"
      "ds_write_addtid_b32 v46 offset:44548\n"
      "v_fmac_f32 v49, s46, v37\n"
      "v_fmac_f32 v50, s46, v38\n"
      "v_mfma_f32_16x16x32_bf16 v[80:83], v[100:103], a[192:195], v[80:83]\n"
      "v_fmac_f32 v51, s46, v39\n"
      "v_cvt_pk_bf16_f32 v20, v20, v24\n"
      "v_fma_f32 v36, 0.5, v36, v60\n"
      "v_mfma_f32_16x16x32_bf16 v[84:87], a[160:163], a[192:195], v[84:87]\n"
      "s_add_u32 s54, s54, 196608\n"
      "ds_write_addtid_b32 v20 offset:19968\n"
      "v_fma_f32 v37, 0.5, v37, v61\n"
      "v_mfma_f32_16x16x32_bf16 v[88:91], v[104:107], a[196:199], v[88:91]\n"
      "s_add_u32 m0, s37, 86536\n"
      "ds_read_b128 a[212:215], v2 offset:4224\n"
      "ds_read2st64_b32 a[236:237], v11 offset0:0 offset1:2\n"
      "ds_read2st64_b32 a[238:239], v14 offset0:0 offset1:2\n"
      "v_mfma_f32_16x16x32_bf16 v[92:95], a[164:167], a[196:199], v[92:95]\n"
      "buffer_load_dwordx4 v8, s[4:7], s54 offen offset:0 nt lds\n"
      "buffer_load_dwordx4 v9, s[4:7], s54 offen offset:1024 nt lds\n"
      "v_mfma_f32_16x16x32_bf16 v[96:99], a[156:159], a[196:199], v[96:99]\n"
      "s_mov_b32 m0, s64\n"
      "v_fma_f32 v38, 0.5, v38, v62\n"
      "v_fma_f32 v39, 0.5, v39, v63\n"
      "v_cvt_pk_bf16_f32 v21, v21, v25\n"
      "v_mfma_f32_16x16x32_bf16 v[76:79], a[152:155], a[200:203], v[76:79]\n"
      "s_nop 0\n"
      "ds_read_b128 a[192:195], v3 offset:0\n"
      "ds_write_addtid_b32 v21 offset:24064\n"
      "v_mfma_f32_16x16x32_bf16 v[80:83], a[144:147], a[200:203], v[80:83]\n"
      "v_fmac_f32 v48, s48, v72\n"
      "v_fmac_f32 v49, s48, v73\n"
      "v_cvt_pk_bf16_f32 v22, v22, v26\n"
      "v_mfma_f32_16x16x32_bf16 v[84:87], a[136:139], a[200:203], v[84:87]\n"
      "s_waitcnt lgkmcnt(2)\n"
      "ds_write_addtid_b32 v22 offset:28160\n"
      "v_fmac_f32 v50, s48, v74\n"
      "v_mfma_f32_16x16x32_bf16 v[88:91], a[148:151], a[204:207], v[88:91]\n"
      "ds_read_b128 a[196:199], v3 offset:64\n"
      "v_fmac_f32 v51, s48, v75\n"
      "v_mfma_f32_16x16x32_bf16 v[92:95], a[140:143], a[204:207], v[92:95]\n"
      "v_fmac_f32 v36, s45, v48\n"
      "v_cvt_pk_bf16_f32 v23, v23, v27\n"
      "v_mfma_f32_16x16x32_bf16 v[96:99], a[132:135], a[204:207], v[96:99]\n"
      "ds_write_addtid_b32 v23 offset:32256\n"
      "ds_read2st64_b32 a[240:241], v12 offset0:0 offset1:2\n"
      "ds_read2st64_b32 a[242:243], v15 offset0:0 offset1:2\n"
      "v_mfma_f32_16x16x32_bf16 v[76:79], a[128:131], a[208:211], v[76:79]\n"
      "ds_read_b128 a[200:203], v3 offset:128\n"
      "v_fmac_f32 v37, s45, v49\n"
      "v_fmac_f32 v38, s45, v50\n"
      "v_mfma_f32_16x16x32_bf16 v[80:83], a[120:123], a[208:211], v[80:83]\n"
      "v_fmac_f32 v39, s45, v51\n"
      "v_fma_f32 v48, s47, v48, -v72\n"
      "v_mfma_f32_16x16x32_bf16 v[84:87], a[112:115], a[208:211], v[84:87]\n"
      "s_nop 0\n"
      "v_fma_f32 v49, s47, v49, -v73\n"
      "v_fma_f32 v50, s47, v50, -v74\n"
      "v_mfma_f32_16x16x32_bf16 v[88:91], a[124:127], a[212:215], v[88:91]\n"
      "s_add_u32 s54, s54, 196608\n"
      "ds_read_b128 a[204:207], v4 offset:0\n"
      "v_fma_f32 v51, s47, v51, -v75\n"
      "v_mfma_f32_16x16x32_bf16 v[92:95], a[116:119], a[212:215], v[92:95]\n"
      "s_add_u32 m0, s37, 90760\n"
      "v_cvt_pk_bf16_f32 v36, v36, v37\n"
      "v_cvt_pk_bf16_f32 v38, v38, v39\n"
      "v_mfma_f32_16x16x32_bf16 v[96:99], a[108:111], a[212:215], v[96:99]\n"
      "s_waitcnt lgkmcnt(1)\n"
      "buffer_store_dwordx4 a[236:239], v16, s[8:11], s59 offen offset:128 sc1\n"
      "buffer_load_dwordx4 v8, s[4:7], s54 offen offset:0 nt lds\n"
      "buffer_load_dwordx4 v9, s[4:7], s54 offen offset:1024 nt lds\n"
      "v_mfma_f32_16x16x32_bf16 v[52:55], a[0:3], a[192:195], 0\n"
      "s_mov_b32 m0, s64\n"
      "ds_read_b128 a[208:211], v4 offset:64\n"
      "ds_write_addtid_b32 v36 offset:37888\n"
      "ds_write_addtid_b32 v38 offset:38400\n"
      "v_add_f32 v76, v76, v88\n"
      "v_mfma_f32_16x16x32_bf16 v[56:59], a[4:7], a[192:195], 0\n"
      "ds_write_addtid_b32 v76 offset:45312\n"
      "v_add_f32 v77, v77, v89\n"
      "v_mfma_f32_16x16x32_bf16 v[60:63], a[8:11], a[192:195], 0\n"
      "ds_write_addtid_b32 v77 offset:45824\n"
      "v_add_f32 v78, v78, v90\n"
      "v_add_f32 v79, v79, v91\n"
      "v_mfma_f32_16x16x32_bf16 v[52:55], a[12:15], a[196:199], v[52:55]\n"
      "ds_read_b128 a[212:215], v4 offset:128\n"
      "ds_write_addtid_b32 v78 offset:46336\n"
      "v_mfma_f32_16x16x32_bf16 v[56:59], a[16:19], a[196:199], v[56:59]\n"
      "ds_write_addtid_b32 v79 offset:46848\n"
      "v_add_f32 v80, v80, v92\n"
      "v_cvt_pk_bf16_f32 v48, v48, v49\n"
      "v_mfma_f32_16x16x32_bf16 v[60:63], a[20:23], a[196:199], v[60:63]\n"
      "s_nop 0\n"
      "ds_write_addtid_b32 v48 offset:39936\n"
      "ds_write_addtid_b32 v80 offset:47360\n"
      "v_mfma_f32_16x16x32_bf16 v[52:55], a[24:27], a[200:203], v[52:55]\n"
      "ds_read_b128 a[192:195], v5 offset:0\n"
      "v_add_f32 v81, v81, v93\n"
      "v_mfma_f32_16x16x32_bf16 v[56:59], a[28:31], a[200:203], v[56:59]\n"
      "buffer_store_dwordx4 a[240:243], v16, s[8:11], s59 offen offset:256 sc1\n"
      "ds_write_addtid_b32 v81 offset:47872\n"
      "v_mfma_f32_16x16x32_bf16 v[60:63], a[32:35], a[200:203], v[60:63]\n"
      "s_waitcnt lgkmcnt(4)\n"
      "v_add_f32 v82, v82, v94\n"
      "v_add_f32 v83, v83, v95\n"
      "v_mfma_f32_16x16x32_bf16 v[52:55], a[36:39], a[204:207], v[52:55]\n"
      "ds_read_b128 a[196:199], v5 offset:64\n"
      "ds_write_addtid_b32 v82 offset:48384\n"
      "ds_write_addtid_b32 v83 offset:48896\n"
      "v_cvt_pk_bf16_f32 v50, v50, v51\n"
      "v_mfma_f32_16x16x32_bf16 v[56:59], a[40:43], a[204:207], v[56:59]\n"
      "ds_write_addtid_b32 v50 offset:40448\n"
      "v_add_f32 v84, v84, v96\n"
      "v_mfma_f32_16x16x32_bf16 v[60:63], a[44:47], a[204:207], v[60:63]\n"
      "ds_write_addtid_b32 v84 offset:49408\n"
      "v_add_f32 v85, v85, v97\n"
      "v_add_f32 v86, v86, v98\n"
      "v_add_f32 v87, v87, v99\n"
      "v_mfma_f32_16x16x32_bf16 v[52:55], a[48:51], a[208:211], v[52:55]\n"
      "ds_read_b128 a[200:203], v5 offset:128\n"
      "ds_write_addtid_b32 v85 offset:49920\n"
      "v_mfma_f32_16x16x32_bf16 v[56:59], a[52:55], a[208:211], v[56:59]\n"
      "ds_write_addtid_b32 v86 offset:50432\n"
      "ds_write_addtid_b32 v87 offset:50944\n"
      "v_mfma_f32_16x16x32_bf16 v[60:63], a[56:59], a[208:211], v[60:63]\n"
      "v_fma_f32 v40, -2.0, v88, v76\n"
      "v_fma_f32 v41, -2.0, v89, v77\n"
      "v_mfma_f32_16x16x32_bf16 v[52:55], a[60:63], a[212:215], v[52:55]\n"
      "v_fma_f32 v42, -2.0, v90, v78\n"
      "v_fma_f32 v43, -2.0, v91, v79\n"
      "v_mfma_f32_16x16x32_bf16 v[56:59], a[64:67], a[212:215], v[56:59]\n"
      "s_waitcnt vmcnt(25)\n"
      "v_fma_f32 v44, -2.0, v92, v80\n"
      "v_fma_f32 v45, -2.0, v93, v81\n"
      "v_mfma_f32_16x16x32_bf16 v[60:63], a[68:71], a[212:215], v[60:63]\n"
      "s_waitcnt lgkmcnt(0)\n"
      "s_barrier\n"
      "s_add_u32 s55, s55, 2\n"
      "s_cmp_lt_u32 s55, 6\n"
      "s_cbranch_scc1 LDloop_%=\n"
      "LDlast_%=:\n"
      "v_mfma_f32_16x16x32_bf16 v[52:55], a[72:75], a[192:195], v[52:55]\n"
      "s_add_u32 s58, s40, 0\n"
      "ds_read_b128 a[204:207], v0 offset:16384\n"
      "ds_read2st64_b32 a[220:221], v10 offset0:181 offset1:183\n"
      "v_fma_f32 v46, -2.0, v94, v82\n"
      "v_mfma_f32_16x16x32_bf16 v[56:59], a[76:79], a[192:195], v[56:59]\n"
      "s_mov_b32 s40, s62\n"
      "ds_read_b128 a[208:211], v0 offset:20480\n"
      "ds_read2st64_b32 v[28:29], v6 offset0:0 offset1:2\n"
      "v_mfma_f32_16x16x32_bf16 v[60:63], a[80:83], a[192:195], v[60:63]\n"
      "s_add_u32 s62, s62, 786432\n"
      "ds_read2st64_b32 a[222:223], v13 offset0:181 offset1:183\n"
      "ds_read2st64_b32 v[30:31], v6 offset0:4 offset1:6\n"
      "v_mfma_f32_16x16x32_bf16 v[52:55], a[84:87], a[196:199], v[52:55]\n"
      "s_add_u32 s59, s58, 196608\n"
      "ds_read_b128 a[212:215], v0 offset:16448\n"
      "v_fma_f32 v47, -2.0, v95, v83\n"
      "v_mfma_f32_16x16x32_bf16 v[56:59], a[88:91], a[196:199], v[56:59]\n"
      "ds_read2st64_b32 v[32:33], v6 offset0:8 offset1:10\n"
      "v_fma_f32 v48, -2.0, v96, v84\n"
      "v_fma_f32 v49, -2.0, v97, v85\n"
      "v_mfma_f32_16x16x32_bf16 v[60:63], a[92:95], a[196:199], v[60:63]\n"
      "ds_read2st64_b32 v[34:35], v6 offset0:12 offset1:14\n"
      "v_fma_f32 v50, -2.0, v98, v86\n"
      "v_mfma_f32_16x16x32_bf16 v[52:55], a[96:99], a[200:203], v[52:55]\n"
      "ds_read_b128 a[192:195], v0 offset:20544\n"
      "v_fma_f32 v51, -2.0, v99, v87\n"
      "v_mfma_f32_16x16x32_bf16 v[56:59], a[100:103], a[200:203], v[56:59]\n"
      "ds_read2st64_b32 v[36:37], v6 offset0:16 offset1:18\n"
      "v_mfma_f32_16x16x32_bf16 v[60:63], a[104:107], a[200:203], v[60:63]\n"
      "s_waitcnt lgkmcnt(7)\n"
      "ds_read_b64_tr_b16 a[244:245], v7 offset:36864\n"
      "ds_read_b64_tr_b16 a[246:247], v7 offset:36872\n"
      "v_mfma_f32_16x16x32_bf16 v[76:79], v[252:255], a[204:207], 0\n"
      "s_nop 0\n"
      "ds_read_b128 a[196:199], v0 offset:16512\n"
      "ds_read2st64_b32 v[38:39], v6 offset0:20 offset1:22\n"
      "v_mfma_f32_16x16x32_bf16 v[80:83], v[244:247], a[204:207], 0\n"
      "v_mfma_f32_16x16x32_bf16 v[84:87], v[236:239], a[204:207], 0\n"
      "s_nop 0\n"
      "ds_read2st64_b32 a[224:225], v11 offset0:181 offset1:183\n"
      "v_mfma_f32_16x16x32_bf16 v[88:91], v[248:251], a[208:211], 0\n"
      "s_waitcnt lgkmcnt(7)\n"
      "ds_read_b128 a[200:203], v0 offset:20608\n"
      "ds_read2st64_b32 a[226:227], v14 offset0:181 offset1:183\n"
      "v_fmac_f32 v40, s46, v28\n"
      "v_fmac_f32 v41, s46, v29\n"
      "v_fmac_f32 v42, s46, v30\n"
      "v_fmac_f32 v43, s46, v31\n"
      "v_mfma_f32_16x16x32_bf16 v[92:95], v[240:243], a[208:211], 0\n"
      "buffer_store_dwordx4 a[220:223], v16, s[8:11], s58 offen offset:0 sc1\n"
      "v_fma_f32 v28, 0.5, v28, v64\n"
      "v_mfma_f32_16x16x32_bf16 v[96:99], v[232:235], a[208:211], 0\n"
      "v_fma_f32 v29, 0.5, v29, v65\n"
      "v_fma_f32 v30, 0.5, v30, v66\n"
      "v_fma_f32 v31, 0.5, v31, v67\n"
      "v_fmac_f32 v40, s48, v52\n"
      "v_mfma_f32_16x16x32_bf16 v[76:79], v[228:231], a[212:215], v[76:79]\n"
      "s_waitcnt lgkmcnt(3)\n"
      "ds_read_b128 a[204:207], v1 offset:16384\n"
      "v_fmac_f32 v41, s48, v53\n"
      "v_mfma_f32_4x4x4_16b_bf16 v[20:23], a[216:217], a[244:245], 0\n"
      "v_fmac_f32 v42, s48, v54\n"
      "v_mfma_f32_4x4x4_16b_bf16 v[24:27], a[216:217], a[246:247], 0\n"
      "v_fmac_f32 v43, s48, v55\n"
      "v_fmac_f32 v28, s45, v40\n"
      "v_mfma_f32_16x16x32_bf16 v[80:83], v[220:223], a[212:215], v[80:83]\n"
      "v_fmac_f32 v29, s45, v41\n"
      "v_fmac_f32 v30, s45, v42\n"
      "v_cvt_pk_bf16_f32 v20, v20, v24\n"
      "v_mfma_f32_16x16x32_bf16 v[84:87], v[212:215], a[212:215], v[84:87]\n"
      "ds_write_addtid_b32 v20 offset:3072\n"
      "v_fmac_f32 v31, s45, v43\n"
      "v_mfma_f32_16x16x32_bf16 v[88:91], v[224:227], a[192:195], v[88:91]\n"
      "s_nop 0\n"
      "ds_read_b128 a[208:211], v1 offset:20480\n"
      "v_cvt_pk_bf16_f32 v21, v21, v25\n"
      "v_mfma_f32_16x16x32_bf16 v[92:95], v[216:219], a[192:195], v[92:95]\n"
      "ds_write_addtid_b32 v21 offset:7168\n"
      "v_fma_f32 v40, s47, v40, -v52\n"
      "v_mfma_f32_16x16x32_bf16 v[96:99], v[208:211], a[192:195], v[96:99]\n"
      "v_fma_f32 v41, s47, v41, -v53\n"
      "v_fma_f32 v42, s47, v42, -v54\n"
      "v_mfma_f32_16x16x32_bf16 v[76:79], v[204:207], a[196:199], v[76:79]\n"
      "ds_read_b128 a[212:215], v1 offset:16448\n"
      "v_cvt_pk_bf16_f32 v22, v22, v26\n"
      "v_mfma_f32_16x16x32_bf16 v[80:83], v[196:199], a[196:199], v[80:83]\n"
      "s_nop 0\n"
      "ds_write_addtid_b32 v22 offset:11264\n"
      "v_fma_f32 v43, s47, v43, -v55\n"
      "v_mfma_f32_16x16x32_bf16 v[84:87], v[188:191], a[196:199], v[84:87]\n"
      "s_waitcnt lgkmcnt(3)\n"
      "v_cvt_pk_bf16_f32 v28, v28, v29\n"
      "v_cvt_pk_bf16_f32 v30, v30, v31\n"
      "v_mfma_f32_16x16x32_bf16 v[88:91], v[200:203], a[200:203], v[88:91]\n"
      "buffer_store_dwordx4 a[224:227], v16, s[8:11], s58 offen offset:128 sc1\n"
      "ds_read_b128 a[192:195], v1 offset:20544\n"
      "v_mfma_f32_16x16x32_bf16 v[92:95], v[192:195], a[200:203], v[92:95]\n"
      "ds_write_addtid_b32 v28 offset:32768\n"
      "ds_write_addtid_b32 v30 offset:33280\n"
      "v_cvt_pk_bf16_f32 v40, v40, v41\n"
      "v_cvt_pk_bf16_f32 v23, v23, v27\n"
      "v_mfma_f32_16x16x32_bf16 v[96:99], v[184:187], a[200:203], v[96:99]\n"
      "ds_write_addtid_b32 v40 offset:34816\n"
      "ds_read2st64_b32 a[228:229], v12 offset0:181 offset1:183\n"
      "v_cvt_pk_bf16_f32 v42, v42, v43\n"
      "v_mfma_f32_16x16x32_bf16 v[76:79], v[180:183], a[204:207], v[76:79]\n"
      "ds_read_b128 a[196:199], v1 offset:16512\n"
      "ds_write_addtid_b32 v42 offset:35328\n"
      "v_mfma_f32_16x16x32_bf16 v[80:83], v[172:175], a[204:207], v[80:83]\n"
      "ds_write_addtid_b32 v23 offset:15360\n"
      "ds_read2st64_b32 a[230:231], v15 offset0:181 offset1:183\n"
      "v_fmac_f32 v44, s46, v32\n"
      "v_mfma_f32_16x16x32_bf16 v[84:87], v[164:167], a[204:207], v[84:87]\n"
      "v_fmac_f32 v45, s46, v33\n"
      "v_fmac_f32 v46, s46, v34\n"
      "v_mfma_f32_16x16x32_bf16 v[88:91], v[176:179], a[208:211], v[88:91]\n"
      "s_nop 0\n"
      "ds_read_b128 a[200:203], v1 offset:20608\n"
      "v_fmac_f32 v47, s46, v35\n"
      "v_mfma_f32_16x16x32_bf16 v[92:95], v[168:171], a[208:211], v[92:95]\n"
      "v_fma_f32 v32, 0.5, v32, v68\n"
      "v_fma_f32 v33, 0.5, v33, v69\n"
      "v_mfma_f32_16x16x32_bf16 v[96:99], v[160:163], a[208:211], v[96:99]\n"
      "s_waitcnt lgkmcnt(5)\n"
      "v_fma_f32 v34, 0.5, v34, v70\n"
      "v_fma_f32 v35, 0.5, v35, v71\n"
      "v_mfma_f32_16x16x32_bf16 v[76:79], v[156:159], a[212:215], v[76:79]\n"
      "ds_read_b128 a[204:207], v2 offset:16384\n"
      "v_fmac_f32 v44, s48, v56\n"
      "v_mfma_f32_16x16x32_bf16 v[80:83], v[148:151], a[212:215], v[80:83]\n"
      "v_fmac_f32 v45, s48, v57\n"
      "v_fmac_f32 v46, s48, v58\n"
      "v_mfma_f32_16x16x32_bf16 v[84:87], v[140:143], a[212:215], v[84:87]\n"
      "v_fmac_f32 v47, s48, v59\n"
      "v_fmac_f32 v32, s45, v44\n"
      "v_mfma_f32_16x16x32_bf16 v[88:91], v[152:155], a[192:195], v[88:91]\n"
      "s_nop 0\n"
      "ds_read_b128 a[208:211], v2 offset:20480\n"
      "ds_read_b64_tr_b16 a[248:249], v7 offset:37376\n"
      "v_mfma_f32_16x16x32_bf16 v[92:95], v[144:147], a[192:195], v[92:95]\n"
      "s_waitcnt lgkmcnt(3)\n"
      "ds_read_b64_tr_b16 a[250:251], v7 offset:37384\n"
      "v_fmac_f32 v33, s45, v45\n"
      "v_fmac_f32 v34, s45, v46\n"
      "v_mfma_f32_16x16x32_bf16 v[96:99], v[136:139], a[192:195], v[96:99]\n"
      "s_nop 0\n"
      "buffer_store_dwordx4 a[228:231], v16, s[8:11], s58 offen offset:256 sc1\n"
      "v_mfma_f32_16x16x32_bf16 v[76:79], v[132:135], a[196:199], v[76:79]\n"
      "ds_read_b128 a[212:215], v2 offset:16448\n"
      "v_fmac_f32 v35, s45, v47\n"
      "v_mfma_f32_16x16x32_bf16 v[80:83], v[124:127], a[196:199], v[80:83]\n"
      "v_fma_f32 v44, s47, v44, -v56\n"
      "v_fma_f32 v45, s47, v45, -v57\n"
      "v_mfma_f32_16x16x32_bf16 v[84:87], v[116:119], a[196:199], v[84:87]\n"
      "s_nop 0\n"
      "v_fma_f32 v46, s47, v46, -v58\n"
      "v_fma_f32 v47, s47, v47, -v59\n"
      "v_mfma_f32_16x16x32_bf16 v[88:91], v[128:131], a[200:203], v[88:91]\n"
      "s_nop 0\n"
      "ds_read_b128 a[192:195], v2 offset:20544\n"
      "v_cvt_pk_bf16_f32 v32, v32, v33\n"
      "v_mfma_f32_16x16x32_bf16 v[92:95], v[120:123], a[200:203], v[92:95]\n"
      "s_waitcnt lgkmcnt(2)\n"
      "ds_write_addtid_b32 v32 offset:40964\n"
      "v_cvt_pk_bf16_f32 v34, v34, v35\n"
      "v_mfma_f32_4x4x4_16b_bf16 v[20:23], a[216:217], a[248:249], 0\n"
      "v_cvt_pk_bf16_f32 v44, v44, v45\n"
      "v_mfma_f32_4x4x4_16b_bf16 v[24:27], a[216:217], a[250:251], 0\n"
      "ds_write_addtid_b32 v34 offset:41476\n"
      "ds_write_addtid_b32 v44 offset:43012\n"
      "v_cvt_pk_bf16_f32 v46, v46, v47\n"
      "v_fmac_f32 v48, s46, v36\n"
      "v_mfma_f32_16x16x32_bf16 v[96:99], v[112:115], a[200:203], v[96:99]\n"
      "s_nop 0\n"
      "ds_write_addtid_b32 v46 offset:43524\n"
      "v_fmac_f32 v49, s46, v37\n"
      "v_cvt_pk_bf16_f32 v20, v20, v24\n"
      "v_fmac_f32 v50, s46, v38\n"
      "v_mfma_f32_16x16x32_bf16 v[76:79], v[108:111], a[204:207], v[76:79]\n"
      "ds_read_b128 a[196:199], v2 offset:16512\n"
      "ds_write_addtid_b32 v20 offset:3584\n"
      "v_mfma_f32_16x16x32_bf16 v[80:83], v[100:103], a[204:207], v[80:83]\n"
      "v_fmac_f32_e64 v51, s46, v39\n"
      "v_fma_f32 v36, 0.5, v36, v72\n"
      "v_mfma_f32_16x16x32_bf16 v[84:87], a[160:163], a[204:207], v[84:87]\n"
      "v_fma_f32 v37, 0.5, v37, v73\n"
      "v_cvt_pk_bf16_f32 v21, v21, v25\n"
      "v_mfma_f32_16x16x32_bf16 v[88:91], v[104:107], a[208:211], v[88:91]\n"
      "ds_read_b128 a[200:203], v2 offset:20608\n"
      "ds_write_addtid_b32 v21 offset:7680\n"
      "v_fma_f32 v38, 0.5, v38, v74\n"
      "v_mfma_f32_16x16x32_bf16 v[92:95], a[164:167], a[208:211], v[92:95]\n"
      "ds_read2st64_b32 a[232:233], v10 offset0:4 offset1:6\n"
      "ds_read2st64_b32 a[234:235], v13 offset0:4 offset1:6\n"
      "v_mfma_f32_16x16x32_bf16 v[96:99], a[156:159], a[208:211], v[96:99]\n"
      "s_waitcnt lgkmcnt(4)\n"
      "v_fma_f32 v39, 0.5, v39, v75\n"
      "v_fmac_f32 v48, s48, v60\n"
      "v_cvt_pk_bf16_f32 v22, v22, v26\n"
      "v_mfma_f32_16x16x32_bf16 v[76:79], a[152:155], a[212:215], v[76:79]\n"
      "ds_read_b128 a[204:207], v3 offset:16896\n"
      "ds_write_addtid_b32 v22 offset:11776\n"
      "v_mfma_f32_16x16x32_bf16 v[80:83], a[144:147], a[212:215], v[80:83]\n"
      "v_fmac_f32 v49, s48, v61\n"
      "v_fmac_f32 v50, s48, v62\n"
      "v_mfma_f32_16x16x32_bf16 v[84:87], a[136:139], a[212:215], v[84:87]\n"
      "v_fmac_f32 v51, s48, v63\n"
      "v_cvt_pk_bf16_f32 v23, v23, v27\n"
      "v_mfma_f32_16x16x32_bf16 v[88:91], a[148:151], a[192:195], v[88:91]\n"
      "s_nop 0\n"
      "ds_read_b128 a[208:211], v3 offset:16960\n"
      "ds_write_addtid_b32 v23 offset:15872\n"
      "v_fmac_f32 v36, s45, v48\n"
      "v_mfma_f32_16x16x32_bf16 v[92:95], a[140:143], a[192:195], v[92:95]\n"
      "v_fmac_f32 v37, s45, v49\n"
      "v_fmac_f32 v38, s45, v50\n"
      "v_mfma_f32_16x16x32_bf16 v[96:99], a[132:135], a[192:195], v[96:99]\n"
      "s_nop 0\n"
      "ds_read2st64_b32 a[236:237], v11 offset0:4 offset1:6\n"
      "ds_read2st64_b32 a[238:239], v14 offset0:4 offset1:6\n"
      "v_mfma_f32_16x16x32_bf16 v[76:79], a[128:131], a[196:199], v[76:79]\n"
      "ds_read_b128 a[212:215], v3 offset:17024\n"
      "v_fmac_f32 v39, s45, v51\n"
      "v_mfma_f32_16x16x32_bf16 v[80:83], a[120:123], a[196:199], v[80:83]\n"
      "v_fma_f32 v48, s47, v48, -v60\n"
      "v_fma_f32 v49, s47, v49, -v61\n"
      "v_mfma_f32_16x16x32_bf16 v[84:87], a[112:115], a[196:199], v[84:87]\n"
      "s_waitcnt lgkmcnt(3)\n"
      "v_fma_f32 v50, s47, v50, -v62\n"
      "v_fma_f32 v51, s47, v51, -v63\n"
      "v_mfma_f32_16x16x32_bf16 v[88:91], a[124:127], a[200:203], v[88:91]\n"
      "ds_read_b128 a[192:195], v4 offset:16896\n"
      "v_cvt_pk_bf16_f32 v36, v36, v37\n"
      "v_mfma_f32_16x16x32_bf16 v[92:95], a[116:119], a[200:203], v[92:95]\n"
      "ds_write_addtid_b32 v36 offset:36864\n"
      "v_cvt_pk_bf16_f32 v38, v38, v39\n"
      "v_mfma_f32_16x16x32_bf16 v[96:99], a[108:111], a[200:203], v[96:99]\n"
      "buffer_store_dwordx4 a[232:235], v16, s[8:11], s59 offen offset:0 sc1\n"
      "ds_write_addtid_b32 v38 offset:37376\n"
      "v_mfma_f32_16x16x32_bf16 v[64:67], a[0:3], a[204:207], 0\n"
      "ds_read_b128 a[196:199], v4 offset:16960\n"
      "v_add_f32 v76, v76, v88\n"
      "v_mfma_f32_16x16x32_bf16 v[68:71], a[4:7], a[204:207], 0\n"
      "ds_write_addtid_b32 v76 offset:51456\n"
      "v_add_f32 v77, v77, v89\n"
      "v_add_f32_e64 v78, v78, v90\n"
      "v_mfma_f32_16x16x32_bf16 v[72:75], a[8:11], a[204:207], 0\n"
      "ds_write_addtid_b32 v77 offset:51968\n"
      "ds_write_addtid_b32 v78 offset:52480\n"
      "v_add_f32 v79, v79, v91\n"
      "v_mfma_f32_16x16x32_bf16 v[64:67], a[12:15], a[208:211], v[64:67]\n"
      "ds_read_b128 a[200:203], v4 offset:17024\n"
      "ds_write_addtid_b32 v79 offset:52992\n"
      "v_mfma_f32_16x16x32_bf16 v[68:71], a[16:19], a[208:211], v[68:71]\n"
      "v_add_f32 v80, v80, v92\n"
      "v_cvt_pk_bf16_f32 v48, v48, v49\n"
      "v_add_f32 v81, v81, v93\n"
      "v_mfma_f32_16x16x32_bf16 v[72:75], a[20:23], a[208:211], v[72:75]\n"
      "s_waitcnt lgkmcnt(4)\n"
      "ds_write_addtid_b32 v48 offset:38912\n"
      "ds_write_addtid_b32 v80 offset:53504\n"
      "v_mfma_f32_16x16x32_bf16 v[64:67], a[24:27], a[212:215], v[64:67]\n"
      "ds_read_b128 a[204:207], v5 offset:16896\n"
      "ds_write_addtid_b32 v81 offset:54016\n"
      "v_mfma_f32_16x16x32_bf16 v[68:71], a[28:31], a[212:215], v[68:71]\n"
      "buffer_store_dwordx4 a[236:239], v16, s[8:11], s59 offen offset:128 sc1\n"
      "v_add_f32 v82, v82, v94\n"
      "v_mfma_f32_16x16x32_bf16 v[72:75], a[32:35], a[212:215], v[72:75]\n"
      "ds_write_addtid_b32 v82 offset:54528\n"
      "v_add_f32 v83, v83, v95\n"
      "v_mfma_f32_16x16x32_bf16 v[64:67], a[36:39], a[192:195], v[64:67]\n"
      "ds_read_b128 a[208:211], v5 offset:16960\n"
      "ds_write_addtid_b32 v83 offset:55040\n"
      "v_cvt_pk_bf16_f32 v50, v50, v51\n"
      "v_mfma_f32_16x16x32_bf16 v[68:71], a[40:43], a[192:195], v[68:71]\n"
      "ds_write_addtid_b32 v50 offset:39424\n"
      "v_add_f32 v84, v84, v96\n"
      "v_mfma_f32_16x16x32_bf16 v[72:75], a[44:47], a[192:195], v[72:75]\n"
      "ds_write_addtid_b32 v84 offset:55552\n"
      "v_add_f32 v85, v85, v97\n"
      "v_add_f32 v86, v86, v98\n"
      "v_add_f32 v87, v87, v99\n"
      "v_mfma_f32_16x16x32_bf16 v[64:67], a[48:51], a[196:199], v[64:67]\n"
      "ds_read_b128 a[212:215], v5 offset:17024\n"
      "ds_write_addtid_b32 v85 offset:56064\n"
      "v_mfma_f32_16x16x32_bf16 v[68:71], a[52:55], a[196:199], v[68:71]\n"
      "s_nop 0\n"
      "ds_write_addtid_b32 v86 offset:56576\n"
      "ds_write_addtid_b32 v87 offset:57088\n"
      "v_mfma_f32_16x16x32_bf16 v[72:75], a[56:59], a[196:199], v[72:75]\n"
      "s_waitcnt lgkmcnt(5)\n"
      "v_fma_f32 v40, -2.0, v88, v76\n"
      "v_fma_f32 v41, -2.0, v89, v77\n"
      "v_mfma_f32_16x16x32_bf16 v[64:67], a[60:63], a[200:203], v[64:67]\n"
      "s_nop 0\n"
      "v_fma_f32 v42, -2.0, v90, v78\n"
      "v_fma_f32 v43, -2.0, v91, v79\n"
      "v_mfma_f32_16x16x32_bf16 v[68:71], a[64:67], a[200:203], v[68:71]\n"
      "s_waitcnt vmcnt(16)\n"
      "v_fma_f32 v44, -2.0, v92, v80\n"
      "v_fma_f32 v45, -2.0, v93, v81\n"
      "v_mfma_f32_16x16x32_bf16 v[72:75], a[68:71], a[200:203], v[72:75]\n"
      "s_waitcnt lgkmcnt(0)\n"
      "s_barrier\n"
      "v_mfma_f32_16x16x32_bf16 v[64:67], a[72:75], a[204:207], v[64:67]\n"
      "s_add_u32 s58, s40, 0\n"
      "ds_read_b128 a[192:195], v0 offset:0\n"
      "ds_read2st64_b32 a[220:221], v10 offset0:177 offset1:179\n"
      "v_fma_f32 v46, -2.0, v94, v82\n"
      "v_mfma_f32_16x16x32_bf16 v[68:71], a[76:79], a[204:207], v[68:71]\n"
      "s_mov_b32 s40, s62\n"
      "ds_read_b128 a[196:199], v0 offset:4096\n"
      "ds_read2st64_b32 v[28:29], v6 offset0:24 offset1:26\n"
      "v_mfma_f32_16x16x32_bf16 v[72:75], a[80:83], a[204:207], v[72:75]\n"
      "s_add_u32 s62, s62, 786432\n"
      "ds_read2st64_b32 a[222:223], v13 offset0:177 offset1:179\n"
      "ds_read2st64_b32 v[30:31], v6 offset0:28 offset1:30\n"
      "v_mfma_f32_16x16x32_bf16 v[64:67], a[84:87], a[208:211], v[64:67]\n"
      "s_add_u32 s59, s58, 196608\n"
      "ds_read_b128 a[200:203], v0 offset:64\n"
      "v_fma_f32 v47, -2.0, v95, v83\n"
      "v_mfma_f32_16x16x32_bf16 v[68:71], a[88:91], a[208:211], v[68:71]\n"
      "ds_read2st64_b32 v[32:33], v6 offset0:32 offset1:34\n"
      "v_fma_f32 v48, -2.0, v96, v84\n"
      "v_fma_f32 v49, -2.0, v97, v85\n"
      "v_mfma_f32_16x16x32_bf16 v[72:75], a[92:95], a[208:211], v[72:75]\n"
      "ds_read2st64_b32 v[34:35], v6 offset0:36 offset1:38\n"
      "v_fma_f32 v50, -2.0, v98, v86\n"
      "v_mfma_f32_16x16x32_bf16 v[64:67], a[96:99], a[212:215], v[64:67]\n"
      "ds_read_b128 a[204:207], v0 offset:4160\n"
      "v_fma_f32 v51, -2.0, v99, v87\n"
      "v_mfma_f32_16x16x32_bf16 v[68:71], a[100:103], a[212:215], v[68:71]\n"
      "s_nop 0\n"
      "ds_read2st64_b32 v[36:37], v6 offset0:40 offset1:42\n"
      "v_mfma_f32_16x16x32_bf16 v[72:75], a[104:107], a[212:215], v[72:75]\n"
      "s_waitcnt lgkmcnt(7)\n"
      "ds_read_b64_tr_b16 a[244:245], v7 offset:53760\n"
      "ds_read_b64_tr_b16 a[246:247], v7 offset:53768\n"
      "v_mfma_f32_16x16x32_bf16 v[76:79], v[252:255], a[192:195], 0\n"
      "ds_read_b128 a[208:211], v0 offset:128\n"
      "ds_read2st64_b32 v[38:39], v6 offset0:44 offset1:46\n"
      "v_mfma_f32_16x16x32_bf16 v[80:83], v[244:247], a[192:195], 0\n"
      "v_mfma_f32_16x16x32_bf16 v[84:87], v[236:239], a[192:195], 0\n"
      "ds_read2st64_b32 a[224:225], v11 offset0:177 offset1:179\n"
      "v_mfma_f32_16x16x32_bf16 v[88:91], v[248:251], a[196:199], 0\n"
      "s_waitcnt lgkmcnt(7)\n"
      "ds_read_b128 a[212:215], v0 offset:4224\n"
      "ds_read2st64_b32 a[226:227], v14 offset0:177 offset1:179\n"
      "v_fmac_f32 v40, s46, v28\n"
      "v_fmac_f32 v41, s46, v29\n"
      "v_fmac_f32 v42, s46, v30\n"
      "v_fmac_f32 v43, s46, v31\n"
      "v_mfma_f32_16x16x32_bf16 v[92:95], v[240:243], a[196:199], 0\n"
      "s_nop 0\n"
      "buffer_store_dwordx4 a[220:223], v16, s[8:11], s58 offen offset:0 sc1\n"
      "v_fma_f32 v28, 0.5, v28, v52\n"
      "v_mfma_f32_16x16x32_bf16 v[96:99], v[232:235], a[196:199], 0\n"
      "v_fma_f32 v29, 0.5, v29, v53\n"
      "v_fma_f32 v30, 0.5, v30, v54\n"
      "v_fma_f32 v31, 0.5, v31, v55\n"
      "v_fmac_f32 v40, s48, v64\n"
      "v_mfma_f32_16x16x32_bf16 v[76:79], v[228:231], a[200:203], v[76:79]\n"
      "s_waitcnt lgkmcnt(3)\n"
      "ds_read_b128 a[192:195], v1 offset:0\n"
      "v_fmac_f32 v41, s48, v65\n"
      "v_mfma_f32_4x4x4_16b_bf16 v[20:23], a[216:217], a[244:245], 0\n"
      "v_fmac_f32 v42, s48, v66\n"
      "v_mfma_f32_4x4x4_16b_bf16 v[24:27], a[216:217], a[246:247], 0\n"
      "v_fmac_f32 v43, s48, v67\n"
      "v_fmac_f32 v28, s45, v40\n"
      "v_mfma_f32_16x16x32_bf16 v[80:83], v[220:223], a[200:203], v[80:83]\n"
      "v_fmac_f32 v29, s45, v41\n"
      "v_fmac_f32 v30, s45, v42\n"
      "v_cvt_pk_bf16_f32 v20, v20, v24\n"
      "v_mfma_f32_16x16x32_bf16 v[84:87], v[212:215], a[200:203], v[84:87]\n"
      "ds_write_addtid_b32 v20 offset:19456\n"
      "v_fmac_f32 v31, s45, v43\n"
      "v_mfma_f32_16x16x32_bf16 v[88:91], v[224:227], a[204:207], v[88:91]\n"
      "ds_read_b128 a[196:199], v1 offset:4096\n"
      "v_cvt_pk_bf16_f32 v21, v21, v25\n"
      "v_mfma_f32_16x16x32_bf16 v[92:95], v[216:219], a[204:207], v[92:95]\n"
      "s_nop 0\n"
      "ds_write_addtid_b32 v21 offset:23552\n"
      "v_fma_f32 v40, s47, v40, -v64\n"
      "v_mfma_f32_16x16x32_bf16 v[96:99], v[208:211], a[204:207], v[96:99]\n"
      "v_fma_f32 v41, s47, v41, -v65\n"
      "v_fma_f32 v42, s47, v42, -v66\n"
      "v_mfma_f32_16x16x32_bf16 v[76:79], v[204:207], a[208:211], v[76:79]\n"
      "ds_read_b128 a[200:203], v1 offset:64\n"
      "v_cvt_pk_bf16_f32 v22, v22, v26\n"
      "v_mfma_f32_16x16x32_bf16 v[80:83], v[196:199], a[208:211], v[80:83]\n"
      "ds_write_addtid_b32 v22 offset:27648\n"
      "v_fma_f32 v43, s47, v43, -v67\n"
      "v_mfma_f32_16x16x32_bf16 v[84:87], v[188:191], a[208:211], v[84:87]\n"
      "s_waitcnt lgkmcnt(3)\n"
      "v_cvt_pk_bf16_f32 v28, v28, v29\n"
      "v_cvt_pk_bf16_f32 v30, v30, v31\n"
      "v_mfma_f32_16x16x32_bf16 v[88:91], v[200:203], a[212:215], v[88:91]\n"
      "s_nop 0\n"
      "buffer_store_dwordx4 a[224:227], v16, s[8:11], s58 offen offset:128 sc1\n"
      "ds_read_b128 a[204:207], v1 offset:4160\n"
      "v_mfma_f32_16x16x32_bf16 v[92:95], v[192:195], a[212:215], v[92:95]\n"
      "ds_write_addtid_b32 v28 offset:33792\n"
      "ds_write_addtid_b32 v30 offset:34304\n"
      "v_cvt_pk_bf16_f32 v40, v40, v41\n"
      "v_cvt_pk_bf16_f32 v23, v23, v27\n"
      "v_mfma_f32_16x16x32_bf16 v[96:99], v[184:187], a[212:215], v[96:99]\n"
      "ds_write_addtid_b32 v40 offset:35840\n"
      "ds_read2st64_b32 a[228:229], v12 offset0:177 offset1:179\n"
      "v_cvt_pk_bf16_f32 v42, v42, v43\n"
      "v_mfma_f32_16x16x32_bf16 v[76:79], v[180:183], a[192:195], v[76:79]\n"
      "ds_read_b128 a[208:211], v1 offset:128\n"
      "ds_write_addtid_b32 v42 offset:36352\n"
      "v_mfma_f32_16x16x32_bf16 v[80:83], v[172:175], a[192:195], v[80:83]\n"
      "ds_write_addtid_b32 v23 offset:31744\n"
      "ds_read2st64_b32 a[230:231], v15 offset0:177 offset1:179\n"
      "v_fmac_f32_e64 v44, s46, v32\n"
      "v_mfma_f32_16x16x32_bf16 v[84:87], v[164:167], a[192:195], v[84:87]\n"
      "v_fmac_f32 v45, s46, v33\n"
      "v_fmac_f32 v46, s46, v34\n"
      "v_mfma_f32_16x16x32_bf16 v[88:91], v[176:179], a[196:199], v[88:91]\n"
      "ds_read_b128 a[212:215], v1 offset:4224\n"
      "v_fmac_f32 v47, s46, v35\n"
      "v_mfma_f32_16x16x32_bf16 v[92:95], v[168:171], a[196:199], v[92:95]\n"
      "s_nop 0\n"
      "v_fma_f32 v32, 0.5, v32, v56\n"
      "v_fma_f32 v33, 0.5, v33, v57\n"
      "v_mfma_f32_16x16x32_bf16 v[96:99], v[160:163], a[196:199], v[96:99]\n"
      "s_waitcnt lgkmcnt(5)\n"
      "v_fma_f32 v34, 0.5, v34, v58\n"
      "v_fma_f32 v35, 0.5, v35, v59\n"
      "v_mfma_f32_16x16x32_bf16 v[76:79], v[156:159], a[200:203], v[76:79]\n"
      "ds_read_b128 a[192:195], v2 offset:0\n"
      "v_fmac_f32 v44, s48, v68\n"
      "v_mfma_f32_16x16x32_bf16 v[80:83], v[148:151], a[200:203], v[80:83]\n"
      "v_fmac_f32 v45, s48, v69\n"
      "v_fmac_f32 v46, s48, v70\n"
      "v_mfma_f32_16x16x32_bf16 v[84:87], v[140:143], a[200:203], v[84:87]\n"
      "v_fmac_f32 v47, s48, v71\n"
      "v_fmac_f32 v32, s45, v44\n"
      "v_mfma_f32_16x16x32_bf16 v[88:91], v[152:155], a[204:207], v[88:91]\n"
      "ds_read_b128 a[196:199], v2 offset:4096\n"
      "ds_read_b64_tr_b16 a[248:249], v7 offset:54272\n"
      "v_mfma_f32_16x16x32_bf16 v[92:95], v[144:147], a[204:207], v[92:95]\n"
      "s_waitcnt lgkmcnt(3)\n"
      "ds_read_b64_tr_b16 a[250:251], v7 offset:54280\n"
      "v_fmac_f32 v33, s45, v45\n"
      "v_fmac_f32 v34, s45, v46\n"
      "v_mfma_f32_16x16x32_bf16 v[96:99], v[136:139], a[204:207], v[96:99]\n"
      "buffer_store_dwordx4 a[228:231], v16, s[8:11], s58 offen offset:256 sc1\n"
      "v_mfma_f32_16x16x32_bf16 v[76:79], v[132:135], a[208:211], v[76:79]\n"
      "ds_read_b128 a[200:203], v2 offset:64\n"
      "v_fmac_f32 v35, s45, v47\n"
      "v_mfma_f32_16x16x32_bf16 v[80:83], v[124:127], a[208:211], v[80:83]\n"
      "v_fma_f32 v44, s47, v44, -v68\n"
      "v_fma_f32 v45, s47, v45, -v69\n"
      "v_mfma_f32_16x16x32_bf16 v[84:87], v[116:119], a[208:211], v[84:87]\n"
      "v_fma_f32 v46, s47, v46, -v70\n"
      "v_fma_f32 v47, s47, v47, -v71\n"
      "v_mfma_f32_16x16x32_bf16 v[88:91], v[128:131], a[212:215], v[88:91]\n"
      "s_nop 0\n"
      "ds_read_b128 a[204:207], v2 offset:4160\n"
      "v_cvt_pk_bf16_f32 v32, v32, v33\n"
      "v_mfma_f32_16x16x32_bf16 v[92:95], v[120:123], a[212:215], v[92:95]\n"
      "s_waitcnt lgkmcnt(2)\n"
      "ds_write_addtid_b32 v32 offset:41988\n"
      "v_cvt_pk_bf16_f32 v34, v34, v35\n"
      "v_mfma_f32_4x4x4_16b_bf16 v[20:23], a[216:217], a[248:249], 0\n"
      "v_cvt_pk_bf16_f32 v44, v44, v45\n"
      "v_mfma_f32_4x4x4_16b_bf16 v[24:27], a[216:217], a[250:251], 0\n"
      "ds_write_addtid_b32 v34 offset:42500\n"
      "ds_write_addtid_b32 v44 offset:44036\n"
      "v_cvt_pk_bf16_f32 v46, v46, v47\n"
      "v_fmac_f32 v48, s46, v36\n"
      "v_mfma_f32_16x16x32_bf16 v[96:99], v[112:115], a[212:215], v[96:99]\n"
      "ds_write_addtid_b32 v46 offset:44548\n"
      "v_fmac_f32 v49, s46, v37\n"
      "v_cvt_pk_bf16_f32 v20, v20, v24\n"
      "v_fmac_f32 v50, s46, v38\n"
      "v_mfma_f32_16x16x32_bf16 v[76:79], v[108:111], a[192:195], v[76:79]\n"
      "s_nop 0\n"
      "ds_read_b128 a[208:211], v2 offset:128\n"
      "ds_write_addtid_b32 v20 offset:19968\n"
      "v_mfma_f32_16x16x32_bf16 v[80:83], v[100:103], a[192:195], v[80:83]\n"
      "v_fmac_f32 v51, s46, v39\n"
      "v_fma_f32 v36, 0.5, v36, v60\n"
      "v_mfma_f32_16x16x32_bf16 v[84:87], a[160:163], a[192:195], v[84:87]\n"
      "v_fma_f32 v37, 0.5, v37, v61\n"
      "v_cvt_pk_bf16_f32 v21, v21, v25\n"
      "v_mfma_f32_16x16x32_bf16 v[88:91], v[104:107], a[196:199], v[88:91]\n"
      "s_nop 0\n"
      "ds_read_b128 a[212:215], v2 offset:4224\n"
      "ds_write_addtid_b32 v21 offset:24064\n"
      "v_fma_f32 v38, 0.5, v38, v62\n"
      "v_mfma_f32_16x16x32_bf16 v[92:95], a[164:167], a[196:199], v[92:95]\n"
      "ds_read2st64_b32 a[232:233], v10 offset0:0 offset1:2\n"
      "ds_read2st64_b32 a[234:235], v13 offset0:0 offset1:2\n"
      "v_mfma_f32_16x16x32_bf16 v[96:99], a[156:159], a[196:199], v[96:99]\n"
      "s_waitcnt lgkmcnt(4)\n"
      "v_fma_f32 v39, 0.5, v39, v63\n"
      "v_fmac_f32 v48, s48, v72\n"
      "v_cvt_pk_bf16_f32 v22, v22, v26\n"
      "v_mfma_f32_16x16x32_bf16 v[76:79], a[152:155], a[200:203], v[76:79]\n"
      "ds_read_b128 a[192:195], v3 offset:33792\n"
      "ds_write_addtid_b32 v22 offset:28160\n"
      "v_mfma_f32_16x16x32_bf16 v[80:83], a[144:147], a[200:203], v[80:83]\n"
      "v_fmac_f32 v49, s48, v73\n"
      "v_fmac_f32 v50, s48, v74\n"
      "v_mfma_f32_16x16x32_bf16 v[84:87], a[136:139], a[200:203], v[84:87]\n"
      "v_fmac_f32_e64 v51, s48, v75\n"
      "v_cvt_pk_bf16_f32 v23, v23, v27\n"
      "v_mfma_f32_16x16x32_bf16 v[88:91], a[148:151], a[204:207], v[88:91]\n"
      "ds_read_b128 a[196:199], v3 offset:33856\n"
      "ds_write_addtid_b32 v23 offset:32256\n"
      "v_fmac_f32 v36, s45, v48\n"
      "v_mfma_f32_16x16x32_bf16 v[92:95], a[140:143], a[204:207], v[92:95]\n"
      "v_fmac_f32 v37, s45, v49\n"
      "v_fmac_f32 v38, s45, v50\n"
      "v_mfma_f32_16x16x32_bf16 v[96:99], a[132:135], a[204:207], v[96:99]\n"
      "s_nop 0\n"
      "ds_read2st64_b32 a[236:237], v11 offset0:0 offset1:2\n"
      "ds_read2st64_b32 a[238:239], v14 offset0:0 offset1:2\n"
      "v_mfma_f32_16x16x32_bf16 v[76:79], a[128:131], a[208:211], v[76:79]\n"
      "ds_read_b128 a[200:203], v3 offset:33920\n"
      "v_fmac_f32 v39, s45, v51\n"
      "v_mfma_f32_16x16x32_bf16 v[80:83], a[120:123], a[208:211], v[80:83]\n"
      "s_nop 0\n"
      "v_fma_f32 v48, s47, v48, -v72\n"
      "v_fma_f32 v49, s47, v49, -v73\n"
      "v_mfma_f32_16x16x32_bf16 v[84:87], a[112:115], a[208:211], v[84:87]\n"
      "s_waitcnt lgkmcnt(3)\n"
      "v_fma_f32 v50, s47, v50, -v74\n"
      "v_fma_f32 v51, s47, v51, -v75\n"
      "v_mfma_f32_16x16x32_bf16 v[88:91], a[124:127], a[212:215], v[88:91]\n"
      "ds_read_b128 a[204:207], v4 offset:33792\n"
      "v_cvt_pk_bf16_f32 v36, v36, v37\n"
      "v_mfma_f32_16x16x32_bf16 v[92:95], a[116:119], a[212:215], v[92:95]\n"
      "s_nop 0\n"
      "ds_write_addtid_b32 v36 offset:37888\n"
      "v_cvt_pk_bf16_f32 v38, v38, v39\n"
      "v_mfma_f32_16x16x32_bf16 v[96:99], a[108:111], a[212:215], v[96:99]\n"
      "buffer_store_dwordx4 a[232:235], v16, s[8:11], s59 offen offset:0 sc1\n"
      "ds_write_addtid_b32 v38 offset:38400\n"
      "v_mfma_f32_16x16x32_bf16 v[52:55], a[0:3], a[192:195], 0\n"
      "ds_read_b128 a[208:211], v4 offset:33856\n"
      "v_add_f32_e64 v76, v76, v88\n"
      "v_mfma_f32_16x16x32_bf16 v[56:59], a[4:7], a[192:195], 0\n"
      "ds_write_addtid_b32 v76 offset:45312\n"
      "v_add_f32 v77, v77, v89\n"
      "v_add_f32 v78, v78, v90\n"
      "v_mfma_f32_16x16x32_bf16 v[60:63], a[8:11], a[192:195], 0\n"
      "ds_write_addtid_b32 v77 offset:45824\n"
      "ds_write_addtid_b32 v78 offset:46336\n"
      "v_add_f32 v79, v79, v91\n"
      "v_mfma_f32_16x16x32_bf16 v[52:55], a[12:15], a[196:199], v[52:55]\n"
      "s_nop 0\n"
      "ds_read_b128 a[212:215], v4 offset:33920\n"
      "ds_write_addtid_b32 v79 offset:46848\n"
      "v_mfma_f32_16x16x32_bf16 v[56:59], a[16:19], a[196:199], v[56:59]\n"
      "v_add_f32 v80, v80, v92\n"
      "v_cvt_pk_bf16_f32 v48, v48, v49\n"
      "v_add_f32_e64 v81, v81, v93\n"
      "v_mfma_f32_16x16x32_bf16 v[60:63], a[20:23], a[196:199], v[60:63]\n"
      "s_waitcnt lgkmcnt(4)\n"
      "ds_write_addtid_b32 v48 offset:39936\n"
      "ds_write_addtid_b32 v80 offset:47360\n"
      "v_mfma_f32_16x16x32_bf16 v[52:55], a[24:27], a[200:203], v[52:55]\n"
      "ds_read_b128 a[192:195], v5 offset:33792\n"
      "ds_write_addtid_b32 v81 offset:47872\n"
      "v_mfma_f32_16x16x32_bf16 v[56:59], a[28:31], a[200:203], v[56:59]\n"
      "buffer_store_dwordx4 a[236:239], v16, s[8:11], s59 offen offset:128 sc1\n"
      "v_add_f32 v82, v82, v94\n"
      "v_mfma_f32_16x16x32_bf16 v[60:63], a[32:35], a[200:203], v[60:63]\n"
      "s_nop 0\n"
      "ds_write_addtid_b32 v82 offset:48384\n"
      "v_add_f32 v83, v83, v95\n"
      "v_mfma_f32_16x16x32_bf16 v[52:55], a[36:39], a[204:207], v[52:55]\n"
      "ds_read_b128 a[196:199], v5 offset:33856\n"
      "ds_write_addtid_b32 v83 offset:48896\n"
      "v_cvt_pk_bf16_f32 v50, v50, v51\n"
      "v_mfma_f32_16x16x32_bf16 v[56:59], a[40:43], a[204:207], v[56:59]\n"
      "ds_write_addtid_b32 v50 offset:40448\n"
      "v_add_f32 v84, v84, v96\n"
      "v_mfma_f32_16x16x32_bf16 v[60:63], a[44:47], a[204:207], v[60:63]\n"
      "ds_write_addtid_b32 v84 offset:49408\n"
      "v_add_f32 v85, v85, v97\n"
      "v_add_f32_e64 v86, v86, v98\n"
      "v_add_f32_e64 v87, v87, v99\n"
      "v_mfma_f32_16x16x32_bf16 v[52:55], a[48:51], a[208:211], v[52:55]\n"
      "s_nop 0\n"
      "ds_read_b128 a[200:203], v5 offset:33920\n"
      "ds_write_addtid_b32 v85 offset:49920\n"
      "v_mfma_f32_16x16x32_bf16 v[56:59], a[52:55], a[208:211], v[56:59]\n"
      "s_nop 0\n"
      "ds_write_addtid_b32 v86 offset:50432\n"
      "ds_write_addtid_b32 v87 offset:50944\n"
      "v_mfma_f32_16x16x32_bf16 v[60:63], a[56:59], a[208:211], v[60:63]\n"
      "s_waitcnt lgkmcnt(5)\n"
      "v_fma_f32 v40, -2.0, v88, v76\n"
      "v_fma_f32 v41, -2.0, v89, v77\n"
      "v_mfma_f32_16x16x32_bf16 v[52:55], a[60:63], a[212:215], v[52:55]\n"
      "s_nop 0\n"
      "v_fma_f32 v42, -2.0, v90, v78\n"
      "v_fma_f32 v43, -2.0, v91, v79\n"
      "v_mfma_f32_16x16x32_bf16 v[56:59], a[64:67], a[212:215], v[56:59]\n"
      "s_waitcnt vmcnt(11)\n"
      "v_fma_f32 v44, -2.0, v92, v80\n"
      "v_fma_f32 v45, -2.0, v93, v81\n"
      "v_mfma_f32_16x16x32_bf16 v[60:63], a[68:71], a[212:215], v[60:63]\n"
      "s_waitcnt lgkmcnt(0)\n"
      "s_barrier\n"
      "v_mfma_f32_16x16x32_bf16 v[52:55], a[72:75], a[192:195], v[52:55]\n"
      "s_add_u32 s58, s40, 0\n"
      "ds_read_b128 a[204:207], v0 offset:16384\n"
      "ds_read2st64_b32 v[28:29], v6 offset0:0 offset1:2\n"
      "v_fma_f32 v46, -2.0, v94, v82\n"
      "v_mfma_f32_16x16x32_bf16 v[56:59], a[76:79], a[192:195], v[56:59]\n"
      "s_mov_b32 s40, s62\n"
      "ds_read_b128 a[208:211], v0 offset:20480\n"
      "ds_read2st64_b32 v[30:31], v6 offset0:4 offset1:6\n"
      "v_mfma_f32_16x16x32_bf16 v[60:63], a[80:83], a[192:195], v[60:63]\n"
      "s_add_u32 s62, s62, 786432\n"
      "ds_read2st64_b32 v[32:33], v6 offset0:8 offset1:10\n"
      "ds_read2st64_b32 v[34:35], v6 offset0:12 offset1:14\n"
      "v_mfma_f32_16x16x32_bf16 v[52:55], a[84:87], a[196:199], v[52:55]\n"
      "ds_read_b128 a[212:215], v0 offset:16448\n"
      "v_fma_f32 v47, -2.0, v95, v83\n"
      "v_mfma_f32_16x16x32_bf16 v[56:59], a[88:91], a[196:199], v[56:59]\n"
      "v_fma_f32 v48, -2.0, v96, v84\n"
      "v_fma_f32 v49, -2.0, v97, v85\n"
      "v_mfma_f32_16x16x32_bf16 v[60:63], a[92:95], a[196:199], v[60:63]\n"
      "ds_read2st64_b32 v[36:37], v6 offset0:16 offset1:18\n"
      "ds_read2st64_b32 v[38:39], v6 offset0:20 offset1:22\n"
      "v_mfma_f32_16x16x32_bf16 v[52:55], a[96:99], a[200:203], v[52:55]\n"
      "ds_read_b128 a[192:195], v0 offset:20544\n"
      "ds_read2st64_b32 a[220:221], v10 offset0:181 offset1:183\n"
      "v_mfma_f32_16x16x32_bf16 v[56:59], a[100:103], a[200:203], v[56:59]\n"
      "ds_read2st64_b32 a[222:223], v13 offset0:181 offset1:183\n"
      "v_fma_f32 v50, -2.0, v98, v86\n"
      "v_fma_f32 v51, -2.0, v99, v87\n"
      "v_mfma_f32_16x16x32_bf16 v[60:63], a[104:107], a[200:203], v[60:63]\n"
      "s_waitcnt lgkmcnt(8)\n"
      "v_fmac_f32 v40, s46, v28\n"
      "v_fmac_f32 v41, s46, v29\n"
      "v_mfma_f32_16x16x32_bf16 v[76:79], v[252:255], a[204:207], 0\n"
      "s_nop 0\n"
      "ds_read_b128 a[196:199], v0 offset:16512\n"
      "v_fmac_f32 v42, s46, v30\n"
      "v_mfma_f32_16x16x32_bf16 v[80:83], v[244:247], a[204:207], 0\n"
      "v_fmac_f32 v43, s46, v31\n"
      "v_fma_f32 v28, 0.5, v28, v64\n"
      "v_mfma_f32_16x16x32_bf16 v[84:87], v[236:239], a[204:207], 0\n"
      "v_fma_f32 v29, 0.5, v29, v65\n"
      "v_fma_f32 v30, 0.5, v30, v66\n"
      "v_mfma_f32_16x16x32_bf16 v[88:91], v[248:251], a[208:211], 0\n"
      "ds_read_b128 a[200:203], v0 offset:20608\n"
      "v_fma_f32 v31, 0.5, v31, v67\n"
      "v_mfma_f32_16x16x32_bf16 v[92:95], v[240:243], a[208:211], 0\n"
      "v_fmac_f32 v40, s48, v52\n"
      "v_fmac_f32 v41, s48, v53\n"
      "v_mfma_f32_16x16x32_bf16 v[96:99], v[232:235], a[208:211], 0\n"
      "s_waitcnt lgkmcnt(2)\n"
      "v_fmac_f32 v42, s48, v54\n"
      "v_fmac_f32 v43, s48, v55\n"
      "v_mfma_f32_16x16x32_bf16 v[76:79], v[228:231], a[212:215], v[76:79]\n"
      "s_nop 0\n"
      "ds_read_b128 a[204:207], v1 offset:16384\n"
      "v_fmac_f32 v28, s45, v40\n"
      "v_mfma_f32_16x16x32_bf16 v[80:83], v[220:223], a[212:215], v[80:83]\n"
      "v_fmac_f32 v29, s45, v41\n"
      "v_fmac_f32 v30, s45, v42\n"
      "v_mfma_f32_16x16x32_bf16 v[84:87], v[212:215], a[212:215], v[84:87]\n"
      "v_fmac_f32 v31, s45, v43\n"
      "v_fma_f32 v40, s47, v40, -v52\n"
      "v_mfma_f32_16x16x32_bf16 v[88:91], v[224:227], a[192:195], v[88:91]\n"
      "ds_read_b128 a[208:211], v1 offset:20480\n"
      "v_fma_f32 v41, s47, v41, -v53\n"
      "v_mfma_f32_16x16x32_bf16 v[92:95], v[216:219], a[192:195], v[92:95]\n"
      "v_fma_f32 v42, s47, v42, -v54\n"
      "v_fma_f32 v43, s47, v43, -v55\n"
      "v_mfma_f32_16x16x32_bf16 v[96:99], v[208:211], a[192:195], v[96:99]\n"
      "s_waitcnt lgkmcnt(1)\n"
      "buffer_store_dwordx4 a[220:223], v16, s[8:11], s58 offen offset:0 sc1\n"
      "v_mfma_f32_16x16x32_bf16 v[76:79], v[204:207], a[196:199], v[76:79]\n"
      "s_nop 0\n"
      "ds_read_b128 a[212:215], v1 offset:16448\n"
      "v_cvt_pk_bf16_f32 v28, v28, v29\n"
      "v_mfma_f32_16x16x32_bf16 v[80:83], v[196:199], a[196:199], v[80:83]\n"
      "ds_write_addtid_b32 v28 offset:32768\n"
      "ds_read2st64_b32 a[224:225], v11 offset0:181 offset1:183\n"
      "v_cvt_pk_bf16_f32 v30, v30, v31\n"
      "v_mfma_f32_16x16x32_bf16 v[84:87], v[188:191], a[196:199], v[84:87]\n"
      "ds_write_addtid_b32 v30 offset:33280\n"
      "ds_read2st64_b32 a[226:227], v14 offset0:181 offset1:183\n"
      "v_cvt_pk_bf16_f32 v40, v40, v41\n"
      "v_mfma_f32_16x16x32_bf16 v[88:91], v[200:203], a[200:203], v[88:91]\n"
      "ds_read_b128 a[192:195], v1 offset:20544\n"
      "ds_write_addtid_b32 v40 offset:34816\n"
      "v_mfma_f32_16x16x32_bf16 v[92:95], v[192:195], a[200:203], v[92:95]\n"
      "v_cvt_pk_bf16_f32 v42, v42, v43\n"
      "v_fmac_f32_e64 v44, s46, v32\n"
      "v_mfma_f32_16x16x32_bf16 v[96:99], v[184:187], a[200:203], v[96:99]\n"
      "ds_write_addtid_b32 v42 offset:35328\n"
      "v_fmac_f32 v45, s46, v33\n"
      "v_mfma_f32_16x16x32_bf16 v[76:79], v[180:183], a[204:207], v[76:79]\n"
      "ds_read_b128 a[196:199], v1 offset:16512\n"
      "v_fmac_f32 v46, s46, v34\n"
      "v_mfma_f32_16x16x32_bf16 v[80:83], v[172:175], a[204:207], v[80:83]\n"
      "v_fmac_f32_e64 v47, s46, v35\n"
      "v_fma_f32 v32, 0.5, v32, v68\n"
      "v_mfma_f32_16x16x32_bf16 v[84:87], v[164:167], a[204:207], v[84:87]\n"
      "s_waitcnt lgkmcnt(4)\n"
      "v_fma_f32 v33, 0.5, v33, v69\n"
      "v_fma_f32 v34, 0.5, v34, v70\n"
      "v_mfma_f32_16x16x32_bf16 v[88:91], v[176:179], a[208:211], v[88:91]\n"
      "ds_read_b128 a[200:203], v1 offset:20608\n"
      "v_fma_f32 v35, 0.5, v35, v71\n"
      "v_fmac_f32 v44, s48, v56\n"
      "v_mfma_f32_16x16x32_bf16 v[92:95], v[168:171], a[208:211], v[92:95]\n"
      "v_fmac_f32 v45, s48, v57\n"
      "v_fmac_f32 v46, s48, v58\n"
      "v_mfma_f32_16x16x32_bf16 v[96:99], v[160:163], a[208:211], v[96:99]\n"
      "v_fmac_f32 v47, s48, v59\n"
      "v_fmac_f32 v32, s45, v44\n"
      "v_mfma_f32_16x16x32_bf16 v[76:79], v[156:159], a[212:215], v[76:79]\n"
      "ds_read_b128 a[204:207], v2 offset:16384\n"
      "v_fmac_f32 v33, s45, v45\n"
      "v_mfma_f32_16x16x32_bf16 v[80:83], v[148:151], a[212:215], v[80:83]\n"
      "v_fmac_f32 v34, s45, v46\n"
      "v_fmac_f32_e64 v35, s45, v47\n"
      "v_mfma_f32_16x16x32_bf16 v[84:87], v[140:143], a[212:215], v[84:87]\n"
      "s_waitcnt lgkmcnt(1)\n"
      "v_fma_f32 v44, s47, v44, -v56\n"
      "v_fma_f32 v45, s47, v45, -v57\n"
      "v_mfma_f32_16x16x32_bf16 v[88:91], v[152:155], a[192:195], v[88:91]\n"
      "buffer_store_dwordx4 a[224:227], v16, s[8:11], s58 offen offset:128 sc1\n"
      "ds_read_b128 a[208:211], v2 offset:20480\n"
      "v_mfma_f32_16x16x32_bf16 v[92:95], v[144:147], a[192:195], v[92:95]\n"
      "s_nop 0\n"
      "v_fma_f32 v46, s47, v46, -v58\n"
      "v_fma_f32 v47, s47, v47, -v59\n"
      "v_mfma_f32_16x16x32_bf16 v[96:99], v[136:139], a[192:195], v[96:99]\n"
      "ds_read2st64_b32 a[228:229], v12 offset0:181 offset1:183\n"
      "ds_read2st64_b32 a[230:231], v15 offset0:181 offset1:183\n"
      "v_cvt_pk_bf16_f32 v32, v32, v33\n"
      "v_mfma_f32_16x16x32_bf16 v[76:79], v[132:135], a[196:199], v[76:79]\n"
      "ds_read_b128 a[212:215], v2 offset:16448\n"
      "ds_write_addtid_b32 v32 offset:40964\n"
      "v_mfma_f32_16x16x32_bf16 v[80:83], v[124:127], a[196:199], v[80:83]\n"
      "v_cvt_pk_bf16_f32 v34, v34, v35\n"
      "v_cvt_pk_bf16_f32 v44, v44, v45\n"
      "v_mfma_f32_16x16x32_bf16 v[84:87], v[116:119], a[196:199], v[84:87]\n"
      "ds_write_addtid_b32 v34 offset:41476\n"
      "ds_write_addtid_b32 v44 offset:43012\n"
      "v_mfma_f32_16x16x32_bf16 v[88:91], v[128:131], a[200:203], v[88:91]\n"
      "ds_read_b128 a[192:195], v2 offset:20544\n"
      "v_cvt_pk_bf16_f32 v46, v46, v47\n"
      "v_mfma_f32_16x16x32_bf16 v[92:95], v[120:123], a[200:203], v[92:95]\n"
      "ds_write_addtid_b32 v46 offset:43524\n"
      "v_fmac_f32 v48, s46, v36\n"
      "v_fmac_f32 v49, s46, v37\n"
      "v_mfma_f32_16x16x32_bf16 v[96:99], v[112:115], a[200:203], v[96:99]\n"
      "s_waitcnt lgkmcnt(4)\n"
      "v_fmac_f32 v50, s46, v38\n"
      "v_fmac_f32_e64 v51, s46, v39\n"
      "v_mfma_f32_16x16x32_bf16 v[76:79], v[108:111], a[204:207], v[76:79]\n"
      "ds_read_b128 a[196:199], v2 offset:16512\n"
      "v_fma_f32 v36, 0.5, v36, v72\n"
      "v_mfma_f32_16x16x32_bf16 v[80:83], v[100:103], a[204:207], v[80:83]\n"
      "v_fma_f32 v37, 0.5, v37, v73\n"
      "v_fma_f32 v38, 0.5, v38, v74\n"
      "v_mfma_f32_16x16x32_bf16 v[84:87], a[160:163], a[204:207], v[84:87]\n"
      "v_fma_f32 v39, 0.5, v39, v75\n"
      "v_fmac_f32 v48, s48, v60\n"
      "v_mfma_f32_16x16x32_bf16 v[88:91], v[104:107], a[208:211], v[88:91]\n"
      "ds_read_b128 a[200:203], v2 offset:20608\n"
      "v_fmac_f32 v49, s48, v61\n"
      "v_mfma_f32_16x16x32_bf16 v[92:95], a[164:167], a[208:211], v[92:95]\n"
      "v_fmac_f32 v50, s48, v62\n"
      "v_fmac_f32 v51, s48, v63\n"
      "v_mfma_f32_16x16x32_bf16 v[96:99], a[156:159], a[208:211], v[96:99]\n"
      "v_fmac_f32 v36, s45, v48\n"
      "v_fmac_f32 v37, s45, v49\n"
      "v_mfma_f32_16x16x32_bf16 v[76:79], a[152:155], a[212:215], v[76:79]\n"
      "ds_read_b128 a[204:207], v3 offset:50688\n"
      "v_fmac_f32 v38, s45, v50\n"
      "v_fmac_f32 v39, s45, v51\n"
      "v_mfma_f32_16x16x32_bf16 v[80:83], a[144:147], a[212:215], v[80:83]\n"
      "s_nop 0\n"
      "buffer_store_dwordx4 a[228:231], v16, s[8:11], s58 offen offset:256 sc1\n"
      "v_mfma_f32_16x16x32_bf16 v[84:87], a[136:139], a[212:215], v[84:87]\n"
      "s_waitcnt lgkmcnt(1)\n"
      "v_fma_f32 v48, s47, v48, -v60\n"
      "v_fma_f32 v49, s47, v49, -v61\n"
      "v_mfma_f32_16x16x32_bf16 v[88:91], a[148:151], a[192:195], v[88:91]\n"
      "ds_read_b128 a[208:211], v3 offset:50752\n"
      "v_fma_f32 v50, s47, v50, -v62\n"
      "v_mfma_f32_16x16x32_bf16 v[92:95], a[140:143], a[192:195], v[92:95]\n"
      "v_fma_f32 v51, s47, v51, -v63\n"
      "v_cvt_pk_bf16_f32 v36, v36, v37\n"
      "v_mfma_f32_16x16x32_bf16 v[96:99], a[132:135], a[192:195], v[96:99]\n"
      "ds_write_addtid_b32 v36 offset:36864\n"
      "v_cvt_pk_bf16_f32 v38, v38, v39\n"
      "v_mfma_f32_16x16x32_bf16 v[76:79], a[128:131], a[196:199], v[76:79]\n"
      "ds_read_b128 a[212:215], v3 offset:50816\n"
      "ds_write_addtid_b32 v38 offset:37376\n"
      "v_cvt_pk_bf16_f32 v48, v48, v49\n"
      "v_cvt_pk_bf16_f32 v50, v50, v51\n"
      "v_mfma_f32_16x16x32_bf16 v[80:83], a[120:123], a[196:199], v[80:83]\n"
      "ds_write_addtid_b32 v48 offset:38912\n"
      "ds_write_addtid_b32 v50 offset:39424\n"
      "v_mfma_f32_16x16x32_bf16 v[84:87], a[112:115], a[196:199], v[84:87]\n"
      "v_mfma_f32_16x16x32_bf16 v[88:91], a[124:127], a[200:203], v[88:91]\n"
      "ds_read_b128 a[192:195], v4 offset:50688\n"
      "v_mfma_f32_16x16x32_bf16 v[92:95], a[116:119], a[200:203], v[92:95]\n"
      "v_mfma_f32_16x16x32_bf16 v[96:99], a[108:111], a[200:203], v[96:99]\n"
      "s_waitcnt lgkmcnt(3)\n"
      "v_mfma_f32_16x16x32_bf16 v[64:67], a[0:3], a[204:207], 0\n"
      "s_nop 0\n"
      "ds_read_b128 a[196:199], v4 offset:50752\n"
      "v_mfma_f32_16x16x32_bf16 v[68:71], a[4:7], a[204:207], 0\n"
      "v_mfma_f32_16x16x32_bf16 v[72:75], a[8:11], a[204:207], 0\n"
      "v_add_f32 v76, v76, v88\n"
      "v_add_f32 v77, v77, v89\n"
      "v_add_f32 v78, v78, v90\n"
      "v_add_f32 v79, v79, v91\n"
      "v_add_f32 v80, v80, v92\n"
      "v_add_f32 v81, v81, v93\n"
      "v_add_f32 v82, v82, v94\n"
      "v_mfma_f32_16x16x32_bf16 v[64:67], a[12:15], a[208:211], v[64:67]\n"
      "s_nop 0\n"
      "ds_read_b128 a[200:203], v4 offset:50816\n"
      "ds_write_addtid_b32 v76 offset:51456\n"
      "ds_write_addtid_b32 v77 offset:51968\n"
      "v_add_f32 v83, v83, v95\n"
      "v_add_f32 v84, v84, v96\n"
      "v_add_f32 v85, v85, v97\n"
      "v_add_f32 v86, v86, v98\n"
      "v_mfma_f32_16x16x32_bf16 v[68:71], a[16:19], a[208:211], v[68:71]\n"
      "ds_write_addtid_b32 v78 offset:52480\n"
      "ds_write_addtid_b32 v79 offset:52992\n"
      "ds_write_addtid_b32 v80 offset:53504\n"
      "v_mfma_f32_16x16x32_bf16 v[72:75], a[20:23], a[208:211], v[72:75]\n"
      "ds_write_addtid_b32 v81 offset:54016\n"
      "ds_write_addtid_b32 v82 offset:54528\n"
      "ds_write_addtid_b32 v83 offset:55040\n"
      "v_add_f32 v87, v87, v99\n"
      "v_mfma_f32_16x16x32_bf16 v[64:67], a[24:27], a[212:215], v[64:67]\n"
      "s_nop 0\n"
      "ds_write_addtid_b32 v84 offset:55552\n"
      "ds_write_addtid_b32 v85 offset:56064\n"
      "v_mfma_f32_16x16x32_bf16 v[68:71], a[28:31], a[212:215], v[68:71]\n"
      "ds_write_addtid_b32 v86 offset:56576\n"
      "ds_write_addtid_b32 v87 offset:57088\n"
      "v_mfma_f32_16x16x32_bf16 v[72:75], a[32:35], a[212:215], v[72:75]\n"
      "s_waitcnt lgkmcnt(0)\n"
      "s_barrier\n"
      "v_mfma_f32_16x16x32_bf16 v[64:67], a[36:39], a[192:195], v[64:67]\n"
      "s_add_u32 s58, s58, 786432\n"
      "ds_read_b128 a[204:207], v5 offset:50688\n"
      "ds_read2st64_b32 v[28:29], v6 offset0:24 offset1:26\n"
      "v_mfma_f32_16x16x32_bf16 v[68:71], a[40:43], a[192:195], v[68:71]\n"
      "ds_read2st64_b32 v[30:31], v6 offset0:28 offset1:30\n"
      "ds_read2st64_b32 v[32:33], v6 offset0:32 offset1:34\n"
      "v_mfma_f32_16x16x32_bf16 v[72:75], a[44:47], a[192:195], v[72:75]\n"
      "ds_read2st64_b32 v[34:35], v6 offset0:36 offset1:38\n"
      "ds_read2st64_b32 v[36:37], v6 offset0:40 offset1:42\n"
      "v_mfma_f32_16x16x32_bf16 v[64:67], a[48:51], a[196:199], v[64:67]\n"
      "ds_read_b128 a[208:211], v5 offset:50752\n"
      "ds_read2st64_b32 v[38:39], v6 offset0:44 offset1:46\n"
      "v_mfma_f32_16x16x32_bf16 v[68:71], a[52:55], a[196:199], v[68:71]\n"
      "v_fma_f32 v40, -2.0, v88, v76\n"
      "v_fma_f32 v41, -2.0, v89, v77\n"
      "v_mfma_f32_16x16x32_bf16 v[72:75], a[56:59], a[196:199], v[72:75]\n"
      "ds_read2st64_b32 a[220:221], v10 offset0:177 offset1:179\n"
      "ds_read2st64_b32 a[222:223], v13 offset0:177 offset1:179\n"
      "v_mfma_f32_16x16x32_bf16 v[64:67], a[60:63], a[200:203], v[64:67]\n"
      "ds_read_b128 a[212:215], v5 offset:50816\n"
      "v_fma_f32 v42, -2.0, v90, v78\n"
      "v_mfma_f32_16x16x32_bf16 v[68:71], a[64:67], a[200:203], v[68:71]\n"
      "ds_read2st64_b32 a[224:225], v11 offset0:177 offset1:179\n"
      "ds_read2st64_b32 a[226:227], v14 offset0:177 offset1:179\n"
      "v_mfma_f32_16x16x32_bf16 v[72:75], a[68:71], a[200:203], v[72:75]\n"
      "s_waitcnt lgkmcnt(9)\n"
      "v_fma_f32 v43, -2.0, v91, v79\n"
      "v_fma_f32 v44, -2.0, v92, v80\n"
      "v_mfma_f32_16x16x32_bf16 v[64:67], a[72:75], a[204:207], v[64:67]\n"
      "s_waitcnt lgkmcnt(3)\n"
      "buffer_store_dwordx4 a[220:223], v16, s[8:11], s58 offen offset:0 sc1\n"
      "v_mfma_f32_16x16x32_bf16 v[68:71], a[76:79], a[204:207], v[68:71]\n"
      "v_fma_f32 v45, -2.0, v93, v81\n"
      "v_fma_f32 v46, -2.0, v94, v82\n"
      "v_mfma_f32_16x16x32_bf16 v[72:75], a[80:83], a[204:207], v[72:75]\n"
      "ds_read2st64_b32 a[228:229], v12 offset0:177 offset1:179\n"
      "ds_read2st64_b32 a[230:231], v15 offset0:177 offset1:179\n"
      "v_mfma_f32_16x16x32_bf16 v[64:67], a[84:87], a[208:211], v[64:67]\n"
      "s_waitcnt lgkmcnt(2)\n"
      "buffer_store_dwordx4 a[224:227], v16, s[8:11], s58 offen offset:128 sc1\n"
      "v_mfma_f32_16x16x32_bf16 v[68:71], a[88:91], a[208:211], v[68:71]\n"
      "s_nop 0\n"
      "v_fma_f32 v47, -2.0, v95, v83\n"
      "v_fma_f32 v48, -2.0, v96, v84\n"
      "v_mfma_f32_16x16x32_bf16 v[72:75], a[92:95], a[208:211], v[72:75]\n"
      "v_fma_f32 v49, -2.0, v97, v85\n"
      "v_fma_f32 v50, -2.0, v98, v86\n"
      "v_mfma_f32_16x16x32_bf16 v[64:67], a[96:99], a[212:215], v[64:67]\n"
      "s_waitcnt lgkmcnt(0)\n"
      "buffer_store_dwordx4 a[228:231], v16, s[8:11], s58 offen offset:256 sc1\n"
      "v_mfma_f32_16x16x32_bf16 v[68:71], a[100:103], a[212:215], v[68:71]\n"
      "s_nop 0\n"
      "v_fma_f32 v51, -2.0, v99, v87\n"
      "v_fmac_f32 v40, s46, v28\n"
      "v_mfma_f32_16x16x32_bf16 v[72:75], a[104:107], a[212:215], v[72:75]\n"
      "v_fmac_f32 v41, s46, v29\n"
      "v_fmac_f32 v42, s46, v30\n"
      "v_fmac_f32 v43, s46, v31\n"
      "v_fma_f32 v28, 0.5, v28, v52\n"
      "v_fma_f32 v29, 0.5, v29, v53\n"
      "v_fma_f32 v30, 0.5, v30, v54\n"
      "v_fma_f32 v31, 0.5, v31, v55\n"
      "v_fmac_f32 v44, s46, v32\n"
      "v_fmac_f32 v45, s46, v33\n"
      "v_pk_fma_f32 v[46:47], v[34:35], s[46:47], v[46:47] op_sel:[0,0,0] op_sel_hi:[1,0,1]\n"
      "v_pk_fma_f32 v[32:33], v[32:33], s[48:49], v[56:57] op_sel:[0,1,0] op_sel_hi:[1,1,1]\n"
      "v_pk_fma_f32 v[34:35], v[34:35], s[48:49], v[58:59] op_sel:[0,1,0] op_sel_hi:[1,1,1]\n"
      "v_pk_fma_f32 v[48:49], v[36:37], s[46:47], v[48:49] op_sel:[0,0,0] op_sel_hi:[1,0,1]\n"
      "v_pk_fma_f32 v[50:51], v[38:39], s[46:47], v[50:51] op_sel:[0,0,0] op_sel_hi:[1,0,1]\n"
      "v_pk_fma_f32 v[36:37], v[36:37], s[48:49], v[60:61] op_sel:[0,1,0] op_sel_hi:[1,1,1]\n"
      "v_pk_fma_f32 v[38:39], v[38:39], s[48:49], v[62:63] op_sel:[0,1,0] op_sel_hi:[1,1,1]\n"
      "v_pk_fma_f32 v[40:41], v[64:65], s[48:49], v[40:41] op_sel:[0,0,0] op_sel_hi:[1,0,1]\n"
      "v_pk_fma_f32 v[42:43], v[66:67], s[48:49], v[42:43] op_sel:[0,0,0] op_sel_hi:[1,0,1]\n"
      "v_pk_fma_f32 v[28:29], v[40:41], s[44:45], v[28:29] op_sel:[0,1,0] op_sel_hi:[1,1,1]\n"
      "v_pk_fma_f32 v[30:31], v[42:43], s[44:45], v[30:31] op_sel:[0,1,0] op_sel_hi:[1,1,1]\n"
      "v_pk_fma_f32 v[40:41], v[40:41], s[46:47], v[64:65] op_sel:[0,1,0] op_sel_hi:[1,1,1] neg_lo:[0,0,1] neg_hi:[0,0,1]\n"
      "v_pk_fma_f32 v[42:43], v[42:43], s[46:47], v[66:67] op_sel:[0,1,0] op_sel_hi:[1,1,1] neg_lo:[0,0,1] neg_hi:[0,0,1]\n"
      "v_cvt_pk_bf16_f32 v28, v28, v29\n"
      "v_cvt_pk_bf16_f32 v30, v30, v31\n"
      "v_cvt_pk_bf16_f32 v40, v40, v41\n"
      "v_cvt_pk_bf16_f32 v42, v42, v43\n"
      "v_pk_fma_f32 v[44:45], v[68:69], s[48:49], v[44:45] op_sel:[0,0,0] op_sel_hi:[1,0,1]\n"
      "v_pk_fma_f32 v[46:47], v[70:71], s[48:49], v[46:47] op_sel:[0,0,0] op_sel_hi:[1,0,1]\n"
      "v_pk_fma_f32 v[32:33], v[44:45], s[44:45], v[32:33] op_sel:[0,1,0] op_sel_hi:[1,1,1]\n"
      "v_pk_fma_f32 v[34:35], v[46:47], s[44:45], v[34:35] op_sel:[0,1,0] op_sel_hi:[1,1,1]\n"
      "v_pk_fma_f32 v[44:45], v[44:45], s[46:47], v[68:69] op_sel:[0,1,0] op_sel_hi:[1,1,1] neg_lo:[0,0,1] neg_hi:[0,0,1]\n"
      "v_pk_fma_f32 v[46:47], v[46:47], s[46:47], v[70:71] op_sel:[0,1,0] op_sel_hi:[1,1,1] neg_lo:[0,0,1] neg_hi:[0,0,1]\n"
      "v_cvt_pk_bf16_f32 v32, v32, v33\n"
      "v_cvt_pk_bf16_f32 v34, v34, v35\n"
      "ds_write_addtid_b32 v28 offset:33792\n"
      "ds_write_addtid_b32 v30 offset:34304\n"
      "ds_write_addtid_b32 v40 offset:35840\n"
      "ds_write_addtid_b32 v42 offset:36352\n"
      "ds_write_addtid_b32 v32 offset:41988\n"
      "ds_write_addtid_b32 v34 offset:42500\n"
      "v_cvt_pk_bf16_f32 v44, v44, v45\n"
      "v_cvt_pk_bf16_f32 v46, v46, v47\n"
      "v_pk_fma_f32 v[48:49], v[72:73], s[48:49], v[48:49] op_sel:[0,0,0] op_sel_hi:[1,0,1]\n"
      "v_pk_fma_f32 v[50:51], v[74:75], s[48:49], v[50:51] op_sel:[0,0,0] op_sel_hi:[1,0,1]\n"
      "v_pk_fma_f32 v[36:37], v[48:49], s[44:45], v[36:37] op_sel:[0,1,0] op_sel_hi:[1,1,1]\n"
      "v_pk_fma_f32 v[38:39], v[50:51], s[44:45], v[38:39] op_sel:[0,1,0] op_sel_hi:[1,1,1]\n"
      "v_pk_fma_f32 v[48:49], v[48:49], s[46:47], v[72:73] op_sel:[0,1,0] op_sel_hi:[1,1,1] neg_lo:[0,0,1] neg_hi:[0,0,1]\n"
      "v_pk_fma_f32 v[50:51], v[50:51], s[46:47], v[74:75] op_sel:[0,1,0] op_sel_hi:[1,1,1] neg_lo:[0,0,1] neg_hi:[0,0,1]\n"
      "v_cvt_pk_bf16_f32 v36, v36, v37\n"
      "v_cvt_pk_bf16_f32 v38, v38, v39\n"
      "v_cvt_pk_bf16_f32 v48, v48, v49\n"
      "v_cvt_pk_bf16_f32 v50, v50, v51\n"
      "ds_write_addtid_b32 v44 offset:44036\n"
      "ds_write_addtid_b32 v46 offset:44548\n"
      "ds_write_addtid_b32 v36 offset:37888\n"
      "ds_write_addtid_b32 v38 offset:38400\n"
      "ds_write_addtid_b32 v48 offset:39936\n"
      "ds_write_addtid_b32 v50 offset:40448\n"
      "s_waitcnt lgkmcnt(0)\n"
      "s_barrier\n"
      "ds_read2st64_b32 a[220:221], v10 offset0:181 offset1:183\n"
      "ds_read2st64_b32 a[222:223], v13 offset0:181 offset1:183\n"
      "ds_read2st64_b32 a[224:225], v11 offset0:181 offset1:183\n"
      "ds_read2st64_b32 a[226:227], v14 offset0:181 offset1:183\n"
      "ds_read2st64_b32 a[228:229], v12 offset0:181 offset1:183\n"
      "ds_read2st64_b32 a[230:231], v15 offset0:181 offset1:183\n"
      "s_add_u32 s58, s58, 786432\n"
      "s_waitcnt lgkmcnt(4)\n"
      "buffer_store_dwordx4 a[220:223], v16, s[8:11], s58 offen offset:0 sc1\n"
      "s_waitcnt lgkmcnt(2)\n"
      "buffer_store_dwordx4 a[224:227], v16, s[8:11], s58 offen offset:128 sc1\n"
      "s_waitcnt lgkmcnt(0)\n"
      "buffer_store_dwordx4 a[228:231], v16, s[8:11], s58 offen offset:256 sc1\n"
      "LDend_%=:\n"
      "s_branch Lfin_%=\n"
      ".p2align 6\n"
      "LS_%=:\n"
      "v_lshlrev_b32 v43, 4, v37\n"
      "v_lshlrev_b32 v44, 2, v37\n"
      "v_and_b32 v43, 16, v43\n"
      "v_and_or_b32 v43, v44, 8, v43\n"
      "v_mul_u32_u24 v44, 192, v38\n"
      "v_mul_u32_u24 v45, 1536, v39\n"
      "s_lshl_b32 s27, s18, 5\n"
      "v_lshlrev_b32 v46, 1, v36\n"
      "v_add3_u32 v43, v43, v44, v45\n"
      "v_bitop3_b32 v46, v46, s27, 32 bitop3:0x6c\n"
      "v_add_u32 v60, v43, v46\n"
      "v_add_u32 v61, 57344, v60\n"
      "v_add_u32 v62, 114688, v60\n"
      "s_lshl_b32 s26, s18, 3\n"
      "v_and_b32 v42, 15, v36\n"
      "v_xor_b32 v42, s26, v42\n"
      "s_lshl_b32 s26, s17, 13\n"
      "s_add_u32 s26, s26, 24832\n"
      "s_mul_i32 s27, s17, 12672\n"
      "v_mul_u32_u24 v44, 192, v42\n"
      "v_lshlrev_b32 v43, 3, v42\n"
      "v_and_b32 v45, 48, v36\n"
      "s_add_u32 s27, s27, 82432\n"
      "v_bitop3_b32 v46, v43, v45, 32 bitop3:0x6c\n"
      "v_add3_u32 v0, v46, v44, s26\n"
      "v_add3_u32 v3, v46, v44, s27\n"
      "v_add_u32 v43, 16, v43\n"
      "v_add_u32 v44, 384, v44\n"
      "v_bitop3_b32 v46, v43, v45, 32 bitop3:0x6c\n"
      "v_add3_u32 v1, v46, v44, s26\n"
      "v_add3_u32 v4, v46, v44, s27\n"
      "v_add_u32 v43, 16, v43\n"
      "v_add_u32 v44, 384, v44\n"
      "v_bitop3_b32 v46, v43, v45, 32 bitop3:0x6c\n"
      "v_add3_u32 v2, v46, v44, s26\n"
      "v_add3_u32 v5, v46, v44, s27\n"
      "v_lshlrev_b32 v43, 31, v36\n"
      "v_and_b32 v44, 2, v36\n"
      "v_or_b32 v43, 0x3f803f80, v43\n"
      "v_mul_u32_u24 v45, 0xc00080, v44\n"
      "v_lshl_add_u32 v44, v44, 22, v43\n"
      "v_add_u32 v45, v45, v43\n"
      "v_accvgpr_write_b32 a216, v44\n"
      "v_accvgpr_write_b32 a217, v45\n"
      "v_lshlrev_b32 v43, 3, v38\n"
      "s_mov_b32 s26, 806420736\n"
      "v_bfe_u32 v43, s26, v43, 8\n"
      "v_lshlrev_b32 v44, 12, v38\n"
      "s_lshl_b32 s28, s18, 8\n"
      "s_add_u32 s28, s28, 82432\n"
      "v_lshl_add_u32 v43, v43, 3, v44\n"
      "v_lshl_add_u32 v44, v39, 2, v37\n"
      "v_lshl_add_u32 v43, v44, 4, v43\n"
      "v_add_u32 v7, s28, v43\n"
      "s_mul_i32 s26, s17, 70144\n"
      "s_lshl_b32 s27, s18, 8\n"
      "s_sub_u32 s26, 70144, s26\n"
      "s_add_u32 s26, s26, s27\n"
      "v_lshl_add_u32 v6, v36, 2, s26\n"
      "v_lshrrev_b32 v43, 3, v36\n"
      "v_and_b32 v44, 7, v36\n"
      "v_mul_u32_u24 v45, 384, v43\n"
      "v_lshl_add_u32 v11, v44, 4, v45\n"
      "v_and_b32 v44, 2, v36\n"
      "v_lshlrev_b32 v43, 3, v43\n"
      "v_lshlrev_b32 v45, 4, v44\n"
      "v_xor_b32 v43, v43, v45\n"
      "v_lshl_add_u32 v43, v44, 7, v43\n"
      "v_and_b32 v44, 1, v36\n"
      "s_lshl_b32 s26, s18, 11\n"
      "v_lshl_add_u32 v43, v44, 6, v43\n"
      "s_add_u32 s26, s26, 12288\n"
      "v_bfe_u32 v44, v36, 2, 1\n"
      "v_add_u32 v43, s26, v43\n"
      "v_mul_i32_i24 v45, -4100, v44\n"
      "s_mov_b32 s27, 8200\n"
      "v_add3_u32 v8, v43, v45, s27\n"
      "v_add_u32 v9, 128, v8\n"
      "s_mul_i32 s62, s18, 393216\n"
      "s_add_u32 s62, s62, s19\n"
      "s_add_u32 s62, s62, s21\n"
      "s_mov_b32 s40, 0x80000000\n"
      "s_and_b32 s9, s9, 0xffff\n"
      "s_mov_b32 s45, 0xbeaaaaab\n"
      "s_mov_b32 s46, 0xbe800000\n"
      "s_mov_b32 s47, 0x3eaaaaab\n"
      "s_add_u32 m0, s63, 6144\n"
      "s_add_u32 s52, s63, 61440\n"
      "buffer_load_dwordx4 v17, s[12:15], s52 offen offset:0 lds\n"
      "buffer_load_dwordx4 v18, s[12:15], s52 offen offset:1024 lds\n"
      "buffer_load_dwordx4 v19, s[12:15], s52 offen offset:2048 lds\n"
      "s_add_u32 m0, s63, 12288\n"
      "s_add_u32 s52, s63, 116736\n"
      "buffer_load_dwordx4 v17, s[12:15], s52 offen offset:0 lds\n"
      "buffer_load_dwordx4 v18, s[12:15], s52 offen offset:1024 lds\n"
      "buffer_load_dwordx4 v19, s[12:15], s52 offen offset:2048 lds\n"
      "LSpro_%=:\n"
      "s_waitcnt vmcnt(6)\n"
      "s_barrier\n"
      "ds_read_b64_tr_b16 a[224:225], v60 offset:24832\n"
      "ds_read_b64_tr_b16 a[226:227], v60 offset:25600\n"
      "ds_read_b64_tr_b16 a[0:1], v62 offset:26112\n"
      "ds_read_b64_tr_b16 a[2:3], v62 offset:26880\n"
      "ds_read_b64_tr_b16 a[220:221], v60 offset:30976\n"
      "ds_read_b64_tr_b16 a[222:223], v60 offset:31744\n"
      "ds_read_b64_tr_b16 a[232:233], v60 offset:24896\n"
      "ds_read_b64_tr_b16 a[234:235], v60 offset:25664\n"
      "ds_read_b64_tr_b16 a[4:5], v62 offset:26176\n"
      "ds_read_b64_tr_b16 a[6:7], v62 offset:26944\n"
      "ds_read_b64_tr_b16 a[228:229], v60 offset:31040\n"
      "ds_read_b64_tr_b16 a[230:231], v60 offset:31808\n"
      "s_add_u32 m0, s63, 18432\n"
      "s_add_u32 s52, s63, 12288\n"
      "s_nop 0\n"
      "s_waitcnt lgkmcnt(10)\n"
      "v_mfma_f32_4x4x4_16b_bf16 v[28:31], a[244:245], a[224:225], 0\n"
      "v_mfma_f32_4x4x4_16b_bf16 v[32:35], a[244:245], a[226:227], 0\n"
      "buffer_load_dwordx4 v17, s[12:15], s52 offen offset:0 lds\n"
      "s_waitcnt lgkmcnt(8)\n"
      "v_mfma_f32_4x4x4_16b_bf16 v[28:31], a[248:249], a[0:1], v[28:31]\n"
      "ds_read_b64_tr_b16 a[240:241], v60 offset:24960\n"
      "v_mfma_f32_4x4x4_16b_bf16 v[32:35], a[248:249], a[2:3], v[32:35]\n"
      "buffer_load_dwordx4 v18, s[12:15], s52 offen offset:1024 lds\n"
      "s_waitcnt lgkmcnt(7)\n"
      "v_mfma_f32_4x4x4_16b_bf16 v[36:39], a[246:247], a[220:221], v[28:31]\n"
      "ds_read_b64_tr_b16 a[242:243], v60 offset:25728\n"
      "v_mfma_f32_4x4x4_16b_bf16 v[40:43], a[246:247], a[222:223], v[32:35]\n"
      "buffer_load_dwordx4 v19, s[12:15], s52 offen offset:2048 lds\n"
      "v_mfma_f32_4x4x4_16b_bf16 v[28:31], a[250:251], a[220:221], v[28:31]\n"
      "ds_read_b64_tr_b16 a[8:9], v62 offset:26240\n"
      "v_mfma_f32_4x4x4_16b_bf16 v[32:35], a[250:251], a[222:223], v[32:35]\n"
      "ds_read_b64_tr_b16 a[10:11], v62 offset:27008\n"
      "ds_read_b64_tr_b16 a[236:237], v60 offset:31104\n"
      "ds_read_b64_tr_b16 a[238:239], v60 offset:31872\n"
      "s_add_u32 m0, s63, 57600\n"
      "s_add_u32 s52, s63, 67584\n"
      "s_nop 0\n"
      "s_waitcnt lgkmcnt(5)\n"
      "v_mfma_f32_4x4x4_16b_bf16 v[44:47], a[244:245], a[232:233], 0\n"
      "v_mfma_f32_4x4x4_16b_bf16 v[48:51], a[244:245], a[234:235], 0\n"
      "buffer_load_dwordx4 v17, s[12:15], s52 offen offset:0 lds\n"
      "v_mfma_f32_4x4x4_16b_bf16 v[44:47], a[248:249], a[4:5], v[44:47]\n"
      "buffer_load_dwordx4 v18, s[12:15], s52 offen offset:1024 lds\n"
      "v_mfma_f32_4x4x4_16b_bf16 v[48:51], a[248:249], a[6:7], v[48:51]\n"
      "buffer_load_dwordx4 v19, s[12:15], s52 offen offset:2048 lds\n"
      "v_mfma_f32_4x4x4_16b_bf16 v[52:55], a[246:247], a[228:229], v[44:47]\n"
      "v_mfma_f32_4x4x4_16b_bf16 v[56:59], a[246:247], a[230:231], v[48:51]\n"
      "v_mfma_f32_4x4x4_16b_bf16 v[44:47], a[250:251], a[228:229], v[44:47]\n"
      "v_mfma_f32_4x4x4_16b_bf16 v[48:51], a[250:251], a[230:231], v[48:51]\n"
      "v_cvt_pk_bf16_f32 v252, v36, v40\n"
      "v_cvt_pk_bf16_f32 v253, v37, v41\n"
      "v_cvt_pk_bf16_f32 v254, v38, v42\n"
      "v_cvt_pk_bf16_f32 v255, v39, v43\n"
      "v_cvt_pk_bf16_f32 v248, v28, v32\n"
      "v_cvt_pk_bf16_f32 v249, v29, v33\n"
      "v_cvt_pk_bf16_f32 v250, v30, v34\n"
      "v_cvt_pk_bf16_f32 v251, v31, v35\n"
      "s_add_u32 m0, s63, 69888\n"
      "s_add_u32 s52, s63, 18432\n"
      "s_nop 0\n"
      "s_waitcnt lgkmcnt(0)\n"
      "v_mfma_f32_4x4x4_16b_bf16 v[28:31], a[244:245], a[240:241], 0\n"
      "v_mfma_f32_4x4x4_16b_bf16 v[32:35], a[244:245], a[242:243], 0\n"
      "buffer_load_dwordx4 v17, s[12:15], s52 offen offset:0 lds\n"
      "v_mfma_f32_4x4x4_16b_bf16 v[28:31], a[248:249], a[8:9], v[28:31]\n"
      "buffer_load_dwordx4 v18, s[12:15], s52 offen offset:1024 lds\n"
      "v_mfma_f32_4x4x4_16b_bf16 v[32:35], a[248:249], a[10:11], v[32:35]\n"
      "buffer_load_dwordx4 v19, s[12:15], s52 offen offset:2048 lds\n"
      "v_mfma_f32_4x4x4_16b_bf16 v[36:39], a[246:247], a[236:237], v[28:31]\n"
      "v_mfma_f32_4x4x4_16b_bf16 v[40:43], a[246:247], a[238:239], v[32:35]\n"
      "v_mfma_f32_4x4x4_16b_bf16 v[28:31], a[250:251], a[236:237], v[28:31]\n"
      "v_mfma_f32_4x4x4_16b_bf16 v[32:35], a[250:251], a[238:239], v[32:35]\n"
      "v_cvt_pk_bf16_f32 v244, v52, v56\n"
      "v_cvt_pk_bf16_f32 v245, v53, v57\n"
      "v_cvt_pk_bf16_f32 v246, v54, v58\n"
      "v_cvt_pk_bf16_f32 v247, v55, v59\n"
      "v_cvt_pk_bf16_f32 v240, v44, v48\n"
      "v_cvt_pk_bf16_f32 v241, v45, v49\n"
      "v_cvt_pk_bf16_f32 v242, v46, v50\n"
      "v_cvt_pk_bf16_f32 v243, v47, v51\n"
      "s_waitcnt vmcnt(9)\n"
      "s_barrier\n"
      "ds_read_b64_tr_b16 a[224:225], v60 offset:0\n"
      "ds_read_b64_tr_b16 a[226:227], v60 offset:768\n"
      "ds_read_b64_tr_b16 a[12:13], v60 offset:12288\n"
      "ds_read_b64_tr_b16 a[14:15], v60 offset:13056\n"
      "ds_read_b64_tr_b16 a[220:221], v60 offset:6144\n"
      "ds_read_b64_tr_b16 a[222:223], v60 offset:6912\n"
      "s_add_u32 m0, s63, 76032\n"
      "s_add_u32 s52, s63, 73728\n"
      "s_nop 0\n"
      "s_waitcnt lgkmcnt(4)\n"
      "v_mfma_f32_4x4x4_16b_bf16 v[44:47], a[244:245], a[224:225], 0\n"
      "v_mfma_f32_4x4x4_16b_bf16 v[48:51], a[244:245], a[226:227], 0\n"
      "buffer_load_dwordx4 v17, s[12:15], s52 offen offset:0 lds\n"
      "s_waitcnt lgkmcnt(2)\n"
      "v_mfma_f32_4x4x4_16b_bf16 v[44:47], a[248:249], a[12:13], v[44:47]\n"
      "ds_read_b64_tr_b16 a[232:233], v60 offset:64\n"
      "v_mfma_f32_4x4x4_16b_bf16 v[48:51], a[248:249], a[14:15], v[48:51]\n"
      "buffer_load_dwordx4 v18, s[12:15], s52 offen offset:1024 lds\n"
      "s_waitcnt lgkmcnt(1)\n"
      "v_mfma_f32_4x4x4_16b_bf16 v[52:55], a[246:247], a[220:221], v[44:47]\n"
      "ds_read_b64_tr_b16 a[234:235], v60 offset:832\n"
      "v_mfma_f32_4x4x4_16b_bf16 v[56:59], a[246:247], a[222:223], v[48:51]\n"
      "buffer_load_dwordx4 v19, s[12:15], s52 offen offset:2048 lds\n"
      "v_mfma_f32_4x4x4_16b_bf16 v[44:47], a[250:251], a[220:221], v[44:47]\n"
      "ds_read_b64_tr_b16 a[16:17], v60 offset:12352\n"
      "v_mfma_f32_4x4x4_16b_bf16 v[48:51], a[250:251], a[222:223], v[48:51]\n"
      "ds_read_b64_tr_b16 a[18:19], v60 offset:13120\n"
      "ds_read_b64_tr_b16 a[228:229], v60 offset:6208\n"
      "ds_read_b64_tr_b16 a[230:231], v60 offset:6976\n"
      "ds_read_b64_tr_b16 a[240:241], v60 offset:128\n"
      "ds_read_b64_tr_b16 a[242:243], v60 offset:896\n"
      "ds_read_b64_tr_b16 a[20:21], v60 offset:12416\n"
      "ds_read_b64_tr_b16 a[22:23], v60 offset:13184\n"
      "ds_read_b64_tr_b16 a[236:237], v60 offset:6272\n"
      "ds_read_b64_tr_b16 a[238:239], v60 offset:7040\n"
      "ds_read_b64_tr_b16 a[168:169], v7 offset:0\n"
      "ds_read_b64_tr_b16 a[170:171], v7 offset:8\n"
      "ds_read_b128 a[204:207], v3 offset:0\n"
      "v_cvt_pk_bf16_f32 v236, v36, v40\n"
      "v_cvt_pk_bf16_f32 v237, v37, v41\n"
      "v_cvt_pk_bf16_f32 v238, v38, v42\n"
      "v_cvt_pk_bf16_f32 v239, v39, v43\n"
      "v_cvt_pk_bf16_f32 v232, v28, v32\n"
      "v_cvt_pk_bf16_f32 v233, v29, v33\n"
      "v_cvt_pk_bf16_f32 v234, v30, v34\n"
      "v_cvt_pk_bf16_f32 v235, v31, v35\n"
      "s_waitcnt vmcnt(6)\n"
      "s_barrier\n"
      "s_add_u32 m0, s63, 116224\n"
      "s_add_u32 s52, s63, 24576\n"
      "s_waitcnt lgkmcnt(7)\n"
      "v_mfma_f32_4x4x4_16b_bf16 v[28:31], a[244:245], a[232:233], 0\n"
      "v_mfma_f32_4x4x4_16b_bf16 v[32:35], a[244:245], a[234:235], 0\n"
      "buffer_load_dwordx4 v17, s[12:15], s52 offen offset:0 lds\n"
      "v_mfma_f32_4x4x4_16b_bf16 v[28:31], a[248:249], a[16:17], v[28:31]\n"
      "ds_read_b64_tr_b16 a[224:225], v60 offset:18432\n"
      "v_mfma_f32_4x4x4_16b_bf16 v[32:35], a[248:249], a[18:19], v[32:35]\n"
      "buffer_load_dwordx4 v18, s[12:15], s52 offen offset:1024 lds\n"
      "v_mfma_f32_4x4x4_16b_bf16 v[36:39], a[246:247], a[228:229], v[28:31]\n"
      "ds_read_b64_tr_b16 a[226:227], v60 offset:19200\n"
      "v_mfma_f32_4x4x4_16b_bf16 v[40:43], a[246:247], a[230:231], v[32:35]\n"
      "buffer_load_dwordx4 v19, s[12:15], s52 offen offset:2048 lds\n"
      "v_mfma_f32_4x4x4_16b_bf16 v[28:31], a[250:251], a[228:229], v[28:31]\n"
      "ds_read_b64_tr_b16 a[24:25], v61 offset:6400\n"
      "v_mfma_f32_4x4x4_16b_bf16 v[32:35], a[250:251], a[230:231], v[32:35]\n"
      "ds_read_b64_tr_b16 a[26:27], v61 offset:7168\n"
      "s_waitcnt lgkmcnt(4)\n"
      "v_mfma_f32_4x4x4_16b_bf16 v[20:23], a[216:217], a[168:169], 0\n"
      "ds_read_b64_tr_b16 a[220:221], v61 offset:256\n"
      "v_mfma_f32_4x4x4_16b_bf16 v[24:27], a[216:217], a[170:171], 0\n"
      "ds_read_b64_tr_b16 a[222:223], v61 offset:1024\n"
      "ds_read_b64_tr_b16 a[172:173], v7 offset:512\n"
      "ds_read_b64_tr_b16 a[174:175], v7 offset:520\n"
      "ds_read_b128 a[208:211], v3 offset:64\n"
      "v_cvt_pk_bf16_f32 v228, v52, v56\n"
      "v_cvt_pk_bf16_f32 v229, v53, v57\n"
      "v_cvt_pk_bf16_f32 v230, v54, v58\n"
      "v_cvt_pk_bf16_f32 v231, v55, v59\n"
      "v_cvt_pk_bf16_f32 v224, v44, v48\n"
      "v_cvt_pk_bf16_f32 v225, v45, v49\n"
      "v_cvt_pk_bf16_f32 v226, v46, v50\n"
      "v_cvt_pk_bf16_f32 v227, v47, v51\n"
      "s_add_u32 m0, s63, 122368\n"
      "s_add_u32 s52, s63, 79872\n"
      "v_mfma_f32_4x4x4_16b_bf16 v[44:47], a[244:245], a[240:241], 0\n"
      "v_mfma_f32_4x4x4_16b_bf16 v[48:51], a[244:245], a[242:243], 0\n"
      "buffer_load_dwordx4 v17, s[12:15], s52 offen offset:0 lds\n"
      "v_mfma_f32_4x4x4_16b_bf16 v[44:47], a[248:249], a[20:21], v[44:47]\n"
      "ds_read_b64_tr_b16 a[232:233], v60 offset:18496\n"
      "v_mfma_f32_4x4x4_16b_bf16 v[48:51], a[248:249], a[22:23], v[48:51]\n"
      "buffer_load_dwordx4 v18, s[12:15], s52 offen offset:1024 lds\n"
      "v_mfma_f32_4x4x4_16b_bf16 v[52:55], a[246:247], a[236:237], v[44:47]\n"
      "ds_read_b64_tr_b16 a[234:235], v60 offset:19264\n"
      "v_mfma_f32_4x4x4_16b_bf16 v[56:59], a[246:247], a[238:239], v[48:51]\n"
      "buffer_load_dwordx4 v19, s[12:15], s52 offen offset:2048 lds\n"
      "v_mfma_f32_4x4x4_16b_bf16 v[44:47], a[250:251], a[236:237], v[44:47]\n"
      "ds_read_b64_tr_b16 a[28:29], v61 offset:6464\n"
      "v_mfma_f32_4x4x4_16b_bf16 v[48:51], a[250:251], a[238:239], v[48:51]\n"
      "ds_read_b64_tr_b16 a[30:31], v61 offset:7232\n"
      "s_nop 0\n"
      "s_waitcnt lgkmcnt(4)\n"
      "v_mfma_f32_4x4x4_16b_bf16 v[100:103], a[216:217], a[172:173], 0\n"
      "ds_read_b64_tr_b16 a[228:229], v61 offset:320\n"
      "v_mfma_f32_4x4x4_16b_bf16 v[104:107], a[216:217], a[174:175], 0\n"
      "ds_read_b64_tr_b16 a[230:231], v61 offset:1088\n"
      "ds_read_b64_tr_b16 a[176:177], v7 offset:1024\n"
      "ds_read_b64_tr_b16 a[178:179], v7 offset:1032\n"
      "v_cvt_pk_bf16_f32 v220, v36, v40\n"
      "v_cvt_pk_bf16_f32 v221, v37, v41\n"
      "v_cvt_pk_bf16_f32 v222, v38, v42\n"
      "v_cvt_pk_bf16_f32 v223, v39, v43\n"
      "v_cvt_pk_bf16_f32 v216, v28, v32\n"
      "v_cvt_pk_bf16_f32 v217, v29, v33\n"
      "v_mfma_f32_16x16x32_bf16 v[64:67], a[0:3], a[204:207], 0\n"
      "v_cvt_pk_bf16_f32 v218, v30, v34\n"
      "v_cvt_pk_bf16_f32 v219, v31, v35\n"
      "v_mfma_f32_16x16x32_bf16 v[68:71], a[4:7], a[204:207], 0\n"
      "v_cvt_pk_bf16_f32 v20, v20, v24\n"
      "v_cvt_pk_bf16_f32 v21, v21, v25\n"
      "v_mfma_f32_16x16x32_bf16 v[72:75], a[8:11], a[204:207], 0\n"
      "s_add_u32 m0, s63, 41216\n"
      "v_cvt_pk_bf16_f32 v22, v22, v26\n"
      "v_cvt_pk_bf16_f32 v23, v23, v27\n"
      "s_add_u32 s52, s63, 30720\n"
      "v_mfma_f32_4x4x4_16b_bf16 v[28:31], a[244:245], a[224:225], 0\n"
      "v_mfma_f32_4x4x4_16b_bf16 v[32:35], a[244:245], a[226:227], 0\n"
      "buffer_load_dwordx4 v17, s[12:15], s52 offen offset:0 lds\n"
      "v_mfma_f32_4x4x4_16b_bf16 v[28:31], a[248:249], a[24:25], v[28:31]\n"
      "ds_read_b64_tr_b16 a[240:241], v60 offset:18560\n"
      "v_mfma_f32_4x4x4_16b_bf16 v[32:35], a[248:249], a[26:27], v[32:35]\n"
      "buffer_load_dwordx4 v18, s[12:15], s52 offen offset:1024 lds\n"
      "v_mfma_f32_4x4x4_16b_bf16 v[36:39], a[246:247], a[220:221], v[28:31]\n"
      "ds_read_b64_tr_b16 a[242:243], v60 offset:19328\n"
      "v_mfma_f32_4x4x4_16b_bf16 v[40:43], a[246:247], a[222:223], v[32:35]\n"
      "buffer_load_dwordx4 v19, s[12:15], s52 offen offset:2048 lds\n"
      "v_mfma_f32_4x4x4_16b_bf16 v[28:31], a[250:251], a[220:221], v[28:31]\n"
      "ds_read_b64_tr_b16 a[32:33], v61 offset:6528\n"
      "v_mfma_f32_4x4x4_16b_bf16 v[32:35], a[250:251], a[222:223], v[32:35]\n"
      "ds_read_b64_tr_b16 a[34:35], v61 offset:7296\n"
      "s_nop 0\n"
      "s_waitcnt lgkmcnt(4)\n"
      "v_mfma_f32_4x4x4_16b_bf16 v[108:111], a[216:217], a[176:177], 0\n"
      "ds_read_b64_tr_b16 a[236:237], v61 offset:384\n"
      "v_mfma_f32_4x4x4_16b_bf16 v[112:115], a[216:217], a[178:179], 0\n"
      "ds_read_b64_tr_b16 a[238:239], v61 offset:1152\n"
      "ds_read_b64_tr_b16 a[180:181], v7 offset:1536\n"
      "ds_read_b64_tr_b16 a[182:183], v7 offset:1544\n"
      "s_mov_b32 m0, s64\n"
      "s_nop 0\n"
      "ds_write_addtid_b32 v20 offset:24832\n"
      "ds_write_addtid_b32 v21 offset:28928\n"
      "ds_write_addtid_b32 v22 offset:33024\n"
      "ds_write_addtid_b32 v23 offset:37120\n"
      "v_cvt_pk_bf16_f32 v212, v52, v56\n"
      "v_cvt_pk_bf16_f32 v213, v53, v57\n"
      "v_cvt_pk_bf16_f32 v214, v54, v58\n"
      "v_cvt_pk_bf16_f32 v215, v55, v59\n"
      "v_cvt_pk_bf16_f32 v208, v44, v48\n"
      "v_cvt_pk_bf16_f32 v209, v45, v49\n"
      "v_mfma_f32_16x16x32_bf16 v[64:67], a[12:15], a[208:211], v[64:67]\n"
      "v_cvt_pk_bf16_f32 v210, v46, v50\n"
      "v_cvt_pk_bf16_f32 v211, v47, v51\n"
      "v_mfma_f32_16x16x32_bf16 v[68:71], a[16:19], a[208:211], v[68:71]\n"
      "v_cvt_pk_bf16_f32 v100, v100, v104\n"
      "v_cvt_pk_bf16_f32 v101, v101, v105\n"
      "v_mfma_f32_16x16x32_bf16 v[72:75], a[20:23], a[208:211], v[72:75]\n"
      "s_waitcnt vmcnt(9)\n"
      "v_cvt_pk_bf16_f32 v102, v102, v106\n"
      "v_cvt_pk_bf16_f32 v103, v103, v107\n"
      "s_barrier\n"
      "s_add_u32 m0, s63, 47360\n"
      "s_add_u32 s52, s63, 86016\n"
      "v_mfma_f32_4x4x4_16b_bf16 v[44:47], a[244:245], a[232:233], 0\n"
      "v_mfma_f32_4x4x4_16b_bf16 v[48:51], a[244:245], a[234:235], 0\n"
      "buffer_load_dwordx4 v17, s[12:15], s52 offen offset:0 lds\n"
      "v_mfma_f32_4x4x4_16b_bf16 v[44:47], a[248:249], a[28:29], v[44:47]\n"
      "ds_read_b64_tr_b16 a[224:225], v61 offset:12544\n"
      "v_mfma_f32_4x4x4_16b_bf16 v[48:51], a[248:249], a[30:31], v[48:51]\n"
      "buffer_load_dwordx4 v18, s[12:15], s52 offen offset:1024 lds\n"
      "v_mfma_f32_4x4x4_16b_bf16 v[52:55], a[246:247], a[228:229], v[44:47]\n"
      "ds_read_b64_tr_b16 a[226:227], v61 offset:13312\n"
      "v_mfma_f32_4x4x4_16b_bf16 v[56:59], a[246:247], a[230:231], v[48:51]\n"
      "buffer_load_dwordx4 v19, s[12:15], s52 offen offset:2048 lds\n"
      "v_mfma_f32_4x4x4_16b_bf16 v[44:47], a[250:251], a[228:229], v[44:47]\n"
      "ds_read_b64_tr_b16 a[36:37], v62 offset:39680\n"
      "v_mfma_f32_4x4x4_16b_bf16 v[48:51], a[250:251], a[230:231], v[48:51]\n"
      "ds_read_b64_tr_b16 a[38:39], v62 offset:40448\n"
      "s_waitcnt lgkmcnt(4)\n"
      "v_mfma_f32_4x4x4_16b_bf16 v[116:119], a[216:217], a[180:181], 0\n"
      "ds_read_b64_tr_b16 a[220:221], v61 offset:18688\n"
      "v_mfma_f32_4x4x4_16b_bf16 v[120:123], a[216:217], a[182:183], 0\n"
      "ds_read_b64_tr_b16 a[222:223], v61 offset:19456\n"
      "s_mov_b32 m0, s64\n"
      "s_nop 0\n"
      "ds_write_addtid_b32 v100 offset:25344\n"
      "ds_write_addtid_b32 v101 offset:29440\n"
      "ds_write_addtid_b32 v102 offset:33536\n"
      "ds_write_addtid_b32 v103 offset:37632\n"
      "ds_read_b128 a[212:215], v3 offset:128\n"
      "v_cvt_pk_bf16_f32 v204, v36, v40\n"
      "v_cvt_pk_bf16_f32 v205, v37, v41\n"
      "v_cvt_pk_bf16_f32 v206, v38, v42\n"
      "v_cvt_pk_bf16_f32 v207, v39, v43\n"
      "v_cvt_pk_bf16_f32 v200, v28, v32\n"
      "v_cvt_pk_bf16_f32 v201, v29, v33\n"
      "v_cvt_pk_bf16_f32 v202, v30, v34\n"
      "v_cvt_pk_bf16_f32 v203, v31, v35\n"
      "v_cvt_pk_bf16_f32 v108, v108, v112\n"
      "v_cvt_pk_bf16_f32 v109, v109, v113\n"
      "v_cvt_pk_bf16_f32 v110, v110, v114\n"
      "v_cvt_pk_bf16_f32 v111, v111, v115\n"
      "s_add_u32 m0, s63, 0\n"
      "s_add_u32 s52, s63, 36864\n"
      "v_mfma_f32_4x4x4_16b_bf16 v[28:31], a[244:245], a[240:241], 0\n"
      "v_mfma_f32_4x4x4_16b_bf16 v[32:35], a[244:245], a[242:243], 0\n"
      "buffer_load_dwordx4 v17, s[12:15], s52 offen offset:0 lds\n"
      "v_mfma_f32_4x4x4_16b_bf16 v[28:31], a[248:249], a[32:33], v[28:31]\n"
      "ds_read_b64_tr_b16 a[232:233], v61 offset:12608\n"
      "v_mfma_f32_4x4x4_16b_bf16 v[32:35], a[248:249], a[34:35], v[32:35]\n"
      "buffer_load_dwordx4 v18, s[12:15], s52 offen offset:1024 lds\n"
      "v_mfma_f32_4x4x4_16b_bf16 v[36:39], a[246:247], a[236:237], v[28:31]\n"
      "ds_read_b64_tr_b16 a[234:235], v61 offset:13376\n"
      "v_mfma_f32_4x4x4_16b_bf16 v[40:43], a[246:247], a[238:239], v[32:35]\n"
      "ds_read_b64_tr_b16 a[40:41], v62 offset:39744\n"
      "v_mfma_f32_4x4x4_16b_bf16 v[28:31], a[250:251], a[236:237], v[28:31]\n"
      "ds_read_b64_tr_b16 a[42:43], v62 offset:40512\n"
      "v_mfma_f32_4x4x4_16b_bf16 v[32:35], a[250:251], a[238:239], v[32:35]\n"
      "ds_read_b64_tr_b16 a[228:229], v61 offset:18752\n"
      "ds_read_b64_tr_b16 a[230:231], v61 offset:19520\n"
      "s_mov_b32 m0, s64\n"
      "s_nop 0\n"
      "ds_write_addtid_b32 v108 offset:25856\n"
      "ds_write_addtid_b32 v109 offset:29952\n"
      "ds_write_addtid_b32 v110 offset:34048\n"
      "ds_write_addtid_b32 v111 offset:38144\n"
      "v_cvt_pk_bf16_f32 v196, v52, v56\n"
      "v_cvt_pk_bf16_f32 v197, v53, v57\n"
      "v_cvt_pk_bf16_f32 v198, v54, v58\n"
      "v_cvt_pk_bf16_f32 v199, v55, v59\n"
      "v_cvt_pk_bf16_f32 v192, v44, v48\n"
      "v_cvt_pk_bf16_f32 v193, v45, v49\n"
      "v_cvt_pk_bf16_f32 v194, v46, v50\n"
      "v_cvt_pk_bf16_f32 v195, v47, v51\n"
      "v_cvt_pk_bf16_f32 v116, v116, v120\n"
      "v_cvt_pk_bf16_f32 v117, v117, v121\n"
      "v_cvt_pk_bf16_f32 v118, v118, v122\n"
      "v_cvt_pk_bf16_f32 v119, v119, v123\n"
      "s_waitcnt lgkmcnt(4)\n"
      "v_mfma_f32_4x4x4_16b_bf16 v[44:47], a[244:245], a[224:225], 0\n"
      "v_mfma_f32_4x4x4_16b_bf16 v[48:51], a[244:245], a[226:227], 0\n"
      "s_add_u32 m0, s63, 0\n"
      "s_nop 0\n"
      "s_nop 0\n"
      "buffer_load_dwordx4 v19, s[12:15], s52 offen offset:2048 lds\n"
      "v_mfma_f32_4x4x4_16b_bf16 v[44:47], a[248:249], a[36:37], v[44:47]\n"
      "ds_read_b64_tr_b16 a[240:241], v61 offset:12672\n"
      "v_mfma_f32_4x4x4_16b_bf16 v[48:51], a[248:249], a[38:39], v[48:51]\n"
      "s_add_u32 m0, s63, 6144\n"
      "v_mfma_f32_4x4x4_16b_bf16 v[52:55], a[246:247], a[220:221], v[44:47]\n"
      "ds_read_b64_tr_b16 a[242:243], v61 offset:13440\n"
      "v_mfma_f32_4x4x4_16b_bf16 v[56:59], a[246:247], a[222:223], v[48:51]\n"
      "s_add_u32 s52, s63, 92160\n"
      "v_mfma_f32_4x4x4_16b_bf16 v[44:47], a[250:251], a[220:221], v[44:47]\n"
      "ds_read_b64_tr_b16 a[44:45], v62 offset:39808\n"
      "v_mfma_f32_4x4x4_16b_bf16 v[48:51], a[250:251], a[222:223], v[48:51]\n"
      "buffer_load_dwordx4 v17, s[12:15], s52 offen offset:0 lds\n"
      "ds_read_b64_tr_b16 a[46:47], v62 offset:40576\n"
      "ds_read_b64_tr_b16 a[236:237], v61 offset:18816\n"
      "ds_read_b64_tr_b16 a[238:239], v61 offset:19584\n"
      "s_mov_b32 m0, s64\n"
      "s_nop 0\n"
      "ds_write_addtid_b32 v116 offset:26368\n"
      "ds_write_addtid_b32 v117 offset:30464\n"
      "ds_write_addtid_b32 v118 offset:34560\n"
      "ds_write_addtid_b32 v119 offset:38656\n"
      "v_cvt_pk_bf16_f32 v188, v36, v40\n"
      "v_cvt_pk_bf16_f32 v189, v37, v41\n"
      "v_mfma_f32_16x16x32_bf16 v[64:67], a[24:27], a[212:215], v[64:67]\n"
      "v_cvt_pk_bf16_f32 v190, v38, v42\n"
      "v_cvt_pk_bf16_f32 v191, v39, v43\n"
      "v_mfma_f32_16x16x32_bf16 v[68:71], a[28:31], a[212:215], v[68:71]\n"
      "s_waitcnt vmcnt(10)\n"
      "v_cvt_pk_bf16_f32 v184, v28, v32\n"
      "v_cvt_pk_bf16_f32 v185, v29, v33\n"
      "v_mfma_f32_16x16x32_bf16 v[72:75], a[32:35], a[212:215], v[72:75]\n"
      "s_waitcnt lgkmcnt(0)\n"
      "v_cvt_pk_bf16_f32 v186, v30, v34\n"
      "v_cvt_pk_bf16_f32 v187, v31, v35\n"
      "s_barrier\n"
      "v_mfma_f32_4x4x4_16b_bf16 v[28:31], a[244:245], a[232:233], 0\n"
      "v_mfma_f32_4x4x4_16b_bf16 v[32:35], a[244:245], a[234:235], 0\n"
      "s_add_u32 m0, s63, 6144\n"
      "s_nop 0\n"
      "buffer_load_dwordx4 v18, s[12:15], s52 offen offset:1024 lds\n"
      "v_mfma_f32_4x4x4_16b_bf16 v[28:31], a[248:249], a[40:41], v[28:31]\n"
      "ds_read_b64_tr_b16 a[224:225], v62 offset:1536\n"
      "v_mfma_f32_4x4x4_16b_bf16 v[32:35], a[248:249], a[42:43], v[32:35]\n"
      "buffer_load_dwordx4 v19, s[12:15], s52 offen offset:2048 lds\n"
      "v_mfma_f32_4x4x4_16b_bf16 v[36:39], a[246:247], a[228:229], v[28:31]\n"
      "ds_read_b64_tr_b16 a[226:227], v62 offset:2304\n"
      "v_mfma_f32_4x4x4_16b_bf16 v[40:43], a[246:247], a[230:231], v[32:35]\n"
      "ds_read_b64_tr_b16 a[48:49], v62 offset:13824\n"
      "v_mfma_f32_4x4x4_16b_bf16 v[28:31], a[250:251], a[228:229], v[28:31]\n"
      "ds_read_b64_tr_b16 a[50:51], v62 offset:14592\n"
      "v_mfma_f32_4x4x4_16b_bf16 v[32:35], a[250:251], a[230:231], v[32:35]\n"
      "ds_read_b64_tr_b16 a[220:221], v62 offset:7680\n"
      "ds_read_b64_tr_b16 a[222:223], v62 offset:8448\n"
      "ds_read_b128 a[192:195], v4 offset:0\n"
      "ds_read_b128 a[196:199], v0 offset:0\n"
      "ds_read_b128 a[200:203], v0 offset:4096\n"
      "v_cvt_pk_bf16_f32 v180, v52, v56\n"
      "v_cvt_pk_bf16_f32 v181, v53, v57\n"
      "v_cvt_pk_bf16_f32 v182, v54, v58\n"
      "v_cvt_pk_bf16_f32 v183, v55, v59\n"
      "v_cvt_pk_bf16_f32 v176, v44, v48\n"
      "v_cvt_pk_bf16_f32 v177, v45, v49\n"
      "v_cvt_pk_bf16_f32 v178, v46, v50\n"
      "v_cvt_pk_bf16_f32 v179, v47, v51\n"
      "s_add_u32 m0, s63, 18432\n"
      "s_add_u32 s52, s63, 43008\n"
      "v_mfma_f32_4x4x4_16b_bf16 v[44:47], a[244:245], a[240:241], 0\n"
      "v_mfma_f32_4x4x4_16b_bf16 v[48:51], a[244:245], a[242:243], 0\n"
      "buffer_load_dwordx4 v17, s[12:15], s52 offen offset:0 lds\n"
      "v_mfma_f32_4x4x4_16b_bf16 v[44:47], a[248:249], a[44:45], v[44:47]\n"
      "ds_read_b64_tr_b16 a[232:233], v62 offset:1600\n"
      "v_mfma_f32_4x4x4_16b_bf16 v[48:51], a[248:249], a[46:47], v[48:51]\n"
      "buffer_load_dwordx4 v18, s[12:15], s52 offen offset:1024 lds\n"
      "v_mfma_f32_4x4x4_16b_bf16 v[52:55], a[246:247], a[236:237], v[44:47]\n"
      "ds_read_b64_tr_b16 a[234:235], v62 offset:2368\n"
      "v_mfma_f32_4x4x4_16b_bf16 v[56:59], a[246:247], a[238:239], v[48:51]\n"
      "ds_read_b64_tr_b16 a[52:53], v62 offset:13888\n"
      "v_mfma_f32_4x4x4_16b_bf16 v[44:47], a[250:251], a[236:237], v[44:47]\n"
      "ds_read_b64_tr_b16 a[54:55], v62 offset:14656\n"
      "v_mfma_f32_4x4x4_16b_bf16 v[48:51], a[250:251], a[238:239], v[48:51]\n"
      "ds_read_b64_tr_b16 a[228:229], v62 offset:7744\n"
      "ds_read_b64_tr_b16 a[230:231], v62 offset:8512\n"
      "ds_read_b128 a[204:207], v0 offset:64\n"
      "ds_read_b128 a[208:211], v0 offset:4160\n"
      "ds_read_b128 a[212:215], v0 offset:128\n"
      "v_cvt_pk_bf16_f32 v172, v36, v40\n"
      "v_cvt_pk_bf16_f32 v173, v37, v41\n"
      "v_cvt_pk_bf16_f32 v174, v38, v42\n"
      "v_cvt_pk_bf16_f32 v175, v39, v43\n"
      "v_cvt_pk_bf16_f32 v168, v28, v32\n"
      "v_cvt_pk_bf16_f32 v169, v29, v33\n"
      "v_cvt_pk_bf16_f32 v170, v30, v34\n"
      "v_cvt_pk_bf16_f32 v171, v31, v35\n"
      "s_nop 0\n"
      "s_waitcnt lgkmcnt(6)\n"
      "v_mfma_f32_4x4x4_16b_bf16 v[28:31], a[244:245], a[224:225], 0\n"
      "v_mfma_f32_4x4x4_16b_bf16 v[32:35], a[244:245], a[226:227], 0\n"
      "buffer_load_dwordx4 v19, s[12:15], s52 offen offset:2048 lds\n"
      "v_mfma_f32_4x4x4_16b_bf16 v[28:31], a[248:249], a[48:49], v[28:31]\n"
      "ds_read_b64_tr_b16 a[240:241], v62 offset:1664\n"
      "v_mfma_f32_4x4x4_16b_bf16 v[32:35], a[248:249], a[50:51], v[32:35]\n"
      "s_add_u32 m0, s63, 57600\n"
      "v_mfma_f32_4x4x4_16b_bf16 v[36:39], a[246:247], a[220:221], v[28:31]\n"
      "ds_read_b64_tr_b16 a[242:243], v62 offset:2432\n"
      "v_mfma_f32_4x4x4_16b_bf16 v[40:43], a[246:247], a[222:223], v[32:35]\n"
      "s_add_u32 s52, s63, 98304\n"
      "v_mfma_f32_4x4x4_16b_bf16 v[28:31], a[250:251], a[220:221], v[28:31]\n"
      "ds_read_b64_tr_b16 a[56:57], v62 offset:13952\n"
      "v_mfma_f32_4x4x4_16b_bf16 v[32:35], a[250:251], a[222:223], v[32:35]\n"
      "buffer_load_dwordx4 v17, s[12:15], s52 offen offset:0 lds\n"
      "ds_read_b64_tr_b16 a[58:59], v62 offset:14720\n"
      "ds_read_b64_tr_b16 a[236:237], v62 offset:7808\n"
      "ds_read_b64_tr_b16 a[238:239], v62 offset:8576\n"
      "v_mfma_f32_16x16x32_bf16 v[64:67], a[36:39], a[192:195], v[64:67]\n"
      "v_cvt_pk_bf16_f32 v164, v52, v56\n"
      "v_cvt_pk_bf16_f32 v165, v53, v57\n"
      "v_mfma_f32_16x16x32_bf16 v[68:71], a[40:43], a[192:195], v[68:71]\n"
      "v_cvt_pk_bf16_f32 v166, v54, v58\n"
      "v_cvt_pk_bf16_f32 v167, v55, v59\n"
      "v_mfma_f32_16x16x32_bf16 v[72:75], a[44:47], a[192:195], v[72:75]\n"
      "s_nop 0\n"
      "v_cvt_pk_bf16_f32 v160, v44, v48\n"
      "v_cvt_pk_bf16_f32 v161, v45, v49\n"
      "v_mfma_f32_16x16x32_bf16 v[76:79], v[252:255], a[196:199], 0\n"
      "s_waitcnt vmcnt(10)\n"
      "v_cvt_pk_bf16_f32 v162, v46, v50\n"
      "v_cvt_pk_bf16_f32 v163, v47, v51\n"
      "s_barrier\n"
      "v_mfma_f32_4x4x4_16b_bf16 v[44:47], a[244:245], a[232:233], 0\n"
      "v_mfma_f32_4x4x4_16b_bf16 v[48:51], a[244:245], a[234:235], 0\n"
      "buffer_load_dwordx4 v18, s[12:15], s52 offen offset:1024 lds\n"
      "s_waitcnt lgkmcnt(0)\n"
      "v_mfma_f32_4x4x4_16b_bf16 v[44:47], a[248:249], a[52:53], v[44:47]\n"
      "ds_read_b64_tr_b16 a[224:225], v60 offset:41216\n"
      "v_mfma_f32_4x4x4_16b_bf16 v[48:51], a[248:249], a[54:55], v[48:51]\n"
      "buffer_load_dwordx4 v19, s[12:15], s52 offen offset:2048 lds\n"
      "v_mfma_f32_4x4x4_16b_bf16 v[52:55], a[246:247], a[228:229], v[44:47]\n"
      "ds_read_b64_tr_b16 a[226:227], v60 offset:41984\n"
      "v_mfma_f32_4x4x4_16b_bf16 v[56:59], a[246:247], a[230:231], v[48:51]\n"
      "ds_read_b64_tr_b16 a[60:61], v62 offset:19968\n"
      "v_mfma_f32_4x4x4_16b_bf16 v[44:47], a[250:251], a[228:229], v[44:47]\n"
      "ds_read_b64_tr_b16 a[62:63], v62 offset:20736\n"
      "v_mfma_f32_4x4x4_16b_bf16 v[48:51], a[250:251], a[230:231], v[48:51]\n"
      "ds_read_b64_tr_b16 a[220:221], v60 offset:47360\n"
      "ds_read_b64_tr_b16 a[222:223], v60 offset:48128\n"
      "ds_read_b128 a[192:195], v0 offset:4224\n"
      "v_mfma_f32_16x16x32_bf16 v[80:83], v[244:247], a[196:199], 0\n"
      "v_cvt_pk_bf16_f32 v156, v36, v40\n"
      "v_cvt_pk_bf16_f32 v157, v37, v41\n"
      "v_mfma_f32_16x16x32_bf16 v[84:87], v[236:239], a[196:199], 0\n"
      "v_cvt_pk_bf16_f32 v158, v38, v42\n"
      "v_cvt_pk_bf16_f32 v159, v39, v43\n"
      "v_mfma_f32_16x16x32_bf16 v[88:91], v[248:251], a[200:203], 0\n"
      "v_cvt_pk_bf16_f32 v152, v28, v32\n"
      "v_cvt_pk_bf16_f32 v153, v29, v33\n"
      "v_mfma_f32_16x16x32_bf16 v[92:95], v[240:243], a[200:203], 0\n"
      "s_add_u32 m0, s63, 69888\n"
      "v_cvt_pk_bf16_f32 v154, v30, v34\n"
      "v_cvt_pk_bf16_f32 v155, v31, v35\n"
      "s_add_u32 s52, s63, 49152\n"
      "v_mfma_f32_4x4x4_16b_bf16 v[28:31], a[244:245], a[240:241], 0\n"
      "v_mfma_f32_4x4x4_16b_bf16 v[32:35], a[244:245], a[242:243], 0\n"
      "buffer_load_dwordx4 v17, s[12:15], s52 offen offset:0 lds\n"
      "v_mfma_f32_4x4x4_16b_bf16 v[28:31], a[248:249], a[56:57], v[28:31]\n"
      "ds_read_b64_tr_b16 a[232:233], v60 offset:41280\n"
      "v_mfma_f32_4x4x4_16b_bf16 v[32:35], a[248:249], a[58:59], v[32:35]\n"
      "buffer_load_dwordx4 v18, s[12:15], s52 offen offset:1024 lds\n"
      "v_mfma_f32_4x4x4_16b_bf16 v[36:39], a[246:247], a[236:237], v[28:31]\n"
      "ds_read_b64_tr_b16 a[234:235], v60 offset:42048\n"
      "v_mfma_f32_4x4x4_16b_bf16 v[40:43], a[246:247], a[238:239], v[32:35]\n"
      "ds_read_b64_tr_b16 a[64:65], v62 offset:20032\n"
      "v_mfma_f32_4x4x4_16b_bf16 v[28:31], a[250:251], a[236:237], v[28:31]\n"
      "ds_read_b64_tr_b16 a[66:67], v62 offset:20800\n"
      "v_mfma_f32_4x4x4_16b_bf16 v[32:35], a[250:251], a[238:239], v[32:35]\n"
      "ds_read_b64_tr_b16 a[228:229], v60 offset:47424\n"
      "ds_read_b64_tr_b16 a[230:231], v60 offset:48192\n"
      "ds_read_b128 a[196:199], v1 offset:0\n"
      "v_mfma_f32_16x16x32_bf16 v[96:99], v[232:235], a[200:203], 0\n"
      "v_cvt_pk_bf16_f32 v148, v52, v56\n"
      "v_cvt_pk_bf16_f32 v149, v53, v57\n"
      "v_mfma_f32_16x16x32_bf16 v[76:79], v[228:231], a[204:207], v[76:79]\n"
      "v_cvt_pk_bf16_f32 v150, v54, v58\n"
      "v_cvt_pk_bf16_f32 v151, v55, v59\n"
      "v_mfma_f32_16x16x32_bf16 v[80:83], v[220:223], a[204:207], v[80:83]\n"
      "s_nop 0\n"
      "v_cvt_pk_bf16_f32 v144, v44, v48\n"
      "v_cvt_pk_bf16_f32 v145, v45, v49\n"
      "v_mfma_f32_16x16x32_bf16 v[84:87], v[212:215], a[204:207], v[84:87]\n"
      "s_waitcnt lgkmcnt(2)\n"
      "v_cvt_pk_bf16_f32 v146, v46, v50\n"
      "v_cvt_pk_bf16_f32 v147, v47, v51\n"
      "v_mfma_f32_4x4x4_16b_bf16 v[44:47], a[244:245], a[224:225], 0\n"
      "v_mfma_f32_4x4x4_16b_bf16 v[48:51], a[244:245], a[226:227], 0\n"
      "buffer_load_dwordx4 v19, s[12:15], s52 offen offset:2048 lds\n"
      "v_mfma_f32_4x4x4_16b_bf16 v[44:47], a[248:249], a[60:61], v[44:47]\n"
      "ds_read_b64_tr_b16 a[240:241], v60 offset:41344\n"
      "v_mfma_f32_4x4x4_16b_bf16 v[48:51], a[248:249], a[62:63], v[48:51]\n"
      "s_add_u32 m0, s63, 76032\n"
      "v_mfma_f32_4x4x4_16b_bf16 v[52:55], a[246:247], a[220:221], v[44:47]\n"
      "ds_read_b64_tr_b16 a[242:243], v60 offset:42112\n"
      "v_mfma_f32_4x4x4_16b_bf16 v[56:59], a[246:247], a[222:223], v[48:51]\n"
      "s_add_u32 s52, s63, 104448\n"
      "v_mfma_f32_4x4x4_16b_bf16 v[44:47], a[250:251], a[220:221], v[44:47]\n"
      "ds_read_b64_tr_b16 a[68:69], v62 offset:20096\n"
      "v_mfma_f32_4x4x4_16b_bf16 v[48:51], a[250:251], a[222:223], v[48:51]\n"
      "buffer_load_dwordx4 v17, s[12:15], s52 offen offset:0 lds\n"
      "ds_read_b64_tr_b16 a[70:71], v62 offset:20864\n"
      "ds_read_b64_tr_b16 a[236:237], v60 offset:47488\n"
      "ds_read_b64_tr_b16 a[238:239], v60 offset:48256\n"
      "ds_read_b128 a[200:203], v1 offset:4096\n"
      "ds_read_b128 a[204:207], v4 offset:64\n"
      "v_mfma_f32_16x16x32_bf16 v[88:91], v[224:227], a[208:211], v[88:91]\n"
      "v_cvt_pk_bf16_f32 v140, v36, v40\n"
      "v_cvt_pk_bf16_f32 v141, v37, v41\n"
      "v_mfma_f32_16x16x32_bf16 v[92:95], v[216:219], a[208:211], v[92:95]\n"
      "v_cvt_pk_bf16_f32 v142, v38, v42\n"
      "v_cvt_pk_bf16_f32 v143, v39, v43\n"
      "v_mfma_f32_16x16x32_bf16 v[96:99], v[208:211], a[208:211], v[96:99]\n"
      "s_nop 0\n"
      "v_cvt_pk_bf16_f32 v136, v28, v32\n"
      "v_cvt_pk_bf16_f32 v137, v29, v33\n"
      "v_mfma_f32_16x16x32_bf16 v[76:79], v[204:207], a[212:215], v[76:79]\n"
      "s_waitcnt vmcnt(10)\n"
      "v_cvt_pk_bf16_f32 v138, v30, v34\n"
      "v_cvt_pk_bf16_f32 v139, v31, v35\n"
      "s_barrier\n"
      "v_mfma_f32_4x4x4_16b_bf16 v[28:31], a[244:245], a[232:233], 0\n"
      "v_mfma_f32_4x4x4_16b_bf16 v[32:35], a[244:245], a[234:235], 0\n"
      "buffer_load_dwordx4 v18, s[12:15], s52 offen offset:1024 lds\n"
      "v_mfma_f32_4x4x4_16b_bf16 v[28:31], a[248:249], a[64:65], v[28:31]\n"
      "ds_read_b64_tr_b16 a[224:225], v60 offset:0\n"
      "v_mfma_f32_4x4x4_16b_bf16 v[32:35], a[248:249], a[66:67], v[32:35]\n"
      "buffer_load_dwordx4 v19, s[12:15], s52 offen offset:2048 lds\n"
      "s_waitcnt lgkmcnt(1)\n"
      "v_mfma_f32_4x4x4_16b_bf16 v[36:39], a[246:247], a[228:229], v[28:31]\n"
      "ds_read_b64_tr_b16 a[226:227], v60 offset:768\n"
      "v_mfma_f32_4x4x4_16b_bf16 v[40:43], a[246:247], a[230:231], v[32:35]\n"
      "ds_read_b64_tr_b16 a[72:73], v60 offset:12288\n"
      "v_mfma_f32_4x4x4_16b_bf16 v[28:31], a[250:251], a[228:229], v[28:31]\n"
      "ds_read_b64_tr_b16 a[74:75], v60 offset:13056\n"
      "v_mfma_f32_4x4x4_16b_bf16 v[32:35], a[250:251], a[230:231], v[32:35]\n"
      "ds_read_b64_tr_b16 a[220:221], v60 offset:6144\n"
      "ds_read_b64_tr_b16 a[222:223], v60 offset:6912\n"
      "ds_read_b128 a[208:211], v1 offset:64\n"
      "v_mfma_f32_16x16x32_bf16 v[80:83], v[196:199], a[212:215], v[80:83]\n"
      "v_cvt_pk_bf16_f32 v132, v52, v56\n"
      "v_cvt_pk_bf16_f32 v133, v53, v57\n"
      "v_mfma_f32_16x16x32_bf16 v[84:87], v[188:191], a[212:215], v[84:87]\n"
      "v_cvt_pk_bf16_f32 v134, v54, v58\n"
      "v_cvt_pk_bf16_f32 v135, v55, v59\n"
      "v_mfma_f32_16x16x32_bf16 v[88:91], v[200:203], a[192:195], v[88:91]\n"
      "v_cvt_pk_bf16_f32 v128, v44, v48\n"
      "v_cvt_pk_bf16_f32 v129, v45, v49\n"
      "v_mfma_f32_16x16x32_bf16 v[92:95], v[192:195], a[192:195], v[92:95]\n"
      "v_cvt_pk_bf16_f32 v130, v46, v50\n"
      "v_cvt_pk_bf16_f32 v131, v47, v51\n"
      "v_mfma_f32_4x4x4_16b_bf16 v[44:47], a[244:245], a[240:241], 0\n"
      "v_mfma_f32_4x4x4_16b_bf16 v[48:51], a[244:245], a[242:243], 0\n"
      "ds_read_b64_tr_b16 a[232:233], v60 offset:64\n"
      "v_mfma_f32_4x4x4_16b_bf16 v[44:47], a[248:249], a[68:69], v[44:47]\n"
      "ds_read_b64_tr_b16 a[234:235], v60 offset:832\n"
      "v_mfma_f32_4x4x4_16b_bf16 v[48:51], a[248:249], a[70:71], v[48:51]\n"
      "ds_read_b64_tr_b16 a[76:77], v60 offset:12352\n"
      "v_mfma_f32_4x4x4_16b_bf16 v[52:55], a[246:247], a[236:237], v[44:47]\n"
      "ds_read_b64_tr_b16 a[78:79], v60 offset:13120\n"
      "v_mfma_f32_4x4x4_16b_bf16 v[56:59], a[246:247], a[238:239], v[48:51]\n"
      "ds_read_b64_tr_b16 a[228:229], v60 offset:6208\n"
      "v_mfma_f32_4x4x4_16b_bf16 v[44:47], a[250:251], a[236:237], v[44:47]\n"
      "ds_read_b64_tr_b16 a[230:231], v60 offset:6976\n"
      "v_mfma_f32_4x4x4_16b_bf16 v[48:51], a[250:251], a[238:239], v[48:51]\n"
      "ds_read_b128 a[212:215], v1 offset:4160\n"
      "v_mfma_f32_16x16x32_bf16 v[96:99], v[184:187], a[192:195], v[96:99]\n"
      "v_cvt_pk_bf16_f32 v124, v36, v40\n"
      "v_cvt_pk_bf16_f32 v125, v37, v41\n"
      "v_mfma_f32_16x16x32_bf16 v[76:79], v[180:183], a[196:199], v[76:79]\n"
      "v_cvt_pk_bf16_f32 v126, v38, v42\n"
      "v_cvt_pk_bf16_f32 v127, v39, v43\n"
      "v_mfma_f32_16x16x32_bf16 v[80:83], v[172:175], a[196:199], v[80:83]\n"
      "s_nop 0\n"
      "v_cvt_pk_bf16_f32 v120, v28, v32\n"
      "v_cvt_pk_bf16_f32 v121, v29, v33\n"
      "v_mfma_f32_16x16x32_bf16 v[84:87], v[164:167], a[196:199], v[84:87]\n"
      "s_waitcnt lgkmcnt(1)\n"
      "v_cvt_pk_bf16_f32 v122, v30, v34\n"
      "v_cvt_pk_bf16_f32 v123, v31, v35\n"
      "v_mfma_f32_4x4x4_16b_bf16 v[28:31], a[244:245], a[224:225], 0\n"
      "v_mfma_f32_4x4x4_16b_bf16 v[32:35], a[244:245], a[226:227], 0\n"
      "ds_read_b64_tr_b16 a[240:241], v60 offset:128\n"
      "v_mfma_f32_4x4x4_16b_bf16 v[28:31], a[248:249], a[72:73], v[28:31]\n"
      "ds_read_b64_tr_b16 a[242:243], v60 offset:896\n"
      "v_mfma_f32_4x4x4_16b_bf16 v[32:35], a[248:249], a[74:75], v[32:35]\n"
      "ds_read_b64_tr_b16 a[80:81], v60 offset:12416\n"
      "v_mfma_f32_4x4x4_16b_bf16 v[36:39], a[246:247], a[220:221], v[28:31]\n"
      "ds_read_b64_tr_b16 a[82:83], v60 offset:13184\n"
      "v_mfma_f32_4x4x4_16b_bf16 v[40:43], a[246:247], a[222:223], v[32:35]\n"
      "ds_read_b64_tr_b16 a[236:237], v60 offset:6272\n"
      "v_mfma_f32_4x4x4_16b_bf16 v[28:31], a[250:251], a[220:221], v[28:31]\n"
      "ds_read_b64_tr_b16 a[238:239], v60 offset:7040\n"
      "v_mfma_f32_4x4x4_16b_bf16 v[32:35], a[250:251], a[222:223], v[32:35]\n"
      "ds_read_b128 a[192:195], v4 offset:128\n"
      "ds_read_b128 a[196:199], v1 offset:128\n"
      "v_mfma_f32_16x16x32_bf16 v[88:91], v[176:179], a[200:203], v[88:91]\n"
      "v_cvt_pk_bf16_f32 v116, v52, v56\n"
      "v_cvt_pk_bf16_f32 v117, v53, v57\n"
      "v_mfma_f32_16x16x32_bf16 v[92:95], v[168:171], a[200:203], v[92:95]\n"
      "v_cvt_pk_bf16_f32 v118, v54, v58\n"
      "v_cvt_pk_bf16_f32 v119, v55, v59\n"
      "v_mfma_f32_16x16x32_bf16 v[96:99], v[160:163], a[200:203], v[96:99]\n"
      "v_cvt_pk_bf16_f32 v112, v44, v48\n"
      "v_cvt_pk_bf16_f32 v113, v45, v49\n"
      "v_mfma_f32_16x16x32_bf16 v[64:67], a[48:51], a[204:207], v[64:67]\n"
      "s_waitcnt vmcnt(6)\n"
      "v_cvt_pk_bf16_f32 v114, v46, v50\n"
      "v_cvt_pk_bf16_f32 v115, v47, v51\n"
      "s_barrier\n"
      "v_mfma_f32_4x4x4_16b_bf16 v[44:47], a[244:245], a[232:233], 0\n"
      "v_mfma_f32_4x4x4_16b_bf16 v[48:51], a[244:245], a[234:235], 0\n"
      "ds_read_b64_tr_b16 a[224:225], v60 offset:18432\n"
      "v_mfma_f32_4x4x4_16b_bf16 v[44:47], a[248:249], a[76:77], v[44:47]\n"
      "ds_read_b64_tr_b16 a[226:227], v60 offset:19200\n"
      "v_mfma_f32_4x4x4_16b_bf16 v[48:51], a[248:249], a[78:79], v[48:51]\n"
      "ds_read_b64_tr_b16 a[84:85], v61 offset:6400\n"
      "v_mfma_f32_4x4x4_16b_bf16 v[52:55], a[246:247], a[228:229], v[44:47]\n"
      "ds_read_b64_tr_b16 a[86:87], v61 offset:7168\n"
      "v_mfma_f32_4x4x4_16b_bf16 v[56:59], a[246:247], a[230:231], v[48:51]\n"
      "ds_read_b64_tr_b16 a[220:221], v61 offset:256\n"
      "v_mfma_f32_4x4x4_16b_bf16 v[44:47], a[250:251], a[228:229], v[44:47]\n"
      "ds_read_b64_tr_b16 a[222:223], v61 offset:1024\n"
      "v_mfma_f32_4x4x4_16b_bf16 v[48:51], a[250:251], a[230:231], v[48:51]\n"
      "ds_read_b128 a[200:203], v1 offset:4224\n"
      "v_mfma_f32_16x16x32_bf16 v[68:71], a[52:55], a[204:207], v[68:71]\n"
      "v_cvt_pk_bf16_f32 v108, v36, v40\n"
      "v_cvt_pk_bf16_f32 v109, v37, v41\n"
      "v_mfma_f32_16x16x32_bf16 v[72:75], a[56:59], a[204:207], v[72:75]\n"
      "v_cvt_pk_bf16_f32 v110, v38, v42\n"
      "v_cvt_pk_bf16_f32 v111, v39, v43\n"
      "v_mfma_f32_16x16x32_bf16 v[76:79], v[156:159], a[208:211], v[76:79]\n"
      "v_cvt_pk_bf16_f32 v104, v28, v32\n"
      "v_cvt_pk_bf16_f32 v105, v29, v33\n"
      "v_mfma_f32_16x16x32_bf16 v[80:83], v[148:151], a[208:211], v[80:83]\n"
      "s_nop 0\n"
      "s_waitcnt lgkmcnt(1)\n"
      "v_cvt_pk_bf16_f32 v106, v30, v34\n"
      "v_cvt_pk_bf16_f32 v107, v31, v35\n"
      "v_mfma_f32_4x4x4_16b_bf16 v[28:31], a[244:245], a[240:241], 0\n"
      "v_mfma_f32_4x4x4_16b_bf16 v[32:35], a[244:245], a[242:243], 0\n"
      "ds_read_b64_tr_b16 a[232:233], v60 offset:18496\n"
      "v_mfma_f32_4x4x4_16b_bf16 v[28:31], a[248:249], a[80:81], v[28:31]\n"
      "ds_read_b64_tr_b16 a[234:235], v60 offset:19264\n"
      "v_mfma_f32_4x4x4_16b_bf16 v[32:35], a[248:249], a[82:83], v[32:35]\n"
      "ds_read_b64_tr_b16 a[88:89], v61 offset:6464\n"
      "v_mfma_f32_4x4x4_16b_bf16 v[36:39], a[246:247], a[236:237], v[28:31]\n"
      "ds_read_b64_tr_b16 a[90:91], v61 offset:7232\n"
      "v_mfma_f32_4x4x4_16b_bf16 v[40:43], a[246:247], a[238:239], v[32:35]\n"
      "ds_read_b64_tr_b16 a[228:229], v61 offset:320\n"
      "v_mfma_f32_4x4x4_16b_bf16 v[28:31], a[250:251], a[236:237], v[28:31]\n"
      "ds_read_b64_tr_b16 a[230:231], v61 offset:1088\n"
      "v_mfma_f32_4x4x4_16b_bf16 v[32:35], a[250:251], a[238:239], v[32:35]\n"
      "ds_read_b128 a[204:207], v5 offset:0\n"
      "v_mfma_f32_16x16x32_bf16 v[84:87], v[140:143], a[208:211], v[84:87]\n"
      "v_cvt_pk_bf16_f32 v100, v52, v56\n"
      "v_cvt_pk_bf16_f32 v101, v53, v57\n"
      "v_mfma_f32_16x16x32_bf16 v[88:91], v[152:155], a[212:215], v[88:91]\n"
      "v_cvt_pk_bf16_f32 v102, v54, v58\n"
      "v_cvt_pk_bf16_f32 v103, v55, v59\n"
      "v_mfma_f32_16x16x32_bf16 v[92:95], v[144:147], a[212:215], v[92:95]\n"
      "v_cvt_pk_bf16_f32 v44, v44, v48\n"
      "v_cvt_pk_bf16_f32 v45, v45, v49\n"
      "v_mfma_f32_16x16x32_bf16 v[96:99], v[136:139], a[212:215], v[96:99]\n"
      "v_cvt_pk_bf16_f32 v46, v46, v50\n"
      "v_cvt_pk_bf16_f32 v47, v47, v51\n"
      "v_mfma_f32_16x16x32_bf16 v[64:67], a[60:63], a[192:195], v[64:67]\n"
      "v_accvgpr_write_b32 a164, v44\n"
      "v_accvgpr_write_b32 a165, v45\n"
      "v_mfma_f32_16x16x32_bf16 v[68:71], a[64:67], a[192:195], v[68:71]\n"
      "v_accvgpr_write_b32 a166, v46\n"
      "v_accvgpr_write_b32 a167, v47\n"
      "v_mfma_f32_4x4x4_16b_bf16 v[44:47], a[244:245], a[224:225], 0\n"
      "v_mfma_f32_4x4x4_16b_bf16 v[48:51], a[244:245], a[226:227], 0\n"
      "ds_read_b64_tr_b16 a[240:241], v60 offset:18560\n"
      "v_mfma_f32_4x4x4_16b_bf16 v[44:47], a[248:249], a[84:85], v[44:47]\n"
      "ds_read_b64_tr_b16 a[242:243], v60 offset:19328\n"
      "v_mfma_f32_4x4x4_16b_bf16 v[48:51], a[248:249], a[86:87], v[48:51]\n"
      "ds_read_b64_tr_b16 a[92:93], v61 offset:6528\n"
      "v_mfma_f32_4x4x4_16b_bf16 v[52:55], a[246:247], a[220:221], v[44:47]\n"
      "ds_read_b64_tr_b16 a[94:95], v61 offset:7296\n"
      "v_mfma_f32_4x4x4_16b_bf16 v[56:59], a[246:247], a[222:223], v[48:51]\n"
      "ds_read_b64_tr_b16 a[236:237], v61 offset:384\n"
      "v_mfma_f32_4x4x4_16b_bf16 v[44:47], a[250:251], a[220:221], v[44:47]\n"
      "ds_read_b64_tr_b16 a[238:239], v61 offset:1152\n"
      "v_mfma_f32_4x4x4_16b_bf16 v[48:51], a[250:251], a[222:223], v[48:51]\n"
      "ds_read_b128 a[208:211], v2 offset:0\n"
      "ds_read_b128 a[212:215], v2 offset:4096\n"
      "v_cvt_pk_bf16_f32 v36, v36, v40\n"
      "v_cvt_pk_bf16_f32 v37, v37, v41\n"
      "v_cvt_pk_bf16_f32 v38, v38, v42\n"
      "v_cvt_pk_bf16_f32 v39, v39, v43\n"
      "v_mfma_f32_16x16x32_bf16 v[72:75], a[68:71], a[192:195], v[72:75]\n"
      "v_accvgpr_write_b32 a160, v36\n"
      "v_accvgpr_write_b32 a161, v37\n"
      "v_mfma_f32_16x16x32_bf16 v[76:79], v[132:135], a[196:199], v[76:79]\n"
      "v_accvgpr_write_b32 a162, v38\n"
      "v_accvgpr_write_b32 a163, v39\n"
      "v_mfma_f32_16x16x32_bf16 v[80:83], v[124:127], a[196:199], v[80:83]\n"
      "v_cvt_pk_bf16_f32 v28, v28, v32\n"
      "v_cvt_pk_bf16_f32 v29, v29, v33\n"
      "v_mfma_f32_16x16x32_bf16 v[84:87], v[116:119], a[196:199], v[84:87]\n"
      "s_waitcnt lgkmcnt(1)\n"
      "v_cvt_pk_bf16_f32 v30, v30, v34\n"
      "v_cvt_pk_bf16_f32 v31, v31, v35\n"
      "v_mfma_f32_16x16x32_bf16 v[88:91], v[128:131], a[200:203], v[88:91]\n"
      "s_nop 0\n"
      "v_accvgpr_write_b32 a156, v28\n"
      "v_accvgpr_write_b32 a157, v29\n"
      "v_mfma_f32_16x16x32_bf16 v[92:95], v[120:123], a[200:203], v[92:95]\n"
      "s_waitcnt vmcnt(0)\n"
      "v_accvgpr_write_b32 a158, v30\n"
      "v_accvgpr_write_b32 a159, v31\n"
      "s_barrier\n"
      "v_mfma_f32_4x4x4_16b_bf16 v[28:31], a[244:245], a[232:233], 0\n"
      "v_mfma_f32_4x4x4_16b_bf16 v[32:35], a[244:245], a[234:235], 0\n"
      "ds_read_b64_tr_b16 a[224:225], v61 offset:12544\n"
      "v_mfma_f32_4x4x4_16b_bf16 v[28:31], a[248:249], a[88:89], v[28:31]\n"
      "ds_read_b64_tr_b16 a[226:227], v61 offset:13312\n"
      "v_mfma_f32_4x4x4_16b_bf16 v[32:35], a[248:249], a[90:91], v[32:35]\n"
      "ds_read_b64_tr_b16 a[96:97], v62 offset:39680\n"
      "v_mfma_f32_4x4x4_16b_bf16 v[36:39], a[246:247], a[228:229], v[28:31]\n"
      "ds_read_b64_tr_b16 a[98:99], v62 offset:40448\n"
      "v_mfma_f32_4x4x4_16b_bf16 v[40:43], a[246:247], a[230:231], v[32:35]\n"
      "ds_read_b64_tr_b16 a[220:221], v61 offset:18688\n"
      "v_mfma_f32_4x4x4_16b_bf16 v[28:31], a[250:251], a[228:229], v[28:31]\n"
      "ds_read_b64_tr_b16 a[222:223], v61 offset:19456\n"
      "v_mfma_f32_4x4x4_16b_bf16 v[32:35], a[250:251], a[230:231], v[32:35]\n"
      "ds_read_b128 a[192:195], v5 offset:64\n"
      "v_cvt_pk_bf16_f32 v52, v52, v56\n"
      "v_cvt_pk_bf16_f32 v53, v53, v57\n"
      "v_cvt_pk_bf16_f32 v54, v54, v58\n"
      "v_cvt_pk_bf16_f32 v55, v55, v59\n"
      "v_mfma_f32_16x16x32_bf16 v[96:99], v[112:115], a[200:203], v[96:99]\n"
      "v_accvgpr_write_b32 a152, v52\n"
      "v_accvgpr_write_b32 a153, v53\n"
      "v_mfma_f32_16x16x32_bf16 v[64:67], a[72:75], a[204:207], v[64:67]\n"
      "v_accvgpr_write_b32 a154, v54\n"
      "v_accvgpr_write_b32 a155, v55\n"
      "v_mfma_f32_16x16x32_bf16 v[68:71], a[76:79], a[204:207], v[68:71]\n"
      "v_cvt_pk_bf16_f32 v44, v44, v48\n"
      "v_cvt_pk_bf16_f32 v45, v45, v49\n"
      "v_mfma_f32_16x16x32_bf16 v[72:75], a[80:83], a[204:207], v[72:75]\n"
      "v_cvt_pk_bf16_f32 v46, v46, v50\n"
      "v_cvt_pk_bf16_f32 v47, v47, v51\n"
      "v_mfma_f32_16x16x32_bf16 v[76:79], v[108:111], a[208:211], v[76:79]\n"
      "s_nop 0\n"
      "v_accvgpr_write_b32 a148, v44\n"
      "v_accvgpr_write_b32 a149, v45\n"
      "v_mfma_f32_16x16x32_bf16 v[80:83], v[100:103], a[208:211], v[80:83]\n"
      "s_nop 0\n"
      "v_accvgpr_write_b32 a150, v46\n"
      "v_accvgpr_write_b32 a151, v47\n"
      "v_mfma_f32_4x4x4_16b_bf16 v[44:47], a[244:245], a[240:241], 0\n"
      "v_mfma_f32_4x4x4_16b_bf16 v[48:51], a[244:245], a[242:243], 0\n"
      "ds_read_b64_tr_b16 a[232:233], v61 offset:12608\n"
      "v_mfma_f32_4x4x4_16b_bf16 v[44:47], a[248:249], a[92:93], v[44:47]\n"
      "ds_read_b64_tr_b16 a[234:235], v61 offset:13376\n"
      "v_mfma_f32_4x4x4_16b_bf16 v[48:51], a[248:249], a[94:95], v[48:51]\n"
      "ds_read_b64_tr_b16 a[100:101], v62 offset:39744\n"
      "v_mfma_f32_4x4x4_16b_bf16 v[52:55], a[246:247], a[236:237], v[44:47]\n"
      "ds_read_b64_tr_b16 a[102:103], v62 offset:40512\n"
      "v_mfma_f32_4x4x4_16b_bf16 v[56:59], a[246:247], a[238:239], v[48:51]\n"
      "ds_read_b64_tr_b16 a[228:229], v61 offset:18752\n"
      "v_mfma_f32_4x4x4_16b_bf16 v[44:47], a[250:251], a[236:237], v[44:47]\n"
      "ds_read_b64_tr_b16 a[230:231], v61 offset:19520\n"
      "v_mfma_f32_4x4x4_16b_bf16 v[48:51], a[250:251], a[238:239], v[48:51]\n"
      "ds_read_b128 a[196:199], v2 offset:64\n"
      "ds_read_b128 a[200:203], v2 offset:4160\n"
      "v_cvt_pk_bf16_f32 v36, v36, v40\n"
      "v_cvt_pk_bf16_f32 v37, v37, v41\n"
      "v_cvt_pk_bf16_f32 v38, v38, v42\n"
      "v_cvt_pk_bf16_f32 v39, v39, v43\n"
      "v_accvgpr_write_b32 a144, v36\n"
      "v_accvgpr_write_b32 a145, v37\n"
      "v_accvgpr_write_b32 a146, v38\n"
      "v_accvgpr_write_b32 a147, v39\n"
      "v_mfma_f32_16x16x32_bf16 v[84:87], a[160:163], a[208:211], v[84:87]\n"
      "s_waitcnt lgkmcnt(3)\n"
      "v_cvt_pk_bf16_f32 v28, v28, v32\n"
      "v_cvt_pk_bf16_f32 v29, v29, v33\n"
      "v_mfma_f32_16x16x32_bf16 v[88:91], v[104:107], a[212:215], v[88:91]\n"
      "s_nop 0\n"
      "v_cvt_pk_bf16_f32 v30, v30, v34\n"
      "v_cvt_pk_bf16_f32 v31, v31, v35\n"
      "v_mfma_f32_16x16x32_bf16 v[92:95], a[164:167], a[212:215], v[92:95]\n"
      "s_nop 0\n"
      "v_accvgpr_write_b32 a140, v28\n"
      "v_accvgpr_write_b32 a141, v29\n"
      "v_mfma_f32_16x16x32_bf16 v[96:99], a[156:159], a[212:215], v[96:99]\n"
      "s_nop 0\n"
      "v_accvgpr_write_b32 a142, v30\n"
      "v_accvgpr_write_b32 a143, v31\n"
      "v_mfma_f32_4x4x4_16b_bf16 v[28:31], a[244:245], a[224:225], 0\n"
      "v_mfma_f32_4x4x4_16b_bf16 v[32:35], a[244:245], a[226:227], 0\n"
      "ds_read_b64_tr_b16 a[240:241], v61 offset:12672\n"
      "v_mfma_f32_4x4x4_16b_bf16 v[28:31], a[248:249], a[96:97], v[28:31]\n"
      "ds_read_b64_tr_b16 a[242:243], v61 offset:13440\n"
      "v_mfma_f32_4x4x4_16b_bf16 v[32:35], a[248:249], a[98:99], v[32:35]\n"
      "ds_read_b64_tr_b16 a[104:105], v62 offset:39808\n"
      "v_mfma_f32_4x4x4_16b_bf16 v[36:39], a[246:247], a[220:221], v[28:31]\n"
      "ds_read_b64_tr_b16 a[106:107], v62 offset:40576\n"
      "v_mfma_f32_4x4x4_16b_bf16 v[40:43], a[246:247], a[222:223], v[32:35]\n"
      "ds_read_b64_tr_b16 a[236:237], v61 offset:18816\n"
      "v_mfma_f32_4x4x4_16b_bf16 v[28:31], a[250:251], a[220:221], v[28:31]\n"
      "ds_read_b64_tr_b16 a[238:239], v61 offset:19584\n"
      "v_mfma_f32_4x4x4_16b_bf16 v[32:35], a[250:251], a[222:223], v[32:35]\n"
      "v_cvt_pk_bf16_f32 v52, v52, v56\n"
      "v_cvt_pk_bf16_f32 v53, v53, v57\n"
      "v_cvt_pk_bf16_f32 v54, v54, v58\n"
      "v_cvt_pk_bf16_f32 v55, v55, v59\n"
      "v_accvgpr_write_b32 a136, v52\n"
      "v_accvgpr_write_b32 a137, v53\n"
      "v_accvgpr_write_b32 a138, v54\n"
      "v_accvgpr_write_b32 a139, v55\n"
      "v_cvt_pk_bf16_f32 v44, v44, v48\n"
      "v_cvt_pk_bf16_f32 v45, v45, v49\n"
      "v_mfma_f32_16x16x32_bf16 v[64:67], a[84:87], a[192:195], v[64:67]\n"
      "s_nop 0\n"
      "v_cvt_pk_bf16_f32 v46, v46, v50\n"
      "v_cvt_pk_bf16_f32 v47, v47, v51\n"
      "v_mfma_f32_16x16x32_bf16 v[68:71], a[88:91], a[192:195], v[68:71]\n"
      "s_nop 0\n"
      "v_accvgpr_write_b32 a132, v44\n"
      "v_accvgpr_write_b32 a133, v45\n"
      "v_mfma_f32_16x16x32_bf16 v[72:75], a[92:95], a[192:195], v[72:75]\n"
      "s_nop 0\n"
      "v_accvgpr_write_b32 a134, v46\n"
      "v_accvgpr_write_b32 a135, v47\n"
      "v_mfma_f32_4x4x4_16b_bf16 v[44:47], a[244:245], a[232:233], 0\n"
      "v_mfma_f32_4x4x4_16b_bf16 v[48:51], a[244:245], a[234:235], 0\n"
      "ds_read_b128 a[204:207], v5 offset:128\n"
      "v_mfma_f32_4x4x4_16b_bf16 v[44:47], a[248:249], a[100:101], v[44:47]\n"
      "v_mfma_f32_4x4x4_16b_bf16 v[48:51], a[248:249], a[102:103], v[48:51]\n"
      "s_waitcnt lgkmcnt(1)\n"
      "v_mfma_f32_4x4x4_16b_bf16 v[52:55], a[246:247], a[228:229], v[44:47]\n"
      "v_mfma_f32_4x4x4_16b_bf16 v[56:59], a[246:247], a[230:231], v[48:51]\n"
      "v_mfma_f32_4x4x4_16b_bf16 v[44:47], a[250:251], a[228:229], v[44:47]\n"
      "v_mfma_f32_4x4x4_16b_bf16 v[48:51], a[250:251], a[230:231], v[48:51]\n"
      "v_cvt_pk_bf16_f32 v36, v36, v40\n"
      "v_cvt_pk_bf16_f32 v37, v37, v41\n"
      "v_cvt_pk_bf16_f32 v38, v38, v42\n"
      "v_cvt_pk_bf16_f32 v39, v39, v43\n"
      "v_mfma_f32_16x16x32_bf16 v[76:79], a[152:155], a[196:199], v[76:79]\n"
      "v_accvgpr_write_b32 a128, v36\n"
      "v_accvgpr_write_b32 a129, v37\n"
      "v_mfma_f32_16x16x32_bf16 v[80:83], a[144:147], a[196:199], v[80:83]\n"
      "v_accvgpr_write_b32 a130, v38\n"
      "v_accvgpr_write_b32 a131, v39\n"
      "v_mfma_f32_16x16x32_bf16 v[84:87], a[136:139], a[196:199], v[84:87]\n"
      "v_cvt_pk_bf16_f32 v28, v28, v32\n"
      "v_cvt_pk_bf16_f32 v29, v29, v33\n"
      "v_mfma_f32_16x16x32_bf16 v[88:91], a[148:151], a[200:203], v[88:91]\n"
      "v_cvt_pk_bf16_f32 v30, v30, v34\n"
      "v_cvt_pk_bf16_f32 v31, v31, v35\n"
      "v_mfma_f32_16x16x32_bf16 v[92:95], a[140:143], a[200:203], v[92:95]\n"
      "v_accvgpr_write_b32 a124, v28\n"
      "v_accvgpr_write_b32 a125, v29\n"
      "v_mfma_f32_16x16x32_bf16 v[96:99], a[132:135], a[200:203], v[96:99]\n"
      "v_accvgpr_write_b32 a126, v30\n"
      "v_accvgpr_write_b32 a127, v31\n"
      "v_mfma_f32_4x4x4_16b_bf16 v[28:31], a[244:245], a[240:241], 0\n"
      "v_mfma_f32_4x4x4_16b_bf16 v[32:35], a[244:245], a[242:243], 0\n"
      "ds_read_b128 a[208:211], v2 offset:128\n"
      "v_mfma_f32_4x4x4_16b_bf16 v[28:31], a[248:249], a[104:105], v[28:31]\n"
      "ds_read_b128 a[212:215], v2 offset:4224\n"
      "v_mfma_f32_4x4x4_16b_bf16 v[32:35], a[248:249], a[106:107], v[32:35]\n"
      "v_mfma_f32_4x4x4_16b_bf16 v[36:39], a[246:247], a[236:237], v[28:31]\n"
      "s_nop 0\n"
      "s_nop 0\n"
      "v_mfma_f32_4x4x4_16b_bf16 v[40:43], a[246:247], a[238:239], v[32:35]\n"
      "v_mfma_f32_4x4x4_16b_bf16 v[28:31], a[250:251], a[236:237], v[28:31]\n"
      "v_mfma_f32_4x4x4_16b_bf16 v[32:35], a[250:251], a[238:239], v[32:35]\n"
      "v_cvt_pk_bf16_f32 v52, v52, v56\n"
      "v_cvt_pk_bf16_f32 v53, v53, v57\n"
      "v_cvt_pk_bf16_f32 v54, v54, v58\n"
      "v_cvt_pk_bf16_f32 v55, v55, v59\n"
      "v_accvgpr_write_b32 a120, v52\n"
      "v_accvgpr_write_b32 a121, v53\n"
      "v_accvgpr_write_b32 a122, v54\n"
      "v_accvgpr_write_b32 a123, v55\n"
      "v_cvt_pk_bf16_f32 v44, v44, v48\n"
      "v_cvt_pk_bf16_f32 v45, v45, v49\n"
      "v_cvt_pk_bf16_f32 v46, v46, v50\n"
      "v_cvt_pk_bf16_f32 v47, v47, v51\n"
      "v_accvgpr_write_b32 a116, v44\n"
      "v_accvgpr_write_b32 a117, v45\n"
      "v_accvgpr_write_b32 a118, v46\n"
      "v_accvgpr_write_b32 a119, v47\n"
      "s_waitcnt lgkmcnt(0)\n"
      "s_barrier\n"
      "s_mov_b32 m0, s64\n"
      "s_nop 0\n"
      "v_mfma_f32_16x16x32_bf16 v[64:67], a[96:99], a[204:207], v[64:67]\n"
      "ds_read_b128 a[192:195], v3 offset:16896\n"
      "ds_read_b64_tr_b16 a[220:221], v7 offset:16896\n"
      "ds_read_b64_tr_b16 a[222:223], v7 offset:16904\n"
      "v_cvt_pk_bf16_f32 v36, v36, v40\n"
      "v_cvt_pk_bf16_f32 v37, v37, v41\n"
      "v_mfma_f32_16x16x32_bf16 v[68:71], a[100:103], a[204:207], v[68:71]\n"
      "ds_read_b128 a[196:199], v3 offset:16960\n"
      "v_cvt_pk_bf16_f32 v38, v38, v42\n"
      "v_cvt_pk_bf16_f32 v39, v39, v43\n"
      "v_accvgpr_write_b32 a112, v36\n"
      "v_accvgpr_write_b32 a113, v37\n"
      "v_mfma_f32_16x16x32_bf16 v[72:75], a[104:107], a[204:207], v[72:75]\n"
      "v_accvgpr_write_b32 a114, v38\n"
      "v_accvgpr_write_b32 a115, v39\n"
      "v_cvt_pk_bf16_f32 v28, v28, v32\n"
      "v_cvt_pk_bf16_f32 v29, v29, v33\n"
      "v_cvt_pk_bf16_f32 v30, v30, v34\n"
      "v_cvt_pk_bf16_f32 v31, v31, v35\n"
      "v_accvgpr_write_b32 a108, v28\n"
      "v_mfma_f32_16x16x32_bf16 v[76:79], a[128:131], a[208:211], v[76:79]\n"
      "ds_read_b128 a[200:203], v3 offset:17024\n"
      "ds_read_b64_tr_b16 a[224:225], v7 offset:17408\n"
      "ds_read_b64_tr_b16 a[226:227], v7 offset:17416\n"
      "v_accvgpr_write_b32 a109, v29\n"
      "v_accvgpr_write_b32 a110, v30\n"
      "v_accvgpr_write_b32 a111, v31\n"
      "v_mfma_f32_16x16x32_bf16 v[80:83], a[120:123], a[208:211], v[80:83]\n"
      "v_mfma_f32_16x16x32_bf16 v[84:87], a[112:115], a[208:211], v[84:87]\n"
      "s_waitcnt lgkmcnt(3)\n"
      "v_mfma_f32_4x4x4_16b_bf16 v[20:23], a[216:217], a[220:221], 0\n"
      "v_mfma_f32_4x4x4_16b_bf16 v[24:27], a[216:217], a[222:223], 0\n"
      "v_mfma_f32_16x16x32_bf16 v[88:91], a[124:127], a[212:215], v[88:91]\n"
      "s_nop 0\n"
      "ds_read_b128 a[204:207], v4 offset:16896\n"
      "v_mfma_f32_16x16x32_bf16 v[92:95], a[116:119], a[212:215], v[92:95]\n"
      "v_mfma_f32_16x16x32_bf16 v[96:99], a[108:111], a[212:215], v[96:99]\n"
      "ds_read_b64_tr_b16 a[228:229], v7 offset:17920\n"
      "ds_read_b64_tr_b16 a[230:231], v7 offset:17928\n"
      "v_cvt_pk_bf16_f32 v20, v20, v24\n"
      "v_cvt_pk_bf16_f32 v21, v21, v25\n"
      "v_cvt_pk_bf16_f32 v22, v22, v26\n"
      "v_sub_f32 v76, v88, v76\n"
      "v_sub_f32 v77, v89, v77\n"
      "v_cvt_pk_bf16_f32 v23, v23, v27\n"
      "v_mfma_f32_16x16x32_bf16 v[52:55], a[0:3], a[192:195], 0\n"
      "ds_read_b128 a[208:211], v4 offset:16960\n"
      "ds_write_addtid_b32 v20 offset:41216\n"
      "ds_write_addtid_b32 v21 offset:45312\n"
      "ds_write_addtid_b32 v22 offset:49408\n"
      "v_mfma_f32_16x16x32_bf16 v[56:59], a[4:7], a[192:195], 0\n"
      "s_waitcnt lgkmcnt(6)\n"
      "ds_write_addtid_b32 v76 offset:0\n"
      "ds_write_addtid_b32 v77 offset:512\n"
      "ds_write_addtid_b32 v23 offset:53504\n"
      "v_sub_f32 v78, v90, v78\n"
      "v_mfma_f32_4x4x4_16b_bf16 v[28:31], a[216:217], a[224:225], 0\n"
      "v_add_f32 v88, v88, v64\n"
      "v_mfma_f32_4x4x4_16b_bf16 v[32:35], a[216:217], a[226:227], 0\n"
      "ds_write_addtid_b32 v78 offset:1024\n"
      "v_sub_f32 v79, v91, v79\n"
      "v_add_f32 v89, v89, v65\n"
      "v_sub_f32 v80, v92, v80\n"
      "v_mfma_f32_16x16x32_bf16 v[60:63], a[8:11], a[192:195], 0\n"
      "ds_write_addtid_b32 v79 offset:1536\n"
      "ds_write_addtid_b32 v80 offset:2048\n"
      "v_add_f32 v90, v90, v66\n"
      "v_mfma_f32_16x16x32_bf16 v[52:55], a[12:15], a[196:199], v[52:55]\n"
      "ds_read_b128 a[212:215], v4 offset:17024\n"
      "v_sub_f32 v81, v93, v81\n"
      "v_cvt_pk_bf16_f32 v28, v28, v32\n"
      "v_mfma_f32_16x16x32_bf16 v[56:59], a[16:19], a[196:199], v[56:59]\n"
      "ds_write_addtid_b32 v28 offset:41728\n"
      "ds_read_b64_tr_b16 a[232:233], v7 offset:18432\n"
      "ds_write_addtid_b32 v81 offset:2560\n"
      "ds_read_b64_tr_b16 a[234:235], v7 offset:18440\n"
      "v_mfma_f32_16x16x32_bf16 v[60:63], a[20:23], a[196:199], v[60:63]\n"
      "v_add_f32 v91, v91, v67\n"
      "v_sub_f32 v82, v94, v82\n"
      "v_add_f32_e64 v92, v92, v68\n"
      "v_mfma_f32_16x16x32_bf16 v[52:55], a[24:27], a[200:203], v[52:55]\n"
      "s_waitcnt lgkmcnt(5)\n"
      "ds_read_b128 a[192:195], v5 offset:16896\n"
      "ds_write_addtid_b32 v82 offset:3072\n"
      "v_cvt_pk_bf16_f32 v29, v29, v33\n"
      "v_mfma_f32_4x4x4_16b_bf16 v[20:23], a[216:217], a[228:229], 0\n"
      "v_sub_f32 v83, v95, v83\n"
      "v_add_f32_e64 v93, v93, v69\n"
      "v_mfma_f32_4x4x4_16b_bf16 v[24:27], a[216:217], a[230:231], 0\n"
      "ds_write_addtid_b32 v29 offset:45824\n"
      "ds_write_addtid_b32 v83 offset:3584\n"
      "v_sub_f32 v84, v96, v84\n"
      "v_mfma_f32_16x16x32_bf16 v[56:59], a[28:31], a[200:203], v[56:59]\n"
      "ds_write_addtid_b32 v84 offset:4096\n"
      "v_cvt_pk_bf16_f32 v30, v30, v34\n"
      "v_add_f32 v94, v94, v70\n"
      "v_sub_f32 v85, v97, v85\n"
      "v_mfma_f32_16x16x32_bf16 v[60:63], a[32:35], a[200:203], v[60:63]\n"
      "s_nop 0\n"
      "ds_write_addtid_b32 v30 offset:49920\n"
      "ds_write_addtid_b32 v85 offset:4608\n"
      "v_cvt_pk_bf16_f32 v20, v20, v24\n"
      "v_mfma_f32_16x16x32_bf16 v[52:55], a[36:39], a[204:207], v[52:55]\n"
      "ds_read_b128 a[196:199], v5 offset:16960\n"
      "ds_write_addtid_b32 v20 offset:42240\n"
      "v_cvt_pk_bf16_f32 v31, v31, v35\n"
      "v_mfma_f32_16x16x32_bf16 v[56:59], a[40:43], a[204:207], v[56:59]\n"
      "ds_write_addtid_b32 v31 offset:54016\n"
      "v_sub_f32 v86, v98, v86\n"
      "v_add_f32 v95, v95, v71\n"
      "v_mfma_f32_16x16x32_bf16 v[60:63], a[44:47], a[204:207], v[60:63]\n"
      "s_waitcnt lgkmcnt(5)\n"
      "ds_write_addtid_b32 v86 offset:5120\n"
      "v_sub_f32 v87, v99, v87\n"
      "v_cvt_pk_bf16_f32 v21, v21, v25\n"
      "v_add_f32_e64 v96, v96, v72\n"
      "v_mfma_f32_4x4x4_16b_bf16 v[28:31], a[216:217], a[232:233], 0\n"
      "v_cvt_pk_bf16_f32 v22, v22, v26\n"
      "v_add_f32 v97, v97, v73\n"
      "v_mfma_f32_4x4x4_16b_bf16 v[32:35], a[216:217], a[234:235], 0\n"
      "ds_write_addtid_b32 v21 offset:46336\n"
      "ds_write_addtid_b32 v22 offset:50432\n"
      "ds_write_addtid_b32 v87 offset:5632\n"
      "v_mfma_f32_16x16x32_bf16 v[52:55], a[48:51], a[208:211], v[52:55]\n"
      "s_nop 0\n"
      "ds_read_b128 a[200:203], v5 offset:17024\n"
      "v_cvt_pk_bf16_f32 v28, v28, v32\n"
      "v_cvt_pk_bf16_f32 v23, v23, v27\n"
      "v_mfma_f32_16x16x32_bf16 v[56:59], a[52:55], a[208:211], v[56:59]\n"
      "ds_write_addtid_b32 v28 offset:42752\n"
      "ds_write_addtid_b32 v23 offset:54528\n"
      "v_cvt_pk_bf16_f32 v29, v29, v33\n"
      "v_cvt_pk_bf16_f32 v30, v30, v34\n"
      "v_cvt_pk_bf16_f32 v31, v31, v35\n"
      "v_mfma_f32_16x16x32_bf16 v[60:63], a[56:59], a[208:211], v[60:63]\n"
      "ds_write_addtid_b32 v29 offset:46848\n"
      "ds_write_addtid_b32 v30 offset:50944\n"
      "v_mfma_f32_16x16x32_bf16 v[52:55], a[60:63], a[212:215], v[52:55]\n"
      "ds_write_addtid_b32 v31 offset:55040\n"
      "v_add_f32 v98, v98, v74\n"
      "v_add_f32 v99, v99, v75\n"
      "v_fma_f32 v40, 0.5, v76, -v88\n"
      "v_mfma_f32_16x16x32_bf16 v[56:59], a[64:67], a[212:215], v[56:59]\n"
      "v_fma_f32 v41, 0.5, v77, -v89\n"
      "v_fma_f32 v42, 0.5, v78, -v90\n"
      "v_fma_f32 v43, 0.5, v79, -v91\n"
      "v_mfma_f32_16x16x32_bf16 v[60:63], a[68:71], a[212:215], v[60:63]\n"
      "s_waitcnt lgkmcnt(0)\n"
      "v_fma_f32 v44, 0.5, v80, -v92\n"
      "v_fma_f32 v45, 0.5, v81, -v93\n"
      "s_barrier\n"
      "LSloop_%=:\n"
      "v_mfma_f32_16x16x32_bf16 v[52:55], a[72:75], a[192:195], v[52:55]\n"
      "ds_read_b128 a[204:207], v0 offset:16384\n"
      "ds_read2st64_b32 v[28:29], v6 offset0:0 offset1:2\n"
      "v_fma_f32 v46, 0.5, v82, -v94\n"
      "v_mfma_f32_16x16x32_bf16 v[56:59], a[76:79], a[192:195], v[56:59]\n"
      "ds_read_b128 a[208:211], v0 offset:20480\n"
      "ds_read2st64_b32 v[30:31], v6 offset0:4 offset1:6\n"
      "v_mfma_f32_16x16x32_bf16 v[60:63], a[80:83], a[192:195], v[60:63]\n"
      "ds_read_b64_tr_b16 a[220:221], v7 offset:33792\n"
      "v_fma_f32 v47, 0.5, v83, -v95\n"
      "v_mfma_f32_16x16x32_bf16 v[52:55], a[84:87], a[196:199], v[52:55]\n"
      "ds_read_b128 a[212:215], v0 offset:16448\n"
      "v_fma_f32 v48, 0.5, v84, -v96\n"
      "v_mfma_f32_16x16x32_bf16 v[56:59], a[88:91], a[196:199], v[56:59]\n"
      "ds_read_b64_tr_b16 a[222:223], v7 offset:33800\n"
      "ds_read2st64_b32 v[32:33], v6 offset0:8 offset1:10\n"
      "v_fma_f32 v49, 0.5, v85, -v97\n"
      "v_mfma_f32_16x16x32_bf16 v[60:63], a[92:95], a[196:199], v[60:63]\n"
      "ds_read2st64_b32 v[34:35], v6 offset0:12 offset1:14\n"
      "v_fma_f32 v50, 0.5, v86, -v98\n"
      "v_mfma_f32_16x16x32_bf16 v[52:55], a[96:99], a[200:203], v[52:55]\n"
      "ds_read_b128 a[192:195], v0 offset:20544\n"
      "ds_read2st64_b32 v[36:37], v6 offset0:16 offset1:18\n"
      "v_mfma_f32_16x16x32_bf16 v[56:59], a[100:103], a[200:203], v[56:59]\n"
      "ds_read2st64_b32 v[38:39], v6 offset0:20 offset1:22\n"
      "v_fma_f32 v51, 0.5, v87, -v99\n"
      "v_mfma_f32_16x16x32_bf16 v[60:63], a[104:107], a[200:203], v[60:63]\n"
      "s_waitcnt lgkmcnt(8)\n"
      "v_fmac_f32 v28, s46, v64\n"
      "v_fmac_f32 v29, s46, v65\n"
      "v_fmac_f32 v30, s46, v66\n"
      "v_mfma_f32_16x16x32_bf16 v[76:79], v[252:255], a[204:207], v[52:55]\n"
      "s_waitcnt lgkmcnt(5)\n"
      "ds_read_b128 a[196:199], v0 offset:16512\n"
      "v_fmac_f32 v31, s46, v67\n"
      "v_mfma_f32_4x4x4_16b_bf16 v[12:15], a[216:217], a[220:221], 0\n"
      "v_sub_f32 v28, v28, v40\n"
      "v_mfma_f32_4x4x4_16b_bf16 v[16:19], a[216:217], a[222:223], 0\n"
      "v_sub_f32 v29, v29, v41\n"
      "v_sub_f32 v30, v30, v42\n"
      "v_mfma_f32_16x16x32_bf16 v[80:83], v[244:247], a[204:207], v[56:59]\n"
      "v_sub_f32 v31, v31, v43\n"
      "v_fmac_f32 v40, s45, v28\n"
      "v_mfma_f32_16x16x32_bf16 v[84:87], v[236:239], a[204:207], v[60:63]\n"
      "v_fmac_f32 v41, s45, v29\n"
      "v_cvt_pk_bf16_f32 v12, v12, v16\n"
      "v_fmac_f32_e64 v42, s45, v30\n"
      "v_mfma_f32_16x16x32_bf16 v[88:91], v[248:251], a[208:211], v[52:55]\n"
      "ds_read_b128 a[200:203], v0 offset:20608\n"
      "ds_write_addtid_b32 v12 offset:24832\n"
      "v_mfma_f32_16x16x32_bf16 v[92:95], v[240:243], a[208:211], v[56:59]\n"
      "ds_read_b64_tr_b16 a[224:225], v7 offset:34304\n"
      "ds_read_b64_tr_b16 a[226:227], v7 offset:34312\n"
      "v_mfma_f32_16x16x32_bf16 v[96:99], v[232:235], a[208:211], v[60:63]\n"
      "v_fmac_f32_e64 v43, s45, v31\n"
      "v_cvt_pk_bf16_f32 v13, v13, v17\n"
      "v_mfma_f32_16x16x32_bf16 v[76:79], v[228:231], a[212:215], v[76:79]\n"
      "ds_read_b128 a[204:207], v1 offset:16384\n"
      "ds_write_addtid_b32 v13 offset:28928\n"
      "v_fma_f32 v28, s47, v28, v52\n"
      "v_mfma_f32_16x16x32_bf16 v[80:83], v[220:223], a[212:215], v[80:83]\n"
      "v_fma_f32 v29, s47, v29, v53\n"
      "v_cvt_pk_bf16_f32 v14, v14, v18\n"
      "v_mfma_f32_16x16x32_bf16 v[84:87], v[212:215], a[212:215], v[84:87]\n"
      "s_waitcnt lgkmcnt(6)\n"
      "ds_write_addtid_b32 v14 offset:33024\n"
      "v_fma_f32 v30, s47, v30, v54\n"
      "v_mfma_f32_16x16x32_bf16 v[88:91], v[224:227], a[192:195], v[88:91]\n"
      "s_nop 0\n"
      "ds_read_b128 a[208:211], v1 offset:20480\n"
      "v_fma_f32 v31, s47, v31, v55\n"
      "v_mfma_f32_16x16x32_bf16 v[92:95], v[216:219], a[192:195], v[92:95]\n"
      "s_nop 0\n"
      "v_cvt_pk_bf16_f32 v40, v40, v41\n"
      "v_cvt_pk_bf16_f32 v15, v15, v19\n"
      "v_mfma_f32_16x16x32_bf16 v[96:99], v[208:211], a[192:195], v[96:99]\n"
      "s_waitcnt lgkmcnt(4)\n"
      "ds_write_addtid_b32 v40 offset:12288\n"
      "ds_write_addtid_b32 v15 offset:37120\n"
      "v_cvt_pk_bf16_f32 v42, v42, v43\n"
      "v_mfma_f32_4x4x4_16b_bf16 v[20:23], a[216:217], a[224:225], 0\n"
      "v_cvt_pk_bf16_f32 v28, v28, v29\n"
      "v_mfma_f32_4x4x4_16b_bf16 v[24:27], a[216:217], a[226:227], 0\n"
      "ds_write_addtid_b32 v42 offset:12800\n"
      "ds_write_addtid_b32 v28 offset:14336\n"
      "v_mfma_f32_16x16x32_bf16 v[76:79], v[204:207], a[196:199], v[76:79]\n"
      "ds_read_b128 a[212:215], v1 offset:16448\n"
      "v_cvt_pk_bf16_f32 v30, v30, v31\n"
      "v_mfma_f32_16x16x32_bf16 v[80:83], v[196:199], a[196:199], v[80:83]\n"
      "ds_write_addtid_b32 v30 offset:14848\n"
      "v_fmac_f32 v32, s46, v68\n"
      "v_cvt_pk_bf16_f32 v20, v20, v24\n"
      "v_mfma_f32_16x16x32_bf16 v[84:87], v[188:191], a[196:199], v[84:87]\n"
      "ds_write_addtid_b32 v20 offset:25344\n"
      "v_fmac_f32 v33, s46, v69\n"
      "v_mfma_f32_16x16x32_bf16 v[88:91], v[200:203], a[200:203], v[88:91]\n"
      "ds_read_b128 a[192:195], v1 offset:20544\n"
      "ds_read_b64_tr_b16 a[228:229], v7 offset:34816\n"
      "v_mfma_f32_16x16x32_bf16 v[92:95], v[192:195], a[200:203], v[92:95]\n"
      "ds_read_b64_tr_b16 a[230:231], v7 offset:34824\n"
      "v_fmac_f32 v34, s46, v70\n"
      "v_mfma_f32_16x16x32_bf16 v[96:99], v[184:187], a[200:203], v[96:99]\n"
      "s_waitcnt lgkmcnt(4)\n"
      "v_fmac_f32 v35, s46, v71\n"
      "v_cvt_pk_bf16_f32 v21, v21, v25\n"
      "v_mfma_f32_16x16x32_bf16 v[76:79], v[180:183], a[204:207], v[76:79]\n"
      "s_nop 0\n"
      "ds_read_b128 a[196:199], v1 offset:16512\n"
      "ds_write_addtid_b32 v21 offset:29440\n"
      "v_sub_f32 v32, v32, v44\n"
      "v_mfma_f32_16x16x32_bf16 v[80:83], v[172:175], a[204:207], v[80:83]\n"
      "v_sub_f32 v33, v33, v45\n"
      "v_cvt_pk_bf16_f32 v22, v22, v26\n"
      "v_mfma_f32_16x16x32_bf16 v[84:87], v[164:167], a[204:207], v[84:87]\n"
      "ds_write_addtid_b32 v22 offset:33536\n"
      "v_sub_f32 v34, v34, v46\n"
      "v_mfma_f32_16x16x32_bf16 v[88:91], v[176:179], a[208:211], v[88:91]\n"
      "s_nop 0\n"
      "ds_read_b128 a[200:203], v1 offset:20608\n"
      "v_sub_f32 v35, v35, v47\n"
      "v_mfma_f32_16x16x32_bf16 v[92:95], v[168:171], a[208:211], v[92:95]\n"
      "s_waitcnt lgkmcnt(4)\n"
      "v_fmac_f32 v44, s45, v32\n"
      "v_cvt_pk_bf16_f32 v23, v23, v27\n"
      "v_mfma_f32_4x4x4_16b_bf16 v[12:15], a[216:217], a[228:229], 0\n"
      "v_fmac_f32 v45, s45, v33\n"
      "v_mfma_f32_4x4x4_16b_bf16 v[16:19], a[216:217], a[230:231], 0\n"
      "ds_write_addtid_b32 v23 offset:37632\n"
      "v_fmac_f32 v46, s45, v34\n"
      "v_mfma_f32_16x16x32_bf16 v[96:99], v[160:163], a[208:211], v[96:99]\n"
      "v_fmac_f32 v47, s45, v35\n"
      "v_fma_f32 v32, s47, v32, v56\n"
      "v_cvt_pk_bf16_f32 v12, v12, v16\n"
      "v_mfma_f32_16x16x32_bf16 v[76:79], v[156:159], a[212:215], v[76:79]\n"
      "ds_read_b128 a[204:207], v2 offset:16384\n"
      "ds_write_addtid_b32 v12 offset:25856\n"
      "v_mfma_f32_16x16x32_bf16 v[80:83], v[148:151], a[212:215], v[80:83]\n"
      "v_fma_f32 v33, s47, v33, v57\n"
      "v_cvt_pk_bf16_f32 v13, v13, v17\n"
      "v_mfma_f32_16x16x32_bf16 v[84:87], v[140:143], a[212:215], v[84:87]\n"
      "ds_read_b64_tr_b16 a[232:233], v7 offset:35328\n"
      "ds_write_addtid_b32 v13 offset:29952\n"
      "v_fma_f32 v34, s47, v34, v58\n"
      "v_mfma_f32_16x16x32_bf16 v[88:91], v[152:155], a[192:195], v[88:91]\n"
      "ds_read_b128 a[208:211], v2 offset:20480\n"
      "ds_read_b64_tr_b16 a[234:235], v7 offset:35336\n"
      "v_mfma_f32_16x16x32_bf16 v[92:95], v[144:147], a[192:195], v[92:95]\n"
      "s_nop 0\n"
      "v_fma_f32 v35, s47, v35, v59\n"
      "v_cvt_pk_bf16_f32 v44, v44, v45\n"
      "v_mfma_f32_16x16x32_bf16 v[96:99], v[136:139], a[192:195], v[96:99]\n"
      "s_waitcnt lgkmcnt(4)\n"
      "ds_write_addtid_b32 v44 offset:20484\n"
      "v_cvt_pk_bf16_f32 v14, v14, v18\n"
      "v_mfma_f32_16x16x32_bf16 v[76:79], v[132:135], a[196:199], v[76:79]\n"
      "ds_read_b128 a[212:215], v2 offset:16448\n"
      "ds_write_addtid_b32 v14 offset:34048\n"
      "v_cvt_pk_bf16_f32 v46, v46, v47\n"
      "v_mfma_f32_16x16x32_bf16 v[80:83], v[124:127], a[196:199], v[80:83]\n"
      "ds_write_addtid_b32 v46 offset:20996\n"
      "v_cvt_pk_bf16_f32 v32, v32, v33\n"
      "v_mfma_f32_16x16x32_bf16 v[84:87], v[116:119], a[196:199], v[84:87]\n"
      "ds_write_addtid_b32 v32 offset:22532\n"
      "v_cvt_pk_bf16_f32 v34, v34, v35\n"
      "v_mfma_f32_16x16x32_bf16 v[88:91], v[128:131], a[200:203], v[88:91]\n"
      "s_waitcnt lgkmcnt(4)\n"
      "ds_read_b128 a[192:195], v2 offset:20544\n"
      "ds_write_addtid_b32 v34 offset:23044\n"
      "v_fmac_f32 v36, s46, v72\n"
      "v_mfma_f32_4x4x4_16b_bf16 v[20:23], a[216:217], a[232:233], 0\n"
      "v_fmac_f32_e64 v37, s46, v73\n"
      "v_mfma_f32_4x4x4_16b_bf16 v[24:27], a[216:217], a[234:235], 0\n"
      "v_fmac_f32 v38, s46, v74\n"
      "v_cvt_pk_bf16_f32 v15, v15, v19\n"
      "v_mfma_f32_16x16x32_bf16 v[92:95], v[120:123], a[200:203], v[92:95]\n"
      "ds_write_addtid_b32 v15 offset:38144\n"
      "v_fmac_f32 v39, s46, v75\n"
      "v_cvt_pk_bf16_f32 v20, v20, v24\n"
      "v_mfma_f32_16x16x32_bf16 v[96:99], v[112:115], a[200:203], v[96:99]\n"
      "ds_write_addtid_b32 v20 offset:26368\n"
      "v_sub_f32 v36, v36, v48\n"
      "v_mfma_f32_16x16x32_bf16 v[76:79], v[108:111], a[204:207], v[76:79]\n"
      "ds_read_b128 a[196:199], v2 offset:16512\n"
      "v_sub_f32 v37, v37, v49\n"
      "v_mfma_f32_16x16x32_bf16 v[80:83], v[100:103], a[204:207], v[80:83]\n"
      "ds_read_b64_tr_b16 a[220:221], v7 offset:35840\n"
      "ds_read_b64_tr_b16 a[222:223], v7 offset:35848\n"
      "v_mfma_f32_16x16x32_bf16 v[84:87], a[160:163], a[204:207], v[84:87]\n"
      "v_sub_f32_e64 v38, v38, v50\n"
      "v_cvt_pk_bf16_f32 v21, v21, v25\n"
      "v_mfma_f32_16x16x32_bf16 v[88:91], v[104:107], a[208:211], v[88:91]\n"
      "ds_read_b128 a[200:203], v2 offset:20608\n"
      "ds_write_addtid_b32 v21 offset:30464\n"
      "v_sub_f32 v39, v39, v51\n"
      "v_mfma_f32_16x16x32_bf16 v[92:95], a[164:167], a[208:211], v[92:95]\n"
      "s_nop 0\n"
      "v_fmac_f32_e64 v48, s45, v36\n"
      "v_cvt_pk_bf16_f32 v22, v22, v26\n"
      "v_mfma_f32_16x16x32_bf16 v[96:99], a[156:159], a[208:211], v[96:99]\n"
      "s_waitcnt lgkmcnt(4)\n"
      "ds_write_addtid_b32 v22 offset:34560\n"
      "v_fmac_f32 v49, s45, v37\n"
      "v_mfma_f32_16x16x32_bf16 v[76:79], a[152:155], a[212:215], v[76:79]\n"
      "s_nop 0\n"
      "ds_read_b128 a[204:207], v3 offset:33792\n"
      "v_fmac_f32_e64 v50, s45, v38\n"
      "v_mfma_f32_16x16x32_bf16 v[80:83], a[144:147], a[212:215], v[80:83]\n"
      "s_nop 0\n"
      "v_fmac_f32 v51, s45, v39\n"
      "v_cvt_pk_bf16_f32 v23, v23, v27\n"
      "v_mfma_f32_16x16x32_bf16 v[84:87], a[136:139], a[212:215], v[84:87]\n"
      "s_waitcnt lgkmcnt(2)\n"
      "ds_write_addtid_b32 v23 offset:38656\n"
      "v_fma_f32 v36, s47, v36, v60\n"
      "v_mfma_f32_4x4x4_16b_bf16 v[12:15], a[216:217], a[220:221], 0\n"
      "v_fma_f32 v37, s47, v37, v61\n"
      "v_mfma_f32_4x4x4_16b_bf16 v[16:19], a[216:217], a[222:223], 0\n"
      "v_fma_f32 v38, s47, v38, v62\n"
      "v_fma_f32 v39, s47, v39, v63\n"
      "v_mfma_f32_16x16x32_bf16 v[88:91], a[148:151], a[192:195], v[88:91]\n"
      "ds_read_b128 a[208:211], v3 offset:33856\n"
      "v_cvt_pk_bf16_f32 v48, v48, v49\n"
      "v_cvt_pk_bf16_f32 v12, v12, v16\n"
      "v_mfma_f32_16x16x32_bf16 v[92:95], a[140:143], a[192:195], v[92:95]\n"
      "ds_write_addtid_b32 v48 offset:16384\n"
      "ds_write_addtid_b32 v12 offset:26880\n"
      "v_mfma_f32_16x16x32_bf16 v[96:99], a[132:135], a[192:195], v[96:99]\n"
      "v_cvt_pk_bf16_f32 v13, v13, v17\n"
      "v_cvt_pk_bf16_f32 v14, v14, v18\n"
      "v_mfma_f32_16x16x32_bf16 v[76:79], a[128:131], a[196:199], v[76:79]\n"
      "ds_read_b128 a[212:215], v3 offset:33920\n"
      "ds_write_addtid_b32 v13 offset:30976\n"
      "v_mfma_f32_16x16x32_bf16 v[80:83], a[120:123], a[196:199], v[80:83]\n"
      "ds_read_b64_tr_b16 a[224:225], v7 offset:36352\n"
      "ds_write_addtid_b32 v14 offset:35072\n"
      "v_cvt_pk_bf16_f32 v50, v50, v51\n"
      "v_mfma_f32_16x16x32_bf16 v[84:87], a[112:115], a[196:199], v[84:87]\n"
      "ds_write_addtid_b32 v50 offset:16896\n"
      "ds_read_b64_tr_b16 a[226:227], v7 offset:36360\n"
      "v_mfma_f32_16x16x32_bf16 v[88:91], a[124:127], a[200:203], v[88:91]\n"
      "ds_read_b128 a[192:195], v4 offset:33792\n"
      "v_cvt_pk_bf16_f32 v36, v36, v37\n"
      "v_mfma_f32_16x16x32_bf16 v[92:95], a[116:119], a[200:203], v[92:95]\n"
      "ds_write_addtid_b32 v36 offset:18432\n"
      "v_cvt_pk_bf16_f32 v15, v15, v19\n"
      "v_mfma_f32_16x16x32_bf16 v[96:99], a[108:111], a[200:203], v[96:99]\n"
      "s_waitcnt lgkmcnt(6)\n"
      "ds_write_addtid_b32 v15 offset:39168\n"
      "v_sub_f32 v76, v88, v76\n"
      "v_sub_f32_e64 v77, v89, v77\n"
      "v_cvt_pk_bf16_f32 v38, v38, v39\n"
      "v_mfma_f32_16x16x32_bf16 v[64:67], a[0:3], a[204:207], 0\n"
      "s_nop 0\n"
      "ds_read_b128 a[196:199], v4 offset:33856\n"
      "ds_write_addtid_b32 v38 offset:18944\n"
      "v_mfma_f32_16x16x32_bf16 v[68:71], a[4:7], a[204:207], 0\n"
      "s_waitcnt lgkmcnt(5)\n"
      "ds_write_addtid_b32 v76 offset:6144\n"
      "ds_write_addtid_b32 v77 offset:6656\n"
      "v_mfma_f32_4x4x4_16b_bf16 v[20:23], a[216:217], a[224:225], 0\n"
      "v_sub_f32 v78, v90, v78\n"
      "v_mfma_f32_4x4x4_16b_bf16 v[24:27], a[216:217], a[226:227], 0\n"
      "ds_write_addtid_b32 v78 offset:7168\n"
      "v_sub_f32 v79, v91, v79\n"
      "v_mfma_f32_16x16x32_bf16 v[72:75], a[8:11], a[204:207], 0\n"
      "ds_write_addtid_b32 v79 offset:7680\n"
      "v_sub_f32 v80, v92, v80\n"
      "v_sub_f32 v81, v93, v81\n"
      "v_mfma_f32_16x16x32_bf16 v[64:67], a[12:15], a[208:211], v[64:67]\n"
      "ds_read_b128 a[200:203], v4 offset:33920\n"
      "ds_write_addtid_b32 v80 offset:8192\n"
      "v_mfma_f32_16x16x32_bf16 v[68:71], a[16:19], a[208:211], v[68:71]\n"
      "ds_write_addtid_b32 v81 offset:8704\n"
      "v_sub_f32 v82, v94, v82\n"
      "v_mfma_f32_16x16x32_bf16 v[72:75], a[20:23], a[208:211], v[72:75]\n"
      "s_nop 0\n"
      "ds_write_addtid_b32 v82 offset:9216\n"
      "v_cvt_pk_bf16_f32 v20, v20, v24\n"
      "v_mfma_f32_16x16x32_bf16 v[64:67], a[24:27], a[212:215], v[64:67]\n"
      "ds_read_b128 a[204:207], v5 offset:33792\n"
      "ds_write_addtid_b32 v20 offset:27392\n"
      "v_sub_f32 v83, v95, v83\n"
      "v_mfma_f32_16x16x32_bf16 v[68:71], a[28:31], a[212:215], v[68:71]\n"
      "ds_write_addtid_b32 v83 offset:9728\n"
      "v_sub_f32_e64 v84, v96, v84\n"
      "v_mfma_f32_16x16x32_bf16 v[72:75], a[32:35], a[212:215], v[72:75]\n"
      "s_waitcnt lgkmcnt(5)\n"
      "ds_write_addtid_b32 v84 offset:10240\n"
      "v_cvt_pk_bf16_f32 v21, v21, v25\n"
      "v_mfma_f32_16x16x32_bf16 v[64:67], a[36:39], a[192:195], v[64:67]\n"
      "ds_read_b128 a[208:211], v5 offset:33856\n"
      "ds_write_addtid_b32 v21 offset:31488\n"
      "v_mfma_f32_16x16x32_bf16 v[68:71], a[40:43], a[192:195], v[68:71]\n"
      "v_sub_f32 v85, v97, v85\n"
      "v_sub_f32 v86, v98, v86\n"
      "v_cvt_pk_bf16_f32 v22, v22, v26\n"
      "v_mfma_f32_16x16x32_bf16 v[72:75], a[44:47], a[192:195], v[72:75]\n"
      "ds_write_addtid_b32 v22 offset:35584\n"
      "ds_write_addtid_b32 v85 offset:10752\n"
      "v_sub_f32 v87, v99, v87\n"
      "v_mfma_f32_16x16x32_bf16 v[64:67], a[48:51], a[196:199], v[64:67]\n"
      "ds_read_b128 a[212:215], v5 offset:33920\n"
      "ds_write_addtid_b32 v86 offset:11264\n"
      "v_mfma_f32_16x16x32_bf16 v[68:71], a[52:55], a[196:199], v[68:71]\n"
      "s_nop 0\n"
      "ds_write_addtid_b32 v87 offset:11776\n"
      "v_cvt_pk_bf16_f32 v23, v23, v27\n"
      "v_mfma_f32_16x16x32_bf16 v[72:75], a[56:59], a[196:199], v[72:75]\n"
      "ds_write_addtid_b32 v23 offset:39680\n"
      "v_fma_f32 v40, 0.5, v76, -v88\n"
      "v_mfma_f32_16x16x32_bf16 v[64:67], a[60:63], a[200:203], v[64:67]\n"
      "v_fma_f32 v41, 0.5, v77, -v89\n"
      "v_fma_f32 v42, 0.5, v78, -v90\n"
      "v_mfma_f32_16x16x32_bf16 v[68:71], a[64:67], a[200:203], v[68:71]\n"
      "v_fma_f32 v43, 0.5, v79, -v91\n"
      "v_fma_f32 v44, 0.5, v80, -v92\n"
      "v_mfma_f32_16x16x32_bf16 v[72:75], a[68:71], a[200:203], v[72:75]\n"
      "s_waitcnt lgkmcnt(0)\n"
      "v_fma_f32 v45, 0.5, v81, -v93\n"
      "s_barrier\n"
      "LSmid_%=:\n"
      "v_mfma_f32_16x16x32_bf16 v[64:67], a[72:75], a[204:207], v[64:67]\n"
      "ds_read_b128 a[192:195], v0 offset:0\n"
      "ds_read2st64_b32 v[28:29], v6 offset0:24 offset1:26\n"
      "v_fma_f32 v46, 0.5, v82, -v94\n"
      "v_mfma_f32_16x16x32_bf16 v[68:71], a[76:79], a[204:207], v[68:71]\n"
      "ds_read_b128 a[196:199], v0 offset:4096\n"
      "ds_read2st64_b32 v[30:31], v6 offset0:28 offset1:30\n"
      "v_mfma_f32_16x16x32_bf16 v[72:75], a[80:83], a[204:207], v[72:75]\n"
      "ds_read_b64_tr_b16 a[220:221], v7 offset:50688\n"
      "v_fma_f32 v47, 0.5, v83, -v95\n"
      "v_mfma_f32_16x16x32_bf16 v[64:67], a[84:87], a[208:211], v[64:67]\n"
      "ds_read_b128 a[200:203], v0 offset:64\n"
      "v_fma_f32 v48, 0.5, v84, -v96\n"
      "v_mfma_f32_16x16x32_bf16 v[68:71], a[88:91], a[208:211], v[68:71]\n"
      "ds_read_b64_tr_b16 a[222:223], v7 offset:50696\n"
      "ds_read2st64_b32 v[32:33], v6 offset0:32 offset1:34\n"
      "v_fma_f32 v49, 0.5, v85, -v97\n"
      "v_mfma_f32_16x16x32_bf16 v[72:75], a[92:95], a[208:211], v[72:75]\n"
      "ds_read2st64_b32 v[34:35], v6 offset0:36 offset1:38\n"
      "v_fma_f32 v50, 0.5, v86, -v98\n"
      "v_mfma_f32_16x16x32_bf16 v[64:67], a[96:99], a[212:215], v[64:67]\n"
      "ds_read_b128 a[204:207], v0 offset:4160\n"
      "ds_read2st64_b32 v[36:37], v6 offset0:40 offset1:42\n"
      "v_mfma_f32_16x16x32_bf16 v[68:71], a[100:103], a[212:215], v[68:71]\n"
      "ds_read2st64_b32 v[38:39], v6 offset0:44 offset1:46\n"
      "v_fma_f32 v51, 0.5, v87, -v99\n"
      "v_mfma_f32_16x16x32_bf16 v[72:75], a[104:107], a[212:215], v[72:75]\n"
      "s_waitcnt lgkmcnt(8)\n"
      "v_fmac_f32 v28, s46, v52\n"
      "v_fmac_f32 v29, s46, v53\n"
      "v_fmac_f32 v30, s46, v54\n"
      "v_mfma_f32_16x16x32_bf16 v[76:79], v[252:255], a[192:195], v[64:67]\n"
      "s_waitcnt lgkmcnt(5)\n"
      "ds_read_b128 a[208:211], v0 offset:128\n"
      "v_fmac_f32 v31, s46, v55\n"
      "v_mfma_f32_4x4x4_16b_bf16 v[12:15], a[216:217], a[220:221], 0\n"
      "v_sub_f32_e64 v28, v28, v40\n"
      "v_mfma_f32_4x4x4_16b_bf16 v[16:19], a[216:217], a[222:223], 0\n"
      "v_sub_f32 v29, v29, v41\n"
      "v_sub_f32 v30, v30, v42\n"
      "v_mfma_f32_16x16x32_bf16 v[80:83], v[244:247], a[192:195], v[68:71]\n"
      "v_sub_f32 v31, v31, v43\n"
      "v_fmac_f32 v40, s45, v28\n"
      "v_mfma_f32_16x16x32_bf16 v[84:87], v[236:239], a[192:195], v[72:75]\n"
      "v_fmac_f32 v41, s45, v29\n"
      "v_cvt_pk_bf16_f32 v12, v12, v16\n"
      "v_fmac_f32 v42, s45, v30\n"
      "v_mfma_f32_16x16x32_bf16 v[88:91], v[248:251], a[196:199], v[64:67]\n"
      "ds_read_b128 a[212:215], v0 offset:4224\n"
      "ds_write_addtid_b32 v12 offset:41216\n"
      "v_mfma_f32_16x16x32_bf16 v[92:95], v[240:243], a[196:199], v[68:71]\n"
      "ds_read_b64_tr_b16 a[224:225], v7 offset:51200\n"
      "ds_read_b64_tr_b16 a[226:227], v7 offset:51208\n"
      "v_mfma_f32_16x16x32_bf16 v[96:99], v[232:235], a[196:199], v[72:75]\n"
      "v_fmac_f32_e64 v43, s45, v31\n"
      "v_cvt_pk_bf16_f32 v13, v13, v17\n"
      "v_mfma_f32_16x16x32_bf16 v[76:79], v[228:231], a[200:203], v[76:79]\n"
      "s_nop 0\n"
      "ds_read_b128 a[192:195], v1 offset:0\n"
      "ds_write_addtid_b32 v13 offset:45312\n"
      "v_fma_f32 v28, s47, v28, v64\n"
      "v_mfma_f32_16x16x32_bf16 v[80:83], v[220:223], a[200:203], v[80:83]\n"
      "s_nop 0\n"
      "v_fma_f32 v29, s47, v29, v65\n"
      "v_cvt_pk_bf16_f32 v14, v14, v18\n"
      "v_mfma_f32_16x16x32_bf16 v[84:87], v[212:215], a[200:203], v[84:87]\n"
      "s_waitcnt lgkmcnt(6)\n"
      "ds_write_addtid_b32 v14 offset:49408\n"
      "v_fma_f32 v30, s47, v30, v66\n"
      "v_mfma_f32_16x16x32_bf16 v[88:91], v[224:227], a[204:207], v[88:91]\n"
      "s_nop 0\n"
      "ds_read_b128 a[196:199], v1 offset:4096\n"
      "v_fma_f32 v31, s47, v31, v67\n"
      "v_mfma_f32_16x16x32_bf16 v[92:95], v[216:219], a[204:207], v[92:95]\n"
      "s_nop 0\n"
      "v_cvt_pk_bf16_f32 v40, v40, v41\n"
      "v_cvt_pk_bf16_f32 v15, v15, v19\n"
      "v_mfma_f32_16x16x32_bf16 v[96:99], v[208:211], a[204:207], v[96:99]\n"
      "s_waitcnt lgkmcnt(4)\n"
      "ds_write_addtid_b32 v40 offset:13312\n"
      "ds_write_addtid_b32 v15 offset:53504\n"
      "v_cvt_pk_bf16_f32 v42, v42, v43\n"
      "v_mfma_f32_4x4x4_16b_bf16 v[20:23], a[216:217], a[224:225], 0\n"
      "v_cvt_pk_bf16_f32 v28, v28, v29\n"
      "v_mfma_f32_4x4x4_16b_bf16 v[24:27], a[216:217], a[226:227], 0\n"
      "ds_write_addtid_b32 v42 offset:13824\n"
      "ds_write_addtid_b32 v28 offset:15360\n"
      "v_mfma_f32_16x16x32_bf16 v[76:79], v[204:207], a[208:211], v[76:79]\n"
      "ds_read_b128 a[200:203], v1 offset:64\n"
      "v_cvt_pk_bf16_f32 v30, v30, v31\n"
      "v_mfma_f32_16x16x32_bf16 v[80:83], v[196:199], a[208:211], v[80:83]\n"
      "ds_write_addtid_b32 v30 offset:15872\n"
      "v_fmac_f32 v32, s46, v56\n"
      "v_cvt_pk_bf16_f32 v20, v20, v24\n"
      "v_mfma_f32_16x16x32_bf16 v[84:87], v[188:191], a[208:211], v[84:87]\n"
      "ds_write_addtid_b32 v20 offset:41728\n"
      "v_fmac_f32 v33, s46, v57\n"
      "v_mfma_f32_16x16x32_bf16 v[88:91], v[200:203], a[212:215], v[88:91]\n"
      "ds_read_b128 a[204:207], v1 offset:4160\n"
      "ds_read_b64_tr_b16 a[228:229], v7 offset:51712\n"
      "v_mfma_f32_16x16x32_bf16 v[92:95], v[192:195], a[212:215], v[92:95]\n"
      "ds_read_b64_tr_b16 a[230:231], v7 offset:51720\n"
      "v_fmac_f32 v34, s46, v58\n"
      "v_mfma_f32_16x16x32_bf16 v[96:99], v[184:187], a[212:215], v[96:99]\n"
      "s_waitcnt lgkmcnt(4)\n"
      "v_fmac_f32 v35, s46, v59\n"
      "v_cvt_pk_bf16_f32 v21, v21, v25\n"
      "v_mfma_f32_16x16x32_bf16 v[76:79], v[180:183], a[192:195], v[76:79]\n"
      "s_nop 0\n"
      "ds_read_b128 a[208:211], v1 offset:128\n"
      "ds_write_addtid_b32 v21 offset:45824\n"
      "v_sub_f32 v32, v32, v44\n"
      "v_mfma_f32_16x16x32_bf16 v[80:83], v[172:175], a[192:195], v[80:83]\n"
      "v_sub_f32 v33, v33, v45\n"
      "v_cvt_pk_bf16_f32 v22, v22, v26\n"
      "v_mfma_f32_16x16x32_bf16 v[84:87], v[164:167], a[192:195], v[84:87]\n"
      "ds_write_addtid_b32 v22 offset:49920\n"
      "v_sub_f32 v34, v34, v46\n"
      "v_mfma_f32_16x16x32_bf16 v[88:91], v[176:179], a[196:199], v[88:91]\n"
      "s_nop 0\n"
      "ds_read_b128 a[212:215], v1 offset:4224\n"
      "v_sub_f32 v35, v35, v47\n"
      "v_mfma_f32_16x16x32_bf16 v[92:95], v[168:171], a[196:199], v[92:95]\n"
      "s_waitcnt lgkmcnt(4)\n"
      "v_fmac_f32 v44, s45, v32\n"
      "v_cvt_pk_bf16_f32 v23, v23, v27\n"
      "v_mfma_f32_4x4x4_16b_bf16 v[12:15], a[216:217], a[228:229], 0\n"
      "v_fmac_f32 v45, s45, v33\n"
      "v_mfma_f32_4x4x4_16b_bf16 v[16:19], a[216:217], a[230:231], 0\n"
      "ds_write_addtid_b32 v23 offset:54016\n"
      "v_fmac_f32 v46, s45, v34\n"
      "v_mfma_f32_16x16x32_bf16 v[96:99], v[160:163], a[196:199], v[96:99]\n"
      "v_fmac_f32 v47, s45, v35\n"
      "v_fma_f32 v32, s47, v32, v68\n"
      "v_cvt_pk_bf16_f32 v12, v12, v16\n"
      "v_mfma_f32_16x16x32_bf16 v[76:79], v[156:159], a[200:203], v[76:79]\n"
      "ds_read_b128 a[192:195], v2 offset:0\n"
      "ds_write_addtid_b32 v12 offset:42240\n"
      "v_mfma_f32_16x16x32_bf16 v[80:83], v[148:151], a[200:203], v[80:83]\n"
      "v_fma_f32 v33, s47, v33, v69\n"
      "v_cvt_pk_bf16_f32 v13, v13, v17\n"
      "v_mfma_f32_16x16x32_bf16 v[84:87], v[140:143], a[200:203], v[84:87]\n"
      "ds_read_b64_tr_b16 a[232:233], v7 offset:52224\n"
      "ds_write_addtid_b32 v13 offset:46336\n"
      "v_fma_f32 v34, s47, v34, v70\n"
      "v_mfma_f32_16x16x32_bf16 v[88:91], v[152:155], a[204:207], v[88:91]\n"
      "ds_read_b128 a[196:199], v2 offset:4096\n"
      "ds_read_b64_tr_b16 a[234:235], v7 offset:52232\n"
      "v_mfma_f32_16x16x32_bf16 v[92:95], v[144:147], a[204:207], v[92:95]\n"
      "s_nop 0\n"
      "v_fma_f32 v35, s47, v35, v71\n"
      "v_cvt_pk_bf16_f32 v44, v44, v45\n"
      "v_mfma_f32_16x16x32_bf16 v[96:99], v[136:139], a[204:207], v[96:99]\n"
      "s_waitcnt lgkmcnt(4)\n"
      "ds_write_addtid_b32 v44 offset:21508\n"
      "v_cvt_pk_bf16_f32 v14, v14, v18\n"
      "v_mfma_f32_16x16x32_bf16 v[76:79], v[132:135], a[208:211], v[76:79]\n"
      "ds_read_b128 a[200:203], v2 offset:64\n"
      "ds_write_addtid_b32 v14 offset:50432\n"
      "v_cvt_pk_bf16_f32 v46, v46, v47\n"
      "v_mfma_f32_16x16x32_bf16 v[80:83], v[124:127], a[208:211], v[80:83]\n"
      "ds_write_addtid_b32 v46 offset:22020\n"
      "v_cvt_pk_bf16_f32 v32, v32, v33\n"
      "v_mfma_f32_16x16x32_bf16 v[84:87], v[116:119], a[208:211], v[84:87]\n"
      "ds_write_addtid_b32 v32 offset:23556\n"
      "v_cvt_pk_bf16_f32 v34, v34, v35\n"
      "v_mfma_f32_16x16x32_bf16 v[88:91], v[128:131], a[212:215], v[88:91]\n"
      "s_waitcnt lgkmcnt(4)\n"
      "ds_read_b128 a[204:207], v2 offset:4160\n"
      "ds_write_addtid_b32 v34 offset:24068\n"
      "v_fmac_f32 v36, s46, v60\n"
      "v_mfma_f32_4x4x4_16b_bf16 v[20:23], a[216:217], a[232:233], 0\n"
      "v_fmac_f32_e64 v37, s46, v61\n"
      "v_mfma_f32_4x4x4_16b_bf16 v[24:27], a[216:217], a[234:235], 0\n"
      "v_fmac_f32 v38, s46, v62\n"
      "v_cvt_pk_bf16_f32 v15, v15, v19\n"
      "v_mfma_f32_16x16x32_bf16 v[92:95], v[120:123], a[212:215], v[92:95]\n"
      "ds_write_addtid_b32 v15 offset:54528\n"
      "v_fmac_f32 v39, s46, v63\n"
      "v_cvt_pk_bf16_f32 v20, v20, v24\n"
      "v_mfma_f32_16x16x32_bf16 v[96:99], v[112:115], a[212:215], v[96:99]\n"
      "ds_write_addtid_b32 v20 offset:42752\n"
      "v_sub_f32 v36, v36, v48\n"
      "v_mfma_f32_16x16x32_bf16 v[76:79], v[108:111], a[192:195], v[76:79]\n"
      "ds_read_b128 a[208:211], v2 offset:128\n"
      "v_sub_f32 v37, v37, v49\n"
      "v_mfma_f32_16x16x32_bf16 v[80:83], v[100:103], a[192:195], v[80:83]\n"
      "ds_read_b64_tr_b16 a[220:221], v7 offset:52736\n"
      "ds_read_b64_tr_b16 a[222:223], v7 offset:52744\n"
      "v_mfma_f32_16x16x32_bf16 v[84:87], a[160:163], a[192:195], v[84:87]\n"
      "v_sub_f32_e64 v38, v38, v50\n"
      "v_cvt_pk_bf16_f32 v21, v21, v25\n"
      "v_mfma_f32_16x16x32_bf16 v[88:91], v[104:107], a[196:199], v[88:91]\n"
      "ds_read_b128 a[212:215], v2 offset:4224\n"
      "ds_write_addtid_b32 v21 offset:46848\n"
      "v_sub_f32 v39, v39, v51\n"
      "v_mfma_f32_16x16x32_bf16 v[92:95], a[164:167], a[196:199], v[92:95]\n"
      "s_nop 0\n"
      "v_fmac_f32_e64 v48, s45, v36\n"
      "v_cvt_pk_bf16_f32 v22, v22, v26\n"
      "v_mfma_f32_16x16x32_bf16 v[96:99], a[156:159], a[196:199], v[96:99]\n"
      "s_waitcnt lgkmcnt(4)\n"
      "ds_write_addtid_b32 v22 offset:50944\n"
      "v_fmac_f32 v49, s45, v37\n"
      "v_mfma_f32_16x16x32_bf16 v[76:79], a[152:155], a[200:203], v[76:79]\n"
      "s_nop 0\n"
      "ds_read_b128 a[192:195], v3 offset:50688\n"
      "v_fmac_f32_e64 v50, s45, v38\n"
      "v_mfma_f32_16x16x32_bf16 v[80:83], a[144:147], a[200:203], v[80:83]\n"
      "s_nop 0\n"
      "v_fmac_f32 v51, s45, v39\n"
      "v_cvt_pk_bf16_f32 v23, v23, v27\n"
      "v_mfma_f32_16x16x32_bf16 v[84:87], a[136:139], a[200:203], v[84:87]\n"
      "s_waitcnt lgkmcnt(2)\n"
      "ds_write_addtid_b32 v23 offset:55040\n"
      "v_fma_f32 v36, s47, v36, v72\n"
      "v_mfma_f32_4x4x4_16b_bf16 v[12:15], a[216:217], a[220:221], 0\n"
      "v_fma_f32 v37, s47, v37, v73\n"
      "v_mfma_f32_4x4x4_16b_bf16 v[16:19], a[216:217], a[222:223], 0\n"
      "v_fma_f32 v38, s47, v38, v74\n"
      "v_fma_f32 v39, s47, v39, v75\n"
      "v_mfma_f32_16x16x32_bf16 v[88:91], a[148:151], a[204:207], v[88:91]\n"
      "ds_read_b128 a[196:199], v3 offset:50752\n"
      "v_cvt_pk_bf16_f32 v48, v48, v49\n"
      "v_cvt_pk_bf16_f32 v12, v12, v16\n"
      "v_mfma_f32_16x16x32_bf16 v[92:95], a[140:143], a[204:207], v[92:95]\n"
      "ds_write_addtid_b32 v48 offset:17408\n"
      "ds_write_addtid_b32 v12 offset:43264\n"
      "v_mfma_f32_16x16x32_bf16 v[96:99], a[132:135], a[204:207], v[96:99]\n"
      "v_cvt_pk_bf16_f32 v13, v13, v17\n"
      "v_cvt_pk_bf16_f32 v14, v14, v18\n"
      "v_mfma_f32_16x16x32_bf16 v[76:79], a[128:131], a[208:211], v[76:79]\n"
      "ds_read_b128 a[200:203], v3 offset:50816\n"
      "ds_write_addtid_b32 v13 offset:47360\n"
      "v_mfma_f32_16x16x32_bf16 v[80:83], a[120:123], a[208:211], v[80:83]\n"
      "ds_read_b64_tr_b16 a[224:225], v7 offset:53248\n"
      "ds_write_addtid_b32 v14 offset:51456\n"
      "v_cvt_pk_bf16_f32 v50, v50, v51\n"
      "v_mfma_f32_16x16x32_bf16 v[84:87], a[112:115], a[208:211], v[84:87]\n"
      "ds_write_addtid_b32 v50 offset:17920\n"
      "ds_read_b64_tr_b16 a[226:227], v7 offset:53256\n"
      "v_mfma_f32_16x16x32_bf16 v[88:91], a[124:127], a[212:215], v[88:91]\n"
      "ds_read_b128 a[204:207], v4 offset:50688\n"
      "v_cvt_pk_bf16_f32 v36, v36, v37\n"
      "v_mfma_f32_16x16x32_bf16 v[92:95], a[116:119], a[212:215], v[92:95]\n"
      "ds_write_addtid_b32 v36 offset:19456\n"
      "v_cvt_pk_bf16_f32 v15, v15, v19\n"
      "v_mfma_f32_16x16x32_bf16 v[96:99], a[108:111], a[212:215], v[96:99]\n"
      "s_waitcnt lgkmcnt(6)\n"
      "ds_write_addtid_b32 v15 offset:55552\n"
      "v_sub_f32 v76, v88, v76\n"
      "v_sub_f32_e64 v77, v89, v77\n"
      "v_cvt_pk_bf16_f32 v38, v38, v39\n"
      "v_mfma_f32_16x16x32_bf16 v[52:55], a[0:3], a[192:195], 0\n"
      "s_nop 0\n"
      "ds_read_b128 a[208:211], v4 offset:50752\n"
      "ds_write_addtid_b32 v38 offset:19968\n"
      "v_mfma_f32_16x16x32_bf16 v[56:59], a[4:7], a[192:195], 0\n"
      "s_waitcnt lgkmcnt(5)\n"
      "ds_write_addtid_b32 v76 offset:0\n"
      "ds_write_addtid_b32 v77 offset:512\n"
      "v_mfma_f32_4x4x4_16b_bf16 v[20:23], a[216:217], a[224:225], 0\n"
      "v_sub_f32 v78, v90, v78\n"
      "v_mfma_f32_4x4x4_16b_bf16 v[24:27], a[216:217], a[226:227], 0\n"
      "ds_write_addtid_b32 v78 offset:1024\n"
      "v_sub_f32 v79, v91, v79\n"
      "v_mfma_f32_16x16x32_bf16 v[60:63], a[8:11], a[192:195], 0\n"
      "ds_write_addtid_b32 v79 offset:1536\n"
      "v_sub_f32 v80, v92, v80\n"
      "v_sub_f32 v81, v93, v81\n"
      "v_mfma_f32_16x16x32_bf16 v[52:55], a[12:15], a[196:199], v[52:55]\n"
      "ds_read_b128 a[212:215], v4 offset:50816\n"
      "ds_write_addtid_b32 v80 offset:2048\n"
      "v_mfma_f32_16x16x32_bf16 v[56:59], a[16:19], a[196:199], v[56:59]\n"
      "ds_write_addtid_b32 v81 offset:2560\n"
      "v_sub_f32 v82, v94, v82\n"
      "v_mfma_f32_16x16x32_bf16 v[60:63], a[20:23], a[196:199], v[60:63]\n"
      "s_nop 0\n"
      "ds_write_addtid_b32 v82 offset:3072\n"
      "v_cvt_pk_bf16_f32 v20, v20, v24\n"
      "v_mfma_f32_16x16x32_bf16 v[52:55], a[24:27], a[200:203], v[52:55]\n"
      "ds_read_b128 a[192:195], v5 offset:50688\n"
      "ds_write_addtid_b32 v20 offset:43776\n"
      "v_sub_f32 v83, v95, v83\n"
      "v_mfma_f32_16x16x32_bf16 v[56:59], a[28:31], a[200:203], v[56:59]\n"
      "ds_write_addtid_b32 v83 offset:3584\n"
      "v_sub_f32_e64 v84, v96, v84\n"
      "v_mfma_f32_16x16x32_bf16 v[60:63], a[32:35], a[200:203], v[60:63]\n"
      "s_waitcnt lgkmcnt(5)\n"
      "ds_write_addtid_b32 v84 offset:4096\n"
      "v_cvt_pk_bf16_f32 v21, v21, v25\n"
      "v_mfma_f32_16x16x32_bf16 v[52:55], a[36:39], a[204:207], v[52:55]\n"
      "ds_read_b128 a[196:199], v5 offset:50752\n"
      "ds_write_addtid_b32 v21 offset:47872\n"
      "v_mfma_f32_16x16x32_bf16 v[56:59], a[40:43], a[204:207], v[56:59]\n"
      "v_sub_f32 v85, v97, v85\n"
      "v_sub_f32 v86, v98, v86\n"
      "v_cvt_pk_bf16_f32 v22, v22, v26\n"
      "v_mfma_f32_16x16x32_bf16 v[60:63], a[44:47], a[204:207], v[60:63]\n"
      "ds_write_addtid_b32 v22 offset:51968\n"
      "ds_write_addtid_b32 v85 offset:4608\n"
      "v_sub_f32 v87, v99, v87\n"
      "v_mfma_f32_16x16x32_bf16 v[52:55], a[48:51], a[208:211], v[52:55]\n"
      "ds_read_b128 a[200:203], v5 offset:50816\n"
      "ds_write_addtid_b32 v86 offset:5120\n"
      "v_mfma_f32_16x16x32_bf16 v[56:59], a[52:55], a[208:211], v[56:59]\n"
      "s_nop 0\n"
      "ds_write_addtid_b32 v87 offset:5632\n"
      "v_cvt_pk_bf16_f32 v23, v23, v27\n"
      "v_mfma_f32_16x16x32_bf16 v[60:63], a[56:59], a[208:211], v[60:63]\n"
      "ds_write_addtid_b32 v23 offset:56064\n"
      "v_fma_f32 v40, 0.5, v76, -v88\n"
      "v_mfma_f32_16x16x32_bf16 v[52:55], a[60:63], a[212:215], v[52:55]\n"
      "v_fma_f32 v41, 0.5, v77, -v89\n"
      "v_fma_f32 v42, 0.5, v78, -v90\n"
      "v_mfma_f32_16x16x32_bf16 v[56:59], a[64:67], a[212:215], v[56:59]\n"
      "v_fma_f32 v43, 0.5, v79, -v91\n"
      "v_fma_f32 v44, 0.5, v80, -v92\n"
      "v_mfma_f32_16x16x32_bf16 v[60:63], a[68:71], a[212:215], v[60:63]\n"
      "s_waitcnt lgkmcnt(0)\n"
      "v_fma_f32 v45, 0.5, v81, -v93\n"
      "s_barrier\n"
      "v_mfma_f32_16x16x32_bf16 v[52:55], a[72:75], a[192:195], v[52:55]\n"
      "ds_read_b128 a[204:207], v0 offset:16384\n"
      "ds_read2st64_b32 v[28:29], v6 offset0:0 offset1:2\n"
      "v_fma_f32 v46, 0.5, v82, -v94\n"
      "v_mfma_f32_16x16x32_bf16 v[56:59], a[76:79], a[192:195], v[56:59]\n"
      "ds_read_b128 a[208:211], v0 offset:20480\n"
      "ds_read2st64_b32 v[30:31], v6 offset0:4 offset1:6\n"
      "v_mfma_f32_16x16x32_bf16 v[60:63], a[80:83], a[192:195], v[60:63]\n"
      "ds_read_b64_tr_b16 a[220:221], v7 offset:0\n"
      "v_fma_f32 v47, 0.5, v83, -v95\n"
      "v_mfma_f32_16x16x32_bf16 v[52:55], a[84:87], a[196:199], v[52:55]\n"
      "ds_read_b128 a[212:215], v0 offset:16448\n"
      "v_fma_f32 v48, 0.5, v84, -v96\n"
      "v_mfma_f32_16x16x32_bf16 v[56:59], a[88:91], a[196:199], v[56:59]\n"
      "ds_read_b64_tr_b16 a[222:223], v7 offset:8\n"
      "ds_read2st64_b32 v[32:33], v6 offset0:8 offset1:10\n"
      "v_fma_f32 v49, 0.5, v85, -v97\n"
      "v_mfma_f32_16x16x32_bf16 v[60:63], a[92:95], a[196:199], v[60:63]\n"
      "ds_read2st64_b32 v[34:35], v6 offset0:12 offset1:14\n"
      "v_fma_f32 v50, 0.5, v86, -v98\n"
      "v_mfma_f32_16x16x32_bf16 v[52:55], a[96:99], a[200:203], v[52:55]\n"
      "ds_read_b128 a[192:195], v0 offset:20544\n"
      "ds_read2st64_b32 v[36:37], v6 offset0:16 offset1:18\n"
      "v_mfma_f32_16x16x32_bf16 v[56:59], a[100:103], a[200:203], v[56:59]\n"
      "ds_read2st64_b32 v[38:39], v6 offset0:20 offset1:22\n"
      "v_fma_f32 v51, 0.5, v87, -v99\n"
      "v_mfma_f32_16x16x32_bf16 v[60:63], a[104:107], a[200:203], v[60:63]\n"
      "s_waitcnt lgkmcnt(8)\n"
      "v_fmac_f32 v28, s46, v64\n"
      "v_fmac_f32 v29, s46, v65\n"
      "v_fmac_f32 v30, s46, v66\n"
      "v_mfma_f32_16x16x32_bf16 v[76:79], v[252:255], a[204:207], v[52:55]\n"
      "s_waitcnt lgkmcnt(5)\n"
      "ds_read_b128 a[196:199], v0 offset:16512\n"
      "v_fmac_f32 v31, s46, v67\n"
      "v_mfma_f32_4x4x4_16b_bf16 v[12:15], a[216:217], a[220:221], 0\n"
      "v_sub_f32_e64 v28, v28, v40\n"
      "v_mfma_f32_4x4x4_16b_bf16 v[16:19], a[216:217], a[222:223], 0\n"
      "v_sub_f32 v29, v29, v41\n"
      "v_sub_f32 v30, v30, v42\n"
      "v_mfma_f32_16x16x32_bf16 v[80:83], v[244:247], a[204:207], v[56:59]\n"
      "v_sub_f32 v31, v31, v43\n"
      "v_fmac_f32 v40, s45, v28\n"
      "v_mfma_f32_16x16x32_bf16 v[84:87], v[236:239], a[204:207], v[60:63]\n"
      "v_fmac_f32 v41, s45, v29\n"
      "v_cvt_pk_bf16_f32 v12, v12, v16\n"
      "v_fmac_f32 v42, s45, v30\n"
      "v_mfma_f32_16x16x32_bf16 v[88:91], v[248:251], a[208:211], v[52:55]\n"
      "ds_read_b128 a[200:203], v0 offset:20608\n"
      "ds_write_addtid_b32 v12 offset:24832\n"
      "v_mfma_f32_16x16x32_bf16 v[92:95], v[240:243], a[208:211], v[56:59]\n"
      "ds_read_b64_tr_b16 a[224:225], v7 offset:512\n"
      "ds_read_b64_tr_b16 a[226:227], v7 offset:520\n"
      "v_mfma_f32_16x16x32_bf16 v[96:99], v[232:235], a[208:211], v[60:63]\n"
      "v_fmac_f32_e64 v43, s45, v31\n"
      "v_cvt_pk_bf16_f32 v13, v13, v17\n"
      "v_mfma_f32_16x16x32_bf16 v[76:79], v[228:231], a[212:215], v[76:79]\n"
      "s_nop 0\n"
      "ds_read_b128 a[204:207], v1 offset:16384\n"
      "ds_write_addtid_b32 v13 offset:28928\n"
      "v_fma_f32 v28, s47, v28, v52\n"
      "v_mfma_f32_16x16x32_bf16 v[80:83], v[220:223], a[212:215], v[80:83]\n"
      "s_nop 0\n"
      "v_fma_f32 v29, s47, v29, v53\n"
      "v_cvt_pk_bf16_f32 v14, v14, v18\n"
      "v_mfma_f32_16x16x32_bf16 v[84:87], v[212:215], a[212:215], v[84:87]\n"
      "s_waitcnt lgkmcnt(6)\n"
      "ds_write_addtid_b32 v14 offset:33024\n"
      "v_fma_f32 v30, s47, v30, v54\n"
      "v_mfma_f32_16x16x32_bf16 v[88:91], v[224:227], a[192:195], v[88:91]\n"
      "s_nop 0\n"
      "ds_read_b128 a[208:211], v1 offset:20480\n"
      "v_fma_f32 v31, s47, v31, v55\n"
      "v_mfma_f32_16x16x32_bf16 v[92:95], v[216:219], a[192:195], v[92:95]\n"
      "s_nop 0\n"
      "v_cvt_pk_bf16_f32 v40, v40, v41\n"
      "v_cvt_pk_bf16_f32 v15, v15, v19\n"
      "v_mfma_f32_16x16x32_bf16 v[96:99], v[208:211], a[192:195], v[96:99]\n"
      "s_waitcnt lgkmcnt(4)\n"
      "ds_write_addtid_b32 v40 offset:12288\n"
      "ds_write_addtid_b32 v15 offset:37120\n"
      "v_cvt_pk_bf16_f32 v42, v42, v43\n"
      "v_mfma_f32_4x4x4_16b_bf16 v[20:23], a[216:217], a[224:225], 0\n"
      "v_cvt_pk_bf16_f32 v28, v28, v29\n"
      "v_mfma_f32_4x4x4_16b_bf16 v[24:27], a[216:217], a[226:227], 0\n"
      "ds_write_addtid_b32 v42 offset:12800\n"
      "ds_write_addtid_b32 v28 offset:14336\n"
      "v_mfma_f32_16x16x32_bf16 v[76:79], v[204:207], a[196:199], v[76:79]\n"
      "ds_read_b128 a[212:215], v1 offset:16448\n"
      "v_cvt_pk_bf16_f32 v30, v30, v31\n"
      "v_mfma_f32_16x16x32_bf16 v[80:83], v[196:199], a[196:199], v[80:83]\n"
      "ds_write_addtid_b32 v30 offset:14848\n"
      "v_fmac_f32 v32, s46, v68\n"
      "v_cvt_pk_bf16_f32 v20, v20, v24\n"
      "v_mfma_f32_16x16x32_bf16 v[84:87], v[188:191], a[196:199], v[84:87]\n"
      "ds_write_addtid_b32 v20 offset:25344\n"
      "v_fmac_f32 v33, s46, v69\n"
      "v_mfma_f32_16x16x32_bf16 v[88:91], v[200:203], a[200:203], v[88:91]\n"
      "ds_read_b128 a[192:195], v1 offset:20544\n"
      "ds_read_b64_tr_b16 a[228:229], v7 offset:1024\n"
      "v_mfma_f32_16x16x32_bf16 v[92:95], v[192:195], a[200:203], v[92:95]\n"
      "ds_read_b64_tr_b16 a[230:231], v7 offset:1032\n"
      "v_fmac_f32 v34, s46, v70\n"
      "v_mfma_f32_16x16x32_bf16 v[96:99], v[184:187], a[200:203], v[96:99]\n"
      "s_waitcnt lgkmcnt(4)\n"
      "v_fmac_f32 v35, s46, v71\n"
      "v_cvt_pk_bf16_f32 v21, v21, v25\n"
      "v_mfma_f32_16x16x32_bf16 v[76:79], v[180:183], a[204:207], v[76:79]\n"
      "s_nop 0\n"
      "ds_read_b128 a[196:199], v1 offset:16512\n"
      "ds_write_addtid_b32 v21 offset:29440\n"
      "v_sub_f32 v32, v32, v44\n"
      "v_mfma_f32_16x16x32_bf16 v[80:83], v[172:175], a[204:207], v[80:83]\n"
      "v_sub_f32 v33, v33, v45\n"
      "v_cvt_pk_bf16_f32 v22, v22, v26\n"
      "v_mfma_f32_16x16x32_bf16 v[84:87], v[164:167], a[204:207], v[84:87]\n"
      "ds_write_addtid_b32 v22 offset:33536\n"
      "v_sub_f32 v34, v34, v46\n"
      "v_mfma_f32_16x16x32_bf16 v[88:91], v[176:179], a[208:211], v[88:91]\n"
      "s_nop 0\n"
      "ds_read_b128 a[200:203], v1 offset:20608\n"
      "v_sub_f32 v35, v35, v47\n"
      "v_mfma_f32_16x16x32_bf16 v[92:95], v[168:171], a[208:211], v[92:95]\n"
      "s_waitcnt lgkmcnt(4)\n"
      "v_fmac_f32 v44, s45, v32\n"
      "v_cvt_pk_bf16_f32 v23, v23, v27\n"
      "v_mfma_f32_4x4x4_16b_bf16 v[12:15], a[216:217], a[228:229], 0\n"
      "v_fmac_f32 v45, s45, v33\n"
      "v_mfma_f32_4x4x4_16b_bf16 v[16:19], a[216:217], a[230:231], 0\n"
      "ds_write_addtid_b32 v23 offset:37632\n"
      "v_fmac_f32 v46, s45, v34\n"
      "v_mfma_f32_16x16x32_bf16 v[96:99], v[160:163], a[208:211], v[96:99]\n"
      "v_fmac_f32 v47, s45, v35\n"
      "v_fma_f32 v32, s47, v32, v56\n"
      "v_cvt_pk_bf16_f32 v12, v12, v16\n"
      "v_mfma_f32_16x16x32_bf16 v[76:79], v[156:159], a[212:215], v[76:79]\n"
      "ds_read_b128 a[204:207], v2 offset:16384\n"
      "ds_write_addtid_b32 v12 offset:25856\n"
      "v_mfma_f32_16x16x32_bf16 v[80:83], v[148:151], a[212:215], v[80:83]\n"
      "v_fma_f32 v33, s47, v33, v57\n"
      "v_cvt_pk_bf16_f32 v13, v13, v17\n"
      "v_mfma_f32_16x16x32_bf16 v[84:87], v[140:143], a[212:215], v[84:87]\n"
      "ds_read_b64_tr_b16 a[232:233], v7 offset:1536\n"
      "ds_write_addtid_b32 v13 offset:29952\n"
      "v_fma_f32 v34, s47, v34, v58\n"
      "v_mfma_f32_16x16x32_bf16 v[88:91], v[152:155], a[192:195], v[88:91]\n"
      "ds_read_b128 a[208:211], v2 offset:20480\n"
      "ds_read_b64_tr_b16 a[234:235], v7 offset:1544\n"
      "v_mfma_f32_16x16x32_bf16 v[92:95], v[144:147], a[192:195], v[92:95]\n"
      "s_nop 0\n"
      "v_fma_f32 v35, s47, v35, v59\n"
      "v_cvt_pk_bf16_f32 v44, v44, v45\n"
      "v_mfma_f32_16x16x32_bf16 v[96:99], v[136:139], a[192:195], v[96:99]\n"
      "s_waitcnt lgkmcnt(4)\n"
      "ds_write_addtid_b32 v44 offset:20484\n"
      "v_cvt_pk_bf16_f32 v14, v14, v18\n"
      "v_mfma_f32_16x16x32_bf16 v[76:79], v[132:135], a[196:199], v[76:79]\n"
      "ds_read_b128 a[212:215], v2 offset:16448\n"
      "ds_write_addtid_b32 v14 offset:34048\n"
      "v_cvt_pk_bf16_f32 v46, v46, v47\n"
      "v_mfma_f32_16x16x32_bf16 v[80:83], v[124:127], a[196:199], v[80:83]\n"
      "ds_write_addtid_b32 v46 offset:20996\n"
      "v_cvt_pk_bf16_f32 v32, v32, v33\n"
      "v_mfma_f32_16x16x32_bf16 v[84:87], v[116:119], a[196:199], v[84:87]\n"
      "ds_write_addtid_b32 v32 offset:22532\n"
      "v_cvt_pk_bf16_f32 v34, v34, v35\n"
      "v_mfma_f32_16x16x32_bf16 v[88:91], v[128:131], a[200:203], v[88:91]\n"
      "s_waitcnt lgkmcnt(4)\n"
      "ds_read_b128 a[192:195], v2 offset:20544\n"
      "ds_write_addtid_b32 v34 offset:23044\n"
      "v_fmac_f32 v36, s46, v72\n"
      "v_mfma_f32_4x4x4_16b_bf16 v[20:23], a[216:217], a[232:233], 0\n"
      "v_fmac_f32_e64 v37, s46, v73\n"
      "v_mfma_f32_4x4x4_16b_bf16 v[24:27], a[216:217], a[234:235], 0\n"
      "v_fmac_f32 v38, s46, v74\n"
      "v_cvt_pk_bf16_f32 v15, v15, v19\n"
      "v_mfma_f32_16x16x32_bf16 v[92:95], v[120:123], a[200:203], v[92:95]\n"
      "ds_write_addtid_b32 v15 offset:38144\n"
      "v_fmac_f32 v39, s46, v75\n"
      "v_cvt_pk_bf16_f32 v20, v20, v24\n"
      "v_mfma_f32_16x16x32_bf16 v[96:99], v[112:115], a[200:203], v[96:99]\n"
      "ds_write_addtid_b32 v20 offset:26368\n"
      "v_sub_f32 v36, v36, v48\n"
      "v_mfma_f32_16x16x32_bf16 v[76:79], v[108:111], a[204:207], v[76:79]\n"
      "ds_read_b128 a[196:199], v2 offset:16512\n"
      "v_sub_f32 v37, v37, v49\n"
      "v_mfma_f32_16x16x32_bf16 v[80:83], v[100:103], a[204:207], v[80:83]\n"
      "ds_read_b64_tr_b16 a[220:221], v7 offset:2048\n"
      "ds_read_b64_tr_b16 a[222:223], v7 offset:2056\n"
      "v_mfma_f32_16x16x32_bf16 v[84:87], a[160:163], a[204:207], v[84:87]\n"
      "v_sub_f32_e64 v38, v38, v50\n"
      "v_cvt_pk_bf16_f32 v21, v21, v25\n"
      "v_mfma_f32_16x16x32_bf16 v[88:91], v[104:107], a[208:211], v[88:91]\n"
      "ds_read_b128 a[200:203], v2 offset:20608\n"
      "ds_write_addtid_b32 v21 offset:30464\n"
      "v_sub_f32 v39, v39, v51\n"
      "v_mfma_f32_16x16x32_bf16 v[92:95], a[164:167], a[208:211], v[92:95]\n"
      "s_nop 0\n"
      "v_fmac_f32_e64 v48, s45, v36\n"
      "v_cvt_pk_bf16_f32 v22, v22, v26\n"
      "v_mfma_f32_16x16x32_bf16 v[96:99], a[156:159], a[208:211], v[96:99]\n"
      "s_waitcnt lgkmcnt(4)\n"
      "ds_write_addtid_b32 v22 offset:34560\n"
      "v_fmac_f32 v49, s45, v37\n"
      "v_mfma_f32_16x16x32_bf16 v[76:79], a[152:155], a[212:215], v[76:79]\n"
      "s_nop 0\n"
      "ds_read_b128 a[204:207], v3 offset:0\n"
      "v_fmac_f32_e64 v50, s45, v38\n"
      "v_mfma_f32_16x16x32_bf16 v[80:83], a[144:147], a[212:215], v[80:83]\n"
      "s_nop 0\n"
      "v_fmac_f32 v51, s45, v39\n"
      "v_cvt_pk_bf16_f32 v23, v23, v27\n"
      "v_mfma_f32_16x16x32_bf16 v[84:87], a[136:139], a[212:215], v[84:87]\n"
      "s_waitcnt lgkmcnt(2)\n"
      "ds_write_addtid_b32 v23 offset:38656\n"
      "v_fma_f32 v36, s47, v36, v60\n"
      "v_mfma_f32_4x4x4_16b_bf16 v[12:15], a[216:217], a[220:221], 0\n"
      "v_fma_f32 v37, s47, v37, v61\n"
      "v_mfma_f32_4x4x4_16b_bf16 v[16:19], a[216:217], a[222:223], 0\n"
      "v_fma_f32 v38, s47, v38, v62\n"
      "v_fma_f32 v39, s47, v39, v63\n"
      "v_mfma_f32_16x16x32_bf16 v[88:91], a[148:151], a[192:195], v[88:91]\n"
      "ds_read_b128 a[208:211], v3 offset:64\n"
      "v_cvt_pk_bf16_f32 v48, v48, v49\n"
      "v_cvt_pk_bf16_f32 v12, v12, v16\n"
      "v_mfma_f32_16x16x32_bf16 v[92:95], a[140:143], a[192:195], v[92:95]\n"
      "ds_write_addtid_b32 v48 offset:16384\n"
      "ds_write_addtid_b32 v12 offset:26880\n"
      "v_mfma_f32_16x16x32_bf16 v[96:99], a[132:135], a[192:195], v[96:99]\n"
      "v_cvt_pk_bf16_f32 v13, v13, v17\n"
      "v_cvt_pk_bf16_f32 v14, v14, v18\n"
      "v_mfma_f32_16x16x32_bf16 v[76:79], a[128:131], a[196:199], v[76:79]\n"
      "ds_read_b128 a[212:215], v3 offset:128\n"
      "ds_write_addtid_b32 v13 offset:30976\n"
      "v_mfma_f32_16x16x32_bf16 v[80:83], a[120:123], a[196:199], v[80:83]\n"
      "ds_read_b64_tr_b16 a[224:225], v7 offset:2560\n"
      "ds_write_addtid_b32 v14 offset:35072\n"
      "v_cvt_pk_bf16_f32 v50, v50, v51\n"
      "v_mfma_f32_16x16x32_bf16 v[84:87], a[112:115], a[196:199], v[84:87]\n"
      "ds_write_addtid_b32 v50 offset:16896\n"
      "ds_read_b64_tr_b16 a[226:227], v7 offset:2568\n"
      "v_mfma_f32_16x16x32_bf16 v[88:91], a[124:127], a[200:203], v[88:91]\n"
      "ds_read_b128 a[192:195], v4 offset:0\n"
      "v_cvt_pk_bf16_f32 v36, v36, v37\n"
      "v_mfma_f32_16x16x32_bf16 v[92:95], a[116:119], a[200:203], v[92:95]\n"
      "ds_write_addtid_b32 v36 offset:18432\n"
      "v_cvt_pk_bf16_f32 v15, v15, v19\n"
      "v_mfma_f32_16x16x32_bf16 v[96:99], a[108:111], a[200:203], v[96:99]\n"
      "s_waitcnt lgkmcnt(6)\n"
      "ds_write_addtid_b32 v15 offset:39168\n"
      "v_sub_f32 v76, v88, v76\n"
      "v_sub_f32_e64 v77, v89, v77\n"
      "v_cvt_pk_bf16_f32 v38, v38, v39\n"
      "v_mfma_f32_16x16x32_bf16 v[64:67], a[0:3], a[204:207], 0\n"
      "s_nop 0\n"
      "ds_read_b128 a[196:199], v4 offset:64\n"
      "ds_write_addtid_b32 v38 offset:18944\n"
      "v_mfma_f32_16x16x32_bf16 v[68:71], a[4:7], a[204:207], 0\n"
      "s_waitcnt lgkmcnt(5)\n"
      "ds_write_addtid_b32 v76 offset:6144\n"
      "ds_write_addtid_b32 v77 offset:6656\n"
      "v_mfma_f32_4x4x4_16b_bf16 v[20:23], a[216:217], a[224:225], 0\n"
      "v_sub_f32 v78, v90, v78\n"
      "v_mfma_f32_4x4x4_16b_bf16 v[24:27], a[216:217], a[226:227], 0\n"
      "ds_write_addtid_b32 v78 offset:7168\n"
      "v_sub_f32 v79, v91, v79\n"
      "v_mfma_f32_16x16x32_bf16 v[72:75], a[8:11], a[204:207], 0\n"
      "ds_write_addtid_b32 v79 offset:7680\n"
      "v_sub_f32 v80, v92, v80\n"
      "v_sub_f32 v81, v93, v81\n"
      "v_mfma_f32_16x16x32_bf16 v[64:67], a[12:15], a[208:211], v[64:67]\n"
      "ds_read_b128 a[200:203], v4 offset:128\n"
      "ds_write_addtid_b32 v80 offset:8192\n"
      "v_mfma_f32_16x16x32_bf16 v[68:71], a[16:19], a[208:211], v[68:71]\n"
      "ds_write_addtid_b32 v81 offset:8704\n"
      "v_sub_f32 v82, v94, v82\n"
      "v_mfma_f32_16x16x32_bf16 v[72:75], a[20:23], a[208:211], v[72:75]\n"
      "s_nop 0\n"
      "ds_write_addtid_b32 v82 offset:9216\n"
      "v_cvt_pk_bf16_f32 v20, v20, v24\n"
      "v_mfma_f32_16x16x32_bf16 v[64:67], a[24:27], a[212:215], v[64:67]\n"
      "ds_read_b128 a[204:207], v5 offset:0\n"
      "ds_write_addtid_b32 v20 offset:27392\n"
      "v_sub_f32 v83, v95, v83\n"
      "v_mfma_f32_16x16x32_bf16 v[68:71], a[28:31], a[212:215], v[68:71]\n"
      "ds_write_addtid_b32 v83 offset:9728\n"
      "v_sub_f32_e64 v84, v96, v84\n"
      "v_mfma_f32_16x16x32_bf16 v[72:75], a[32:35], a[212:215], v[72:75]\n"
      "s_waitcnt lgkmcnt(5)\n"
      "ds_write_addtid_b32 v84 offset:10240\n"
      "v_cvt_pk_bf16_f32 v21, v21, v25\n"
      "v_mfma_f32_16x16x32_bf16 v[64:67], a[36:39], a[192:195], v[64:67]\n"
      "ds_read_b128 a[208:211], v5 offset:64\n"
      "ds_write_addtid_b32 v21 offset:31488\n"
      "v_mfma_f32_16x16x32_bf16 v[68:71], a[40:43], a[192:195], v[68:71]\n"
      "v_sub_f32 v85, v97, v85\n"
      "v_sub_f32 v86, v98, v86\n"
      "v_cvt_pk_bf16_f32 v22, v22, v26\n"
      "v_mfma_f32_16x16x32_bf16 v[72:75], a[44:47], a[192:195], v[72:75]\n"
      "ds_write_addtid_b32 v22 offset:35584\n"
      "ds_write_addtid_b32 v85 offset:10752\n"
      "v_sub_f32 v87, v99, v87\n"
      "v_mfma_f32_16x16x32_bf16 v[64:67], a[48:51], a[196:199], v[64:67]\n"
      "ds_read_b128 a[212:215], v5 offset:128\n"
      "ds_write_addtid_b32 v86 offset:11264\n"
      "v_mfma_f32_16x16x32_bf16 v[68:71], a[52:55], a[196:199], v[68:71]\n"
      "s_nop 0\n"
      "ds_write_addtid_b32 v87 offset:11776\n"
      "v_cvt_pk_bf16_f32 v23, v23, v27\n"
      "v_mfma_f32_16x16x32_bf16 v[72:75], a[56:59], a[196:199], v[72:75]\n"
      "ds_write_addtid_b32 v23 offset:39680\n"
      "v_fma_f32 v40, 0.5, v76, -v88\n"
      "v_mfma_f32_16x16x32_bf16 v[64:67], a[60:63], a[200:203], v[64:67]\n"
      "v_fma_f32 v41, 0.5, v77, -v89\n"
      "v_fma_f32 v42, 0.5, v78, -v90\n"
      "v_mfma_f32_16x16x32_bf16 v[68:71], a[64:67], a[200:203], v[68:71]\n"
      "v_fma_f32 v43, 0.5, v79, -v91\n"
      "v_fma_f32 v44, 0.5, v80, -v92\n"
      "v_mfma_f32_16x16x32_bf16 v[72:75], a[68:71], a[200:203], v[72:75]\n"
      "s_waitcnt lgkmcnt(0)\n"
      "v_fma_f32 v45, 0.5, v81, -v93\n"
      "s_barrier\n"
      "v_mfma_f32_16x16x32_bf16 v[64:67], a[72:75], a[204:207], v[64:67]\n"
      "ds_read_b128 a[192:195], v0 offset:0\n"
      "ds_read2st64_b32 v[28:29], v6 offset0:24 offset1:26\n"
      "v_fma_f32 v46, 0.5, v82, -v94\n"
      "v_mfma_f32_16x16x32_bf16 v[68:71], a[76:79], a[204:207], v[68:71]\n"
      "ds_read_b128 a[196:199], v0 offset:4096\n"
      "ds_read2st64_b32 v[30:31], v6 offset0:28 offset1:30\n"
      "v_mfma_f32_16x16x32_bf16 v[72:75], a[80:83], a[204:207], v[72:75]\n"
      "ds_read_b64_tr_b16 a[220:221], v7 offset:16896\n"
      "v_fma_f32 v47, 0.5, v83, -v95\n"
      "v_mfma_f32_16x16x32_bf16 v[64:67], a[84:87], a[208:211], v[64:67]\n"
      "ds_read_b128 a[200:203], v0 offset:64\n"
      "v_fma_f32 v48, 0.5, v84, -v96\n"
      "v_mfma_f32_16x16x32_bf16 v[68:71], a[88:91], a[208:211], v[68:71]\n"
      "ds_read_b64_tr_b16 a[222:223], v7 offset:16904\n"
      "ds_read2st64_b32 v[32:33], v6 offset0:32 offset1:34\n"
      "v_fma_f32 v49, 0.5, v85, -v97\n"
      "v_mfma_f32_16x16x32_bf16 v[72:75], a[92:95], a[208:211], v[72:75]\n"
      "ds_read2st64_b32 v[34:35], v6 offset0:36 offset1:38\n"
      "v_fma_f32 v50, 0.5, v86, -v98\n"
      "v_mfma_f32_16x16x32_bf16 v[64:67], a[96:99], a[212:215], v[64:67]\n"
      "ds_read_b128 a[204:207], v0 offset:4160\n"
      "ds_read2st64_b32 v[36:37], v6 offset0:40 offset1:42\n"
      "v_mfma_f32_16x16x32_bf16 v[68:71], a[100:103], a[212:215], v[68:71]\n"
      "ds_read2st64_b32 v[38:39], v6 offset0:44 offset1:46\n"
      "v_fma_f32 v51, 0.5, v87, -v99\n"
      "v_mfma_f32_16x16x32_bf16 v[72:75], a[104:107], a[212:215], v[72:75]\n"
      "s_waitcnt lgkmcnt(8)\n"
      "v_fmac_f32 v28, s46, v52\n"
      "v_fmac_f32 v29, s46, v53\n"
      "v_fmac_f32 v30, s46, v54\n"
      "v_mfma_f32_16x16x32_bf16 v[76:79], v[252:255], a[192:195], v[64:67]\n"
      "s_waitcnt lgkmcnt(5)\n"
      "ds_read_b128 a[208:211], v0 offset:128\n"
      "v_fmac_f32 v31, s46, v55\n"
      "v_mfma_f32_4x4x4_16b_bf16 v[12:15], a[216:217], a[220:221], 0\n"
      "v_sub_f32_e64 v28, v28, v40\n"
      "v_mfma_f32_4x4x4_16b_bf16 v[16:19], a[216:217], a[222:223], 0\n"
      "v_sub_f32 v29, v29, v41\n"
      "v_sub_f32 v30, v30, v42\n"
      "v_mfma_f32_16x16x32_bf16 v[80:83], v[244:247], a[192:195], v[68:71]\n"
      "v_sub_f32 v31, v31, v43\n"
      "v_fmac_f32 v40, s45, v28\n"
      "v_mfma_f32_16x16x32_bf16 v[84:87], v[236:239], a[192:195], v[72:75]\n"
      "v_fmac_f32 v41, s45, v29\n"
      "v_cvt_pk_bf16_f32 v12, v12, v16\n"
      "v_fmac_f32 v42, s45, v30\n"
      "v_mfma_f32_16x16x32_bf16 v[88:91], v[248:251], a[196:199], v[64:67]\n"
      "ds_read_b128 a[212:215], v0 offset:4224\n"
      "ds_write_addtid_b32 v12 offset:41216\n"
      "v_mfma_f32_16x16x32_bf16 v[92:95], v[240:243], a[196:199], v[68:71]\n"
      "ds_read_b64_tr_b16 a[224:225], v7 offset:17408\n"
      "ds_read_b64_tr_b16 a[226:227], v7 offset:17416\n"
      "v_mfma_f32_16x16x32_bf16 v[96:99], v[232:235], a[196:199], v[72:75]\n"
      "v_fmac_f32_e64 v43, s45, v31\n"
      "v_cvt_pk_bf16_f32 v13, v13, v17\n"
      "v_mfma_f32_16x16x32_bf16 v[76:79], v[228:231], a[200:203], v[76:79]\n"
      "s_nop 0\n"
      "ds_read_b128 a[192:195], v1 offset:0\n"
      "ds_write_addtid_b32 v13 offset:45312\n"
      "v_fma_f32 v28, s47, v28, v64\n"
      "v_mfma_f32_16x16x32_bf16 v[80:83], v[220:223], a[200:203], v[80:83]\n"
      "s_nop 0\n"
      "v_fma_f32 v29, s47, v29, v65\n"
      "v_cvt_pk_bf16_f32 v14, v14, v18\n"
      "v_mfma_f32_16x16x32_bf16 v[84:87], v[212:215], a[200:203], v[84:87]\n"
      "s_waitcnt lgkmcnt(6)\n"
      "ds_write_addtid_b32 v14 offset:49408\n"
      "v_fma_f32 v30, s47, v30, v66\n"
      "v_mfma_f32_16x16x32_bf16 v[88:91], v[224:227], a[204:207], v[88:91]\n"
      "s_nop 0\n"
      "ds_read_b128 a[196:199], v1 offset:4096\n"
      "v_fma_f32 v31, s47, v31, v67\n"
      "v_mfma_f32_16x16x32_bf16 v[92:95], v[216:219], a[204:207], v[92:95]\n"
      "s_nop 0\n"
      "v_cvt_pk_bf16_f32 v40, v40, v41\n"
      "v_cvt_pk_bf16_f32 v15, v15, v19\n"
      "v_mfma_f32_16x16x32_bf16 v[96:99], v[208:211], a[204:207], v[96:99]\n"
      "s_waitcnt lgkmcnt(4)\n"
      "ds_write_addtid_b32 v40 offset:13312\n"
      "ds_write_addtid_b32 v15 offset:53504\n"
      "v_cvt_pk_bf16_f32 v42, v42, v43\n"
      "v_mfma_f32_4x4x4_16b_bf16 v[20:23], a[216:217], a[224:225], 0\n"
      "v_cvt_pk_bf16_f32 v28, v28, v29\n"
      "v_mfma_f32_4x4x4_16b_bf16 v[24:27], a[216:217], a[226:227], 0\n"
      "ds_write_addtid_b32 v42 offset:13824\n"
      "ds_write_addtid_b32 v28 offset:15360\n"
      "v_mfma_f32_16x16x32_bf16 v[76:79], v[204:207], a[208:211], v[76:79]\n"
      "ds_read_b128 a[200:203], v1 offset:64\n"
      "v_cvt_pk_bf16_f32 v30, v30, v31\n"
      "v_mfma_f32_16x16x32_bf16 v[80:83], v[196:199], a[208:211], v[80:83]\n"
      "ds_write_addtid_b32 v30 offset:15872\n"
      "v_fmac_f32 v32, s46, v56\n"
      "v_cvt_pk_bf16_f32 v20, v20, v24\n"
      "v_mfma_f32_16x16x32_bf16 v[84:87], v[188:191], a[208:211], v[84:87]\n"
      "ds_write_addtid_b32 v20 offset:41728\n"
      "v_fmac_f32 v33, s46, v57\n"
      "v_mfma_f32_16x16x32_bf16 v[88:91], v[200:203], a[212:215], v[88:91]\n"
      "ds_read_b128 a[204:207], v1 offset:4160\n"
      "ds_read_b64_tr_b16 a[228:229], v7 offset:17920\n"
      "v_mfma_f32_16x16x32_bf16 v[92:95], v[192:195], a[212:215], v[92:95]\n"
      "ds_read_b64_tr_b16 a[230:231], v7 offset:17928\n"
      "v_fmac_f32 v34, s46, v58\n"
      "v_mfma_f32_16x16x32_bf16 v[96:99], v[184:187], a[212:215], v[96:99]\n"
      "s_waitcnt lgkmcnt(4)\n"
      "v_fmac_f32 v35, s46, v59\n"
      "v_cvt_pk_bf16_f32 v21, v21, v25\n"
      "v_mfma_f32_16x16x32_bf16 v[76:79], v[180:183], a[192:195], v[76:79]\n"
      "s_nop 0\n"
      "ds_read_b128 a[208:211], v1 offset:128\n"
      "ds_write_addtid_b32 v21 offset:45824\n"
      "v_sub_f32 v32, v32, v44\n"
      "v_mfma_f32_16x16x32_bf16 v[80:83], v[172:175], a[192:195], v[80:83]\n"
      "v_sub_f32 v33, v33, v45\n"
      "v_cvt_pk_bf16_f32 v22, v22, v26\n"
      "v_mfma_f32_16x16x32_bf16 v[84:87], v[164:167], a[192:195], v[84:87]\n"
      "ds_write_addtid_b32 v22 offset:49920\n"
      "v_sub_f32 v34, v34, v46\n"
      "v_mfma_f32_16x16x32_bf16 v[88:91], v[176:179], a[196:199], v[88:91]\n"
      "s_nop 0\n"
      "ds_read_b128 a[212:215], v1 offset:4224\n"
      "v_sub_f32 v35, v35, v47\n"
      "v_mfma_f32_16x16x32_bf16 v[92:95], v[168:171], a[196:199], v[92:95]\n"
      "s_waitcnt lgkmcnt(4)\n"
      "v_fmac_f32 v44, s45, v32\n"
      "v_cvt_pk_bf16_f32 v23, v23, v27\n"
      "v_mfma_f32_4x4x4_16b_bf16 v[12:15], a[216:217], a[228:229], 0\n"
      "v_fmac_f32 v45, s45, v33\n"
      "v_mfma_f32_4x4x4_16b_bf16 v[16:19], a[216:217], a[230:231], 0\n"
      "ds_write_addtid_b32 v23 offset:54016\n"
      "v_fmac_f32 v46, s45, v34\n"
      "v_mfma_f32_16x16x32_bf16 v[96:99], v[160:163], a[196:199], v[96:99]\n"
      "v_fmac_f32 v47, s45, v35\n"
      "v_fma_f32 v32, s47, v32, v68\n"
      "v_cvt_pk_bf16_f32 v12, v12, v16\n"
      "v_mfma_f32_16x16x32_bf16 v[76:79], v[156:159], a[200:203], v[76:79]\n"
      "ds_read_b128 a[192:195], v2 offset:0\n"
      "ds_write_addtid_b32 v12 offset:42240\n"
      "v_mfma_f32_16x16x32_bf16 v[80:83], v[148:151], a[200:203], v[80:83]\n"
      "v_fma_f32 v33, s47, v33, v69\n"
      "v_cvt_pk_bf16_f32 v13, v13, v17\n"
      "v_mfma_f32_16x16x32_bf16 v[84:87], v[140:143], a[200:203], v[84:87]\n"
      "ds_read_b64_tr_b16 a[232:233], v7 offset:18432\n"
      "ds_write_addtid_b32 v13 offset:46336\n"
      "v_fma_f32 v34, s47, v34, v70\n"
      "v_mfma_f32_16x16x32_bf16 v[88:91], v[152:155], a[204:207], v[88:91]\n"
      "ds_read_b128 a[196:199], v2 offset:4096\n"
      "ds_read_b64_tr_b16 a[234:235], v7 offset:18440\n"
      "v_mfma_f32_16x16x32_bf16 v[92:95], v[144:147], a[204:207], v[92:95]\n"
      "s_nop 0\n"
      "v_fma_f32 v35, s47, v35, v71\n"
      "v_cvt_pk_bf16_f32 v44, v44, v45\n"
      "v_mfma_f32_16x16x32_bf16 v[96:99], v[136:139], a[204:207], v[96:99]\n"
      "s_waitcnt lgkmcnt(4)\n"
      "ds_write_addtid_b32 v44 offset:21508\n"
      "v_cvt_pk_bf16_f32 v14, v14, v18\n"
      "v_mfma_f32_16x16x32_bf16 v[76:79], v[132:135], a[208:211], v[76:79]\n"
      "ds_read_b128 a[200:203], v2 offset:64\n"
      "ds_write_addtid_b32 v14 offset:50432\n"
      "v_cvt_pk_bf16_f32 v46, v46, v47\n"
      "v_mfma_f32_16x16x32_bf16 v[80:83], v[124:127], a[208:211], v[80:83]\n"
      "ds_write_addtid_b32 v46 offset:22020\n"
      "v_cvt_pk_bf16_f32 v32, v32, v33\n"
      "v_mfma_f32_16x16x32_bf16 v[84:87], v[116:119], a[208:211], v[84:87]\n"
      "ds_write_addtid_b32 v32 offset:23556\n"
      "v_cvt_pk_bf16_f32 v34, v34, v35\n"
      "v_mfma_f32_16x16x32_bf16 v[88:91], v[128:131], a[212:215], v[88:91]\n"
      "s_waitcnt lgkmcnt(4)\n"
      "ds_read_b128 a[204:207], v2 offset:4160\n"
      "ds_write_addtid_b32 v34 offset:24068\n"
      "v_fmac_f32 v36, s46, v60\n"
      "v_mfma_f32_4x4x4_16b_bf16 v[20:23], a[216:217], a[232:233], 0\n"
      "v_fmac_f32_e64 v37, s46, v61\n"
      "v_mfma_f32_4x4x4_16b_bf16 v[24:27], a[216:217], a[234:235], 0\n"
      "v_fmac_f32 v38, s46, v62\n"
      "v_cvt_pk_bf16_f32 v15, v15, v19\n"
      "v_mfma_f32_16x16x32_bf16 v[92:95], v[120:123], a[212:215], v[92:95]\n"
      "ds_write_addtid_b32 v15 offset:54528\n"
      "v_fmac_f32 v39, s46, v63\n"
      "v_cvt_pk_bf16_f32 v20, v20, v24\n"
      "v_mfma_f32_16x16x32_bf16 v[96:99], v[112:115], a[212:215], v[96:99]\n"
      "ds_write_addtid_b32 v20 offset:42752\n"
      "v_sub_f32 v36, v36, v48\n"
      "v_mfma_f32_16x16x32_bf16 v[76:79], v[108:111], a[192:195], v[76:79]\n"
      "ds_read_b128 a[208:211], v2 offset:128\n"
      "v_sub_f32 v37, v37, v49\n"
      "v_mfma_f32_16x16x32_bf16 v[80:83], v[100:103], a[192:195], v[80:83]\n"
      "ds_read_b64_tr_b16 a[220:221], v7 offset:18944\n"
      "ds_read_b64_tr_b16 a[222:223], v7 offset:18952\n"
      "v_mfma_f32_16x16x32_bf16 v[84:87], a[160:163], a[192:195], v[84:87]\n"
      "v_sub_f32_e64 v38, v38, v50\n"
      "v_cvt_pk_bf16_f32 v21, v21, v25\n"
      "v_mfma_f32_16x16x32_bf16 v[88:91], v[104:107], a[196:199], v[88:91]\n"
      "ds_read_b128 a[212:215], v2 offset:4224\n"
      "ds_write_addtid_b32 v21 offset:46848\n"
      "v_sub_f32 v39, v39, v51\n"
      "v_mfma_f32_16x16x32_bf16 v[92:95], a[164:167], a[196:199], v[92:95]\n"
      "s_nop 0\n"
      "v_fmac_f32_e64 v48, s45, v36\n"
      "v_cvt_pk_bf16_f32 v22, v22, v26\n"
      "v_mfma_f32_16x16x32_bf16 v[96:99], a[156:159], a[196:199], v[96:99]\n"
      "s_waitcnt lgkmcnt(4)\n"
      "ds_write_addtid_b32 v22 offset:50944\n"
      "v_fmac_f32 v49, s45, v37\n"
      "v_mfma_f32_16x16x32_bf16 v[76:79], a[152:155], a[200:203], v[76:79]\n"
      "s_nop 0\n"
      "ds_read_b128 a[192:195], v3 offset:16896\n"
      "v_fmac_f32_e64 v50, s45, v38\n"
      "v_mfma_f32_16x16x32_bf16 v[80:83], a[144:147], a[200:203], v[80:83]\n"
      "s_nop 0\n"
      "v_fmac_f32 v51, s45, v39\n"
      "v_cvt_pk_bf16_f32 v23, v23, v27\n"
      "v_mfma_f32_16x16x32_bf16 v[84:87], a[136:139], a[200:203], v[84:87]\n"
      "s_waitcnt lgkmcnt(2)\n"
      "ds_write_addtid_b32 v23 offset:55040\n"
      "v_fma_f32 v36, s47, v36, v72\n"
      "v_mfma_f32_4x4x4_16b_bf16 v[12:15], a[216:217], a[220:221], 0\n"
      "v_fma_f32 v37, s47, v37, v73\n"
      "v_mfma_f32_4x4x4_16b_bf16 v[16:19], a[216:217], a[222:223], 0\n"
      "v_fma_f32 v38, s47, v38, v74\n"
      "v_fma_f32 v39, s47, v39, v75\n"
      "v_mfma_f32_16x16x32_bf16 v[88:91], a[148:151], a[204:207], v[88:91]\n"
      "ds_read_b128 a[196:199], v3 offset:16960\n"
      "v_cvt_pk_bf16_f32 v48, v48, v49\n"
      "v_cvt_pk_bf16_f32 v12, v12, v16\n"
      "v_mfma_f32_16x16x32_bf16 v[92:95], a[140:143], a[204:207], v[92:95]\n"
      "ds_write_addtid_b32 v48 offset:17408\n"
      "ds_write_addtid_b32 v12 offset:43264\n"
      "v_mfma_f32_16x16x32_bf16 v[96:99], a[132:135], a[204:207], v[96:99]\n"
      "v_cvt_pk_bf16_f32 v13, v13, v17\n"
      "v_cvt_pk_bf16_f32 v14, v14, v18\n"
      "v_mfma_f32_16x16x32_bf16 v[76:79], a[128:131], a[208:211], v[76:79]\n"
      "ds_read_b128 a[200:203], v3 offset:17024\n"
      "ds_write_addtid_b32 v13 offset:47360\n"
      "v_mfma_f32_16x16x32_bf16 v[80:83], a[120:123], a[208:211], v[80:83]\n"
      "ds_read_b64_tr_b16 a[224:225], v7 offset:19456\n"
      "ds_write_addtid_b32 v14 offset:51456\n"
      "v_cvt_pk_bf16_f32 v50, v50, v51\n"
      "v_mfma_f32_16x16x32_bf16 v[84:87], a[112:115], a[208:211], v[84:87]\n"
      "ds_write_addtid_b32 v50 offset:17920\n"
      "ds_read_b64_tr_b16 a[226:227], v7 offset:19464\n"
      "v_mfma_f32_16x16x32_bf16 v[88:91], a[124:127], a[212:215], v[88:91]\n"
      "ds_read_b128 a[204:207], v4 offset:16896\n"
      "v_cvt_pk_bf16_f32 v36, v36, v37\n"
      "v_mfma_f32_16x16x32_bf16 v[92:95], a[116:119], a[212:215], v[92:95]\n"
      "ds_write_addtid_b32 v36 offset:19456\n"
      "v_cvt_pk_bf16_f32 v15, v15, v19\n"
      "v_mfma_f32_16x16x32_bf16 v[96:99], a[108:111], a[212:215], v[96:99]\n"
      "s_waitcnt lgkmcnt(6)\n"
      "ds_write_addtid_b32 v15 offset:55552\n"
      "v_sub_f32 v76, v88, v76\n"
      "v_sub_f32_e64 v77, v89, v77\n"
      "v_cvt_pk_bf16_f32 v38, v38, v39\n"
      "v_mfma_f32_16x16x32_bf16 v[52:55], a[0:3], a[192:195], 0\n"
      "s_nop 0\n"
      "ds_read_b128 a[208:211], v4 offset:16960\n"
      "ds_write_addtid_b32 v38 offset:19968\n"
      "v_mfma_f32_16x16x32_bf16 v[56:59], a[4:7], a[192:195], 0\n"
      "s_waitcnt lgkmcnt(5)\n"
      "ds_write_addtid_b32 v76 offset:0\n"
      "ds_write_addtid_b32 v77 offset:512\n"
      "v_mfma_f32_4x4x4_16b_bf16 v[20:23], a[216:217], a[224:225], 0\n"
      "v_sub_f32 v78, v90, v78\n"
      "v_mfma_f32_4x4x4_16b_bf16 v[24:27], a[216:217], a[226:227], 0\n"
      "ds_write_addtid_b32 v78 offset:1024\n"
      "v_sub_f32 v79, v91, v79\n"
      "v_mfma_f32_16x16x32_bf16 v[60:63], a[8:11], a[192:195], 0\n"
      "ds_write_addtid_b32 v79 offset:1536\n"
      "v_sub_f32 v80, v92, v80\n"
      "v_sub_f32 v81, v93, v81\n"
      "v_mfma_f32_16x16x32_bf16 v[52:55], a[12:15], a[196:199], v[52:55]\n"
      "ds_read_b128 a[212:215], v4 offset:17024\n"
      "ds_write_addtid_b32 v80 offset:2048\n"
      "v_mfma_f32_16x16x32_bf16 v[56:59], a[16:19], a[196:199], v[56:59]\n"
      "ds_write_addtid_b32 v81 offset:2560\n"
      "v_sub_f32 v82, v94, v82\n"
      "v_mfma_f32_16x16x32_bf16 v[60:63], a[20:23], a[196:199], v[60:63]\n"
      "s_nop 0\n"
      "ds_write_addtid_b32 v82 offset:3072\n"
      "v_cvt_pk_bf16_f32 v20, v20, v24\n"
      "v_mfma_f32_16x16x32_bf16 v[52:55], a[24:27], a[200:203], v[52:55]\n"
      "ds_read_b128 a[192:195], v5 offset:16896\n"
      "ds_write_addtid_b32 v20 offset:43776\n"
      "v_sub_f32 v83, v95, v83\n"
      "v_mfma_f32_16x16x32_bf16 v[56:59], a[28:31], a[200:203], v[56:59]\n"
      "ds_write_addtid_b32 v83 offset:3584\n"
      "v_sub_f32_e64 v84, v96, v84\n"
      "v_mfma_f32_16x16x32_bf16 v[60:63], a[32:35], a[200:203], v[60:63]\n"
      "s_waitcnt lgkmcnt(5)\n"
      "ds_write_addtid_b32 v84 offset:4096\n"
      "v_cvt_pk_bf16_f32 v21, v21, v25\n"
      "v_mfma_f32_16x16x32_bf16 v[52:55], a[36:39], a[204:207], v[52:55]\n"
      "ds_read_b128 a[196:199], v5 offset:16960\n"
      "ds_write_addtid_b32 v21 offset:47872\n"
      "v_mfma_f32_16x16x32_bf16 v[56:59], a[40:43], a[204:207], v[56:59]\n"
      "v_sub_f32 v85, v97, v85\n"
      "v_sub_f32 v86, v98, v86\n"
      "v_cvt_pk_bf16_f32 v22, v22, v26\n"
      "v_mfma_f32_16x16x32_bf16 v[60:63], a[44:47], a[204:207], v[60:63]\n"
      "ds_write_addtid_b32 v22 offset:51968\n"
      "ds_write_addtid_b32 v85 offset:4608\n"
      "v_sub_f32 v87, v99, v87\n"
      "v_mfma_f32_16x16x32_bf16 v[52:55], a[48:51], a[208:211], v[52:55]\n"
      "ds_read_b128 a[200:203], v5 offset:17024\n"
      "ds_write_addtid_b32 v86 offset:5120\n"
      "v_mfma_f32_16x16x32_bf16 v[56:59], a[52:55], a[208:211], v[56:59]\n"
      "s_nop 0\n"
      "ds_write_addtid_b32 v87 offset:5632\n"
      "v_cvt_pk_bf16_f32 v23, v23, v27\n"
      "v_mfma_f32_16x16x32_bf16 v[60:63], a[56:59], a[208:211], v[60:63]\n"
      "ds_write_addtid_b32 v23 offset:56064\n"
      "v_fma_f32 v40, 0.5, v76, -v88\n"
      "v_mfma_f32_16x16x32_bf16 v[52:55], a[60:63], a[212:215], v[52:55]\n"
      "v_fma_f32 v41, 0.5, v77, -v89\n"
      "v_fma_f32 v42, 0.5, v78, -v90\n"
      "v_mfma_f32_16x16x32_bf16 v[56:59], a[64:67], a[212:215], v[56:59]\n"
      "s_nop 0\n"
      "v_fma_f32 v43, 0.5, v79, -v91\n"
      "v_fma_f32 v44, 0.5, v80, -v92\n"
      "v_mfma_f32_16x16x32_bf16 v[60:63], a[68:71], a[212:215], v[60:63]\n"
      "s_waitcnt lgkmcnt(0)\n"
      "v_fma_f32 v45, 0.5, v81, -v93\n"
      "s_barrier\n"
      "s_add_u32 s55, s55, 2\n"
      "s_cmp_lt_u32 s55, 6\n"
      "s_cbranch_scc1 LSloop_%=\n"
      "LSlast_%=:\n"
      "v_mfma_f32_16x16x32_bf16 v[52:55], a[72:75], a[192:195], v[52:55]\n"
      "s_add_u32 s59, s62, 8847360\n"
      "ds_read_b128 a[204:207], v0 offset:16384\n"
      "ds_read2st64_b32 v[28:29], v6 offset0:0 offset1:2\n"
      "v_fma_f32 v46, 0.5, v82, -v94\n"
      "v_mfma_f32_16x16x32_bf16 v[56:59], a[76:79], a[192:195], v[56:59]\n"
      "ds_read_b128 a[208:211], v0 offset:20480\n"
      "ds_read2st64_b32 v[30:31], v6 offset0:4 offset1:6\n"
      "v_mfma_f32_16x16x32_bf16 v[60:63], a[80:83], a[192:195], v[60:63]\n"
      "ds_read_b64_tr_b16 a[220:221], v7 offset:33792\n"
      "v_fma_f32 v47, 0.5, v83, -v95\n"
      "v_mfma_f32_16x16x32_bf16 v[52:55], a[84:87], a[196:199], v[52:55]\n"
      "ds_read_b128 a[212:215], v0 offset:16448\n"
      "v_fma_f32 v48, 0.5, v84, -v96\n"
      "v_mfma_f32_16x16x32_bf16 v[56:59], a[88:91], a[196:199], v[56:59]\n"
      "ds_read_b64_tr_b16 a[222:223], v7 offset:33800\n"
      "ds_read2st64_b32 v[32:33], v6 offset0:8 offset1:10\n"
      "v_fma_f32 v49, 0.5, v85, -v97\n"
      "v_mfma_f32_16x16x32_bf16 v[60:63], a[92:95], a[196:199], v[60:63]\n"
      "ds_read2st64_b32 v[34:35], v6 offset0:12 offset1:14\n"
      "v_fma_f32 v50, 0.5, v86, -v98\n"
      "v_mfma_f32_16x16x32_bf16 v[52:55], a[96:99], a[200:203], v[52:55]\n"
      "ds_read_b128 a[192:195], v0 offset:20544\n"
      "v_fma_f32 v51, 0.5, v87, -v99\n"
      "v_mfma_f32_16x16x32_bf16 v[56:59], a[100:103], a[200:203], v[56:59]\n"
      "ds_read2st64_b32 v[36:37], v6 offset0:16 offset1:18\n"
      "v_mfma_f32_16x16x32_bf16 v[60:63], a[104:107], a[200:203], v[60:63]\n"
      "s_waitcnt lgkmcnt(7)\n"
      "ds_read2st64_b32 v[38:39], v6 offset0:20 offset1:22\n"
      "v_fmac_f32 v28, s46, v64\n"
      "v_fmac_f32 v29, s46, v65\n"
      "v_fmac_f32_e64 v30, s46, v66\n"
      "v_mfma_f32_16x16x32_bf16 v[76:79], v[252:255], a[204:207], v[52:55]\n"
      "s_waitcnt lgkmcnt(5)\n"
      "ds_read_b128 a[196:199], v0 offset:16512\n"
      "v_fmac_f32 v31, s46, v67\n"
      "v_mfma_f32_4x4x4_16b_bf16 v[12:15], a[216:217], a[220:221], 0\n"
      "v_sub_f32 v28, v28, v40\n"
      "v_mfma_f32_4x4x4_16b_bf16 v[16:19], a[216:217], a[222:223], 0\n"
      "ds_read2st64_b32 a[240:241], v8 offset0:4 offset1:6\n"
      "ds_read2st64_b32 a[242:243], v9 offset0:4 offset1:6\n"
      "v_sub_f32 v29, v29, v41\n"
      "v_mfma_f32_16x16x32_bf16 v[80:83], v[244:247], a[204:207], v[56:59]\n"
      "v_sub_f32 v30, v30, v42\n"
      "v_sub_f32 v31, v31, v43\n"
      "v_mfma_f32_16x16x32_bf16 v[84:87], v[236:239], a[204:207], v[60:63]\n"
      "v_fmac_f32 v40, s45, v28\n"
      "v_fmac_f32 v41, s45, v29\n"
      "v_mfma_f32_16x16x32_bf16 v[88:91], v[248:251], a[208:211], v[52:55]\n"
      "ds_read_b128 a[200:203], v0 offset:20608\n"
      "v_cvt_pk_bf16_f32 v12, v12, v16\n"
      "v_mfma_f32_16x16x32_bf16 v[92:95], v[240:243], a[208:211], v[56:59]\n"
      "s_nop 0\n"
      "ds_write_addtid_b32 v12 offset:24832\n"
      "ds_read_b64_tr_b16 a[224:225], v7 offset:34304\n"
      "v_fmac_f32 v42, s45, v30\n"
      "v_mfma_f32_16x16x32_bf16 v[96:99], v[232:235], a[208:211], v[60:63]\n"
      "ds_read_b64_tr_b16 a[226:227], v7 offset:34312\n"
      "v_fmac_f32 v43, s45, v31\n"
      "v_mfma_f32_16x16x32_bf16 v[76:79], v[228:231], a[212:215], v[76:79]\n"
      "ds_read_b128 a[204:207], v1 offset:16384\n"
      "v_cvt_pk_bf16_f32 v13, v13, v17\n"
      "v_mfma_f32_16x16x32_bf16 v[80:83], v[220:223], a[212:215], v[80:83]\n"
      "ds_write_addtid_b32 v13 offset:28928\n"
      "v_fma_f32 v28, s47, v28, v52\n"
      "v_mfma_f32_16x16x32_bf16 v[84:87], v[212:215], a[212:215], v[84:87]\n"
      "s_waitcnt lgkmcnt(6)\n"
      "v_fma_f32 v29, s47, v29, v53\n"
      "v_cvt_pk_bf16_f32 v14, v14, v18\n"
      "v_mfma_f32_16x16x32_bf16 v[88:91], v[224:227], a[192:195], v[88:91]\n"
      "s_nop 0\n"
      "ds_read_b128 a[208:211], v1 offset:20480\n"
      "ds_write_addtid_b32 v14 offset:33024\n"
      "v_fma_f32 v30, s47, v30, v54\n"
      "v_mfma_f32_16x16x32_bf16 v[92:95], v[216:219], a[192:195], v[92:95]\n"
      "s_nop 0\n"
      "v_fma_f32 v31, s47, v31, v55\n"
      "v_cvt_pk_bf16_f32 v15, v15, v19\n"
      "v_mfma_f32_16x16x32_bf16 v[96:99], v[208:211], a[192:195], v[96:99]\n"
      "s_waitcnt lgkmcnt(2)\n"
      "ds_write_addtid_b32 v15 offset:37120\n"
      "v_cvt_pk_bf16_f32 v40, v40, v41\n"
      "v_mfma_f32_4x4x4_16b_bf16 v[20:23], a[216:217], a[224:225], 0\n"
      "v_cvt_pk_bf16_f32 v42, v42, v43\n"
      "v_mfma_f32_4x4x4_16b_bf16 v[24:27], a[216:217], a[226:227], 0\n"
      "ds_write_addtid_b32 v40 offset:12288\n"
      "ds_write_addtid_b32 v42 offset:12800\n"
      "v_cvt_pk_bf16_f32 v28, v28, v29\n"
      "v_mfma_f32_16x16x32_bf16 v[76:79], v[204:207], a[196:199], v[76:79]\n"
      "ds_read_b128 a[212:215], v1 offset:16448\n"
      "ds_write_addtid_b32 v28 offset:14336\n"
      "v_cvt_pk_bf16_f32 v20, v20, v24\n"
      "v_mfma_f32_16x16x32_bf16 v[80:83], v[196:199], a[196:199], v[80:83]\n"
      "ds_write_addtid_b32 v20 offset:25344\n"
      "v_cvt_pk_bf16_f32 v30, v30, v31\n"
      "v_mfma_f32_16x16x32_bf16 v[84:87], v[188:191], a[196:199], v[84:87]\n"
      "buffer_store_dwordx4 a[240:243], v11, s[8:11], s59 offen offset:256 sc1\n"
      "v_mfma_f32_16x16x32_bf16 v[88:91], v[200:203], a[200:203], v[88:91]\n"
      "ds_read_b128 a[192:195], v1 offset:20544\n"
      "ds_write_addtid_b32 v30 offset:14848\n"
      "v_mfma_f32_16x16x32_bf16 v[92:95], v[192:195], a[200:203], v[92:95]\n"
      "ds_read_b64_tr_b16 a[228:229], v7 offset:34816\n"
      "ds_read_b64_tr_b16 a[230:231], v7 offset:34824\n"
      "v_fmac_f32 v32, s46, v68\n"
      "v_mfma_f32_16x16x32_bf16 v[96:99], v[184:187], a[200:203], v[96:99]\n"
      "v_fmac_f32 v33, s46, v69\n"
      "v_cvt_pk_bf16_f32 v21, v21, v25\n"
      "v_mfma_f32_16x16x32_bf16 v[76:79], v[180:183], a[204:207], v[76:79]\n"
      "ds_read_b128 a[196:199], v1 offset:16512\n"
      "ds_write_addtid_b32 v21 offset:29440\n"
      "v_mfma_f32_16x16x32_bf16 v[80:83], v[172:175], a[204:207], v[80:83]\n"
      "v_fmac_f32 v34, s46, v70\n"
      "v_fmac_f32 v35, s46, v71\n"
      "v_mfma_f32_16x16x32_bf16 v[84:87], v[164:167], a[204:207], v[84:87]\n"
      "s_waitcnt lgkmcnt(6)\n"
      "v_sub_f32 v32, v32, v44\n"
      "v_cvt_pk_bf16_f32 v22, v22, v26\n"
      "v_mfma_f32_16x16x32_bf16 v[88:91], v[176:179], a[208:211], v[88:91]\n"
      "ds_read_b128 a[200:203], v1 offset:20608\n"
      "ds_write_addtid_b32 v22 offset:33536\n"
      "v_sub_f32 v33, v33, v45\n"
      "v_mfma_f32_16x16x32_bf16 v[92:95], v[168:171], a[208:211], v[92:95]\n"
      "s_waitcnt lgkmcnt(4)\n"
      "v_sub_f32_e64 v34, v34, v46\n"
      "v_cvt_pk_bf16_f32 v23, v23, v27\n"
      "v_mfma_f32_4x4x4_16b_bf16 v[12:15], a[216:217], a[228:229], 0\n"
      "v_sub_f32 v35, v35, v47\n"
      "v_mfma_f32_4x4x4_16b_bf16 v[16:19], a[216:217], a[230:231], 0\n"
      "ds_write_addtid_b32 v23 offset:37632\n"
      "v_fmac_f32 v44, s45, v32\n"
      "v_mfma_f32_16x16x32_bf16 v[96:99], v[160:163], a[208:211], v[96:99]\n"
      "v_fmac_f32 v45, s45, v33\n"
      "v_fmac_f32 v46, s45, v34\n"
      "v_cvt_pk_bf16_f32 v12, v12, v16\n"
      "v_mfma_f32_16x16x32_bf16 v[76:79], v[156:159], a[212:215], v[76:79]\n"
      "ds_read_b128 a[204:207], v2 offset:16384\n"
      "ds_write_addtid_b32 v12 offset:25856\n"
      "v_mfma_f32_16x16x32_bf16 v[80:83], v[148:151], a[212:215], v[80:83]\n"
      "v_fmac_f32 v47, s45, v35\n"
      "v_cvt_pk_bf16_f32 v13, v13, v17\n"
      "v_mfma_f32_16x16x32_bf16 v[84:87], v[140:143], a[212:215], v[84:87]\n"
      "s_nop 0\n"
      "ds_read_b64_tr_b16 a[232:233], v7 offset:35328\n"
      "ds_write_addtid_b32 v13 offset:29952\n"
      "v_fma_f32 v32, s47, v32, v56\n"
      "v_mfma_f32_16x16x32_bf16 v[88:91], v[152:155], a[192:195], v[88:91]\n"
      "ds_read_b128 a[208:211], v2 offset:20480\n"
      "ds_read_b64_tr_b16 a[234:235], v7 offset:35336\n"
      "v_mfma_f32_16x16x32_bf16 v[92:95], v[144:147], a[192:195], v[92:95]\n"
      "v_fma_f32 v33, s47, v33, v57\n"
      "v_fma_f32 v34, s47, v34, v58\n"
      "v_mfma_f32_16x16x32_bf16 v[96:99], v[136:139], a[192:195], v[96:99]\n"
      "s_waitcnt lgkmcnt(4)\n"
      "v_fma_f32 v35, s47, v35, v59\n"
      "v_cvt_pk_bf16_f32 v14, v14, v18\n"
      "v_mfma_f32_16x16x32_bf16 v[76:79], v[132:135], a[196:199], v[76:79]\n"
      "ds_read_b128 a[212:215], v2 offset:16448\n"
      "ds_write_addtid_b32 v14 offset:34048\n"
      "v_mfma_f32_16x16x32_bf16 v[80:83], v[124:127], a[196:199], v[80:83]\n"
      "s_nop 0\n"
      "v_cvt_pk_bf16_f32 v44, v44, v45\n"
      "v_cvt_pk_bf16_f32 v46, v46, v47\n"
      "v_mfma_f32_16x16x32_bf16 v[84:87], v[116:119], a[196:199], v[84:87]\n"
      "s_nop 0\n"
      "ds_write_addtid_b32 v44 offset:20484\n"
      "ds_write_addtid_b32 v46 offset:20996\n"
      "v_cvt_pk_bf16_f32 v32, v32, v33\n"
      "v_mfma_f32_16x16x32_bf16 v[88:91], v[128:131], a[200:203], v[88:91]\n"
      "s_waitcnt lgkmcnt(4)\n"
      "ds_read_b128 a[192:195], v2 offset:20544\n"
      "ds_write_addtid_b32 v32 offset:22532\n"
      "v_mfma_f32_4x4x4_16b_bf16 v[20:23], a[216:217], a[232:233], 0\n"
      "v_cvt_pk_bf16_f32 v34, v34, v35\n"
      "v_cvt_pk_bf16_f32 v15, v15, v19\n"
      "v_mfma_f32_4x4x4_16b_bf16 v[24:27], a[216:217], a[234:235], 0\n"
      "ds_write_addtid_b32 v34 offset:23044\n"
      "ds_write_addtid_b32 v15 offset:38144\n"
      "v_fmac_f32_e64 v36, s46, v72\n"
      "v_mfma_f32_16x16x32_bf16 v[92:95], v[120:123], a[200:203], v[92:95]\n"
      "v_fmac_f32 v37, s46, v73\n"
      "v_cvt_pk_bf16_f32 v20, v20, v24\n"
      "v_mfma_f32_16x16x32_bf16 v[96:99], v[112:115], a[200:203], v[96:99]\n"
      "ds_write_addtid_b32 v20 offset:26368\n"
      "v_fmac_f32 v38, s46, v74\n"
      "v_mfma_f32_16x16x32_bf16 v[76:79], v[108:111], a[204:207], v[76:79]\n"
      "ds_read_b128 a[196:199], v2 offset:16512\n"
      "v_fmac_f32_e64 v39, s46, v75\n"
      "v_mfma_f32_16x16x32_bf16 v[80:83], v[100:103], a[204:207], v[80:83]\n"
      "ds_read_b64_tr_b16 a[220:221], v7 offset:35840\n"
      "ds_read_b64_tr_b16 a[222:223], v7 offset:35848\n"
      "v_sub_f32 v36, v36, v48\n"
      "v_mfma_f32_16x16x32_bf16 v[84:87], a[160:163], a[204:207], v[84:87]\n"
      "v_sub_f32 v37, v37, v49\n"
      "v_cvt_pk_bf16_f32 v21, v21, v25\n"
      "v_mfma_f32_16x16x32_bf16 v[88:91], v[104:107], a[208:211], v[88:91]\n"
      "ds_read_b128 a[200:203], v2 offset:20608\n"
      "ds_write_addtid_b32 v21 offset:30464\n"
      "v_mfma_f32_16x16x32_bf16 v[92:95], a[164:167], a[208:211], v[92:95]\n"
      "v_sub_f32 v38, v38, v50\n"
      "v_sub_f32 v39, v39, v51\n"
      "v_mfma_f32_16x16x32_bf16 v[96:99], a[156:159], a[208:211], v[96:99]\n"
      "s_waitcnt lgkmcnt(3)\n"
      "v_fmac_f32 v48, s45, v36\n"
      "v_cvt_pk_bf16_f32 v22, v22, v26\n"
      "v_mfma_f32_16x16x32_bf16 v[76:79], a[152:155], a[212:215], v[76:79]\n"
      "ds_read_b128 a[204:207], v3 offset:33792\n"
      "ds_write_addtid_b32 v22 offset:34560\n"
      "v_fmac_f32 v49, s45, v37\n"
      "v_mfma_f32_16x16x32_bf16 v[80:83], a[144:147], a[212:215], v[80:83]\n"
      "v_fmac_f32 v50, s45, v38\n"
      "v_cvt_pk_bf16_f32 v23, v23, v27\n"
      "v_mfma_f32_16x16x32_bf16 v[84:87], a[136:139], a[212:215], v[84:87]\n"
      "s_waitcnt lgkmcnt(2)\n"
      "ds_write_addtid_b32 v23 offset:38656\n"
      "v_fmac_f32 v51, s45, v39\n"
      "v_mfma_f32_4x4x4_16b_bf16 v[12:15], a[216:217], a[220:221], 0\n"
      "v_fma_f32 v36, s47, v36, v60\n"
      "v_mfma_f32_4x4x4_16b_bf16 v[16:19], a[216:217], a[222:223], 0\n"
      "v_fma_f32 v37, s47, v37, v61\n"
      "v_fma_f32 v38, s47, v38, v62\n"
      "v_mfma_f32_16x16x32_bf16 v[88:91], a[148:151], a[192:195], v[88:91]\n"
      "ds_read_b128 a[208:211], v3 offset:33856\n"
      "v_fma_f32 v39, s47, v39, v63\n"
      "v_cvt_pk_bf16_f32 v12, v12, v16\n"
      "v_mfma_f32_16x16x32_bf16 v[92:95], a[140:143], a[192:195], v[92:95]\n"
      "ds_write_addtid_b32 v12 offset:26880\n"
      "v_cvt_pk_bf16_f32 v13, v13, v17\n"
      "v_mfma_f32_16x16x32_bf16 v[96:99], a[132:135], a[192:195], v[96:99]\n"
      "ds_write_addtid_b32 v13 offset:30976\n"
      "v_cvt_pk_bf16_f32 v48, v48, v49\n"
      "v_mfma_f32_16x16x32_bf16 v[76:79], a[128:131], a[196:199], v[76:79]\n"
      "ds_read_b128 a[212:215], v3 offset:33920\n"
      "ds_write_addtid_b32 v48 offset:16384\n"
      "v_mfma_f32_16x16x32_bf16 v[80:83], a[120:123], a[196:199], v[80:83]\n"
      "ds_read_b64_tr_b16 a[224:225], v7 offset:36352\n"
      "ds_read_b64_tr_b16 a[226:227], v7 offset:36360\n"
      "v_cvt_pk_bf16_f32 v14, v14, v18\n"
      "v_mfma_f32_16x16x32_bf16 v[84:87], a[112:115], a[196:199], v[84:87]\n"
      "ds_write_addtid_b32 v14 offset:35072\n"
      "v_cvt_pk_bf16_f32 v50, v50, v51\n"
      "v_mfma_f32_16x16x32_bf16 v[88:91], a[124:127], a[200:203], v[88:91]\n"
      "ds_read_b128 a[192:195], v4 offset:33792\n"
      "ds_write_addtid_b32 v50 offset:16896\n"
      "v_mfma_f32_16x16x32_bf16 v[92:95], a[116:119], a[200:203], v[92:95]\n"
      "s_nop 0\n"
      "v_cvt_pk_bf16_f32 v36, v36, v37\n"
      "v_cvt_pk_bf16_f32 v15, v15, v19\n"
      "v_mfma_f32_16x16x32_bf16 v[96:99], a[108:111], a[200:203], v[96:99]\n"
      "s_waitcnt lgkmcnt(5)\n"
      "ds_write_addtid_b32 v36 offset:18432\n"
      "ds_write_addtid_b32 v15 offset:39168\n"
      "v_sub_f32 v76, v88, v76\n"
      "v_sub_f32 v77, v89, v77\n"
      "v_mfma_f32_16x16x32_bf16 v[64:67], a[0:3], a[204:207], 0\n"
      "s_nop 0\n"
      "ds_read_b128 a[196:199], v4 offset:33856\n"
      "ds_write_addtid_b32 v76 offset:6144\n"
      "v_mfma_f32_16x16x32_bf16 v[68:71], a[4:7], a[204:207], 0\n"
      "s_waitcnt lgkmcnt(6)\n"
      "ds_write_addtid_b32 v77 offset:6656\n"
      "v_sub_f32 v78, v90, v78\n"
      "v_mfma_f32_4x4x4_16b_bf16 v[20:23], a[216:217], a[224:225], 0\n"
      "v_sub_f32 v79, v91, v79\n"
      "v_cvt_pk_bf16_f32 v38, v38, v39\n"
      "v_mfma_f32_4x4x4_16b_bf16 v[24:27], a[216:217], a[226:227], 0\n"
      "ds_write_addtid_b32 v38 offset:18944\n"
      "ds_write_addtid_b32 v78 offset:7168\n"
      "v_mfma_f32_16x16x32_bf16 v[72:75], a[8:11], a[204:207], 0\n"
      "ds_write_addtid_b32 v79 offset:7680\n"
      "v_sub_f32 v80, v92, v80\n"
      "v_mfma_f32_16x16x32_bf16 v[64:67], a[12:15], a[208:211], v[64:67]\n"
      "ds_read_b128 a[200:203], v4 offset:33920\n"
      "ds_write_addtid_b32 v80 offset:8192\n"
      "v_sub_f32 v81, v93, v81\n"
      "v_mfma_f32_16x16x32_bf16 v[68:71], a[16:19], a[208:211], v[68:71]\n"
      "ds_write_addtid_b32 v81 offset:8704\n"
      "v_sub_f32_e64 v82, v94, v82\n"
      "v_mfma_f32_16x16x32_bf16 v[72:75], a[20:23], a[208:211], v[72:75]\n"
      "ds_write_addtid_b32 v82 offset:9216\n"
      "v_cvt_pk_bf16_f32 v20, v20, v24\n"
      "v_mfma_f32_16x16x32_bf16 v[64:67], a[24:27], a[212:215], v[64:67]\n"
      "ds_read_b128 a[204:207], v5 offset:33792\n"
      "ds_write_addtid_b32 v20 offset:27392\n"
      "v_mfma_f32_16x16x32_bf16 v[68:71], a[28:31], a[212:215], v[68:71]\n"
      "v_sub_f32 v83, v95, v83\n"
      "v_sub_f32 v84, v96, v84\n"
      "v_cvt_pk_bf16_f32 v21, v21, v25\n"
      "v_mfma_f32_16x16x32_bf16 v[72:75], a[32:35], a[212:215], v[72:75]\n"
      "s_waitcnt lgkmcnt(4)\n"
      "ds_write_addtid_b32 v21 offset:31488\n"
      "ds_write_addtid_b32 v83 offset:9728\n"
      "v_mfma_f32_16x16x32_bf16 v[64:67], a[36:39], a[192:195], v[64:67]\n"
      "s_nop 0\n"
      "ds_read_b128 a[208:211], v5 offset:33856\n"
      "ds_write_addtid_b32 v84 offset:10240\n"
      "v_mfma_f32_16x16x32_bf16 v[68:71], a[40:43], a[192:195], v[68:71]\n"
      "v_sub_f32 v85, v97, v85\n"
      "v_sub_f32 v86, v98, v86\n"
      "v_cvt_pk_bf16_f32 v22, v22, v26\n"
      "v_mfma_f32_16x16x32_bf16 v[72:75], a[44:47], a[192:195], v[72:75]\n"
      "ds_write_addtid_b32 v22 offset:35584\n"
      "ds_write_addtid_b32 v85 offset:10752\n"
      "v_sub_f32 v87, v99, v87\n"
      "v_mfma_f32_16x16x32_bf16 v[64:67], a[48:51], a[196:199], v[64:67]\n"
      "s_nop 0\n"
      "ds_read_b128 a[212:215], v5 offset:33920\n"
      "ds_write_addtid_b32 v86 offset:11264\n"
      "v_mfma_f32_16x16x32_bf16 v[68:71], a[52:55], a[196:199], v[68:71]\n"
      "ds_write_addtid_b32 v87 offset:11776\n"
      "v_cvt_pk_bf16_f32 v23, v23, v27\n"
      "v_mfma_f32_16x16x32_bf16 v[72:75], a[56:59], a[196:199], v[72:75]\n"
      "ds_write_addtid_b32 v23 offset:39680\n"
      "v_fma_f32 v40, 0.5, v76, -v88\n"
      "v_mfma_f32_16x16x32_bf16 v[64:67], a[60:63], a[200:203], v[64:67]\n"
      "v_fma_f32 v41, 0.5, v77, -v89\n"
      "v_fma_f32 v42, 0.5, v78, -v90\n"
      "v_mfma_f32_16x16x32_bf16 v[68:71], a[64:67], a[200:203], v[68:71]\n"
      "s_nop 0\n"
      "v_fma_f32 v43, 0.5, v79, -v91\n"
      "v_fma_f32 v44, 0.5, v80, -v92\n"
      "v_mfma_f32_16x16x32_bf16 v[72:75], a[68:71], a[200:203], v[72:75]\n"
      "s_waitcnt lgkmcnt(0)\n"
      "v_fma_f32 v45, 0.5, v81, -v93\n"
      "s_barrier\n"
      "v_mfma_f32_16x16x32_bf16 v[64:67], a[72:75], a[204:207], v[64:67]\n"
      "s_add_u32 s59, s62, 9633792\n"
      "ds_read_b128 a[192:195], v0 offset:0\n"
      "ds_read2st64_b32 v[28:29], v6 offset0:24 offset1:26\n"
      "v_fma_f32 v46, 0.5, v82, -v94\n"
      "v_mfma_f32_16x16x32_bf16 v[68:71], a[76:79], a[204:207], v[68:71]\n"
      "s_nop 0\n"
      "ds_read_b128 a[196:199], v0 offset:4096\n"
      "ds_read2st64_b32 v[30:31], v6 offset0:28 offset1:30\n"
      "v_mfma_f32_16x16x32_bf16 v[72:75], a[80:83], a[204:207], v[72:75]\n"
      "ds_read_b64_tr_b16 a[220:221], v7 offset:50688\n"
      "v_fma_f32 v47, 0.5, v83, -v95\n"
      "v_mfma_f32_16x16x32_bf16 v[64:67], a[84:87], a[208:211], v[64:67]\n"
      "ds_read_b128 a[200:203], v0 offset:64\n"
      "v_fma_f32 v48, 0.5, v84, -v96\n"
      "v_mfma_f32_16x16x32_bf16 v[68:71], a[88:91], a[208:211], v[68:71]\n"
      "ds_read_b64_tr_b16 a[222:223], v7 offset:50696\n"
      "ds_read2st64_b32 v[32:33], v6 offset0:32 offset1:34\n"
      "v_fma_f32 v49, 0.5, v85, -v97\n"
      "v_mfma_f32_16x16x32_bf16 v[72:75], a[92:95], a[208:211], v[72:75]\n"
      "ds_read2st64_b32 v[34:35], v6 offset0:36 offset1:38\n"
      "v_fma_f32 v50, 0.5, v86, -v98\n"
      "v_mfma_f32_16x16x32_bf16 v[64:67], a[96:99], a[212:215], v[64:67]\n"
      "ds_read_b128 a[204:207], v0 offset:4160\n"
      "v_fma_f32 v51, 0.5, v87, -v99\n"
      "v_mfma_f32_16x16x32_bf16 v[68:71], a[100:103], a[212:215], v[68:71]\n"
      "ds_read2st64_b32 v[36:37], v6 offset0:40 offset1:42\n"
      "v_mfma_f32_16x16x32_bf16 v[72:75], a[104:107], a[212:215], v[72:75]\n"
      "s_waitcnt lgkmcnt(7)\n"
      "ds_read2st64_b32 v[38:39], v6 offset0:44 offset1:46\n"
      "v_fmac_f32 v28, s46, v52\n"
      "v_fmac_f32 v29, s46, v53\n"
      "v_fmac_f32 v30, s46, v54\n"
      "v_mfma_f32_16x16x32_bf16 v[76:79], v[252:255], a[192:195], v[64:67]\n"
      "s_waitcnt lgkmcnt(5)\n"
      "ds_read_b128 a[208:211], v0 offset:128\n"
      "v_fmac_f32 v31, s46, v55\n"
      "v_mfma_f32_4x4x4_16b_bf16 v[12:15], a[216:217], a[220:221], 0\n"
      "v_sub_f32 v28, v28, v40\n"
      "v_mfma_f32_4x4x4_16b_bf16 v[16:19], a[216:217], a[222:223], 0\n"
      "ds_read2st64_b32 a[240:241], v8 offset0:0 offset1:2\n"
      "ds_read2st64_b32 a[242:243], v9 offset0:0 offset1:2\n"
      "v_sub_f32 v29, v29, v41\n"
      "v_mfma_f32_16x16x32_bf16 v[80:83], v[244:247], a[192:195], v[68:71]\n"
      "v_sub_f32 v30, v30, v42\n"
      "v_sub_f32 v31, v31, v43\n"
      "v_mfma_f32_16x16x32_bf16 v[84:87], v[236:239], a[192:195], v[72:75]\n"
      "v_fmac_f32 v40, s45, v28\n"
      "v_fmac_f32 v41, s45, v29\n"
      "v_mfma_f32_16x16x32_bf16 v[88:91], v[248:251], a[196:199], v[64:67]\n"
      "ds_read_b128 a[212:215], v0 offset:4224\n"
      "v_cvt_pk_bf16_f32 v12, v12, v16\n"
      "v_mfma_f32_16x16x32_bf16 v[92:95], v[240:243], a[196:199], v[68:71]\n"
      "ds_write_addtid_b32 v12 offset:41216\n"
      "ds_read_b64_tr_b16 a[224:225], v7 offset:51200\n"
      "v_fmac_f32 v42, s45, v30\n"
      "v_mfma_f32_16x16x32_bf16 v[96:99], v[232:235], a[196:199], v[72:75]\n"
      "ds_read_b64_tr_b16 a[226:227], v7 offset:51208\n"
      "v_fmac_f32 v43, s45, v31\n"
      "v_mfma_f32_16x16x32_bf16 v[76:79], v[228:231], a[200:203], v[76:79]\n"
      "ds_read_b128 a[192:195], v1 offset:0\n"
      "v_cvt_pk_bf16_f32 v13, v13, v17\n"
      "v_mfma_f32_16x16x32_bf16 v[80:83], v[220:223], a[200:203], v[80:83]\n"
      "ds_write_addtid_b32 v13 offset:45312\n"
      "v_fma_f32 v28, s47, v28, v64\n"
      "v_mfma_f32_16x16x32_bf16 v[84:87], v[212:215], a[200:203], v[84:87]\n"
      "s_waitcnt lgkmcnt(6)\n"
      "v_fma_f32 v29, s47, v29, v65\n"
      "v_cvt_pk_bf16_f32 v14, v14, v18\n"
      "v_mfma_f32_16x16x32_bf16 v[88:91], v[224:227], a[204:207], v[88:91]\n"
      "s_nop 0\n"
      "ds_read_b128 a[196:199], v1 offset:4096\n"
      "ds_write_addtid_b32 v14 offset:49408\n"
      "v_fma_f32 v30, s47, v30, v66\n"
      "v_mfma_f32_16x16x32_bf16 v[92:95], v[216:219], a[204:207], v[92:95]\n"
      "s_nop 0\n"
      "v_fma_f32 v31, s47, v31, v67\n"
      "v_cvt_pk_bf16_f32 v15, v15, v19\n"
      "v_mfma_f32_16x16x32_bf16 v[96:99], v[208:211], a[204:207], v[96:99]\n"
      "s_waitcnt lgkmcnt(2)\n"
      "ds_write_addtid_b32 v15 offset:53504\n"
      "v_cvt_pk_bf16_f32 v40, v40, v41\n"
      "v_mfma_f32_4x4x4_16b_bf16 v[20:23], a[216:217], a[224:225], 0\n"
      "v_cvt_pk_bf16_f32 v42, v42, v43\n"
      "v_mfma_f32_4x4x4_16b_bf16 v[24:27], a[216:217], a[226:227], 0\n"
      "ds_write_addtid_b32 v40 offset:13312\n"
      "ds_write_addtid_b32 v42 offset:13824\n"
      "v_cvt_pk_bf16_f32 v28, v28, v29\n"
      "v_mfma_f32_16x16x32_bf16 v[76:79], v[204:207], a[208:211], v[76:79]\n"
      "ds_read_b128 a[200:203], v1 offset:64\n"
      "ds_write_addtid_b32 v28 offset:15360\n"
      "v_cvt_pk_bf16_f32 v20, v20, v24\n"
      "v_mfma_f32_16x16x32_bf16 v[80:83], v[196:199], a[208:211], v[80:83]\n"
      "ds_write_addtid_b32 v20 offset:41728\n"
      "v_cvt_pk_bf16_f32 v30, v30, v31\n"
      "v_mfma_f32_16x16x32_bf16 v[84:87], v[188:191], a[208:211], v[84:87]\n"
      "buffer_store_dwordx4 a[240:243], v11, s[8:11], s59 offen offset:256 sc1\n"
      "v_mfma_f32_16x16x32_bf16 v[88:91], v[200:203], a[212:215], v[88:91]\n"
      "ds_read_b128 a[204:207], v1 offset:4160\n"
      "ds_write_addtid_b32 v30 offset:15872\n"
      "v_mfma_f32_16x16x32_bf16 v[92:95], v[192:195], a[212:215], v[92:95]\n"
      "ds_read_b64_tr_b16 a[228:229], v7 offset:51712\n"
      "ds_read_b64_tr_b16 a[230:231], v7 offset:51720\n"
      "v_fmac_f32_e64 v32, s46, v56\n"
      "v_mfma_f32_16x16x32_bf16 v[96:99], v[184:187], a[212:215], v[96:99]\n"
      "v_fmac_f32 v33, s46, v57\n"
      "v_cvt_pk_bf16_f32 v21, v21, v25\n"
      "v_mfma_f32_16x16x32_bf16 v[76:79], v[180:183], a[192:195], v[76:79]\n"
      "ds_read_b128 a[208:211], v1 offset:128\n"
      "ds_write_addtid_b32 v21 offset:45824\n"
      "v_mfma_f32_16x16x32_bf16 v[80:83], v[172:175], a[192:195], v[80:83]\n"
      "v_fmac_f32 v34, s46, v58\n"
      "v_fmac_f32_e64 v35, s46, v59\n"
      "v_mfma_f32_16x16x32_bf16 v[84:87], v[164:167], a[192:195], v[84:87]\n"
      "s_waitcnt lgkmcnt(6)\n"
      "v_sub_f32 v32, v32, v44\n"
      "v_cvt_pk_bf16_f32 v22, v22, v26\n"
      "v_mfma_f32_16x16x32_bf16 v[88:91], v[176:179], a[196:199], v[88:91]\n"
      "ds_read_b128 a[212:215], v1 offset:4224\n"
      "ds_write_addtid_b32 v22 offset:49920\n"
      "v_sub_f32 v33, v33, v45\n"
      "v_mfma_f32_16x16x32_bf16 v[92:95], v[168:171], a[196:199], v[92:95]\n"
      "s_waitcnt lgkmcnt(4)\n"
      "v_sub_f32 v34, v34, v46\n"
      "v_cvt_pk_bf16_f32 v23, v23, v27\n"
      "v_mfma_f32_4x4x4_16b_bf16 v[12:15], a[216:217], a[228:229], 0\n"
      "v_sub_f32 v35, v35, v47\n"
      "v_mfma_f32_4x4x4_16b_bf16 v[16:19], a[216:217], a[230:231], 0\n"
      "ds_write_addtid_b32 v23 offset:54016\n"
      "v_fmac_f32 v44, s45, v32\n"
      "v_mfma_f32_16x16x32_bf16 v[96:99], v[160:163], a[196:199], v[96:99]\n"
      "s_nop 0\n"
      "v_fmac_f32_e64 v45, s45, v33\n"
      "v_fmac_f32_e64 v46, s45, v34\n"
      "v_cvt_pk_bf16_f32 v12, v12, v16\n"
      "v_mfma_f32_16x16x32_bf16 v[76:79], v[156:159], a[200:203], v[76:79]\n"
      "s_nop 0\n"
      "ds_read_b128 a[192:195], v2 offset:0\n"
      "ds_write_addtid_b32 v12 offset:42240\n"
      "v_mfma_f32_16x16x32_bf16 v[80:83], v[148:151], a[200:203], v[80:83]\n"
      "s_nop 0\n"
      "v_fmac_f32_e64 v47, s45, v35\n"
      "v_cvt_pk_bf16_f32 v13, v13, v17\n"
      "v_mfma_f32_16x16x32_bf16 v[84:87], v[140:143], a[200:203], v[84:87]\n"
      "s_nop 0\n"
      "ds_read_b64_tr_b16 a[232:233], v7 offset:52224\n"
      "ds_write_addtid_b32 v13 offset:46336\n"
      "v_fma_f32 v32, s47, v32, v68\n"
      "v_mfma_f32_16x16x32_bf16 v[88:91], v[152:155], a[204:207], v[88:91]\n"
      "s_nop 0\n"
      "ds_read_b128 a[196:199], v2 offset:4096\n"
      "ds_read_b64_tr_b16 a[234:235], v7 offset:52232\n"
      "v_mfma_f32_16x16x32_bf16 v[92:95], v[144:147], a[204:207], v[92:95]\n"
      "s_nop 0\n"
      "v_fma_f32 v33, s47, v33, v69\n"
      "v_fma_f32 v34, s47, v34, v70\n"
      "v_mfma_f32_16x16x32_bf16 v[96:99], v[136:139], a[204:207], v[96:99]\n"
      "s_waitcnt lgkmcnt(4)\n"
      "v_fma_f32 v35, s47, v35, v71\n"
      "v_cvt_pk_bf16_f32 v14, v14, v18\n"
      "v_mfma_f32_16x16x32_bf16 v[76:79], v[132:135], a[208:211], v[76:79]\n"
      "s_nop 0\n"
      "ds_read_b128 a[200:203], v2 offset:64\n"
      "ds_write_addtid_b32 v14 offset:50432\n"
      "v_mfma_f32_16x16x32_bf16 v[80:83], v[124:127], a[208:211], v[80:83]\n"
      "s_nop 0\n"
      "v_cvt_pk_bf16_f32 v44, v44, v45\n"
      "v_cvt_pk_bf16_f32 v46, v46, v47\n"
      "v_mfma_f32_16x16x32_bf16 v[84:87], v[116:119], a[208:211], v[84:87]\n"
      "s_nop 0\n"
      "ds_write_addtid_b32 v44 offset:21508\n"
      "ds_write_addtid_b32 v46 offset:22020\n"
      "v_cvt_pk_bf16_f32 v32, v32, v33\n"
      "v_mfma_f32_16x16x32_bf16 v[88:91], v[128:131], a[212:215], v[88:91]\n"
      "s_waitcnt lgkmcnt(4)\n"
      "ds_read_b128 a[204:207], v2 offset:4160\n"
      "ds_write_addtid_b32 v32 offset:23556\n"
      "v_mfma_f32_4x4x4_16b_bf16 v[20:23], a[216:217], a[232:233], 0\n"
      "v_cvt_pk_bf16_f32 v34, v34, v35\n"
      "v_cvt_pk_bf16_f32 v15, v15, v19\n"
      "v_mfma_f32_4x4x4_16b_bf16 v[24:27], a[216:217], a[234:235], 0\n"
      "ds_write_addtid_b32 v34 offset:24068\n"
      "ds_write_addtid_b32 v15 offset:54528\n"
      "v_fmac_f32 v36, s46, v60\n"
      "v_mfma_f32_16x16x32_bf16 v[92:95], v[120:123], a[212:215], v[92:95]\n"
      "v_fmac_f32 v37, s46, v61\n"
      "v_cvt_pk_bf16_f32 v20, v20, v24\n"
      "v_mfma_f32_16x16x32_bf16 v[96:99], v[112:115], a[212:215], v[96:99]\n"
      "ds_write_addtid_b32 v20 offset:42752\n"
      "v_fmac_f32 v38, s46, v62\n"
      "v_mfma_f32_16x16x32_bf16 v[76:79], v[108:111], a[192:195], v[76:79]\n"
      "ds_read_b128 a[208:211], v2 offset:128\n"
      "v_fmac_f32 v39, s46, v63\n"
      "v_mfma_f32_16x16x32_bf16 v[80:83], v[100:103], a[192:195], v[80:83]\n"
      "ds_read_b64_tr_b16 a[220:221], v7 offset:52736\n"
      "ds_read_b64_tr_b16 a[222:223], v7 offset:52744\n"
      "v_sub_f32 v36, v36, v48\n"
      "v_mfma_f32_16x16x32_bf16 v[84:87], a[160:163], a[192:195], v[84:87]\n"
      "v_sub_f32 v37, v37, v49\n"
      "v_cvt_pk_bf16_f32 v21, v21, v25\n"
      "v_mfma_f32_16x16x32_bf16 v[88:91], v[104:107], a[196:199], v[88:91]\n"
      "ds_read_b128 a[212:215], v2 offset:4224\n"
      "ds_write_addtid_b32 v21 offset:46848\n"
      "v_mfma_f32_16x16x32_bf16 v[92:95], a[164:167], a[196:199], v[92:95]\n"
      "v_sub_f32 v38, v38, v50\n"
      "v_sub_f32 v39, v39, v51\n"
      "v_mfma_f32_16x16x32_bf16 v[96:99], a[156:159], a[196:199], v[96:99]\n"
      "s_waitcnt lgkmcnt(3)\n"
      "v_fmac_f32 v48, s45, v36\n"
      "v_cvt_pk_bf16_f32 v22, v22, v26\n"
      "v_mfma_f32_16x16x32_bf16 v[76:79], a[152:155], a[200:203], v[76:79]\n"
      "ds_read_b128 a[192:195], v3 offset:50688\n"
      "ds_write_addtid_b32 v22 offset:50944\n"
      "v_fmac_f32 v49, s45, v37\n"
      "v_mfma_f32_16x16x32_bf16 v[80:83], a[144:147], a[200:203], v[80:83]\n"
      "v_fmac_f32 v50, s45, v38\n"
      "v_cvt_pk_bf16_f32 v23, v23, v27\n"
      "v_mfma_f32_16x16x32_bf16 v[84:87], a[136:139], a[200:203], v[84:87]\n"
      "s_waitcnt lgkmcnt(2)\n"
      "ds_write_addtid_b32 v23 offset:55040\n"
      "v_fmac_f32 v51, s45, v39\n"
      "v_mfma_f32_4x4x4_16b_bf16 v[12:15], a[216:217], a[220:221], 0\n"
      "v_fma_f32 v36, s47, v36, v72\n"
      "v_mfma_f32_4x4x4_16b_bf16 v[16:19], a[216:217], a[222:223], 0\n"
      "v_fma_f32 v37, s47, v37, v73\n"
      "v_fma_f32 v38, s47, v38, v74\n"
      "v_mfma_f32_16x16x32_bf16 v[88:91], a[148:151], a[204:207], v[88:91]\n"
      "ds_read_b128 a[196:199], v3 offset:50752\n"
      "v_fma_f32 v39, s47, v39, v75\n"
      "v_cvt_pk_bf16_f32 v12, v12, v16\n"
      "v_mfma_f32_16x16x32_bf16 v[92:95], a[140:143], a[204:207], v[92:95]\n"
      "ds_write_addtid_b32 v12 offset:43264\n"
      "v_cvt_pk_bf16_f32 v13, v13, v17\n"
      "v_mfma_f32_16x16x32_bf16 v[96:99], a[132:135], a[204:207], v[96:99]\n"
      "ds_write_addtid_b32 v13 offset:47360\n"
      "v_cvt_pk_bf16_f32 v48, v48, v49\n"
      "v_mfma_f32_16x16x32_bf16 v[76:79], a[128:131], a[208:211], v[76:79]\n"
      "ds_read_b128 a[200:203], v3 offset:50816\n"
      "ds_write_addtid_b32 v48 offset:17408\n"
      "v_mfma_f32_16x16x32_bf16 v[80:83], a[120:123], a[208:211], v[80:83]\n"
      "ds_read_b64_tr_b16 a[224:225], v7 offset:53248\n"
      "ds_read_b64_tr_b16 a[226:227], v7 offset:53256\n"
      "v_cvt_pk_bf16_f32 v14, v14, v18\n"
      "v_mfma_f32_16x16x32_bf16 v[84:87], a[112:115], a[208:211], v[84:87]\n"
      "ds_write_addtid_b32 v14 offset:51456\n"
      "v_cvt_pk_bf16_f32 v50, v50, v51\n"
      "v_mfma_f32_16x16x32_bf16 v[88:91], a[124:127], a[212:215], v[88:91]\n"
      "ds_read_b128 a[204:207], v4 offset:50688\n"
      "ds_write_addtid_b32 v50 offset:17920\n"
      "v_mfma_f32_16x16x32_bf16 v[92:95], a[116:119], a[212:215], v[92:95]\n"
      "v_cvt_pk_bf16_f32 v36, v36, v37\n"
      "v_cvt_pk_bf16_f32 v15, v15, v19\n"
      "v_mfma_f32_16x16x32_bf16 v[96:99], a[108:111], a[212:215], v[96:99]\n"
      "s_waitcnt lgkmcnt(5)\n"
      "ds_write_addtid_b32 v36 offset:19456\n"
      "ds_write_addtid_b32 v15 offset:55552\n"
      "v_sub_f32 v76, v88, v76\n"
      "v_sub_f32_e64 v77, v89, v77\n"
      "v_mfma_f32_16x16x32_bf16 v[52:55], a[0:3], a[192:195], 0\n"
      "ds_read_b128 a[208:211], v4 offset:50752\n"
      "ds_write_addtid_b32 v76 offset:0\n"
      "v_mfma_f32_16x16x32_bf16 v[56:59], a[4:7], a[192:195], 0\n"
      "s_waitcnt lgkmcnt(6)\n"
      "ds_write_addtid_b32 v77 offset:512\n"
      "v_sub_f32 v78, v90, v78\n"
      "v_mfma_f32_4x4x4_16b_bf16 v[20:23], a[216:217], a[224:225], 0\n"
      "v_sub_f32_e64 v79, v91, v79\n"
      "v_cvt_pk_bf16_f32 v38, v38, v39\n"
      "v_mfma_f32_4x4x4_16b_bf16 v[24:27], a[216:217], a[226:227], 0\n"
      "ds_write_addtid_b32 v38 offset:19968\n"
      "ds_write_addtid_b32 v78 offset:1024\n"
      "v_mfma_f32_16x16x32_bf16 v[60:63], a[8:11], a[192:195], 0\n"
      "ds_write_addtid_b32 v79 offset:1536\n"
      "v_sub_f32 v80, v92, v80\n"
      "v_mfma_f32_16x16x32_bf16 v[52:55], a[12:15], a[196:199], v[52:55]\n"
      "s_nop 0\n"
      "ds_read_b128 a[212:215], v4 offset:50816\n"
      "ds_write_addtid_b32 v80 offset:2048\n"
      "v_sub_f32 v81, v93, v81\n"
      "v_mfma_f32_16x16x32_bf16 v[56:59], a[16:19], a[196:199], v[56:59]\n"
      "ds_write_addtid_b32 v81 offset:2560\n"
      "v_sub_f32 v82, v94, v82\n"
      "v_mfma_f32_16x16x32_bf16 v[60:63], a[20:23], a[196:199], v[60:63]\n"
      "ds_write_addtid_b32 v82 offset:3072\n"
      "v_cvt_pk_bf16_f32 v20, v20, v24\n"
      "v_mfma_f32_16x16x32_bf16 v[52:55], a[24:27], a[200:203], v[52:55]\n"
      "ds_read_b128 a[192:195], v5 offset:50688\n"
      "ds_write_addtid_b32 v20 offset:43776\n"
      "v_mfma_f32_16x16x32_bf16 v[56:59], a[28:31], a[200:203], v[56:59]\n"
      "v_sub_f32 v83, v95, v83\n"
      "v_sub_f32_e64 v84, v96, v84\n"
      "v_cvt_pk_bf16_f32 v21, v21, v25\n"
      "v_mfma_f32_16x16x32_bf16 v[60:63], a[32:35], a[200:203], v[60:63]\n"
      "s_waitcnt lgkmcnt(4)\n"
      "ds_write_addtid_b32 v21 offset:47872\n"
      "ds_write_addtid_b32 v83 offset:3584\n"
      "v_mfma_f32_16x16x32_bf16 v[52:55], a[36:39], a[204:207], v[52:55]\n"
      "ds_read_b128 a[196:199], v5 offset:50752\n"
      "ds_write_addtid_b32 v84 offset:4096\n"
      "v_mfma_f32_16x16x32_bf16 v[56:59], a[40:43], a[204:207], v[56:59]\n"
      "v_sub_f32 v85, v97, v85\n"
      "v_sub_f32 v86, v98, v86\n"
      "v_cvt_pk_bf16_f32 v22, v22, v26\n"
      "v_mfma_f32_16x16x32_bf16 v[60:63], a[44:47], a[204:207], v[60:63]\n"
      "ds_write_addtid_b32 v22 offset:51968\n"
      "ds_write_addtid_b32 v85 offset:4608\n"
      "v_sub_f32 v87, v99, v87\n"
      "v_mfma_f32_16x16x32_bf16 v[52:55], a[48:51], a[208:211], v[52:55]\n"
      "ds_read_b128 a[200:203], v5 offset:50816\n"
      "ds_write_addtid_b32 v86 offset:5120\n"
      "v_mfma_f32_16x16x32_bf16 v[56:59], a[52:55], a[208:211], v[56:59]\n"
      "s_nop 0\n"
      "ds_write_addtid_b32 v87 offset:5632\n"
      "v_cvt_pk_bf16_f32 v23, v23, v27\n"
      "v_mfma_f32_16x16x32_bf16 v[60:63], a[56:59], a[208:211], v[60:63]\n"
      "ds_write_addtid_b32 v23 offset:56064\n"
      "v_fma_f32 v40, 0.5, v76, -v88\n"
      "v_mfma_f32_16x16x32_bf16 v[52:55], a[60:63], a[212:215], v[52:55]\n"
      "v_fma_f32 v41, 0.5, v77, -v89\n"
      "v_fma_f32 v42, 0.5, v78, -v90\n"
      "v_mfma_f32_16x16x32_bf16 v[56:59], a[64:67], a[212:215], v[56:59]\n"
      "v_fma_f32 v43, 0.5, v79, -v91\n"
      "v_fma_f32 v44, 0.5, v80, -v92\n"
      "v_mfma_f32_16x16x32_bf16 v[60:63], a[68:71], a[212:215], v[60:63]\n"
      "s_waitcnt lgkmcnt(0)\n"
      "v_fma_f32 v45, 0.5, v81, -v93\n"
      "s_barrier\n"
      "v_mfma_f32_16x16x32_bf16 v[52:55], a[72:75], a[192:195], v[52:55]\n"
      "s_add_u32 s59, s62, 10420224\n"
      "ds_read_b128 a[204:207], v0 offset:16384\n"
      "ds_read2st64_b32 v[28:29], v6 offset0:0 offset1:2\n"
      "v_fma_f32 v46, 0.5, v82, -v94\n"
      "v_mfma_f32_16x16x32_bf16 v[56:59], a[76:79], a[192:195], v[56:59]\n"
      "ds_read_b128 a[208:211], v0 offset:20480\n"
      "ds_read2st64_b32 v[30:31], v6 offset0:4 offset1:6\n"
      "v_mfma_f32_16x16x32_bf16 v[60:63], a[80:83], a[192:195], v[60:63]\n"
      "ds_read2st64_b32 v[32:33], v6 offset0:8 offset1:10\n"
      "v_fma_f32 v47, 0.5, v83, -v95\n"
      "v_mfma_f32_16x16x32_bf16 v[52:55], a[84:87], a[196:199], v[52:55]\n"
      "ds_read_b128 a[212:215], v0 offset:16448\n"
      "v_fma_f32 v48, 0.5, v84, -v96\n"
      "v_mfma_f32_16x16x32_bf16 v[56:59], a[88:91], a[196:199], v[56:59]\n"
      "ds_read2st64_b32 v[34:35], v6 offset0:12 offset1:14\n"
      "ds_read2st64_b32 v[36:37], v6 offset0:16 offset1:18\n"
      "v_mfma_f32_16x16x32_bf16 v[60:63], a[92:95], a[196:199], v[60:63]\n"
      "s_nop 0\n"
      "v_bfe_u32 v16, v11, 6, 1\n"
      "v_mul_i32_i24 v17, 12296, v16\n"
      "s_mov_b32 s50, -8200\n"
      "v_add3_u32 v12, v8, v17, s50\n"
      "v_add_u32 v14, 128, v12\n"
      "v_mul_i32_i24 v17, 8, v16\n"
      "s_mov_b32 s50, -4104\n"
      "v_add3_u32 v13, v8, v17, s50\n"
      "v_add_u32 v15, 128, v13\n"
      "v_mfma_f32_16x16x32_bf16 v[52:55], a[96:99], a[200:203], v[52:55]\n"
      "ds_read_b128 a[192:195], v0 offset:20544\n"
      "ds_read2st64_b32 a[232:233], v12 offset0:4 offset1:6\n"
      "v_mfma_f32_16x16x32_bf16 v[56:59], a[100:103], a[200:203], v[56:59]\n"
      "ds_read2st64_b32 a[234:235], v14 offset0:4 offset1:6\n"
      "ds_read2st64_b32 v[38:39], v6 offset0:20 offset1:22\n"
      "v_fma_f32 v49, 0.5, v85, -v97\n"
      "v_fma_f32 v50, 0.5, v86, -v98\n"
      "v_fma_f32 v51, 0.5, v87, -v99\n"
      "v_mfma_f32_16x16x32_bf16 v[60:63], a[104:107], a[200:203], v[60:63]\n"
      "s_waitcnt lgkmcnt(6)\n"
      "v_fmac_f32 v28, s46, v64\n"
      "v_fmac_f32 v29, s46, v65\n"
      "v_mfma_f32_16x16x32_bf16 v[76:79], v[252:255], a[204:207], v[52:55]\n"
      "ds_read_b128 a[196:199], v0 offset:16512\n"
      "v_fmac_f32 v30, s46, v66\n"
      "v_mfma_f32_16x16x32_bf16 v[80:83], v[244:247], a[204:207], v[56:59]\n"
      "v_fmac_f32 v31, s46, v67\n"
      "v_sub_f32 v28, v28, v40\n"
      "v_mfma_f32_16x16x32_bf16 v[84:87], v[236:239], a[204:207], v[60:63]\n"
      "v_sub_f32 v29, v29, v41\n"
      "v_sub_f32 v30, v30, v42\n"
      "v_mfma_f32_16x16x32_bf16 v[88:91], v[248:251], a[208:211], v[52:55]\n"
      "ds_read_b128 a[200:203], v0 offset:20608\n"
      "v_sub_f32 v31, v31, v43\n"
      "v_mfma_f32_16x16x32_bf16 v[92:95], v[240:243], a[208:211], v[56:59]\n"
      "v_fmac_f32 v40, s45, v28\n"
      "v_fmac_f32_e64 v41, s45, v29\n"
      "v_mfma_f32_16x16x32_bf16 v[96:99], v[232:235], a[208:211], v[60:63]\n"
      "v_fmac_f32 v42, s45, v30\n"
      "v_fmac_f32 v43, s45, v31\n"
      "v_mfma_f32_16x16x32_bf16 v[76:79], v[228:231], a[212:215], v[76:79]\n"
      "ds_read_b128 a[204:207], v1 offset:16384\n"
      "v_fma_f32 v28, s47, v28, v52\n"
      "v_mfma_f32_16x16x32_bf16 v[80:83], v[220:223], a[212:215], v[80:83]\n"
      "v_fma_f32 v29, s47, v29, v53\n"
      "v_mfma_f32_16x16x32_bf16 v[84:87], v[212:215], a[212:215], v[84:87]\n"
      "s_waitcnt lgkmcnt(1)\n"
      "v_fma_f32 v30, s47, v30, v54\n"
      "v_mfma_f32_16x16x32_bf16 v[88:91], v[224:227], a[192:195], v[88:91]\n"
      "ds_read_b128 a[208:211], v1 offset:20480\n"
      "v_fma_f32 v31, s47, v31, v55\n"
      "v_mfma_f32_16x16x32_bf16 v[92:95], v[216:219], a[192:195], v[92:95]\n"
      "v_cvt_pk_bf16_f32 v40, v40, v41\n"
      "v_mfma_f32_16x16x32_bf16 v[96:99], v[208:211], a[192:195], v[96:99]\n"
      "s_nop 0\n"
      "buffer_store_dwordx4 a[232:235], v11, s[8:11], s59 offen offset:0 sc1\n"
      "v_mfma_f32_16x16x32_bf16 v[76:79], v[204:207], a[196:199], v[76:79]\n"
      "ds_read_b128 a[212:215], v1 offset:16448\n"
      "ds_write_addtid_b32 v40 offset:12288\n"
      "v_mfma_f32_16x16x32_bf16 v[80:83], v[196:199], a[196:199], v[80:83]\n"
      "ds_read2st64_b32 a[236:237], v13 offset0:4 offset1:6\n"
      "ds_read2st64_b32 a[238:239], v15 offset0:4 offset1:6\n"
      "v_mfma_f32_16x16x32_bf16 v[84:87], v[188:191], a[196:199], v[84:87]\n"
      "v_cvt_pk_bf16_f32 v42, v42, v43\n"
      "v_cvt_pk_bf16_f32 v28, v28, v29\n"
      "v_mfma_f32_16x16x32_bf16 v[88:91], v[200:203], a[200:203], v[88:91]\n"
      "ds_read_b128 a[192:195], v1 offset:20544\n"
      "ds_write_addtid_b32 v42 offset:12800\n"
      "v_mfma_f32_16x16x32_bf16 v[92:95], v[192:195], a[200:203], v[92:95]\n"
      "ds_write_addtid_b32 v28 offset:14336\n"
      "v_cvt_pk_bf16_f32 v30, v30, v31\n"
      "v_mfma_f32_16x16x32_bf16 v[96:99], v[184:187], a[200:203], v[96:99]\n"
      "s_waitcnt lgkmcnt(6)\n"
      "ds_write_addtid_b32 v30 offset:14848\n"
      "v_fmac_f32 v32, s46, v68\n"
      "v_mfma_f32_16x16x32_bf16 v[76:79], v[180:183], a[204:207], v[76:79]\n"
      "ds_read_b128 a[196:199], v1 offset:16512\n"
      "v_fmac_f32 v33, s46, v69\n"
      "v_mfma_f32_16x16x32_bf16 v[80:83], v[172:175], a[204:207], v[80:83]\n"
      "v_fmac_f32 v34, s46, v70\n"
      "v_fmac_f32_e64 v35, s46, v71\n"
      "v_mfma_f32_16x16x32_bf16 v[84:87], v[164:167], a[204:207], v[84:87]\n"
      "v_sub_f32 v32, v32, v44\n"
      "v_sub_f32 v33, v33, v45\n"
      "v_mfma_f32_16x16x32_bf16 v[88:91], v[176:179], a[208:211], v[88:91]\n"
      "ds_read_b128 a[200:203], v1 offset:20608\n"
      "v_sub_f32 v34, v34, v46\n"
      "v_mfma_f32_16x16x32_bf16 v[92:95], v[168:171], a[208:211], v[92:95]\n"
      "v_sub_f32 v35, v35, v47\n"
      "v_fmac_f32 v44, s45, v32\n"
      "v_mfma_f32_16x16x32_bf16 v[96:99], v[160:163], a[208:211], v[96:99]\n"
      "v_fmac_f32 v45, s45, v33\n"
      "v_fmac_f32 v46, s45, v34\n"
      "v_mfma_f32_16x16x32_bf16 v[76:79], v[156:159], a[212:215], v[76:79]\n"
      "ds_read_b128 a[204:207], v2 offset:16384\n"
      "v_fmac_f32 v47, s45, v35\n"
      "v_mfma_f32_16x16x32_bf16 v[80:83], v[148:151], a[212:215], v[80:83]\n"
      "s_nop 0\n"
      "v_fma_f32 v32, s47, v32, v56\n"
      "v_fma_f32 v33, s47, v33, v57\n"
      "v_mfma_f32_16x16x32_bf16 v[84:87], v[140:143], a[212:215], v[84:87]\n"
      "s_waitcnt lgkmcnt(1)\n"
      "v_fma_f32 v34, s47, v34, v58\n"
      "v_fma_f32 v35, s47, v35, v59\n"
      "v_mfma_f32_16x16x32_bf16 v[88:91], v[152:155], a[192:195], v[88:91]\n"
      "buffer_store_dwordx4 a[236:239], v11, s[8:11], s59 offen offset:128 sc1\n"
      "ds_read_b128 a[208:211], v2 offset:20480\n"
      "v_mfma_f32_16x16x32_bf16 v[92:95], v[144:147], a[192:195], v[92:95]\n"
      "v_cvt_pk_bf16_f32 v44, v44, v45\n"
      "v_cvt_pk_bf16_f32 v46, v46, v47\n"
      "v_mfma_f32_16x16x32_bf16 v[96:99], v[136:139], a[192:195], v[96:99]\n"
      "ds_write_addtid_b32 v44 offset:20484\n"
      "ds_read2st64_b32 a[240:241], v8 offset0:4 offset1:6\n"
      "v_mfma_f32_16x16x32_bf16 v[76:79], v[132:135], a[196:199], v[76:79]\n"
      "ds_read_b128 a[212:215], v2 offset:16448\n"
      "ds_write_addtid_b32 v46 offset:20996\n"
      "v_mfma_f32_16x16x32_bf16 v[80:83], v[124:127], a[196:199], v[80:83]\n"
      "ds_read2st64_b32 a[242:243], v9 offset0:4 offset1:6\n"
      "v_cvt_pk_bf16_f32 v32, v32, v33\n"
      "v_mfma_f32_16x16x32_bf16 v[84:87], v[116:119], a[196:199], v[84:87]\n"
      "ds_write_addtid_b32 v32 offset:22532\n"
      "v_cvt_pk_bf16_f32 v34, v34, v35\n"
      "v_mfma_f32_16x16x32_bf16 v[88:91], v[128:131], a[200:203], v[88:91]\n"
      "ds_read_b128 a[192:195], v2 offset:20544\n"
      "ds_write_addtid_b32 v34 offset:23044\n"
      "v_mfma_f32_16x16x32_bf16 v[92:95], v[120:123], a[200:203], v[92:95]\n"
      "v_fmac_f32 v36, s46, v72\n"
      "v_fmac_f32 v37, s46, v73\n"
      "v_mfma_f32_16x16x32_bf16 v[96:99], v[112:115], a[200:203], v[96:99]\n"
      "s_waitcnt lgkmcnt(5)\n"
      "v_fmac_f32 v38, s46, v74\n"
      "v_fmac_f32 v39, s46, v75\n"
      "v_mfma_f32_16x16x32_bf16 v[76:79], v[108:111], a[204:207], v[76:79]\n"
      "ds_read_b128 a[196:199], v2 offset:16512\n"
      "v_sub_f32 v36, v36, v48\n"
      "v_mfma_f32_16x16x32_bf16 v[80:83], v[100:103], a[204:207], v[80:83]\n"
      "v_sub_f32 v37, v37, v49\n"
      "v_sub_f32 v38, v38, v50\n"
      "v_mfma_f32_16x16x32_bf16 v[84:87], a[160:163], a[204:207], v[84:87]\n"
      "v_sub_f32 v39, v39, v51\n"
      "v_fmac_f32 v48, s45, v36\n"
      "v_mfma_f32_16x16x32_bf16 v[88:91], v[104:107], a[208:211], v[88:91]\n"
      "ds_read_b128 a[200:203], v2 offset:20608\n"
      "v_fmac_f32 v49, s45, v37\n"
      "v_mfma_f32_16x16x32_bf16 v[92:95], a[164:167], a[208:211], v[92:95]\n"
      "v_fmac_f32 v50, s45, v38\n"
      "v_fmac_f32 v51, s45, v39\n"
      "v_mfma_f32_16x16x32_bf16 v[96:99], a[156:159], a[208:211], v[96:99]\n"
      "s_nop 0\n"
      "v_fma_f32 v36, s47, v36, v60\n"
      "v_fma_f32 v37, s47, v37, v61\n"
      "v_mfma_f32_16x16x32_bf16 v[76:79], a[152:155], a[212:215], v[76:79]\n"
      "s_waitcnt lgkmcnt(1)\n"
      "ds_read_b128 a[204:207], v3 offset:0\n"
      "v_fma_f32 v38, s47, v38, v62\n"
      "v_mfma_f32_16x16x32_bf16 v[80:83], a[144:147], a[212:215], v[80:83]\n"
      "s_nop 0\n"
      "buffer_store_dwordx4 a[240:243], v11, s[8:11], s59 offen offset:256 sc1\n"
      "v_mfma_f32_16x16x32_bf16 v[84:87], a[136:139], a[212:215], v[84:87]\n"
      "v_fma_f32 v39, s47, v39, v63\n"
      "v_cvt_pk_bf16_f32 v48, v48, v49\n"
      "v_mfma_f32_16x16x32_bf16 v[88:91], a[148:151], a[192:195], v[88:91]\n"
      "ds_read_b128 a[208:211], v3 offset:64\n"
      "ds_write_addtid_b32 v48 offset:16384\n"
      "v_mfma_f32_16x16x32_bf16 v[92:95], a[140:143], a[192:195], v[92:95]\n"
      "v_cvt_pk_bf16_f32 v50, v50, v51\n"
      "v_cvt_pk_bf16_f32 v36, v36, v37\n"
      "v_mfma_f32_16x16x32_bf16 v[96:99], a[132:135], a[192:195], v[96:99]\n"
      "ds_write_addtid_b32 v50 offset:16896\n"
      "ds_write_addtid_b32 v36 offset:18432\n"
      "v_mfma_f32_16x16x32_bf16 v[76:79], a[128:131], a[196:199], v[76:79]\n"
      "ds_read_b128 a[212:215], v3 offset:128\n"
      "v_cvt_pk_bf16_f32 v38, v38, v39\n"
      "v_mfma_f32_16x16x32_bf16 v[80:83], a[120:123], a[196:199], v[80:83]\n"
      "ds_write_addtid_b32 v38 offset:18944\n"
      "v_mfma_f32_16x16x32_bf16 v[84:87], a[112:115], a[196:199], v[84:87]\n"
      "s_waitcnt lgkmcnt(4)\n"
      "v_mfma_f32_16x16x32_bf16 v[88:91], a[124:127], a[200:203], v[88:91]\n"
      "ds_read_b128 a[192:195], v4 offset:0\n"
      "v_mfma_f32_16x16x32_bf16 v[92:95], a[116:119], a[200:203], v[92:95]\n"
      "v_mfma_f32_16x16x32_bf16 v[96:99], a[108:111], a[200:203], v[96:99]\n"
      "v_mfma_f32_16x16x32_bf16 v[64:67], a[0:3], a[204:207], 0\n"
      "s_nop 0\n"
      "ds_read_b128 a[196:199], v4 offset:64\n"
      "v_mfma_f32_16x16x32_bf16 v[68:71], a[4:7], a[204:207], 0\n"
      "v_mfma_f32_16x16x32_bf16 v[72:75], a[8:11], a[204:207], 0\n"
      "v_mfma_f32_16x16x32_bf16 v[64:67], a[12:15], a[208:211], v[64:67]\n"
      "ds_read_b128 a[200:203], v4 offset:128\n"
      "v_sub_f32 v76, v88, v76\n"
      "v_sub_f32 v77, v89, v77\n"
      "v_sub_f32 v78, v90, v78\n"
      "v_sub_f32 v79, v91, v79\n"
      "v_sub_f32 v80, v92, v80\n"
      "v_sub_f32 v81, v93, v81\n"
      "v_mfma_f32_16x16x32_bf16 v[68:71], a[16:19], a[208:211], v[68:71]\n"
      "ds_write_addtid_b32 v76 offset:6144\n"
      "ds_write_addtid_b32 v77 offset:6656\n"
      "ds_write_addtid_b32 v78 offset:7168\n"
      "v_sub_f32 v82, v94, v82\n"
      "v_sub_f32 v83, v95, v83\n"
      "v_sub_f32 v84, v96, v84\n"
      "v_sub_f32_e64 v85, v97, v85\n"
      "v_mfma_f32_16x16x32_bf16 v[72:75], a[20:23], a[208:211], v[72:75]\n"
      "s_waitcnt lgkmcnt(3)\n"
      "ds_write_addtid_b32 v79 offset:7680\n"
      "ds_write_addtid_b32 v80 offset:8192\n"
      "ds_write_addtid_b32 v81 offset:8704\n"
      "v_sub_f32 v86, v98, v86\n"
      "v_sub_f32 v87, v99, v87\n"
      "v_mfma_f32_16x16x32_bf16 v[64:67], a[24:27], a[212:215], v[64:67]\n"
      "ds_write_addtid_b32 v82 offset:9216\n"
      "ds_write_addtid_b32 v83 offset:9728\n"
      "ds_write_addtid_b32 v84 offset:10240\n"
      "v_mfma_f32_16x16x32_bf16 v[68:71], a[28:31], a[212:215], v[68:71]\n"
      "ds_write_addtid_b32 v85 offset:10752\n"
      "ds_write_addtid_b32 v86 offset:11264\n"
      "ds_write_addtid_b32 v87 offset:11776\n"
      "v_mfma_f32_16x16x32_bf16 v[72:75], a[32:35], a[212:215], v[72:75]\n"
      "s_waitcnt lgkmcnt(0)\n"
      "s_barrier\n"
      "v_mfma_f32_16x16x32_bf16 v[64:67], a[36:39], a[192:195], v[64:67]\n"
      "s_add_u32 s59, s59, 786432\n"
      "ds_read_b128 a[204:207], v5 offset:0\n"
      "ds_read2st64_b32 v[28:29], v6 offset0:24 offset1:26\n"
      "v_mfma_f32_16x16x32_bf16 v[68:71], a[40:43], a[192:195], v[68:71]\n"
      "ds_read2st64_b32 v[30:31], v6 offset0:28 offset1:30\n"
      "ds_read2st64_b32 v[32:33], v6 offset0:32 offset1:34\n"
      "v_mfma_f32_16x16x32_bf16 v[72:75], a[44:47], a[192:195], v[72:75]\n"
      "ds_read2st64_b32 v[34:35], v6 offset0:36 offset1:38\n"
      "ds_read2st64_b32 v[36:37], v6 offset0:40 offset1:42\n"
      "v_mfma_f32_16x16x32_bf16 v[64:67], a[48:51], a[196:199], v[64:67]\n"
      "ds_read_b128 a[208:211], v5 offset:64\n"
      "ds_read2st64_b32 v[38:39], v6 offset0:44 offset1:46\n"
      "v_mfma_f32_16x16x32_bf16 v[68:71], a[52:55], a[196:199], v[68:71]\n"
      "v_fma_f32 v40, 0.5, v76, -v88\n"
      "v_fma_f32 v41, 0.5, v77, -v89\n"
      "v_mfma_f32_16x16x32_bf16 v[72:75], a[56:59], a[196:199], v[72:75]\n"
      "ds_read2st64_b32 a[232:233], v12 offset0:0 offset1:2\n"
      "ds_read2st64_b32 a[234:235], v14 offset0:0 offset1:2\n"
      "v_mfma_f32_16x16x32_bf16 v[64:67], a[60:63], a[200:203], v[64:67]\n"
      "ds_read_b128 a[212:215], v5 offset:128\n"
      "v_fma_f32 v42, 0.5, v78, -v90\n"
      "v_mfma_f32_16x16x32_bf16 v[68:71], a[64:67], a[200:203], v[68:71]\n"
      "ds_read2st64_b32 a[236:237], v13 offset0:0 offset1:2\n"
      "ds_read2st64_b32 a[238:239], v15 offset0:0 offset1:2\n"
      "v_mfma_f32_16x16x32_bf16 v[72:75], a[68:71], a[200:203], v[72:75]\n"
      "s_waitcnt lgkmcnt(9)\n"
      "v_fma_f32 v43, 0.5, v79, -v91\n"
      "v_fma_f32 v44, 0.5, v80, -v92\n"
      "v_mfma_f32_16x16x32_bf16 v[64:67], a[72:75], a[204:207], v[64:67]\n"
      "s_waitcnt lgkmcnt(3)\n"
      "buffer_store_dwordx4 a[232:235], v11, s[8:11], s59 offen offset:0 sc1\n"
      "v_mfma_f32_16x16x32_bf16 v[68:71], a[76:79], a[204:207], v[68:71]\n"
      "v_fma_f32 v45, 0.5, v81, -v93\n"
      "v_fma_f32 v46, 0.5, v82, -v94\n"
      "v_mfma_f32_16x16x32_bf16 v[72:75], a[80:83], a[204:207], v[72:75]\n"
      "ds_read2st64_b32 a[240:241], v8 offset0:0 offset1:2\n"
      "ds_read2st64_b32 a[242:243], v9 offset0:0 offset1:2\n"
      "v_mfma_f32_16x16x32_bf16 v[64:67], a[84:87], a[208:211], v[64:67]\n"
      "s_waitcnt lgkmcnt(2)\n"
      "buffer_store_dwordx4 a[236:239], v11, s[8:11], s59 offen offset:128 sc1\n"
      "v_mfma_f32_16x16x32_bf16 v[68:71], a[88:91], a[208:211], v[68:71]\n"
      "s_nop 0\n"
      "v_fma_f32 v47, 0.5, v83, -v95\n"
      "v_fma_f32 v48, 0.5, v84, -v96\n"
      "v_mfma_f32_16x16x32_bf16 v[72:75], a[92:95], a[208:211], v[72:75]\n"
      "v_fma_f32 v49, 0.5, v85, -v97\n"
      "v_fma_f32 v50, 0.5, v86, -v98\n"
      "v_mfma_f32_16x16x32_bf16 v[64:67], a[96:99], a[212:215], v[64:67]\n"
      "s_waitcnt lgkmcnt(0)\n"
      "buffer_store_dwordx4 a[240:243], v11, s[8:11], s59 offen offset:256 sc1\n"
      "v_mfma_f32_16x16x32_bf16 v[68:71], a[100:103], a[212:215], v[68:71]\n"
      "s_nop 0\n"
      "v_fma_f32 v51, 0.5, v87, -v99\n"
      "v_fmac_f32 v28, s46, v52\n"
      "v_mfma_f32_16x16x32_bf16 v[72:75], a[104:107], a[212:215], v[72:75]\n"
      "v_fmac_f32 v29, s46, v53\n"
      "v_fmac_f32 v30, s46, v54\n"
      "v_fmac_f32 v31, s46, v55\n"
      "v_sub_f32 v28, v28, v40\n"
      "v_sub_f32 v29, v29, v41\n"
      "v_sub_f32 v30, v30, v42\n"
      "v_sub_f32 v31, v31, v43\n"
      "v_fmac_f32 v40, s45, v28\n"
      "v_fmac_f32 v41, s45, v29\n"
      "v_pk_fma_f32 v[42:43], v[30:31], s[44:45], v[42:43] op_sel:[0,1,0] op_sel_hi:[1,1,1]\n"
      "v_cvt_pk_bf16_f32 v40, v40, v41\n"
      "v_cvt_pk_bf16_f32 v42, v42, v43\n"
      "v_pk_fma_f32 v[32:33], v[56:57], s[46:47], v[32:33] op_sel:[0,0,0] op_sel_hi:[1,0,1]\n"
      "v_pk_fma_f32 v[34:35], v[58:59], s[46:47], v[34:35] op_sel:[0,0,0] op_sel_hi:[1,0,1]\n"
      "v_pk_add_f32 v[32:33], v[32:33], v[44:45] neg_lo:[0,1] neg_hi:[0,1]\n"
      "v_pk_add_f32 v[34:35], v[34:35], v[46:47] neg_lo:[0,1] neg_hi:[0,1]\n"
      "v_pk_fma_f32 v[44:45], v[32:33], s[44:45], v[44:45] op_sel:[0,1,0] op_sel_hi:[1,1,1]\n"
      "v_pk_fma_f32 v[46:47], v[34:35], s[44:45], v[46:47] op_sel:[0,1,0] op_sel_hi:[1,1,1]\n"
      "v_cvt_pk_bf16_f32 v44, v44, v45\n"
      "v_cvt_pk_bf16_f32 v46, v46, v47\n"
      "v_pk_fma_f32 v[36:37], v[60:61], s[46:47], v[36:37] op_sel:[0,0,0] op_sel_hi:[1,0,1]\n"
      "v_pk_fma_f32 v[38:39], v[62:63], s[46:47], v[38:39] op_sel:[0,0,0] op_sel_hi:[1,0,1]\n"
      "v_pk_add_f32 v[36:37], v[36:37], v[48:49] neg_lo:[0,1] neg_hi:[0,1]\n"
      "v_pk_add_f32 v[38:39], v[38:39], v[50:51] neg_lo:[0,1] neg_hi:[0,1]\n"
      "v_pk_fma_f32 v[48:49], v[36:37], s[44:45], v[48:49] op_sel:[0,1,0] op_sel_hi:[1,1,1]\n"
      "v_pk_fma_f32 v[50:51], v[38:39], s[44:45], v[50:51] op_sel:[0,1,0] op_sel_hi:[1,1,1]\n"
      "v_cvt_pk_bf16_f32 v48, v48, v49\n"
      "v_cvt_pk_bf16_f32 v50, v50, v51\n"
      "ds_write_addtid_b32 v40 offset:13312\n"
      "ds_write_addtid_b32 v42 offset:13824\n"
      "ds_write_addtid_b32 v44 offset:21508\n"
      "ds_write_addtid_b32 v46 offset:22020\n"
      "ds_write_addtid_b32 v48 offset:17408\n"
      "ds_write_addtid_b32 v50 offset:17920\n"
      "v_pk_fma_f32 v[28:29], v[28:29], s[46:47], v[64:65] op_sel:[0,1,0] op_sel_hi:[1,1,1]\n"
      "v_pk_fma_f32 v[30:31], v[30:31], s[46:47], v[66:67] op_sel:[0,1,0] op_sel_hi:[1,1,1]\n"
      "v_cvt_pk_bf16_f32 v28, v28, v29\n"
      "v_cvt_pk_bf16_f32 v30, v30, v31\n"
      "v_pk_fma_f32 v[32:33], v[32:33], s[46:47], v[68:69] op_sel:[0,1,0] op_sel_hi:[1,1,1]\n"
      "v_pk_fma_f32 v[34:35], v[34:35], s[46:47], v[70:71] op_sel:[0,1,0] op_sel_hi:[1,1,1]\n"
      "v_cvt_pk_bf16_f32 v32, v32, v33\n"
      "v_cvt_pk_bf16_f32 v34, v34, v35\n"
      "v_pk_fma_f32 v[36:37], v[36:37], s[46:47], v[72:73] op_sel:[0,1,0] op_sel_hi:[1,1,1]\n"
      "v_pk_fma_f32 v[38:39], v[38:39], s[46:47], v[74:75] op_sel:[0,1,0] op_sel_hi:[1,1,1]\n"
      "v_cvt_pk_bf16_f32 v36, v36, v37\n"
      "v_cvt_pk_bf16_f32 v38, v38, v39\n"
      "ds_write_addtid_b32 v28 offset:15360\n"
      "ds_write_addtid_b32 v30 offset:15872\n"
      "ds_write_addtid_b32 v32 offset:23556\n"
      "ds_write_addtid_b32 v34 offset:24068\n"
      "ds_write_addtid_b32 v36 offset:19456\n"
      "ds_write_addtid_b32 v38 offset:19968\n"
      "s_waitcnt lgkmcnt(0)\n"
      "s_barrier\n"
      "ds_read2st64_b32 a[232:233], v12 offset0:4 offset1:6\n"
      "ds_read2st64_b32 a[234:235], v14 offset0:4 offset1:6\n"
      "ds_read2st64_b32 a[236:237], v13 offset0:4 offset1:6\n"
      "ds_read2st64_b32 a[238:239], v15 offset0:4 offset1:6\n"
      "ds_read2st64_b32 a[240:241], v8 offset0:4 offset1:6\n"
      "ds_read2st64_b32 a[242:243], v9 offset0:4 offset1:6\n"
      "s_add_u32 s59, s59, 786432\n"
      "s_waitcnt lgkmcnt(4)\n"
      "buffer_store_dwordx4 a[232:235], v11, s[8:11], s59 offen offset:0 sc1\n"
      "s_waitcnt lgkmcnt(2)\n"
      "buffer_store_dwordx4 a[236:239], v11, s[8:11], s59 offen offset:128 sc1\n"
      "s_waitcnt lgkmcnt(0)\n"
      "buffer_store_dwordx4 a[240:243], v11, s[8:11], s59 offen offset:256 sc1\n"
      "LSend_%=:\n"
      "Lfin_%=:\n"
      "s_waitcnt vmcnt(0) lgkmcnt(0)\n"
      "s_endpgm\n"
      ".rept 512\n"
      "s_nop 0\n"
      ".endr\n"
      ".Lwg_end:\n"
      : "+{v0}"(tid)
      : "{s[0:1]}"(__builtin_amdgcn_kernarg_segment_ptr()), "{s2}"(blockIdx.x)
      : "v1", "v2", "v3", "v4", "v5", "v6", "v7", "v8", "v9", "v10", "v11", "v12", "v13", "v14", "v15", "v16", "v17", "v18", "v19", "v20", "v21", "v22", "v23", "v24", "v25", "v26", "v27", "v28", "v29", "v30", "v31", "v32", "v33", "v34", "v35", "v36", "v37", "v38", "v39", "v40", "v41", "v42", "v43", "v44", "v45", "v46", "v47", "v48", "v49", "v50", "v51", "v52", "v53", "v54", "v55", "v56", "v57", "v58", "v59", "v60", "v61", "v62", "v63", "v64", "v65", "v66", "v67", "v68", "v69", "v70", "v71", "v72", "v73", "v74", "v75", "v76", "v77", "v78", "v79", "v80", "v81", "v82", "v83", "v84", "v85", "v86", "v87", "v88", "v89", "v90", "v91", "v92", "v93", "v94", "v95", "v96", "v97", "v98", "v99", "v100", "v101", "v102", "v103", "v104", "v105", "v106", "v107", "v108", "v109", "v110", "v111", "v112", "v113", "v114", "v115", "v116", "v117", "v118", "v119", "v120", "v121", "v122", "v123", "v124", "v125", "v126", "v127", "v128", "v129", "v130", "v131", "v132", "v133", "v134", "v135", "v136", "v137", "v138", "v139", "v140", "v141", "v142", "v143", "v144", "v145", "v146", "v147", "v148", "v149", "v150", "v151", "v152", "v153", "v154", "v155", "v156", "v157", "v158", "v159", "v160", "v161", "v162", "v163", "v164", "v165", "v166", "v167", "v168", "v169", "v170", "v171", "v172", "v173", "v174", "v175", "v176", "v177", "v178", "v179", "v180", "v181", "v182", "v183", "v184", "v185", "v186", "v187", "v188", "v189", "v190", "v191", "v192", "v193", "v194", "v195", "v196", "v197", "v198", "v199", "v200", "v201", "v202", "v203", "v204", "v205", "v206", "v207", "v208", "v209", "v210", "v211", "v212", "v213", "v214", "v215", "v216", "v217", "v218", "v219", "v220", "v221", "v222", "v223", "v224", "v225", "v226", "v227", "v228", "v229", "v230", "v231", "v232", "v233", "v234", "v235", "v236", "v237", "v238", "v239", "v240", "v241", "v242", "v243", "v244", "v245", "v246", "v247", "v248", "v249", "v250", "v251", "v252", "v253", "v254", "v255", "a0", "a1", "a2", "a3", "a4", "a5", "a6", "a7", "a8", "a9", "a10", "a11", "a12", "a13", "a14", "a15", "a16", "a17", "a18", "a19", "a20", "a21", "a22", "a23", "a24", "a25", "a26", "a27", "a28", "a29", "a30", "a31", "a32", "a33", "a34", "a35", "a36", "a37", "a38", "a39", "a40", "a41", "a42", "a43", "a44", "a45", "a46", "a47", "a48", "a49", "a50", "a51", "a52", "a53", "a54", "a55", "a56", "a57", "a58", "a59", "a60", "a61", "a62", "a63", "a64", "a65", "a66", "a67", "a68", "a69", "a70", "a71", "a72", "a73", "a74", "a75", "a76", "a77", "a78", "a79", "a80", "a81", "a82", "a83", "a84", "a85", "a86", "a87", "a88", "a89", "a90", "a91", "a92", "a93", "a94", "a95", "a96", "a97", "a98", "a99", "a100", "a101", "a102", "a103", "a104", "a105", "a106", "a107", "a108", "a109", "a110", "a111", "a112", "a113", "a114", "a115", "a116", "a117", "a118", "a119", "a120", "a121", "a122", "a123", "a124", "a125", "a126", "a127", "a128", "a129", "a130", "a131", "a132", "a133", "a134", "a135", "a136", "a137", "a138", "a139", "a140", "a141", "a142", "a143", "a144", "a145", "a146", "a147", "a148", "a149", "a150", "a151", "a152", "a153", "a154", "a155", "a156", "a157", "a158", "a159", "a160", "a161", "a162", "a163", "a164", "a165", "a166", "a167", "a168", "a169", "a170", "a171", "a172", "a173", "a174", "a175", "a176", "a177", "a178", "a179", "a180", "a181", "a182", "a183", "a184", "a185", "a186", "a187", "a188", "a189", "a190", "a191", "a192", "a193", "a194", "a195", "a196", "a197", "a198", "a199", "a200", "a201", "a202", "a203", "a204", "a205", "a206", "a207", "a208", "a209", "a210", "a211", "a212", "a213", "a214", "a215", "a216", "a217", "a218", "a219", "a220", "a221", "a222", "a223", "a224", "a225", "a226", "a227", "a228", "a229", "a230", "a231", "a232", "a233", "a234", "a235", "a236", "a237", "a238", "a239", "a240", "a241", "a242", "a243", "a244", "a245", "a246", "a247", "a248", "a249", "a250", "a251", "a252", "a253", "a254", "a255", "s3", "s4", "s5", "s6", "s7", "s8", "s9", "s10", "s11", "s12", "s13", "s14", "s15", "s16", "s17", "s18", "s19", "s20", "s21", "s22", "s23", "s24", "s25", "s26", "s27", "s28", "s29", "s30", "s31", "s33", "s34", "s35", "s36", "s37", "s38", "s39", "s40", "s41", "s42", "s43", "s44", "s45", "s46", "s47", "s48", "s49", "s50", "s51", "s52", "s53", "s54", "s55", "s56", "s57", "s58", "s59", "s60", "s61", "s62", "s63", "s64", "s65", "s66", "s67", "s68", "s69", "s70", "s71", "s72", "s73", "s74", "s75", "s76", "s77", "s78", "s79", "s80", "s81", "s82", "s83", "s84", "s85", "s86", "s87", "s88", "s89", "s90", "s91", "s92", "s93", "s94", "s95", "s96", "s97", "s98", "s99", "scc", "vcc", "memory");
  asm volatile("" :: "v"((unsigned)(uintptr_t)smem) : "memory");
}

extern "C" void kernel_launch(void* const* d_in, const int* in_sizes, int n_in, void* d_out,
                              int out_size, void* d_ws, size_t ws_size, hipStream_t stream) {
  conv_kernel<<<256, 256, 0, stream>>>((const char*)d_in[0], (const char*)d_in[1], (char*)d_out, (char*)d_ws);
}
```

```cpp
#include <hip/hip_runtime.h>
#include <hip/hip_bf16.h>
#include <stdint.h>

__global__ void __launch_bounds__(256, 1)
conv_kernel(const char* __restrict__ x, const char* __restrict__ w, char* __restrict__ out, char* __restrict__ dbg) {
  __shared__ __attribute__((aligned(1024))) char smem[163840];
  unsigned tid = threadIdx.x;
  asm volatile(
      "s_load_dwordx2 s[12:13], s[0:1], 8\n"
      "s_load_dwordx2 s[4:5], s[0:1], 0\n"
      "s_load_dwordx2 s[8:9], s[0:1], 16\n"
      "v_readfirstlane_b32 s16, v0\n"
      "s_getpc_b64 s[26:27]\n"
      ".Lwg_pc:\n"
      "v_and_b32 v37, 3, v0\n"
      "s_lshr_b32 s23, s2, 3\n"
      "s_lshl_b32 s28, s16, 3\n"
      "v_lshlrev_b32 v40, 7, v37\n"
      "s_lshl_b32 s29, s23, 11\n"
      "s_mov_b32 m0, 150016\n"
      "s_add_u32 s28, s28, s29\n"
      "v_and_b32 v36, 63, v0\n"
      "v_add_u32 v40, s28, v40\n"
      "s_lshr_b32 s16, s16, 6\n"
      "v_min_u32 v40, .Lwg_end-.Lwg_pc-4, v40\n"
      "s_and_b32 s18, s16, 1\n"
      "global_load_lds_dword v40, s[26:27]\n"
      "v_lshlrev_b32 v17, 4, v36\n"
      "s_lshr_b32 s17, s16, 1\n"
      "s_bfe_u32 s28, s2, 0x20001\n"
      "s_mul_i32 s19, s28, 12582912\n"
      "s_and_b32 s28, s23, 1\n"
      "s_mul_i32 s20, s28, 98304\n"
      "s_lshr_b32 s28, s23, 1\n"
      "s_mul_i32 s29, s28, 3072\n"
      "s_bitcmp1_b32 s2, 0\n"
      "s_cselect_b32 s28, 49152, 0\n"
      "s_add_u32 s21, s20, s29\n"
      "s_add_u32 s21, s21, s28\n"
      "s_add_u32 s6, s19, 12783616\n"
      "s_min_u32 s6, s6, 50335744\n"
      "s_mov_b32 s7, 0x20000\n"
      "s_mov_b32 s10, 50331648\n"
      "s_mov_b32 s11, 0x20000\n"
      "s_mov_b32 s14, 165888\n"
      "s_mov_b32 s15, 0x20000\n"
      "s_mul_i32 s63, s18, 3072\n"
      "s_add_u32 s54, s19, s20\n"
      "s_sub_u32 s54, s54, 196608\n"
      "s_lshl_b32 s64, s18, 8\n"
      "s_mul_i32 s28, s17, 24832\n"
      "s_add_u32 s64, s64, s28\n"
      "s_mov_b32 s55, 0\n"
      "v_bitop3_b32 v18, v36, v17, 32 bitop3:0x6c\n"
      "v_xor_b32 v19, 32, v17\n"
      "v_bfe_u32 v38, v36, 2, 2\n"
      "v_lshrrev_b32 v39, 4, v36\n"
      "s_cmp_eq_u32 s17, 1\n"
      "s_cbranch_scc1 LBD_%=\n"
      "v_lshlrev_b32 v40, 3, v17\n"
      "s_mul_i32 s53, s18, 2688\n"
      "s_mul_i32 s34, s23, 5184\n"
      "v_min_u32 v40, 2560, v40\n"
      "s_add_u32 s53, s53, s34\n"
      "v_lshlrev_b32 v43, 4, v37\n"
      "s_mul_i32 s26, s17, 0x8080\n"
      "s_sub_u32 s26, 0xbf80, s26\n"
      "s_mov_b32 s27, 0\n"
      "s_lshl_b32 s34, s17, 7\n"
      "s_mov_b32 s29, 0\n"
      "s_sub_u32 s28, s26, s34\n"
      "s_mov_b32 s31, 0\n"
      "s_sub_u32 s30, s28, s34\n"
      "v_lshlrev_b64 v[44:45], v43, s[26:27]\n"
      "v_lshlrev_b64 v[46:47], v43, s[28:29]\n"
      "s_xor_b32 s28, s28, 0x8000\n"
      "v_lshlrev_b64 v[48:49], v43, s[30:31]\n"
      "v_lshlrev_b64 v[50:51], v43, s[28:29]\n"
      "v_accvgpr_write_b32 a244, v44\n"
      "v_accvgpr_write_b32 a245, v45\n"
      "v_accvgpr_write_b32 a246, v46\n"
      "v_accvgpr_write_b32 a247, v47\n"
      "v_accvgpr_write_b32 a248, v48\n"
      "v_accvgpr_write_b32 a249, v49\n"
      "v_accvgpr_write_b32 a250, v50\n"
      "v_accvgpr_write_b32 a251, v51\n"
      "s_waitcnt lgkmcnt(0)\n"
      "s_and_b32 s13, s13, 0xffff\n"
      "buffer_load_dword v40, s[12:15], s53 offen lds\n"
      "s_add_u32 m0, s63, 30976\n"
      "s_add_u32 s52, s63, 55296\n"
      "buffer_load_dwordx4 v17, s[12:15], s52 offen offset:0 lds\n"
      "buffer_load_dwordx4 v18, s[12:15], s52 offen offset:1024 lds\n"
      "buffer_load_dwordx4 v19, s[12:15], s52 offen offset:2048 lds\n"
      "s_add_u32 m0, s63, 140800\n"
      "s_add_u32 s52, s63, 110592\n"
      "buffer_load_dwordx4 v17, s[12:15], s52 offen offset:0 lds\n"
      "buffer_load_dwordx4 v18, s[12:15], s52 offen offset:1024 lds\n"
      "buffer_load_dwordx4 v19, s[12:15], s52 offen offset:2048 lds\n"
      "s_branch LS_%=\n"
      "LBD_%=:\n"
      "s_sub_u32 s26, s21, s20\n"
      "s_lshl_b32 s37, s18, 11\n"
      "s_add_u32 s26, s26, s37\n"
      "s_add_u32 s27, s26, 3712\n"
      "v_add_u32 v44, s27, v17\n"
      "s_cmp_eq_u32 s18, 1\n"
      "s_cselect_b32 s28, 16, 48\n"
      "s_cselect_b32 s29, 0, 32\n"
      "s_cselect_b32 s27, 48, 64\n"
      "v_cmp_le_u32 vcc, s28, v36\n"
      "v_cmp_gt_u32 s[30:31], s29, v36\n"
      "v_xor_b32 v45, 32, v44\n"
      "s_cmp_eq_u32 s26, 0\n"
      "s_cselect_b32 s28, 24, 0\n"
      "v_cndmask_b32 v47, v44, v45, vcc\n"
      "v_cndmask_b32 v46, v44, v45, s[30:31]\n"
      "v_cmp_le_u32 vcc, s28, v36\n"
      "v_cmp_gt_u32 s[30:31], s27, v36\n"
      "s_cmp_eq_u32 s26, 97280\n"
      "s_cselect_b32 s27, 24, 64\n"
      "v_cmp_gt_u32 s[34:35], s27, v36\n"
      "v_subrev_u32 v45, 256, v44\n"
      "v_cndmask_b32 v8, 2.0, v47, vcc\n"
      "v_cndmask_b32 v48, v45, v46, s[30:31]\n"
      "v_cndmask_b32 v9, 2.0, v48, s[34:35]\n"
      "s_add_u32 s34, s19, s21\n"
      "s_cmp_eq_u32 s19, 0\n"
      "s_cselect_b32 s36, 0x80000000, s54\n"
      "s_waitcnt lgkmcnt(0)\n"
      "s_load_dword s44, s[4:5], s34\n"
      "s_and_b32 s13, s13, 0xffff\n"
      "s_add_u32 m0, s63, 24832\n"
      "s_add_u32 s52, s63, 0\n"
      "buffer_load_dwordx4 v17, s[12:15], s52 offen offset:0 lds\n"
      "buffer_load_dwordx4 v18, s[12:15], s52 offen offset:1024 lds\n"
      "buffer_load_dwordx4 v19, s[12:15], s52 offen offset:2048 lds\n"
      "s_add_u32 s4, s4, -4096\n"
      "s_addc_u32 s5, s5, -1\n"
      "s_and_b32 s5, s5, 0xffff\n"
      "s_add_u32 s53, s54, 196608\n"
      "s_add_u32 m0, s37, 82432\n"
      "s_nop 0\n"
      "buffer_load_dwordx4 v8, s[4:7], s53 offen offset:0 nt lds\n"
      "buffer_load_dwordx4 v9, s[4:7], s53 offen offset:1024 nt lds\n"
      "s_add_u32 s53, s54, 393216\n"
      "s_add_u32 m0, s37, 86536\n"
      "s_nop 0\n"
      "buffer_load_dwordx4 v8, s[4:7], s53 offen offset:0 nt lds\n"
      "buffer_load_dwordx4 v9, s[4:7], s53 offen offset:1024 nt lds\n"
      "s_add_u32 s53, s54, 589824\n"
      "s_add_u32 m0, s37, 90760\n"
      "s_nop 0\n"
      "buffer_load_dwordx4 v8, s[4:7], s53 offen offset:0 nt lds\n"
      "buffer_load_dwordx4 v9, s[4:7], s53 offen offset:1024 nt lds\n"
      "s_add_u32 s53, s54, 786432\n"
      "s_add_u32 m0, s37, 95104\n"
      "s_nop 0\n"
      "buffer_load_dwordx4 v8, s[4:7], s53 offen offset:0 nt lds\n"
      "buffer_load_dwordx4 v9, s[4:7], s53 offen offset:1024 nt lds\n"
      "s_add_u32 s53, s36, 0\n"
      "s_add_u32 m0, s37, 150272\n"
      "s_nop 0\n"
      "buffer_load_dwordx4 v8, s[4:7], s53 offen offset:0 nt lds\n"
      "buffer_load_dwordx4 v9, s[4:7], s53 offen offset:1024 nt lds\n"
      "v_lshlrev_b32 v43, 4, v37\n"
      "v_lshlrev_b32 v44, 2, v37\n"
      "v_and_b32 v43, 16, v43\n"
      "v_and_or_b32 v43, v44, 8, v43\n"
      "v_mul_u32_u24 v44, 192, v38\n"
      "v_mul_u32_u24 v45, 1536, v39\n"
      "s_lshl_b32 s27, s18, 5\n"
      "v_lshlrev_b32 v46, 1, v36\n"
      "v_add3_u32 v43, v43, v44, v45\n"
      "v_bitop3_b32 v46, v46, s27, 32 bitop3:0x6c\n"
      "v_add_u32 v60, v43, v46\n"
      "v_add_u32 v61, 57344, v60\n"
      "v_add_u32 v62, 114688, v60\n"
      "v_lshlrev_b32 v43, 4, v37\n"
      "s_mul_i32 s26, s17, 0x8080\n"
      "s_sub_u32 s26, 0xbf80, s26\n"
      "s_mov_b32 s27, 0\n"
      "s_lshl_b32 s34, s17, 7\n"
      "s_mov_b32 s29, 0\n"
      "s_sub_u32 s28, s26, s34\n"
      "s_mov_b32 s31, 0\n"
      "s_sub_u32 s30, s28, s34\n"
      "v_lshlrev_b64 v[44:45], v43, s[26:27]\n"
      "v_lshlrev_b64 v[46:47], v43, s[28:29]\n"
      "s_xor_b32 s28, s28, 0x8000\n"
      "v_lshlrev_b64 v[48:49], v43, s[30:31]\n"
      "v_lshlrev_b64 v[50:51], v43, s[28:29]\n"
      "v_accvgpr_write_b32 a244, v44\n"
      "v_accvgpr_write_b32 a245, v45\n"
      "v_accvgpr_write_b32 a246, v46\n"
      "v_accvgpr_write_b32 a247, v47\n"
      "v_accvgpr_write_b32 a248, v48\n"
      "v_accvgpr_write_b32 a249, v49\n"
      "v_accvgpr_write_b32 a250, v50\n"
      "v_accvgpr_write_b32 a251, v51\n"
      "s_lshl_b32 s26, s18, 3\n"
      "v_and_b32 v42, 15, v36\n"
      "v_xor_b32 v42, s26, v42\n"
      "s_lshl_b32 s26, s17, 13\n"
      "s_add_u32 s26, s26, 24832\n"
      "s_mul_i32 s27, s17, 12672\n"
      "v_mul_u32_u24 v44, 192, v42\n"
      "v_lshlrev_b32 v43, 3, v42\n"
      "v_and_b32 v45, 48, v36\n"
      "s_add_u32 s27, s27, 82432\n"
      "v_bitop3_b32 v46, v43, v45, 32 bitop3:0x6c\n"
      "v_add3_u32 v0, v46, v44, s26\n"
      "v_add3_u32 v3, v46, v44, s27\n"
      "v_add_u32 v43, 16, v43\n"
      "v_add_u32 v44, 384, v44\n"
      "v_bitop3_b32 v46, v43, v45, 32 bitop3:0x6c\n"
      "v_add3_u32 v1, v46, v44, s26\n"
      "v_add3_u32 v4, v46, v44, s27\n"
      "v_add_u32 v43, 16, v43\n"
      "v_add_u32 v44, 384, v44\n"
      "v_bitop3_b32 v46, v43, v45, 32 bitop3:0x6c\n"
      "v_add3_u32 v2, v46, v44, s26\n"
      "v_add3_u32 v5, v46, v44, s27\n"
      "v_lshlrev_b32 v43, 31, v36\n"
      "v_and_b32 v44, 2, v36\n"
      "v_or_b32 v43, 0x3f803f80, v43\n"
      "v_mul_u32_u24 v45, 0xc00080, v44\n"
      "v_lshl_add_u32 v44, v44, 22, v43\n"
      "v_add_u32 v45, v45, v43\n"
      "v_accvgpr_write_b32 a216, v44\n"
      "v_accvgpr_write_b32 a217, v45\n"
      "v_lshlrev_b32 v43, 3, v38\n"
      "s_mov_b32 s26, 806420736\n"
      "v_bfe_u32 v43, s26, v43, 8\n"
      "v_lshlrev_b32 v44, 12, v38\n"
      "s_lshl_b32 s28, s18, 8\n"
      "s_add_u32 s28, s28, 82432\n"
      "v_lshl_add_u32 v43, v43, 3, v44\n"
      "v_lshl_add_u32 v44, v39, 2, v37\n"
      "v_lshl_add_u32 v43, v44, 4, v43\n"
      "v_add_u32 v7, s28, v43\n"
      "s_mul_i32 s26, s17, 70144\n"
      "s_lshl_b32 s27, s18, 8\n"
      "s_sub_u32 s26, 70144, s26\n"
      "s_add_u32 s26, s26, s27\n"
      "v_lshl_add_u32 v6, v36, 2, s26\n"
      "v_lshrrev_b32 v43, 3, v36\n"
      "v_and_b32 v44, 7, v36\n"
      "v_mul_u32_u24 v45, 384, v43\n"
      "v_lshl_add_u32 v16, v44, 4, v45\n"
      "v_and_b32 v44, 2, v36\n"
      "v_lshlrev_b32 v43, 3, v43\n"
      "v_lshlrev_b32 v45, 4, v44\n"
      "v_xor_b32 v43, v43, v45\n"
      "v_lshl_add_u32 v43, v44, 7, v43\n"
      "v_and_b32 v44, 1, v36\n"
      "s_lshl_b32 s26, s18, 11\n"
      "v_lshl_add_u32 v43, v44, 6, v43\n"
      "s_add_u32 s26, s26, 12288\n"
      "v_bfe_u32 v44, v36, 2, 1\n"
      "v_add_u32 v43, s26, v43\n"
      "v_mul_i32_i24 v45, 8196, v44\n"
      "s_mov_b32 s27, 0\n"
      "v_add3_u32 v10, v43, v45, s27\n"
      "v_add_u32 v13, 128, v10\n"
      "v_mul_i32_i24 v45, -4092, v44\n"
      "s_mov_b32 s27, 4096\n"
      "v_add3_u32 v11, v43, v45, s27\n"
      "v_add_u32 v14, 128, v11\n"
      "v_mul_i32_i24 v45, -4100, v44\n"
      "s_mov_b32 s27, 8200\n"
      "v_add3_u32 v12, v43, v45, s27\n"
      "v_add_u32 v15, 128, v12\n"
      "s_mul_i32 s62, s18, 393216\n"
      "s_add_u32 s62, s62, s19\n"
      "s_add_u32 s62, s62, s21\n"
      "s_mov_b32 s40, 0x80000000\n"
      "s_and_b32 s9, s9, 0xffff\n"
      "s_mov_b32 s45, 0xbf2aaaab\n"
      "s_mov_b32 s46, 0xbe800000\n"
      "s_mov_b32 s47, 0x3f2aaaab\n"
      "s_mov_b32 s48, 0xc0c00000\n"
      "s_mov_b32 s49, 0x3f000000\n"
      "s_add_u32 m0, s63, 0\n"
      "s_add_u32 s52, s63, 6144\n"
      "buffer_load_dwordx4 v17, s[12:15], s52 offen offset:0 lds\n"
      "buffer_load_dwordx4 v18, s[12:15], s52 offen offset:1024 lds\n"
      "buffer_load_dwordx4 v19, s[12:15], s52 offen offset:2048 lds\n"
      "LDpro_%=:\n"
      "s_waitcnt vmcnt(13)\n"
      "s_barrier\n"
      "ds_read_b64_tr_b16 a[0:1], v60 offset:24832\n"
      "ds_read_b64_tr_b16 a[2:3], v60 offset:25600\n"
      "ds_read_b64_tr_b16 a[224:225], v62 offset:26112\n"
      "ds_read_b64_tr_b16 a[226:227], v62 offset:26880\n"
      "ds_read_b64_tr_b16 a[220:221], v60 offset:30976\n"
      "ds_read_b64_tr_b16 a[222:223], v60 offset:31744\n"
      "ds_read_b64_tr_b16 a[4:5], v60 offset:24896\n"
      "ds_read_b64_tr_b16 a[6:7], v60 offset:25664\n"
      "ds_read_b64_tr_b16 a[232:233], v62 offset:26176\n"
      "ds_read_b64_tr_b16 a[234:235], v62 offset:26944\n"
      "ds_read_b64_tr_b16 a[228:229], v60 offset:31040\n"
      "ds_read_b64_tr_b16 a[230:231], v60 offset:31808\n"
      "s_add_u32 m0, s63, 63744\n"
      "s_add_u32 s52, s63, 122880\n"
      "s_waitcnt lgkmcnt(10)\n"
      "v_mfma_f32_4x4x4_16b_bf16 v[28:31], a[244:245], a[0:1], 0\n"
      "v_mfma_f32_4x4x4_16b_bf16 v[32:35], a[244:245], a[2:3], 0\n"
      "buffer_load_dwordx4 v17, s[12:15], s52 offen offset:0 lds\n"
      "s_waitcnt lgkmcnt(8)\n"
      "v_mfma_f32_4x4x4_16b_bf16 v[28:31], a[248:249], a[224:225], v[28:31]\n"
      "ds_read_b64_tr_b16 a[8:9], v60 offset:24960\n"
      "v_mfma_f32_4x4x4_16b_bf16 v[32:35], a[248:249], a[226:227], v[32:35]\n"
      "buffer_load_dwordx4 v18, s[12:15], s52 offen offset:1024 lds\n"
      "s_waitcnt lgkmcnt(7)\n"
      "v_mfma_f32_4x4x4_16b_bf16 v[36:39], a[246:247], a[220:221], v[28:31]\n"
      "ds_read_b64_tr_b16 a[10:11], v60 offset:25728\n"
      "v_mfma_f32_4x4x4_16b_bf16 v[40:43], a[246:247], a[222:223], v[32:35]\n"
      "buffer_load_dwordx4 v19, s[12:15], s52 offen offset:2048 lds\n"
      "v_mfma_f32_4x4x4_16b_bf16 v[28:31], a[250:251], a[220:221], v[28:31]\n"
      "ds_read_b64_tr_b16 a[240:241], v62 offset:26240\n"
      "v_mfma_f32_4x4x4_16b_bf16 v[32:35], a[250:251], a[222:223], v[32:35]\n"
      "ds_read_b64_tr_b16 a[242:243], v62 offset:27008\n"
      "ds_read_b64_tr_b16 a[236:237], v60 offset:31104\n"
      "ds_read_b64_tr_b16 a[238:239], v60 offset:31872\n"
      "s_add_u32 m0, s63, 154368\n"
      "s_add_u32 s52, s63, 129024\n"
      "s_nop 0\n"
      "s_waitcnt lgkmcnt(5)\n"
      "v_mfma_f32_4x4x4_16b_bf16 v[44:47], a[244:245], a[4:5], 0\n"
      "v_mfma_f32_4x4x4_16b_bf16 v[48:51], a[244:245], a[6:7], 0\n"
      "buffer_load_dwordx4 v17, s[12:15], s52 offen offset:0 lds\n"
      "v_mfma_f32_4x4x4_16b_bf16 v[44:47], a[248:249], a[232:233], v[44:47]\n"
      "buffer_load_dwordx4 v18, s[12:15], s52 offen offset:1024 lds\n"
      "v_mfma_f32_4x4x4_16b_bf16 v[48:51], a[248:249], a[234:235], v[48:51]\n"
      "buffer_load_dwordx4 v19, s[12:15], s52 offen offset:2048 lds\n"
      "v_mfma_f32_4x4x4_16b_bf16 v[52:55], a[246:247], a[228:229], v[44:47]\n"
      "v_mfma_f32_4x4x4_16b_bf16 v[56:59], a[246:247], a[230:231], v[48:51]\n"
      "v_mfma_f32_4x4x4_16b_bf16 v[44:47], a[250:251], a[228:229], v[44:47]\n"
      "v_mfma_f32_4x4x4_16b_bf16 v[48:51], a[250:251], a[230:231], v[48:51]\n"
      "v_cvt_pk_bf16_f32 v252, v36, v40\n"
      "v_cvt_pk_bf16_f32 v253, v37, v41\n"
      "v_cvt_pk_bf16_f32 v254, v38, v42\n"
      "v_cvt_pk_bf16_f32 v255, v39, v43\n"
      "v_cvt_pk_bf16_f32 v248, v28, v32\n"
      "v_cvt_pk_bf16_f32 v249, v29, v33\n"
      "v_cvt_pk_bf16_f32 v250, v30, v34\n"
      "v_cvt_pk_bf16_f32 v251, v31, v35\n"
      "s_nop 0\n"
      "s_waitcnt lgkmcnt(0)\n"
      "v_mfma_f32_4x4x4_16b_bf16 v[28:31], a[244:245], a[8:9], 0\n"
      "v_mfma_f32_4x4x4_16b_bf16 v[32:35], a[244:245], a[10:11], 0\n"
      "s_nop 0\n"
      "v_mfma_f32_4x4x4_16b_bf16 v[28:31], a[248:249], a[240:241], v[28:31]\n"
      "v_mfma_f32_4x4x4_16b_bf16 v[32:35], a[248:249], a[242:243], v[32:35]\n"
      "s_nop 0\n"
      "v_mfma_f32_4x4x4_16b_bf16 v[36:39], a[246:247], a[236:237], v[28:31]\n"
      "v_mfma_f32_4x4x4_16b_bf16 v[40:43], a[246:247], a[238:239], v[32:35]\n"
      "v_mfma_f32_4x4x4_16b_bf16 v[28:31], a[250:251], a[236:237], v[28:31]\n"
      "v_mfma_f32_4x4x4_16b_bf16 v[32:35], a[250:251], a[238:239], v[32:35]\n"
      "v_cvt_pk_bf16_f32 v244, v52, v56\n"
      "v_cvt_pk_bf16_f32 v245, v53, v57\n"
      "v_cvt_pk_bf16_f32 v246, v54, v58\n"
      "v_cvt_pk_bf16_f32 v247, v55, v59\n"
      "v_cvt_pk_bf16_f32 v240, v44, v48\n"
      "v_cvt_pk_bf16_f32 v241, v45, v49\n"
      "v_cvt_pk_bf16_f32 v242, v46, v50\n"
      "v_cvt_pk_bf16_f32 v243, v47, v51\n"
      "s_waitcnt vmcnt(6)\n"
      "s_barrier\n"
      "ds_read_b64_tr_b16 a[12:13], v60 offset:0\n"
      "ds_read_b64_tr_b16 a[14:15], v60 offset:768\n"
      "ds_read_b64_tr_b16 a[224:225], v60 offset:12288\n"
      "ds_read_b64_tr_b16 a[226:227], v60 offset:13056\n"
      "ds_read_b64_tr_b16 a[220:221], v60 offset:6144\n"
      "ds_read_b64_tr_b16 a[222:223], v60 offset:6912\n"
      "s_add_u32 m0, s63, 128512\n"
      "s_add_u32 s52, s63, 135168\n"
      "s_waitcnt lgkmcnt(4)\n"
      "v_mfma_f32_4x4x4_16b_bf16 v[44:47], a[244:245], a[12:13], 0\n"
      "v_mfma_f32_4x4x4_16b_bf16 v[48:51], a[244:245], a[14:15], 0\n"
      "buffer_load_dwordx4 v17, s[12:15], s52 offen offset:0 lds\n"
      "s_waitcnt lgkmcnt(2)\n"
      "v_mfma_f32_4x4x4_16b_bf16 v[44:47], a[248:249], a[224:225], v[44:47]\n"
      "ds_read_b64_tr_b16 a[16:17], v60 offset:64\n"
      "v_mfma_f32_4x4x4_16b_bf16 v[48:51], a[248:249], a[226:227], v[48:51]\n"
      "buffer_load_dwordx4 v18, s[12:15], s52 offen offset:1024 lds\n"
      "s_nop 0\n"
      "s_waitcnt lgkmcnt(1)\n"
      "v_mfma_f32_4x4x4_16b_bf16 v[52:55], a[246:247], a[220:221], v[44:47]\n"
      "ds_read_b64_tr_b16 a[18:19], v60 offset:832\n"
      "v_mfma_f32_4x4x4_16b_bf16 v[56:59], a[246:247], a[222:223], v[48:51]\n"
      "buffer_load_dwordx4 v19, s[12:15], s52 offen offset:2048 lds\n"
      "v_mfma_f32_4x4x4_16b_bf16 v[44:47], a[250:251], a[220:221], v[44:47]\n"
      "ds_read_b64_tr_b16 a[232:233], v60 offset:12352\n"
      "v_mfma_f32_4x4x4_16b_bf16 v[48:51], a[250:251], a[222:223], v[48:51]\n"
      "ds_read_b64_tr_b16 a[234:235], v60 offset:13120\n"
      "ds_read_b64_tr_b16 a[228:229], v60 offset:6208\n"
      "ds_read_b64_tr_b16 a[230:231], v60 offset:6976\n"
      "ds_read_b64_tr_b16 a[20:21], v60 offset:128\n"
      "ds_read_b64_tr_b16 a[22:23], v60 offset:896\n"
      "ds_read_b64_tr_b16 a[240:241], v60 offset:12416\n"
      "ds_read_b64_tr_b16 a[242:243], v60 offset:13184\n"
      "ds_read_b64_tr_b16 a[236:237], v60 offset:6272\n"
      "ds_read_b64_tr_b16 a[238:239], v60 offset:7040\n"
      "ds_read_b64_tr_b16 a[168:169], v7 offset:2048\n"
      "ds_read_b64_tr_b16 a[170:171], v7 offset:2056\n"
      "ds_read_b128 a[204:207], v3 offset:55168\n"
      "v_cvt_pk_bf16_f32 v236, v36, v40\n"
      "v_cvt_pk_bf16_f32 v237, v37, v41\n"
      "v_cvt_pk_bf16_f32 v238, v38, v42\n"
      "v_cvt_pk_bf16_f32 v239, v39, v43\n"
      "v_cvt_pk_bf16_f32 v232, v28, v32\n"
      "v_cvt_pk_bf16_f32 v233, v29, v33\n"
      "v_cvt_pk_bf16_f32 v234, v30, v34\n"
      "v_cvt_pk_bf16_f32 v235, v31, v35\n"
      "s_waitcnt vmcnt(6)\n"
      "s_barrier\n"
      "s_nop 0\n"
      "s_waitcnt lgkmcnt(8)\n"
      "v_mfma_f32_4x4x4_16b_bf16 v[28:31], a[244:245], a[16:17], 0\n"
      "v_mfma_f32_4x4x4_16b_bf16 v[32:35], a[244:245], a[18:19], 0\n"
      "ds_read_b64_tr_b16 a[24:25], v60 offset:18432\n"
      "v_mfma_f32_4x4x4_16b_bf16 v[28:31], a[248:249], a[232:233], v[28:31]\n"
      "ds_read_b64_tr_b16 a[26:27], v60 offset:19200\n"
      "v_mfma_f32_4x4x4_16b_bf16 v[32:35], a[248:249], a[234:235], v[32:35]\n"
      "ds_read_b64_tr_b16 a[224:225], v61 offset:6400\n"
      "v_mfma_f32_4x4x4_16b_bf16 v[36:39], a[246:247], a[228:229], v[28:31]\n"
      "ds_read_b64_tr_b16 a[226:227], v61 offset:7168\n"
      "v_mfma_f32_4x4x4_16b_bf16 v[40:43], a[246:247], a[230:231], v[32:35]\n"
      "ds_read_b64_tr_b16 a[220:221], v61 offset:256\n"
      "v_mfma_f32_4x4x4_16b_bf16 v[28:31], a[250:251], a[228:229], v[28:31]\n"
      "ds_read_b64_tr_b16 a[222:223], v61 offset:1024\n"
      "v_mfma_f32_4x4x4_16b_bf16 v[32:35], a[250:251], a[230:231], v[32:35]\n"
      "ds_read_b64_tr_b16 a[172:173], v7 offset:2560\n"
      "s_nop 0\n"
      "s_waitcnt lgkmcnt(7)\n"
      "v_mfma_f32_4x4x4_16b_bf16 v[20:23], a[216:217], a[168:169], 0\n"
      "ds_read_b64_tr_b16 a[174:175], v7 offset:2568\n"
      "v_mfma_f32_4x4x4_16b_bf16 v[24:27], a[216:217], a[170:171], 0\n"
      "ds_read_b128 a[208:211], v3 offset:55232\n"
      "v_cvt_pk_bf16_f32 v228, v52, v56\n"
      "v_cvt_pk_bf16_f32 v229, v53, v57\n"
      "v_cvt_pk_bf16_f32 v230, v54, v58\n"
      "v_cvt_pk_bf16_f32 v231, v55, v59\n"
      "v_cvt_pk_bf16_f32 v224, v44, v48\n"
      "v_cvt_pk_bf16_f32 v225, v45, v49\n"
      "v_cvt_pk_bf16_f32 v226, v46, v50\n"
      "v_cvt_pk_bf16_f32 v227, v47, v51\n"
      "s_add_u32 m0, s63, 134656\n"
      "s_add_u32 s52, s63, 141312\n"
      "v_mfma_f32_4x4x4_16b_bf16 v[44:47], a[244:245], a[20:21], 0\n"
      "v_mfma_f32_4x4x4_16b_bf16 v[48:51], a[244:245], a[22:23], 0\n"
      "buffer_load_dwordx4 v17, s[12:15], s52 offen offset:0 lds\n"
      "v_mfma_f32_4x4x4_16b_bf16 v[44:47], a[248:249], a[240:241], v[44:47]\n"
      "ds_read_b64_tr_b16 a[28:29], v60 offset:18496\n"
      "v_mfma_f32_4x4x4_16b_bf16 v[48:51], a[248:249], a[242:243], v[48:51]\n"
      "buffer_load_dwordx4 v18, s[12:15], s52 offen offset:1024 lds\n"
      "v_mfma_f32_4x4x4_16b_bf16 v[52:55], a[246:247], a[236:237], v[44:47]\n"
      "ds_read_b64_tr_b16 a[30:31], v60 offset:19264\n"
      "v_mfma_f32_4x4x4_16b_bf16 v[56:59], a[246:247], a[238:239], v[48:51]\n"
      "buffer_load_dwordx4 v19, s[12:15], s52 offen offset:2048 lds\n"
      "v_mfma_f32_4x4x4_16b_bf16 v[44:47], a[250:251], a[236:237], v[44:47]\n"
      "ds_read_b64_tr_b16 a[232:233], v61 offset:6464\n"
      "v_mfma_f32_4x4x4_16b_bf16 v[48:51], a[250:251], a[238:239], v[48:51]\n"
      "s_add_u32 s53, s54, 983040\n"
      "s_nop 0\n"
      "s_waitcnt lgkmcnt(3)\n"
      "v_mfma_f32_4x4x4_16b_bf16 v[100:103], a[216:217], a[172:173], 0\n"
      "ds_read_b64_tr_b16 a[234:235], v61 offset:7232\n"
      "v_mfma_f32_4x4x4_16b_bf16 v[104:107], a[216:217], a[174:175], 0\n"
      "s_add_u32 m0, s37, 99328\n"
      "ds_read_b64_tr_b16 a[228:229], v61 offset:320\n"
      "buffer_load_dwordx4 v8, s[4:7], s53 offen offset:0 nt lds\n"
      "buffer_load_dwordx4 v9, s[4:7], s53 offen offset:1024 nt lds\n"
      "ds_read_b64_tr_b16 a[230:231], v61 offset:1088\n"
      "s_add_u32 s53, s54, 1179648\n"
      "ds_read_b64_tr_b16 a[176:177], v7 offset:3072\n"
      "s_add_u32 m0, s37, 103432\n"
      "ds_read_b64_tr_b16 a[178:179], v7 offset:3080\n"
      "buffer_load_dwordx4 v8, s[4:7], s53 offen offset:0 nt lds\n"
      "buffer_load_dwordx4 v9, s[4:7], s53 offen offset:1024 nt lds\n"
      "v_cvt_pk_bf16_f32 v220, v36, v40\n"
      "v_cvt_pk_bf16_f32 v221, v37, v41\n"
      "v_cvt_pk_bf16_f32 v222, v38, v42\n"
      "v_cvt_pk_bf16_f32 v223, v39, v43\n"
      "v_cvt_pk_bf16_f32 v216, v28, v32\n"
      "v_cvt_pk_bf16_f32 v217, v29, v33\n"
      "v_mfma_f32_16x16x32_bf16 v[64:67], a[0:3], a[204:207], 0\n"
      "v_cvt_pk_bf16_f32 v218, v30, v34\n"
      "v_cvt_pk_bf16_f32 v219, v31, v35\n"
      "v_mfma_f32_16x16x32_bf16 v[68:71], a[4:7], a[204:207], 0\n"
      "s_add_u32 m0, s37, 107656\n"
      "v_cvt_pk_bf16_f32 v20, v20, v24\n"
      "v_cvt_pk_bf16_f32 v21, v21, v25\n"
      "v_mfma_f32_16x16x32_bf16 v[72:75], a[8:11], a[204:207], 0\n"
      "s_add_u32 s53, s54, 1376256\n"
      "v_cvt_pk_bf16_f32 v22, v22, v26\n"
      "v_cvt_pk_bf16_f32 v23, v23, v27\n"
      "v_mfma_f32_4x4x4_16b_bf16 v[28:31], a[244:245], a[24:25], 0\n"
      "v_mfma_f32_4x4x4_16b_bf16 v[32:35], a[244:245], a[26:27], 0\n"
      "buffer_load_dwordx4 v8, s[4:7], s53 offen offset:0 nt lds\n"
      "buffer_load_dwordx4 v9, s[4:7], s53 offen offset:1024 nt lds\n"
      "v_mfma_f32_4x4x4_16b_bf16 v[28:31], a[248:249], a[224:225], v[28:31]\n"
      "ds_read_b64_tr_b16 a[32:33], v60 offset:18560\n"
      "v_mfma_f32_4x4x4_16b_bf16 v[32:35], a[248:249], a[226:227], v[32:35]\n"
      "s_add_u32 s53, s54, 1572864\n"
      "v_mfma_f32_4x4x4_16b_bf16 v[36:39], a[246:247], a[220:221], v[28:31]\n"
      "ds_read_b64_tr_b16 a[34:35], v60 offset:19328\n"
      "v_mfma_f32_4x4x4_16b_bf16 v[40:43], a[246:247], a[222:223], v[32:35]\n"
      "s_add_u32 m0, s37, 112000\n"
      "v_mfma_f32_4x4x4_16b_bf16 v[28:31], a[250:251], a[220:221], v[28:31]\n"
      "ds_read_b64_tr_b16 a[240:241], v61 offset:6528\n"
      "v_mfma_f32_4x4x4_16b_bf16 v[32:35], a[250:251], a[222:223], v[32:35]\n"
      "buffer_load_dwordx4 v8, s[4:7], s53 offen offset:0 nt lds\n"
      "buffer_load_dwordx4 v9, s[4:7], s53 offen offset:1024 nt lds\n"
      "s_nop 0\n"
      "s_waitcnt lgkmcnt(3)\n"
      "v_mfma_f32_4x4x4_16b_bf16 v[108:111], a[216:217], a[176:177], 0\n"
      "ds_read_b64_tr_b16 a[242:243], v61 offset:7296\n"
      "v_mfma_f32_4x4x4_16b_bf16 v[112:115], a[216:217], a[178:179], 0\n"
      "ds_read_b64_tr_b16 a[236:237], v61 offset:384\n"
      "ds_read_b64_tr_b16 a[238:239], v61 offset:1152\n"
      "ds_read_b64_tr_b16 a[180:181], v7 offset:3584\n"
      "ds_read_b64_tr_b16 a[182:183], v7 offset:3592\n"
      "s_mov_b32 m0, s64\n"
      "s_nop 0\n"
      "ds_write_addtid_b32 v20 offset:2048\n"
      "ds_write_addtid_b32 v21 offset:6144\n"
      "ds_write_addtid_b32 v22 offset:10240\n"
      "ds_write_addtid_b32 v23 offset:14336\n"
      "v_cvt_pk_bf16_f32 v212, v52, v56\n"
      "v_cvt_pk_bf16_f32 v213, v53, v57\n"
      "v_cvt_pk_bf16_f32 v214, v54, v58\n"
      "v_cvt_pk_bf16_f32 v215, v55, v59\n"
      "v_cvt_pk_bf16_f32 v208, v44, v48\n"
      "v_cvt_pk_bf16_f32 v209, v45, v49\n"
      "v_mfma_f32_16x16x32_bf16 v[64:67], a[12:15], a[208:211], v[64:67]\n"
      "v_cvt_pk_bf16_f32 v210, v46, v50\n"
      "v_cvt_pk_bf16_f32 v211, v47, v51\n"
      "v_mfma_f32_16x16x32_bf16 v[68:71], a[16:19], a[208:211], v[68:71]\n"
      "v_cvt_pk_bf16_f32 v100, v100, v104\n"
      "v_cvt_pk_bf16_f32 v101, v101, v105\n"
      "v_mfma_f32_16x16x32_bf16 v[72:75], a[20:23], a[208:211], v[72:75]\n"
      "s_waitcnt vmcnt(14)\n"
      "v_cvt_pk_bf16_f32 v102, v102, v106\n"
      "v_cvt_pk_bf16_f32 v103, v103, v107\n"
      "s_barrier\n"
      "v_mfma_f32_4x4x4_16b_bf16 v[44:47], a[244:245], a[28:29], 0\n"
      "v_mfma_f32_4x4x4_16b_bf16 v[48:51], a[244:245], a[30:31], 0\n"
      "ds_read_b64_tr_b16 a[36:37], v61 offset:12544\n"
      "v_mfma_f32_4x4x4_16b_bf16 v[44:47], a[248:249], a[232:233], v[44:47]\n"
      "ds_read_b64_tr_b16 a[38:39], v61 offset:13312\n"
      "v_mfma_f32_4x4x4_16b_bf16 v[48:51], a[248:249], a[234:235], v[48:51]\n"
      "ds_read_b64_tr_b16 a[224:225], v62 offset:39680\n"
      "v_mfma_f32_4x4x4_16b_bf16 v[52:55], a[246:247], a[228:229], v[44:47]\n"
      "ds_read_b64_tr_b16 a[226:227], v62 offset:40448\n"
      "v_mfma_f32_4x4x4_16b_bf16 v[56:59], a[246:247], a[230:231], v[48:51]\n"
      "ds_read_b64_tr_b16 a[220:221], v61 offset:18688\n"
      "v_mfma_f32_4x4x4_16b_bf16 v[44:47], a[250:251], a[228:229], v[44:47]\n"
      "ds_read_b64_tr_b16 a[222:223], v61 offset:19456\n"
      "v_mfma_f32_4x4x4_16b_bf16 v[48:51], a[250:251], a[230:231], v[48:51]\n"
      "ds_write_addtid_b32 v100 offset:2560\n"
      "s_nop 0\n"
      "s_waitcnt lgkmcnt(7)\n"
      "v_mfma_f32_4x4x4_16b_bf16 v[116:119], a[216:217], a[180:181], 0\n"
      "ds_write_addtid_b32 v101 offset:6656\n"
      "v_mfma_f32_4x4x4_16b_bf16 v[120:123], a[216:217], a[182:183], 0\n"
      "ds_write_addtid_b32 v102 offset:10752\n"
      "ds_write_addtid_b32 v103 offset:14848\n"
      "ds_read_b128 a[212:215], v3 offset:55296\n"
      "v_cvt_pk_bf16_f32 v204, v36, v40\n"
      "v_cvt_pk_bf16_f32 v205, v37, v41\n"
      "v_cvt_pk_bf16_f32 v206, v38, v42\n"
      "v_cvt_pk_bf16_f32 v207, v39, v43\n"
      "v_cvt_pk_bf16_f32 v200, v28, v32\n"
      "v_cvt_pk_bf16_f32 v201, v29, v33\n"
      "v_cvt_pk_bf16_f32 v202, v30, v34\n"
      "v_cvt_pk_bf16_f32 v203, v31, v35\n"
      "v_cvt_pk_bf16_f32 v108, v108, v112\n"
      "v_cvt_pk_bf16_f32 v109, v109, v113\n"
      "v_cvt_pk_bf16_f32 v110, v110, v114\n"
      "v_cvt_pk_bf16_f32 v111, v111, v115\n"
      "v_mfma_f32_4x4x4_16b_bf16 v[28:31], a[244:245], a[32:33], 0\n"
      "v_mfma_f32_4x4x4_16b_bf16 v[32:35], a[244:245], a[34:35], 0\n"
      "ds_read_b64_tr_b16 a[40:41], v61 offset:12608\n"
      "v_mfma_f32_4x4x4_16b_bf16 v[28:31], a[248:249], a[240:241], v[28:31]\n"
      "ds_read_b64_tr_b16 a[42:43], v61 offset:13376\n"
      "v_mfma_f32_4x4x4_16b_bf16 v[32:35], a[248:249], a[242:243], v[32:35]\n"
      "ds_read_b64_tr_b16 a[232:233], v62 offset:39744\n"
      "v_mfma_f32_4x4x4_16b_bf16 v[36:39], a[246:247], a[236:237], v[28:31]\n"
      "ds_read_b64_tr_b16 a[234:235], v62 offset:40512\n"
      "v_mfma_f32_4x4x4_16b_bf16 v[40:43], a[246:247], a[238:239], v[32:35]\n"
      "ds_read_b64_tr_b16 a[228:229], v61 offset:18752\n"
      "v_mfma_f32_4x4x4_16b_bf16 v[28:31], a[250:251], a[236:237], v[28:31]\n"
      "ds_read_b64_tr_b16 a[230:231], v61 offset:19520\n"
      "v_mfma_f32_4x4x4_16b_bf16 v[32:35], a[250:251], a[238:239], v[32:35]\n"
      "ds_write_addtid_b32 v108 offset:3072\n"
      "ds_write_addtid_b32 v109 offset:7168\n"
      "ds_write_addtid_b32 v110 offset:11264\n"
      "ds_write_addtid_b32 v111 offset:15360\n"
      "v_cvt_pk_bf16_f32 v196, v52, v56\n"
      "v_cvt_pk_bf16_f32 v197, v53, v57\n"
      "v_cvt_pk_bf16_f32 v198, v54, v58\n"
      "v_cvt_pk_bf16_f32 v199, v55, v59\n"
      "v_cvt_pk_bf16_f32 v192, v44, v48\n"
      "v_cvt_pk_bf16_f32 v193, v45, v49\n"
      "v_cvt_pk_bf16_f32 v194, v46, v50\n"
      "v_cvt_pk_bf16_f32 v195, v47, v51\n"
      "v_cvt_pk_bf16_f32 v116, v116, v120\n"
      "v_cvt_pk_bf16_f32 v117, v117, v121\n"
      "v_cvt_pk_bf16_f32 v118, v118, v122\n"
      "v_cvt_pk_bf16_f32 v119, v119, v123\n"
      "s_add_u32 m0, s63, 12288\n"
      "s_add_u32 s52, s63, 147456\n"
      "s_nop 0\n"
      "s_waitcnt lgkmcnt(3)\n"
      "v_mfma_f32_4x4x4_16b_bf16 v[44:47], a[244:245], a[36:37], 0\n"
      "v_mfma_f32_4x4x4_16b_bf16 v[48:51], a[244:245], a[38:39], 0\n"
      "buffer_load_dwordx4 v17, s[12:15], s52 offen offset:0 lds\n"
      "v_mfma_f32_4x4x4_16b_bf16 v[44:47], a[248:249], a[224:225], v[44:47]\n"
      "ds_read_b64_tr_b16 a[44:45], v61 offset:12672\n"
      "v_mfma_f32_4x4x4_16b_bf16 v[48:51], a[248:249], a[226:227], v[48:51]\n"
      "buffer_load_dwordx4 v18, s[12:15], s52 offen offset:1024 lds\n"
      "v_mfma_f32_4x4x4_16b_bf16 v[52:55], a[246:247], a[220:221], v[44:47]\n"
      "ds_read_b64_tr_b16 a[46:47], v61 offset:13440\n"
      "v_mfma_f32_4x4x4_16b_bf16 v[56:59], a[246:247], a[222:223], v[48:51]\n"
      "buffer_load_dwordx4 v19, s[12:15], s52 offen offset:2048 lds\n"
      "v_mfma_f32_4x4x4_16b_bf16 v[44:47], a[250:251], a[220:221], v[44:47]\n"
      "ds_read_b64_tr_b16 a[240:241], v62 offset:39808\n"
      "v_mfma_f32_4x4x4_16b_bf16 v[48:51], a[250:251], a[222:223], v[48:51]\n"
      "ds_read_b64_tr_b16 a[242:243], v62 offset:40576\n"
      "ds_read_b64_tr_b16 a[236:237], v61 offset:18816\n"
      "ds_read_b64_tr_b16 a[238:239], v61 offset:19584\n"
      "s_mov_b32 m0, s64\n"
      "s_nop 0\n"
      "ds_write_addtid_b32 v116 offset:3584\n"
      "ds_write_addtid_b32 v117 offset:7680\n"
      "ds_write_addtid_b32 v118 offset:11776\n"
      "ds_write_addtid_b32 v119 offset:15872\n"
      "v_cvt_pk_bf16_f32 v188, v36, v40\n"
      "v_cvt_pk_bf16_f32 v189, v37, v41\n"
      "v_mfma_f32_16x16x32_bf16 v[64:67], a[24:27], a[212:215], v[64:67]\n"
      "v_cvt_pk_bf16_f32 v190, v38, v42\n"
      "v_cvt_pk_bf16_f32 v191, v39, v43\n"
      "v_mfma_f32_16x16x32_bf16 v[68:71], a[28:31], a[212:215], v[68:71]\n"
      "s_waitcnt vmcnt(14)\n"
      "v_cvt_pk_bf16_f32 v184, v28, v32\n"
      "v_cvt_pk_bf16_f32 v185, v29, v33\n"
      "v_mfma_f32_16x16x32_bf16 v[72:75], a[32:35], a[212:215], v[72:75]\n"
      "s_waitcnt lgkmcnt(0)\n"
      "v_cvt_pk_bf16_f32 v186, v30, v34\n"
      "v_cvt_pk_bf16_f32 v187, v31, v35\n"
      "s_nop 0\n"
      "s_barrier\n"
      "v_mfma_f32_4x4x4_16b_bf16 v[28:31], a[244:245], a[40:41], 0\n"
      "v_mfma_f32_4x4x4_16b_bf16 v[32:35], a[244:245], a[42:43], 0\n"
      "ds_read_b64_tr_b16 a[48:49], v62 offset:1536\n"
      "v_mfma_f32_4x4x4_16b_bf16 v[28:31], a[248:249], a[232:233], v[28:31]\n"
      "ds_read_b64_tr_b16 a[50:51], v62 offset:2304\n"
      "v_mfma_f32_4x4x4_16b_bf16 v[32:35], a[248:249], a[234:235], v[32:35]\n"
      "ds_read_b64_tr_b16 a[224:225], v62 offset:13824\n"
      "v_mfma_f32_4x4x4_16b_bf16 v[36:39], a[246:247], a[228:229], v[28:31]\n"
      "ds_read_b64_tr_b16 a[226:227], v62 offset:14592\n"
      "v_mfma_f32_4x4x4_16b_bf16 v[40:43], a[246:247], a[230:231], v[32:35]\n"
      "ds_read_b64_tr_b16 a[220:221], v62 offset:7680\n"
      "v_mfma_f32_4x4x4_16b_bf16 v[28:31], a[250:251], a[228:229], v[28:31]\n"
      "ds_read_b64_tr_b16 a[222:223], v62 offset:8448\n"
      "v_mfma_f32_4x4x4_16b_bf16 v[32:35], a[250:251], a[230:231], v[32:35]\n"
      "ds_read_b128 a[192:195], v4 offset:55168\n"
      "ds_read_b128 a[196:199], v0 offset:0\n"
      "ds_read_b128 a[200:203], v0 offset:4096\n"
      "v_cvt_pk_bf16_f32 v180, v52, v56\n"
      "v_cvt_pk_bf16_f32 v181, v53, v57\n"
      "v_cvt_pk_bf16_f32 v182, v54, v58\n"
      "v_cvt_pk_bf16_f32 v183, v55, v59\n"
      "v_cvt_pk_bf16_f32 v176, v44, v48\n"
      "v_cvt_pk_bf16_f32 v177, v45, v49\n"
      "v_cvt_pk_bf16_f32 v178, v46, v50\n"
      "v_cvt_pk_bf16_f32 v179, v47, v51\n"
      "v_mfma_f32_4x4x4_16b_bf16 v[44:47], a[244:245], a[44:45], 0\n"
      "v_mfma_f32_4x4x4_16b_bf16 v[48:51], a[244:245], a[46:47], 0\n"
      "ds_read_b64_tr_b16 a[52:53], v62 offset:1600\n"
      "v_mfma_f32_4x4x4_16b_bf16 v[44:47], a[248:249], a[240:241], v[44:47]\n"
      "ds_read_b64_tr_b16 a[54:55], v62 offset:2368\n"
      "v_mfma_f32_4x4x4_16b_bf16 v[48:51], a[248:249], a[242:243], v[48:51]\n"
      "ds_read_b64_tr_b16 a[232:233], v62 offset:13888\n"
      "v_mfma_f32_4x4x4_16b_bf16 v[52:55], a[246:247], a[236:237], v[44:47]\n"
      "ds_read_b64_tr_b16 a[234:235], v62 offset:14656\n"
      "v_mfma_f32_4x4x4_16b_bf16 v[56:59], a[246:247], a[238:239], v[48:51]\n"
      "ds_read_b64_tr_b16 a[228:229], v62 offset:7744\n"
      "v_mfma_f32_4x4x4_16b_bf16 v[44:47], a[250:251], a[236:237], v[44:47]\n"
      "ds_read_b64_tr_b16 a[230:231], v62 offset:8512\n"
      "v_mfma_f32_4x4x4_16b_bf16 v[48:51], a[250:251], a[238:239], v[48:51]\n"
      "ds_read_b128 a[204:207], v0 offset:64\n"
      "ds_read_b128 a[208:211], v0 offset:4160\n"
      "ds_read_b128 a[212:215], v0 offset:128\n"
      "v_cvt_pk_bf16_f32 v172, v36, v40\n"
      "v_cvt_pk_bf16_f32 v173, v37, v41\n"
      "v_cvt_pk_bf16_f32 v174, v38, v42\n"
      "v_cvt_pk_bf16_f32 v175, v39, v43\n"
      "v_cvt_pk_bf16_f32 v168, v28, v32\n"
      "v_cvt_pk_bf16_f32 v169, v29, v33\n"
      "v_cvt_pk_bf16_f32 v170, v30, v34\n"
      "v_cvt_pk_bf16_f32 v171, v31, v35\n"
      "s_add_u32 m0, s63, 63744\n"
      "s_add_u32 s52, s63, 153600\n"
      "s_nop 0\n"
      "s_waitcnt lgkmcnt(4)\n"
      "v_mfma_f32_4x4x4_16b_bf16 v[28:31], a[244:245], a[48:49], 0\n"
      "v_mfma_f32_4x4x4_16b_bf16 v[32:35], a[244:245], a[50:51], 0\n"
      "buffer_load_dwordx4 v17, s[12:15], s52 offen offset:0 lds\n"
      "v_mfma_f32_4x4x4_16b_bf16 v[28:31], a[248:249], a[224:225], v[28:31]\n"
      "ds_read_b64_tr_b16 a[56:57], v62 offset:1664\n"
      "v_mfma_f32_4x4x4_16b_bf16 v[32:35], a[248:249], a[226:227], v[32:35]\n"
      "buffer_load_dwordx4 v18, s[12:15], s52 offen offset:1024 lds\n"
      "v_mfma_f32_4x4x4_16b_bf16 v[36:39], a[246:247], a[220:221], v[28:31]\n"
      "ds_read_b64_tr_b16 a[58:59], v62 offset:2432\n"
      "v_mfma_f32_4x4x4_16b_bf16 v[40:43], a[246:247], a[222:223], v[32:35]\n"
      "buffer_load_dwordx4 v19, s[12:15], s52 offen offset:2048 lds\n"
      "v_mfma_f32_4x4x4_16b_bf16 v[28:31], a[250:251], a[220:221], v[28:31]\n"
      "ds_read_b64_tr_b16 a[240:241], v62 offset:13952\n"
      "v_mfma_f32_4x4x4_16b_bf16 v[32:35], a[250:251], a[222:223], v[32:35]\n"
      "ds_read_b64_tr_b16 a[242:243], v62 offset:14720\n"
      "ds_read_b64_tr_b16 a[236:237], v62 offset:7808\n"
      "ds_read_b64_tr_b16 a[238:239], v62 offset:8576\n"
      "v_mfma_f32_16x16x32_bf16 v[64:67], a[36:39], a[192:195], v[64:67]\n"
      "v_cvt_pk_bf16_f32 v164, v52, v56\n"
      "v_cvt_pk_bf16_f32 v165, v53, v57\n"
      "v_mfma_f32_16x16x32_bf16 v[68:71], a[40:43], a[192:195], v[68:71]\n"
      "v_cvt_pk_bf16_f32 v166, v54, v58\n"
      "v_cvt_pk_bf16_f32 v167, v55, v59\n"
      "v_mfma_f32_16x16x32_bf16 v[72:75], a[44:47], a[192:195], v[72:75]\n"
      "v_cvt_pk_bf16_f32 v160, v44, v48\n"
      "v_cvt_pk_bf16_f32 v161, v45, v49\n"
      "v_mfma_f32_16x16x32_bf16 v[76:79], v[252:255], a[196:199], 0\n"
      "s_waitcnt vmcnt(14)\n"
      "v_cvt_pk_bf16_f32 v162, v46, v50\n"
      "v_cvt_pk_bf16_f32 v163, v47, v51\n"
      "s_barrier\n"
      "s_add_u32 m0, s63, 154368\n"
      "s_add_u32 s52, s63, 159744\n"
      "v_mfma_f32_4x4x4_16b_bf16 v[44:47], a[244:245], a[52:53], 0\n"
      "v_mfma_f32_4x4x4_16b_bf16 v[48:51], a[244:245], a[54:55], 0\n"
      "buffer_load_dwordx4 v17, s[12:15], s52 offen offset:0 lds\n"
      "v_mfma_f32_4x4x4_16b_bf16 v[44:47], a[248:249], a[232:233], v[44:47]\n"
      "ds_read_b64_tr_b16 a[60:61], v60 offset:41216\n"
      "v_mfma_f32_4x4x4_16b_bf16 v[48:51], a[248:249], a[234:235], v[48:51]\n"
      "buffer_load_dwordx4 v18, s[12:15], s52 offen offset:1024 lds\n"
      "s_nop 0\n"
      "s_waitcnt lgkmcnt(1)\n"
      "v_mfma_f32_4x4x4_16b_bf16 v[52:55], a[246:247], a[228:229], v[44:47]\n"
      "ds_read_b64_tr_b16 a[62:63], v60 offset:41984\n"
      "v_mfma_f32_4x4x4_16b_bf16 v[56:59], a[246:247], a[230:231], v[48:51]\n"
      "buffer_load_dwordx4 v19, s[12:15], s52 offen offset:2048 lds\n"
      "v_mfma_f32_4x4x4_16b_bf16 v[44:47], a[250:251], a[228:229], v[44:47]\n"
      "ds_read_b64_tr_b16 a[224:225], v62 offset:19968\n"
      "v_mfma_f32_4x4x4_16b_bf16 v[48:51], a[250:251], a[230:231], v[48:51]\n"
      "ds_read_b64_tr_b16 a[226:227], v62 offset:20736\n"
      "ds_read_b64_tr_b16 a[220:221], v60 offset:47360\n"
      "ds_read_b64_tr_b16 a[222:223], v60 offset:48128\n"
      "ds_read_b128 a[192:195], v0 offset:4224\n"
      "v_mfma_f32_16x16x32_bf16 v[80:83], v[244:247], a[196:199], 0\n"
      "v_cvt_pk_bf16_f32 v156, v36, v40\n"
      "v_cvt_pk_bf16_f32 v157, v37, v41\n"
      "v_mfma_f32_16x16x32_bf16 v[84:87], v[236:239], a[196:199], 0\n"
      "v_cvt_pk_bf16_f32 v158, v38, v42\n"
      "v_cvt_pk_bf16_f32 v159, v39, v43\n"
      "v_mfma_f32_16x16x32_bf16 v[88:91], v[248:251], a[200:203], 0\n"
      "v_cvt_pk_bf16_f32 v152, v28, v32\n"
      "v_cvt_pk_bf16_f32 v153, v29, v33\n"
      "v_mfma_f32_16x16x32_bf16 v[92:95], v[240:243], a[200:203], 0\n"
      "v_cvt_pk_bf16_f32 v154, v30, v34\n"
      "v_cvt_pk_bf16_f32 v155, v31, v35\n"
      "v_mfma_f32_4x4x4_16b_bf16 v[28:31], a[244:245], a[56:57], 0\n"
      "v_mfma_f32_4x4x4_16b_bf16 v[32:35], a[244:245], a[58:59], 0\n"
      "ds_read_b64_tr_b16 a[64:65], v60 offset:41280\n"
      "v_mfma_f32_4x4x4_16b_bf16 v[28:31], a[248:249], a[240:241], v[28:31]\n"
      "ds_read_b64_tr_b16 a[66:67], v60 offset:42048\n"
      "v_mfma_f32_4x4x4_16b_bf16 v[32:35], a[248:249], a[242:243], v[32:35]\n"
      "ds_read_b64_tr_b16 a[232:233], v62 offset:20032\n"
      "v_mfma_f32_4x4x4_16b_bf16 v[36:39], a[246:247], a[236:237], v[28:31]\n"
      "ds_read_b64_tr_b16 a[234:235], v62 offset:20800\n"
      "v_mfma_f32_4x4x4_16b_bf16 v[40:43], a[246:247], a[238:239], v[32:35]\n"
      "ds_read_b64_tr_b16 a[228:229], v60 offset:47424\n"
      "v_mfma_f32_4x4x4_16b_bf16 v[28:31], a[250:251], a[236:237], v[28:31]\n"
      "ds_read_b64_tr_b16 a[230:231], v60 offset:48192\n"
      "v_mfma_f32_4x4x4_16b_bf16 v[32:35], a[250:251], a[238:239], v[32:35]\n"
      "ds_read_b128 a[196:199], v1 offset:0\n"
      "v_mfma_f32_16x16x32_bf16 v[96:99], v[232:235], a[200:203], 0\n"
      "v_cvt_pk_bf16_f32 v148, v52, v56\n"
      "v_cvt_pk_bf16_f32 v149, v53, v57\n"
      "v_mfma_f32_16x16x32_bf16 v[76:79], v[228:231], a[204:207], v[76:79]\n"
      "v_cvt_pk_bf16_f32 v150, v54, v58\n"
      "v_cvt_pk_bf16_f32 v151, v55, v59\n"
      "v_mfma_f32_16x16x32_bf16 v[80:83], v[220:223], a[204:207], v[80:83]\n"
      "s_nop 0\n"
      "v_cvt_pk_bf16_f32 v144, v44, v48\n"
      "v_cvt_pk_bf16_f32 v145, v45, v49\n"
      "v_mfma_f32_16x16x32_bf16 v[84:87], v[212:215], a[204:207], v[84:87]\n"
      "s_waitcnt lgkmcnt(1)\n"
      "v_cvt_pk_bf16_f32 v146, v46, v50\n"
      "v_cvt_pk_bf16_f32 v147, v47, v51\n"
      "v_mfma_f32_4x4x4_16b_bf16 v[44:47], a[244:245], a[60:61], 0\n"
      "v_mfma_f32_4x4x4_16b_bf16 v[48:51], a[244:245], a[62:63], 0\n"
      "ds_read_b64_tr_b16 a[68:69], v60 offset:41344\n"
      "v_mfma_f32_4x4x4_16b_bf16 v[44:47], a[248:249], a[224:225], v[44:47]\n"
      "ds_read_b64_tr_b16 a[70:71], v60 offset:42112\n"
      "v_mfma_f32_4x4x4_16b_bf16 v[48:51], a[248:249], a[226:227], v[48:51]\n"
      "ds_read_b64_tr_b16 a[240:241], v62 offset:20096\n"
      "v_mfma_f32_4x4x4_16b_bf16 v[52:55], a[246:247], a[220:221], v[44:47]\n"
      "ds_read_b64_tr_b16 a[242:243], v62 offset:20864\n"
      "v_mfma_f32_4x4x4_16b_bf16 v[56:59], a[246:247], a[222:223], v[48:51]\n"
      "ds_read_b64_tr_b16 a[236:237], v60 offset:47488\n"
      "v_mfma_f32_4x4x4_16b_bf16 v[44:47], a[250:251], a[220:221], v[44:47]\n"
      "ds_read_b64_tr_b16 a[238:239], v60 offset:48256\n"
      "v_mfma_f32_4x4x4_16b_bf16 v[48:51], a[250:251], a[222:223], v[48:51]\n"
      "ds_read_b128 a[200:203], v1 offset:4096\n"
      "ds_read_b128 a[204:207], v4 offset:55232\n"
      "v_mfma_f32_16x16x32_bf16 v[88:91], v[224:227], a[208:211], v[88:91]\n"
      "v_cvt_pk_bf16_f32 v140, v36, v40\n"
      "v_cvt_pk_bf16_f32 v141, v37, v41\n"
      "v_mfma_f32_16x16x32_bf16 v[92:95], v[216:219], a[208:211], v[92:95]\n"
      "v_cvt_pk_bf16_f32 v142, v38, v42\n"
      "v_cvt_pk_bf16_f32 v143, v39, v43\n"
      "v_mfma_f32_16x16x32_bf16 v[96:99], v[208:211], a[208:211], v[96:99]\n"
      "v_cvt_pk_bf16_f32 v136, v28, v32\n"
      "v_cvt_pk_bf16_f32 v137, v29, v33\n"
      "v_mfma_f32_16x16x32_bf16 v[76:79], v[204:207], a[212:215], v[76:79]\n"
      "s_waitcnt vmcnt(6)\n"
      "v_cvt_pk_bf16_f32 v138, v30, v34\n"
      "v_cvt_pk_bf16_f32 v139, v31, v35\n"
      "s_barrier\n"
      "s_add_u32 s53, s54, 1769472\n"
      "s_add_u32 m0, s37, 116224\n"
      "v_mfma_f32_4x4x4_16b_bf16 v[28:31], a[244:245], a[64:65], 0\n"
      "v_mfma_f32_4x4x4_16b_bf16 v[32:35], a[244:245], a[66:67], 0\n"
      "buffer_load_dwordx4 v8, s[4:7], s53 offen offset:0 nt lds\n"
      "buffer_load_dwordx4 v9, s[4:7], s53 offen offset:1024 nt lds\n"
      "v_mfma_f32_4x4x4_16b_bf16 v[28:31], a[248:249], a[232:233], v[28:31]\n"
      "ds_read_b64_tr_b16 a[72:73], v60 offset:0\n"
      "v_mfma_f32_4x4x4_16b_bf16 v[32:35], a[248:249], a[234:235], v[32:35]\n"
      "ds_read_b64_tr_b16 a[74:75], v60 offset:768\n"
      "v_mfma_f32_4x4x4_16b_bf16 v[36:39], a[246:247], a[228:229], v[28:31]\n"
      "ds_read_b64_tr_b16 a[224:225], v60 offset:12288\n"
      "v_mfma_f32_4x4x4_16b_bf16 v[40:43], a[246:247], a[230:231], v[32:35]\n"
      "ds_read_b64_tr_b16 a[226:227], v60 offset:13056\n"
      "v_mfma_f32_4x4x4_16b_bf16 v[28:31], a[250:251], a[228:229], v[28:31]\n"
      "ds_read_b64_tr_b16 a[220:221], v60 offset:6144\n"
      "v_mfma_f32_4x4x4_16b_bf16 v[32:35], a[250:251], a[230:231], v[32:35]\n"
      "ds_read_b64_tr_b16 a[222:223], v60 offset:6912\n"
      "ds_read_b128 a[208:211], v1 offset:64\n"
      "v_mfma_f32_16x16x32_bf16 v[80:83], v[196:199], a[212:215], v[80:83]\n"
      "v_cvt_pk_bf16_f32 v132, v52, v56\n"
      "v_cvt_pk_bf16_f32 v133, v53, v57\n"
      "v_mfma_f32_16x16x32_bf16 v[84:87], v[188:191], a[212:215], v[84:87]\n"
      "v_cvt_pk_bf16_f32 v134, v54, v58\n"
      "v_cvt_pk_bf16_f32 v135, v55, v59\n"
      "v_mfma_f32_16x16x32_bf16 v[88:91], v[200:203], a[192:195], v[88:91]\n"
      "s_add_u32 m0, s37, 120328\n"
      "v_cvt_pk_bf16_f32 v128, v44, v48\n"
      "v_cvt_pk_bf16_f32 v129, v45, v49\n"
      "v_mfma_f32_16x16x32_bf16 v[92:95], v[192:195], a[192:195], v[92:95]\n"
      "s_add_u32 s53, s54, 1966080\n"
      "v_cvt_pk_bf16_f32 v130, v46, v50\n"
      "v_cvt_pk_bf16_f32 v131, v47, v51\n"
      "s_nop 0\n"
      "s_waitcnt lgkmcnt(1)\n"
      "v_mfma_f32_4x4x4_16b_bf16 v[44:47], a[244:245], a[68:69], 0\n"
      "v_mfma_f32_4x4x4_16b_bf16 v[48:51], a[244:245], a[70:71], 0\n"
      "buffer_load_dwordx4 v8, s[4:7], s53 offen offset:0 nt lds\n"
      "buffer_load_dwordx4 v9, s[4:7], s53 offen offset:1024 nt lds\n"
      "v_mfma_f32_4x4x4_16b_bf16 v[44:47], a[248:249], a[240:241], v[44:47]\n"
      "ds_read_b64_tr_b16 a[76:77], v60 offset:64\n"
      "v_mfma_f32_4x4x4_16b_bf16 v[48:51], a[248:249], a[242:243], v[48:51]\n"
      "ds_read_b64_tr_b16 a[78:79], v60 offset:832\n"
      "v_mfma_f32_4x4x4_16b_bf16 v[52:55], a[246:247], a[236:237], v[44:47]\n"
      "ds_read_b64_tr_b16 a[232:233], v60 offset:12352\n"
      "v_mfma_f32_4x4x4_16b_bf16 v[56:59], a[246:247], a[238:239], v[48:51]\n"
      "ds_read_b64_tr_b16 a[234:235], v60 offset:13120\n"
      "v_mfma_f32_4x4x4_16b_bf16 v[44:47], a[250:251], a[236:237], v[44:47]\n"
      "ds_read_b64_tr_b16 a[228:229], v60 offset:6208\n"
      "v_mfma_f32_4x4x4_16b_bf16 v[48:51], a[250:251], a[238:239], v[48:51]\n"
      "ds_read_b64_tr_b16 a[230:231], v60 offset:6976\n"
      "ds_read_b128 a[212:215], v1 offset:4160\n"
      "v_mfma_f32_16x16x32_bf16 v[96:99], v[184:187], a[192:195], v[96:99]\n"
      "s_nop 0\n"
      "v_cvt_pk_bf16_f32 v124, v36, v40\n"
      "v_cvt_pk_bf16_f32 v125, v37, v41\n"
      "v_mfma_f32_16x16x32_bf16 v[76:79], v[180:183], a[196:199], v[76:79]\n"
      "s_nop 0\n"
      "v_cvt_pk_bf16_f32 v126, v38, v42\n"
      "v_cvt_pk_bf16_f32 v127, v39, v43\n"
      "v_mfma_f32_16x16x32_bf16 v[80:83], v[172:175], a[196:199], v[80:83]\n"
      "s_add_u32 m0, s37, 124552\n"
      "v_cvt_pk_bf16_f32 v120, v28, v32\n"
      "v_cvt_pk_bf16_f32 v121, v29, v33\n"
      "v_mfma_f32_16x16x32_bf16 v[84:87], v[164:167], a[196:199], v[84:87]\n"
      "s_add_u32 s53, s54, 2162688\n"
      "v_cvt_pk_bf16_f32 v122, v30, v34\n"
      "v_cvt_pk_bf16_f32 v123, v31, v35\n"
      "v_mfma_f32_4x4x4_16b_bf16 v[28:31], a[244:245], a[72:73], 0\n"
      "v_mfma_f32_4x4x4_16b_bf16 v[32:35], a[244:245], a[74:75], 0\n"
      "buffer_load_dwordx4 v8, s[4:7], s53 offen offset:0 nt lds\n"
      "buffer_load_dwordx4 v9, s[4:7], s53 offen offset:1024 nt lds\n"
      "v_mfma_f32_4x4x4_16b_bf16 v[28:31], a[248:249], a[224:225], v[28:31]\n"
      "ds_read_b64_tr_b16 a[80:81], v60 offset:128\n"
      "v_mfma_f32_4x4x4_16b_bf16 v[32:35], a[248:249], a[226:227], v[32:35]\n"
      "ds_read_b64_tr_b16 a[82:83], v60 offset:896\n"
      "v_mfma_f32_4x4x4_16b_bf16 v[36:39], a[246:247], a[220:221], v[28:31]\n"
      "ds_read_b64_tr_b16 a[240:241], v60 offset:12416\n"
      "v_mfma_f32_4x4x4_16b_bf16 v[40:43], a[246:247], a[222:223], v[32:35]\n"
      "ds_read_b64_tr_b16 a[242:243], v60 offset:13184\n"
      "v_mfma_f32_4x4x4_16b_bf16 v[28:31], a[250:251], a[220:221], v[28:31]\n"
      "ds_read_b64_tr_b16 a[236:237], v60 offset:6272\n"
      "v_mfma_f32_4x4x4_16b_bf16 v[32:35], a[250:251], a[222:223], v[32:35]\n"
      "ds_read_b64_tr_b16 a[238:239], v60 offset:7040\n"
      "ds_read_b128 a[192:195], v4 offset:55296\n"
      "ds_read_b128 a[196:199], v1 offset:128\n"
      "v_mfma_f32_16x16x32_bf16 v[88:91], v[176:179], a[200:203], v[88:91]\n"
      "s_nop 0\n"
      "v_cvt_pk_bf16_f32 v116, v52, v56\n"
      "v_cvt_pk_bf16_f32 v117, v53, v57\n"
      "v_mfma_f32_16x16x32_bf16 v[92:95], v[168:171], a[200:203], v[92:95]\n"
      "s_nop 0\n"
      "v_cvt_pk_bf16_f32 v118, v54, v58\n"
      "v_cvt_pk_bf16_f32 v119, v55, v59\n"
      "v_mfma_f32_16x16x32_bf16 v[96:99], v[160:163], a[200:203], v[96:99]\n"
      "s_nop 0\n"
      "v_cvt_pk_bf16_f32 v112, v44, v48\n"
      "v_cvt_pk_bf16_f32 v113, v45, v49\n"
      "v_mfma_f32_16x16x32_bf16 v[64:67], a[48:51], a[204:207], v[64:67]\n"
      "s_waitcnt vmcnt(9)\n"
      "v_cvt_pk_bf16_f32 v114, v46, v50\n"
      "v_cvt_pk_bf16_f32 v115, v47, v51\n"
      "s_barrier\n"
      "s_add_u32 s53, s54, 2359296\n"
      "s_add_u32 m0, s37, 128896\n"
      "s_waitcnt lgkmcnt(0)\n"
      "v_mfma_f32_4x4x4_16b_bf16 v[44:47], a[244:245], a[76:77], 0\n"
      "v_mfma_f32_4x4x4_16b_bf16 v[48:51], a[244:245], a[78:79], 0\n"
      "buffer_load_dwordx4 v8, s[4:7], s53 offen offset:0 nt lds\n"
      "buffer_load_dwordx4 v9, s[4:7], s53 offen offset:1024 nt lds\n"
      "v_mfma_f32_4x4x4_16b_bf16 v[44:47], a[248:249], a[232:233], v[44:47]\n"
      "ds_read_b64_tr_b16 a[84:85], v60 offset:18432\n"
      "v_mfma_f32_4x4x4_16b_bf16 v[48:51], a[248:249], a[234:235], v[48:51]\n"
      "ds_read_b64_tr_b16 a[86:87], v60 offset:19200\n"
      "v_mfma_f32_4x4x4_16b_bf16 v[52:55], a[246:247], a[228:229], v[44:47]\n"
      "ds_read_b64_tr_b16 a[224:225], v61 offset:6400\n"
      "v_mfma_f32_4x4x4_16b_bf16 v[56:59], a[246:247], a[230:231], v[48:51]\n"
      "ds_read_b64_tr_b16 a[226:227], v61 offset:7168\n"
      "v_mfma_f32_4x4x4_16b_bf16 v[44:47], a[250:251], a[228:229], v[44:47]\n"
      "ds_read_b64_tr_b16 a[220:221], v61 offset:256\n"
      "v_mfma_f32_4x4x4_16b_bf16 v[48:51], a[250:251], a[230:231], v[48:51]\n"
      "ds_read_b64_tr_b16 a[222:223], v61 offset:1024\n"
      "ds_read_b128 a[200:203], v1 offset:4224\n"
      "v_mfma_f32_16x16x32_bf16 v[68:71], a[52:55], a[204:207], v[68:71]\n"
      "v_cvt_pk_bf16_f32 v108, v36, v40\n"
      "v_cvt_pk_bf16_f32 v109, v37, v41\n"
      "v_mfma_f32_16x16x32_bf16 v[72:75], a[56:59], a[204:207], v[72:75]\n"
      "v_cvt_pk_bf16_f32 v110, v38, v42\n"
      "v_cvt_pk_bf16_f32 v111, v39, v43\n"
      "v_mfma_f32_16x16x32_bf16 v[76:79], v[156:159], a[208:211], v[76:79]\n"
      "s_add_u32 m0, s37, 133120\n"
      "v_cvt_pk_bf16_f32 v104, v28, v32\n"
      "v_cvt_pk_bf16_f32 v105, v29, v33\n"
      "v_mfma_f32_16x16x32_bf16 v[80:83], v[148:151], a[208:211], v[80:83]\n"
      "s_add_u32 s53, s54, 2555904\n"
      "v_cvt_pk_bf16_f32 v106, v30, v34\n"
      "v_cvt_pk_bf16_f32 v107, v31, v35\n"
      "v_mfma_f32_4x4x4_16b_bf16 v[28:31], a[244:245], a[80:81], 0\n"
      "v_mfma_f32_4x4x4_16b_bf16 v[32:35], a[244:245], a[82:83], 0\n"
      "buffer_load_dwordx4 v8, s[4:7], s53 offen offset:0 nt lds\n"
      "buffer_load_dwordx4 v9, s[4:7], s53 offen offset:1024 nt lds\n"
      "v_mfma_f32_4x4x4_16b_bf16 v[28:31], a[248:249], a[240:241], v[28:31]\n"
      "ds_read_b64_tr_b16 a[88:89], v60 offset:18496\n"
      "v_mfma_f32_4x4x4_16b_bf16 v[32:35], a[248:249], a[242:243], v[32:35]\n"
      "ds_read_b64_tr_b16 a[90:91], v60 offset:19264\n"
      "v_mfma_f32_4x4x4_16b_bf16 v[36:39], a[246:247], a[236:237], v[28:31]\n"
      "ds_read_b64_tr_b16 a[232:233], v61 offset:6464\n"
      "v_mfma_f32_4x4x4_16b_bf16 v[40:43], a[246:247], a[238:239], v[32:35]\n"
      "ds_read_b64_tr_b16 a[234:235], v61 offset:7232\n"
      "v_mfma_f32_4x4x4_16b_bf16 v[28:31], a[250:251], a[236:237], v[28:31]\n"
      "ds_read_b64_tr_b16 a[228:229], v61 offset:320\n"
      "v_mfma_f32_4x4x4_16b_bf16 v[32:35], a[250:251], a[238:239], v[32:35]\n"
      "ds_read_b64_tr_b16 a[230:231], v61 offset:1088\n"
      "ds_read_b128 a[204:207], v5 offset:55168\n"
      "v_mfma_f32_16x16x32_bf16 v[84:87], v[140:143], a[208:211], v[84:87]\n"
      "v_cvt_pk_bf16_f32 v100, v52, v56\n"
      "v_cvt_pk_bf16_f32 v101, v53, v57\n"
      "v_mfma_f32_16x16x32_bf16 v[88:91], v[152:155], a[212:215], v[88:91]\n"
      "v_cvt_pk_bf16_f32 v102, v54, v58\n"
      "v_cvt_pk_bf16_f32 v103, v55, v59\n"
      "v_mfma_f32_16x16x32_bf16 v[92:95], v[144:147], a[212:215], v[92:95]\n"
      "v_cvt_pk_bf16_f32 v44, v44, v48\n"
      "v_cvt_pk_bf16_f32 v45, v45, v49\n"
      "v_mfma_f32_16x16x32_bf16 v[96:99], v[136:139], a[212:215], v[96:99]\n"
      "v_cvt_pk_bf16_f32 v46, v46, v50\n"
      "v_cvt_pk_bf16_f32 v47, v47, v51\n"
      "v_mfma_f32_16x16x32_bf16 v[64:67], a[60:63], a[192:195], v[64:67]\n"
      "s_add_u32 m0, s37, 137224\n"
      "v_accvgpr_write_b32 a164, v44\n"
      "v_accvgpr_write_b32 a165, v45\n"
      "v_mfma_f32_16x16x32_bf16 v[68:71], a[64:67], a[192:195], v[68:71]\n"
      "s_add_u32 s53, s54, 2752512\n"
      "v_accvgpr_write_b32 a166, v46\n"
      "v_accvgpr_write_b32 a167, v47\n"
      "s_nop 0\n"
      "s_waitcnt lgkmcnt(0)\n"
      "v_mfma_f32_4x4x4_16b_bf16 v[44:47], a[244:245], a[84:85], 0\n"
      "v_mfma_f32_4x4x4_16b_bf16 v[48:51], a[244:245], a[86:87], 0\n"
      "buffer_load_dwordx4 v8, s[4:7], s53 offen offset:0 nt lds\n"
      "buffer_load_dwordx4 v9, s[4:7], s53 offen offset:1024 nt lds\n"
      "v_mfma_f32_4x4x4_16b_bf16 v[44:47], a[248:249], a[224:225], v[44:47]\n"
      "ds_read_b64_tr_b16 a[92:93], v60 offset:18560\n"
      "v_mfma_f32_4x4x4_16b_bf16 v[48:51], a[248:249], a[226:227], v[48:51]\n"
      "ds_read_b64_tr_b16 a[94:95], v60 offset:19328\n"
      "v_mfma_f32_4x4x4_16b_bf16 v[52:55], a[246:247], a[220:221], v[44:47]\n"
      "ds_read_b64_tr_b16 a[240:241], v61 offset:6528\n"
      "v_mfma_f32_4x4x4_16b_bf16 v[56:59], a[246:247], a[222:223], v[48:51]\n"
      "ds_read_b64_tr_b16 a[242:243], v61 offset:7296\n"
      "v_mfma_f32_4x4x4_16b_bf16 v[44:47], a[250:251], a[220:221], v[44:47]\n"
      "ds_read_b64_tr_b16 a[236:237], v61 offset:384\n"
      "v_mfma_f32_4x4x4_16b_bf16 v[48:51], a[250:251], a[222:223], v[48:51]\n"
      "ds_read_b64_tr_b16 a[238:239], v61 offset:1152\n"
      "ds_read_b128 a[208:211], v2 offset:0\n"
      "ds_read_b128 a[212:215], v2 offset:4096\n"
      "v_cvt_pk_bf16_f32 v36, v36, v40\n"
      "v_cvt_pk_bf16_f32 v37, v37, v41\n"
      "v_cvt_pk_bf16_f32 v38, v38, v42\n"
      "v_cvt_pk_bf16_f32 v39, v39, v43\n"
      "v_mfma_f32_16x16x32_bf16 v[72:75], a[68:71], a[192:195], v[72:75]\n"
      "v_accvgpr_write_b32 a160, v36\n"
      "v_accvgpr_write_b32 a161, v37\n"
      "v_mfma_f32_16x16x32_bf16 v[76:79], v[132:135], a[196:199], v[76:79]\n"
      "s_nop 0\n"
      "v_accvgpr_write_b32 a162, v38\n"
      "v_accvgpr_write_b32 a163, v39\n"
      "v_mfma_f32_16x16x32_bf16 v[80:83], v[124:127], a[196:199], v[80:83]\n"
      "s_nop 0\n"
      "v_cvt_pk_bf16_f32 v28, v28, v32\n"
      "v_cvt_pk_bf16_f32 v29, v29, v33\n"
      "v_mfma_f32_16x16x32_bf16 v[84:87], v[116:119], a[196:199], v[84:87]\n"
      "s_nop 0\n"
      "v_cvt_pk_bf16_f32 v30, v30, v34\n"
      "v_cvt_pk_bf16_f32 v31, v31, v35\n"
      "v_mfma_f32_16x16x32_bf16 v[88:91], v[128:131], a[200:203], v[88:91]\n"
      "s_nop 0\n"
      "v_accvgpr_write_b32 a156, v28\n"
      "v_accvgpr_write_b32 a157, v29\n"
      "v_mfma_f32_16x16x32_bf16 v[92:95], v[120:123], a[200:203], v[92:95]\n"
      "s_waitcnt vmcnt(12)\n"
      "v_accvgpr_write_b32 a158, v30\n"
      "v_accvgpr_write_b32 a159, v31\n"
      "s_barrier\n"
      "s_add_u32 s53, s54, 2949120\n"
      "s_add_u32 m0, s37, 141448\n"
      "v_mfma_f32_4x4x4_16b_bf16 v[28:31], a[244:245], a[88:89], 0\n"
      "v_mfma_f32_4x4x4_16b_bf16 v[32:35], a[244:245], a[90:91], 0\n"
      "buffer_load_dwordx4 v8, s[4:7], s53 offen offset:0 nt lds\n"
      "buffer_load_dwordx4 v9, s[4:7], s53 offen offset:1024 nt lds\n"
      "v_mfma_f32_4x4x4_16b_bf16 v[28:31], a[248:249], a[232:233], v[28:31]\n"
      "ds_read_b64_tr_b16 a[96:97], v61 offset:12544\n"
      "v_mfma_f32_4x4x4_16b_bf16 v[32:35], a[248:249], a[234:235], v[32:35]\n"
      "ds_read_b64_tr_b16 a[98:99], v61 offset:13312\n"
      "v_mfma_f32_4x4x4_16b_bf16 v[36:39], a[246:247], a[228:229], v[28:31]\n"
      "ds_read_b64_tr_b16 a[224:225], v62 offset:39680\n"
      "v_mfma_f32_4x4x4_16b_bf16 v[40:43], a[246:247], a[230:231], v[32:35]\n"
      "ds_read_b64_tr_b16 a[226:227], v62 offset:40448\n"
      "v_mfma_f32_4x4x4_16b_bf16 v[28:31], a[250:251], a[228:229], v[28:31]\n"
      "ds_read_b64_tr_b16 a[220:221], v61 offset:18688\n"
      "v_mfma_f32_4x4x4_16b_bf16 v[32:35], a[250:251], a[230:231], v[32:35]\n"
      "ds_read_b64_tr_b16 a[222:223], v61 offset:19456\n"
      "ds_read_b128 a[192:195], v5 offset:55232\n"
      "v_cvt_pk_bf16_f32 v52, v52, v56\n"
      "v_cvt_pk_bf16_f32 v53, v53, v57\n"
      "v_cvt_pk_bf16_f32 v54, v54, v58\n"
      "v_cvt_pk_bf16_f32 v55, v55, v59\n"
      "v_mfma_f32_16x16x32_bf16 v[96:99], v[112:115], a[200:203], v[96:99]\n"
      "s_nop 0\n"
      "v_accvgpr_write_b32 a152, v52\n"
      "v_accvgpr_write_b32 a153, v53\n"
      "v_mfma_f32_16x16x32_bf16 v[64:67], a[72:75], a[204:207], v[64:67]\n"
      "s_nop 0\n"
      "v_accvgpr_write_b32 a154, v54\n"
      "v_accvgpr_write_b32 a155, v55\n"
      "v_mfma_f32_16x16x32_bf16 v[68:71], a[76:79], a[204:207], v[68:71]\n"
      "s_nop 0\n"
      "v_cvt_pk_bf16_f32 v44, v44, v48\n"
      "v_cvt_pk_bf16_f32 v45, v45, v49\n"
      "v_mfma_f32_16x16x32_bf16 v[72:75], a[80:83], a[204:207], v[72:75]\n"
      "s_waitcnt lgkmcnt(0)\n"
      "v_cvt_pk_bf16_f32 v46, v46, v50\n"
      "v_cvt_pk_bf16_f32 v47, v47, v51\n"
      "v_mfma_f32_16x16x32_bf16 v[76:79], v[108:111], a[208:211], v[76:79]\n"
      "s_add_u32 m0, s37, 145792\n"
      "v_accvgpr_write_b32 a148, v44\n"
      "v_accvgpr_write_b32 a149, v45\n"
      "v_mfma_f32_16x16x32_bf16 v[80:83], v[100:103], a[208:211], v[80:83]\n"
      "s_add_u32 s53, s54, 3145728\n"
      "v_accvgpr_write_b32 a150, v46\n"
      "v_accvgpr_write_b32 a151, v47\n"
      "v_mfma_f32_4x4x4_16b_bf16 v[44:47], a[244:245], a[92:93], 0\n"
      "v_mfma_f32_4x4x4_16b_bf16 v[48:51], a[244:245], a[94:95], 0\n"
      "buffer_load_dwordx4 v8, s[4:7], s53 offen offset:0 nt lds\n"
      "buffer_load_dwordx4 v9, s[4:7], s53 offen offset:1024 nt lds\n"
      "v_mfma_f32_4x4x4_16b_bf16 v[44:47], a[248:249], a[240:241], v[44:47]\n"
      "ds_read_b64_tr_b16 a[100:101], v61 offset:12608\n"
      "v_mfma_f32_4x4x4_16b_bf16 v[48:51], a[248:249], a[242:243], v[48:51]\n"
      "ds_read_b64_tr_b16 a[102:103], v61 offset:13376\n"
      "v_mfma_f32_4x4x4_16b_bf16 v[52:55], a[246:247], a[236:237], v[44:47]\n"
      "ds_read_b64_tr_b16 a[232:233], v62 offset:39744\n"
      "v_mfma_f32_4x4x4_16b_bf16 v[56:59], a[246:247], a[238:239], v[48:51]\n"
      "ds_read_b64_tr_b16 a[234:235], v62 offset:40512\n"
      "v_mfma_f32_4x4x4_16b_bf16 v[44:47], a[250:251], a[236:237], v[44:47]\n"
      "ds_read_b64_tr_b16 a[228:229], v61 offset:18752\n"
      "v_mfma_f32_4x4x4_16b_bf16 v[48:51], a[250:251], a[238:239], v[48:51]\n"
      "ds_read_b64_tr_b16 a[230:231], v61 offset:19520\n"
      "ds_read_b128 a[196:199], v2 offset:64\n"
      "ds_read_b128 a[200:203], v2 offset:4160\n"
      "v_cvt_pk_bf16_f32 v36, v36, v40\n"
      "v_cvt_pk_bf16_f32 v37, v37, v41\n"
      "v_cvt_pk_bf16_f32 v38, v38, v42\n"
      "v_cvt_pk_bf16_f32 v39, v39, v43\n"
      "v_accvgpr_write_b32 a144, v36\n"
      "v_accvgpr_write_b32 a145, v37\n"
      "v_accvgpr_write_b32 a146, v38\n"
      "v_accvgpr_write_b32 a147, v39\n"
      "v_mfma_f32_16x16x32_bf16 v[84:87], a[160:163], a[208:211], v[84:87]\n"
      "s_nop 0\n"
      "v_cvt_pk_bf16_f32 v28, v28, v32\n"
      "v_cvt_pk_bf16_f32 v29, v29, v33\n"
      "v_mfma_f32_16x16x32_bf16 v[88:91], v[104:107], a[212:215], v[88:91]\n"
      "s_nop 0\n"
      "v_cvt_pk_bf16_f32 v30, v30, v34\n"
      "v_cvt_pk_bf16_f32 v31, v31, v35\n"
      "v_mfma_f32_16x16x32_bf16 v[92:95], a[164:167], a[212:215], v[92:95]\n"
      "s_nop 0\n"
      "v_accvgpr_write_b32 a140, v28\n"
      "v_accvgpr_write_b32 a141, v29\n"
      "v_mfma_f32_16x16x32_bf16 v[96:99], a[156:159], a[212:215], v[96:99]\n"
      "s_nop 0\n"
      "v_accvgpr_write_b32 a142, v30\n"
      "v_accvgpr_write_b32 a143, v31\n"
      "v_mfma_f32_4x4x4_16b_bf16 v[28:31], a[244:245], a[96:97], 0\n"
      "v_mfma_f32_4x4x4_16b_bf16 v[32:35], a[244:245], a[98:99], 0\n"
      "ds_read_b64_tr_b16 a[104:105], v61 offset:12672\n"
      "v_mfma_f32_4x4x4_16b_bf16 v[28:31], a[248:249], a[224:225], v[28:31]\n"
      "ds_read_b64_tr_b16 a[106:107], v61 offset:13440\n"
      "v_mfma_f32_4x4x4_16b_bf16 v[32:35], a[248:249], a[226:227], v[32:35]\n"
      "ds_read_b64_tr_b16 a[240:241], v62 offset:39808\n"
      "v_mfma_f32_4x4x4_16b_bf16 v[36:39], a[246:247], a[220:221], v[28:31]\n"
      "ds_read_b64_tr_b16 a[242:243], v62 offset:40576\n"
      "v_mfma_f32_4x4x4_16b_bf16 v[40:43], a[246:247], a[222:223], v[32:35]\n"
      "ds_read_b64_tr_b16 a[236:237], v61 offset:18816\n"
      "v_mfma_f32_4x4x4_16b_bf16 v[28:31], a[250:251], a[220:221], v[28:31]\n"
      "ds_read_b64_tr_b16 a[238:239], v61 offset:19584\n"
      "v_mfma_f32_4x4x4_16b_bf16 v[32:35], a[250:251], a[222:223], v[32:35]\n"
      "v_cvt_pk_bf16_f32 v52, v52, v56\n"
      "v_cvt_pk_bf16_f32 v53, v53, v57\n"
      "v_cvt_pk_bf16_f32 v54, v54, v58\n"
      "v_cvt_pk_bf16_f32 v55, v55, v59\n"
      "v_accvgpr_write_b32 a136, v52\n"
      "v_accvgpr_write_b32 a137, v53\n"
      "v_accvgpr_write_b32 a138, v54\n"
      "v_accvgpr_write_b32 a139, v55\n"
      "v_cvt_pk_bf16_f32 v44, v44, v48\n"
      "v_cvt_pk_bf16_f32 v45, v45, v49\n"
      "v_mfma_f32_16x16x32_bf16 v[64:67], a[84:87], a[192:195], v[64:67]\n"
      "s_nop 0\n"
      "v_cvt_pk_bf16_f32 v46, v46, v50\n"
      "v_cvt_pk_bf16_f32 v47, v47, v51\n"
      "v_mfma_f32_16x16x32_bf16 v[68:71], a[88:91], a[192:195], v[68:71]\n"
      "s_nop 0\n"
      "v_accvgpr_write_b32 a132, v44\n"
      "v_accvgpr_write_b32 a133, v45\n"
      "v_mfma_f32_16x16x32_bf16 v[72:75], a[92:95], a[192:195], v[72:75]\n"
      "s_waitcnt lgkmcnt(0)\n"
      "v_accvgpr_write_b32 a134, v46\n"
      "v_accvgpr_write_b32 a135, v47\n"
      "v_mfma_f32_4x4x4_16b_bf16 v[44:47], a[244:245], a[100:101], 0\n"
      "v_mfma_f32_4x4x4_16b_bf16 v[48:51], a[244:245], a[102:103], 0\n"
      "ds_read_b128 a[204:207], v5 offset:55296\n"
      "v_mfma_f32_4x4x4_16b_bf16 v[44:47], a[248:249], a[232:233], v[44:47]\n"
      "v_mfma_f32_4x4x4_16b_bf16 v[48:51], a[248:249], a[234:235], v[48:51]\n"
      "s_nop 0\n"
      "v_mfma_f32_4x4x4_16b_bf16 v[52:55], a[246:247], a[228:229], v[44:47]\n"
      "v_mfma_f32_4x4x4_16b_bf16 v[56:59], a[246:247], a[230:231], v[48:51]\n"
      "v_mfma_f32_4x4x4_16b_bf16 v[44:47], a[250:251], a[228:229], v[44:47]\n"
      "v_mfma_f32_4x4x4_16b_bf16 v[48:51], a[250:251], a[230:231], v[48:51]\n"
      "v_cvt_pk_bf16_f32 v36, v36, v40\n"
      "v_cvt_pk_bf16_f32 v37, v37, v41\n"
      "v_cvt_pk_bf16_f32 v38, v38, v42\n"
      "v_cvt_pk_bf16_f32 v39, v39, v43\n"
      "v_mfma_f32_16x16x32_bf16 v[76:79], a[152:155], a[196:199], v[76:79]\n"
      "v_accvgpr_write_b32 a128, v36\n"
      "v_accvgpr_write_b32 a129, v37\n"
      "v_mfma_f32_16x16x32_bf16 v[80:83], a[144:147], a[196:199], v[80:83]\n"
      "v_accvgpr_write_b32 a130, v38\n"
      "v_accvgpr_write_b32 a131, v39\n"
      "v_mfma_f32_16x16x32_bf16 v[84:87], a[136:139], a[196:199], v[84:87]\n"
      "v_cvt_pk_bf16_f32 v28, v28, v32\n"
      "v_cvt_pk_bf16_f32 v29, v29, v33\n"
      "v_mfma_f32_16x16x32_bf16 v[88:91], a[148:151], a[200:203], v[88:91]\n"
      "v_cvt_pk_bf16_f32 v30, v30, v34\n"
      "v_cvt_pk_bf16_f32 v31, v31, v35\n"
      "v_mfma_f32_16x16x32_bf16 v[92:95], a[140:143], a[200:203], v[92:95]\n"
      "v_accvgpr_write_b32 a124, v28\n"
      "v_accvgpr_write_b32 a125, v29\n"
      "v_mfma_f32_16x16x32_bf16 v[96:99], a[132:135], a[200:203], v[96:99]\n"
      "v_accvgpr_write_b32 a126, v30\n"
      "v_accvgpr_write_b32 a127, v31\n"
      "v_mfma_f32_4x4x4_16b_bf16 v[28:31], a[244:245], a[104:105], 0\n"
      "v_mfma_f32_4x4x4_16b_bf16 v[32:35], a[244:245], a[106:107], 0\n"
      "ds_read_b128 a[208:211], v2 offset:128\n"
      "v_mfma_f32_4x4x4_16b_bf16 v[28:31], a[248:249], a[240:241], v[28:31]\n"
      "ds_read_b128 a[212:215], v2 offset:4224\n"
      "v_mfma_f32_4x4x4_16b_bf16 v[32:35], a[248:249], a[242:243], v[32:35]\n"
      "v_mfma_f32_4x4x4_16b_bf16 v[36:39], a[246:247], a[236:237], v[28:31]\n"
      "s_nop 0\n"
      "s_nop 0\n"
      "v_mfma_f32_4x4x4_16b_bf16 v[40:43], a[246:247], a[238:239], v[32:35]\n"
      "v_mfma_f32_4x4x4_16b_bf16 v[28:31], a[250:251], a[236:237], v[28:31]\n"
      "v_mfma_f32_4x4x4_16b_bf16 v[32:35], a[250:251], a[238:239], v[32:35]\n"
      "v_cvt_pk_bf16_f32 v52, v52, v56\n"
      "v_cvt_pk_bf16_f32 v53, v53, v57\n"
      "v_cvt_pk_bf16_f32 v54, v54, v58\n"
      "v_cvt_pk_bf16_f32 v55, v55, v59\n"
      "v_accvgpr_write_b32 a120, v52\n"
      "v_accvgpr_write_b32 a121, v53\n"
      "v_accvgpr_write_b32 a122, v54\n"
      "v_accvgpr_write_b32 a123, v55\n"
      "v_cvt_pk_bf16_f32 v44, v44, v48\n"
      "v_cvt_pk_bf16_f32 v45, v45, v49\n"
      "v_cvt_pk_bf16_f32 v46, v46, v50\n"
      "v_cvt_pk_bf16_f32 v47, v47, v51\n"
      "v_accvgpr_write_b32 a116, v44\n"
      "v_accvgpr_write_b32 a117, v45\n"
      "v_accvgpr_write_b32 a118, v46\n"
      "v_accvgpr_write_b32 a119, v47\n"
      "s_waitcnt lgkmcnt(0)\n"
      "s_barrier\n"
      "s_mov_b32 m0, s64\n"
      "s_nop 0\n"
      "v_mfma_f32_16x16x32_bf16 v[64:67], a[96:99], a[204:207], v[64:67]\n"
      "s_add_u32 s53, s54, 3342336\n"
      "ds_read_b128 a[192:195], v3 offset:0\n"
      "ds_read_b64_tr_b16 a[220:221], v7 offset:18944\n"
      "ds_read_b64_tr_b16 a[222:223], v7 offset:18952\n"
      "v_cvt_pk_bf16_f32 v36, v36, v40\n"
      "v_cvt_pk_bf16_f32 v37, v37, v41\n"
      "v_cvt_pk_bf16_f32 v38, v38, v42\n"
      "v_cvt_pk_bf16_f32 v39, v39, v43\n"
      "v_mfma_f32_16x16x32_bf16 v[68:71], a[100:103], a[204:207], v[68:71]\n"
      "s_add_u32 m0, s37, 82432\n"
      "ds_read_b128 a[196:199], v3 offset:64\n"
      "v_accvgpr_write_b32 a112, v36\n"
      "v_accvgpr_write_b32 a113, v37\n"
      "v_accvgpr_write_b32 a114, v38\n"
      "v_accvgpr_write_b32 a115, v39\n"
      "v_cvt_pk_bf16_f32 v28, v28, v32\n"
      "v_cvt_pk_bf16_f32 v29, v29, v33\n"
      "v_mfma_f32_16x16x32_bf16 v[72:75], a[104:107], a[204:207], v[72:75]\n"
      "s_nop 0\n"
      "buffer_load_dwordx4 v8, s[4:7], s53 offen offset:0 nt lds\n"
      "buffer_load_dwordx4 v9, s[4:7], s53 offen offset:1024 nt lds\n"
      "v_cvt_pk_bf16_f32 v30, v30, v34\n"
      "v_mfma_f32_16x16x32_bf16 v[76:79], a[128:131], a[208:211], v[76:79]\n"
      "s_mov_b32 m0, s64\n"
      "ds_read_b128 a[200:203], v3 offset:128\n"
      "ds_read_b64_tr_b16 a[224:225], v7 offset:19456\n"
      "ds_read_b64_tr_b16 a[226:227], v7 offset:19464\n"
      "v_cvt_pk_bf16_f32 v31, v31, v35\n"
      "v_accvgpr_write_b32 a108, v28\n"
      "v_accvgpr_write_b32 a109, v29\n"
      "v_accvgpr_write_b32 a110, v30\n"
      "v_accvgpr_write_b32 a111, v31\n"
      "v_mfma_f32_16x16x32_bf16 v[80:83], a[120:123], a[208:211], v[80:83]\n"
      "s_nop 0\n"
      "buffer_load_dword a252, v8, s[4:7], s40 offen\n"
      "v_mfma_f32_16x16x32_bf16 v[84:87], a[112:115], a[208:211], v[84:87]\n"
      "s_waitcnt lgkmcnt(3)\n"
      "buffer_load_dword a252, v8, s[4:7], s40 offen\n"
      "buffer_load_dword a252, v8, s[4:7], s40 offen\n"
      "v_mfma_f32_4x4x4_16b_bf16 v[20:23], a[216:217], a[220:221], 0\n"
      "buffer_load_dword a252, v8, s[4:7], s40 offen\n"
      "v_mfma_f32_4x4x4_16b_bf16 v[24:27], a[216:217], a[222:223], 0\n"
      "buffer_load_dword a252, v8, s[4:7], s40 offen\n"
      "v_mfma_f32_16x16x32_bf16 v[88:91], a[124:127], a[212:215], v[88:91]\n"
      "s_add_u32 s53, s54, 3538944\n"
      "ds_read_b128 a[204:207], v4 offset:0\n"
      "v_mfma_f32_16x16x32_bf16 v[92:95], a[116:119], a[212:215], v[92:95]\n"
      "s_add_u32 m0, s37, 86536\n"
      "v_cvt_pk_bf16_f32 v20, v20, v24\n"
      "v_cvt_pk_bf16_f32 v21, v21, v25\n"
      "v_cvt_pk_bf16_f32 v22, v22, v26\n"
      "v_mfma_f32_16x16x32_bf16 v[96:99], a[108:111], a[212:215], v[96:99]\n"
      "s_nop 0\n"
      "buffer_load_dwordx4 v8, s[4:7], s53 offen offset:0 nt lds\n"
      "buffer_load_dwordx4 v9, s[4:7], s53 offen offset:1024 nt lds\n"
      "ds_read_b64_tr_b16 a[228:229], v7 offset:19968\n"
      "ds_read_b64_tr_b16 a[230:231], v7 offset:19976\n"
      "v_mfma_f32_16x16x32_bf16 v[52:55], a[0:3], a[192:195], 0\n"
      "s_mov_b32 m0, s64\n"
      "ds_read_b128 a[208:211], v4 offset:64\n"
      "ds_write_addtid_b32 v20 offset:18432\n"
      "ds_write_addtid_b32 v21 offset:22528\n"
      "v_mfma_f32_16x16x32_bf16 v[56:59], a[4:7], a[192:195], 0\n"
      "s_waitcnt lgkmcnt(5)\n"
      "ds_write_addtid_b32 v22 offset:26624\n"
      "v_add_f32 v88, v76, v88\n"
      "v_add_f32 v89, v77, v89\n"
      "v_mfma_f32_4x4x4_16b_bf16 v[28:31], a[216:217], a[224:225], 0\n"
      "v_add_f32 v90, v78, v90\n"
      "v_mfma_f32_4x4x4_16b_bf16 v[32:35], a[216:217], a[226:227], 0\n"
      "ds_write_addtid_b32 v88 offset:45312\n"
      "ds_write_addtid_b32 v89 offset:45824\n"
      "ds_write_addtid_b32 v90 offset:46336\n"
      "v_add_f32 v91, v79, v91\n"
      "v_mfma_f32_16x16x32_bf16 v[60:63], a[8:11], a[192:195], 0\n"
      "s_nop 0\n"
      "ds_write_addtid_b32 v91 offset:46848\n"
      "v_cvt_pk_bf16_f32 v23, v23, v27\n"
      "v_cvt_pk_bf16_f32 v28, v28, v32\n"
      "v_mfma_f32_16x16x32_bf16 v[52:55], a[12:15], a[196:199], v[52:55]\n"
      "s_add_u32 s53, s54, 3735552\n"
      "ds_read_b128 a[212:215], v4 offset:128\n"
      "ds_write_addtid_b32 v28 offset:18944\n"
      "ds_write_addtid_b32 v23 offset:30720\n"
      "v_mfma_f32_16x16x32_bf16 v[56:59], a[16:19], a[196:199], v[56:59]\n"
      "s_add_u32 m0, s37, 90760\n"
      "ds_read_b64_tr_b16 a[232:233], v7 offset:20480\n"
      "ds_read_b64_tr_b16 a[234:235], v7 offset:20488\n"
      "v_add_f32_e64 v92, v80, v92\n"
      "v_mfma_f32_16x16x32_bf16 v[60:63], a[20:23], a[196:199], v[60:63]\n"
      "s_waitcnt lgkmcnt(6)\n"
      "buffer_load_dwordx4 v8, s[4:7], s53 offen offset:0 nt lds\n"
      "buffer_load_dwordx4 v9, s[4:7], s53 offen offset:1024 nt lds\n"
      "v_mfma_f32_16x16x32_bf16 v[52:55], a[24:27], a[200:203], v[52:55]\n"
      "s_mov_b32 m0, s64\n"
      "ds_read_b128 a[192:195], v5 offset:0\n"
      "ds_write_addtid_b32 v92 offset:47360\n"
      "v_add_f32 v93, v81, v93\n"
      "v_mfma_f32_4x4x4_16b_bf16 v[20:23], a[216:217], a[228:229], 0\n"
      "v_cvt_pk_bf16_f32 v29, v29, v33\n"
      "v_add_f32 v94, v82, v94\n"
      "v_mfma_f32_4x4x4_16b_bf16 v[24:27], a[216:217], a[230:231], 0\n"
      "ds_write_addtid_b32 v29 offset:23040\n"
      "ds_write_addtid_b32 v93 offset:47872\n"
      "ds_write_addtid_b32 v94 offset:48384\n"
      "v_mfma_f32_16x16x32_bf16 v[56:59], a[28:31], a[200:203], v[56:59]\n"
      "v_add_f32 v95, v83, v95\n"
      "v_cvt_pk_bf16_f32 v30, v30, v34\n"
      "v_add_f32 v96, v84, v96\n"
      "v_mfma_f32_16x16x32_bf16 v[60:63], a[32:35], a[200:203], v[60:63]\n"
      "ds_write_addtid_b32 v30 offset:27136\n"
      "ds_write_addtid_b32 v95 offset:48896\n"
      "ds_write_addtid_b32 v96 offset:49408\n"
      "v_mfma_f32_16x16x32_bf16 v[52:55], a[36:39], a[204:207], v[52:55]\n"
      "ds_read_b128 a[196:199], v5 offset:64\n"
      "v_cvt_pk_bf16_f32 v31, v31, v35\n"
      "v_cvt_pk_bf16_f32 v20, v20, v24\n"
      "v_mfma_f32_16x16x32_bf16 v[56:59], a[40:43], a[204:207], v[56:59]\n"
      "ds_write_addtid_b32 v20 offset:19456\n"
      "ds_write_addtid_b32 v31 offset:31232\n"
      "v_add_f32_e64 v97, v85, v97\n"
      "v_mfma_f32_16x16x32_bf16 v[60:63], a[44:47], a[204:207], v[60:63]\n"
      "s_waitcnt lgkmcnt(6)\n"
      "ds_write_addtid_b32 v97 offset:49920\n"
      "v_add_f32 v98, v86, v98\n"
      "v_add_f32_e64 v99, v87, v99\n"
      "v_mfma_f32_4x4x4_16b_bf16 v[28:31], a[216:217], a[232:233], 0\n"
      "v_cvt_pk_bf16_f32 v21, v21, v25\n"
      "v_cvt_pk_bf16_f32 v22, v22, v26\n"
      "v_mfma_f32_4x4x4_16b_bf16 v[32:35], a[216:217], a[234:235], 0\n"
      "ds_write_addtid_b32 v21 offset:23552\n"
      "ds_write_addtid_b32 v98 offset:50432\n"
      "ds_write_addtid_b32 v22 offset:27648\n"
      "v_mfma_f32_16x16x32_bf16 v[52:55], a[48:51], a[208:211], v[52:55]\n"
      "ds_read_b128 a[200:203], v5 offset:128\n"
      "ds_write_addtid_b32 v99 offset:50944\n"
      "v_cvt_pk_bf16_f32 v28, v28, v32\n"
      "v_mfma_f32_16x16x32_bf16 v[56:59], a[52:55], a[208:211], v[56:59]\n"
      "s_nop 0\n"
      "ds_write_addtid_b32 v28 offset:19968\n"
      "v_cvt_pk_bf16_f32 v29, v29, v33\n"
      "v_cvt_pk_bf16_f32 v30, v30, v34\n"
      "v_cvt_pk_bf16_f32 v31, v31, v35\n"
      "v_cvt_pk_bf16_f32 v23, v23, v27\n"
      "v_mfma_f32_16x16x32_bf16 v[60:63], a[56:59], a[208:211], v[60:63]\n"
      "s_nop 0\n"
      "ds_write_addtid_b32 v29 offset:24064\n"
      "ds_write_addtid_b32 v23 offset:31744\n"
      "v_mfma_f32_16x16x32_bf16 v[52:55], a[60:63], a[212:215], v[52:55]\n"
      "s_nop 0\n"
      "ds_write_addtid_b32 v30 offset:28160\n"
      "ds_write_addtid_b32 v31 offset:32256\n"
      "v_fma_f32 v40, 2.0, v76, -v88\n"
      "v_mfma_f32_16x16x32_bf16 v[56:59], a[64:67], a[212:215], v[56:59]\n"
      "s_waitcnt vmcnt(19)\n"
      "v_fma_f32 v41, 2.0, v77, -v89\n"
      "v_fma_f32 v42, 2.0, v78, -v90\n"
      "v_fma_f32 v43, 2.0, v79, -v91\n"
      "v_mfma_f32_16x16x32_bf16 v[60:63], a[68:71], a[212:215], v[60:63]\n"
      "s_waitcnt lgkmcnt(0)\n"
      "v_fma_f32 v44, 2.0, v80, -v92\n"
      "v_fma_f32 v45, 2.0, v81, -v93\n"
      "s_barrier\n"
      "s_add_u32 s54, s54, 3735552\n"
      "LDloop_%=:\n"
      "v_mfma_f32_16x16x32_bf16 v[52:55], a[72:75], a[192:195], v[52:55]\n"
      "s_add_u32 s58, s40, 0\n"
      "ds_read_b128 a[204:207], v0 offset:16384\n"
      "ds_read2st64_b32 a[220:221], v10 offset0:181 offset1:183\n"
      "ds_read2st64_b32 a[222:223], v13 offset0:181 offset1:183\n"
      "ds_read2st64_b32 a[224:225], v11 offset0:181 offset1:183\n"
      "v_mfma_f32_16x16x32_bf16 v[56:59], a[76:79], a[192:195], v[56:59]\n"
      "s_mov_b32 s40, s62\n"
      "ds_read_b128 a[208:211], v0 offset:16448\n"
      "v_fma_f32 v46, 2.0, v82, -v94\n"
      "v_mfma_f32_16x16x32_bf16 v[60:63], a[80:83], a[192:195], v[60:63]\n"
      "s_add_u32 s62, s62, 786432\n"
      "ds_read2st64_b32 v[28:29], v6 offset0:0 offset1:2\n"
      "ds_read2st64_b32 a[226:227], v14 offset0:181 offset1:183\n"
      "v_mfma_f32_16x16x32_bf16 v[52:55], a[84:87], a[196:199], v[52:55]\n"
      "s_add_u32 s59, s58, 196608\n"
      "ds_read_b128 a[212:215], v0 offset:16512\n"
      "v_fma_f32 v47, 2.0, v83, -v95\n"
      "v_mfma_f32_16x16x32_bf16 v[56:59], a[88:91], a[196:199], v[56:59]\n"
      "s_nop 0\n"
      "s_waitcnt lgkmcnt(5)\n"
      "ds_read2st64_b32 v[30:31], v6 offset0:4 offset1:6\n"
      "v_fma_f32 v48, 2.0, v84, -v96\n"
      "v_mfma_f32_16x16x32_bf16 v[60:63], a[92:95], a[196:199], v[60:63]\n"
      "s_add_u32 s54, s54, 196608\n"
      "v_fma_f32 v49, 2.0, v85, -v97\n"
      "v_fma_f32 v50, 2.0, v86, -v98\n"
      "v_fma_f32 v51, 2.0, v87, -v99\n"
      "v_mfma_f32_16x16x32_bf16 v[52:55], a[96:99], a[200:203], v[52:55]\n"
      "s_add_u32 m0, s37, 95104\n"
      "buffer_store_dwordx4 a[220:223], v16, s[8:11], s58 offen offset:0 sc1\n"
      "ds_read_b128 a[192:195], v1 offset:16384\n"
      "v_mfma_f32_16x16x32_bf16 v[56:59], a[100:103], a[200:203], v[56:59]\n"
      "buffer_load_dwordx4 v8, s[4:7], s54 offen offset:0 nt lds\n"
      "buffer_load_dwordx4 v9, s[4:7], s54 offen offset:1024 nt lds\n"
      "v_mfma_f32_16x16x32_bf16 v[60:63], a[104:107], a[200:203], v[60:63]\n"
      "s_mov_b32 m0, s64\n"
      "ds_read2st64_b32 v[32:33], v6 offset0:8 offset1:10\n"
      "ds_read2st64_b32 v[34:35], v6 offset0:12 offset1:14\n"
      "v_mfma_f32_16x16x32_bf16 v[76:79], v[252:255], a[204:207], 0\n"
      "ds_read_b128 a[196:199], v1 offset:16448\n"
      "ds_read2st64_b32 v[36:37], v6 offset0:16 offset1:18\n"
      "v_mfma_f32_16x16x32_bf16 v[80:83], v[244:247], a[204:207], 0\n"
      "s_nop 0\n"
      "ds_read_b64_tr_b16 a[244:245], v7 offset:36864\n"
      "ds_read2st64_b32 a[228:229], v12 offset0:181 offset1:183\n"
      "v_mfma_f32_16x16x32_bf16 v[84:87], v[236:239], a[204:207], 0\n"
      "s_waitcnt lgkmcnt(7)\n"
      "ds_read_b64_tr_b16 a[246:247], v7 offset:36872\n"
      "ds_read2st64_b32 a[230:231], v15 offset0:181 offset1:183\n"
      "v_fmac_f32 v40, s46, v28\n"
      "v_mfma_f32_16x16x32_bf16 v[76:79], v[228:231], a[208:211], v[76:79]\n"
      "ds_read_b128 a[200:203], v1 offset:16512\n"
      "ds_read2st64_b32 v[38:39], v6 offset0:20 offset1:22\n"
      "v_mfma_f32_16x16x32_bf16 v[80:83], v[220:223], a[208:211], v[80:83]\n"
      "buffer_store_dwordx4 a[224:227], v16, s[8:11], s58 offen offset:128 sc1\n"
      "v_fmac_f32 v41, s46, v29\n"
      "v_mfma_f32_16x16x32_bf16 v[84:87], v[212:215], a[208:211], v[84:87]\n"
      "v_fmac_f32 v42, s46, v30\n"
      "v_fmac_f32 v43, s46, v31\n"
      "v_mfma_f32_16x16x32_bf16 v[76:79], v[204:207], a[212:215], v[76:79]\n"
      "s_nop 0\n"
      "ds_read_b128 a[204:207], v2 offset:16384\n"
      "v_fma_f32 v28, 0.5, v28, v64\n"
      "v_mfma_f32_16x16x32_bf16 v[80:83], v[196:199], a[212:215], v[80:83]\n"
      "v_fma_f32 v29, 0.5, v29, v65\n"
      "v_fma_f32 v30, 0.5, v30, v66\n"
      "v_fma_f32 v31, 0.5, v31, v67\n"
      "v_mfma_f32_16x16x32_bf16 v[84:87], v[188:191], a[212:215], v[84:87]\n"
      "s_waitcnt lgkmcnt(1)\n"
      "v_fmac_f32 v40, s48, v52\n"
      "v_fmac_f32_e64 v41, s48, v53\n"
      "v_mfma_f32_4x4x4_16b_bf16 v[20:23], a[216:217], a[244:245], 0\n"
      "v_fmac_f32 v42, s48, v54\n"
      "v_mfma_f32_4x4x4_16b_bf16 v[24:27], a[216:217], a[246:247], 0\n"
      "v_fmac_f32 v43, s48, v55\n"
      "v_fmac_f32 v28, s45, v40\n"
      "v_mfma_f32_16x16x32_bf16 v[76:79], v[180:183], a[192:195], v[76:79]\n"
      "ds_read_b128 a[208:211], v2 offset:16448\n"
      "v_fmac_f32 v29, s45, v41\n"
      "v_mfma_f32_16x16x32_bf16 v[80:83], v[172:175], a[192:195], v[80:83]\n"
      "v_cvt_pk_bf16_f32 v20, v20, v24\n"
      "v_cvt_pk_bf16_f32 v21, v21, v25\n"
      "v_mfma_f32_16x16x32_bf16 v[84:87], v[164:167], a[192:195], v[84:87]\n"
      "s_add_u32 s54, s54, 196608\n"
      "ds_write_addtid_b32 v20 offset:3072\n"
      "ds_write_addtid_b32 v21 offset:7168\n"
      "v_fmac_f32_e64 v30, s45, v42\n"
      "v_mfma_f32_16x16x32_bf16 v[76:79], v[156:159], a[196:199], v[76:79]\n"
      "s_add_u32 m0, s37, 99328\n"
      "ds_read_b128 a[212:215], v2 offset:16512\n"
      "v_fmac_f32 v31, s45, v43\n"
      "v_mfma_f32_16x16x32_bf16 v[80:83], v[148:151], a[196:199], v[80:83]\n"
      "buffer_load_dwordx4 v8, s[4:7], s54 offen offset:0 nt lds\n"
      "buffer_load_dwordx4 v9, s[4:7], s54 offen offset:1024 nt lds\n"
      "v_mfma_f32_16x16x32_bf16 v[84:87], v[140:143], a[196:199], v[84:87]\n"
      "s_mov_b32 m0, s64\n"
      "v_fma_f32 v40, s47, v40, -v52\n"
      "v_cvt_pk_bf16_f32 v22, v22, v26\n"
      "v_mfma_f32_16x16x32_bf16 v[76:79], v[132:135], a[200:203], v[76:79]\n"
      "buffer_store_dwordx4 a[228:231], v16, s[8:11], s58 offen offset:256 sc1\n"
      "ds_read_b128 a[192:195], v0 offset:20480\n"
      "v_mfma_f32_16x16x32_bf16 v[80:83], v[124:127], a[200:203], v[80:83]\n"
      "ds_read2st64_b32 a[232:233], v10 offset0:4 offset1:6\n"
      "ds_write_addtid_b32 v22 offset:11264\n"
      "v_fma_f32 v41, s47, v41, -v53\n"
      "v_mfma_f32_16x16x32_bf16 v[84:87], v[116:119], a[200:203], v[84:87]\n"
      "s_waitcnt lgkmcnt(3)\n"
      "ds_read2st64_b32 a[234:235], v13 offset0:4 offset1:6\n"
      "v_fma_f32 v42, s47, v42, -v54\n"
      "v_mfma_f32_16x16x32_bf16 v[76:79], v[108:111], a[204:207], v[76:79]\n"
      "ds_read_b128 a[196:199], v0 offset:20544\n"
      "v_cvt_pk_bf16_f32 v23, v23, v27\n"
      "v_mfma_f32_16x16x32_bf16 v[80:83], v[100:103], a[204:207], v[80:83]\n"
      "s_nop 0\n"
      "ds_write_addtid_b32 v23 offset:15360\n"
      "v_fma_f32 v43, s47, v43, -v55\n"
      "v_mfma_f32_16x16x32_bf16 v[84:87], a[160:163], a[204:207], v[84:87]\n"
      "v_cvt_pk_bf16_f32 v28, v28, v29\n"
      "v_cvt_pk_bf16_f32 v30, v30, v31\n"
      "v_mfma_f32_16x16x32_bf16 v[76:79], a[152:155], a[208:211], v[76:79]\n"
      "ds_read_b128 a[200:203], v0 offset:20608\n"
      "ds_write_addtid_b32 v28 offset:32768\n"
      "v_mfma_f32_16x16x32_bf16 v[80:83], a[144:147], a[208:211], v[80:83]\n"
      "ds_write_addtid_b32 v30 offset:33280\n"
      "v_cvt_pk_bf16_f32 v40, v40, v41\n"
      "v_mfma_f32_16x16x32_bf16 v[84:87], a[136:139], a[208:211], v[84:87]\n"
      "ds_write_addtid_b32 v40 offset:34816\n"
      "v_cvt_pk_bf16_f32 v42, v42, v43\n"
      "v_fmac_f32 v44, s46, v32\n"
      "v_mfma_f32_16x16x32_bf16 v[76:79], a[128:131], a[212:215], v[76:79]\n"
      "s_nop 0\n"
      "ds_read_b128 a[204:207], v1 offset:20480\n"
      "ds_write_addtid_b32 v42 offset:35328\n"
      "v_fmac_f32 v45, s46, v33\n"
      "v_mfma_f32_16x16x32_bf16 v[80:83], a[120:123], a[212:215], v[80:83]\n"
      "v_fmac_f32 v46, s46, v34\n"
      "v_fmac_f32 v47, s46, v35\n"
      "v_mfma_f32_16x16x32_bf16 v[84:87], a[112:115], a[212:215], v[84:87]\n"
      "s_waitcnt lgkmcnt(3)\n"
      "v_fma_f32 v32, 0.5, v32, v68\n"
      "v_fma_f32 v33, 0.5, v33, v69\n"
      "v_mfma_f32_16x16x32_bf16 v[88:91], v[248:251], a[192:195], v[76:79]\n"
      "ds_read_b128 a[208:211], v1 offset:20544\n"
      "v_fma_f32 v34, 0.5, v34, v70\n"
      "v_mfma_f32_16x16x32_bf16 v[92:95], v[240:243], a[192:195], v[80:83]\n"
      "v_fma_f32 v35, 0.5, v35, v71\n"
      "v_fmac_f32 v44, s48, v56\n"
      "v_mfma_f32_16x16x32_bf16 v[96:99], v[232:235], a[192:195], v[84:87]\n"
      "s_nop 0\n"
      "buffer_store_dwordx4 a[232:235], v16, s[8:11], s59 offen offset:0 sc1\n"
      "ds_read_b64_tr_b16 a[248:249], v7 offset:37376\n"
      "v_mfma_f32_16x16x32_bf16 v[88:91], v[224:227], a[196:199], v[88:91]\n"
      "ds_read_b128 a[212:215], v1 offset:20608\n"
      "ds_read_b64_tr_b16 a[250:251], v7 offset:37384\n"
      "v_fmac_f32 v45, s48, v57\n"
      "v_mfma_f32_16x16x32_bf16 v[92:95], v[216:219], a[196:199], v[92:95]\n"
      "v_fmac_f32 v46, s48, v58\n"
      "v_fmac_f32 v47, s48, v59\n"
      "v_mfma_f32_16x16x32_bf16 v[96:99], v[208:211], a[196:199], v[96:99]\n"
      "v_fmac_f32 v32, s45, v44\n"
      "v_fmac_f32_e64 v33, s45, v45\n"
      "v_mfma_f32_16x16x32_bf16 v[88:91], v[200:203], a[200:203], v[88:91]\n"
      "ds_read_b128 a[192:195], v2 offset:20480\n"
      "v_fmac_f32 v34, s45, v46\n"
      "v_mfma_f32_16x16x32_bf16 v[92:95], v[192:195], a[200:203], v[92:95]\n"
      "v_fmac_f32_e64 v35, s45, v47\n"
      "v_fma_f32 v44, s47, v44, -v56\n"
      "v_mfma_f32_16x16x32_bf16 v[96:99], v[184:187], a[200:203], v[96:99]\n"
      "s_waitcnt lgkmcnt(1)\n"
      "v_fma_f32 v45, s47, v45, -v57\n"
      "v_fma_f32 v46, s47, v46, -v58\n"
      "v_mfma_f32_16x16x32_bf16 v[88:91], v[176:179], a[204:207], v[88:91]\n"
      "ds_read_b128 a[196:199], v2 offset:20544\n"
      "v_fma_f32 v47, s47, v47, -v59\n"
      "v_mfma_f32_4x4x4_16b_bf16 v[20:23], a[216:217], a[248:249], 0\n"
      "v_cvt_pk_bf16_f32 v32, v32, v33\n"
      "v_mfma_f32_4x4x4_16b_bf16 v[24:27], a[216:217], a[250:251], 0\n"
      "ds_write_addtid_b32 v32 offset:40964\n"
      "v_cvt_pk_bf16_f32 v34, v34, v35\n"
      "v_mfma_f32_16x16x32_bf16 v[92:95], v[168:171], a[204:207], v[92:95]\n"
      "ds_write_addtid_b32 v34 offset:41476\n"
      "v_cvt_pk_bf16_f32 v44, v44, v45\n"
      "v_cvt_pk_bf16_f32 v20, v20, v24\n"
      "v_mfma_f32_16x16x32_bf16 v[96:99], v[160:163], a[204:207], v[96:99]\n"
      "s_add_u32 s54, s54, 196608\n"
      "ds_write_addtid_b32 v44 offset:43012\n"
      "ds_write_addtid_b32 v20 offset:3584\n"
      "v_cvt_pk_bf16_f32 v46, v46, v47\n"
      "v_mfma_f32_16x16x32_bf16 v[88:91], v[152:155], a[208:211], v[88:91]\n"
      "s_add_u32 m0, s37, 103432\n"
      "ds_read_b128 a[200:203], v2 offset:20608\n"
      "ds_read2st64_b32 a[236:237], v11 offset0:4 offset1:6\n"
      "v_mfma_f32_16x16x32_bf16 v[92:95], v[144:147], a[208:211], v[92:95]\n"
      "buffer_load_dwordx4 v8, s[4:7], s54 offen offset:0 nt lds\n"
      "buffer_load_dwordx4 v9, s[4:7], s54 offen offset:1024 nt lds\n"
      "ds_read2st64_b32 a[238:239], v14 offset0:4 offset1:6\n"
      "v_mfma_f32_16x16x32_bf16 v[96:99], v[136:139], a[208:211], v[96:99]\n"
      "s_mov_b32 m0, s64\n"
      "v_fmac_f32 v48, s46, v36\n"
      "v_fmac_f32 v49, s46, v37\n"
      "v_mfma_f32_16x16x32_bf16 v[88:91], v[128:131], a[212:215], v[88:91]\n"
      "s_nop 0\n"
      "ds_read_b128 a[204:207], v3 offset:16896\n"
      "ds_write_addtid_b32 v46 offset:43524\n"
      "v_mfma_f32_16x16x32_bf16 v[92:95], v[120:123], a[212:215], v[92:95]\n"
      "v_fmac_f32 v50, s46, v38\n"
      "v_cvt_pk_bf16_f32 v21, v21, v25\n"
      "v_fmac_f32 v51, s46, v39\n"
      "v_mfma_f32_16x16x32_bf16 v[96:99], v[112:115], a[212:215], v[96:99]\n"
      "s_waitcnt lgkmcnt(3)\n"
      "ds_write_addtid_b32 v21 offset:7680\n"
      "v_cvt_pk_bf16_f32 v22, v22, v26\n"
      "v_mfma_f32_16x16x32_bf16 v[88:91], v[104:107], a[192:195], v[88:91]\n"
      "s_nop 0\n"
      "ds_read_b128 a[208:211], v3 offset:16960\n"
      "ds_write_addtid_b32 v22 offset:11776\n"
      "v_mfma_f32_16x16x32_bf16 v[92:95], a[164:167], a[192:195], v[92:95]\n"
      "v_fma_f32 v36, 0.5, v36, v72\n"
      "v_fma_f32 v37, 0.5, v37, v73\n"
      "v_mfma_f32_16x16x32_bf16 v[96:99], a[156:159], a[192:195], v[96:99]\n"
      "ds_read2st64_b32 a[240:241], v12 offset0:4 offset1:6\n"
      "ds_read2st64_b32 a[242:243], v15 offset0:4 offset1:6\n"
      "v_fma_f32 v38, 0.5, v38, v74\n"
      "v_mfma_f32_16x16x32_bf16 v[88:91], a[148:151], a[196:199], v[88:91]\n"
      "ds_read_b128 a[212:215], v3 offset:17024\n"
      "v_cvt_pk_bf16_f32 v23, v23, v27\n"
      "v_mfma_f32_16x16x32_bf16 v[92:95], a[140:143], a[196:199], v[92:95]\n"
      "ds_write_addtid_b32 v23 offset:15872\n"
      "v_fma_f32 v39, 0.5, v39, v75\n"
      "v_mfma_f32_16x16x32_bf16 v[96:99], a[132:135], a[196:199], v[96:99]\n"
      "s_waitcnt lgkmcnt(4)\n"
      "v_fmac_f32 v48, s48, v60\n"
      "v_fmac_f32 v49, s48, v61\n"
      "v_mfma_f32_16x16x32_bf16 v[88:91], a[124:127], a[200:203], v[88:91]\n"
      "s_add_u32 s54, s54, 196608\n"
      "ds_read_b128 a[192:195], v4 offset:16896\n"
      "v_fmac_f32 v50, s48, v62\n"
      "v_mfma_f32_16x16x32_bf16 v[92:95], a[116:119], a[200:203], v[92:95]\n"
      "s_add_u32 m0, s37, 107656\n"
      "v_fmac_f32 v51, s48, v63\n"
      "v_fmac_f32 v36, s45, v48\n"
      "v_mfma_f32_16x16x32_bf16 v[96:99], a[108:111], a[200:203], v[96:99]\n"
      "buffer_store_dwordx4 a[236:239], v16, s[8:11], s59 offen offset:128 sc1\n"
      "buffer_load_dwordx4 v8, s[4:7], s54 offen offset:0 nt lds\n"
      "buffer_load_dwordx4 v9, s[4:7], s54 offen offset:1024 nt lds\n"
      "v_mfma_f32_16x16x32_bf16 v[64:67], a[0:3], a[204:207], 0\n"
      "s_mov_b32 m0, s64\n"
      "ds_read_b128 a[196:199], v4 offset:16960\n"
      "ds_write_addtid_b32 v88 offset:51456\n"
      "v_fmac_f32 v37, s45, v49\n"
      "v_mfma_f32_16x16x32_bf16 v[68:71], a[4:7], a[204:207], 0\n"
      "ds_write_addtid_b32 v89 offset:51968\n"
      "ds_write_addtid_b32 v90 offset:52480\n"
      "v_mfma_f32_16x16x32_bf16 v[72:75], a[8:11], a[204:207], 0\n"
      "ds_write_addtid_b32 v91 offset:52992\n"
      "ds_write_addtid_b32 v92 offset:53504\n"
      "v_fmac_f32 v38, s45, v50\n"
      "v_mfma_f32_16x16x32_bf16 v[64:67], a[12:15], a[208:211], v[64:67]\n"
      "s_nop 0\n"
      "ds_read_b128 a[200:203], v4 offset:17024\n"
      "ds_write_addtid_b32 v93 offset:54016\n"
      "v_mfma_f32_16x16x32_bf16 v[68:71], a[16:19], a[208:211], v[68:71]\n"
      "s_nop 0\n"
      "ds_write_addtid_b32 v94 offset:54528\n"
      "ds_write_addtid_b32 v95 offset:55040\n"
      "v_mfma_f32_16x16x32_bf16 v[72:75], a[20:23], a[208:211], v[72:75]\n"
      "s_waitcnt lgkmcnt(7)\n"
      "ds_write_addtid_b32 v96 offset:55552\n"
      "ds_write_addtid_b32 v97 offset:56064\n"
      "v_fmac_f32 v39, s45, v51\n"
      "v_mfma_f32_16x16x32_bf16 v[64:67], a[24:27], a[212:215], v[64:67]\n"
      "ds_read_b128 a[204:207], v5 offset:16896\n"
      "ds_write_addtid_b32 v98 offset:56576\n"
      "v_mfma_f32_16x16x32_bf16 v[68:71], a[28:31], a[212:215], v[68:71]\n"
      "buffer_store_dwordx4 a[240:243], v16, s[8:11], s59 offen offset:256 sc1\n"
      "v_mfma_f32_16x16x32_bf16 v[72:75], a[32:35], a[212:215], v[72:75]\n"
      "s_nop 0\n"
      "ds_write_addtid_b32 v99 offset:57088\n"
      "v_fma_f32 v48, s47, v48, -v60\n"
      "v_mfma_f32_16x16x32_bf16 v[64:67], a[36:39], a[192:195], v[64:67]\n"
      "ds_read_b128 a[208:211], v5 offset:16960\n"
      "v_fma_f32 v49, s47, v49, -v61\n"
      "v_fma_f32 v50, s47, v50, -v62\n"
      "v_fma_f32 v51, s47, v51, -v63\n"
      "v_cvt_pk_bf16_f32 v36, v36, v37\n"
      "v_cvt_pk_bf16_f32 v38, v38, v39\n"
      "v_cvt_pk_bf16_f32 v48, v48, v49\n"
      "v_cvt_pk_bf16_f32 v50, v50, v51\n"
      "v_mfma_f32_16x16x32_bf16 v[68:71], a[40:43], a[192:195], v[68:71]\n"
      "ds_write_addtid_b32 v36 offset:36864\n"
      "ds_write_addtid_b32 v38 offset:37376\n"
      "v_mfma_f32_16x16x32_bf16 v[72:75], a[44:47], a[192:195], v[72:75]\n"
      "ds_write_addtid_b32 v48 offset:38912\n"
      "ds_write_addtid_b32 v50 offset:39424\n"
      "v_mfma_f32_16x16x32_bf16 v[64:67], a[48:51], a[196:199], v[64:67]\n"
      "ds_read_b128 a[212:215], v5 offset:17024\n"
      "v_fma_f32 v40, 2.0, v76, -v88\n"
      "v_mfma_f32_16x16x32_bf16 v[68:71], a[52:55], a[196:199], v[68:71]\n"
      "v_fma_f32 v41, 2.0, v77, -v89\n"
      "v_fma_f32 v42, 2.0, v78, -v90\n"
      "v_mfma_f32_16x16x32_bf16 v[72:75], a[56:59], a[196:199], v[72:75]\n"
      "s_waitcnt lgkmcnt(5)\n"
      "v_fma_f32 v43, 2.0, v79, -v91\n"
      "v_fma_f32 v44, 2.0, v80, -v92\n"
      "v_mfma_f32_16x16x32_bf16 v[64:67], a[60:63], a[200:203], v[64:67]\n"
      "v_fma_f32 v45, 2.0, v81, -v93\n"
      "v_mfma_f32_16x16x32_bf16 v[68:71], a[64:67], a[200:203], v[68:71]\n"
      "s_waitcnt vmcnt(25)\n"
      "v_mfma_f32_16x16x32_bf16 v[72:75], a[68:71], a[200:203], v[72:75]\n"
      "s_waitcnt lgkmcnt(0)\n"
      "s_barrier\n"
      "LDmid_%=:\n"
      "v_mfma_f32_16x16x32_bf16 v[64:67], a[72:75], a[204:207], v[64:67]\n"
      "s_add_u32 s58, s40, 0\n"
      "ds_read_b128 a[192:195], v0 offset:0\n"
      "ds_read2st64_b32 a[220:221], v10 offset0:177 offset1:179\n"
      "ds_read2st64_b32 a[222:223], v13 offset0:177 offset1:179\n"
      "ds_read2st64_b32 a[224:225], v11 offset0:177 offset1:179\n"
      "v_mfma_f32_16x16x32_bf16 v[68:71], a[76:79], a[204:207], v[68:71]\n"
      "s_mov_b32 s40, s62\n"
      "ds_read_b128 a[196:199], v0 offset:64\n"
      "v_fma_f32 v46, 2.0, v82, -v94\n"
      "v_mfma_f32_16x16x32_bf16 v[72:75], a[80:83], a[204:207], v[72:75]\n"
      "s_add_u32 s62, s62, 786432\n"
      "ds_read2st64_b32 v[28:29], v6 offset0:24 offset1:26\n"
      "ds_read2st64_b32 a[226:227], v14 offset0:177 offset1:179\n"
      "v_mfma_f32_16x16x32_bf16 v[64:67], a[84:87], a[208:211], v[64:67]\n"
      "s_add_u32 s59, s58, 196608\n"
      "ds_read_b128 a[200:203], v0 offset:128\n"
      "v_fma_f32 v47, 2.0, v83, -v95\n"
      "v_mfma_f32_16x16x32_bf16 v[68:71], a[88:91], a[208:211], v[68:71]\n"
      "s_nop 0\n"
      "s_waitcnt lgkmcnt(5)\n"
      "ds_read2st64_b32 v[30:31], v6 offset0:28 offset1:30\n"
      "v_fma_f32 v48, 2.0, v84, -v96\n"
      "v_mfma_f32_16x16x32_bf16 v[72:75], a[92:95], a[208:211], v[72:75]\n"
      "s_add_u32 s54, s54, 196608\n"
      "v_fma_f32 v49, 2.0, v85, -v97\n"
      "v_fma_f32 v50, 2.0, v86, -v98\n"
      "v_fma_f32 v51, 2.0, v87, -v99\n"
      "v_mfma_f32_16x16x32_bf16 v[64:67], a[96:99], a[212:215], v[64:67]\n"
      "s_add_u32 m0, s37, 112000\n"
      "buffer_store_dwordx4 a[220:223], v16, s[8:11], s58 offen offset:0 sc1\n"
      "ds_read_b128 a[204:207], v1 offset:0\n"
      "v_mfma_f32_16x16x32_bf16 v[68:71], a[100:103], a[212:215], v[68:71]\n"
      "buffer_load_dwordx4 v8, s[4:7], s54 offen offset:0 nt lds\n"
      "buffer_load_dwordx4 v9, s[4:7], s54 offen offset:1024 nt lds\n"
      "v_mfma_f32_16x16x32_bf16 v[72:75], a[104:107], a[212:215], v[72:75]\n"
      "s_mov_b32 m0, s64\n"
      "ds_read2st64_b32 v[32:33], v6 offset0:32 offset1:34\n"
      "ds_read2st64_b32 v[34:35], v6 offset0:36 offset1:38\n"
      "v_mfma_f32_16x16x32_bf16 v[76:79], v[252:255], a[192:195], 0\n"
      "s_nop 0\n"
      "ds_read_b128 a[208:211], v1 offset:64\n"
      "ds_read2st64_b32 v[36:37], v6 offset0:40 offset1:42\n"
      "v_mfma_f32_16x16x32_bf16 v[80:83], v[244:247], a[192:195], 0\n"
      "ds_read_b64_tr_b16 a[244:245], v7 offset:53760\n"
      "ds_read2st64_b32 a[228:229], v12 offset0:177 offset1:179\n"
      "v_mfma_f32_16x16x32_bf16 v[84:87], v[236:239], a[192:195], 0\n"
      "s_waitcnt lgkmcnt(7)\n"
      "ds_read_b64_tr_b16 a[246:247], v7 offset:53768\n"
      "ds_read2st64_b32 a[230:231], v15 offset0:177 offset1:179\n"
      "v_fmac_f32 v40, s46, v28\n"
      "v_mfma_f32_16x16x32_bf16 v[76:79], v[228:231], a[196:199], v[76:79]\n"
      "ds_read_b128 a[212:215], v1 offset:128\n"
      "ds_read2st64_b32 v[38:39], v6 offset0:44 offset1:46\n"
      "v_mfma_f32_16x16x32_bf16 v[80:83], v[220:223], a[196:199], v[80:83]\n"
      "buffer_store_dwordx4 a[224:227], v16, s[8:11], s58 offen offset:128 sc1\n"
      "v_fmac_f32 v41, s46, v29\n"
      "v_mfma_f32_16x16x32_bf16 v[84:87], v[212:215], a[196:199], v[84:87]\n"
      "v_fmac_f32 v42, s46, v30\n"
      "v_fmac_f32 v43, s46, v31\n"
      "v_mfma_f32_16x16x32_bf16 v[76:79], v[204:207], a[200:203], v[76:79]\n"
      "s_nop 0\n"
      "ds_read_b128 a[192:195], v2 offset:0\n"
      "v_fma_f32 v28, 0.5, v28, v52\n"
      "v_mfma_f32_16x16x32_bf16 v[80:83], v[196:199], a[200:203], v[80:83]\n"
      "v_fma_f32 v29, 0.5, v29, v53\n"
      "v_fma_f32 v30, 0.5, v30, v54\n"
      "v_fma_f32 v31, 0.5, v31, v55\n"
      "v_mfma_f32_16x16x32_bf16 v[84:87], v[188:191], a[200:203], v[84:87]\n"
      "s_waitcnt lgkmcnt(1)\n"
      "v_fmac_f32 v40, s48, v64\n"
      "v_fmac_f32 v41, s48, v65\n"
      "v_mfma_f32_4x4x4_16b_bf16 v[20:23], a[216:217], a[244:245], 0\n"
      "v_fmac_f32 v42, s48, v66\n"
      "v_mfma_f32_4x4x4_16b_bf16 v[24:27], a[216:217], a[246:247], 0\n"
      "v_fmac_f32 v43, s48, v67\n"
      "v_fmac_f32 v28, s45, v40\n"
      "v_mfma_f32_16x16x32_bf16 v[76:79], v[180:183], a[204:207], v[76:79]\n"
      "ds_read_b128 a[196:199], v2 offset:64\n"
      "v_fmac_f32 v29, s45, v41\n"
      "v_mfma_f32_16x16x32_bf16 v[80:83], v[172:175], a[204:207], v[80:83]\n"
      "s_nop 0\n"
      "v_cvt_pk_bf16_f32 v20, v20, v24\n"
      "v_cvt_pk_bf16_f32 v21, v21, v25\n"
      "v_mfma_f32_16x16x32_bf16 v[84:87], v[164:167], a[204:207], v[84:87]\n"
      "s_add_u32 s54, s54, 196608\n"
      "ds_write_addtid_b32 v20 offset:19456\n"
      "ds_write_addtid_b32 v21 offset:23552\n"
      "v_fmac_f32_e64 v30, s45, v42\n"
      "v_mfma_f32_16x16x32_bf16 v[76:79], v[156:159], a[208:211], v[76:79]\n"
      "s_add_u32 m0, s37, 116224\n"
      "ds_read_b128 a[200:203], v2 offset:128\n"
      "v_fmac_f32 v31, s45, v43\n"
      "v_mfma_f32_16x16x32_bf16 v[80:83], v[148:151], a[208:211], v[80:83]\n"
      "buffer_load_dwordx4 v8, s[4:7], s54 offen offset:0 nt lds\n"
      "buffer_load_dwordx4 v9, s[4:7], s54 offen offset:1024 nt lds\n"
      "v_mfma_f32_16x16x32_bf16 v[84:87], v[140:143], a[208:211], v[84:87]\n"
      "s_mov_b32 m0, s64\n"
      "v_fma_f32 v40, s47, v40, -v64\n"
      "v_cvt_pk_bf16_f32 v22, v22, v26\n"
      "v_mfma_f32_16x16x32_bf16 v[76:79], v[132:135], a[212:215], v[76:79]\n"
      "buffer_store_dwordx4 a[228:231], v16, s[8:11], s58 offen offset:256 sc1\n"
      "ds_read_b128 a[204:207], v0 offset:4096\n"
      "v_mfma_f32_16x16x32_bf16 v[80:83], v[124:127], a[212:215], v[80:83]\n"
      "ds_read2st64_b32 a[232:233], v10 offset0:0 offset1:2\n"
      "ds_write_addtid_b32 v22 offset:27648\n"
      "v_fma_f32 v41, s47, v41, -v65\n"
      "v_mfma_f32_16x16x32_bf16 v[84:87], v[116:119], a[212:215], v[84:87]\n"
      "s_waitcnt lgkmcnt(3)\n"
      "ds_read2st64_b32 a[234:235], v13 offset0:0 offset1:2\n"
      "v_fma_f32 v42, s47, v42, -v66\n"
      "v_mfma_f32_16x16x32_bf16 v[76:79], v[108:111], a[192:195], v[76:79]\n"
      "ds_read_b128 a[208:211], v0 offset:4160\n"
      "v_cvt_pk_bf16_f32 v23, v23, v27\n"
      "v_mfma_f32_16x16x32_bf16 v[80:83], v[100:103], a[192:195], v[80:83]\n"
      "s_nop 0\n"
      "ds_write_addtid_b32 v23 offset:31744\n"
      "v_fma_f32 v43, s47, v43, -v67\n"
      "v_mfma_f32_16x16x32_bf16 v[84:87], a[160:163], a[192:195], v[84:87]\n"
      "v_cvt_pk_bf16_f32 v28, v28, v29\n"
      "v_cvt_pk_bf16_f32 v30, v30, v31\n"
      "v_mfma_f32_16x16x32_bf16 v[76:79], a[152:155], a[196:199], v[76:79]\n"
      "ds_read_b128 a[212:215], v0 offset:4224\n"
      "ds_write_addtid_b32 v28 offset:33792\n"
      "v_mfma_f32_16x16x32_bf16 v[80:83], a[144:147], a[196:199], v[80:83]\n"
      "ds_write_addtid_b32 v30 offset:34304\n"
      "v_cvt_pk_bf16_f32 v40, v40, v41\n"
      "v_mfma_f32_16x16x32_bf16 v[84:87], a[136:139], a[196:199], v[84:87]\n"
      "ds_write_addtid_b32 v40 offset:35840\n"
      "v_cvt_pk_bf16_f32 v42, v42, v43\n"
      "v_fmac_f32 v44, s46, v32\n"
      "v_mfma_f32_16x16x32_bf16 v[76:79], a[128:131], a[200:203], v[76:79]\n"
      "s_nop 0\n"
      "ds_read_b128 a[192:195], v1 offset:4096\n"
      "ds_write_addtid_b32 v42 offset:36352\n"
      "v_fmac_f32 v45, s46, v33\n"
      "v_mfma_f32_16x16x32_bf16 v[80:83], a[120:123], a[200:203], v[80:83]\n"
      "v_fmac_f32 v46, s46, v34\n"
      "v_fmac_f32 v47, s46, v35\n"
      "v_mfma_f32_16x16x32_bf16 v[84:87], a[112:115], a[200:203], v[84:87]\n"
      "s_waitcnt lgkmcnt(3)\n"
      "v_fma_f32 v32, 0.5, v32, v56\n"
      "v_fma_f32 v33, 0.5, v33, v57\n"
      "v_mfma_f32_16x16x32_bf16 v[88:91], v[248:251], a[204:207], v[76:79]\n"
      "ds_read_b128 a[196:199], v1 offset:4160\n"
      "v_fma_f32 v34, 0.5, v34, v58\n"
      "v_mfma_f32_16x16x32_bf16 v[92:95], v[240:243], a[204:207], v[80:83]\n"
      "v_fma_f32 v35, 0.5, v35, v59\n"
      "v_fmac_f32 v44, s48, v68\n"
      "v_mfma_f32_16x16x32_bf16 v[96:99], v[232:235], a[204:207], v[84:87]\n"
      "s_nop 0\n"
      "buffer_store_dwordx4 a[232:235], v16, s[8:11], s59 offen offset:0 sc1\n"
      "ds_read_b64_tr_b16 a[248:249], v7 offset:54272\n"
      "v_mfma_f32_16x16x32_bf16 v[88:91], v[224:227], a[208:211], v[88:91]\n"
      "ds_read_b128 a[200:203], v1 offset:4224\n"
      "ds_read_b64_tr_b16 a[250:251], v7 offset:54280\n"
      "v_fmac_f32 v45, s48, v69\n"
      "v_mfma_f32_16x16x32_bf16 v[92:95], v[216:219], a[208:211], v[92:95]\n"
      "v_fmac_f32 v46, s48, v70\n"
      "v_fmac_f32 v47, s48, v71\n"
      "v_mfma_f32_16x16x32_bf16 v[96:99], v[208:211], a[208:211], v[96:99]\n"
      "v_fmac_f32 v32, s45, v44\n"
      "v_fmac_f32 v33, s45, v45\n"
      "v_mfma_f32_16x16x32_bf16 v[88:91], v[200:203], a[212:215], v[88:91]\n"
      "ds_read_b128 a[204:207], v2 offset:4096\n"
      "v_fmac_f32 v34, s45, v46\n"
      "v_mfma_f32_16x16x32_bf16 v[92:95], v[192:195], a[212:215], v[92:95]\n"
      "v_fmac_f32 v35, s45, v47\n"
      "v_fma_f32 v44, s47, v44, -v68\n"
      "v_mfma_f32_16x16x32_bf16 v[96:99], v[184:187], a[212:215], v[96:99]\n"
      "s_waitcnt lgkmcnt(1)\n"
      "v_fma_f32 v45, s47, v45, -v69\n"
      "v_fma_f32 v46, s47, v46, -v70\n"
      "v_mfma_f32_16x16x32_bf16 v[88:91], v[176:179], a[192:195], v[88:91]\n"
      "ds_read_b128 a[208:211], v2 offset:4160\n"
      "v_fma_f32 v47, s47, v47, -v71\n"
      "v_mfma_f32_4x4x4_16b_bf16 v[20:23], a[216:217], a[248:249], 0\n"
      "v_cvt_pk_bf16_f32 v32, v32, v33\n"
      "v_mfma_f32_4x4x4_16b_bf16 v[24:27], a[216:217], a[250:251], 0\n"
      "ds_write_addtid_b32 v32 offset:41988\n"
      "v_cvt_pk_bf16_f32 v34, v34, v35\n"
      "v_mfma_f32_16x16x32_bf16 v[92:95], v[168:171], a[192:195], v[92:95]\n"
      "ds_write_addtid_b32 v34 offset:42500\n"
      "v_cvt_pk_bf16_f32 v44, v44, v45\n"
      "v_cvt_pk_bf16_f32 v20, v20, v24\n"
      "v_mfma_f32_16x16x32_bf16 v[96:99], v[160:163], a[192:195], v[96:99]\n"
      "s_add_u32 s54, s54, 196608\n"
      "ds_write_addtid_b32 v44 offset:44036\n"
      "ds_write_addtid_b32 v20 offset:19968\n"
      "v_cvt_pk_bf16_f32 v46, v46, v47\n"
      "v_mfma_f32_16x16x32_bf16 v[88:91], v[152:155], a[196:199], v[88:91]\n"
      "s_add_u32 m0, s37, 120328\n"
      "ds_read_b128 a[212:215], v2 offset:4224\n"
      "ds_read2st64_b32 a[236:237], v11 offset0:0 offset1:2\n"
      "v_mfma_f32_16x16x32_bf16 v[92:95], v[144:147], a[196:199], v[92:95]\n"
      "buffer_load_dwordx4 v8, s[4:7], s54 offen offset:0 nt lds\n"
      "buffer_load_dwordx4 v9, s[4:7], s54 offen offset:1024 nt lds\n"
      "ds_read2st64_b32 a[238:239], v14 offset0:0 offset1:2\n"
      "v_mfma_f32_16x16x32_bf16 v[96:99], v[136:139], a[196:199], v[96:99]\n"
      "s_mov_b32 m0, s64\n"
      "v_fmac_f32 v48, s46, v36\n"
      "v_fmac_f32 v49, s46, v37\n"
      "v_mfma_f32_16x16x32_bf16 v[88:91], v[128:131], a[200:203], v[88:91]\n"
      "s_nop 0\n"
      "ds_read_b128 a[192:195], v3 offset:33792\n"
      "ds_write_addtid_b32 v46 offset:44548\n"
      "v_mfma_f32_16x16x32_bf16 v[92:95], v[120:123], a[200:203], v[92:95]\n"
      "v_fmac_f32 v50, s46, v38\n"
      "v_cvt_pk_bf16_f32 v21, v21, v25\n"
      "v_fmac_f32 v51, s46, v39\n"
      "v_mfma_f32_16x16x32_bf16 v[96:99], v[112:115], a[200:203], v[96:99]\n"
      "s_waitcnt lgkmcnt(3)\n"
      "ds_write_addtid_b32 v21 offset:24064\n"
      "v_cvt_pk_bf16_f32 v22, v22, v26\n"
      "v_mfma_f32_16x16x32_bf16 v[88:91], v[104:107], a[204:207], v[88:91]\n"
      "s_nop 0\n"
      "ds_read_b128 a[196:199], v3 offset:33856\n"
      "ds_write_addtid_b32 v22 offset:28160\n"
      "v_mfma_f32_16x16x32_bf16 v[92:95], a[164:167], a[204:207], v[92:95]\n"
      "v_fma_f32 v36, 0.5, v36, v60\n"
      "v_fma_f32 v37, 0.5, v37, v61\n"
      "v_mfma_f32_16x16x32_bf16 v[96:99], a[156:159], a[204:207], v[96:99]\n"
      "ds_read2st64_b32 a[240:241], v12 offset0:0 offset1:2\n"
      "ds_read2st64_b32 a[242:243], v15 offset0:0 offset1:2\n"
      "v_fma_f32 v38, 0.5, v38, v62\n"
      "v_mfma_f32_16x16x32_bf16 v[88:91], a[148:151], a[208:211], v[88:91]\n"
      "ds_read_b128 a[200:203], v3 offset:33920\n"
      "v_cvt_pk_bf16_f32 v23, v23, v27\n"
      "v_mfma_f32_16x16x32_bf16 v[92:95], a[140:143], a[208:211], v[92:95]\n"
      "ds_write_addtid_b32 v23 offset:32256\n"
      "v_fma_f32 v39, 0.5, v39, v63\n"
      "v_mfma_f32_16x16x32_bf16 v[96:99], a[132:135], a[208:211], v[96:99]\n"
      "s_waitcnt lgkmcnt(4)\n"
      "v_fmac_f32 v48, s48, v72\n"
      "v_fmac_f32 v49, s48, v73\n"
      "v_mfma_f32_16x16x32_bf16 v[88:91], a[124:127], a[212:215], v[88:91]\n"
      "s_add_u32 s54, s54, 196608\n"
      "ds_read_b128 a[204:207], v4 offset:33792\n"
      "v_fmac_f32 v50, s48, v74\n"
      "v_mfma_f32_16x16x32_bf16 v[92:95], a[116:119], a[212:215], v[92:95]\n"
      "s_add_u32 m0, s37, 124552\n"
      "v_fmac_f32 v51, s48, v75\n"
      "v_fmac_f32 v36, s45, v48\n"
      "v_mfma_f32_16x16x32_bf16 v[96:99], a[108:111], a[212:215], v[96:99]\n"
      "buffer_store_dwordx4 a[236:239], v16, s[8:11], s59 offen offset:128 sc1\n"
      "buffer_load_dwordx4 v8, s[4:7], s54 offen offset:0 nt lds\n"
      "buffer_load_dwordx4 v9, s[4:7], s54 offen offset:1024 nt lds\n"
      "v_mfma_f32_16x16x32_bf16 v[52:55], a[0:3], a[192:195], 0\n"
      "s_mov_b32 m0, s64\n"
      "ds_read_b128 a[208:211], v4 offset:33856\n"
      "ds_write_addtid_b32 v88 offset:45312\n"
      "v_fmac_f32 v37, s45, v49\n"
      "v_mfma_f32_16x16x32_bf16 v[56:59], a[4:7], a[192:195], 0\n"
      "ds_write_addtid_b32 v89 offset:45824\n"
      "ds_write_addtid_b32 v90 offset:46336\n"
      "v_mfma_f32_16x16x32_bf16 v[60:63], a[8:11], a[192:195], 0\n"
      "ds_write_addtid_b32 v91 offset:46848\n"
      "ds_write_addtid_b32 v92 offset:47360\n"
      "v_fmac_f32 v38, s45, v50\n"
      "v_mfma_f32_16x16x32_bf16 v[52:55], a[12:15], a[196:199], v[52:55]\n"
      "s_nop 0\n"
      "ds_read_b128 a[212:215], v4 offset:33920\n"
      "ds_write_addtid_b32 v93 offset:47872\n"
      "v_mfma_f32_16x16x32_bf16 v[56:59], a[16:19], a[196:199], v[56:59]\n"
      "s_nop 0\n"
      "ds_write_addtid_b32 v94 offset:48384\n"
      "ds_write_addtid_b32 v95 offset:48896\n"
      "v_mfma_f32_16x16x32_bf16 v[60:63], a[20:23], a[196:199], v[60:63]\n"
      "s_waitcnt lgkmcnt(7)\n"
      "ds_write_addtid_b32 v96 offset:49408\n"
      "ds_write_addtid_b32 v97 offset:49920\n"
      "v_fmac_f32 v39, s45, v51\n"
      "v_mfma_f32_16x16x32_bf16 v[52:55], a[24:27], a[200:203], v[52:55]\n"
      "ds_read_b128 a[192:195], v5 offset:33792\n"
      "ds_write_addtid_b32 v98 offset:50432\n"
      "v_mfma_f32_16x16x32_bf16 v[56:59], a[28:31], a[200:203], v[56:59]\n"
      "buffer_store_dwordx4 a[240:243], v16, s[8:11], s59 offen offset:256 sc1\n"
      "v_mfma_f32_16x16x32_bf16 v[60:63], a[32:35], a[200:203], v[60:63]\n"
      "s_nop 0\n"
      "ds_write_addtid_b32 v99 offset:50944\n"
      "v_fma_f32 v48, s47, v48, -v72\n"
      "v_mfma_f32_16x16x32_bf16 v[52:55], a[36:39], a[204:207], v[52:55]\n"
      "ds_read_b128 a[196:199], v5 offset:33856\n"
      "v_fma_f32 v49, s47, v49, -v73\n"
      "v_fma_f32 v50, s47, v50, -v74\n"
      "v_fma_f32 v51, s47, v51, -v75\n"
      "v_cvt_pk_bf16_f32 v36, v36, v37\n"
      "v_cvt_pk_bf16_f32 v38, v38, v39\n"
      "v_cvt_pk_bf16_f32 v48, v48, v49\n"
      "v_cvt_pk_bf16_f32 v50, v50, v51\n"
      "v_mfma_f32_16x16x32_bf16 v[56:59], a[40:43], a[204:207], v[56:59]\n"
      "ds_write_addtid_b32 v36 offset:37888\n"
      "ds_write_addtid_b32 v38 offset:38400\n"
      "v_mfma_f32_16x16x32_bf16 v[60:63], a[44:47], a[204:207], v[60:63]\n"
      "ds_write_addtid_b32 v48 offset:39936\n"
      "ds_write_addtid_b32 v50 offset:40448\n"
      "v_mfma_f32_16x16x32_bf16 v[52:55], a[48:51], a[208:211], v[52:55]\n"
      "ds_read_b128 a[200:203], v5 offset:33920\n"
      "v_fma_f32 v40, 2.0, v76, -v88\n"
      "v_mfma_f32_16x16x32_bf16 v[56:59], a[52:55], a[208:211], v[56:59]\n"
      "v_fma_f32 v41, 2.0, v77, -v89\n"
      "v_fma_f32 v42, 2.0, v78, -v90\n"
      "v_mfma_f32_16x16x32_bf16 v[60:63], a[56:59], a[208:211], v[60:63]\n"
      "s_waitcnt lgkmcnt(5)\n"
      "v_fma_f32 v43, 2.0, v79, -v91\n"
      "v_fma_f32 v44, 2.0, v80, -v92\n"
      "v_mfma_f32_16x16x32_bf16 v[52:55], a[60:63], a[212:215], v[52:55]\n"
      "v_fma_f32 v45, 2.0, v81, -v93\n"
      "v_mfma_f32_16x16x32_bf16 v[56:59], a[64:67], a[212:215], v[56:59]\n"
      "s_waitcnt vmcnt(25)\n"
      "v_mfma_f32_16x16x32_bf16 v[60:63], a[68:71], a[212:215], v[60:63]\n"
      "s_waitcnt lgkmcnt(0)\n"
      "s_barrier\n"
      "v_mfma_f32_16x16x32_bf16 v[52:55], a[72:75], a[192:195], v[52:55]\n"
      "s_add_u32 s58, s40, 0\n"
      "ds_read_b128 a[204:207], v0 offset:16384\n"
      "ds_read2st64_b32 a[220:221], v10 offset0:181 offset1:183\n"
      "ds_read2st64_b32 a[222:223], v13 offset0:181 offset1:183\n"
      "ds_read2st64_b32 a[224:225], v11 offset0:181 offset1:183\n"
      "v_mfma_f32_16x16x32_bf16 v[56:59], a[76:79], a[192:195], v[56:59]\n"
      "s_mov_b32 s40, s62\n"
      "ds_read_b128 a[208:211], v0 offset:16448\n"
      "v_fma_f32 v46, 2.0, v82, -v94\n"
      "v_mfma_f32_16x16x32_bf16 v[60:63], a[80:83], a[192:195], v[60:63]\n"
      "s_add_u32 s62, s62, 786432\n"
      "ds_read2st64_b32 v[28:29], v6 offset0:0 offset1:2\n"
      "ds_read2st64_b32 a[226:227], v14 offset0:181 offset1:183\n"
      "v_mfma_f32_16x16x32_bf16 v[52:55], a[84:87], a[196:199], v[52:55]\n"
      "s_add_u32 s59, s58, 196608\n"
      "ds_read_b128 a[212:215], v0 offset:16512\n"
      "v_fma_f32 v47, 2.0, v83, -v95\n"
      "v_mfma_f32_16x16x32_bf16 v[56:59], a[88:91], a[196:199], v[56:59]\n"
      "s_nop 0\n"
      "s_waitcnt lgkmcnt(5)\n"
      "ds_read2st64_b32 v[30:31], v6 offset0:4 offset1:6\n"
      "v_fma_f32 v48, 2.0, v84, -v96\n"
      "v_mfma_f32_16x16x32_bf16 v[60:63], a[92:95], a[196:199], v[60:63]\n"
      "s_add_u32 s54, s54, 196608\n"
      "v_fma_f32 v49, 2.0, v85, -v97\n"
      "v_fma_f32 v50, 2.0, v86, -v98\n"
      "v_fma_f32 v51, 2.0, v87, -v99\n"
      "v_mfma_f32_16x16x32_bf16 v[52:55], a[96:99], a[200:203], v[52:55]\n"
      "s_add_u32 m0, s37, 128896\n"
      "buffer_store_dwordx4 a[220:223], v16, s[8:11], s58 offen offset:0 sc1\n"
      "ds_read_b128 a[192:195], v1 offset:16384\n"
      "v_mfma_f32_16x16x32_bf16 v[56:59], a[100:103], a[200:203], v[56:59]\n"
      "buffer_load_dwordx4 v8, s[4:7], s54 offen offset:0 nt lds\n"
      "buffer_load_dwordx4 v9, s[4:7], s54 offen offset:1024 nt lds\n"
      "v_mfma_f32_16x16x32_bf16 v[60:63], a[104:107], a[200:203], v[60:63]\n"
      "s_mov_b32 m0, s64\n"
      "ds_read2st64_b32 v[32:33], v6 offset0:8 offset1:10\n"
      "ds_read2st64_b32 v[34:35], v6 offset0:12 offset1:14\n"
      "v_mfma_f32_16x16x32_bf16 v[76:79], v[252:255], a[204:207], 0\n"
      "s_nop 0\n"
      "ds_read_b128 a[196:199], v1 offset:16448\n"
      "ds_read2st64_b32 v[36:37], v6 offset0:16 offset1:18\n"
      "v_mfma_f32_16x16x32_bf16 v[80:83], v[244:247], a[204:207], 0\n"
      "ds_read_b64_tr_b16 a[244:245], v7 offset:3072\n"
      "ds_read2st64_b32 a[228:229], v12 offset0:181 offset1:183\n"
      "v_mfma_f32_16x16x32_bf16 v[84:87], v[236:239], a[204:207], 0\n"
      "s_waitcnt lgkmcnt(7)\n"
      "ds_read_b64_tr_b16 a[246:247], v7 offset:3080\n"
      "ds_read2st64_b32 a[230:231], v15 offset0:181 offset1:183\n"
      "v_fmac_f32 v40, s46, v28\n"
      "v_mfma_f32_16x16x32_bf16 v[76:79], v[228:231], a[208:211], v[76:79]\n"
      "ds_read_b128 a[200:203], v1 offset:16512\n"
      "ds_read2st64_b32 v[38:39], v6 offset0:20 offset1:22\n"
      "v_mfma_f32_16x16x32_bf16 v[80:83], v[220:223], a[208:211], v[80:83]\n"
      "buffer_store_dwordx4 a[224:227], v16, s[8:11], s58 offen offset:128 sc1\n"
      "v_fmac_f32 v41, s46, v29\n"
      "v_mfma_f32_16x16x32_bf16 v[84:87], v[212:215], a[208:211], v[84:87]\n"
      "v_fmac_f32 v42, s46, v30\n"
      "v_fmac_f32 v43, s46, v31\n"
      "v_mfma_f32_16x16x32_bf16 v[76:79], v[204:207], a[212:215], v[76:79]\n"
      "s_nop 0\n"
      "ds_read_b128 a[204:207], v2 offset:16384\n"
      "v_fma_f32 v28, 0.5, v28, v64\n"
      "v_mfma_f32_16x16x32_bf16 v[80:83], v[196:199], a[212:215], v[80:83]\n"
      "v_fma_f32 v29, 0.5, v29, v65\n"
      "v_fma_f32 v30, 0.5, v30, v66\n"
      "v_fma_f32 v31, 0.5, v31, v67\n"
      "v_mfma_f32_16x16x32_bf16 v[84:87], v[188:191], a[212:215], v[84:87]\n"
      "s_waitcnt lgkmcnt(1)\n"
      "v_fmac_f32 v40, s48, v52\n"
      "v_fmac_f32 v41, s48, v53\n"
      "v_mfma_f32_4x4x4_16b_bf16 v[20:23], a[216:217], a[244:245], 0\n"
      "v_fmac_f32 v42, s48, v54\n"
      "v_mfma_f32_4x4x4_16b_bf16 v[24:27], a[216:217], a[246:247], 0\n"
      "v_fmac_f32 v43, s48, v55\n"
      "v_fmac_f32 v28, s45, v40\n"
      "v_mfma_f32_16x16x32_bf16 v[76:79], v[180:183], a[192:195], v[76:79]\n"
      "ds_read_b128 a[208:211], v2 offset:16448\n"
      "v_fmac_f32 v29, s45, v41\n"
      "v_mfma_f32_16x16x32_bf16 v[80:83], v[172:175], a[192:195], v[80:83]\n"
      "s_nop 0\n"
      "v_cvt_pk_bf16_f32 v20, v20, v24\n"
      "v_cvt_pk_bf16_f32 v21, v21, v25\n"
      "v_mfma_f32_16x16x32_bf16 v[84:87], v[164:167], a[192:195], v[84:87]\n"
      "s_add_u32 s54, s54, 196608\n"
      "ds_write_addtid_b32 v20 offset:3072\n"
      "ds_write_addtid_b32 v21 offset:7168\n"
      "v_fmac_f32_e64 v30, s45, v42\n"
      "v_mfma_f32_16x16x32_bf16 v[76:79], v[156:159], a[196:199], v[76:79]\n"
      "s_add_u32 m0, s37, 133120\n"
      "ds_read_b128 a[212:215], v2 offset:16512\n"
      "v_fmac_f32 v31, s45, v43\n"
      "v_mfma_f32_16x16x32_bf16 v[80:83], v[148:151], a[196:199], v[80:83]\n"
      "buffer_load_dwordx4 v8, s[4:7], s54 offen offset:0 nt lds\n"
      "buffer_load_dwordx4 v9, s[4:7], s54 offen offset:1024 nt lds\n"
      "v_mfma_f32_16x16x32_bf16 v[84:87], v[140:143], a[196:199], v[84:87]\n"
      "s_mov_b32 m0, s64\n"
      "v_fma_f32 v40, s47, v40, -v52\n"
      "v_cvt_pk_bf16_f32 v22, v22, v26\n"
      "v_mfma_f32_16x16x32_bf16 v[76:79], v[132:135], a[200:203], v[76:79]\n"
      "buffer_store_dwordx4 a[228:231], v16, s[8:11], s58 offen offset:256 sc1\n"
      "ds_read_b128 a[192:195], v0 offset:20480\n"
      "v_mfma_f32_16x16x32_bf16 v[80:83], v[124:127], a[200:203], v[80:83]\n"
      "ds_read2st64_b32 a[232:233], v10 offset0:4 offset1:6\n"
      "ds_write_addtid_b32 v22 offset:11264\n"
      "v_fma_f32 v41, s47, v41, -v53\n"
      "v_mfma_f32_16x16x32_bf16 v[84:87], v[116:119], a[200:203], v[84:87]\n"
      "s_waitcnt lgkmcnt(3)\n"
      "ds_read2st64_b32 a[234:235], v13 offset0:4 offset1:6\n"
      "v_fma_f32 v42, s47, v42, -v54\n"
      "v_mfma_f32_16x16x32_bf16 v[76:79], v[108:111], a[204:207], v[76:79]\n"
      "ds_read_b128 a[196:199], v0 offset:20544\n"
      "v_cvt_pk_bf16_f32 v23, v23, v27\n"
      "v_mfma_f32_16x16x32_bf16 v[80:83], v[100:103], a[204:207], v[80:83]\n"
      "s_nop 0\n"
      "ds_write_addtid_b32 v23 offset:15360\n"
      "v_fma_f32 v43, s47, v43, -v55\n"
      "v_mfma_f32_16x16x32_bf16 v[84:87], a[160:163], a[204:207], v[84:87]\n"
      "v_cvt_pk_bf16_f32 v28, v28, v29\n"
      "v_cvt_pk_bf16_f32 v30, v30, v31\n"
      "v_mfma_f32_16x16x32_bf16 v[76:79], a[152:155], a[208:211], v[76:79]\n"
      "ds_read_b128 a[200:203], v0 offset:20608\n"
      "ds_write_addtid_b32 v28 offset:32768\n"
      "v_mfma_f32_16x16x32_bf16 v[80:83], a[144:147], a[208:211], v[80:83]\n"
      "ds_write_addtid_b32 v30 offset:33280\n"
      "v_cvt_pk_bf16_f32 v40, v40, v41\n"
      "v_mfma_f32_16x16x32_bf16 v[84:87], a[136:139], a[208:211], v[84:87]\n"
      "ds_write_addtid_b32 v40 offset:34816\n"
      "v_cvt_pk_bf16_f32 v42, v42, v43\n"
      "v_fmac_f32 v44, s46, v32\n"
      "v_mfma_f32_16x16x32_bf16 v[76:79], a[128:131], a[212:215], v[76:79]\n"
      "s_nop 0\n"
      "ds_read_b128 a[204:207], v1 offset:20480\n"
      "ds_write_addtid_b32 v42 offset:35328\n"
      "v_fmac_f32 v45, s46, v33\n"
      "v_mfma_f32_16x16x32_bf16 v[80:83], a[120:123], a[212:215], v[80:83]\n"
      "v_fmac_f32 v46, s46, v34\n"
      "v_fmac_f32 v47, s46, v35\n"
      "v_mfma_f32_16x16x32_bf16 v[84:87], a[112:115], a[212:215], v[84:87]\n"
      "s_waitcnt lgkmcnt(3)\n"
      "v_fma_f32 v32, 0.5, v32, v68\n"
      "v_fma_f32 v33, 0.5, v33, v69\n"
      "v_mfma_f32_16x16x32_bf16 v[88:91], v[248:251], a[192:195], v[76:79]\n"
      "ds_read_b128 a[208:211], v1 offset:20544\n"
      "v_fma_f32 v34, 0.5, v34, v70\n"
      "v_mfma_f32_16x16x32_bf16 v[92:95], v[240:243], a[192:195], v[80:83]\n"
      "v_fma_f32 v35, 0.5, v35, v71\n"
      "v_fmac_f32 v44, s48, v56\n"
      "v_mfma_f32_16x16x32_bf16 v[96:99], v[232:235], a[192:195], v[84:87]\n"
      "s_nop 0\n"
      "buffer_store_dwordx4 a[232:235], v16, s[8:11], s59 offen offset:0 sc1\n"
      "ds_read_b64_tr_b16 a[248:249], v7 offset:3584\n"
      "v_mfma_f32_16x16x32_bf16 v[88:91], v[224:227], a[196:199], v[88:91]\n"
      "ds_read_b128 a[212:215], v1 offset:20608\n"
      "ds_read_b64_tr_b16 a[250:251], v7 offset:3592\n"
      "v_fmac_f32 v45, s48, v57\n"
      "v_mfma_f32_16x16x32_bf16 v[92:95], v[216:219], a[196:199], v[92:95]\n"
      "v_fmac_f32 v46, s48, v58\n"
      "v_fmac_f32 v47, s48, v59\n"
      "v_mfma_f32_16x16x32_bf16 v[96:99], v[208:211], a[196:199], v[96:99]\n"
      "v_fmac_f32 v32, s45, v44\n"
      "v_fmac_f32 v33, s45, v45\n"
      "v_mfma_f32_16x16x32_bf16 v[88:91], v[200:203], a[200:203], v[88:91]\n"
      "ds_read_b128 a[192:195], v2 offset:20480\n"
      "v_fmac_f32 v34, s45, v46\n"
      "v_mfma_f32_16x16x32_bf16 v[92:95], v[192:195], a[200:203], v[92:95]\n"
      "v_fmac_f32 v35, s45, v47\n"
      "v_fma_f32 v44, s47, v44, -v56\n"
      "v_mfma_f32_16x16x32_bf16 v[96:99], v[184:187], a[200:203], v[96:99]\n"
      "s_waitcnt lgkmcnt(1)\n"
      "v_fma_f32 v45, s47, v45, -v57\n"
      "v_fma_f32 v46, s47, v46, -v58\n"
      "v_mfma_f32_16x16x32_bf16 v[88:91], v[176:179], a[204:207], v[88:91]\n"
      "ds_read_b128 a[196:199], v2 offset:20544\n"
      "v_fma_f32 v47, s47, v47, -v59\n"
      "v_mfma_f32_4x4x4_16b_bf16 v[20:23], a[216:217], a[248:249], 0\n"
      "v_cvt_pk_bf16_f32 v32, v32, v33\n"
      "v_mfma_f32_4x4x4_16b_bf16 v[24:27], a[216:217], a[250:251], 0\n"
      "ds_write_addtid_b32 v32 offset:40964\n"
      "v_cvt_pk_bf16_f32 v34, v34, v35\n"
      "v_mfma_f32_16x16x32_bf16 v[92:95], v[168:171], a[204:207], v[92:95]\n"
      "ds_write_addtid_b32 v34 offset:41476\n"
      "v_cvt_pk_bf16_f32 v44, v44, v45\n"
      "v_cvt_pk_bf16_f32 v20, v20, v24\n"
      "v_mfma_f32_16x16x32_bf16 v[96:99], v[160:163], a[204:207], v[96:99]\n"
      "s_add_u32 s54, s54, 196608\n"
      "ds_write_addtid_b32 v44 offset:43012\n"
      "ds_write_addtid_b32 v20 offset:3584\n"
      "v_cvt_pk_bf16_f32 v46, v46, v47\n"
      "v_mfma_f32_16x16x32_bf16 v[88:91], v[152:155], a[208:211], v[88:91]\n"
      "s_add_u32 m0, s37, 137224\n"
      "ds_read_b128 a[200:203], v2 offset:20608\n"
      "ds_read2st64_b32 a[236:237], v11 offset0:4 offset1:6\n"
      "v_mfma_f32_16x16x32_bf16 v[92:95], v[144:147], a[208:211], v[92:95]\n"
      "buffer_load_dwordx4 v8, s[4:7], s54 offen offset:0 nt lds\n"
      "buffer_load_dwordx4 v9, s[4:7], s54 offen offset:1024 nt lds\n"
      "ds_read2st64_b32 a[238:239], v14 offset0:4 offset1:6\n"
      "v_mfma_f32_16x16x32_bf16 v[96:99], v[136:139], a[208:211], v[96:99]\n"
      "s_mov_b32 m0, s64\n"
      "v_fmac_f32 v48, s46, v36\n"
      "v_fmac_f32 v49, s46, v37\n"
      "v_mfma_f32_16x16x32_bf16 v[88:91], v[128:131], a[212:215], v[88:91]\n"
      "s_nop 0\n"
      "ds_read_b128 a[204:207], v3 offset:50688\n"
      "ds_write_addtid_b32 v46 offset:43524\n"
      "v_mfma_f32_16x16x32_bf16 v[92:95], v[120:123], a[212:215], v[92:95]\n"
      "v_fmac_f32 v50, s46, v38\n"
      "v_cvt_pk_bf16_f32 v21, v21, v25\n"
      "v_fmac_f32 v51, s46, v39\n"
      "v_mfma_f32_16x16x32_bf16 v[96:99], v[112:115], a[212:215], v[96:99]\n"
      "s_waitcnt lgkmcnt(3)\n"
      "ds_write_addtid_b32 v21 offset:7680\n"
      "v_cvt_pk_bf16_f32 v22, v22, v26\n"
      "v_mfma_f32_16x16x32_bf16 v[88:91], v[104:107], a[192:195], v[88:91]\n"
      "s_nop 0\n"
      "ds_read_b128 a[208:211], v3 offset:50752\n"
      "ds_write_addtid_b32 v22 offset:11776\n"
      "v_mfma_f32_16x16x32_bf16 v[92:95], a[164:167], a[192:195], v[92:95]\n"
      "v_fma_f32 v36, 0.5, v36, v72\n"
      "v_fma_f32 v37, 0.5, v37, v73\n"
      "v_mfma_f32_16x16x32_bf16 v[96:99], a[156:159], a[192:195], v[96:99]\n"
      "ds_read2st64_b32 a[240:241], v12 offset0:4 offset1:6\n"
      "ds_read2st64_b32 a[242:243], v15 offset0:4 offset1:6\n"
      "v_fma_f32 v38, 0.5, v38, v74\n"
      "v_mfma_f32_16x16x32_bf16 v[88:91], a[148:151], a[196:199], v[88:91]\n"
      "ds_read_b128 a[212:215], v3 offset:50816\n"
      "v_cvt_pk_bf16_f32 v23, v23, v27\n"
      "v_mfma_f32_16x16x32_bf16 v[92:95], a[140:143], a[196:199], v[92:95]\n"
      "ds_write_addtid_b32 v23 offset:15872\n"
      "v_fma_f32 v39, 0.5, v39, v75\n"
      "v_mfma_f32_16x16x32_bf16 v[96:99], a[132:135], a[196:199], v[96:99]\n"
      "s_waitcnt lgkmcnt(4)\n"
      "v_fmac_f32 v48, s48, v60\n"
      "v_fmac_f32 v49, s48, v61\n"
      "v_mfma_f32_16x16x32_bf16 v[88:91], a[124:127], a[200:203], v[88:91]\n"
      "s_add_u32 s54, s54, 196608\n"
      "ds_read_b128 a[192:195], v4 offset:50688\n"
      "v_fmac_f32 v50, s48, v62\n"
      "v_mfma_f32_16x16x32_bf16 v[92:95], a[116:119], a[200:203], v[92:95]\n"
      "s_add_u32 m0, s37, 141448\n"
      "v_fmac_f32 v51, s48, v63\n"
      "v_fmac_f32 v36, s45, v48\n"
      "v_mfma_f32_16x16x32_bf16 v[96:99], a[108:111], a[200:203], v[96:99]\n"
      "buffer_store_dwordx4 a[236:239], v16, s[8:11], s59 offen offset:128 sc1\n"
      "buffer_load_dwordx4 v8, s[4:7], s54 offen offset:0 nt lds\n"
      "buffer_load_dwordx4 v9, s[4:7], s54 offen offset:1024 nt lds\n"
      "v_mfma_f32_16x16x32_bf16 v[64:67], a[0:3], a[204:207], 0\n"
      "s_mov_b32 m0, s64\n"
      "ds_read_b128 a[196:199], v4 offset:50752\n"
      "ds_write_addtid_b32 v88 offset:51456\n"
      "v_fmac_f32 v37, s45, v49\n"
      "v_mfma_f32_16x16x32_bf16 v[68:71], a[4:7], a[204:207], 0\n"
      "ds_write_addtid_b32 v89 offset:51968\n"
      "ds_write_addtid_b32 v90 offset:52480\n"
      "v_mfma_f32_16x16x32_bf16 v[72:75], a[8:11], a[204:207], 0\n"
      "ds_write_addtid_b32 v91 offset:52992\n"
      "ds_write_addtid_b32 v92 offset:53504\n"
      "v_fmac_f32 v38, s45, v50\n"
      "v_mfma_f32_16x16x32_bf16 v[64:67], a[12:15], a[208:211], v[64:67]\n"
      "s_nop 0\n"
      "ds_read_b128 a[200:203], v4 offset:50816\n"
      "ds_write_addtid_b32 v93 offset:54016\n"
      "v_mfma_f32_16x16x32_bf16 v[68:71], a[16:19], a[208:211], v[68:71]\n"
      "s_nop 0\n"
      "ds_write_addtid_b32 v94 offset:54528\n"
      "ds_write_addtid_b32 v95 offset:55040\n"
      "v_mfma_f32_16x16x32_bf16 v[72:75], a[20:23], a[208:211], v[72:75]\n"
      "s_waitcnt lgkmcnt(7)\n"
      "ds_write_addtid_b32 v96 offset:55552\n"
      "ds_write_addtid_b32 v97 offset:56064\n"
      "v_fmac_f32 v39, s45, v51\n"
      "v_mfma_f32_16x16x32_bf16 v[64:67], a[24:27], a[212:215], v[64:67]\n"
      "ds_read_b128 a[204:207], v5 offset:50688\n"
      "ds_write_addtid_b32 v98 offset:56576\n"
      "v_mfma_f32_16x16x32_bf16 v[68:71], a[28:31], a[212:215], v[68:71]\n"
      "buffer_store_dwordx4 a[240:243], v16, s[8:11], s59 offen offset:256 sc1\n"
      "v_mfma_f32_16x16x32_bf16 v[72:75], a[32:35], a[212:215], v[72:75]\n"
      "s_nop 0\n"
      "ds_write_addtid_b32 v99 offset:57088\n"
      "v_fma_f32 v48, s47, v48, -v60\n"
      "v_mfma_f32_16x16x32_bf16 v[64:67], a[36:39], a[192:195], v[64:67]\n"
      "ds_read_b128 a[208:211], v5 offset:50752\n"
      "v_fma_f32 v49, s47, v49, -v61\n"
      "v_fma_f32 v50, s47, v50, -v62\n"
      "v_fma_f32 v51, s47, v51, -v63\n"
      "v_cvt_pk_bf16_f32 v36, v36, v37\n"
      "v_cvt_pk_bf16_f32 v38, v38, v39\n"
      "v_cvt_pk_bf16_f32 v48, v48, v49\n"
      "v_cvt_pk_bf16_f32 v50, v50, v51\n"
      "v_mfma_f32_16x16x32_bf16 v[68:71], a[40:43], a[192:195], v[68:71]\n"
      "ds_write_addtid_b32 v36 offset:36864\n"
      "ds_write_addtid_b32 v38 offset:37376\n"
      "v_mfma_f32_16x16x32_bf16 v[72:75], a[44:47], a[192:195], v[72:75]\n"
      "ds_write_addtid_b32 v48 offset:38912\n"
      "ds_write_addtid_b32 v50 offset:39424\n"
      "v_mfma_f32_16x16x32_bf16 v[64:67], a[48:51], a[196:199], v[64:67]\n"
      "ds_read_b128 a[212:215], v5 offset:50816\n"
      "v_fma_f32 v40, 2.0, v76, -v88\n"
      "v_mfma_f32_16x16x32_bf16 v[68:71], a[52:55], a[196:199], v[68:71]\n"
      "v_fma_f32 v41, 2.0, v77, -v89\n"
      "v_fma_f32 v42, 2.0, v78, -v90\n"
      "v_mfma_f32_16x16x32_bf16 v[72:75], a[56:59], a[196:199], v[72:75]\n"
      "s_waitcnt lgkmcnt(5)\n"
      "v_fma_f32 v43, 2.0, v79, -v91\n"
      "v_fma_f32 v44, 2.0, v80, -v92\n"
      "v_mfma_f32_16x16x32_bf16 v[64:67], a[60:63], a[200:203], v[64:67]\n"
      "v_fma_f32 v45, 2.0, v81, -v93\n"
      "v_mfma_f32_16x16x32_bf16 v[68:71], a[64:67], a[200:203], v[68:71]\n"
      "s_waitcnt vmcnt(25)\n"
      "v_mfma_f32_16x16x32_bf16 v[72:75], a[68:71], a[200:203], v[72:75]\n"
      "s_waitcnt lgkmcnt(0)\n"
      "s_barrier\n"
      "v_mfma_f32_16x16x32_bf16 v[64:67], a[72:75], a[204:207], v[64:67]\n"
      "s_add_u32 s58, s40, 0\n"
      "ds_read_b128 a[192:195], v0 offset:0\n"
      "ds_read2st64_b32 a[220:221], v10 offset0:177 offset1:179\n"
      "ds_read2st64_b32 a[222:223], v13 offset0:177 offset1:179\n"
      "ds_read2st64_b32 a[224:225], v11 offset0:177 offset1:179\n"
      "v_mfma_f32_16x16x32_bf16 v[68:71], a[76:79], a[204:207], v[68:71]\n"
      "s_mov_b32 s40, s62\n"
      "ds_read_b128 a[196:199], v0 offset:64\n"
      "v_fma_f32 v46, 2.0, v82, -v94\n"
      "v_mfma_f32_16x16x32_bf16 v[72:75], a[80:83], a[204:207], v[72:75]\n"
      "s_add_u32 s62, s62, 786432\n"
      "ds_read2st64_b32 v[28:29], v6 offset0:24 offset1:26\n"
      "ds_read2st64_b32 a[226:227], v14 offset0:177 offset1:179\n"
      "v_mfma_f32_16x16x32_bf16 v[64:67], a[84:87], a[208:211], v[64:67]\n"
      "s_add_u32 s59, s58, 196608\n"
      "ds_read_b128 a[200:203], v0 offset:128\n"
      "v_fma_f32 v47, 2.0, v83, -v95\n"
      "v_mfma_f32_16x16x32_bf16 v[68:71], a[88:91], a[208:211], v[68:71]\n"
      "s_nop 0\n"
      "s_waitcnt lgkmcnt(5)\n"
      "ds_read2st64_b32 v[30:31], v6 offset0:28 offset1:30\n"
      "v_fma_f32 v48, 2.0, v84, -v96\n"
      "v_mfma_f32_16x16x32_bf16 v[72:75], a[92:95], a[208:211], v[72:75]\n"
      "s_add_u32 s54, s54, 196608\n"
      "v_fma_f32 v49, 2.0, v85, -v97\n"
      "v_fma_f32 v50, 2.0, v86, -v98\n"
      "v_fma_f32 v51, 2.0, v87, -v99\n"
      "v_mfma_f32_16x16x32_bf16 v[64:67], a[96:99], a[212:215], v[64:67]\n"
      "s_add_u32 m0, s37, 145792\n"
      "buffer_store_dwordx4 a[220:223], v16, s[8:11], s58 offen offset:0 sc1\n"
      "ds_read_b128 a[204:207], v1 offset:0\n"
      "v_mfma_f32_16x16x32_bf16 v[68:71], a[100:103], a[212:215], v[68:71]\n"
      "buffer_load_dwordx4 v8, s[4:7], s54 offen offset:0 nt lds\n"
      "buffer_load_dwordx4 v9, s[4:7], s54 offen offset:1024 nt lds\n"
      "v_mfma_f32_16x16x32_bf16 v[72:75], a[104:107], a[212:215], v[72:75]\n"
      "s_mov_b32 m0, s64\n"
      "ds_read2st64_b32 v[32:33], v6 offset0:32 offset1:34\n"
      "ds_read2st64_b32 v[34:35], v6 offset0:36 offset1:38\n"
      "v_mfma_f32_16x16x32_bf16 v[76:79], v[252:255], a[192:195], 0\n"
      "s_nop 0\n"
      "ds_read_b128 a[208:211], v1 offset:64\n"
      "ds_read2st64_b32 v[36:37], v6 offset0:40 offset1:42\n"
      "v_mfma_f32_16x16x32_bf16 v[80:83], v[244:247], a[192:195], 0\n"
      "ds_read_b64_tr_b16 a[244:245], v7 offset:19968\n"
      "ds_read2st64_b32 a[228:229], v12 offset0:177 offset1:179\n"
      "v_mfma_f32_16x16x32_bf16 v[84:87], v[236:239], a[192:195], 0\n"
      "s_waitcnt lgkmcnt(7)\n"
      "ds_read_b64_tr_b16 a[246:247], v7 offset:19976\n"
      "ds_read2st64_b32 a[230:231], v15 offset0:177 offset1:179\n"
      "v_fmac_f32 v40, s46, v28\n"
      "v_mfma_f32_16x16x32_bf16 v[76:79], v[228:231], a[196:199], v[76:79]\n"
      "ds_read_b128 a[212:215], v1 offset:128\n"
      "ds_read2st64_b32 v[38:39], v6 offset0:44 offset1:46\n"
      "v_mfma_f32_16x16x32_bf16 v[80:83], v[220:223], a[196:199], v[80:83]\n"
      "buffer_store_dwordx4 a[224:227], v16, s[8:11], s58 offen offset:128 sc1\n"
      "v_fmac_f32 v41, s46, v29\n"
      "v_mfma_f32_16x16x32_bf16 v[84:87], v[212:215], a[196:199], v[84:87]\n"
      "v_fmac_f32 v42, s46, v30\n"
      "v_fmac_f32 v43, s46, v31\n"
      "v_mfma_f32_16x16x32_bf16 v[76:79], v[204:207], a[200:203], v[76:79]\n"
      "s_nop 0\n"
      "ds_read_b128 a[192:195], v2 offset:0\n"
      "v_fma_f32 v28, 0.5, v28, v52\n"
      "v_mfma_f32_16x16x32_bf16 v[80:83], v[196:199], a[200:203], v[80:83]\n"
      "v_fma_f32 v29, 0.5, v29, v53\n"
      "v_fma_f32 v30, 0.5, v30, v54\n"
      "v_fma_f32 v31, 0.5, v31, v55\n"
      "v_mfma_f32_16x16x32_bf16 v[84:87], v[188:191], a[200:203], v[84:87]\n"
      "s_waitcnt lgkmcnt(1)\n"
      "v_fmac_f32 v40, s48, v64\n"
      "v_fmac_f32 v41, s48, v65\n"
      "v_mfma_f32_4x4x4_16b_bf16 v[20:23], a[216:217], a[244:245], 0\n"
      "v_fmac_f32 v42, s48, v66\n"
      "v_mfma_f32_4x4x4_16b_bf16 v[24:27], a[216:217], a[246:247], 0\n"
      "v_fmac_f32 v43, s48, v67\n"
      "v_fmac_f32 v28, s45, v40\n"
      "v_mfma_f32_16x16x32_bf16 v[76:79], v[180:183], a[204:207], v[76:79]\n"
      "ds_read_b128 a[196:199], v2 offset:64\n"
      "v_fmac_f32 v29, s45, v41\n"
      "v_mfma_f32_16x16x32_bf16 v[80:83], v[172:175], a[204:207], v[80:83]\n"
      "s_nop 0\n"
      "v_cvt_pk_bf16_f32 v20, v20, v24\n"
      "v_cvt_pk_bf16_f32 v21, v21, v25\n"
      "v_mfma_f32_16x16x32_bf16 v[84:87], v[164:167], a[204:207], v[84:87]\n"
      "s_add_u32 s54, s54, 196608\n"
      "ds_write_addtid_b32 v20 offset:19456\n"
      "ds_write_addtid_b32 v21 offset:23552\n"
      "v_fmac_f32_e64 v30, s45, v42\n"
      "v_mfma_f32_16x16x32_bf16 v[76:79], v[156:159], a[208:211], v[76:79]\n"
      "s_add_u32 m0, s37, 82432\n"
      "ds_read_b128 a[200:203], v2 offset:128\n"
      "v_fmac_f32 v31, s45, v43\n"
      "v_mfma_f32_16x16x32_bf16 v[80:83], v[148:151], a[208:211], v[80:83]\n"
      "buffer_load_dwordx4 v8, s[4:7], s54 offen offset:0 nt lds\n"
      "buffer_load_dwordx4 v9, s[4:7], s54 offen offset:1024 nt lds\n"
      "v_mfma_f32_16x16x32_bf16 v[84:87], v[140:143], a[208:211], v[84:87]\n"
      "s_mov_b32 m0, s64\n"
      "v_fma_f32 v40, s47, v40, -v64\n"
      "v_cvt_pk_bf16_f32 v22, v22, v26\n"
      "v_mfma_f32_16x16x32_bf16 v[76:79], v[132:135], a[212:215], v[76:79]\n"
      "buffer_store_dwordx4 a[228:231], v16, s[8:11], s58 offen offset:256 sc1\n"
      "ds_read_b128 a[204:207], v0 offset:4096\n"
      "v_mfma_f32_16x16x32_bf16 v[80:83], v[124:127], a[212:215], v[80:83]\n"
      "ds_read2st64_b32 a[232:233], v10 offset0:0 offset1:2\n"
      "ds_write_addtid_b32 v22 offset:27648\n"
      "v_fma_f32 v41, s47, v41, -v65\n"
      "v_mfma_f32_16x16x32_bf16 v[84:87], v[116:119], a[212:215], v[84:87]\n"
      "s_waitcnt lgkmcnt(3)\n"
      "ds_read2st64_b32 a[234:235], v13 offset0:0 offset1:2\n"
      "v_fma_f32 v42, s47, v42, -v66\n"
      "v_mfma_f32_16x16x32_bf16 v[76:79], v[108:111], a[192:195], v[76:79]\n"
      "ds_read_b128 a[208:211], v0 offset:4160\n"
      "v_cvt_pk_bf16_f32 v23, v23, v27\n"
      "v_mfma_f32_16x16x32_bf16 v[80:83], v[100:103], a[192:195], v[80:83]\n"
      "s_nop 0\n"
      "ds_write_addtid_b32 v23 offset:31744\n"
      "v_fma_f32 v43, s47, v43, -v67\n"
      "v_mfma_f32_16x16x32_bf16 v[84:87], a[160:163], a[192:195], v[84:87]\n"
      "v_cvt_pk_bf16_f32 v28, v28, v29\n"
      "v_cvt_pk_bf16_f32 v30, v30, v31\n"
      "v_mfma_f32_16x16x32_bf16 v[76:79], a[152:155], a[196:199], v[76:79]\n"
      "ds_read_b128 a[212:215], v0 offset:4224\n"
      "ds_write_addtid_b32 v28 offset:33792\n"
      "v_mfma_f32_16x16x32_bf16 v[80:83], a[144:147], a[196:199], v[80:83]\n"
      "ds_write_addtid_b32 v30 offset:34304\n"
      "v_cvt_pk_bf16_f32 v40, v40, v41\n"
      "v_mfma_f32_16x16x32_bf16 v[84:87], a[136:139], a[196:199], v[84:87]\n"
      "ds_write_addtid_b32 v40 offset:35840\n"
      "v_cvt_pk_bf16_f32 v42, v42, v43\n"
      "v_fmac_f32 v44, s46, v32\n"
      "v_mfma_f32_16x16x32_bf16 v[76:79], a[128:131], a[200:203], v[76:79]\n"
      "s_nop 0\n"
      "ds_read_b128 a[192:195], v1 offset:4096\n"
      "ds_write_addtid_b32 v42 offset:36352\n"
      "v_fmac_f32 v45, s46, v33\n"
      "v_mfma_f32_16x16x32_bf16 v[80:83], a[120:123], a[200:203], v[80:83]\n"
      "v_fmac_f32 v46, s46, v34\n"
      "v_fmac_f32 v47, s46, v35\n"
      "v_mfma_f32_16x16x32_bf16 v[84:87], a[112:115], a[200:203], v[84:87]\n"
      "s_waitcnt lgkmcnt(3)\n"
      "v_fma_f32 v32, 0.5, v32, v56\n"
      "v_fma_f32 v33, 0.5, v33, v57\n"
      "v_mfma_f32_16x16x32_bf16 v[88:91], v[248:251], a[204:207], v[76:79]\n"
      "ds_read_b128 a[196:199], v1 offset:4160\n"
      "v_fma_f32 v34, 0.5, v34, v58\n"
      "v_mfma_f32_16x16x32_bf16 v[92:95], v[240:243], a[204:207], v[80:83]\n"
      "v_fma_f32 v35, 0.5, v35, v59\n"
      "v_fmac_f32 v44, s48, v68\n"
      "v_mfma_f32_16x16x32_bf16 v[96:99], v[232:235], a[204:207], v[84:87]\n"
      "s_nop 0\n"
      "buffer_store_dwordx4 a[232:235], v16, s[8:11], s59 offen offset:0 sc1\n"
      "ds_read_b64_tr_b16 a[248:249], v7 offset:20480\n"
      "v_mfma_f32_16x16x32_bf16 v[88:91], v[224:227], a[208:211], v[88:91]\n"
      "ds_read_b128 a[200:203], v1 offset:4224\n"
      "ds_read_b64_tr_b16 a[250:251], v7 offset:20488\n"
      "v_fmac_f32 v45, s48, v69\n"
      "v_mfma_f32_16x16x32_bf16 v[92:95], v[216:219], a[208:211], v[92:95]\n"
      "v_fmac_f32 v46, s48, v70\n"
      "v_fmac_f32 v47, s48, v71\n"
      "v_mfma_f32_16x16x32_bf16 v[96:99], v[208:211], a[208:211], v[96:99]\n"
      "v_fmac_f32 v32, s45, v44\n"
      "v_fmac_f32 v33, s45, v45\n"
      "v_mfma_f32_16x16x32_bf16 v[88:91], v[200:203], a[212:215], v[88:91]\n"
      "ds_read_b128 a[204:207], v2 offset:4096\n"
      "v_fmac_f32 v34, s45, v46\n"
      "v_mfma_f32_16x16x32_bf16 v[92:95], v[192:195], a[212:215], v[92:95]\n"
      "v_fmac_f32 v35, s45, v47\n"
      "v_fma_f32 v44, s47, v44, -v68\n"
      "v_mfma_f32_16x16x32_bf16 v[96:99], v[184:187], a[212:215], v[96:99]\n"
      "s_waitcnt lgkmcnt(1)\n"
      "v_fma_f32 v45, s47, v45, -v69\n"
      "v_fma_f32 v46, s47, v46, -v70\n"
      "v_mfma_f32_16x16x32_bf16 v[88:91], v[176:179], a[192:195], v[88:91]\n"
      "ds_read_b128 a[208:211], v2 offset:4160\n"
      "v_fma_f32 v47, s47, v47, -v71\n"
      "v_mfma_f32_4x4x4_16b_bf16 v[20:23], a[216:217], a[248:249], 0\n"
      "v_cvt_pk_bf16_f32 v32, v32, v33\n"
      "v_mfma_f32_4x4x4_16b_bf16 v[24:27], a[216:217], a[250:251], 0\n"
      "ds_write_addtid_b32 v32 offset:41988\n"
      "v_cvt_pk_bf16_f32 v34, v34, v35\n"
      "v_mfma_f32_16x16x32_bf16 v[92:95], v[168:171], a[192:195], v[92:95]\n"
      "ds_write_addtid_b32 v34 offset:42500\n"
      "v_cvt_pk_bf16_f32 v44, v44, v45\n"
      "v_cvt_pk_bf16_f32 v20, v20, v24\n"
      "v_mfma_f32_16x16x32_bf16 v[96:99], v[160:163], a[192:195], v[96:99]\n"
      "s_add_u32 s54, s54, 196608\n"
      "ds_write_addtid_b32 v44 offset:44036\n"
      "ds_write_addtid_b32 v20 offset:19968\n"
      "v_cvt_pk_bf16_f32 v46, v46, v47\n"
      "v_mfma_f32_16x16x32_bf16 v[88:91], v[152:155], a[196:199], v[88:91]\n"
      "s_add_u32 m0, s37, 86536\n"
      "ds_read_b128 a[212:215], v2 offset:4224\n"
      "ds_read2st64_b32 a[236:237], v11 offset0:0 offset1:2\n"
      "v_mfma_f32_16x16x32_bf16 v[92:95], v[144:147], a[196:199], v[92:95]\n"
      "buffer_load_dwordx4 v8, s[4:7], s54 offen offset:0 nt lds\n"
      "buffer_load_dwordx4 v9, s[4:7], s54 offen offset:1024 nt lds\n"
      "ds_read2st64_b32 a[238:239], v14 offset0:0 offset1:2\n"
      "v_mfma_f32_16x16x32_bf16 v[96:99], v[136:139], a[196:199], v[96:99]\n"
      "s_mov_b32 m0, s64\n"
      "v_fmac_f32 v48, s46, v36\n"
      "v_fmac_f32 v49, s46, v37\n"
      "v_mfma_f32_16x16x32_bf16 v[88:91], v[128:131], a[200:203], v[88:91]\n"
      "s_nop 0\n"
      "ds_read_b128 a[192:195], v3 offset:0\n"
      "ds_write_addtid_b32 v46 offset:44548\n"
      "v_mfma_f32_16x16x32_bf16 v[92:95], v[120:123], a[200:203], v[92:95]\n"
      "v_fmac_f32 v50, s46, v38\n"
      "v_cvt_pk_bf16_f32 v21, v21, v25\n"
      "v_fmac_f32 v51, s46, v39\n"
      "v_mfma_f32_16x16x32_bf16 v[96:99], v[112:115], a[200:203], v[96:99]\n"
      "s_waitcnt lgkmcnt(3)\n"
      "ds_write_addtid_b32 v21 offset:24064\n"
      "v_cvt_pk_bf16_f32 v22, v22, v26\n"
      "v_mfma_f32_16x16x32_bf16 v[88:91], v[104:107], a[204:207], v[88:91]\n"
      "s_nop 0\n"
      "ds_read_b128 a[196:199], v3 offset:64\n"
      "ds_write_addtid_b32 v22 offset:28160\n"
      "v_mfma_f32_16x16x32_bf16 v[92:95], a[164:167], a[204:207], v[92:95]\n"
      "v_fma_f32 v36, 0.5, v36, v60\n"
      "v_fma_f32 v37, 0.5, v37, v61\n"
      "v_mfma_f32_16x16x32_bf16 v[96:99], a[156:159], a[204:207], v[96:99]\n"
      "ds_read2st64_b32 a[240:241], v12 offset0:0 offset1:2\n"
      "ds_read2st64_b32 a[242:243], v15 offset0:0 offset1:2\n"
      "v_fma_f32 v38, 0.5, v38, v62\n"
      "v_mfma_f32_16x16x32_bf16 v[88:91], a[148:151], a[208:211], v[88:91]\n"
      "ds_read_b128 a[200:203], v3 offset:128\n"
      "v_cvt_pk_bf16_f32 v23, v23, v27\n"
      "v_mfma_f32_16x16x32_bf16 v[92:95], a[140:143], a[208:211], v[92:95]\n"
      "ds_write_addtid_b32 v23 offset:32256\n"
      "v_fma_f32 v39, 0.5, v39, v63\n"
      "v_mfma_f32_16x16x32_bf16 v[96:99], a[132:135], a[208:211], v[96:99]\n"
      "s_waitcnt lgkmcnt(4)\n"
      "v_fmac_f32 v48, s48, v72\n"
      "v_fmac_f32 v49, s48, v73\n"
      "v_mfma_f32_16x16x32_bf16 v[88:91], a[124:127], a[212:215], v[88:91]\n"
      "s_add_u32 s54, s54, 196608\n"
      "ds_read_b128 a[204:207], v4 offset:0\n"
      "v_fmac_f32 v50, s48, v74\n"
      "v_mfma_f32_16x16x32_bf16 v[92:95], a[116:119], a[212:215], v[92:95]\n"
      "s_add_u32 m0, s37, 90760\n"
      "v_fmac_f32 v51, s48, v75\n"
      "v_fmac_f32 v36, s45, v48\n"
      "v_mfma_f32_16x16x32_bf16 v[96:99], a[108:111], a[212:215], v[96:99]\n"
      "buffer_store_dwordx4 a[236:239], v16, s[8:11], s59 offen offset:128 sc1\n"
      "buffer_load_dwordx4 v8, s[4:7], s54 offen offset:0 nt lds\n"
      "buffer_load_dwordx4 v9, s[4:7], s54 offen offset:1024 nt lds\n"
      "v_mfma_f32_16x16x32_bf16 v[52:55], a[0:3], a[192:195], 0\n"
      "s_mov_b32 m0, s64\n"
      "ds_read_b128 a[208:211], v4 offset:64\n"
      "ds_write_addtid_b32 v88 offset:45312\n"
      "v_fmac_f32 v37, s45, v49\n"
      "v_mfma_f32_16x16x32_bf16 v[56:59], a[4:7], a[192:195], 0\n"
      "ds_write_addtid_b32 v89 offset:45824\n"
      "ds_write_addtid_b32 v90 offset:46336\n"
      "v_mfma_f32_16x16x32_bf16 v[60:63], a[8:11], a[192:195], 0\n"
      "ds_write_addtid_b32 v91 offset:46848\n"
      "ds_write_addtid_b32 v92 offset:47360\n"
      "v_fmac_f32 v38, s45, v50\n"
      "v_mfma_f32_16x16x32_bf16 v[52:55], a[12:15], a[196:199], v[52:55]\n"
      "s_nop 0\n"
      "ds_read_b128 a[212:215], v4 offset:128\n"
      "ds_write_addtid_b32 v93 offset:47872\n"
      "v_mfma_f32_16x16x32_bf16 v[56:59], a[16:19], a[196:199], v[56:59]\n"
      "s_nop 0\n"
      "ds_write_addtid_b32 v94 offset:48384\n"
      "ds_write_addtid_b32 v95 offset:48896\n"
      "v_mfma_f32_16x16x32_bf16 v[60:63], a[20:23], a[196:199], v[60:63]\n"
      "s_waitcnt lgkmcnt(7)\n"
      "ds_write_addtid_b32 v96 offset:49408\n"
      "ds_write_addtid_b32 v97 offset:49920\n"
      "v_fmac_f32 v39, s45, v51\n"
      "v_mfma_f32_16x16x32_bf16 v[52:55], a[24:27], a[200:203], v[52:55]\n"
      "ds_read_b128 a[192:195], v5 offset:0\n"
      "ds_write_addtid_b32 v98 offset:50432\n"
      "v_mfma_f32_16x16x32_bf16 v[56:59], a[28:31], a[200:203], v[56:59]\n"
      "buffer_store_dwordx4 a[240:243], v16, s[8:11], s59 offen offset:256 sc1\n"
      "v_mfma_f32_16x16x32_bf16 v[60:63], a[32:35], a[200:203], v[60:63]\n"
      "s_nop 0\n"
      "ds_write_addtid_b32 v99 offset:50944\n"
      "v_fma_f32 v48, s47, v48, -v72\n"
      "v_mfma_f32_16x16x32_bf16 v[52:55], a[36:39], a[204:207], v[52:55]\n"
      "ds_read_b128 a[196:199], v5 offset:64\n"
      "v_fma_f32 v49, s47, v49, -v73\n"
      "v_fma_f32 v50, s47, v50, -v74\n"
      "v_fma_f32 v51, s47, v51, -v75\n"
      "v_cvt_pk_bf16_f32 v36, v36, v37\n"
      "v_cvt_pk_bf16_f32 v38, v38, v39\n"
      "v_cvt_pk_bf16_f32 v48, v48, v49\n"
      "v_cvt_pk_bf16_f32 v50, v50, v51\n"
      "v_mfma_f32_16x16x32_bf16 v[56:59], a[40:43], a[204:207], v[56:59]\n"
      "ds_write_addtid_b32 v36 offset:37888\n"
      "ds_write_addtid_b32 v38 offset:38400\n"
      "v_mfma_f32_16x16x32_bf16 v[60:63], a[44:47], a[204:207], v[60:63]\n"
      "ds_write_addtid_b32 v48 offset:39936\n"
      "ds_write_addtid_b32 v50 offset:40448\n"
      "v_mfma_f32_16x16x32_bf16 v[52:55], a[48:51], a[208:211], v[52:55]\n"
      "ds_read_b128 a[200:203], v5 offset:128\n"
      "v_fma_f32 v40, 2.0, v76, -v88\n"
      "v_mfma_f32_16x16x32_bf16 v[56:59], a[52:55], a[208:211], v[56:59]\n"
      "v_fma_f32 v41, 2.0, v77, -v89\n"
      "v_fma_f32 v42, 2.0, v78, -v90\n"
      "v_mfma_f32_16x16x32_bf16 v[60:63], a[56:59], a[208:211], v[60:63]\n"
      "s_waitcnt lgkmcnt(5)\n"
      "v_fma_f32 v43, 2.0, v79, -v91\n"
      "v_fma_f32 v44, 2.0, v80, -v92\n"
      "v_mfma_f32_16x16x32_bf16 v[52:55], a[60:63], a[212:215], v[52:55]\n"
      "s_nop 0\n"
      "v_fma_f32 v45, 2.0, v81, -v93\n"
      "v_mfma_f32_16x16x32_bf16 v[56:59], a[64:67], a[212:215], v[56:59]\n"
      "s_waitcnt vmcnt(25)\n"
      "v_mfma_f32_16x16x32_bf16 v[60:63], a[68:71], a[212:215], v[60:63]\n"
      "s_waitcnt lgkmcnt(0)\n"
      "s_barrier\n"
      "s_add_u32 s55, s55, 2\n"
      "s_cmp_lt_u32 s55, 6\n"
      "s_cbranch_scc1 LDloop_%=\n"
      "LDlast_%=:\n"
      "v_mfma_f32_16x16x32_bf16 v[52:55], a[72:75], a[192:195], v[52:55]\n"
      "s_add_u32 s58, s40, 0\n"
      "ds_read_b128 a[204:207], v0 offset:16384\n"
      "ds_read2st64_b32 a[220:221], v10 offset0:181 offset1:183\n"
      "v_fma_f32 v46, 2.0, v82, -v94\n"
      "v_mfma_f32_16x16x32_bf16 v[56:59], a[76:79], a[192:195], v[56:59]\n"
      "s_mov_b32 s40, s62\n"
      "ds_read_b128 a[208:211], v0 offset:16448\n"
      "ds_read2st64_b32 v[28:29], v6 offset0:0 offset1:2\n"
      "v_mfma_f32_16x16x32_bf16 v[60:63], a[80:83], a[192:195], v[60:63]\n"
      "s_add_u32 s62, s62, 786432\n"
      "ds_read2st64_b32 a[222:223], v13 offset0:181 offset1:183\n"
      "ds_read2st64_b32 v[30:31], v6 offset0:4 offset1:6\n"
      "v_mfma_f32_16x16x32_bf16 v[52:55], a[84:87], a[196:199], v[52:55]\n"
      "s_add_u32 s59, s58, 196608\n"
      "ds_read_b128 a[212:215], v0 offset:16512\n"
      "v_fma_f32 v47, 2.0, v83, -v95\n"
      "v_mfma_f32_16x16x32_bf16 v[56:59], a[88:91], a[196:199], v[56:59]\n"
      "ds_read2st64_b32 v[32:33], v6 offset0:8 offset1:10\n"
      "v_fma_f32 v48, 2.0, v84, -v96\n"
      "v_mfma_f32_16x16x32_bf16 v[60:63], a[92:95], a[196:199], v[60:63]\n"
      "v_fma_f32 v49, 2.0, v85, -v97\n"
      "v_fma_f32 v50, 2.0, v86, -v98\n"
      "v_fma_f32 v51, 2.0, v87, -v99\n"
      "v_mfma_f32_16x16x32_bf16 v[52:55], a[96:99], a[200:203], v[52:55]\n"
      "ds_read_b128 a[192:195], v1 offset:16384\n"
      "ds_read2st64_b32 v[34:35], v6 offset0:12 offset1:14\n"
      "v_mfma_f32_16x16x32_bf16 v[56:59], a[100:103], a[200:203], v[56:59]\n"
      "s_nop 0\n"
      "ds_read2st64_b32 v[36:37], v6 offset0:16 offset1:18\n"
      "v_mfma_f32_16x16x32_bf16 v[60:63], a[104:107], a[200:203], v[60:63]\n"
      "s_waitcnt lgkmcnt(7)\n"
      "ds_read2st64_b32 v[38:39], v6 offset0:20 offset1:22\n"
      "v_mfma_f32_16x16x32_bf16 v[76:79], v[252:255], a[204:207], 0\n"
      "ds_read_b128 a[196:199], v1 offset:16448\n"
      "v_mfma_f32_16x16x32_bf16 v[80:83], v[244:247], a[204:207], 0\n"
      "ds_read2st64_b32 a[224:225], v11 offset0:181 offset1:183\n"
      "v_mfma_f32_16x16x32_bf16 v[84:87], v[236:239], a[204:207], 0\n"
      "ds_read2st64_b32 a[226:227], v14 offset0:181 offset1:183\n"
      "v_mfma_f32_16x16x32_bf16 v[76:79], v[228:231], a[208:211], v[76:79]\n"
      "s_waitcnt lgkmcnt(7)\n"
      "ds_read_b128 a[200:203], v1 offset:16512\n"
      "v_fmac_f32 v40, s46, v28\n"
      "v_fmac_f32 v41, s46, v29\n"
      "v_fmac_f32 v42, s46, v30\n"
      "v_fmac_f32 v43, s46, v31\n"
      "v_fma_f32 v28, 0.5, v28, v64\n"
      "v_mfma_f32_16x16x32_bf16 v[80:83], v[220:223], a[208:211], v[80:83]\n"
      "s_nop 0\n"
      "buffer_store_dwordx4 a[220:223], v16, s[8:11], s58 offen offset:0 sc1\n"
      "v_fma_f32 v29, 0.5, v29, v65\n"
      "v_mfma_f32_16x16x32_bf16 v[84:87], v[212:215], a[208:211], v[84:87]\n"
      "v_fma_f32 v30, 0.5, v30, v66\n"
      "v_fma_f32 v31, 0.5, v31, v67\n"
      "v_fmac_f32 v40, s48, v52\n"
      "v_fmac_f32 v41, s48, v53\n"
      "v_mfma_f32_16x16x32_bf16 v[76:79], v[204:207], a[212:215], v[76:79]\n"
      "ds_read_b128 a[204:207], v2 offset:16384\n"
      "v_fmac_f32 v42, s48, v54\n"
      "v_mfma_f32_16x16x32_bf16 v[80:83], v[196:199], a[212:215], v[80:83]\n"
      "s_nop 0\n"
      "ds_read_b64_tr_b16 a[244:245], v7 offset:36352\n"
      "ds_read_b64_tr_b16 a[246:247], v7 offset:36360\n"
      "v_mfma_f32_16x16x32_bf16 v[84:87], v[188:191], a[212:215], v[84:87]\n"
      "s_waitcnt lgkmcnt(3)\n"
      "v_fmac_f32 v43, s48, v55\n"
      "v_fmac_f32 v28, s45, v40\n"
      "v_mfma_f32_16x16x32_bf16 v[76:79], v[180:183], a[192:195], v[76:79]\n"
      "ds_read_b128 a[208:211], v2 offset:16448\n"
      "v_fmac_f32 v29, s45, v41\n"
      "v_mfma_f32_16x16x32_bf16 v[80:83], v[172:175], a[192:195], v[80:83]\n"
      "v_fmac_f32 v30, s45, v42\n"
      "v_fmac_f32 v31, s45, v43\n"
      "v_mfma_f32_16x16x32_bf16 v[84:87], v[164:167], a[192:195], v[84:87]\n"
      "v_fma_f32 v40, s47, v40, -v52\n"
      "v_fma_f32 v41, s47, v41, -v53\n"
      "v_mfma_f32_16x16x32_bf16 v[76:79], v[156:159], a[196:199], v[76:79]\n"
      "ds_read_b128 a[212:215], v2 offset:16512\n"
      "v_fma_f32 v42, s47, v42, -v54\n"
      "v_mfma_f32_16x16x32_bf16 v[80:83], v[148:151], a[196:199], v[80:83]\n"
      "s_nop 0\n"
      "v_fma_f32 v43, s47, v43, -v55\n"
      "v_cvt_pk_bf16_f32 v28, v28, v29\n"
      "v_mfma_f32_16x16x32_bf16 v[84:87], v[140:143], a[196:199], v[84:87]\n"
      "s_waitcnt lgkmcnt(1)\n"
      "ds_write_addtid_b32 v28 offset:32768\n"
      "v_cvt_pk_bf16_f32 v30, v30, v31\n"
      "v_mfma_f32_4x4x4_16b_bf16 v[20:23], a[216:217], a[244:245], 0\n"
      "v_cvt_pk_bf16_f32 v40, v40, v41\n"
      "v_mfma_f32_4x4x4_16b_bf16 v[24:27], a[216:217], a[246:247], 0\n"
      "ds_write_addtid_b32 v30 offset:33280\n"
      "ds_write_addtid_b32 v40 offset:34816\n"
      "v_cvt_pk_bf16_f32 v42, v42, v43\n"
      "v_mfma_f32_16x16x32_bf16 v[76:79], v[132:135], a[200:203], v[76:79]\n"
      "buffer_store_dwordx4 a[224:227], v16, s[8:11], s58 offen offset:128 sc1\n"
      "ds_read_b128 a[192:195], v0 offset:20480\n"
      "v_mfma_f32_16x16x32_bf16 v[80:83], v[124:127], a[200:203], v[80:83]\n"
      "ds_write_addtid_b32 v42 offset:35328\n"
      "ds_read2st64_b32 a[228:229], v12 offset0:181 offset1:183\n"
      "v_fmac_f32 v44, s46, v32\n"
      "v_cvt_pk_bf16_f32 v20, v20, v24\n"
      "v_mfma_f32_16x16x32_bf16 v[84:87], v[116:119], a[200:203], v[84:87]\n"
      "ds_write_addtid_b32 v20 offset:2560\n"
      "ds_read2st64_b32 a[230:231], v15 offset0:181 offset1:183\n"
      "v_mfma_f32_16x16x32_bf16 v[76:79], v[108:111], a[204:207], v[76:79]\n"
      "s_nop 0\n"
      "ds_read_b128 a[196:199], v0 offset:20544\n"
      "v_fmac_f32 v45, s46, v33\n"
      "v_mfma_f32_16x16x32_bf16 v[80:83], v[100:103], a[204:207], v[80:83]\n"
      "v_fmac_f32 v46, s46, v34\n"
      "v_cvt_pk_bf16_f32 v21, v21, v25\n"
      "v_fmac_f32 v47, s46, v35\n"
      "v_mfma_f32_16x16x32_bf16 v[84:87], a[160:163], a[204:207], v[84:87]\n"
      "s_nop 0\n"
      "ds_write_addtid_b32 v21 offset:6656\n"
      "v_fma_f32 v32, 0.5, v32, v68\n"
      "v_mfma_f32_16x16x32_bf16 v[76:79], a[152:155], a[208:211], v[76:79]\n"
      "ds_read_b128 a[200:203], v0 offset:20608\n"
      "v_cvt_pk_bf16_f32 v22, v22, v26\n"
      "v_mfma_f32_16x16x32_bf16 v[80:83], a[144:147], a[208:211], v[80:83]\n"
      "s_nop 0\n"
      "ds_write_addtid_b32 v22 offset:10752\n"
      "v_fma_f32 v33, 0.5, v33, v69\n"
      "v_mfma_f32_16x16x32_bf16 v[84:87], a[136:139], a[208:211], v[84:87]\n"
      "s_waitcnt lgkmcnt(4)\n"
      "ds_read_b64_tr_b16 a[248:249], v7 offset:36864\n"
      "ds_read_b64_tr_b16 a[250:251], v7 offset:36872\n"
      "v_fma_f32 v34, 0.5, v34, v70\n"
      "v_mfma_f32_16x16x32_bf16 v[76:79], a[128:131], a[212:215], v[76:79]\n"
      "ds_read_b128 a[204:207], v1 offset:20480\n"
      "v_fma_f32 v35, 0.5, v35, v71\n"
      "v_mfma_f32_16x16x32_bf16 v[80:83], a[120:123], a[212:215], v[80:83]\n"
      "v_cvt_pk_bf16_f32 v23, v23, v27\n"
      "v_fmac_f32 v44, s48, v56\n"
      "v_mfma_f32_16x16x32_bf16 v[84:87], a[112:115], a[212:215], v[84:87]\n"
      "ds_write_addtid_b32 v23 offset:14848\n"
      "v_fmac_f32 v45, s48, v57\n"
      "v_mfma_f32_16x16x32_bf16 v[88:91], v[248:251], a[192:195], v[76:79]\n"
      "ds_read_b128 a[208:211], v1 offset:20544\n"
      "v_fmac_f32 v46, s48, v58\n"
      "v_mfma_f32_16x16x32_bf16 v[92:95], v[240:243], a[192:195], v[80:83]\n"
      "v_fmac_f32 v47, s48, v59\n"
      "v_fmac_f32_e64 v32, s45, v44\n"
      "v_mfma_f32_16x16x32_bf16 v[96:99], v[232:235], a[192:195], v[84:87]\n"
      "s_waitcnt lgkmcnt(2)\n"
      "buffer_store_dwordx4 a[228:231], v16, s[8:11], s58 offen offset:256 sc1\n"
      "v_mfma_f32_16x16x32_bf16 v[88:91], v[224:227], a[196:199], v[88:91]\n"
      "ds_read_b128 a[212:215], v1 offset:20608\n"
      "v_fmac_f32 v33, s45, v45\n"
      "v_mfma_f32_4x4x4_16b_bf16 v[20:23], a[216:217], a[248:249], 0\n"
      "v_fmac_f32 v34, s45, v46\n"
      "v_mfma_f32_4x4x4_16b_bf16 v[24:27], a[216:217], a[250:251], 0\n"
      "v_fmac_f32 v35, s45, v47\n"
      "v_fma_f32 v44, s47, v44, -v56\n"
      "v_mfma_f32_16x16x32_bf16 v[92:95], v[216:219], a[196:199], v[92:95]\n"
      "v_fma_f32 v45, s47, v45, -v57\n"
      "v_fma_f32 v46, s47, v46, -v58\n"
      "v_cvt_pk_bf16_f32 v20, v20, v24\n"
      "v_mfma_f32_16x16x32_bf16 v[96:99], v[208:211], a[196:199], v[96:99]\n"
      "ds_write_addtid_b32 v20 offset:3072\n"
      "v_cvt_pk_bf16_f32 v21, v21, v25\n"
      "v_mfma_f32_16x16x32_bf16 v[88:91], v[200:203], a[200:203], v[88:91]\n"
      "ds_read_b128 a[192:195], v2 offset:20480\n"
      "ds_write_addtid_b32 v21 offset:7168\n"
      "v_mfma_f32_16x16x32_bf16 v[92:95], v[192:195], a[200:203], v[92:95]\n"
      "v_fma_f32 v47, s47, v47, -v59\n"
      "v_cvt_pk_bf16_f32 v32, v32, v33\n"
      "v_mfma_f32_16x16x32_bf16 v[96:99], v[184:187], a[200:203], v[96:99]\n"
      "ds_write_addtid_b32 v32 offset:40964\n"
      "v_cvt_pk_bf16_f32 v34, v34, v35\n"
      "v_mfma_f32_16x16x32_bf16 v[88:91], v[176:179], a[204:207], v[88:91]\n"
      "ds_read_b128 a[196:199], v2 offset:20544\n"
      "ds_write_addtid_b32 v34 offset:41476\n"
      "v_cvt_pk_bf16_f32 v44, v44, v45\n"
      "v_mfma_f32_16x16x32_bf16 v[92:95], v[168:171], a[204:207], v[92:95]\n"
      "ds_write_addtid_b32 v44 offset:43012\n"
      "v_cvt_pk_bf16_f32 v46, v46, v47\n"
      "v_cvt_pk_bf16_f32 v22, v22, v26\n"
      "v_mfma_f32_16x16x32_bf16 v[96:99], v[160:163], a[204:207], v[96:99]\n"
      "s_waitcnt lgkmcnt(4)\n"
      "ds_write_addtid_b32 v46 offset:43524\n"
      "ds_write_addtid_b32 v22 offset:11264\n"
      "v_mfma_f32_16x16x32_bf16 v[88:91], v[152:155], a[208:211], v[88:91]\n"
      "ds_read_b128 a[200:203], v2 offset:20608\n"
      "ds_read2st64_b32 a[232:233], v10 offset0:4 offset1:6\n"
      "v_fmac_f32 v48, s46, v36\n"
      "v_mfma_f32_16x16x32_bf16 v[92:95], v[144:147], a[208:211], v[92:95]\n"
      "ds_read2st64_b32 a[234:235], v13 offset0:4 offset1:6\n"
      "v_fmac_f32 v49, s46, v37\n"
      "v_mfma_f32_16x16x32_bf16 v[96:99], v[136:139], a[208:211], v[96:99]\n"
      "v_fmac_f32 v50, s46, v38\n"
      "v_cvt_pk_bf16_f32 v23, v23, v27\n"
      "v_mfma_f32_16x16x32_bf16 v[88:91], v[128:131], a[212:215], v[88:91]\n"
      "ds_read_b128 a[204:207], v3 offset:16896\n"
      "ds_write_addtid_b32 v23 offset:15360\n"
      "v_mfma_f32_16x16x32_bf16 v[92:95], v[120:123], a[212:215], v[92:95]\n"
      "v_fmac_f32 v51, s46, v39\n"
      "v_fma_f32 v36, 0.5, v36, v72\n"
      "v_fma_f32 v37, 0.5, v37, v73\n"
      "v_mfma_f32_16x16x32_bf16 v[96:99], v[112:115], a[212:215], v[96:99]\n"
      "v_fma_f32 v38, 0.5, v38, v74\n"
      "v_fma_f32 v39, 0.5, v39, v75\n"
      "v_mfma_f32_16x16x32_bf16 v[88:91], v[104:107], a[192:195], v[88:91]\n"
      "s_nop 0\n"
      "ds_read_b128 a[208:211], v3 offset:16960\n"
      "ds_read_b64_tr_b16 a[244:245], v7 offset:37376\n"
      "v_mfma_f32_16x16x32_bf16 v[92:95], a[164:167], a[192:195], v[92:95]\n"
      "ds_read_b64_tr_b16 a[246:247], v7 offset:37384\n"
      "v_fmac_f32 v48, s48, v60\n"
      "v_mfma_f32_16x16x32_bf16 v[96:99], a[156:159], a[192:195], v[96:99]\n"
      "s_waitcnt lgkmcnt(4)\n"
      "ds_read2st64_b32 a[236:237], v11 offset0:4 offset1:6\n"
      "ds_read2st64_b32 a[238:239], v14 offset0:4 offset1:6\n"
      "v_fmac_f32 v49, s48, v61\n"
      "v_mfma_f32_16x16x32_bf16 v[88:91], a[148:151], a[196:199], v[88:91]\n"
      "ds_read_b128 a[212:215], v3 offset:17024\n"
      "v_fmac_f32 v50, s48, v62\n"
      "v_mfma_f32_16x16x32_bf16 v[92:95], a[140:143], a[196:199], v[92:95]\n"
      "v_fmac_f32 v51, s48, v63\n"
      "v_fmac_f32 v36, s45, v48\n"
      "v_mfma_f32_16x16x32_bf16 v[96:99], a[132:135], a[196:199], v[96:99]\n"
      "v_fmac_f32 v37, s45, v49\n"
      "v_fmac_f32 v38, s45, v50\n"
      "v_mfma_f32_16x16x32_bf16 v[88:91], a[124:127], a[200:203], v[88:91]\n"
      "ds_read_b128 a[192:195], v4 offset:16896\n"
      "v_fmac_f32 v39, s45, v51\n"
      "v_mfma_f32_16x16x32_bf16 v[92:95], a[116:119], a[200:203], v[92:95]\n"
      "s_waitcnt lgkmcnt(2)\n"
      "v_fma_f32 v48, s47, v48, -v60\n"
      "v_fma_f32 v49, s47, v49, -v61\n"
      "v_mfma_f32_4x4x4_16b_bf16 v[20:23], a[216:217], a[244:245], 0\n"
      "v_fma_f32 v50, s47, v50, -v62\n"
      "v_mfma_f32_4x4x4_16b_bf16 v[24:27], a[216:217], a[246:247], 0\n"
      "v_fma_f32 v51, s47, v51, -v63\n"
      "v_cvt_pk_bf16_f32 v36, v36, v37\n"
      "v_mfma_f32_16x16x32_bf16 v[96:99], a[108:111], a[200:203], v[96:99]\n"
      "buffer_store_dwordx4 a[232:235], v16, s[8:11], s59 offen offset:0 sc1\n"
      "ds_write_addtid_b32 v36 offset:36864\n"
      "v_mfma_f32_16x16x32_bf16 v[64:67], a[0:3], a[204:207], 0\n"
      "ds_read_b128 a[196:199], v4 offset:16960\n"
      "ds_write_addtid_b32 v88 offset:51456\n"
      "v_mfma_f32_16x16x32_bf16 v[68:71], a[4:7], a[204:207], 0\n"
      "ds_write_addtid_b32 v89 offset:51968\n"
      "ds_write_addtid_b32 v90 offset:52480\n"
      "v_cvt_pk_bf16_f32 v20, v20, v24\n"
      "v_mfma_f32_16x16x32_bf16 v[72:75], a[8:11], a[204:207], 0\n"
      "ds_write_addtid_b32 v20 offset:3584\n"
      "ds_write_addtid_b32 v91 offset:52992\n"
      "v_mfma_f32_16x16x32_bf16 v[64:67], a[12:15], a[208:211], v[64:67]\n"
      "ds_read_b128 a[200:203], v4 offset:17024\n"
      "ds_write_addtid_b32 v92 offset:53504\n"
      "v_mfma_f32_16x16x32_bf16 v[68:71], a[16:19], a[208:211], v[68:71]\n"
      "s_nop 0\n"
      "ds_write_addtid_b32 v93 offset:54016\n"
      "ds_write_addtid_b32 v94 offset:54528\n"
      "v_mfma_f32_16x16x32_bf16 v[72:75], a[20:23], a[208:211], v[72:75]\n"
      "s_waitcnt lgkmcnt(8)\n"
      "ds_write_addtid_b32 v95 offset:55040\n"
      "ds_write_addtid_b32 v96 offset:55552\n"
      "v_cvt_pk_bf16_f32 v38, v38, v39\n"
      "v_mfma_f32_16x16x32_bf16 v[64:67], a[24:27], a[212:215], v[64:67]\n"
      "ds_read_b128 a[204:207], v5 offset:16896\n"
      "ds_write_addtid_b32 v38 offset:37376\n"
      "v_mfma_f32_16x16x32_bf16 v[68:71], a[28:31], a[212:215], v[68:71]\n"
      "buffer_store_dwordx4 a[236:239], v16, s[8:11], s59 offen offset:128 sc1\n"
      "v_mfma_f32_16x16x32_bf16 v[72:75], a[32:35], a[212:215], v[72:75]\n"
      "ds_write_addtid_b32 v97 offset:56064\n"
      "ds_write_addtid_b32 v98 offset:56576\n"
      "v_mfma_f32_16x16x32_bf16 v[64:67], a[36:39], a[192:195], v[64:67]\n"
      "ds_read_b128 a[208:211], v5 offset:16960\n"
      "ds_write_addtid_b32 v99 offset:57088\n"
      "v_cvt_pk_bf16_f32 v48, v48, v49\n"
      "v_cvt_pk_bf16_f32 v50, v50, v51\n"
      "v_mfma_f32_16x16x32_bf16 v[68:71], a[40:43], a[192:195], v[68:71]\n"
      "ds_write_addtid_b32 v48 offset:38912\n"
      "ds_write_addtid_b32 v50 offset:39424\n"
      "v_mfma_f32_16x16x32_bf16 v[72:75], a[44:47], a[192:195], v[72:75]\n"
      "v_cvt_pk_bf16_f32 v21, v21, v25\n"
      "ds_write_addtid_b32 v21 offset:7680\n"
      "v_cvt_pk_bf16_f32 v22, v22, v26\n"
      "ds_write_addtid_b32 v22 offset:11776\n"
      "v_cvt_pk_bf16_f32 v23, v23, v27\n"
      "ds_write_addtid_b32 v23 offset:15872\n"
      "v_mfma_f32_16x16x32_bf16 v[64:67], a[48:51], a[196:199], v[64:67]\n"
      "ds_read_b128 a[212:215], v5 offset:17024\n"
      "v_fma_f32 v40, 2.0, v76, -v88\n"
      "v_mfma_f32_16x16x32_bf16 v[68:71], a[52:55], a[196:199], v[68:71]\n"
      "v_fma_f32 v41, 2.0, v77, -v89\n"
      "v_fma_f32 v42, 2.0, v78, -v90\n"
      "v_mfma_f32_16x16x32_bf16 v[72:75], a[56:59], a[196:199], v[72:75]\n"
      "s_waitcnt lgkmcnt(3)\n"
      "v_fma_f32 v43, 2.0, v79, -v91\n"
      "v_fma_f32 v44, 2.0, v80, -v92\n"
      "v_mfma_f32_16x16x32_bf16 v[64:67], a[60:63], a[200:203], v[64:67]\n"
      "v_fma_f32 v45, 2.0, v81, -v93\n"
      "v_mfma_f32_16x16x32_bf16 v[68:71], a[64:67], a[200:203], v[68:71]\n"
      "s_waitcnt vmcnt(16)\n"
      "v_mfma_f32_16x16x32_bf16 v[72:75], a[68:71], a[200:203], v[72:75]\n"
      "s_waitcnt lgkmcnt(0)\n"
      "s_barrier\n"
      "v_mfma_f32_16x16x32_bf16 v[64:67], a[72:75], a[204:207], v[64:67]\n"
      "s_add_u32 s58, s40, 0\n"
      "ds_read_b128 a[192:195], v0 offset:0\n"
      "ds_read2st64_b32 a[220:221], v10 offset0:177 offset1:179\n"
      "v_fma_f32 v46, 2.0, v82, -v94\n"
      "v_mfma_f32_16x16x32_bf16 v[68:71], a[76:79], a[204:207], v[68:71]\n"
      "s_mov_b32 s40, s62\n"
      "ds_read_b128 a[196:199], v0 offset:64\n"
      "ds_read2st64_b32 v[28:29], v6 offset0:24 offset1:26\n"
      "v_mfma_f32_16x16x32_bf16 v[72:75], a[80:83], a[204:207], v[72:75]\n"
      "s_add_u32 s62, s62, 786432\n"
      "ds_read2st64_b32 a[222:223], v13 offset0:177 offset1:179\n"
      "ds_read2st64_b32 v[30:31], v6 offset0:28 offset1:30\n"
      "v_mfma_f32_16x16x32_bf16 v[64:67], a[84:87], a[208:211], v[64:67]\n"
      "s_add_u32 s59, s58, 196608\n"
      "ds_read_b128 a[200:203], v0 offset:128\n"
      "v_fma_f32 v47, 2.0, v83, -v95\n"
      "v_mfma_f32_16x16x32_bf16 v[68:71], a[88:91], a[208:211], v[68:71]\n"
      "ds_read2st64_b32 v[32:33], v6 offset0:32 offset1:34\n"
      "v_fma_f32 v48, 2.0, v84, -v96\n"
      "v_mfma_f32_16x16x32_bf16 v[72:75], a[92:95], a[208:211], v[72:75]\n"
      "v_fma_f32 v49, 2.0, v85, -v97\n"
      "v_fma_f32 v50, 2.0, v86, -v98\n"
      "v_fma_f32 v51, 2.0, v87, -v99\n"
      "v_mfma_f32_16x16x32_bf16 v[64:67], a[96:99], a[212:215], v[64:67]\n"
      "ds_read_b128 a[204:207], v1 offset:0\n"
      "ds_read2st64_b32 v[34:35], v6 offset0:36 offset1:38\n"
      "v_mfma_f32_16x16x32_bf16 v[68:71], a[100:103], a[212:215], v[68:71]\n"
      "ds_read2st64_b32 v[36:37], v6 offset0:40 offset1:42\n"
      "v_mfma_f32_16x16x32_bf16 v[72:75], a[104:107], a[212:215], v[72:75]\n"
      "s_waitcnt lgkmcnt(7)\n"
      "ds_read2st64_b32 v[38:39], v6 offset0:44 offset1:46\n"
      "v_mfma_f32_16x16x32_bf16 v[76:79], v[252:255], a[192:195], 0\n"
      "s_nop 0\n"
      "ds_read_b128 a[208:211], v1 offset:64\n"
      "v_mfma_f32_16x16x32_bf16 v[80:83], v[244:247], a[192:195], 0\n"
      "ds_read2st64_b32 a[224:225], v11 offset0:177 offset1:179\n"
      "v_mfma_f32_16x16x32_bf16 v[84:87], v[236:239], a[192:195], 0\n"
      "ds_read2st64_b32 a[226:227], v14 offset0:177 offset1:179\n"
      "v_mfma_f32_16x16x32_bf16 v[76:79], v[228:231], a[196:199], v[76:79]\n"
      "s_waitcnt lgkmcnt(7)\n"
      "ds_read_b128 a[212:215], v1 offset:128\n"
      "v_fmac_f32 v40, s46, v28\n"
      "v_fmac_f32 v41, s46, v29\n"
      "v_fmac_f32 v42, s46, v30\n"
      "v_fmac_f32 v43, s46, v31\n"
      "v_fma_f32 v28, 0.5, v28, v52\n"
      "v_mfma_f32_16x16x32_bf16 v[80:83], v[220:223], a[196:199], v[80:83]\n"
      "buffer_store_dwordx4 a[220:223], v16, s[8:11], s58 offen offset:0 sc1\n"
      "v_fma_f32 v29, 0.5, v29, v53\n"
      "v_mfma_f32_16x16x32_bf16 v[84:87], v[212:215], a[196:199], v[84:87]\n"
      "s_nop 0\n"
      "v_fma_f32 v30, 0.5, v30, v54\n"
      "v_fma_f32 v31, 0.5, v31, v55\n"
      "v_fmac_f32 v40, s48, v64\n"
      "v_fmac_f32 v41, s48, v65\n"
      "v_mfma_f32_16x16x32_bf16 v[76:79], v[204:207], a[200:203], v[76:79]\n"
      "ds_read_b128 a[192:195], v2 offset:0\n"
      "v_fmac_f32 v42, s48, v66\n"
      "v_mfma_f32_16x16x32_bf16 v[80:83], v[196:199], a[200:203], v[80:83]\n"
      "s_nop 0\n"
      "ds_read_b64_tr_b16 a[244:245], v7 offset:53248\n"
      "ds_read_b64_tr_b16 a[246:247], v7 offset:53256\n"
      "v_mfma_f32_16x16x32_bf16 v[84:87], v[188:191], a[200:203], v[84:87]\n"
      "s_waitcnt lgkmcnt(3)\n"
      "v_fmac_f32 v43, s48, v67\n"
      "v_fmac_f32 v28, s45, v40\n"
      "v_mfma_f32_16x16x32_bf16 v[76:79], v[180:183], a[204:207], v[76:79]\n"
      "ds_read_b128 a[196:199], v2 offset:64\n"
      "v_fmac_f32 v29, s45, v41\n"
      "v_mfma_f32_16x16x32_bf16 v[80:83], v[172:175], a[204:207], v[80:83]\n"
      "v_fmac_f32_e64 v30, s45, v42\n"
      "v_fmac_f32_e64 v31, s45, v43\n"
      "v_mfma_f32_16x16x32_bf16 v[84:87], v[164:167], a[204:207], v[84:87]\n"
      "s_nop 0\n"
      "v_fma_f32 v40, s47, v40, -v64\n"
      "v_fma_f32 v41, s47, v41, -v65\n"
      "v_mfma_f32_16x16x32_bf16 v[76:79], v[156:159], a[208:211], v[76:79]\n"
      "s_nop 0\n"
      "ds_read_b128 a[200:203], v2 offset:128\n"
      "v_fma_f32 v42, s47, v42, -v66\n"
      "v_mfma_f32_16x16x32_bf16 v[80:83], v[148:151], a[208:211], v[80:83]\n"
      "s_nop 0\n"
      "v_fma_f32 v43, s47, v43, -v67\n"
      "v_cvt_pk_bf16_f32 v28, v28, v29\n"
      "v_mfma_f32_16x16x32_bf16 v[84:87], v[140:143], a[208:211], v[84:87]\n"
      "s_waitcnt lgkmcnt(1)\n"
      "ds_write_addtid_b32 v28 offset:33792\n"
      "v_cvt_pk_bf16_f32 v30, v30, v31\n"
      "v_mfma_f32_4x4x4_16b_bf16 v[20:23], a[216:217], a[244:245], 0\n"
      "v_cvt_pk_bf16_f32 v40, v40, v41\n"
      "v_mfma_f32_4x4x4_16b_bf16 v[24:27], a[216:217], a[246:247], 0\n"
      "ds_write_addtid_b32 v30 offset:34304\n"
      "ds_write_addtid_b32 v40 offset:35840\n"
      "v_cvt_pk_bf16_f32 v42, v42, v43\n"
      "v_mfma_f32_16x16x32_bf16 v[76:79], v[132:135], a[212:215], v[76:79]\n"
      "buffer_store_dwordx4 a[224:227], v16, s[8:11], s58 offen offset:128 sc1\n"
      "ds_read_b128 a[204:207], v0 offset:4096\n"
      "v_mfma_f32_16x16x32_bf16 v[80:83], v[124:127], a[212:215], v[80:83]\n"
      "ds_write_addtid_b32 v42 offset:36352\n"
      "ds_read2st64_b32 a[228:229], v12 offset0:177 offset1:179\n"
      "v_fmac_f32 v44, s46, v32\n"
      "v_cvt_pk_bf16_f32 v20, v20, v24\n"
      "v_mfma_f32_16x16x32_bf16 v[84:87], v[116:119], a[212:215], v[84:87]\n"
      "ds_write_addtid_b32 v20 offset:18944\n"
      "ds_read2st64_b32 a[230:231], v15 offset0:177 offset1:179\n"
      "v_mfma_f32_16x16x32_bf16 v[76:79], v[108:111], a[192:195], v[76:79]\n"
      "s_nop 0\n"
      "ds_read_b128 a[208:211], v0 offset:4160\n"
      "v_fmac_f32 v45, s46, v33\n"
      "v_mfma_f32_16x16x32_bf16 v[80:83], v[100:103], a[192:195], v[80:83]\n"
      "v_fmac_f32 v46, s46, v34\n"
      "v_cvt_pk_bf16_f32 v21, v21, v25\n"
      "v_fmac_f32 v47, s46, v35\n"
      "v_mfma_f32_16x16x32_bf16 v[84:87], a[160:163], a[192:195], v[84:87]\n"
      "s_nop 0\n"
      "ds_write_addtid_b32 v21 offset:23040\n"
      "v_fma_f32 v32, 0.5, v32, v56\n"
      "v_mfma_f32_16x16x32_bf16 v[76:79], a[152:155], a[196:199], v[76:79]\n"
      "ds_read_b128 a[212:215], v0 offset:4224\n"
      "v_cvt_pk_bf16_f32 v22, v22, v26\n"
      "v_mfma_f32_16x16x32_bf16 v[80:83], a[144:147], a[196:199], v[80:83]\n"
      "s_nop 0\n"
      "ds_write_addtid_b32 v22 offset:27136\n"
      "v_fma_f32 v33, 0.5, v33, v57\n"
      "v_mfma_f32_16x16x32_bf16 v[84:87], a[136:139], a[196:199], v[84:87]\n"
      "s_waitcnt lgkmcnt(4)\n"
      "ds_read_b64_tr_b16 a[248:249], v7 offset:53760\n"
      "ds_read_b64_tr_b16 a[250:251], v7 offset:53768\n"
      "v_fma_f32 v34, 0.5, v34, v58\n"
      "v_mfma_f32_16x16x32_bf16 v[76:79], a[128:131], a[200:203], v[76:79]\n"
      "ds_read_b128 a[192:195], v1 offset:4096\n"
      "v_fma_f32 v35, 0.5, v35, v59\n"
      "v_mfma_f32_16x16x32_bf16 v[80:83], a[120:123], a[200:203], v[80:83]\n"
      "v_cvt_pk_bf16_f32 v23, v23, v27\n"
      "v_fmac_f32 v44, s48, v68\n"
      "v_mfma_f32_16x16x32_bf16 v[84:87], a[112:115], a[200:203], v[84:87]\n"
      "ds_write_addtid_b32 v23 offset:31232\n"
      "v_fmac_f32 v45, s48, v69\n"
      "v_mfma_f32_16x16x32_bf16 v[88:91], v[248:251], a[204:207], v[76:79]\n"
      "ds_read_b128 a[196:199], v1 offset:4160\n"
      "v_fmac_f32 v46, s48, v70\n"
      "v_mfma_f32_16x16x32_bf16 v[92:95], v[240:243], a[204:207], v[80:83]\n"
      "v_fmac_f32 v47, s48, v71\n"
      "v_fmac_f32_e64 v32, s45, v44\n"
      "v_mfma_f32_16x16x32_bf16 v[96:99], v[232:235], a[204:207], v[84:87]\n"
      "s_waitcnt lgkmcnt(2)\n"
      "buffer_store_dwordx4 a[228:231], v16, s[8:11], s58 offen offset:256 sc1\n"
      "v_mfma_f32_16x16x32_bf16 v[88:91], v[224:227], a[208:211], v[88:91]\n"
      "ds_read_b128 a[200:203], v1 offset:4224\n"
      "v_fmac_f32 v33, s45, v45\n"
      "v_mfma_f32_4x4x4_16b_bf16 v[20:23], a[216:217], a[248:249], 0\n"
      "v_fmac_f32 v34, s45, v46\n"
      "v_mfma_f32_4x4x4_16b_bf16 v[24:27], a[216:217], a[250:251], 0\n"
      "v_fmac_f32 v35, s45, v47\n"
      "v_fma_f32 v44, s47, v44, -v68\n"
      "v_mfma_f32_16x16x32_bf16 v[92:95], v[216:219], a[208:211], v[92:95]\n"
      "v_fma_f32 v45, s47, v45, -v69\n"
      "v_fma_f32 v46, s47, v46, -v70\n"
      "v_cvt_pk_bf16_f32 v20, v20, v24\n"
      "v_mfma_f32_16x16x32_bf16 v[96:99], v[208:211], a[208:211], v[96:99]\n"
      "ds_write_addtid_b32 v20 offset:19456\n"
      "v_cvt_pk_bf16_f32 v21, v21, v25\n"
      "v_mfma_f32_16x16x32_bf16 v[88:91], v[200:203], a[212:215], v[88:91]\n"
      "ds_read_b128 a[204:207], v2 offset:4096\n"
      "ds_write_addtid_b32 v21 offset:23552\n"
      "v_mfma_f32_16x16x32_bf16 v[92:95], v[192:195], a[212:215], v[92:95]\n"
      "v_fma_f32 v47, s47, v47, -v71\n"
      "v_cvt_pk_bf16_f32 v32, v32, v33\n"
      "v_mfma_f32_16x16x32_bf16 v[96:99], v[184:187], a[212:215], v[96:99]\n"
      "ds_write_addtid_b32 v32 offset:41988\n"
      "v_cvt_pk_bf16_f32 v34, v34, v35\n"
      "v_mfma_f32_16x16x32_bf16 v[88:91], v[176:179], a[192:195], v[88:91]\n"
      "ds_read_b128 a[208:211], v2 offset:4160\n"
      "ds_write_addtid_b32 v34 offset:42500\n"
      "v_cvt_pk_bf16_f32 v44, v44, v45\n"
      "v_mfma_f32_16x16x32_bf16 v[92:95], v[168:171], a[192:195], v[92:95]\n"
      "ds_write_addtid_b32 v44 offset:44036\n"
      "v_cvt_pk_bf16_f32 v46, v46, v47\n"
      "v_cvt_pk_bf16_f32 v22, v22, v26\n"
      "v_mfma_f32_16x16x32_bf16 v[96:99], v[160:163], a[192:195], v[96:99]\n"
      "s_waitcnt lgkmcnt(4)\n"
      "ds_write_addtid_b32 v46 offset:44548\n"
      "ds_write_addtid_b32 v22 offset:27648\n"
      "v_mfma_f32_16x16x32_bf16 v[88:91], v[152:155], a[196:199], v[88:91]\n"
      "ds_read_b128 a[212:215], v2 offset:4224\n"
      "ds_read2st64_b32 a[232:233], v10 offset0:0 offset1:2\n"
      "v_fmac_f32 v48, s46, v36\n"
      "v_mfma_f32_16x16x32_bf16 v[92:95], v[144:147], a[196:199], v[92:95]\n"
      "ds_read2st64_b32 a[234:235], v13 offset0:0 offset1:2\n"
      "v_fmac_f32 v49, s46, v37\n"
      "v_mfma_f32_16x16x32_bf16 v[96:99], v[136:139], a[196:199], v[96:99]\n"
      "v_fmac_f32 v50, s46, v38\n"
      "v_cvt_pk_bf16_f32 v23, v23, v27\n"
      "v_mfma_f32_16x16x32_bf16 v[88:91], v[128:131], a[200:203], v[88:91]\n"
      "ds_read_b128 a[192:195], v3 offset:33792\n"
      "ds_write_addtid_b32 v23 offset:31744\n"
      "v_mfma_f32_16x16x32_bf16 v[92:95], v[120:123], a[200:203], v[92:95]\n"
      "v_fmac_f32 v51, s46, v39\n"
      "v_fma_f32 v36, 0.5, v36, v60\n"
      "v_fma_f32 v37, 0.5, v37, v61\n"
      "v_mfma_f32_16x16x32_bf16 v[96:99], v[112:115], a[200:203], v[96:99]\n"
      "v_fma_f32 v38, 0.5, v38, v62\n"
      "v_fma_f32 v39, 0.5, v39, v63\n"
      "v_mfma_f32_16x16x32_bf16 v[88:91], v[104:107], a[204:207], v[88:91]\n"
      "s_nop 0\n"
      "ds_read_b128 a[196:199], v3 offset:33856\n"
      "ds_read_b64_tr_b16 a[244:245], v7 offset:54272\n"
      "v_mfma_f32_16x16x32_bf16 v[92:95], a[164:167], a[204:207], v[92:95]\n"
      "ds_read_b64_tr_b16 a[246:247], v7 offset:54280\n"
      "v_fmac_f32 v48, s48, v72\n"
      "v_mfma_f32_16x16x32_bf16 v[96:99], a[156:159], a[204:207], v[96:99]\n"
      "s_waitcnt lgkmcnt(4)\n"
      "ds_read2st64_b32 a[236:237], v11 offset0:0 offset1:2\n"
      "ds_read2st64_b32 a[238:239], v14 offset0:0 offset1:2\n"
      "v_fmac_f32 v49, s48, v73\n"
      "v_mfma_f32_16x16x32_bf16 v[88:91], a[148:151], a[208:211], v[88:91]\n"
      "ds_read_b128 a[200:203], v3 offset:33920\n"
      "v_fmac_f32 v50, s48, v74\n"
      "v_mfma_f32_16x16x32_bf16 v[92:95], a[140:143], a[208:211], v[92:95]\n"
      "v_fmac_f32 v51, s48, v75\n"
      "v_fmac_f32 v36, s45, v48\n"
      "v_mfma_f32_16x16x32_bf16 v[96:99], a[132:135], a[208:211], v[96:99]\n"
      "v_fmac_f32 v37, s45, v49\n"
      "v_fmac_f32 v38, s45, v50\n"
      "v_mfma_f32_16x16x32_bf16 v[88:91], a[124:127], a[212:215], v[88:91]\n"
      "ds_read_b128 a[204:207], v4 offset:33792\n"
      "v_fmac_f32 v39, s45, v51\n"
      "v_mfma_f32_16x16x32_bf16 v[92:95], a[116:119], a[212:215], v[92:95]\n"
      "s_waitcnt lgkmcnt(2)\n"
      "v_fma_f32 v48, s47, v48, -v72\n"
      "v_fma_f32 v49, s47, v49, -v73\n"
      "v_mfma_f32_4x4x4_16b_bf16 v[20:23], a[216:217], a[244:245], 0\n"
      "v_fma_f32 v50, s47, v50, -v74\n"
      "v_mfma_f32_4x4x4_16b_bf16 v[24:27], a[216:217], a[246:247], 0\n"
      "v_fma_f32 v51, s47, v51, -v75\n"
      "v_cvt_pk_bf16_f32 v36, v36, v37\n"
      "v_mfma_f32_16x16x32_bf16 v[96:99], a[108:111], a[212:215], v[96:99]\n"
      "buffer_store_dwordx4 a[232:235], v16, s[8:11], s59 offen offset:0 sc1\n"
      "ds_write_addtid_b32 v36 offset:37888\n"
      "v_mfma_f32_16x16x32_bf16 v[52:55], a[0:3], a[192:195], 0\n"
      "ds_read_b128 a[208:211], v4 offset:33856\n"
      "ds_write_addtid_b32 v88 offset:45312\n"
      "v_mfma_f32_16x16x32_bf16 v[56:59], a[4:7], a[192:195], 0\n"
      "ds_write_addtid_b32 v89 offset:45824\n"
      "ds_write_addtid_b32 v90 offset:46336\n"
      "v_cvt_pk_bf16_f32 v20, v20, v24\n"
      "v_mfma_f32_16x16x32_bf16 v[60:63], a[8:11], a[192:195], 0\n"
      "ds_write_addtid_b32 v20 offset:19968\n"
      "ds_write_addtid_b32 v91 offset:46848\n"
      "v_mfma_f32_16x16x32_bf16 v[52:55], a[12:15], a[196:199], v[52:55]\n"
      "ds_read_b128 a[212:215], v4 offset:33920\n"
      "ds_write_addtid_b32 v92 offset:47360\n"
      "v_mfma_f32_16x16x32_bf16 v[56:59], a[16:19], a[196:199], v[56:59]\n"
      "s_nop 0\n"
      "ds_write_addtid_b32 v93 offset:47872\n"
      "ds_write_addtid_b32 v94 offset:48384\n"
      "v_mfma_f32_16x16x32_bf16 v[60:63], a[20:23], a[196:199], v[60:63]\n"
      "s_waitcnt lgkmcnt(8)\n"
      "ds_write_addtid_b32 v95 offset:48896\n"
      "ds_write_addtid_b32 v96 offset:49408\n"
      "v_cvt_pk_bf16_f32 v38, v38, v39\n"
      "v_mfma_f32_16x16x32_bf16 v[52:55], a[24:27], a[200:203], v[52:55]\n"
      "ds_read_b128 a[192:195], v5 offset:33792\n"
      "ds_write_addtid_b32 v38 offset:38400\n"
      "v_mfma_f32_16x16x32_bf16 v[56:59], a[28:31], a[200:203], v[56:59]\n"
      "buffer_store_dwordx4 a[236:239], v16, s[8:11], s59 offen offset:128 sc1\n"
      "v_mfma_f32_16x16x32_bf16 v[60:63], a[32:35], a[200:203], v[60:63]\n"
      "ds_write_addtid_b32 v97 offset:49920\n"
      "ds_write_addtid_b32 v98 offset:50432\n"
      "v_mfma_f32_16x16x32_bf16 v[52:55], a[36:39], a[204:207], v[52:55]\n"
      "ds_read_b128 a[196:199], v5 offset:33856\n"
      "ds_write_addtid_b32 v99 offset:50944\n"
      "v_cvt_pk_bf16_f32 v48, v48, v49\n"
      "v_cvt_pk_bf16_f32 v50, v50, v51\n"
      "v_mfma_f32_16x16x32_bf16 v[56:59], a[40:43], a[204:207], v[56:59]\n"
      "ds_write_addtid_b32 v48 offset:39936\n"
      "ds_write_addtid_b32 v50 offset:40448\n"
      "v_mfma_f32_16x16x32_bf16 v[60:63], a[44:47], a[204:207], v[60:63]\n"
      "v_cvt_pk_bf16_f32 v21, v21, v25\n"
      "ds_write_addtid_b32 v21 offset:24064\n"
      "v_cvt_pk_bf16_f32 v22, v22, v26\n"
      "ds_write_addtid_b32 v22 offset:28160\n"
      "v_cvt_pk_bf16_f32 v23, v23, v27\n"
      "ds_write_addtid_b32 v23 offset:32256\n"
      "v_mfma_f32_16x16x32_bf16 v[52:55], a[48:51], a[208:211], v[52:55]\n"
      "ds_read_b128 a[200:203], v5 offset:33920\n"
      "v_fma_f32 v40, 2.0, v76, -v88\n"
      "v_mfma_f32_16x16x32_bf16 v[56:59], a[52:55], a[208:211], v[56:59]\n"
      "v_fma_f32 v41, 2.0, v77, -v89\n"
      "v_fma_f32 v42, 2.0, v78, -v90\n"
      "v_mfma_f32_16x16x32_bf16 v[60:63], a[56:59], a[208:211], v[60:63]\n"
      "s_waitcnt lgkmcnt(3)\n"
      "v_fma_f32 v43, 2.0, v79, -v91\n"
      "v_fma_f32 v44, 2.0, v80, -v92\n"
      "v_mfma_f32_16x16x32_bf16 v[52:55], a[60:63], a[212:215], v[52:55]\n"
      "v_fma_f32 v45, 2.0, v81, -v93\n"
      "v_mfma_f32_16x16x32_bf16 v[56:59], a[64:67], a[212:215], v[56:59]\n"
      "s_waitcnt vmcnt(11)\n"
      "v_mfma_f32_16x16x32_bf16 v[60:63], a[68:71], a[212:215], v[60:63]\n"
      "s_waitcnt lgkmcnt(0)\n"
      "s_barrier\n"
      "v_mfma_f32_16x16x32_bf16 v[52:55], a[72:75], a[192:195], v[52:55]\n"
      "s_add_u32 s58, s40, 0\n"
      "ds_read_b128 a[204:207], v0 offset:16384\n"
      "ds_read2st64_b32 v[28:29], v6 offset0:0 offset1:2\n"
      "v_fma_f32 v46, 2.0, v82, -v94\n"
      "v_mfma_f32_16x16x32_bf16 v[56:59], a[76:79], a[192:195], v[56:59]\n"
      "s_mov_b32 s40, s62\n"
      "ds_read_b128 a[208:211], v0 offset:16448\n"
      "ds_read2st64_b32 v[30:31], v6 offset0:4 offset1:6\n"
      "v_mfma_f32_16x16x32_bf16 v[60:63], a[80:83], a[192:195], v[60:63]\n"
      "s_add_u32 s62, s62, 786432\n"
      "ds_read2st64_b32 v[32:33], v6 offset0:8 offset1:10\n"
      "ds_read2st64_b32 v[34:35], v6 offset0:12 offset1:14\n"
      "v_mfma_f32_16x16x32_bf16 v[52:55], a[84:87], a[196:199], v[52:55]\n"
      "ds_read_b128 a[212:215], v0 offset:16512\n"
      "v_fma_f32 v47, 2.0, v83, -v95\n"
      "v_mfma_f32_16x16x32_bf16 v[56:59], a[88:91], a[196:199], v[56:59]\n"
      "v_fma_f32 v48, 2.0, v84, -v96\n"
      "v_fma_f32 v49, 2.0, v85, -v97\n"
      "v_mfma_f32_16x16x32_bf16 v[60:63], a[92:95], a[196:199], v[60:63]\n"
      "ds_read2st64_b32 v[36:37], v6 offset0:16 offset1:18\n"
      "ds_read2st64_b32 v[38:39], v6 offset0:20 offset1:22\n"
      "v_mfma_f32_16x16x32_bf16 v[52:55], a[96:99], a[200:203], v[52:55]\n"
      "ds_read_b128 a[192:195], v1 offset:16384\n"
      "v_fma_f32 v50, 2.0, v86, -v98\n"
      "v_mfma_f32_16x16x32_bf16 v[56:59], a[100:103], a[200:203], v[56:59]\n"
      "v_fma_f32 v51, 2.0, v87, -v99\n"
      "v_mfma_f32_16x16x32_bf16 v[60:63], a[104:107], a[200:203], v[60:63]\n"
      "s_waitcnt lgkmcnt(6)\n"
      "v_fmac_f32 v40, s46, v28\n"
      "v_fmac_f32 v41, s46, v29\n"
      "v_fmac_f32 v42, s46, v30\n"
      "v_fmac_f32 v43, s46, v31\n"
      "v_mfma_f32_16x16x32_bf16 v[76:79], v[252:255], a[204:207], 0\n"
      "s_nop 0\n"
      "ds_read_b128 a[196:199], v1 offset:16448\n"
      "ds_read2st64_b32 a[220:221], v10 offset0:181 offset1:183\n"
      "v_mfma_f32_16x16x32_bf16 v[80:83], v[244:247], a[204:207], 0\n"
      "ds_read2st64_b32 a[222:223], v13 offset0:181 offset1:183\n"
      "v_fma_f32 v28, 0.5, v28, v64\n"
      "v_mfma_f32_16x16x32_bf16 v[84:87], v[236:239], a[204:207], 0\n"
      "v_fma_f32 v29, 0.5, v29, v65\n"
      "v_fma_f32 v30, 0.5, v30, v66\n"
      "v_mfma_f32_16x16x32_bf16 v[76:79], v[228:231], a[208:211], v[76:79]\n"
      "ds_read_b128 a[200:203], v1 offset:16512\n"
      "v_fma_f32 v31, 0.5, v31, v67\n"
      "v_mfma_f32_16x16x32_bf16 v[80:83], v[220:223], a[208:211], v[80:83]\n"
      "v_fmac_f32 v40, s48, v52\n"
      "v_fmac_f32 v41, s48, v53\n"
      "v_mfma_f32_16x16x32_bf16 v[84:87], v[212:215], a[208:211], v[84:87]\n"
      "s_waitcnt lgkmcnt(3)\n"
      "v_fmac_f32 v42, s48, v54\n"
      "v_fmac_f32 v43, s48, v55\n"
      "v_fmac_f32 v28, s45, v40\n"
      "v_mfma_f32_16x16x32_bf16 v[76:79], v[204:207], a[212:215], v[76:79]\n"
      "ds_read_b128 a[204:207], v2 offset:16384\n"
      "v_fmac_f32 v29, s45, v41\n"
      "v_mfma_f32_16x16x32_bf16 v[80:83], v[196:199], a[212:215], v[80:83]\n"
      "v_fmac_f32 v30, s45, v42\n"
      "v_fmac_f32 v31, s45, v43\n"
      "v_mfma_f32_16x16x32_bf16 v[84:87], v[188:191], a[212:215], v[84:87]\n"
      "ds_read2st64_b32 a[224:225], v11 offset0:181 offset1:183\n"
      "ds_read2st64_b32 a[226:227], v14 offset0:181 offset1:183\n"
      "v_mfma_f32_16x16x32_bf16 v[76:79], v[180:183], a[192:195], v[76:79]\n"
      "s_nop 0\n"
      "ds_read_b128 a[208:211], v2 offset:16448\n"
      "v_fma_f32 v40, s47, v40, -v52\n"
      "v_mfma_f32_16x16x32_bf16 v[80:83], v[172:175], a[192:195], v[80:83]\n"
      "v_fma_f32 v41, s47, v41, -v53\n"
      "v_fma_f32 v42, s47, v42, -v54\n"
      "v_mfma_f32_16x16x32_bf16 v[84:87], v[164:167], a[192:195], v[84:87]\n"
      "v_fma_f32 v43, s47, v43, -v55\n"
      "v_cvt_pk_bf16_f32 v28, v28, v29\n"
      "v_mfma_f32_16x16x32_bf16 v[76:79], v[156:159], a[196:199], v[76:79]\n"
      "ds_read_b128 a[212:215], v2 offset:16512\n"
      "ds_write_addtid_b32 v28 offset:32768\n"
      "v_mfma_f32_16x16x32_bf16 v[80:83], v[148:151], a[196:199], v[80:83]\n"
      "s_waitcnt lgkmcnt(3)\n"
      "v_cvt_pk_bf16_f32 v30, v30, v31\n"
      "v_cvt_pk_bf16_f32 v40, v40, v41\n"
      "v_mfma_f32_16x16x32_bf16 v[84:87], v[140:143], a[196:199], v[84:87]\n"
      "s_nop 0\n"
      "buffer_store_dwordx4 a[220:223], v16, s[8:11], s58 offen offset:0 sc1\n"
      "ds_write_addtid_b32 v30 offset:33280\n"
      "v_mfma_f32_16x16x32_bf16 v[76:79], v[132:135], a[200:203], v[76:79]\n"
      "ds_read_b128 a[192:195], v0 offset:20480\n"
      "ds_write_addtid_b32 v40 offset:34816\n"
      "v_mfma_f32_16x16x32_bf16 v[80:83], v[124:127], a[200:203], v[80:83]\n"
      "v_cvt_pk_bf16_f32 v42, v42, v43\n"
      "v_fmac_f32_e64 v44, s46, v32\n"
      "v_mfma_f32_16x16x32_bf16 v[84:87], v[116:119], a[200:203], v[84:87]\n"
      "ds_write_addtid_b32 v42 offset:35328\n"
      "v_fmac_f32 v45, s46, v33\n"
      "v_fmac_f32 v46, s46, v34\n"
      "v_mfma_f32_16x16x32_bf16 v[76:79], v[108:111], a[204:207], v[76:79]\n"
      "ds_read_b128 a[196:199], v0 offset:20544\n"
      "v_fmac_f32 v47, s46, v35\n"
      "v_mfma_f32_16x16x32_bf16 v[80:83], v[100:103], a[204:207], v[80:83]\n"
      "s_nop 0\n"
      "v_fma_f32 v32, 0.5, v32, v68\n"
      "v_fma_f32 v33, 0.5, v33, v69\n"
      "v_mfma_f32_16x16x32_bf16 v[84:87], a[160:163], a[204:207], v[84:87]\n"
      "s_waitcnt lgkmcnt(4)\n"
      "v_fma_f32 v34, 0.5, v34, v70\n"
      "v_fma_f32 v35, 0.5, v35, v71\n"
      "v_mfma_f32_16x16x32_bf16 v[76:79], a[152:155], a[208:211], v[76:79]\n"
      "ds_read_b128 a[200:203], v0 offset:20608\n"
      "v_fmac_f32 v44, s48, v56\n"
      "v_mfma_f32_16x16x32_bf16 v[80:83], a[144:147], a[208:211], v[80:83]\n"
      "buffer_store_dwordx4 a[224:227], v16, s[8:11], s58 offen offset:128 sc1\n"
      "v_mfma_f32_16x16x32_bf16 v[84:87], a[136:139], a[208:211], v[84:87]\n"
      "v_fmac_f32 v45, s48, v57\n"
      "v_fmac_f32 v46, s48, v58\n"
      "v_fmac_f32 v47, s48, v59\n"
      "v_mfma_f32_16x16x32_bf16 v[76:79], a[128:131], a[212:215], v[76:79]\n"
      "ds_read_b128 a[204:207], v1 offset:20480\n"
      "v_fmac_f32 v32, s45, v44\n"
      "v_mfma_f32_16x16x32_bf16 v[80:83], a[120:123], a[212:215], v[80:83]\n"
      "v_fmac_f32 v33, s45, v45\n"
      "v_fmac_f32 v34, s45, v46\n"
      "v_mfma_f32_16x16x32_bf16 v[84:87], a[112:115], a[212:215], v[84:87]\n"
      "s_waitcnt lgkmcnt(1)\n"
      "ds_read2st64_b32 a[228:229], v12 offset0:181 offset1:183\n"
      "ds_read2st64_b32 a[230:231], v15 offset0:181 offset1:183\n"
      "v_mfma_f32_16x16x32_bf16 v[88:91], v[248:251], a[192:195], v[76:79]\n"
      "ds_read_b128 a[208:211], v1 offset:20544\n"
      "v_fmac_f32 v35, s45, v47\n"
      "v_mfma_f32_16x16x32_bf16 v[92:95], v[240:243], a[192:195], v[80:83]\n"
      "v_fma_f32 v44, s47, v44, -v56\n"
      "v_fma_f32 v45, s47, v45, -v57\n"
      "v_mfma_f32_16x16x32_bf16 v[96:99], v[232:235], a[192:195], v[84:87]\n"
      "v_fma_f32 v46, s47, v46, -v58\n"
      "v_fma_f32 v47, s47, v47, -v59\n"
      "v_mfma_f32_16x16x32_bf16 v[88:91], v[224:227], a[196:199], v[88:91]\n"
      "ds_read_b128 a[212:215], v1 offset:20608\n"
      "v_cvt_pk_bf16_f32 v32, v32, v33\n"
      "v_mfma_f32_16x16x32_bf16 v[92:95], v[216:219], a[196:199], v[92:95]\n"
      "ds_write_addtid_b32 v32 offset:40964\n"
      "v_cvt_pk_bf16_f32 v34, v34, v35\n"
      "v_cvt_pk_bf16_f32 v44, v44, v45\n"
      "v_mfma_f32_16x16x32_bf16 v[96:99], v[208:211], a[196:199], v[96:99]\n"
      "ds_write_addtid_b32 v34 offset:41476\n"
      "ds_write_addtid_b32 v44 offset:43012\n"
      "v_mfma_f32_16x16x32_bf16 v[88:91], v[200:203], a[200:203], v[88:91]\n"
      "ds_read_b128 a[192:195], v2 offset:20480\n"
      "v_cvt_pk_bf16_f32 v46, v46, v47\n"
      "v_mfma_f32_16x16x32_bf16 v[92:95], v[192:195], a[200:203], v[92:95]\n"
      "ds_write_addtid_b32 v46 offset:43524\n"
      "v_fmac_f32 v48, s46, v36\n"
      "v_mfma_f32_16x16x32_bf16 v[96:99], v[184:187], a[200:203], v[96:99]\n"
      "s_waitcnt lgkmcnt(5)\n"
      "v_fmac_f32 v49, s46, v37\n"
      "v_fmac_f32 v50, s46, v38\n"
      "v_mfma_f32_16x16x32_bf16 v[88:91], v[176:179], a[204:207], v[88:91]\n"
      "ds_read_b128 a[196:199], v2 offset:20544\n"
      "v_fmac_f32 v51, s46, v39\n"
      "v_mfma_f32_16x16x32_bf16 v[92:95], v[168:171], a[204:207], v[92:95]\n"
      "v_fma_f32 v36, 0.5, v36, v72\n"
      "v_fma_f32 v37, 0.5, v37, v73\n"
      "v_fma_f32 v38, 0.5, v38, v74\n"
      "v_mfma_f32_16x16x32_bf16 v[96:99], v[160:163], a[204:207], v[96:99]\n"
      "v_fma_f32 v39, 0.5, v39, v75\n"
      "v_fmac_f32 v48, s48, v60\n"
      "v_mfma_f32_16x16x32_bf16 v[88:91], v[152:155], a[208:211], v[88:91]\n"
      "ds_read_b128 a[200:203], v2 offset:20608\n"
      "v_fmac_f32 v49, s48, v61\n"
      "v_mfma_f32_16x16x32_bf16 v[92:95], v[144:147], a[208:211], v[92:95]\n"
      "buffer_store_dwordx4 a[228:231], v16, s[8:11], s58 offen offset:256 sc1\n"
      "v_mfma_f32_16x16x32_bf16 v[96:99], v[136:139], a[208:211], v[96:99]\n"
      "v_fmac_f32 v50, s48, v62\n"
      "v_fmac_f32 v51, s48, v63\n"
      "v_mfma_f32_16x16x32_bf16 v[88:91], v[128:131], a[212:215], v[88:91]\n"
      "ds_read_b128 a[204:207], v3 offset:50688\n"
      "v_fmac_f32 v36, s45, v48\n"
      "v_mfma_f32_16x16x32_bf16 v[92:95], v[120:123], a[212:215], v[92:95]\n"
      "v_fmac_f32 v37, s45, v49\n"
      "v_fmac_f32 v38, s45, v50\n"
      "v_fmac_f32 v39, s45, v51\n"
      "v_mfma_f32_16x16x32_bf16 v[96:99], v[112:115], a[212:215], v[96:99]\n"
      "s_waitcnt lgkmcnt(1)\n"
      "v_fma_f32 v48, s47, v48, -v60\n"
      "v_fma_f32 v49, s47, v49, -v61\n"
      "v_mfma_f32_16x16x32_bf16 v[88:91], v[104:107], a[192:195], v[88:91]\n"
      "ds_read_b128 a[208:211], v3 offset:50752\n"
      "v_fma_f32 v50, s47, v50, -v62\n"
      "v_mfma_f32_16x16x32_bf16 v[92:95], a[164:167], a[192:195], v[92:95]\n"
      "v_fma_f32 v51, s47, v51, -v63\n"
      "v_cvt_pk_bf16_f32 v36, v36, v37\n"
      "v_mfma_f32_16x16x32_bf16 v[96:99], a[156:159], a[192:195], v[96:99]\n"
      "ds_write_addtid_b32 v36 offset:36864\n"
      "v_cvt_pk_bf16_f32 v38, v38, v39\n"
      "v_mfma_f32_16x16x32_bf16 v[88:91], a[148:151], a[196:199], v[88:91]\n"
      "ds_read_b128 a[212:215], v3 offset:50816\n"
      "ds_write_addtid_b32 v38 offset:37376\n"
      "v_cvt_pk_bf16_f32 v48, v48, v49\n"
      "v_cvt_pk_bf16_f32 v50, v50, v51\n"
      "v_mfma_f32_16x16x32_bf16 v[92:95], a[140:143], a[196:199], v[92:95]\n"
      "ds_write_addtid_b32 v48 offset:38912\n"
      "ds_write_addtid_b32 v50 offset:39424\n"
      "v_mfma_f32_16x16x32_bf16 v[96:99], a[132:135], a[196:199], v[96:99]\n"
      "v_mfma_f32_16x16x32_bf16 v[88:91], a[124:127], a[200:203], v[88:91]\n"
      "ds_read_b128 a[192:195], v4 offset:50688\n"
      "v_mfma_f32_16x16x32_bf16 v[92:95], a[116:119], a[200:203], v[92:95]\n"
      "v_mfma_f32_16x16x32_bf16 v[96:99], a[108:111], a[200:203], v[96:99]\n"
      "s_waitcnt lgkmcnt(3)\n"
      "v_mfma_f32_16x16x32_bf16 v[64:67], a[0:3], a[204:207], 0\n"
      "ds_read_b128 a[196:199], v4 offset:50752\n"
      "v_mfma_f32_16x16x32_bf16 v[68:71], a[4:7], a[204:207], 0\n"
      "s_nop 0\n"
      "v_mfma_f32_16x16x32_bf16 v[72:75], a[8:11], a[204:207], 0\n"
      "ds_write_addtid_b32 v88 offset:51456\n"
      "ds_write_addtid_b32 v89 offset:51968\n"
      "v_mfma_f32_16x16x32_bf16 v[64:67], a[12:15], a[208:211], v[64:67]\n"
      "ds_read_b128 a[200:203], v4 offset:50816\n"
      "ds_write_addtid_b32 v90 offset:52480\n"
      "v_mfma_f32_16x16x32_bf16 v[68:71], a[16:19], a[208:211], v[68:71]\n"
      "ds_write_addtid_b32 v91 offset:52992\n"
      "ds_write_addtid_b32 v92 offset:53504\n"
      "v_mfma_f32_16x16x32_bf16 v[72:75], a[20:23], a[208:211], v[72:75]\n"
      "ds_write_addtid_b32 v93 offset:54016\n"
      "ds_write_addtid_b32 v94 offset:54528\n"
      "v_mfma_f32_16x16x32_bf16 v[64:67], a[24:27], a[212:215], v[64:67]\n"
      "ds_write_addtid_b32 v95 offset:55040\n"
      "ds_write_addtid_b32 v96 offset:55552\n"
      "v_mfma_f32_16x16x32_bf16 v[68:71], a[28:31], a[212:215], v[68:71]\n"
      "ds_write_addtid_b32 v97 offset:56064\n"
      "ds_write_addtid_b32 v98 offset:56576\n"
      "ds_write_addtid_b32 v99 offset:57088\n"
      "v_mfma_f32_16x16x32_bf16 v[72:75], a[32:35], a[212:215], v[72:75]\n"
      "s_waitcnt lgkmcnt(0)\n"
      "s_barrier\n"
      "v_mfma_f32_16x16x32_bf16 v[64:67], a[36:39], a[192:195], v[64:67]\n"
      "s_add_u32 s58, s58, 786432\n"
      "ds_read_b128 a[204:207], v5 offset:50688\n"
      "ds_read2st64_b32 v[28:29], v6 offset0:24 offset1:26\n"
      "v_mfma_f32_16x16x32_bf16 v[68:71], a[40:43], a[192:195], v[68:71]\n"
      "ds_read2st64_b32 v[30:31], v6 offset0:28 offset1:30\n"
      "ds_read2st64_b32 v[32:33], v6 offset0:32 offset1:34\n"
      "v_mfma_f32_16x16x32_bf16 v[72:75], a[44:47], a[192:195], v[72:75]\n"
      "ds_read2st64_b32 v[34:35], v6 offset0:36 offset1:38\n"
      "ds_read2st64_b32 v[36:37], v6 offset0:40 offset1:42\n"
      "v_mfma_f32_16x16x32_bf16 v[64:67], a[48:51], a[196:199], v[64:67]\n"
      "ds_read_b128 a[208:211], v5 offset:50752\n"
      "ds_read2st64_b32 v[38:39], v6 offset0:44 offset1:46\n"
      "v_mfma_f32_16x16x32_bf16 v[68:71], a[52:55], a[196:199], v[68:71]\n"
      "v_fma_f32 v40, 2.0, v76, -v88\n"
      "v_fma_f32 v41, 2.0, v77, -v89\n"
      "v_mfma_f32_16x16x32_bf16 v[72:75], a[56:59], a[196:199], v[72:75]\n"
      "ds_read2st64_b32 a[220:221], v10 offset0:177 offset1:179\n"
      "ds_read2st64_b32 a[222:223], v13 offset0:177 offset1:179\n"
      "v_mfma_f32_16x16x32_bf16 v[64:67], a[60:63], a[200:203], v[64:67]\n"
      "ds_read_b128 a[212:215], v5 offset:50816\n"
      "v_fma_f32 v42, 2.0, v78, -v90\n"
      "v_mfma_f32_16x16x32_bf16 v[68:71], a[64:67], a[200:203], v[68:71]\n"
      "s_nop 0\n"
      "ds_read2st64_b32 a[224:225], v11 offset0:177 offset1:179\n"
      "ds_read2st64_b32 a[226:227], v14 offset0:177 offset1:179\n"
      "v_mfma_f32_16x16x32_bf16 v[72:75], a[68:71], a[200:203], v[72:75]\n"
      "s_waitcnt lgkmcnt(9)\n"
      "v_fma_f32 v43, 2.0, v79, -v91\n"
      "v_fma_f32 v44, 2.0, v80, -v92\n"
      "v_mfma_f32_16x16x32_bf16 v[64:67], a[72:75], a[204:207], v[64:67]\n"
      "s_waitcnt lgkmcnt(3)\n"
      "buffer_store_dwordx4 a[220:223], v16, s[8:11], s58 offen offset:0 sc1\n"
      "v_mfma_f32_16x16x32_bf16 v[68:71], a[76:79], a[204:207], v[68:71]\n"
      "s_nop 0\n"
      "v_fma_f32 v45, 2.0, v81, -v93\n"
      "v_fma_f32 v46, 2.0, v82, -v94\n"
      "v_mfma_f32_16x16x32_bf16 v[72:75], a[80:83], a[204:207], v[72:75]\n"
      "ds_read2st64_b32 a[228:229], v12 offset0:177 offset1:179\n"
      "ds_read2st64_b32 a[230:231], v15 offset0:177 offset1:179\n"
      "v_mfma_f32_16x16x32_bf16 v[64:67], a[84:87], a[208:211], v[64:67]\n"
      "s_waitcnt lgkmcnt(2)\n"
      "buffer_store_dwordx4 a[224:227], v16, s[8:11], s58 offen offset:128 sc1\n"
      "v_mfma_f32_16x16x32_bf16 v[68:71], a[88:91], a[208:211], v[68:71]\n"
      "s_nop 0\n"
      "v_fma_f32 v47, 2.0, v83, -v95\n"
      "v_fma_f32 v48, 2.0, v84, -v96\n"
      "v_mfma_f32_16x16x32_bf16 v[72:75], a[92:95], a[208:211], v[72:75]\n"
      "v_fma_f32 v49, 2.0, v85, -v97\n"
      "v_fma_f32 v50, 2.0, v86, -v98\n"
      "v_mfma_f32_16x16x32_bf16 v[64:67], a[96:99], a[212:215], v[64:67]\n"
      "s_waitcnt lgkmcnt(0)\n"
      "buffer_store_dwordx4 a[228:231], v16, s[8:11], s58 offen offset:256 sc1\n"
      "v_mfma_f32_16x16x32_bf16 v[68:71], a[100:103], a[212:215], v[68:71]\n"
      "s_nop 0\n"
      "v_fma_f32 v51, 2.0, v87, -v99\n"
      "v_fmac_f32 v40, s46, v28\n"
      "v_mfma_f32_16x16x32_bf16 v[72:75], a[104:107], a[212:215], v[72:75]\n"
      "v_fmac_f32 v41, s46, v29\n"
      "v_fmac_f32 v42, s46, v30\n"
      "v_fmac_f32 v43, s46, v31\n"
      "v_fma_f32 v28, 0.5, v28, v52\n"
      "v_fma_f32 v29, 0.5, v29, v53\n"
      "v_fma_f32 v30, 0.5, v30, v54\n"
      "v_fma_f32 v31, 0.5, v31, v55\n"
      "v_fmac_f32 v44, s46, v32\n"
      "v_fmac_f32 v45, s46, v33\n"
      "v_pk_fma_f32 v[46:47], v[34:35], s[46:47], v[46:47] op_sel:[0,0,0] op_sel_hi:[1,0,1]\n"
      "v_pk_fma_f32 v[32:33], v[32:33], s[48:49], v[56:57] op_sel:[0,1,0] op_sel_hi:[1,1,1]\n"
      "v_pk_fma_f32 v[34:35], v[34:35], s[48:49], v[58:59] op_sel:[0,1,0] op_sel_hi:[1,1,1]\n"
      "v_pk_fma_f32 v[48:49], v[36:37], s[46:47], v[48:49] op_sel:[0,0,0] op_sel_hi:[1,0,1]\n"
      "v_pk_fma_f32 v[50:51], v[38:39], s[46:47], v[50:51] op_sel:[0,0,0] op_sel_hi:[1,0,1]\n"
      "v_pk_fma_f32 v[36:37], v[36:37], s[48:49], v[60:61] op_sel:[0,1,0] op_sel_hi:[1,1,1]\n"
      "v_pk_fma_f32 v[38:39], v[38:39], s[48:49], v[62:63] op_sel:[0,1,0] op_sel_hi:[1,1,1]\n"
      "v_pk_fma_f32 v[40:41], v[64:65], s[48:49], v[40:41] op_sel:[0,0,0] op_sel_hi:[1,0,1]\n"
      "v_pk_fma_f32 v[42:43], v[66:67], s[48:49], v[42:43] op_sel:[0,0,0] op_sel_hi:[1,0,1]\n"
      "v_pk_fma_f32 v[28:29], v[40:41], s[44:45], v[28:29] op_sel:[0,1,0] op_sel_hi:[1,1,1]\n"
      "v_pk_fma_f32 v[30:31], v[42:43], s[44:45], v[30:31] op_sel:[0,1,0] op_sel_hi:[1,1,1]\n"
      "v_pk_fma_f32 v[40:41], v[40:41], s[46:47], v[64:65] op_sel:[0,1,0] op_sel_hi:[1,1,1] neg_lo:[0,0,1] neg_hi:[0,0,1]\n"
      "v_pk_fma_f32 v[42:43], v[42:43], s[46:47], v[66:67] op_sel:[0,1,0] op_sel_hi:[1,1,1] neg_lo:[0,0,1] neg_hi:[0,0,1]\n"
      "v_cvt_pk_bf16_f32 v28, v28, v29\n"
      "v_cvt_pk_bf16_f32 v30, v30, v31\n"
      "v_cvt_pk_bf16_f32 v40, v40, v41\n"
      "v_cvt_pk_bf16_f32 v42, v42, v43\n"
      "v_pk_fma_f32 v[44:45], v[68:69], s[48:49], v[44:45] op_sel:[0,0,0] op_sel_hi:[1,0,1]\n"
      "v_pk_fma_f32 v[46:47], v[70:71], s[48:49], v[46:47] op_sel:[0,0,0] op_sel_hi:[1,0,1]\n"
      "v_pk_fma_f32 v[32:33], v[44:45], s[44:45], v[32:33] op_sel:[0,1,0] op_sel_hi:[1,1,1]\n"
      "v_pk_fma_f32 v[34:35], v[46:47], s[44:45], v[34:35] op_sel:[0,1,0] op_sel_hi:[1,1,1]\n"
      "v_pk_fma_f32 v[44:45], v[44:45], s[46:47], v[68:69] op_sel:[0,1,0] op_sel_hi:[1,1,1] neg_lo:[0,0,1] neg_hi:[0,0,1]\n"
      "v_pk_fma_f32 v[46:47], v[46:47], s[46:47], v[70:71] op_sel:[0,1,0] op_sel_hi:[1,1,1] neg_lo:[0,0,1] neg_hi:[0,0,1]\n"
      "v_cvt_pk_bf16_f32 v32, v32, v33\n"
      "v_cvt_pk_bf16_f32 v34, v34, v35\n"
      "ds_write_addtid_b32 v28 offset:33792\n"
      "ds_write_addtid_b32 v30 offset:34304\n"
      "ds_write_addtid_b32 v40 offset:35840\n"
      "ds_write_addtid_b32 v42 offset:36352\n"
      "ds_write_addtid_b32 v32 offset:41988\n"
      "ds_write_addtid_b32 v34 offset:42500\n"
      "v_cvt_pk_bf16_f32 v44, v44, v45\n"
      "v_cvt_pk_bf16_f32 v46, v46, v47\n"
      "v_pk_fma_f32 v[48:49], v[72:73], s[48:49], v[48:49] op_sel:[0,0,0] op_sel_hi:[1,0,1]\n"
      "v_pk_fma_f32 v[50:51], v[74:75], s[48:49], v[50:51] op_sel:[0,0,0] op_sel_hi:[1,0,1]\n"
      "v_pk_fma_f32 v[36:37], v[48:49], s[44:45], v[36:37] op_sel:[0,1,0] op_sel_hi:[1,1,1]\n"
      "v_pk_fma_f32 v[38:39], v[50:51], s[44:45], v[38:39] op_sel:[0,1,0] op_sel_hi:[1,1,1]\n"
      "v_pk_fma_f32 v[48:49], v[48:49], s[46:47], v[72:73] op_sel:[0,1,0] op_sel_hi:[1,1,1] neg_lo:[0,0,1] neg_hi:[0,0,1]\n"
      "v_pk_fma_f32 v[50:51], v[50:51], s[46:47], v[74:75] op_sel:[0,1,0] op_sel_hi:[1,1,1] neg_lo:[0,0,1] neg_hi:[0,0,1]\n"
      "v_cvt_pk_bf16_f32 v36, v36, v37\n"
      "v_cvt_pk_bf16_f32 v38, v38, v39\n"
      "v_cvt_pk_bf16_f32 v48, v48, v49\n"
      "v_cvt_pk_bf16_f32 v50, v50, v51\n"
      "ds_write_addtid_b32 v44 offset:44036\n"
      "ds_write_addtid_b32 v46 offset:44548\n"
      "ds_write_addtid_b32 v36 offset:37888\n"
      "ds_write_addtid_b32 v38 offset:38400\n"
      "ds_write_addtid_b32 v48 offset:39936\n"
      "ds_write_addtid_b32 v50 offset:40448\n"
      "s_waitcnt lgkmcnt(0)\n"
      "s_barrier\n"
      "ds_read2st64_b32 a[220:221], v10 offset0:181 offset1:183\n"
      "ds_read2st64_b32 a[222:223], v13 offset0:181 offset1:183\n"
      "ds_read2st64_b32 a[224:225], v11 offset0:181 offset1:183\n"
      "ds_read2st64_b32 a[226:227], v14 offset0:181 offset1:183\n"
      "ds_read2st64_b32 a[228:229], v12 offset0:181 offset1:183\n"
      "ds_read2st64_b32 a[230:231], v15 offset0:181 offset1:183\n"
      "s_add_u32 s58, s58, 786432\n"
      "s_waitcnt lgkmcnt(4)\n"
      "buffer_store_dwordx4 a[220:223], v16, s[8:11], s58 offen offset:0 sc1\n"
      "s_waitcnt lgkmcnt(2)\n"
      "buffer_store_dwordx4 a[224:227], v16, s[8:11], s58 offen offset:128 sc1\n"
      "s_waitcnt lgkmcnt(0)\n"
      "buffer_store_dwordx4 a[228:231], v16, s[8:11], s58 offen offset:256 sc1\n"
      "LDend_%=:\n"
      "s_branch Lfin_%=\n"
      ".p2align 6\n"
      "LS_%=:\n"
      "v_lshlrev_b32 v43, 4, v37\n"
      "v_lshlrev_b32 v44, 2, v37\n"
      "v_and_b32 v43, 16, v43\n"
      "v_and_or_b32 v43, v44, 8, v43\n"
      "v_mul_u32_u24 v44, 192, v38\n"
      "v_mul_u32_u24 v45, 1536, v39\n"
      "s_lshl_b32 s27, s18, 5\n"
      "v_lshlrev_b32 v46, 1, v36\n"
      "v_add3_u32 v43, v43, v44, v45\n"
      "v_bitop3_b32 v46, v46, s27, 32 bitop3:0x6c\n"
      "v_add_u32 v60, v43, v46\n"
      "v_add_u32 v61, 57344, v60\n"
      "v_add_u32 v62, 114688, v60\n"
      "s_lshl_b32 s26, s18, 3\n"
      "v_and_b32 v42, 15, v36\n"
      "v_xor_b32 v42, s26, v42\n"
      "s_lshl_b32 s26, s17, 13\n"
      "s_add_u32 s26, s26, 24832\n"
      "s_mul_i32 s27, s17, 12672\n"
      "v_mul_u32_u24 v44, 192, v42\n"
      "v_lshlrev_b32 v43, 3, v42\n"
      "v_and_b32 v45, 48, v36\n"
      "s_add_u32 s27, s27, 82432\n"
      "v_bitop3_b32 v46, v43, v45, 32 bitop3:0x6c\n"
      "v_add3_u32 v0, v46, v44, s26\n"
      "v_add3_u32 v3, v46, v44, s27\n"
      "v_add_u32 v43, 16, v43\n"
      "v_add_u32 v44, 384, v44\n"
      "v_bitop3_b32 v46, v43, v45, 32 bitop3:0x6c\n"
      "v_add3_u32 v1, v46, v44, s26\n"
      "v_add3_u32 v4, v46, v44, s27\n"
      "v_add_u32 v43, 16, v43\n"
      "v_add_u32 v44, 384, v44\n"
      "v_bitop3_b32 v46, v43, v45, 32 bitop3:0x6c\n"
      "v_add3_u32 v2, v46, v44, s26\n"
      "v_add3_u32 v5, v46, v44, s27\n"
      "v_lshlrev_b32 v43, 31, v36\n"
      "v_and_b32 v44, 2, v36\n"
      "v_or_b32 v43, 0x3f803f80, v43\n"
      "v_mul_u32_u24 v45, 0xc00080, v44\n"
      "v_lshl_add_u32 v44, v44, 22, v43\n"
      "v_add_u32 v45, v45, v43\n"
      "v_accvgpr_write_b32 a216, v44\n"
      "v_accvgpr_write_b32 a217, v45\n"
      "v_lshlrev_b32 v43, 3, v38\n"
      "s_mov_b32 s26, 806420736\n"
      "v_bfe_u32 v43, s26, v43, 8\n"
      "v_lshlrev_b32 v44, 12, v38\n"
      "s_lshl_b32 s28, s18, 8\n"
      "s_add_u32 s28, s28, 82432\n"
      "v_lshl_add_u32 v43, v43, 3, v44\n"
      "v_lshl_add_u32 v44, v39, 2, v37\n"
      "v_lshl_add_u32 v43, v44, 4, v43\n"
      "v_add_u32 v7, s28, v43\n"
      "s_mul_i32 s26, s17, 70144\n"
      "s_lshl_b32 s27, s18, 8\n"
      "s_sub_u32 s26, 70144, s26\n"
      "s_add_u32 s26, s26, s27\n"
      "v_lshl_add_u32 v6, v36, 2, s26\n"
      "v_lshrrev_b32 v43, 3, v36\n"
      "v_and_b32 v44, 7, v36\n"
      "v_mul_u32_u24 v45, 384, v43\n"
      "v_lshl_add_u32 v11, v44, 4, v45\n"
      "v_and_b32 v44, 2, v36\n"
      "v_lshlrev_b32 v43, 3, v43\n"
      "v_lshlrev_b32 v45, 4, v44\n"
      "v_xor_b32 v43, v43, v45\n"
      "v_lshl_add_u32 v43, v44, 7, v43\n"
      "v_and_b32 v44, 1, v36\n"
      "s_lshl_b32 s26, s18, 11\n"
      "v_lshl_add_u32 v43, v44, 6, v43\n"
      "s_add_u32 s26, s26, 12288\n"
      "v_bfe_u32 v44, v36, 2, 1\n"
      "v_add_u32 v43, s26, v43\n"
      "v_mul_i32_i24 v45, -4100, v44\n"
      "s_mov_b32 s27, 8200\n"
      "v_add3_u32 v8, v43, v45, s27\n"
      "v_add_u32 v9, 128, v8\n"
      "s_mul_i32 s62, s18, 393216\n"
      "s_add_u32 s62, s62, s19\n"
      "s_add_u32 s62, s62, s21\n"
      "s_mov_b32 s40, 0x80000000\n"
      "s_and_b32 s9, s9, 0xffff\n"
      "s_mov_b32 s45, 0xbeaaaaab\n"
      "s_mov_b32 s46, 0xbe800000\n"
      "s_mov_b32 s47, 0x3eaaaaab\n"
      "s_add_u32 m0, s63, 6144\n"
      "s_add_u32 s52, s63, 61440\n"
      "buffer_load_dwordx4 v17, s[12:15], s52 offen offset:0 lds\n"
      "buffer_load_dwordx4 v18, s[12:15], s52 offen offset:1024 lds\n"
      "buffer_load_dwordx4 v19, s[12:15], s52 offen offset:2048 lds\n"
      "s_add_u32 m0, s63, 12288\n"
      "s_add_u32 s52, s63, 116736\n"
      "buffer_load_dwordx4 v17, s[12:15], s52 offen offset:0 lds\n"
      "buffer_load_dwordx4 v18, s[12:15], s52 offen offset:1024 lds\n"
      "buffer_load_dwordx4 v19, s[12:15], s52 offen offset:2048 lds\n"
      "LSpro_%=:\n"
      "s_waitcnt vmcnt(6)\n"
      "s_barrier\n"
      "ds_read_b64_tr_b16 a[224:225], v60 offset:24832\n"
      "ds_read_b64_tr_b16 a[226:227], v60 offset:25600\n"
      "ds_read_b64_tr_b16 a[0:1], v62 offset:26112\n"
      "ds_read_b64_tr_b16 a[2:3], v62 offset:26880\n"
      "ds_read_b64_tr_b16 a[220:221], v60 offset:30976\n"
      "ds_read_b64_tr_b16 a[222:223], v60 offset:31744\n"
      "ds_read_b64_tr_b16 a[232:233], v60 offset:24896\n"
      "ds_read_b64_tr_b16 a[234:235], v60 offset:25664\n"
      "ds_read_b64_tr_b16 a[4:5], v62 offset:26176\n"
      "ds_read_b64_tr_b16 a[6:7], v62 offset:26944\n"
      "ds_read_b64_tr_b16 a[228:229], v60 offset:31040\n"
      "ds_read_b64_tr_b16 a[230:231], v60 offset:31808\n"
      "s_add_u32 m0, s63, 18432\n"
      "s_add_u32 s52, s63, 12288\n"
      "s_nop 0\n"
      "s_waitcnt lgkmcnt(10)\n"
      "v_mfma_f32_4x4x4_16b_bf16 v[28:31], a[244:245], a[224:225], 0\n"
      "v_mfma_f32_4x4x4_16b_bf16 v[32:35], a[244:245], a[226:227], 0\n"
      "buffer_load_dwordx4 v17, s[12:15], s52 offen offset:0 lds\n"
      "s_waitcnt lgkmcnt(8)\n"
      "v_mfma_f32_4x4x4_16b_bf16 v[28:31], a[248:249], a[0:1], v[28:31]\n"
      "ds_read_b64_tr_b16 a[240:241], v60 offset:24960\n"
      "v_mfma_f32_4x4x4_16b_bf16 v[32:35], a[248:249], a[2:3], v[32:35]\n"
      "buffer_load_dwordx4 v18, s[12:15], s52 offen offset:1024 lds\n"
      "s_waitcnt lgkmcnt(7)\n"
      "v_mfma_f32_4x4x4_16b_bf16 v[36:39], a[246:247], a[220:221], v[28:31]\n"
      "ds_read_b64_tr_b16 a[242:243], v60 offset:25728\n"
      "v_mfma_f32_4x4x4_16b_bf16 v[40:43], a[246:247], a[222:223], v[32:35]\n"
      "buffer_load_dwordx4 v19, s[12:15], s52 offen offset:2048 lds\n"
      "v_mfma_f32_4x4x4_16b_bf16 v[28:31], a[250:251], a[220:221], v[28:31]\n"
      "ds_read_b64_tr_b16 a[8:9], v62 offset:26240\n"
      "v_mfma_f32_4x4x4_16b_bf16 v[32:35], a[250:251], a[222:223], v[32:35]\n"
      "ds_read_b64_tr_b16 a[10:11], v62 offset:27008\n"
      "ds_read_b64_tr_b16 a[236:237], v60 offset:31104\n"
      "ds_read_b64_tr_b16 a[238:239], v60 offset:31872\n"
      "s_add_u32 m0, s63, 57600\n"
      "s_add_u32 s52, s63, 67584\n"
      "s_nop 0\n"
      "s_waitcnt lgkmcnt(5)\n"
      "v_mfma_f32_4x4x4_16b_bf16 v[44:47], a[244:245], a[232:233], 0\n"
      "v_mfma_f32_4x4x4_16b_bf16 v[48:51], a[244:245], a[234:235], 0\n"
      "buffer_load_dwordx4 v17, s[12:15], s52 offen offset:0 lds\n"
      "v_mfma_f32_4x4x4_16b_bf16 v[44:47], a[248:249], a[4:5], v[44:47]\n"
      "buffer_load_dwordx4 v18, s[12:15], s52 offen offset:1024 lds\n"
      "v_mfma_f32_4x4x4_16b_bf16 v[48:51], a[248:249], a[6:7], v[48:51]\n"
      "buffer_load_dwordx4 v19, s[12:15], s52 offen offset:2048 lds\n"
      "v_mfma_f32_4x4x4_16b_bf16 v[52:55], a[246:247], a[228:229], v[44:47]\n"
      "v_mfma_f32_4x4x4_16b_bf16 v[56:59], a[246:247], a[230:231], v[48:51]\n"
      "v_mfma_f32_4x4x4_16b_bf16 v[44:47], a[250:251], a[228:229], v[44:47]\n"
      "v_mfma_f32_4x4x4_16b_bf16 v[48:51], a[250:251], a[230:231], v[48:51]\n"
      "v_cvt_pk_bf16_f32 v252, v36, v40\n"
      "v_cvt_pk_bf16_f32 v253, v37, v41\n"
      "v_cvt_pk_bf16_f32 v254, v38, v42\n"
      "v_cvt_pk_bf16_f32 v255, v39, v43\n"
      "v_cvt_pk_bf16_f32 v248, v28, v32\n"
      "v_cvt_pk_bf16_f32 v249, v29, v33\n"
      "v_cvt_pk_bf16_f32 v250, v30, v34\n"
      "v_cvt_pk_bf16_f32 v251, v31, v35\n"
      "s_add_u32 m0, s63, 69888\n"
      "s_add_u32 s52, s63, 18432\n"
      "s_nop 0\n"
      "s_waitcnt lgkmcnt(0)\n"
      "v_mfma_f32_4x4x4_16b_bf16 v[28:31], a[244:245], a[240:241], 0\n"
      "v_mfma_f32_4x4x4_16b_bf16 v[32:35], a[244:245], a[242:243], 0\n"
      "buffer_load_dwordx4 v17, s[12:15], s52 offen offset:0 lds\n"
      "v_mfma_f32_4x4x4_16b_bf16 v[28:31], a[248:249], a[8:9], v[28:31]\n"
      "buffer_load_dwordx4 v18, s[12:15], s52 offen offset:1024 lds\n"
      "v_mfma_f32_4x4x4_16b_bf16 v[32:35], a[248:249], a[10:11], v[32:35]\n"
      "buffer_load_dwordx4 v19, s[12:15], s52 offen offset:2048 lds\n"
      "v_mfma_f32_4x4x4_16b_bf16 v[36:39], a[246:247], a[236:237], v[28:31]\n"
      "v_mfma_f32_4x4x4_16b_bf16 v[40:43], a[246:247], a[238:239], v[32:35]\n"
      "v_mfma_f32_4x4x4_16b_bf16 v[28:31], a[250:251], a[236:237], v[28:31]\n"
      "v_mfma_f32_4x4x4_16b_bf16 v[32:35], a[250:251], a[238:239], v[32:35]\n"
      "v_cvt_pk_bf16_f32 v244, v52, v56\n"
      "v_cvt_pk_bf16_f32 v245, v53, v57\n"
      "v_cvt_pk_bf16_f32 v246, v54, v58\n"
      "v_cvt_pk_bf16_f32 v247, v55, v59\n"
      "v_cvt_pk_bf16_f32 v240, v44, v48\n"
      "v_cvt_pk_bf16_f32 v241, v45, v49\n"
      "v_cvt_pk_bf16_f32 v242, v46, v50\n"
      "v_cvt_pk_bf16_f32 v243, v47, v51\n"
      "s_waitcnt vmcnt(9)\n"
      "s_barrier\n"
      "ds_read_b64_tr_b16 a[224:225], v60 offset:0\n"
      "ds_read_b64_tr_b16 a[226:227], v60 offset:768\n"
      "ds_read_b64_tr_b16 a[12:13], v60 offset:12288\n"
      "ds_read_b64_tr_b16 a[14:15], v60 offset:13056\n"
      "ds_read_b64_tr_b16 a[220:221], v60 offset:6144\n"
      "ds_read_b64_tr_b16 a[222:223], v60 offset:6912\n"
      "s_add_u32 m0, s63, 76032\n"
      "s_add_u32 s52, s63, 73728\n"
      "s_nop 0\n"
      "s_waitcnt lgkmcnt(4)\n"
      "v_mfma_f32_4x4x4_16b_bf16 v[44:47], a[244:245], a[224:225], 0\n"
      "v_mfma_f32_4x4x4_16b_bf16 v[48:51], a[244:245], a[226:227], 0\n"
      "buffer_load_dwordx4 v17, s[12:15], s52 offen offset:0 lds\n"
      "s_waitcnt lgkmcnt(2)\n"
      "v_mfma_f32_4x4x4_16b_bf16 v[44:47], a[248:249], a[12:13], v[44:47]\n"
      "ds_read_b64_tr_b16 a[232:233], v60 offset:64\n"
      "v_mfma_f32_4x4x4_16b_bf16 v[48:51], a[248:249], a[14:15], v[48:51]\n"
      "buffer_load_dwordx4 v18, s[12:15], s52 offen offset:1024 lds\n"
      "s_waitcnt lgkmcnt(1)\n"
      "v_mfma_f32_4x4x4_16b_bf16 v[52:55], a[246:247], a[220:221], v[44:47]\n"
      "ds_read_b64_tr_b16 a[234:235], v60 offset:832\n"
      "v_mfma_f32_4x4x4_16b_bf16 v[56:59], a[246:247], a[222:223], v[48:51]\n"
      "buffer_load_dwordx4 v19, s[12:15], s52 offen offset:2048 lds\n"
      "v_mfma_f32_4x4x4_16b_bf16 v[44:47], a[250:251], a[220:221], v[44:47]\n"
      "ds_read_b64_tr_b16 a[16:17], v60 offset:12352\n"
      "v_mfma_f32_4x4x4_16b_bf16 v[48:51], a[250:251], a[222:223], v[48:51]\n"
      "ds_read_b64_tr_b16 a[18:19], v60 offset:13120\n"
      "ds_read_b64_tr_b16 a[228:229], v60 offset:6208\n"
      "ds_read_b64_tr_b16 a[230:231], v60 offset:6976\n"
      "ds_read_b64_tr_b16 a[240:241], v60 offset:128\n"
      "ds_read_b64_tr_b16 a[242:243], v60 offset:896\n"
      "ds_read_b64_tr_b16 a[20:21], v60 offset:12416\n"
      "ds_read_b64_tr_b16 a[22:23], v60 offset:13184\n"
      "ds_read_b64_tr_b16 a[236:237], v60 offset:6272\n"
      "ds_read_b64_tr_b16 a[238:239], v60 offset:7040\n"
      "ds_read_b64_tr_b16 a[168:169], v7 offset:0\n"
      "ds_read_b64_tr_b16 a[170:171], v7 offset:8\n"
      "ds_read_b128 a[204:207], v3 offset:0\n"
      "v_cvt_pk_bf16_f32 v236, v36, v40\n"
      "v_cvt_pk_bf16_f32 v237, v37, v41\n"
      "v_cvt_pk_bf16_f32 v238, v38, v42\n"
      "v_cvt_pk_bf16_f32 v239, v39, v43\n"
      "v_cvt_pk_bf16_f32 v232, v28, v32\n"
      "v_cvt_pk_bf16_f32 v233, v29, v33\n"
      "v_cvt_pk_bf16_f32 v234, v30, v34\n"
      "v_cvt_pk_bf16_f32 v235, v31, v35\n"
      "s_waitcnt vmcnt(6)\n"
      "s_barrier\n"
      "s_add_u32 m0, s63, 116224\n"
      "s_add_u32 s52, s63, 24576\n"
      "s_waitcnt lgkmcnt(7)\n"
      "v_mfma_f32_4x4x4_16b_bf16 v[28:31], a[244:245], a[232:233], 0\n"
      "v_mfma_f32_4x4x4_16b_bf16 v[32:35], a[244:245], a[234:235], 0\n"
      "buffer_load_dwordx4 v17, s[12:15], s52 offen offset:0 lds\n"
      "v_mfma_f32_4x4x4_16b_bf16 v[28:31], a[248:249], a[16:17], v[28:31]\n"
      "ds_read_b64_tr_b16 a[224:225], v60 offset:18432\n"
      "v_mfma_f32_4x4x4_16b_bf16 v[32:35], a[248:249], a[18:19], v[32:35]\n"
      "buffer_load_dwordx4 v18, s[12:15], s52 offen offset:1024 lds\n"
      "v_mfma_f32_4x4x4_16b_bf16 v[36:39], a[246:247], a[228:229], v[28:31]\n"
      "ds_read_b64_tr_b16 a[226:227], v60 offset:19200\n"
      "v_mfma_f32_4x4x4_16b_bf16 v[40:43], a[246:247], a[230:231], v[32:35]\n"
      "buffer_load_dwordx4 v19, s[12:15], s52 offen offset:2048 lds\n"
      "v_mfma_f32_4x4x4_16b_bf16 v[28:31], a[250:251], a[228:229], v[28:31]\n"
      "ds_read_b64_tr_b16 a[24:25], v61 offset:6400\n"
      "v_mfma_f32_4x4x4_16b_bf16 v[32:35], a[250:251], a[230:231], v[32:35]\n"
      "ds_read_b64_tr_b16 a[26:27], v61 offset:7168\n"
      "s_waitcnt lgkmcnt(4)\n"
      "v_mfma_f32_4x4x4_16b_bf16 v[20:23], a[216:217], a[168:169], 0\n"
      "ds_read_b64_tr_b16 a[220:221], v61 offset:256\n"
      "v_mfma_f32_4x4x4_16b_bf16 v[24:27], a[216:217], a[170:171], 0\n"
      "ds_read_b64_tr_b16 a[222:223], v61 offset:1024\n"
      "ds_read_b64_tr_b16 a[172:173], v7 offset:512\n"
      "ds_read_b64_tr_b16 a[174:175], v7 offset:520\n"
      "ds_read_b128 a[208:211], v3 offset:64\n"
      "v_cvt_pk_bf16_f32 v228, v52, v56\n"
      "v_cvt_pk_bf16_f32 v229, v53, v57\n"
      "v_cvt_pk_bf16_f32 v230, v54, v58\n"
      "v_cvt_pk_bf16_f32 v231, v55, v59\n"
      "v_cvt_pk_bf16_f32 v224, v44, v48\n"
      "v_cvt_pk_bf16_f32 v225, v45, v49\n"
      "v_cvt_pk_bf16_f32 v226, v46, v50\n"
      "v_cvt_pk_bf16_f32 v227, v47, v51\n"
      "s_add_u32 m0, s63, 122368\n"
      "s_add_u32 s52, s63, 79872\n"
      "v_mfma_f32_4x4x4_16b_bf16 v[44:47], a[244:245], a[240:241], 0\n"
      "v_mfma_f32_4x4x4_16b_bf16 v[48:51], a[244:245], a[242:243], 0\n"
      "buffer_load_dwordx4 v17, s[12:15], s52 offen offset:0 lds\n"
      "v_mfma_f32_4x4x4_16b_bf16 v[44:47], a[248:249], a[20:21], v[44:47]\n"
      "ds_read_b64_tr_b16 a[232:233], v60 offset:18496\n"
      "v_mfma_f32_4x4x4_16b_bf16 v[48:51], a[248:249], a[22:23], v[48:51]\n"
      "buffer_load_dwordx4 v18, s[12:15], s52 offen offset:1024 lds\n"
      "v_mfma_f32_4x4x4_16b_bf16 v[52:55], a[246:247], a[236:237], v[44:47]\n"
      "ds_read_b64_tr_b16 a[234:235], v60 offset:19264\n"
      "v_mfma_f32_4x4x4_16b_bf16 v[56:59], a[246:247], a[238:239], v[48:51]\n"
      "buffer_load_dwordx4 v19, s[12:15], s52 offen offset:2048 lds\n"
      "v_mfma_f32_4x4x4_16b_bf16 v[44:47], a[250:251], a[236:237], v[44:47]\n"
      "ds_read_b64_tr_b16 a[28:29], v61 offset:6464\n"
      "v_mfma_f32_4x4x4_16b_bf16 v[48:51], a[250:251], a[238:239], v[48:51]\n"
      "ds_read_b64_tr_b16 a[30:31], v61 offset:7232\n"
      "s_nop 0\n"
      "s_waitcnt lgkmcnt(4)\n"
      "v_mfma_f32_4x4x4_16b_bf16 v[100:103], a[216:217], a[172:173], 0\n"
      "ds_read_b64_tr_b16 a[228:229], v61 offset:320\n"
      "v_mfma_f32_4x4x4_16b_bf16 v[104:107], a[216:217], a[174:175], 0\n"
      "ds_read_b64_tr_b16 a[230:231], v61 offset:1088\n"
      "ds_read_b64_tr_b16 a[176:177], v7 offset:1024\n"
      "ds_read_b64_tr_b16 a[178:179], v7 offset:1032\n"
      "v_cvt_pk_bf16_f32 v220, v36, v40\n"
      "v_cvt_pk_bf16_f32 v221, v37, v41\n"
      "v_cvt_pk_bf16_f32 v222, v38, v42\n"
      "v_cvt_pk_bf16_f32 v223, v39, v43\n"
      "v_cvt_pk_bf16_f32 v216, v28, v32\n"
      "v_cvt_pk_bf16_f32 v217, v29, v33\n"
      "v_mfma_f32_16x16x32_bf16 v[64:67], a[0:3], a[204:207], 0\n"
      "v_cvt_pk_bf16_f32 v218, v30, v34\n"
      "v_cvt_pk_bf16_f32 v219, v31, v35\n"
      "v_mfma_f32_16x16x32_bf16 v[68:71], a[4:7], a[204:207], 0\n"
      "v_cvt_pk_bf16_f32 v20, v20, v24\n"
      "v_cvt_pk_bf16_f32 v21, v21, v25\n"
      "v_mfma_f32_16x16x32_bf16 v[72:75], a[8:11], a[204:207], 0\n"
      "s_add_u32 m0, s63, 41216\n"
      "v_cvt_pk_bf16_f32 v22, v22, v26\n"
      "v_cvt_pk_bf16_f32 v23, v23, v27\n"
      "s_add_u32 s52, s63, 30720\n"
      "v_mfma_f32_4x4x4_16b_bf16 v[28:31], a[244:245], a[224:225], 0\n"
      "v_mfma_f32_4x4x4_16b_bf16 v[32:35], a[244:245], a[226:227], 0\n"
      "buffer_load_dwordx4 v17, s[12:15], s52 offen offset:0 lds\n"
      "v_mfma_f32_4x4x4_16b_bf16 v[28:31], a[248:249], a[24:25], v[28:31]\n"
      "ds_read_b64_tr_b16 a[240:241], v60 offset:18560\n"
      "v_mfma_f32_4x4x4_16b_bf16 v[32:35], a[248:249], a[26:27], v[32:35]\n"
      "buffer_load_dwordx4 v18, s[12:15], s52 offen offset:1024 lds\n"
      "v_mfma_f32_4x4x4_16b_bf16 v[36:39], a[246:247], a[220:221], v[28:31]\n"
      "ds_read_b64_tr_b16 a[242:243], v60 offset:19328\n"
      "v_mfma_f32_4x4x4_16b_bf16 v[40:43], a[246:247], a[222:223], v[32:35]\n"
      "buffer_load_dwordx4 v19, s[12:15], s52 offen offset:2048 lds\n"
      "v_mfma_f32_4x4x4_16b_bf16 v[28:31], a[250:251], a[220:221], v[28:31]\n"
      "ds_read_b64_tr_b16 a[32:33], v61 offset:6528\n"
      "v_mfma_f32_4x4x4_16b_bf16 v[32:35], a[250:251], a[222:223], v[32:35]\n"
      "ds_read_b64_tr_b16 a[34:35], v61 offset:7296\n"
      "s_nop 0\n"
      "s_waitcnt lgkmcnt(4)\n"
      "v_mfma_f32_4x4x4_16b_bf16 v[108:111], a[216:217], a[176:177], 0\n"
      "ds_read_b64_tr_b16 a[236:237], v61 offset:384\n"
      "v_mfma_f32_4x4x4_16b_bf16 v[112:115], a[216:217], a[178:179], 0\n"
      "ds_read_b64_tr_b16 a[238:239], v61 offset:1152\n"
      "ds_read_b64_tr_b16 a[180:181], v7 offset:1536\n"
      "ds_read_b64_tr_b16 a[182:183], v7 offset:1544\n"
      "s_mov_b32 m0, s64\n"
      "s_nop 0\n"
      "ds_write_addtid_b32 v20 offset:24832\n"
      "ds_write_addtid_b32 v21 offset:28928\n"
      "ds_write_addtid_b32 v22 offset:33024\n"
      "ds_write_addtid_b32 v23 offset:37120\n"
      "v_cvt_pk_bf16_f32 v212, v52, v56\n"
      "v_cvt_pk_bf16_f32 v213, v53, v57\n"
      "v_cvt_pk_bf16_f32 v214, v54, v58\n"
      "v_cvt_pk_bf16_f32 v215, v55, v59\n"
      "v_cvt_pk_bf16_f32 v208, v44, v48\n"
      "v_cvt_pk_bf16_f32 v209, v45, v49\n"
      "v_mfma_f32_16x16x32_bf16 v[64:67], a[12:15], a[208:211], v[64:67]\n"
      "v_cvt_pk_bf16_f32 v210, v46, v50\n"
      "v_cvt_pk_bf16_f32 v211, v47, v51\n"
      "v_mfma_f32_16x16x32_bf16 v[68:71], a[16:19], a[208:211], v[68:71]\n"
      "v_cvt_pk_bf16_f32 v100, v100, v104\n"
      "v_cvt_pk_bf16_f32 v101, v101, v105\n"
      "v_mfma_f32_16x16x32_bf16 v[72:75], a[20:23], a[208:211], v[72:75]\n"
      "s_waitcnt vmcnt(9)\n"
      "v_cvt_pk_bf16_f32 v102, v102, v106\n"
      "v_cvt_pk_bf16_f32 v103, v103, v107\n"
      "s_barrier\n"
      "s_add_u32 m0, s63, 47360\n"
      "s_add_u32 s52, s63, 86016\n"
      "v_mfma_f32_4x4x4_16b_bf16 v[44:47], a[244:245], a[232:233], 0\n"
      "v_mfma_f32_4x4x4_16b_bf16 v[48:51], a[244:245], a[234:235], 0\n"
      "buffer_load_dwordx4 v17, s[12:15], s52 offen offset:0 lds\n"
      "v_mfma_f32_4x4x4_16b_bf16 v[44:47], a[248:249], a[28:29], v[44:47]\n"
      "ds_read_b64_tr_b16 a[224:225], v61 offset:12544\n"
      "v_mfma_f32_4x4x4_16b_bf16 v[48:51], a[248:249], a[30:31], v[48:51]\n"
      "buffer_load_dwordx4 v18, s[12:15], s52 offen offset:1024 lds\n"
      "v_mfma_f32_4x4x4_16b_bf16 v[52:55], a[246:247], a[228:229], v[44:47]\n"
      "ds_read_b64_tr_b16 a[226:227], v61 offset:13312\n"
      "v_mfma_f32_4x4x4_16b_bf16 v[56:59], a[246:247], a[230:231], v[48:51]\n"
      "buffer_load_dwordx4 v19, s[12:15], s52 offen offset:2048 lds\n"
      "v_mfma_f32_4x4x4_16b_bf16 v[44:47], a[250:251], a[228:229], v[44:47]\n"
      "ds_read_b64_tr_b16 a[36:37], v62 offset:39680\n"
      "v_mfma_f32_4x4x4_16b_bf16 v[48:51], a[250:251], a[230:231], v[48:51]\n"
      "ds_read_b64_tr_b16 a[38:39], v62 offset:40448\n"
      "s_waitcnt lgkmcnt(4)\n"
      "v_mfma_f32_4x4x4_16b_bf16 v[116:119], a[216:217], a[180:181], 0\n"
      "ds_read_b64_tr_b16 a[220:221], v61 offset:18688\n"
      "v_mfma_f32_4x4x4_16b_bf16 v[120:123], a[216:217], a[182:183], 0\n"
      "ds_read_b64_tr_b16 a[222:223], v61 offset:19456\n"
      "s_mov_b32 m0, s64\n"
      "s_nop 0\n"
      "ds_write_addtid_b32 v100 offset:25344\n"
      "ds_write_addtid_b32 v101 offset:29440\n"
      "ds_write_addtid_b32 v102 offset:33536\n"
      "ds_write_addtid_b32 v103 offset:37632\n"
      "ds_read_b128 a[212:215], v3 offset:128\n"
      "v_cvt_pk_bf16_f32 v204, v36, v40\n"
      "v_cvt_pk_bf16_f32 v205, v37, v41\n"
      "v_cvt_pk_bf16_f32 v206, v38, v42\n"
      "v_cvt_pk_bf16_f32 v207, v39, v43\n"
      "v_cvt_pk_bf16_f32 v200, v28, v32\n"
      "v_cvt_pk_bf16_f32 v201, v29, v33\n"
      "v_cvt_pk_bf16_f32 v202, v30, v34\n"
      "v_cvt_pk_bf16_f32 v203, v31, v35\n"
      "v_cvt_pk_bf16_f32 v108, v108, v112\n"
      "v_cvt_pk_bf16_f32 v109, v109, v113\n"
      "v_cvt_pk_bf16_f32 v110, v110, v114\n"
      "v_cvt_pk_bf16_f32 v111, v111, v115\n"
      "s_add_u32 m0, s63, 0\n"
      "s_add_u32 s52, s63, 36864\n"
      "v_mfma_f32_4x4x4_16b_bf16 v[28:31], a[244:245], a[240:241], 0\n"
      "v_mfma_f32_4x4x4_16b_bf16 v[32:35], a[244:245], a[242:243], 0\n"
      "buffer_load_dwordx4 v17, s[12:15], s52 offen offset:0 lds\n"
      "v_mfma_f32_4x4x4_16b_bf16 v[28:31], a[248:249], a[32:33], v[28:31]\n"
      "ds_read_b64_tr_b16 a[232:233], v61 offset:12608\n"
      "v_mfma_f32_4x4x4_16b_bf16 v[32:35], a[248:249], a[34:35], v[32:35]\n"
      "buffer_load_dwordx4 v18, s[12:15], s52 offen offset:1024 lds\n"
      "v_mfma_f32_4x4x4_16b_bf16 v[36:39], a[246:247], a[236:237], v[28:31]\n"
      "ds_read_b64_tr_b16 a[234:235], v61 offset:13376\n"
      "v_mfma_f32_4x4x4_16b_bf16 v[40:43], a[246:247], a[238:239], v[32:35]\n"
      "ds_read_b64_tr_b16 a[40:41], v62 offset:39744\n"
      "v_mfma_f32_4x4x4_16b_bf16 v[28:31], a[250:251], a[236:237], v[28:31]\n"
      "ds_read_b64_tr_b16 a[42:43], v62 offset:40512\n"
      "v_mfma_f32_4x4x4_16b_bf16 v[32:35], a[250:251], a[238:239], v[32:35]\n"
      "ds_read_b64_tr_b16 a[228:229], v61 offset:18752\n"
      "ds_read_b64_tr_b16 a[230:231], v61 offset:19520\n"
      "s_mov_b32 m0, s64\n"
      "s_nop 0\n"
      "ds_write_addtid_b32 v108 offset:25856\n"
      "ds_write_addtid_b32 v109 offset:29952\n"
      "ds_write_addtid_b32 v110 offset:34048\n"
      "ds_write_addtid_b32 v111 offset:38144\n"
      "v_cvt_pk_bf16_f32 v196, v52, v56\n"
      "v_cvt_pk_bf16_f32 v197, v53, v57\n"
      "v_cvt_pk_bf16_f32 v198, v54, v58\n"
      "v_cvt_pk_bf16_f32 v199, v55, v59\n"
      "v_cvt_pk_bf16_f32 v192, v44, v48\n"
      "v_cvt_pk_bf16_f32 v193, v45, v49\n"
      "v_cvt_pk_bf16_f32 v194, v46, v50\n"
      "v_cvt_pk_bf16_f32 v195, v47, v51\n"
      "v_cvt_pk_bf16_f32 v116, v116, v120\n"
      "v_cvt_pk_bf16_f32 v117, v117, v121\n"
      "v_cvt_pk_bf16_f32 v118, v118, v122\n"
      "v_cvt_pk_bf16_f32 v119, v119, v123\n"
      "s_waitcnt lgkmcnt(4)\n"
      "v_mfma_f32_4x4x4_16b_bf16 v[44:47], a[244:245], a[224:225], 0\n"
      "v_mfma_f32_4x4x4_16b_bf16 v[48:51], a[244:245], a[226:227], 0\n"
      "s_add_u32 m0, s63, 0\n"
      "s_nop 0\n"
      "s_nop 0\n"
      "buffer_load_dwordx4 v19, s[12:15], s52 offen offset:2048 lds\n"
      "v_mfma_f32_4x4x4_16b_bf16 v[44:47], a[248:249], a[36:37], v[44:47]\n"
      "ds_read_b64_tr_b16 a[240:241], v61 offset:12672\n"
      "v_mfma_f32_4x4x4_16b_bf16 v[48:51], a[248:249], a[38:39], v[48:51]\n"
      "s_add_u32 m0, s63, 6144\n"
      "v_mfma_f32_4x4x4_16b_bf16 v[52:55], a[246:247], a[220:221], v[44:47]\n"
      "ds_read_b64_tr_b16 a[242:243], v61 offset:13440\n"
      "v_mfma_f32_4x4x4_16b_bf16 v[56:59], a[246:247], a[222:223], v[48:51]\n"
      "s_add_u32 s52, s63, 92160\n"
      "v_mfma_f32_4x4x4_16b_bf16 v[44:47], a[250:251], a[220:221], v[44:47]\n"
      "ds_read_b64_tr_b16 a[44:45], v62 offset:39808\n"
      "v_mfma_f32_4x4x4_16b_bf16 v[48:51], a[250:251], a[222:223], v[48:51]\n"
      "buffer_load_dwordx4 v17, s[12:15], s52 offen offset:0 lds\n"
      "ds_read_b64_tr_b16 a[46:47], v62 offset:40576\n"
      "ds_read_b64_tr_b16 a[236:237], v61 offset:18816\n"
      "ds_read_b64_tr_b16 a[238:239], v61 offset:19584\n"
      "s_mov_b32 m0, s64\n"
      "s_nop 0\n"
      "ds_write_addtid_b32 v116 offset:26368\n"
      "ds_write_addtid_b32 v117 offset:30464\n"
      "ds_write_addtid_b32 v118 offset:34560\n"
      "ds_write_addtid_b32 v119 offset:38656\n"
      "v_cvt_pk_bf16_f32 v188, v36, v40\n"
      "v_cvt_pk_bf16_f32 v189, v37, v41\n"
      "v_mfma_f32_16x16x32_bf16 v[64:67], a[24:27], a[212:215], v[64:67]\n"
      "v_cvt_pk_bf16_f32 v190, v38, v42\n"
      "v_cvt_pk_bf16_f32 v191, v39, v43\n"
      "v_mfma_f32_16x16x32_bf16 v[68:71], a[28:31], a[212:215], v[68:71]\n"
      "s_waitcnt vmcnt(10)\n"
      "v_cvt_pk_bf16_f32 v184, v28, v32\n"
      "v_cvt_pk_bf16_f32 v185, v29, v33\n"
      "v_mfma_f32_16x16x32_bf16 v[72:75], a[32:35], a[212:215], v[72:75]\n"
      "s_waitcnt lgkmcnt(0)\n"
      "v_cvt_pk_bf16_f32 v186, v30, v34\n"
      "v_cvt_pk_bf16_f32 v187, v31, v35\n"
      "s_barrier\n"
      "v_mfma_f32_4x4x4_16b_bf16 v[28:31], a[244:245], a[232:233], 0\n"
      "v_mfma_f32_4x4x4_16b_bf16 v[32:35], a[244:245], a[234:235], 0\n"
      "s_add_u32 m0, s63, 6144\n"
      "s_nop 0\n"
      "buffer_load_dwordx4 v18, s[12:15], s52 offen offset:1024 lds\n"
      "v_mfma_f32_4x4x4_16b_bf16 v[28:31], a[248:249], a[40:41], v[28:31]\n"
      "ds_read_b64_tr_b16 a[224:225], v62 offset:1536\n"
      "v_mfma_f32_4x4x4_16b_bf16 v[32:35], a[248:249], a[42:43], v[32:35]\n"
      "buffer_load_dwordx4 v19, s[12:15], s52 offen offset:2048 lds\n"
      "v_mfma_f32_4x4x4_16b_bf16 v[36:39], a[246:247], a[228:229], v[28:31]\n"
      "ds_read_b64_tr_b16 a[226:227], v62 offset:2304\n"
      "v_mfma_f32_4x4x4_16b_bf16 v[40:43], a[246:247], a[230:231], v[32:35]\n"
      "ds_read_b64_tr_b16 a[48:49], v62 offset:13824\n"
      "v_mfma_f32_4x4x4_16b_bf16 v[28:31], a[250:251], a[228:229], v[28:31]\n"
      "ds_read_b64_tr_b16 a[50:51], v62 offset:14592\n"
      "v_mfma_f32_4x4x4_16b_bf16 v[32:35], a[250:251], a[230:231], v[32:35]\n"
      "ds_read_b64_tr_b16 a[220:221], v62 offset:7680\n"
      "ds_read_b64_tr_b16 a[222:223], v62 offset:8448\n"
      "ds_read_b128 a[192:195], v4 offset:0\n"
      "ds_read_b128 a[196:199], v0 offset:0\n"
      "ds_read_b128 a[200:203], v0 offset:4096\n"
      "v_cvt_pk_bf16_f32 v180, v52, v56\n"
      "v_cvt_pk_bf16_f32 v181, v53, v57\n"
      "v_cvt_pk_bf16_f32 v182, v54, v58\n"
      "v_cvt_pk_bf16_f32 v183, v55, v59\n"
      "v_cvt_pk_bf16_f32 v176, v44, v48\n"
      "v_cvt_pk_bf16_f32 v177, v45, v49\n"
      "v_cvt_pk_bf16_f32 v178, v46, v50\n"
      "v_cvt_pk_bf16_f32 v179, v47, v51\n"
      "s_add_u32 m0, s63, 18432\n"
      "s_add_u32 s52, s63, 43008\n"
      "v_mfma_f32_4x4x4_16b_bf16 v[44:47], a[244:245], a[240:241], 0\n"
      "v_mfma_f32_4x4x4_16b_bf16 v[48:51], a[244:245], a[242:243], 0\n"
      "buffer_load_dwordx4 v17, s[12:15], s52 offen offset:0 lds\n"
      "v_mfma_f32_4x4x4_16b_bf16 v[44:47], a[248:249], a[44:45], v[44:47]\n"
      "ds_read_b64_tr_b16 a[232:233], v62 offset:1600\n"
      "v_mfma_f32_4x4x4_16b_bf16 v[48:51], a[248:249], a[46:47], v[48:51]\n"
      "buffer_load_dwordx4 v18, s[12:15], s52 offen offset:1024 lds\n"
      "v_mfma_f32_4x4x4_16b_bf16 v[52:55], a[246:247], a[236:237], v[44:47]\n"
      "ds_read_b64_tr_b16 a[234:235], v62 offset:2368\n"
      "v_mfma_f32_4x4x4_16b_bf16 v[56:59], a[246:247], a[238:239], v[48:51]\n"
      "ds_read_b64_tr_b16 a[52:53], v62 offset:13888\n"
      "v_mfma_f32_4x4x4_16b_bf16 v[44:47], a[250:251], a[236:237], v[44:47]\n"
      "ds_read_b64_tr_b16 a[54:55], v62 offset:14656\n"
      "v_mfma_f32_4x4x4_16b_bf16 v[48:51], a[250:251], a[238:239], v[48:51]\n"
      "ds_read_b64_tr_b16 a[228:229], v62 offset:7744\n"
      "ds_read_b64_tr_b16 a[230:231], v62 offset:8512\n"
      "ds_read_b128 a[204:207], v0 offset:64\n"
      "ds_read_b128 a[208:211], v0 offset:4160\n"
      "ds_read_b128 a[212:215], v0 offset:128\n"
      "v_cvt_pk_bf16_f32 v172, v36, v40\n"
      "v_cvt_pk_bf16_f32 v173, v37, v41\n"
      "v_cvt_pk_bf16_f32 v174, v38, v42\n"
      "v_cvt_pk_bf16_f32 v175, v39, v43\n"
      "v_cvt_pk_bf16_f32 v168, v28, v32\n"
      "v_cvt_pk_bf16_f32 v169, v29, v33\n"
      "v_cvt_pk_bf16_f32 v170, v30, v34\n"
      "v_cvt_pk_bf16_f32 v171, v31, v35\n"
      "s_nop 0\n"
      "s_waitcnt lgkmcnt(6)\n"
      "v_mfma_f32_4x4x4_16b_bf16 v[28:31], a[244:245], a[224:225], 0\n"
      "v_mfma_f32_4x4x4_16b_bf16 v[32:35], a[244:245], a[226:227], 0\n"
      "buffer_load_dwordx4 v19, s[12:15], s52 offen offset:2048 lds\n"
      "v_mfma_f32_4x4x4_16b_bf16 v[28:31], a[248:249], a[48:49], v[28:31]\n"
      "ds_read_b64_tr_b16 a[240:241], v62 offset:1664\n"
      "v_mfma_f32_4x4x4_16b_bf16 v[32:35], a[248:249], a[50:51], v[32:35]\n"
      "s_add_u32 m0, s63, 57600\n"
      "v_mfma_f32_4x4x4_16b_bf16 v[36:39], a[246:247], a[220:221], v[28:31]\n"
      "ds_read_b64_tr_b16 a[242:243], v62 offset:2432\n"
      "v_mfma_f32_4x4x4_16b_bf16 v[40:43], a[246:247], a[222:223], v[32:35]\n"
      "s_add_u32 s52, s63, 98304\n"
      "v_mfma_f32_4x4x4_16b_bf16 v[28:31], a[250:251], a[220:221], v[28:31]\n"
      "ds_read_b64_tr_b16 a[56:57], v62 offset:13952\n"
      "v_mfma_f32_4x4x4_16b_bf16 v[32:35], a[250:251], a[222:223], v[32:35]\n"
      "buffer_load_dwordx4 v17, s[12:15], s52 offen offset:0 lds\n"
      "ds_read_b64_tr_b16 a[58:59], v62 offset:14720\n"
      "ds_read_b64_tr_b16 a[236:237], v62 offset:7808\n"
      "ds_read_b64_tr_b16 a[238:239], v62 offset:8576\n"
      "v_mfma_f32_16x16x32_bf16 v[64:67], a[36:39], a[192:195], v[64:67]\n"
      "v_cvt_pk_bf16_f32 v164, v52, v56\n"
      "v_cvt_pk_bf16_f32 v165, v53, v57\n"
      "v_mfma_f32_16x16x32_bf16 v[68:71], a[40:43], a[192:195], v[68:71]\n"
      "v_cvt_pk_bf16_f32 v166, v54, v58\n"
      "v_cvt_pk_bf16_f32 v167, v55, v59\n"
      "v_mfma_f32_16x16x32_bf16 v[72:75], a[44:47], a[192:195], v[72:75]\n"
      "s_nop 0\n"
      "v_cvt_pk_bf16_f32 v160, v44, v48\n"
      "v_cvt_pk_bf16_f32 v161, v45, v49\n"
      "v_mfma_f32_16x16x32_bf16 v[76:79], v[252:255], a[196:199], 0\n"
      "s_waitcnt vmcnt(10)\n"
      "v_cvt_pk_bf16_f32 v162, v46, v50\n"
      "v_cvt_pk_bf16_f32 v163, v47, v51\n"
      "s_barrier\n"
      "v_mfma_f32_4x4x4_16b_bf16 v[44:47], a[244:245], a[232:233], 0\n"
      "v_mfma_f32_4x4x4_16b_bf16 v[48:51], a[244:245], a[234:235], 0\n"
      "buffer_load_dwordx4 v18, s[12:15], s52 offen offset:1024 lds\n"
      "s_waitcnt lgkmcnt(0)\n"
      "v_mfma_f32_4x4x4_16b_bf16 v[44:47], a[248:249], a[52:53], v[44:47]\n"
      "ds_read_b64_tr_b16 a[224:225], v60 offset:41216\n"
      "v_mfma_f32_4x4x4_16b_bf16 v[48:51], a[248:249], a[54:55], v[48:51]\n"
      "buffer_load_dwordx4 v19, s[12:15], s52 offen offset:2048 lds\n"
      "v_mfma_f32_4x4x4_16b_bf16 v[52:55], a[246:247], a[228:229], v[44:47]\n"
      "ds_read_b64_tr_b16 a[226:227], v60 offset:41984\n"
      "v_mfma_f32_4x4x4_16b_bf16 v[56:59], a[246:247], a[230:231], v[48:51]\n"
      "ds_read_b64_tr_b16 a[60:61], v62 offset:19968\n"
      "v_mfma_f32_4x4x4_16b_bf16 v[44:47], a[250:251], a[228:229], v[44:47]\n"
      "ds_read_b64_tr_b16 a[62:63], v62 offset:20736\n"
      "v_mfma_f32_4x4x4_16b_bf16 v[48:51], a[250:251], a[230:231], v[48:51]\n"
      "ds_read_b64_tr_b16 a[220:221], v60 offset:47360\n"
      "ds_read_b64_tr_b16 a[222:223], v60 offset:48128\n"
      "ds_read_b128 a[192:195], v0 offset:4224\n"
      "v_mfma_f32_16x16x32_bf16 v[80:83], v[244:247], a[196:199], 0\n"
      "v_cvt_pk_bf16_f32 v156, v36, v40\n"
      "v_cvt_pk_bf16_f32 v157, v37, v41\n"
      "v_mfma_f32_16x16x32_bf16 v[84:87], v[236:239], a[196:199], 0\n"
      "v_cvt_pk_bf16_f32 v158, v38, v42\n"
      "v_cvt_pk_bf16_f32 v159, v39, v43\n"
      "v_mfma_f32_16x16x32_bf16 v[88:91], v[248:251], a[200:203], 0\n"
      "v_cvt_pk_bf16_f32 v152, v28, v32\n"
      "v_cvt_pk_bf16_f32 v153, v29, v33\n"
      "v_mfma_f32_16x16x32_bf16 v[92:95], v[240:243], a[200:203], 0\n"
      "s_add_u32 m0, s63, 69888\n"
      "v_cvt_pk_bf16_f32 v154, v30, v34\n"
      "v_cvt_pk_bf16_f32 v155, v31, v35\n"
      "s_add_u32 s52, s63, 49152\n"
      "v_mfma_f32_4x4x4_16b_bf16 v[28:31], a[244:245], a[240:241], 0\n"
      "v_mfma_f32_4x4x4_16b_bf16 v[32:35], a[244:245], a[242:243], 0\n"
      "buffer_load_dwordx4 v17, s[12:15], s52 offen offset:0 lds\n"
      "v_mfma_f32_4x4x4_16b_bf16 v[28:31], a[248:249], a[56:57], v[28:31]\n"
      "ds_read_b64_tr_b16 a[232:233], v60 offset:41280\n"
      "v_mfma_f32_4x4x4_16b_bf16 v[32:35], a[248:249], a[58:59], v[32:35]\n"
      "buffer_load_dwordx4 v18, s[12:15], s52 offen offset:1024 lds\n"
      "v_mfma_f32_4x4x4_16b_bf16 v[36:39], a[246:247], a[236:237], v[28:31]\n"
      "ds_read_b64_tr_b16 a[234:235], v60 offset:42048\n"
      "v_mfma_f32_4x4x4_16b_bf16 v[40:43], a[246:247], a[238:239], v[32:35]\n"
      "ds_read_b64_tr_b16 a[64:65], v62 offset:20032\n"
      "v_mfma_f32_4x4x4_16b_bf16 v[28:31], a[250:251], a[236:237], v[28:31]\n"
      "ds_read_b64_tr_b16 a[66:67], v62 offset:20800\n"
      "v_mfma_f32_4x4x4_16b_bf16 v[32:35], a[250:251], a[238:239], v[32:35]\n"
      "ds_read_b64_tr_b16 a[228:229], v60 offset:47424\n"
      "ds_read_b64_tr_b16 a[230:231], v60 offset:48192\n"
      "ds_read_b128 a[196:199], v1 offset:0\n"
      "v_mfma_f32_16x16x32_bf16 v[96:99], v[232:235], a[200:203], 0\n"
      "v_cvt_pk_bf16_f32 v148, v52, v56\n"
      "v_cvt_pk_bf16_f32 v149, v53, v57\n"
      "v_mfma_f32_16x16x32_bf16 v[76:79], v[228:231], a[204:207], v[76:79]\n"
      "v_cvt_pk_bf16_f32 v150, v54, v58\n"
      "v_cvt_pk_bf16_f32 v151, v55, v59\n"
      "v_mfma_f32_16x16x32_bf16 v[80:83], v[220:223], a[204:207], v[80:83]\n"
      "s_nop 0\n"
      "v_cvt_pk_bf16_f32 v144, v44, v48\n"
      "v_cvt_pk_bf16_f32 v145, v45, v49\n"
      "v_mfma_f32_16x16x32_bf16 v[84:87], v[212:215], a[204:207], v[84:87]\n"
      "s_waitcnt lgkmcnt(2)\n"
      "v_cvt_pk_bf16_f32 v146, v46, v50\n"
      "v_cvt_pk_bf16_f32 v147, v47, v51\n"
      "v_mfma_f32_4x4x4_16b_bf16 v[44:47], a[244:245], a[224:225], 0\n"
      "v_mfma_f32_4x4x4_16b_bf16 v[48:51], a[244:245], a[226:227], 0\n"
      "buffer_load_dwordx4 v19, s[12:15], s52 offen offset:2048 lds\n"
      "v_mfma_f32_4x4x4_16b_bf16 v[44:47], a[248:249], a[60:61], v[44:47]\n"
      "ds_read_b64_tr_b16 a[240:241], v60 offset:41344\n"
      "v_mfma_f32_4x4x4_16b_bf16 v[48:51], a[248:249], a[62:63], v[48:51]\n"
      "s_add_u32 m0, s63, 76032\n"
      "v_mfma_f32_4x4x4_16b_bf16 v[52:55], a[246:247], a[220:221], v[44:47]\n"
      "ds_read_b64_tr_b16 a[242:243], v60 offset:42112\n"
      "v_mfma_f32_4x4x4_16b_bf16 v[56:59], a[246:247], a[222:223], v[48:51]\n"
      "s_add_u32 s52, s63, 104448\n"
      "v_mfma_f32_4x4x4_16b_bf16 v[44:47], a[250:251], a[220:221], v[44:47]\n"
      "ds_read_b64_tr_b16 a[68:69], v62 offset:20096\n"
      "v_mfma_f32_4x4x4_16b_bf16 v[48:51], a[250:251], a[222:223], v[48:51]\n"
      "buffer_load_dwordx4 v17, s[12:15], s52 offen offset:0 lds\n"
      "ds_read_b64_tr_b16 a[70:71], v62 offset:20864\n"
      "ds_read_b64_tr_b16 a[236:237], v60 offset:47488\n"
      "ds_read_b64_tr_b16 a[238:239], v60 offset:48256\n"
      "ds_read_b128 a[200:203], v1 offset:4096\n"
      "ds_read_b128 a[204:207], v4 offset:64\n"
      "v_mfma_f32_16x16x32_bf16 v[88:91], v[224:227], a[208:211], v[88:91]\n"
      "v_cvt_pk_bf16_f32 v140, v36, v40\n"
      "v_cvt_pk_bf16_f32 v141, v37, v41\n"
      "v_mfma_f32_16x16x32_bf16 v[92:95], v[216:219], a[208:211], v[92:95]\n"
      "v_cvt_pk_bf16_f32 v142, v38, v42\n"
      "v_cvt_pk_bf16_f32 v143, v39, v43\n"
      "v_mfma_f32_16x16x32_bf16 v[96:99], v[208:211], a[208:211], v[96:99]\n"
      "s_nop 0\n"
      "v_cvt_pk_bf16_f32 v136, v28, v32\n"
      "v_cvt_pk_bf16_f32 v137, v29, v33\n"
      "v_mfma_f32_16x16x32_bf16 v[76:79], v[204:207], a[212:215], v[76:79]\n"
      "s_waitcnt vmcnt(10)\n"
      "v_cvt_pk_bf16_f32 v138, v30, v34\n"
      "v_cvt_pk_bf16_f32 v139, v31, v35\n"
      "s_barrier\n"
      "v_mfma_f32_4x4x4_16b_bf16 v[28:31], a[244:245], a[232:233], 0\n"
      "v_mfma_f32_4x4x4_16b_bf16 v[32:35], a[244:245], a[234:235], 0\n"
      "buffer_load_dwordx4 v18, s[12:15], s52 offen offset:1024 lds\n"
      "v_mfma_f32_4x4x4_16b_bf16 v[28:31], a[248:249], a[64:65], v[28:31]\n"
      "ds_read_b64_tr_b16 a[224:225], v60 offset:0\n"
      "v_mfma_f32_4x4x4_16b_bf16 v[32:35], a[248:249], a[66:67], v[32:35]\n"
      "buffer_load_dwordx4 v19, s[12:15], s52 offen offset:2048 lds\n"
      "s_waitcnt lgkmcnt(1)\n"
      "v_mfma_f32_4x4x4_16b_bf16 v[36:39], a[246:247], a[228:229], v[28:31]\n"
      "ds_read_b64_tr_b16 a[226:227], v60 offset:768\n"
      "v_mfma_f32_4x4x4_16b_bf16 v[40:43], a[246:247], a[230:231], v[32:35]\n"
      "ds_read_b64_tr_b16 a[72:73], v60 offset:12288\n"
      "v_mfma_f32_4x4x4_16b_bf16 v[28:31], a[250:251], a[228:229], v[28:31]\n"
      "ds_read_b64_tr_b16 a[74:75], v60 offset:13056\n"
      "v_mfma_f32_4x4x4_16b_bf16 v[32:35], a[250:251], a[230:231], v[32:35]\n"
      "ds_read_b64_tr_b16 a[220:221], v60 offset:6144\n"
      "ds_read_b64_tr_b16 a[222:223], v60 offset:6912\n"
      "ds_read_b128 a[208:211], v1 offset:64\n"
      "v_mfma_f32_16x16x32_bf16 v[80:83], v[196:199], a[212:215], v[80:83]\n"
      "v_cvt_pk_bf16_f32 v132, v52, v56\n"
      "v_cvt_pk_bf16_f32 v133, v53, v57\n"
      "v_mfma_f32_16x16x32_bf16 v[84:87], v[188:191], a[212:215], v[84:87]\n"
      "v_cvt_pk_bf16_f32 v134, v54, v58\n"
      "v_cvt_pk_bf16_f32 v135, v55, v59\n"
      "v_mfma_f32_16x16x32_bf16 v[88:91], v[200:203], a[192:195], v[88:91]\n"
      "v_cvt_pk_bf16_f32 v128, v44, v48\n"
      "v_cvt_pk_bf16_f32 v129, v45, v49\n"
      "v_mfma_f32_16x16x32_bf16 v[92:95], v[192:195], a[192:195], v[92:95]\n"
      "v_cvt_pk_bf16_f32 v130, v46, v50\n"
      "v_cvt_pk_bf16_f32 v131, v47, v51\n"
      "v_mfma_f32_4x4x4_16b_bf16 v[44:47], a[244:245], a[240:241], 0\n"
      "v_mfma_f32_4x4x4_16b_bf16 v[48:51], a[244:245], a[242:243], 0\n"
      "ds_read_b64_tr_b16 a[232:233], v60 offset:64\n"
      "v_mfma_f32_4x4x4_16b_bf16 v[44:47], a[248:249], a[68:69], v[44:47]\n"
      "ds_read_b64_tr_b16 a[234:235], v60 offset:832\n"
      "v_mfma_f32_4x4x4_16b_bf16 v[48:51], a[248:249], a[70:71], v[48:51]\n"
      "ds_read_b64_tr_b16 a[76:77], v60 offset:12352\n"
      "v_mfma_f32_4x4x4_16b_bf16 v[52:55], a[246:247], a[236:237], v[44:47]\n"
      "ds_read_b64_tr_b16 a[78:79], v60 offset:13120\n"
      "v_mfma_f32_4x4x4_16b_bf16 v[56:59], a[246:247], a[238:239], v[48:51]\n"
      "ds_read_b64_tr_b16 a[228:229], v60 offset:6208\n"
      "v_mfma_f32_4x4x4_16b_bf16 v[44:47], a[250:251], a[236:237], v[44:47]\n"
      "ds_read_b64_tr_b16 a[230:231], v60 offset:6976\n"
      "v_mfma_f32_4x4x4_16b_bf16 v[48:51], a[250:251], a[238:239], v[48:51]\n"
      "ds_read_b128 a[212:215], v1 offset:4160\n"
      "v_mfma_f32_16x16x32_bf16 v[96:99], v[184:187], a[192:195], v[96:99]\n"
      "v_cvt_pk_bf16_f32 v124, v36, v40\n"
      "v_cvt_pk_bf16_f32 v125, v37, v41\n"
      "v_mfma_f32_16x16x32_bf16 v[76:79], v[180:183], a[196:199], v[76:79]\n"
      "v_cvt_pk_bf16_f32 v126, v38, v42\n"
      "v_cvt_pk_bf16_f32 v127, v39, v43\n"
      "v_mfma_f32_16x16x32_bf16 v[80:83], v[172:175], a[196:199], v[80:83]\n"
      "s_nop 0\n"
      "v_cvt_pk_bf16_f32 v120, v28, v32\n"
      "v_cvt_pk_bf16_f32 v121, v29, v33\n"
      "v_mfma_f32_16x16x32_bf16 v[84:87], v[164:167], a[196:199], v[84:87]\n"
      "s_waitcnt lgkmcnt(1)\n"
      "v_cvt_pk_bf16_f32 v122, v30, v34\n"
      "v_cvt_pk_bf16_f32 v123, v31, v35\n"
      "v_mfma_f32_4x4x4_16b_bf16 v[28:31], a[244:245], a[224:225], 0\n"
      "v_mfma_f32_4x4x4_16b_bf16 v[32:35], a[244:245], a[226:227], 0\n"
      "ds_read_b64_tr_b16 a[240:241], v60 offset:128\n"
      "v_mfma_f32_4x4x4_16b_bf16 v[28:31], a[248:249], a[72:73], v[28:31]\n"
      "ds_read_b64_tr_b16 a[242:243], v60 offset:896\n"
      "v_mfma_f32_4x4x4_16b_bf16 v[32:35], a[248:249], a[74:75], v[32:35]\n"
      "ds_read_b64_tr_b16 a[80:81], v60 offset:12416\n"
      "v_mfma_f32_4x4x4_16b_bf16 v[36:39], a[246:247], a[220:221], v[28:31]\n"
      "ds_read_b64_tr_b16 a[82:83], v60 offset:13184\n"
      "v_mfma_f32_4x4x4_16b_bf16 v[40:43], a[246:247], a[222:223], v[32:35]\n"
      "ds_read_b64_tr_b16 a[236:237], v60 offset:6272\n"
      "v_mfma_f32_4x4x4_16b_bf16 v[28:31], a[250:251], a[220:221], v[28:31]\n"
      "ds_read_b64_tr_b16 a[238:239], v60 offset:7040\n"
      "v_mfma_f32_4x4x4_16b_bf16 v[32:35], a[250:251], a[222:223], v[32:35]\n"
      "ds_read_b128 a[192:195], v4 offset:128\n"
      "ds_read_b128 a[196:199], v1 offset:128\n"
      "v_mfma_f32_16x16x32_bf16 v[88:91], v[176:179], a[200:203], v[88:91]\n"
      "v_cvt_pk_bf16_f32 v116, v52, v56\n"
      "v_cvt_pk_bf16_f32 v117, v53, v57\n"
      "v_mfma_f32_16x16x32_bf16 v[92:95], v[168:171], a[200:203], v[92:95]\n"
      "v_cvt_pk_bf16_f32 v118, v54, v58\n"
      "v_cvt_pk_bf16_f32 v119, v55, v59\n"
      "v_mfma_f32_16x16x32_bf16 v[96:99], v[160:163], a[200:203], v[96:99]\n"
      "v_cvt_pk_bf16_f32 v112, v44, v48\n"
      "v_cvt_pk_bf16_f32 v113, v45, v49\n"
      "v_mfma_f32_16x16x32_bf16 v[64:67], a[48:51], a[204:207], v[64:67]\n"
      "s_waitcnt vmcnt(6)\n"
      "v_cvt_pk_bf16_f32 v114, v46, v50\n"
      "v_cvt_pk_bf16_f32 v115, v47, v51\n"
      "s_barrier\n"
      "v_mfma_f32_4x4x4_16b_bf16 v[44:47], a[244:245], a[232:233], 0\n"
      "v_mfma_f32_4x4x4_16b_bf16 v[48:51], a[244:245], a[234:235], 0\n"
      "ds_read_b64_tr_b16 a[224:225], v60 offset:18432\n"
      "v_mfma_f32_4x4x4_16b_bf16 v[44:47], a[248:249], a[76:77], v[44:47]\n"
      "ds_read_b64_tr_b16 a[226:227], v60 offset:19200\n"
      "v_mfma_f32_4x4x4_16b_bf16 v[48:51], a[248:249], a[78:79], v[48:51]\n"
      "ds_read_b64_tr_b16 a[84:85], v61 offset:6400\n"
      "v_mfma_f32_4x4x4_16b_bf16 v[52:55], a[246:247], a[228:229], v[44:47]\n"
      "ds_read_b64_tr_b16 a[86:87], v61 offset:7168\n"
      "v_mfma_f32_4x4x4_16b_bf16 v[56:59], a[246:247], a[230:231], v[48:51]\n"
      "ds_read_b64_tr_b16 a[220:221], v61 offset:256\n"
      "v_mfma_f32_4x4x4_16b_bf16 v[44:47], a[250:251], a[228:229], v[44:47]\n"
      "ds_read_b64_tr_b16 a[222:223], v61 offset:1024\n"
      "v_mfma_f32_4x4x4_16b_bf16 v[48:51], a[250:251], a[230:231], v[48:51]\n"
      "ds_read_b128 a[200:203], v1 offset:4224\n"
      "v_mfma_f32_16x16x32_bf16 v[68:71], a[52:55], a[204:207], v[68:71]\n"
      "v_cvt_pk_bf16_f32 v108, v36, v40\n"
      "v_cvt_pk_bf16_f32 v109, v37, v41\n"
      "v_mfma_f32_16x16x32_bf16 v[72:75], a[56:59], a[204:207], v[72:75]\n"
      "v_cvt_pk_bf16_f32 v110, v38, v42\n"
      "v_cvt_pk_bf16_f32 v111, v39, v43\n"
      "v_mfma_f32_16x16x32_bf16 v[76:79], v[156:159], a[208:211], v[76:79]\n"
      "v_cvt_pk_bf16_f32 v104, v28, v32\n"
      "v_cvt_pk_bf16_f32 v105, v29, v33\n"
      "v_mfma_f32_16x16x32_bf16 v[80:83], v[148:151], a[208:211], v[80:83]\n"
      "s_nop 0\n"
      "s_waitcnt lgkmcnt(1)\n"
      "v_cvt_pk_bf16_f32 v106, v30, v34\n"
      "v_cvt_pk_bf16_f32 v107, v31, v35\n"
      "v_mfma_f32_4x4x4_16b_bf16 v[28:31], a[244:245], a[240:241], 0\n"
      "v_mfma_f32_4x4x4_16b_bf16 v[32:35], a[244:245], a[242:243], 0\n"
      "ds_read_b64_tr_b16 a[232:233], v60 offset:18496\n"
      "v_mfma_f32_4x4x4_16b_bf16 v[28:31], a[248:249], a[80:81], v[28:31]\n"
      "ds_read_b64_tr_b16 a[234:235], v60 offset:19264\n"
      "v_mfma_f32_4x4x4_16b_bf16 v[32:35], a[248:249], a[82:83], v[32:35]\n"
      "ds_read_b64_tr_b16 a[88:89], v61 offset:6464\n"
      "v_mfma_f32_4x4x4_16b_bf16 v[36:39], a[246:247], a[236:237], v[28:31]\n"
      "ds_read_b64_tr_b16 a[90:91], v61 offset:7232\n"
      "v_mfma_f32_4x4x4_16b_bf16 v[40:43], a[246:247], a[238:239], v[32:35]\n"
      "ds_read_b64_tr_b16 a[228:229], v61 offset:320\n"
      "v_mfma_f32_4x4x4_16b_bf16 v[28:31], a[250:251], a[236:237], v[28:31]\n"
      "ds_read_b64_tr_b16 a[230:231], v61 offset:1088\n"
      "v_mfma_f32_4x4x4_16b_bf16 v[32:35], a[250:251], a[238:239], v[32:35]\n"
      "ds_read_b128 a[204:207], v5 offset:0\n"
      "v_mfma_f32_16x16x32_bf16 v[84:87], v[140:143], a[208:211], v[84:87]\n"
      "v_cvt_pk_bf16_f32 v100, v52, v56\n"
      "v_cvt_pk_bf16_f32 v101, v53, v57\n"
      "v_mfma_f32_16x16x32_bf16 v[88:91], v[152:155], a[212:215], v[88:91]\n"
      "v_cvt_pk_bf16_f32 v102, v54, v58\n"
      "v_cvt_pk_bf16_f32 v103, v55, v59\n"
      "v_mfma_f32_16x16x32_bf16 v[92:95], v[144:147], a[212:215], v[92:95]\n"
      "v_cvt_pk_bf16_f32 v44, v44, v48\n"
      "v_cvt_pk_bf16_f32 v45, v45, v49\n"
      "v_mfma_f32_16x16x32_bf16 v[96:99], v[136:139], a[212:215], v[96:99]\n"
      "v_cvt_pk_bf16_f32 v46, v46, v50\n"
      "v_cvt_pk_bf16_f32 v47, v47, v51\n"
      "v_mfma_f32_16x16x32_bf16 v[64:67], a[60:63], a[192:195], v[64:67]\n"
      "v_accvgpr_write_b32 a164, v44\n"
      "v_accvgpr_write_b32 a165, v45\n"
      "v_mfma_f32_16x16x32_bf16 v[68:71], a[64:67], a[192:195], v[68:71]\n"
      "v_accvgpr_write_b32 a166, v46\n"
      "v_accvgpr_write_b32 a167, v47\n"
      "v_mfma_f32_4x4x4_16b_bf16 v[44:47], a[244:245], a[224:225], 0\n"
      "v_mfma_f32_4x4x4_16b_bf16 v[48:51], a[244:245], a[226:227], 0\n"
      "ds_read_b64_tr_b16 a[240:241], v60 offset:18560\n"
      "v_mfma_f32_4x4x4_16b_bf16 v[44:47], a[248:249], a[84:85], v[44:47]\n"
      "ds_read_b64_tr_b16 a[242:243], v60 offset:19328\n"
      "v_mfma_f32_4x4x4_16b_bf16 v[48:51], a[248:249], a[86:87], v[48:51]\n"
      "ds_read_b64_tr_b16 a[92:93], v61 offset:6528\n"
      "v_mfma_f32_4x4x4_16b_bf16 v[52:55], a[246:247], a[220:221], v[44:47]\n"
      "ds_read_b64_tr_b16 a[94:95], v61 offset:7296\n"
      "v_mfma_f32_4x4x4_16b_bf16 v[56:59], a[246:247], a[222:223], v[48:51]\n"
      "ds_read_b64_tr_b16 a[236:237], v61 offset:384\n"
      "v_mfma_f32_4x4x4_16b_bf16 v[44:47], a[250:251], a[220:221], v[44:47]\n"
      "ds_read_b64_tr_b16 a[238:239], v61 offset:1152\n"
      "v_mfma_f32_4x4x4_16b_bf16 v[48:51], a[250:251], a[222:223], v[48:51]\n"
      "ds_read_b128 a[208:211], v2 offset:0\n"
      "ds_read_b128 a[212:215], v2 offset:4096\n"
      "v_cvt_pk_bf16_f32 v36, v36, v40\n"
      "v_cvt_pk_bf16_f32 v37, v37, v41\n"
      "v_cvt_pk_bf16_f32 v38, v38, v42\n"
      "v_cvt_pk_bf16_f32 v39, v39, v43\n"
      "v_mfma_f32_16x16x32_bf16 v[72:75], a[68:71], a[192:195], v[72:75]\n"
      "v_accvgpr_write_b32 a160, v36\n"
      "v_accvgpr_write_b32 a161, v37\n"
      "v_mfma_f32_16x16x32_bf16 v[76:79], v[132:135], a[196:199], v[76:79]\n"
      "v_accvgpr_write_b32 a162, v38\n"
      "v_accvgpr_write_b32 a163, v39\n"
      "v_mfma_f32_16x16x32_bf16 v[80:83], v[124:127], a[196:199], v[80:83]\n"
      "v_cvt_pk_bf16_f32 v28, v28, v32\n"
      "v_cvt_pk_bf16_f32 v29, v29, v33\n"
      "v_mfma_f32_16x16x32_bf16 v[84:87], v[116:119], a[196:199], v[84:87]\n"
      "s_waitcnt lgkmcnt(1)\n"
      "v_cvt_pk_bf16_f32 v30, v30, v34\n"
      "v_cvt_pk_bf16_f32 v31, v31, v35\n"
      "v_mfma_f32_16x16x32_bf16 v[88:91], v[128:131], a[200:203], v[88:91]\n"
      "s_nop 0\n"
      "v_accvgpr_write_b32 a156, v28\n"
      "v_accvgpr_write_b32 a157, v29\n"
      "v_mfma_f32_16x16x32_bf16 v[92:95], v[120:123], a[200:203], v[92:95]\n"
      "s_waitcnt vmcnt(0)\n"
      "v_accvgpr_write_b32 a158, v30\n"
      "v_accvgpr_write_b32 a159, v31\n"
      "s_barrier\n"
      "v_mfma_f32_4x4x4_16b_bf16 v[28:31], a[244:245], a[232:233], 0\n"
      "v_mfma_f32_4x4x4_16b_bf16 v[32:35], a[244:245], a[234:235], 0\n"
      "ds_read_b64_tr_b16 a[224:225], v61 offset:12544\n"
      "v_mfma_f32_4x4x4_16b_bf16 v[28:31], a[248:249], a[88:89], v[28:31]\n"
      "ds_read_b64_tr_b16 a[226:227], v61 offset:13312\n"
      "v_mfma_f32_4x4x4_16b_bf16 v[32:35], a[248:249], a[90:91], v[32:35]\n"
      "ds_read_b64_tr_b16 a[96:97], v62 offset:39680\n"
      "v_mfma_f32_4x4x4_16b_bf16 v[36:39], a[246:247], a[228:229], v[28:31]\n"
      "ds_read_b64_tr_b16 a[98:99], v62 offset:40448\n"
      "v_mfma_f32_4x4x4_16b_bf16 v[40:43], a[246:247], a[230:231], v[32:35]\n"
      "ds_read_b64_tr_b16 a[220:221], v61 offset:18688\n"
      "v_mfma_f32_4x4x4_16b_bf16 v[28:31], a[250:251], a[228:229], v[28:31]\n"
      "ds_read_b64_tr_b16 a[222:223], v61 offset:19456\n"
      "v_mfma_f32_4x4x4_16b_bf16 v[32:35], a[250:251], a[230:231], v[32:35]\n"
      "ds_read_b128 a[192:195], v5 offset:64\n"
      "v_cvt_pk_bf16_f32 v52, v52, v56\n"
      "v_cvt_pk_bf16_f32 v53, v53, v57\n"
      "v_cvt_pk_bf16_f32 v54, v54, v58\n"
      "v_cvt_pk_bf16_f32 v55, v55, v59\n"
      "v_mfma_f32_16x16x32_bf16 v[96:99], v[112:115], a[200:203], v[96:99]\n"
      "v_accvgpr_write_b32 a152, v52\n"
      "v_accvgpr_write_b32 a153, v53\n"
      "v_mfma_f32_16x16x32_bf16 v[64:67], a[72:75], a[204:207], v[64:67]\n"
      "v_accvgpr_write_b32 a154, v54\n"
      "v_accvgpr_write_b32 a155, v55\n"
      "v_mfma_f32_16x16x32_bf16 v[68:71], a[76:79], a[204:207], v[68:71]\n"
      "v_cvt_pk_bf16_f32 v44, v44, v48\n"
      "v_cvt_pk_bf16_f32 v45, v45, v49\n"
      "v_mfma_f32_16x16x32_bf16 v[72:75], a[80:83], a[204:207], v[72:75]\n"
      "v_cvt_pk_bf16_f32 v46, v46, v50\n"
      "v_cvt_pk_bf16_f32 v47, v47, v51\n"
      "v_mfma_f32_16x16x32_bf16 v[76:79], v[108:111], a[208:211], v[76:79]\n"
      "s_nop 0\n"
      "v_accvgpr_write_b32 a148, v44\n"
      "v_accvgpr_write_b32 a149, v45\n"
      "v_mfma_f32_16x16x32_bf16 v[80:83], v[100:103], a[208:211], v[80:83]\n"
      "s_nop 0\n"
      "v_accvgpr_write_b32 a150, v46\n"
      "v_accvgpr_write_b32 a151, v47\n"
      "v_mfma_f32_4x4x4_16b_bf16 v[44:47], a[244:245], a[240:241], 0\n"
      "v_mfma_f32_4x4x4_16b_bf16 v[48:51], a[244:245], a[242:243], 0\n"
      "ds_read_b64_tr_b16 a[232:233], v61 offset:12608\n"
      "v_mfma_f32_4x4x4_16b_bf16 v[44:47], a[248:249], a[92:93], v[44:47]\n"
      "ds_read_b64_tr_b16 a[234:235], v61 offset:13376\n"
      "v_mfma_f32_4x4x4_16b_bf16 v[48:51], a[248:249], a[94:95], v[48:51]\n"
      "ds_read_b64_tr_b16 a[100:101], v62 offset:39744\n"
      "v_mfma_f32_4x4x4_16b_bf16 v[52:55], a[246:247], a[236:237], v[44:47]\n"
      "ds_read_b64_tr_b16 a[102:103], v62 offset:40512\n"
      "v_mfma_f32_4x4x4_16b_bf16 v[56:59], a[246:247], a[238:239], v[48:51]\n"
      "ds_read_b64_tr_b16 a[228:229], v61 offset:18752\n"
      "v_mfma_f32_4x4x4_16b_bf16 v[44:47], a[250:251], a[236:237], v[44:47]\n"
      "ds_read_b64_tr_b16 a[230:231], v61 offset:19520\n"
      "v_mfma_f32_4x4x4_16b_bf16 v[48:51], a[250:251], a[238:239], v[48:51]\n"
      "ds_read_b128 a[196:199], v2 offset:64\n"
      "ds_read_b128 a[200:203], v2 offset:4160\n"
      "v_cvt_pk_bf16_f32 v36, v36, v40\n"
      "v_cvt_pk_bf16_f32 v37, v37, v41\n"
      "v_cvt_pk_bf16_f32 v38, v38, v42\n"
      "v_cvt_pk_bf16_f32 v39, v39, v43\n"
      "v_accvgpr_write_b32 a144, v36\n"
      "v_accvgpr_write_b32 a145, v37\n"
      "v_accvgpr_write_b32 a146, v38\n"
      "v_accvgpr_write_b32 a147, v39\n"
      "v_mfma_f32_16x16x32_bf16 v[84:87], a[160:163], a[208:211], v[84:87]\n"
      "s_waitcnt lgkmcnt(3)\n"
      "v_cvt_pk_bf16_f32 v28, v28, v32\n"
      "v_cvt_pk_bf16_f32 v29, v29, v33\n"
      "v_mfma_f32_16x16x32_bf16 v[88:91], v[104:107], a[212:215], v[88:91]\n"
      "s_nop 0\n"
      "v_cvt_pk_bf16_f32 v30, v30, v34\n"
      "v_cvt_pk_bf16_f32 v31, v31, v35\n"
      "v_mfma_f32_16x16x32_bf16 v[92:95], a[164:167], a[212:215], v[92:95]\n"
      "s_nop 0\n"
      "v_accvgpr_write_b32 a140, v28\n"
      "v_accvgpr_write_b32 a141, v29\n"
      "v_mfma_f32_16x16x32_bf16 v[96:99], a[156:159], a[212:215], v[96:99]\n"
      "s_nop 0\n"
      "v_accvgpr_write_b32 a142, v30\n"
      "v_accvgpr_write_b32 a143, v31\n"
      "v_mfma_f32_4x4x4_16b_bf16 v[28:31], a[244:245], a[224:225], 0\n"
      "v_mfma_f32_4x4x4_16b_bf16 v[32:35], a[244:245], a[226:227], 0\n"
      "ds_read_b64_tr_b16 a[240:241], v61 offset:12672\n"
      "v_mfma_f32_4x4x4_16b_bf16 v[28:31], a[248:249], a[96:97], v[28:31]\n"
      "ds_read_b64_tr_b16 a[242:243], v61 offset:13440\n"
      "v_mfma_f32_4x4x4_16b_bf16 v[32:35], a[248:249], a[98:99], v[32:35]\n"
      "ds_read_b64_tr_b16 a[104:105], v62 offset:39808\n"
      "v_mfma_f32_4x4x4_16b_bf16 v[36:39], a[246:247], a[220:221], v[28:31]\n"
      "ds_read_b64_tr_b16 a[106:107], v62 offset:40576\n"
      "v_mfma_f32_4x4x4_16b_bf16 v[40:43], a[246:247], a[222:223], v[32:35]\n"
      "ds_read_b64_tr_b16 a[236:237], v61 offset:18816\n"
      "v_mfma_f32_4x4x4_16b_bf16 v[28:31], a[250:251], a[220:221], v[28:31]\n"
      "ds_read_b64_tr_b16 a[238:239], v61 offset:19584\n"
      "v_mfma_f32_4x4x4_16b_bf16 v[32:35], a[250:251], a[222:223], v[32:35]\n"
      "v_cvt_pk_bf16_f32 v52, v52, v56\n"
      "v_cvt_pk_bf16_f32 v53, v53, v57\n"
      "v_cvt_pk_bf16_f32 v54, v54, v58\n"
      "v_cvt_pk_bf16_f32 v55, v55, v59\n"
      "v_accvgpr_write_b32 a136, v52\n"
      "v_accvgpr_write_b32 a137, v53\n"
      "v_accvgpr_write_b32 a138, v54\n"
      "v_accvgpr_write_b32 a139, v55\n"
      "v_cvt_pk_bf16_f32 v44, v44, v48\n"
      "v_cvt_pk_bf16_f32 v45, v45, v49\n"
      "v_mfma_f32_16x16x32_bf16 v[64:67], a[84:87], a[192:195], v[64:67]\n"
      "s_nop 0\n"
      "v_cvt_pk_bf16_f32 v46, v46, v50\n"
      "v_cvt_pk_bf16_f32 v47, v47, v51\n"
      "v_mfma_f32_16x16x32_bf16 v[68:71], a[88:91], a[192:195], v[68:71]\n"
      "s_nop 0\n"
      "v_accvgpr_write_b32 a132, v44\n"
      "v_accvgpr_write_b32 a133, v45\n"
      "v_mfma_f32_16x16x32_bf16 v[72:75], a[92:95], a[192:195], v[72:75]\n"
      "s_nop 0\n"
      "v_accvgpr_write_b32 a134, v46\n"
      "v_accvgpr_write_b32 a135, v47\n"
      "v_mfma_f32_4x4x4_16b_bf16 v[44:47], a[244:245], a[232:233], 0\n"
      "v_mfma_f32_4x4x4_16b_bf16 v[48:51], a[244:245], a[234:235], 0\n"
      "ds_read_b128 a[204:207], v5 offset:128\n"
      "v_mfma_f32_4x4x4_16b_bf16 v[44:47], a[248:249], a[100:101], v[44:47]\n"
      "v_mfma_f32_4x4x4_16b_bf16 v[48:51], a[248:249], a[102:103], v[48:51]\n"
      "s_waitcnt lgkmcnt(1)\n"
      "v_mfma_f32_4x4x4_16b_bf16 v[52:55], a[246:247], a[228:229], v[44:47]\n"
      "v_mfma_f32_4x4x4_16b_bf16 v[56:59], a[246:247], a[230:231], v[48:51]\n"
      "v_mfma_f32_4x4x4_16b_bf16 v[44:47], a[250:251], a[228:229], v[44:47]\n"
      "v_mfma_f32_4x4x4_16b_bf16 v[48:51], a[250:251], a[230:231], v[48:51]\n"
      "v_cvt_pk_bf16_f32 v36, v36, v40\n"
      "v_cvt_pk_bf16_f32 v37, v37, v41\n"
      "v_cvt_pk_bf16_f32 v38, v38, v42\n"
      "v_cvt_pk_bf16_f32 v39, v39, v43\n"
      "v_mfma_f32_16x16x32_bf16 v[76:79], a[152:155], a[196:199], v[76:79]\n"
      "v_accvgpr_write_b32 a128, v36\n"
      "v_accvgpr_write_b32 a129, v37\n"
      "v_mfma_f32_16x16x32_bf16 v[80:83], a[144:147], a[196:199], v[80:83]\n"
      "v_accvgpr_write_b32 a130, v38\n"
      "v_accvgpr_write_b32 a131, v39\n"
      "v_mfma_f32_16x16x32_bf16 v[84:87], a[136:139], a[196:199], v[84:87]\n"
      "v_cvt_pk_bf16_f32 v28, v28, v32\n"
      "v_cvt_pk_bf16_f32 v29, v29, v33\n"
      "v_mfma_f32_16x16x32_bf16 v[88:91], a[148:151], a[200:203], v[88:91]\n"
      "v_cvt_pk_bf16_f32 v30, v30, v34\n"
      "v_cvt_pk_bf16_f32 v31, v31, v35\n"
      "v_mfma_f32_16x16x32_bf16 v[92:95], a[140:143], a[200:203], v[92:95]\n"
      "v_accvgpr_write_b32 a124, v28\n"
      "v_accvgpr_write_b32 a125, v29\n"
      "v_mfma_f32_16x16x32_bf16 v[96:99], a[132:135], a[200:203], v[96:99]\n"
      "v_accvgpr_write_b32 a126, v30\n"
      "v_accvgpr_write_b32 a127, v31\n"
      "v_mfma_f32_4x4x4_16b_bf16 v[28:31], a[244:245], a[240:241], 0\n"
      "v_mfma_f32_4x4x4_16b_bf16 v[32:35], a[244:245], a[242:243], 0\n"
      "ds_read_b128 a[208:211], v2 offset:128\n"
      "v_mfma_f32_4x4x4_16b_bf16 v[28:31], a[248:249], a[104:105], v[28:31]\n"
      "ds_read_b128 a[212:215], v2 offset:4224\n"
      "v_mfma_f32_4x4x4_16b_bf16 v[32:35], a[248:249], a[106:107], v[32:35]\n"
      "v_mfma_f32_4x4x4_16b_bf16 v[36:39], a[246:247], a[236:237], v[28:31]\n"
      "s_nop 0\n"
      "s_nop 0\n"
      "v_mfma_f32_4x4x4_16b_bf16 v[40:43], a[246:247], a[238:239], v[32:35]\n"
      "v_mfma_f32_4x4x4_16b_bf16 v[28:31], a[250:251], a[236:237], v[28:31]\n"
      "v_mfma_f32_4x4x4_16b_bf16 v[32:35], a[250:251], a[238:239], v[32:35]\n"
      "v_cvt_pk_bf16_f32 v52, v52, v56\n"
      "v_cvt_pk_bf16_f32 v53, v53, v57\n"
      "v_cvt_pk_bf16_f32 v54, v54, v58\n"
      "v_cvt_pk_bf16_f32 v55, v55, v59\n"
      "v_accvgpr_write_b32 a120, v52\n"
      "v_accvgpr_write_b32 a121, v53\n"
      "v_accvgpr_write_b32 a122, v54\n"
      "v_accvgpr_write_b32 a123, v55\n"
      "v_cvt_pk_bf16_f32 v44, v44, v48\n"
      "v_cvt_pk_bf16_f32 v45, v45, v49\n"
      "v_cvt_pk_bf16_f32 v46, v46, v50\n"
      "v_cvt_pk_bf16_f32 v47, v47, v51\n"
      "v_accvgpr_write_b32 a116, v44\n"
      "v_accvgpr_write_b32 a117, v45\n"
      "v_accvgpr_write_b32 a118, v46\n"
      "v_accvgpr_write_b32 a119, v47\n"
      "s_waitcnt lgkmcnt(0)\n"
      "s_barrier\n"
      "s_mov_b32 m0, s64\n"
      "s_nop 0\n"
      "v_mfma_f32_16x16x32_bf16 v[64:67], a[96:99], a[204:207], v[64:67]\n"
      "ds_read_b128 a[192:195], v3 offset:16896\n"
      "ds_read_b64_tr_b16 a[220:221], v7 offset:16896\n"
      "ds_read_b64_tr_b16 a[222:223], v7 offset:16904\n"
      "v_cvt_pk_bf16_f32 v36, v36, v40\n"
      "v_cvt_pk_bf16_f32 v37, v37, v41\n"
      "v_mfma_f32_16x16x32_bf16 v[68:71], a[100:103], a[204:207], v[68:71]\n"
      "ds_read_b128 a[196:199], v3 offset:16960\n"
      "v_cvt_pk_bf16_f32 v38, v38, v42\n"
      "v_cvt_pk_bf16_f32 v39, v39, v43\n"
      "v_accvgpr_write_b32 a112, v36\n"
      "v_accvgpr_write_b32 a113, v37\n"
      "v_mfma_f32_16x16x32_bf16 v[72:75], a[104:107], a[204:207], v[72:75]\n"
      "v_accvgpr_write_b32 a114, v38\n"
      "v_accvgpr_write_b32 a115, v39\n"
      "v_cvt_pk_bf16_f32 v28, v28, v32\n"
      "v_cvt_pk_bf16_f32 v29, v29, v33\n"
      "v_cvt_pk_bf16_f32 v30, v30, v34\n"
      "v_cvt_pk_bf16_f32 v31, v31, v35\n"
      "v_accvgpr_write_b32 a108, v28\n"
      "v_mfma_f32_16x16x32_bf16 v[76:79], a[128:131], a[208:211], v[76:79]\n"
      "ds_read_b128 a[200:203], v3 offset:17024\n"
      "ds_read_b64_tr_b16 a[224:225], v7 offset:17408\n"
      "ds_read_b64_tr_b16 a[226:227], v7 offset:17416\n"
      "v_accvgpr_write_b32 a109, v29\n"
      "v_accvgpr_write_b32 a110, v30\n"
      "v_accvgpr_write_b32 a111, v31\n"
      "v_mfma_f32_16x16x32_bf16 v[80:83], a[120:123], a[208:211], v[80:83]\n"
      "v_mfma_f32_16x16x32_bf16 v[84:87], a[112:115], a[208:211], v[84:87]\n"
      "s_waitcnt lgkmcnt(3)\n"
      "v_mfma_f32_4x4x4_16b_bf16 v[20:23], a[216:217], a[220:221], 0\n"
      "v_mfma_f32_4x4x4_16b_bf16 v[24:27], a[216:217], a[222:223], 0\n"
      "v_mfma_f32_16x16x32_bf16 v[88:91], a[124:127], a[212:215], v[88:91]\n"
      "s_nop 0\n"
      "ds_read_b128 a[204:207], v4 offset:16896\n"
      "v_mfma_f32_16x16x32_bf16 v[92:95], a[116:119], a[212:215], v[92:95]\n"
      "v_mfma_f32_16x16x32_bf16 v[96:99], a[108:111], a[212:215], v[96:99]\n"
      "ds_read_b64_tr_b16 a[228:229], v7 offset:17920\n"
      "ds_read_b64_tr_b16 a[230:231], v7 offset:17928\n"
      "v_cvt_pk_bf16_f32 v20, v20, v24\n"
      "v_cvt_pk_bf16_f32 v21, v21, v25\n"
      "v_cvt_pk_bf16_f32 v22, v22, v26\n"
      "v_sub_f32 v76, v88, v76\n"
      "v_sub_f32 v77, v89, v77\n"
      "v_cvt_pk_bf16_f32 v23, v23, v27\n"
      "v_mfma_f32_16x16x32_bf16 v[52:55], a[0:3], a[192:195], 0\n"
      "ds_read_b128 a[208:211], v4 offset:16960\n"
      "ds_write_addtid_b32 v20 offset:41216\n"
      "ds_write_addtid_b32 v21 offset:45312\n"
      "ds_write_addtid_b32 v22 offset:49408\n"
      "v_mfma_f32_16x16x32_bf16 v[56:59], a[4:7], a[192:195], 0\n"
      "s_waitcnt lgkmcnt(6)\n"
      "ds_write_addtid_b32 v76 offset:0\n"
      "ds_write_addtid_b32 v77 offset:512\n"
      "ds_write_addtid_b32 v23 offset:53504\n"
      "v_sub_f32 v78, v90, v78\n"
      "v_mfma_f32_4x4x4_16b_bf16 v[28:31], a[216:217], a[224:225], 0\n"
      "v_add_f32 v88, v88, v64\n"
      "v_mfma_f32_4x4x4_16b_bf16 v[32:35], a[216:217], a[226:227], 0\n"
      "ds_write_addtid_b32 v78 offset:1024\n"
      "v_sub_f32 v79, v91, v79\n"
      "v_add_f32 v89, v89, v65\n"
      "v_sub_f32 v80, v92, v80\n"
      "v_mfma_f32_16x16x32_bf16 v[60:63], a[8:11], a[192:195], 0\n"
      "ds_write_addtid_b32 v79 offset:1536\n"
      "ds_write_addtid_b32 v80 offset:2048\n"
      "v_add_f32 v90, v90, v66\n"
      "v_mfma_f32_16x16x32_bf16 v[52:55], a[12:15], a[196:199], v[52:55]\n"
      "ds_read_b128 a[212:215], v4 offset:17024\n"
      "v_sub_f32 v81, v93, v81\n"
      "v_cvt_pk_bf16_f32 v28, v28, v32\n"
      "v_mfma_f32_16x16x32_bf16 v[56:59], a[16:19], a[196:199], v[56:59]\n"
      "ds_write_addtid_b32 v28 offset:41728\n"
      "ds_read_b64_tr_b16 a[232:233], v7 offset:18432\n"
      "ds_write_addtid_b32 v81 offset:2560\n"
      "ds_read_b64_tr_b16 a[234:235], v7 offset:18440\n"
      "v_mfma_f32_16x16x32_bf16 v[60:63], a[20:23], a[196:199], v[60:63]\n"
      "v_add_f32 v91, v91, v67\n"
      "v_sub_f32 v82, v94, v82\n"
      "v_add_f32_e64 v92, v92, v68\n"
      "v_mfma_f32_16x16x32_bf16 v[52:55], a[24:27], a[200:203], v[52:55]\n"
      "s_waitcnt lgkmcnt(5)\n"
      "ds_read_b128 a[192:195], v5 offset:16896\n"
      "ds_write_addtid_b32 v82 offset:3072\n"
      "v_cvt_pk_bf16_f32 v29, v29, v33\n"
      "v_mfma_f32_4x4x4_16b_bf16 v[20:23], a[216:217], a[228:229], 0\n"
      "v_sub_f32 v83, v95, v83\n"
      "v_add_f32_e64 v93, v93, v69\n"
      "v_mfma_f32_4x4x4_16b_bf16 v[24:27], a[216:217], a[230:231], 0\n"
      "ds_write_addtid_b32 v29 offset:45824\n"
      "ds_write_addtid_b32 v83 offset:3584\n"
      "v_sub_f32 v84, v96, v84\n"
      "v_mfma_f32_16x16x32_bf16 v[56:59], a[28:31], a[200:203], v[56:59]\n"
      "ds_write_addtid_b32 v84 offset:4096\n"
      "v_cvt_pk_bf16_f32 v30, v30, v34\n"
      "v_add_f32 v94, v94, v70\n"
      "v_sub_f32 v85, v97, v85\n"
      "v_mfma_f32_16x16x32_bf16 v[60:63], a[32:35], a[200:203], v[60:63]\n"
      "s_nop 0\n"
      "ds_write_addtid_b32 v30 offset:49920\n"
      "ds_write_addtid_b32 v85 offset:4608\n"
      "v_cvt_pk_bf16_f32 v20, v20, v24\n"
      "v_mfma_f32_16x16x32_bf16 v[52:55], a[36:39], a[204:207], v[52:55]\n"
      "ds_read_b128 a[196:199], v5 offset:16960\n"
      "ds_write_addtid_b32 v20 offset:42240\n"
      "v_cvt_pk_bf16_f32 v31, v31, v35\n"
      "v_mfma_f32_16x16x32_bf16 v[56:59], a[40:43], a[204:207], v[56:59]\n"
      "ds_write_addtid_b32 v31 offset:54016\n"
      "v_sub_f32 v86, v98, v86\n"
      "v_add_f32 v95, v95, v71\n"
      "v_mfma_f32_16x16x32_bf16 v[60:63], a[44:47], a[204:207], v[60:63]\n"
      "s_waitcnt lgkmcnt(5)\n"
      "ds_write_addtid_b32 v86 offset:5120\n"
      "v_sub_f32 v87, v99, v87\n"
      "v_cvt_pk_bf16_f32 v21, v21, v25\n"
      "v_add_f32_e64 v96, v96, v72\n"
      "v_mfma_f32_4x4x4_16b_bf16 v[28:31], a[216:217], a[232:233], 0\n"
      "v_cvt_pk_bf16_f32 v22, v22, v26\n"
      "v_add_f32 v97, v97, v73\n"
      "v_mfma_f32_4x4x4_16b_bf16 v[32:35], a[216:217], a[234:235], 0\n"
      "ds_write_addtid_b32 v21 offset:46336\n"
      "ds_write_addtid_b32 v22 offset:50432\n"
      "ds_write_addtid_b32 v87 offset:5632\n"
      "v_mfma_f32_16x16x32_bf16 v[52:55], a[48:51], a[208:211], v[52:55]\n"
      "s_nop 0\n"
      "ds_read_b128 a[200:203], v5 offset:17024\n"
      "v_cvt_pk_bf16_f32 v28, v28, v32\n"
      "v_cvt_pk_bf16_f32 v23, v23, v27\n"
      "v_mfma_f32_16x16x32_bf16 v[56:59], a[52:55], a[208:211], v[56:59]\n"
      "ds_write_addtid_b32 v28 offset:42752\n"
      "ds_write_addtid_b32 v23 offset:54528\n"
      "v_cvt_pk_bf16_f32 v29, v29, v33\n"
      "v_cvt_pk_bf16_f32 v30, v30, v34\n"
      "v_cvt_pk_bf16_f32 v31, v31, v35\n"
      "v_mfma_f32_16x16x32_bf16 v[60:63], a[56:59], a[208:211], v[60:63]\n"
      "ds_write_addtid_b32 v29 offset:46848\n"
      "ds_write_addtid_b32 v30 offset:50944\n"
      "v_mfma_f32_16x16x32_bf16 v[52:55], a[60:63], a[212:215], v[52:55]\n"
      "ds_write_addtid_b32 v31 offset:55040\n"
      "v_add_f32 v98, v98, v74\n"
      "v_add_f32 v99, v99, v75\n"
      "v_fma_f32 v40, 0.5, v76, -v88\n"
      "v_mfma_f32_16x16x32_bf16 v[56:59], a[64:67], a[212:215], v[56:59]\n"
      "v_fma_f32 v41, 0.5, v77, -v89\n"
      "v_fma_f32 v42, 0.5, v78, -v90\n"
      "v_fma_f32 v43, 0.5, v79, -v91\n"
      "v_mfma_f32_16x16x32_bf16 v[60:63], a[68:71], a[212:215], v[60:63]\n"
      "s_waitcnt lgkmcnt(0)\n"
      "v_fma_f32 v44, 0.5, v80, -v92\n"
      "v_fma_f32 v45, 0.5, v81, -v93\n"
      "s_barrier\n"
      "LSloop_%=:\n"
      "v_mfma_f32_16x16x32_bf16 v[52:55], a[72:75], a[192:195], v[52:55]\n"
      "ds_read_b128 a[204:207], v0 offset:16384\n"
      "ds_read2st64_b32 v[28:29], v6 offset0:0 offset1:2\n"
      "v_fma_f32 v46, 0.5, v82, -v94\n"
      "v_mfma_f32_16x16x32_bf16 v[56:59], a[76:79], a[192:195], v[56:59]\n"
      "ds_read_b128 a[208:211], v0 offset:16448\n"
      "ds_read2st64_b32 v[30:31], v6 offset0:4 offset1:6\n"
      "v_mfma_f32_16x16x32_bf16 v[60:63], a[80:83], a[192:195], v[60:63]\n"
      "ds_read_b64_tr_b16 a[220:221], v7 offset:33792\n"
      "v_fma_f32 v47, 0.5, v83, -v95\n"
      "v_mfma_f32_16x16x32_bf16 v[52:55], a[84:87], a[196:199], v[52:55]\n"
      "ds_read_b128 a[212:215], v0 offset:16512\n"
      "v_fma_f32 v48, 0.5, v84, -v96\n"
      "v_mfma_f32_16x16x32_bf16 v[56:59], a[88:91], a[196:199], v[56:59]\n"
      "ds_read_b64_tr_b16 a[222:223], v7 offset:33800\n"
      "ds_read2st64_b32 v[32:33], v6 offset0:8 offset1:10\n"
      "v_fma_f32 v49, 0.5, v85, -v97\n"
      "v_mfma_f32_16x16x32_bf16 v[60:63], a[92:95], a[196:199], v[60:63]\n"
      "ds_read2st64_b32 v[34:35], v6 offset0:12 offset1:14\n"
      "v_fma_f32 v50, 0.5, v86, -v98\n"
      "v_mfma_f32_16x16x32_bf16 v[52:55], a[96:99], a[200:203], v[52:55]\n"
      "ds_read_b128 a[192:195], v1 offset:16384\n"
      "ds_read2st64_b32 v[36:37], v6 offset0:16 offset1:18\n"
      "v_mfma_f32_16x16x32_bf16 v[56:59], a[100:103], a[200:203], v[56:59]\n"
      "ds_read2st64_b32 v[38:39], v6 offset0:20 offset1:22\n"
      "v_fma_f32 v51, 0.5, v87, -v99\n"
      "v_mfma_f32_16x16x32_bf16 v[60:63], a[104:107], a[200:203], v[60:63]\n"
      "s_waitcnt lgkmcnt(8)\n"
      "v_fmac_f32 v28, s46, v64\n"
      "v_fmac_f32 v29, s46, v65\n"
      "v_fmac_f32 v30, s46, v66\n"
      "v_mfma_f32_16x16x32_bf16 v[76:79], v[252:255], a[204:207], v[52:55]\n"
      "s_waitcnt lgkmcnt(5)\n"
      "ds_read_b128 a[196:199], v1 offset:16448\n"
      "v_fmac_f32 v31, s46, v67\n"
      "v_mfma_f32_4x4x4_16b_bf16 v[12:15], a[216:217], a[220:221], 0\n"
      "v_sub_f32 v28, v28, v40\n"
      "v_mfma_f32_4x4x4_16b_bf16 v[16:19], a[216:217], a[222:223], 0\n"
      "v_sub_f32 v29, v29, v41\n"
      "v_sub_f32 v30, v30, v42\n"
      "v_mfma_f32_16x16x32_bf16 v[80:83], v[244:247], a[204:207], v[56:59]\n"
      "v_sub_f32 v31, v31, v43\n"
      "v_fmac_f32 v40, s45, v28\n"
      "v_mfma_f32_16x16x32_bf16 v[84:87], v[236:239], a[204:207], v[60:63]\n"
      "v_fmac_f32 v41, s45, v29\n"
      "v_cvt_pk_bf16_f32 v12, v12, v16\n"
      "v_fmac_f32_e64 v42, s45, v30\n"
      "v_mfma_f32_16x16x32_bf16 v[76:79], v[228:231], a[208:211], v[76:79]\n"
      "ds_read_b128 a[200:203], v1 offset:16512\n"
      "ds_write_addtid_b32 v12 offset:24832\n"
      "v_mfma_f32_16x16x32_bf16 v[80:83], v[220:223], a[208:211], v[80:83]\n"
      "ds_read_b64_tr_b16 a[224:225], v7 offset:34304\n"
      "ds_read_b64_tr_b16 a[226:227], v7 offset:34312\n"
      "v_mfma_f32_16x16x32_bf16 v[84:87], v[212:215], a[208:211], v[84:87]\n"
      "v_fmac_f32_e64 v43, s45, v31\n"
      "v_cvt_pk_bf16_f32 v13, v13, v17\n"
      "v_mfma_f32_16x16x32_bf16 v[76:79], v[204:207], a[212:215], v[76:79]\n"
      "ds_read_b128 a[204:207], v2 offset:16384\n"
      "ds_write_addtid_b32 v13 offset:28928\n"
      "v_fma_f32 v28, s47, v28, v52\n"
      "v_mfma_f32_16x16x32_bf16 v[80:83], v[196:199], a[212:215], v[80:83]\n"
      "v_fma_f32 v29, s47, v29, v53\n"
      "v_cvt_pk_bf16_f32 v14, v14, v18\n"
      "v_mfma_f32_16x16x32_bf16 v[84:87], v[188:191], a[212:215], v[84:87]\n"
      "s_waitcnt lgkmcnt(5)\n"
      "ds_write_addtid_b32 v14 offset:33024\n"
      "v_fma_f32 v30, s47, v30, v54\n"
      "v_mfma_f32_16x16x32_bf16 v[76:79], v[180:183], a[192:195], v[76:79]\n"
      "s_nop 0\n"
      "ds_read_b128 a[208:211], v2 offset:16448\n"
      "v_fma_f32 v31, s47, v31, v55\n"
      "v_mfma_f32_16x16x32_bf16 v[80:83], v[172:175], a[192:195], v[80:83]\n"
      "s_nop 0\n"
      "v_cvt_pk_bf16_f32 v40, v40, v41\n"
      "v_cvt_pk_bf16_f32 v15, v15, v19\n"
      "v_mfma_f32_16x16x32_bf16 v[84:87], v[164:167], a[192:195], v[84:87]\n"
      "s_waitcnt lgkmcnt(4)\n"
      "ds_write_addtid_b32 v40 offset:12288\n"
      "ds_write_addtid_b32 v15 offset:37120\n"
      "v_cvt_pk_bf16_f32 v42, v42, v43\n"
      "v_mfma_f32_4x4x4_16b_bf16 v[20:23], a[216:217], a[224:225], 0\n"
      "v_cvt_pk_bf16_f32 v28, v28, v29\n"
      "v_mfma_f32_4x4x4_16b_bf16 v[24:27], a[216:217], a[226:227], 0\n"
      "ds_write_addtid_b32 v42 offset:12800\n"
      "ds_write_addtid_b32 v28 offset:14336\n"
      "v_mfma_f32_16x16x32_bf16 v[76:79], v[156:159], a[196:199], v[76:79]\n"
      "ds_read_b128 a[212:215], v2 offset:16512\n"
      "v_cvt_pk_bf16_f32 v30, v30, v31\n"
      "v_mfma_f32_16x16x32_bf16 v[80:83], v[148:151], a[196:199], v[80:83]\n"
      "ds_write_addtid_b32 v30 offset:14848\n"
      "v_fmac_f32 v32, s46, v68\n"
      "v_cvt_pk_bf16_f32 v20, v20, v24\n"
      "v_mfma_f32_16x16x32_bf16 v[84:87], v[140:143], a[196:199], v[84:87]\n"
      "ds_write_addtid_b32 v20 offset:25344\n"
      "v_fmac_f32 v33, s46, v69\n"
      "v_mfma_f32_16x16x32_bf16 v[76:79], v[132:135], a[200:203], v[76:79]\n"
      "ds_read_b128 a[192:195], v0 offset:20480\n"
      "ds_read_b64_tr_b16 a[228:229], v7 offset:34816\n"
      "v_mfma_f32_16x16x32_bf16 v[80:83], v[124:127], a[200:203], v[80:83]\n"
      "ds_read_b64_tr_b16 a[230:231], v7 offset:34824\n"
      "v_fmac_f32 v34, s46, v70\n"
      "v_mfma_f32_16x16x32_bf16 v[84:87], v[116:119], a[200:203], v[84:87]\n"
      "s_waitcnt lgkmcnt(4)\n"
      "v_fmac_f32 v35, s46, v71\n"
      "v_cvt_pk_bf16_f32 v21, v21, v25\n"
      "v_mfma_f32_16x16x32_bf16 v[76:79], v[108:111], a[204:207], v[76:79]\n"
      "s_nop 0\n"
      "ds_read_b128 a[196:199], v0 offset:20544\n"
      "ds_write_addtid_b32 v21 offset:29440\n"
      "v_sub_f32 v32, v32, v44\n"
      "v_mfma_f32_16x16x32_bf16 v[80:83], v[100:103], a[204:207], v[80:83]\n"
      "v_sub_f32 v33, v33, v45\n"
      "v_cvt_pk_bf16_f32 v22, v22, v26\n"
      "v_mfma_f32_16x16x32_bf16 v[84:87], a[160:163], a[204:207], v[84:87]\n"
      "ds_write_addtid_b32 v22 offset:33536\n"
      "v_sub_f32 v34, v34, v46\n"
      "v_mfma_f32_16x16x32_bf16 v[76:79], a[152:155], a[208:211], v[76:79]\n"
      "s_nop 0\n"
      "ds_read_b128 a[200:203], v0 offset:20608\n"
      "v_sub_f32 v35, v35, v47\n"
      "v_mfma_f32_16x16x32_bf16 v[80:83], a[144:147], a[208:211], v[80:83]\n"
      "s_waitcnt lgkmcnt(4)\n"
      "v_fmac_f32 v44, s45, v32\n"
      "v_cvt_pk_bf16_f32 v23, v23, v27\n"
      "v_mfma_f32_4x4x4_16b_bf16 v[12:15], a[216:217], a[228:229], 0\n"
      "v_fmac_f32 v45, s45, v33\n"
      "v_mfma_f32_4x4x4_16b_bf16 v[16:19], a[216:217], a[230:231], 0\n"
      "ds_write_addtid_b32 v23 offset:37632\n"
      "v_fmac_f32 v46, s45, v34\n"
      "v_mfma_f32_16x16x32_bf16 v[84:87], a[136:139], a[208:211], v[84:87]\n"
      "v_fmac_f32 v47, s45, v35\n"
      "v_fma_f32 v32, s47, v32, v56\n"
      "v_cvt_pk_bf16_f32 v12, v12, v16\n"
      "v_mfma_f32_16x16x32_bf16 v[76:79], a[128:131], a[212:215], v[76:79]\n"
      "ds_read_b128 a[204:207], v1 offset:20480\n"
      "ds_write_addtid_b32 v12 offset:25856\n"
      "v_mfma_f32_16x16x32_bf16 v[80:83], a[120:123], a[212:215], v[80:83]\n"
      "v_fma_f32 v33, s47, v33, v57\n"
      "v_cvt_pk_bf16_f32 v13, v13, v17\n"
      "v_mfma_f32_16x16x32_bf16 v[84:87], a[112:115], a[212:215], v[84:87]\n"
      "ds_read_b64_tr_b16 a[232:233], v7 offset:35328\n"
      "ds_write_addtid_b32 v13 offset:29952\n"
      "v_fma_f32 v34, s47, v34, v58\n"
      "v_mfma_f32_16x16x32_bf16 v[88:91], v[248:251], a[192:195], v[52:55]\n"
      "ds_read_b128 a[208:211], v1 offset:20544\n"
      "ds_read_b64_tr_b16 a[234:235], v7 offset:35336\n"
      "v_mfma_f32_16x16x32_bf16 v[92:95], v[240:243], a[192:195], v[56:59]\n"
      "s_nop 0\n"
      "v_fma_f32 v35, s47, v35, v59\n"
      "v_cvt_pk_bf16_f32 v44, v44, v45\n"
      "v_mfma_f32_16x16x32_bf16 v[96:99], v[232:235], a[192:195], v[60:63]\n"
      "s_waitcnt lgkmcnt(4)\n"
      "ds_write_addtid_b32 v44 offset:20484\n"
      "v_cvt_pk_bf16_f32 v14, v14, v18\n"
      "v_mfma_f32_16x16x32_bf16 v[88:91], v[224:227], a[196:199], v[88:91]\n"
      "ds_read_b128 a[212:215], v1 offset:20608\n"
      "ds_write_addtid_b32 v14 offset:34048\n"
      "v_cvt_pk_bf16_f32 v46, v46, v47\n"
      "v_mfma_f32_16x16x32_bf16 v[92:95], v[216:219], a[196:199], v[92:95]\n"
      "ds_write_addtid_b32 v46 offset:20996\n"
      "v_cvt_pk_bf16_f32 v32, v32, v33\n"
      "v_mfma_f32_16x16x32_bf16 v[96:99], v[208:211], a[196:199], v[96:99]\n"
      "ds_write_addtid_b32 v32 offset:22532\n"
      "v_cvt_pk_bf16_f32 v34, v34, v35\n"
      "v_mfma_f32_16x16x32_bf16 v[88:91], v[200:203], a[200:203], v[88:91]\n"
      "s_waitcnt lgkmcnt(4)\n"
      "ds_read_b128 a[192:195], v2 offset:20480\n"
      "ds_write_addtid_b32 v34 offset:23044\n"
      "v_fmac_f32 v36, s46, v72\n"
      "v_mfma_f32_4x4x4_16b_bf16 v[20:23], a[216:217], a[232:233], 0\n"
      "v_fmac_f32_e64 v37, s46, v73\n"
      "v_mfma_f32_4x4x4_16b_bf16 v[24:27], a[216:217], a[234:235], 0\n"
      "v_fmac_f32 v38, s46, v74\n"
      "v_cvt_pk_bf16_f32 v15, v15, v19\n"
      "v_mfma_f32_16x16x32_bf16 v[92:95], v[192:195], a[200:203], v[92:95]\n"
      "ds_write_addtid_b32 v15 offset:38144\n"
      "v_fmac_f32 v39, s46, v75\n"
      "v_cvt_pk_bf16_f32 v20, v20, v24\n"
      "v_mfma_f32_16x16x32_bf16 v[96:99], v[184:187], a[200:203], v[96:99]\n"
      "ds_write_addtid_b32 v20 offset:26368\n"
      "v_sub_f32 v36, v36, v48\n"
      "v_mfma_f32_16x16x32_bf16 v[88:91], v[176:179], a[204:207], v[88:91]\n"
      "ds_read_b128 a[196:199], v2 offset:20544\n"
      "v_sub_f32 v37, v37, v49\n"
      "v_mfma_f32_16x16x32_bf16 v[92:95], v[168:171], a[204:207], v[92:95]\n"
      "ds_read_b64_tr_b16 a[220:221], v7 offset:35840\n"
      "ds_read_b64_tr_b16 a[222:223], v7 offset:35848\n"
      "v_mfma_f32_16x16x32_bf16 v[96:99], v[160:163], a[204:207], v[96:99]\n"
      "v_sub_f32_e64 v38, v38, v50\n"
      "v_cvt_pk_bf16_f32 v21, v21, v25\n"
      "v_mfma_f32_16x16x32_bf16 v[88:91], v[152:155], a[208:211], v[88:91]\n"
      "ds_read_b128 a[200:203], v2 offset:20608\n"
      "ds_write_addtid_b32 v21 offset:30464\n"
      "v_sub_f32 v39, v39, v51\n"
      "v_mfma_f32_16x16x32_bf16 v[92:95], v[144:147], a[208:211], v[92:95]\n"
      "s_nop 0\n"
      "v_fmac_f32_e64 v48, s45, v36\n"
      "v_cvt_pk_bf16_f32 v22, v22, v26\n"
      "v_mfma_f32_16x16x32_bf16 v[96:99], v[136:139], a[208:211], v[96:99]\n"
      "s_waitcnt lgkmcnt(3)\n"
      "ds_write_addtid_b32 v22 offset:34560\n"
      "v_fmac_f32 v49, s45, v37\n"
      "v_mfma_f32_16x16x32_bf16 v[88:91], v[128:131], a[212:215], v[88:91]\n"
      "s_nop 0\n"
      "ds_read_b128 a[204:207], v3 offset:33792\n"
      "v_fmac_f32_e64 v50, s45, v38\n"
      "v_mfma_f32_16x16x32_bf16 v[92:95], v[120:123], a[212:215], v[92:95]\n"
      "s_nop 0\n"
      "v_fmac_f32 v51, s45, v39\n"
      "v_cvt_pk_bf16_f32 v23, v23, v27\n"
      "v_mfma_f32_16x16x32_bf16 v[96:99], v[112:115], a[212:215], v[96:99]\n"
      "s_waitcnt lgkmcnt(2)\n"
      "ds_write_addtid_b32 v23 offset:38656\n"
      "v_fma_f32 v36, s47, v36, v60\n"
      "v_mfma_f32_4x4x4_16b_bf16 v[12:15], a[216:217], a[220:221], 0\n"
      "v_fma_f32 v37, s47, v37, v61\n"
      "v_mfma_f32_4x4x4_16b_bf16 v[16:19], a[216:217], a[222:223], 0\n"
      "v_fma_f32 v38, s47, v38, v62\n"
      "v_fma_f32 v39, s47, v39, v63\n"
      "v_mfma_f32_16x16x32_bf16 v[88:91], v[104:107], a[192:195], v[88:91]\n"
      "ds_read_b128 a[208:211], v3 offset:33856\n"
      "v_cvt_pk_bf16_f32 v48, v48, v49\n"
      "v_cvt_pk_bf16_f32 v12, v12, v16\n"
      "v_mfma_f32_16x16x32_bf16 v[92:95], a[164:167], a[192:195], v[92:95]\n"
      "ds_write_addtid_b32 v48 offset:16384\n"
      "ds_write_addtid_b32 v12 offset:26880\n"
      "v_mfma_f32_16x16x32_bf16 v[96:99], a[156:159], a[192:195], v[96:99]\n"
      "v_cvt_pk_bf16_f32 v13, v13, v17\n"
      "v_cvt_pk_bf16_f32 v14, v14, v18\n"
      "v_mfma_f32_16x16x32_bf16 v[88:91], a[148:151], a[196:199], v[88:91]\n"
      "ds_read_b128 a[212:215], v3 offset:33920\n"
      "ds_write_addtid_b32 v13 offset:30976\n"
      "v_mfma_f32_16x16x32_bf16 v[92:95], a[140:143], a[196:199], v[92:95]\n"
      "ds_read_b64_tr_b16 a[224:225], v7 offset:36352\n"
      "ds_write_addtid_b32 v14 offset:35072\n"
      "v_cvt_pk_bf16_f32 v50, v50, v51\n"
      "v_mfma_f32_16x16x32_bf16 v[96:99], a[132:135], a[196:199], v[96:99]\n"
      "ds_write_addtid_b32 v50 offset:16896\n"
      "ds_read_b64_tr_b16 a[226:227], v7 offset:36360\n"
      "v_mfma_f32_16x16x32_bf16 v[88:91], a[124:127], a[200:203], v[88:91]\n"
      "ds_read_b128 a[192:195], v4 offset:33792\n"
      "v_cvt_pk_bf16_f32 v36, v36, v37\n"
      "v_mfma_f32_16x16x32_bf16 v[92:95], a[116:119], a[200:203], v[92:95]\n"
      "ds_write_addtid_b32 v36 offset:18432\n"
      "v_cvt_pk_bf16_f32 v15, v15, v19\n"
      "v_mfma_f32_16x16x32_bf16 v[96:99], a[108:111], a[200:203], v[96:99]\n"
      "s_waitcnt lgkmcnt(6)\n"
      "ds_write_addtid_b32 v15 offset:39168\n"
      "v_sub_f32 v76, v88, v76\n"
      "v_sub_f32_e64 v77, v89, v77\n"
      "v_cvt_pk_bf16_f32 v38, v38, v39\n"
      "v_mfma_f32_16x16x32_bf16 v[64:67], a[0:3], a[204:207], 0\n"
      "s_nop 0\n"
      "ds_read_b128 a[196:199], v4 offset:33856\n"
      "ds_write_addtid_b32 v38 offset:18944\n"
      "v_mfma_f32_16x16x32_bf16 v[68:71], a[4:7], a[204:207], 0\n"
      "s_waitcnt lgkmcnt(5)\n"
      "ds_write_addtid_b32 v76 offset:6144\n"
      "ds_write_addtid_b32 v77 offset:6656\n"
      "v_mfma_f32_4x4x4_16b_bf16 v[20:23], a[216:217], a[224:225], 0\n"
      "v_sub_f32 v78, v90, v78\n"
      "v_mfma_f32_4x4x4_16b_bf16 v[24:27], a[216:217], a[226:227], 0\n"
      "ds_write_addtid_b32 v78 offset:7168\n"
      "v_sub_f32 v79, v91, v79\n"
      "v_mfma_f32_16x16x32_bf16 v[72:75], a[8:11], a[204:207], 0\n"
      "ds_write_addtid_b32 v79 offset:7680\n"
      "v_sub_f32 v80, v92, v80\n"
      "v_sub_f32 v81, v93, v81\n"
      "v_mfma_f32_16x16x32_bf16 v[64:67], a[12:15], a[208:211], v[64:67]\n"
      "ds_read_b128 a[200:203], v4 offset:33920\n"
      "ds_write_addtid_b32 v80 offset:8192\n"
      "v_mfma_f32_16x16x32_bf16 v[68:71], a[16:19], a[208:211], v[68:71]\n"
      "ds_write_addtid_b32 v81 offset:8704\n"
      "v_sub_f32 v82, v94, v82\n"
      "v_mfma_f32_16x16x32_bf16 v[72:75], a[20:23], a[208:211], v[72:75]\n"
      "s_nop 0\n"
      "ds_write_addtid_b32 v82 offset:9216\n"
      "v_cvt_pk_bf16_f32 v20, v20, v24\n"
      "v_mfma_f32_16x16x32_bf16 v[64:67], a[24:27], a[212:215], v[64:67]\n"
      "ds_read_b128 a[204:207], v5 offset:33792\n"
      "ds_write_addtid_b32 v20 offset:27392\n"
      "v_sub_f32 v83, v95, v83\n"
      "v_mfma_f32_16x16x32_bf16 v[68:71], a[28:31], a[212:215], v[68:71]\n"
      "ds_write_addtid_b32 v83 offset:9728\n"
      "v_sub_f32_e64 v84, v96, v84\n"
      "v_mfma_f32_16x16x32_bf16 v[72:75], a[32:35], a[212:215], v[72:75]\n"
      "s_waitcnt lgkmcnt(5)\n"
      "ds_write_addtid_b32 v84 offset:10240\n"
      "v_cvt_pk_bf16_f32 v21, v21, v25\n"
      "v_mfma_f32_16x16x32_bf16 v[64:67], a[36:39], a[192:195], v[64:67]\n"
      "ds_read_b128 a[208:211], v5 offset:33856\n"
      "ds_write_addtid_b32 v21 offset:31488\n"
      "v_mfma_f32_16x16x32_bf16 v[68:71], a[40:43], a[192:195], v[68:71]\n"
      "v_sub_f32 v85, v97, v85\n"
      "v_sub_f32 v86, v98, v86\n"
      "v_cvt_pk_bf16_f32 v22, v22, v26\n"
      "v_mfma_f32_16x16x32_bf16 v[72:75], a[44:47], a[192:195], v[72:75]\n"
      "ds_write_addtid_b32 v22 offset:35584\n"
      "ds_write_addtid_b32 v85 offset:10752\n"
      "v_sub_f32 v87, v99, v87\n"
      "v_mfma_f32_16x16x32_bf16 v[64:67], a[48:51], a[196:199], v[64:67]\n"
      "ds_read_b128 a[212:215], v5 offset:33920\n"
      "ds_write_addtid_b32 v86 offset:11264\n"
      "v_mfma_f32_16x16x32_bf16 v[68:71], a[52:55], a[196:199], v[68:71]\n"
      "s_nop 0\n"
      "ds_write_addtid_b32 v87 offset:11776\n"
      "v_cvt_pk_bf16_f32 v23, v23, v27\n"
      "v_mfma_f32_16x16x32_bf16 v[72:75], a[56:59], a[196:199], v[72:75]\n"
      "ds_write_addtid_b32 v23 offset:39680\n"
      "v_fma_f32 v40, 0.5, v76, -v88\n"
      "v_mfma_f32_16x16x32_bf16 v[64:67], a[60:63], a[200:203], v[64:67]\n"
      "v_fma_f32 v41, 0.5, v77, -v89\n"
      "v_fma_f32 v42, 0.5, v78, -v90\n"
      "v_mfma_f32_16x16x32_bf16 v[68:71], a[64:67], a[200:203], v[68:71]\n"
      "v_fma_f32 v43, 0.5, v79, -v91\n"
      "v_fma_f32 v44, 0.5, v80, -v92\n"
      "v_mfma_f32_16x16x32_bf16 v[72:75], a[68:71], a[200:203], v[72:75]\n"
      "s_waitcnt lgkmcnt(0)\n"
      "v_fma_f32 v45, 0.5, v81, -v93\n"
      "s_barrier\n"
      "LSmid_%=:\n"
      "v_mfma_f32_16x16x32_bf16 v[64:67], a[72:75], a[204:207], v[64:67]\n"
      "ds_read_b128 a[192:195], v0 offset:0\n"
      "ds_read2st64_b32 v[28:29], v6 offset0:24 offset1:26\n"
      "v_fma_f32 v46, 0.5, v82, -v94\n"
      "v_mfma_f32_16x16x32_bf16 v[68:71], a[76:79], a[204:207], v[68:71]\n"
      "ds_read_b128 a[196:199], v0 offset:64\n"
      "ds_read2st64_b32 v[30:31], v6 offset0:28 offset1:30\n"
      "v_mfma_f32_16x16x32_bf16 v[72:75], a[80:83], a[204:207], v[72:75]\n"
      "ds_read_b64_tr_b16 a[220:221], v7 offset:50688\n"
      "v_fma_f32 v47, 0.5, v83, -v95\n"
      "v_mfma_f32_16x16x32_bf16 v[64:67], a[84:87], a[208:211], v[64:67]\n"
      "ds_read_b128 a[200:203], v0 offset:128\n"
      "v_fma_f32 v48, 0.5, v84, -v96\n"
      "v_mfma_f32_16x16x32_bf16 v[68:71], a[88:91], a[208:211], v[68:71]\n"
      "ds_read_b64_tr_b16 a[222:223], v7 offset:50696\n"
      "ds_read2st64_b32 v[32:33], v6 offset0:32 offset1:34\n"
      "v_fma_f32 v49, 0.5, v85, -v97\n"
      "v_mfma_f32_16x16x32_bf16 v[72:75], a[92:95], a[208:211], v[72:75]\n"
      "ds_read2st64_b32 v[34:35], v6 offset0:36 offset1:38\n"
      "v_fma_f32 v50, 0.5, v86, -v98\n"
      "v_mfma_f32_16x16x32_bf16 v[64:67], a[96:99], a[212:215], v[64:67]\n"
      "ds_read_b128 a[204:207], v1 offset:0\n"
      "ds_read2st64_b32 v[36:37], v6 offset0:40 offset1:42\n"
      "v_mfma_f32_16x16x32_bf16 v[68:71], a[100:103], a[212:215], v[68:71]\n"
      "ds_read2st64_b32 v[38:39], v6 offset0:44 offset1:46\n"
      "v_fma_f32 v51, 0.5, v87, -v99\n"
      "v_mfma_f32_16x16x32_bf16 v[72:75], a[104:107], a[212:215], v[72:75]\n"
      "s_waitcnt lgkmcnt(8)\n"
      "v_fmac_f32 v28, s46, v52\n"
      "v_fmac_f32 v29, s46, v53\n"
      "v_fmac_f32 v30, s46, v54\n"
      "v_mfma_f32_16x16x32_bf16 v[76:79], v[252:255], a[192:195], v[64:67]\n"
      "s_waitcnt lgkmcnt(5)\n"
      "ds_read_b128 a[208:211], v1 offset:64\n"
      "v_fmac_f32 v31, s46, v55\n"
      "v_mfma_f32_4x4x4_16b_bf16 v[12:15], a[216:217], a[220:221], 0\n"
      "v_sub_f32_e64 v28, v28, v40\n"
      "v_mfma_f32_4x4x4_16b_bf16 v[16:19], a[216:217], a[222:223], 0\n"
      "v_sub_f32 v29, v29, v41\n"
      "v_sub_f32 v30, v30, v42\n"
      "v_mfma_f32_16x16x32_bf16 v[80:83], v[244:247], a[192:195], v[68:71]\n"
      "v_sub_f32 v31, v31, v43\n"
      "v_fmac_f32 v40, s45, v28\n"
      "v_mfma_f32_16x16x32_bf16 v[84:87], v[236:239], a[192:195], v[72:75]\n"
      "v_fmac_f32 v41, s45, v29\n"
      "v_cvt_pk_bf16_f32 v12, v12, v16\n"
      "v_fmac_f32 v42, s45, v30\n"
      "v_mfma_f32_16x16x32_bf16 v[76:79], v[228:231], a[196:199], v[76:79]\n"
      "ds_read_b128 a[212:215], v1 offset:128\n"
      "ds_write_addtid_b32 v12 offset:41216\n"
      "v_mfma_f32_16x16x32_bf16 v[80:83], v[220:223], a[196:199], v[80:83]\n"
      "ds_read_b64_tr_b16 a[224:225], v7 offset:51200\n"
      "ds_read_b64_tr_b16 a[226:227], v7 offset:51208\n"
      "v_mfma_f32_16x16x32_bf16 v[84:87], v[212:215], a[196:199], v[84:87]\n"
      "v_fmac_f32_e64 v43, s45, v31\n"
      "v_cvt_pk_bf16_f32 v13, v13, v17\n"
      "v_mfma_f32_16x16x32_bf16 v[76:79], v[204:207], a[200:203], v[76:79]\n"
      "s_nop 0\n"
      "ds_read_b128 a[192:195], v2 offset:0\n"
      "ds_write_addtid_b32 v13 offset:45312\n"
      "v_fma_f32 v28, s47, v28, v64\n"
      "v_mfma_f32_16x16x32_bf16 v[80:83], v[196:199], a[200:203], v[80:83]\n"
      "s_nop 0\n"
      "v_fma_f32 v29, s47, v29, v65\n"
      "v_cvt_pk_bf16_f32 v14, v14, v18\n"
      "v_mfma_f32_16x16x32_bf16 v[84:87], v[188:191], a[200:203], v[84:87]\n"
      "s_waitcnt lgkmcnt(5)\n"
      "ds_write_addtid_b32 v14 offset:49408\n"
      "v_fma_f32 v30, s47, v30, v66\n"
      "v_mfma_f32_16x16x32_bf16 v[76:79], v[180:183], a[204:207], v[76:79]\n"
      "s_nop 0\n"
      "ds_read_b128 a[196:199], v2 offset:64\n"
      "v_fma_f32 v31, s47, v31, v67\n"
      "v_mfma_f32_16x16x32_bf16 v[80:83], v[172:175], a[204:207], v[80:83]\n"
      "s_nop 0\n"
      "v_cvt_pk_bf16_f32 v40, v40, v41\n"
      "v_cvt_pk_bf16_f32 v15, v15, v19\n"
      "v_mfma_f32_16x16x32_bf16 v[84:87], v[164:167], a[204:207], v[84:87]\n"
      "s_waitcnt lgkmcnt(4)\n"
      "ds_write_addtid_b32 v40 offset:13312\n"
      "ds_write_addtid_b32 v15 offset:53504\n"
      "v_cvt_pk_bf16_f32 v42, v42, v43\n"
      "v_mfma_f32_4x4x4_16b_bf16 v[20:23], a[216:217], a[224:225], 0\n"
      "v_cvt_pk_bf16_f32 v28, v28, v29\n"
      "v_mfma_f32_4x4x4_16b_bf16 v[24:27], a[216:217], a[226:227], 0\n"
      "ds_write_addtid_b32 v42 offset:13824\n"
      "ds_write_addtid_b32 v28 offset:15360\n"
      "v_mfma_f32_16x16x32_bf16 v[76:79], v[156:159], a[208:211], v[76:79]\n"
      "ds_read_b128 a[200:203], v2 offset:128\n"
      "v_cvt_pk_bf16_f32 v30, v30, v31\n"
      "v_mfma_f32_16x16x32_bf16 v[80:83], v[148:151], a[208:211], v[80:83]\n"
      "ds_write_addtid_b32 v30 offset:15872\n"
      "v_fmac_f32 v32, s46, v56\n"
      "v_cvt_pk_bf16_f32 v20, v20, v24\n"
      "v_mfma_f32_16x16x32_bf16 v[84:87], v[140:143], a[208:211], v[84:87]\n"
      "ds_write_addtid_b32 v20 offset:41728\n"
      "v_fmac_f32 v33, s46, v57\n"
      "v_mfma_f32_16x16x32_bf16 v[76:79], v[132:135], a[212:215], v[76:79]\n"
      "ds_read_b128 a[204:207], v0 offset:4096\n"
      "ds_read_b64_tr_b16 a[228:229], v7 offset:51712\n"
      "v_mfma_f32_16x16x32_bf16 v[80:83], v[124:127], a[212:215], v[80:83]\n"
      "ds_read_b64_tr_b16 a[230:231], v7 offset:51720\n"
      "v_fmac_f32 v34, s46, v58\n"
      "v_mfma_f32_16x16x32_bf16 v[84:87], v[116:119], a[212:215], v[84:87]\n"
      "s_waitcnt lgkmcnt(4)\n"
      "v_fmac_f32 v35, s46, v59\n"
      "v_cvt_pk_bf16_f32 v21, v21, v25\n"
      "v_mfma_f32_16x16x32_bf16 v[76:79], v[108:111], a[192:195], v[76:79]\n"
      "s_nop 0\n"
      "ds_read_b128 a[208:211], v0 offset:4160\n"
      "ds_write_addtid_b32 v21 offset:45824\n"
      "v_sub_f32 v32, v32, v44\n"
      "v_mfma_f32_16x16x32_bf16 v[80:83], v[100:103], a[192:195], v[80:83]\n"
      "v_sub_f32 v33, v33, v45\n"
      "v_cvt_pk_bf16_f32 v22, v22, v26\n"
      "v_mfma_f32_16x16x32_bf16 v[84:87], a[160:163], a[192:195], v[84:87]\n"
      "ds_write_addtid_b32 v22 offset:49920\n"
      "v_sub_f32 v34, v34, v46\n"
      "v_mfma_f32_16x16x32_bf16 v[76:79], a[152:155], a[196:199], v[76:79]\n"
      "s_nop 0\n"
      "ds_read_b128 a[212:215], v0 offset:4224\n"
      "v_sub_f32 v35, v35, v47\n"
      "v_mfma_f32_16x16x32_bf16 v[80:83], a[144:147], a[196:199], v[80:83]\n"
      "s_waitcnt lgkmcnt(4)\n"
      "v_fmac_f32 v44, s45, v32\n"
      "v_cvt_pk_bf16_f32 v23, v23, v27\n"
      "v_mfma_f32_4x4x4_16b_bf16 v[12:15], a[216:217], a[228:229], 0\n"
      "v_fmac_f32 v45, s45, v33\n"
      "v_mfma_f32_4x4x4_16b_bf16 v[16:19], a[216:217], a[230:231], 0\n"
      "ds_write_addtid_b32 v23 offset:54016\n"
      "v_fmac_f32 v46, s45, v34\n"
      "v_mfma_f32_16x16x32_bf16 v[84:87], a[136:139], a[196:199], v[84:87]\n"
      "v_fmac_f32 v47, s45, v35\n"
      "v_fma_f32 v32, s47, v32, v68\n"
      "v_cvt_pk_bf16_f32 v12, v12, v16\n"
      "v_mfma_f32_16x16x32_bf16 v[76:79], a[128:131], a[200:203], v[76:79]\n"
      "ds_read_b128 a[192:195], v1 offset:4096\n"
      "ds_write_addtid_b32 v12 offset:42240\n"
      "v_mfma_f32_16x16x32_bf16 v[80:83], a[120:123], a[200:203], v[80:83]\n"
      "v_fma_f32 v33, s47, v33, v69\n"
      "v_cvt_pk_bf16_f32 v13, v13, v17\n"
      "v_mfma_f32_16x16x32_bf16 v[84:87], a[112:115], a[200:203], v[84:87]\n"
      "ds_read_b64_tr_b16 a[232:233], v7 offset:52224\n"
      "ds_write_addtid_b32 v13 offset:46336\n"
      "v_fma_f32 v34, s47, v34, v70\n"
      "v_mfma_f32_16x16x32_bf16 v[88:91], v[248:251], a[204:207], v[64:67]\n"
      "ds_read_b128 a[196:199], v1 offset:4160\n"
      "ds_read_b64_tr_b16 a[234:235], v7 offset:52232\n"
      "v_mfma_f32_16x16x32_bf16 v[92:95], v[240:243], a[204:207], v[68:71]\n"
      "s_nop 0\n"
      "v_fma_f32 v35, s47, v35, v71\n"
      "v_cvt_pk_bf16_f32 v44, v44, v45\n"
      "v_mfma_f32_16x16x32_bf16 v[96:99], v[232:235], a[204:207], v[72:75]\n"
      "s_waitcnt lgkmcnt(4)\n"
      "ds_write_addtid_b32 v44 offset:21508\n"
      "v_cvt_pk_bf16_f32 v14, v14, v18\n"
      "v_mfma_f32_16x16x32_bf16 v[88:91], v[224:227], a[208:211], v[88:91]\n"
      "ds_read_b128 a[200:203], v1 offset:4224\n"
      "ds_write_addtid_b32 v14 offset:50432\n"
      "v_cvt_pk_bf16_f32 v46, v46, v47\n"
      "v_mfma_f32_16x16x32_bf16 v[92:95], v[216:219], a[208:211], v[92:95]\n"
      "ds_write_addtid_b32 v46 offset:22020\n"
      "v_cvt_pk_bf16_f32 v32, v32, v33\n"
      "v_mfma_f32_16x16x32_bf16 v[96:99], v[208:211], a[208:211], v[96:99]\n"
      "ds_write_addtid_b32 v32 offset:23556\n"
      "v_cvt_pk_bf16_f32 v34, v34, v35\n"
      "v_mfma_f32_16x16x32_bf16 v[88:91], v[200:203], a[212:215], v[88:91]\n"
      "s_waitcnt lgkmcnt(4)\n"
      "ds_read_b128 a[204:207], v2 offset:4096\n"
      "ds_write_addtid_b32 v34 offset:24068\n"
      "v_fmac_f32 v36, s46, v60\n"
      "v_mfma_f32_4x4x4_16b_bf16 v[20:23], a[216:217], a[232:233], 0\n"
      "v_fmac_f32_e64 v37, s46, v61\n"
      "v_mfma_f32_4x4x4_16b_bf16 v[24:27], a[216:217], a[234:235], 0\n"
      "v_fmac_f32 v38, s46, v62\n"
      "v_cvt_pk_bf16_f32 v15, v15, v19\n"
      "v_mfma_f32_16x16x32_bf16 v[92:95], v[192:195], a[212:215], v[92:95]\n"
      "ds_write_addtid_b32 v15 offset:54528\n"
      "v_fmac_f32 v39, s46, v63\n"
      "v_cvt_pk_bf16_f32 v20, v20, v24\n"
      "v_mfma_f32_16x16x32_bf16 v[96:99], v[184:187], a[212:215], v[96:99]\n"
      "ds_write_addtid_b32 v20 offset:42752\n"
      "v_sub_f32 v36, v36, v48\n"
      "v_mfma_f32_16x16x32_bf16 v[88:91], v[176:179], a[192:195], v[88:91]\n"
      "ds_read_b128 a[208:211], v2 offset:4160\n"
      "v_sub_f32 v37, v37, v49\n"
      "v_mfma_f32_16x16x32_bf16 v[92:95], v[168:171], a[192:195], v[92:95]\n"
      "ds_read_b64_tr_b16 a[220:221], v7 offset:52736\n"
      "ds_read_b64_tr_b16 a[222:223], v7 offset:52744\n"
      "v_mfma_f32_16x16x32_bf16 v[96:99], v[160:163], a[192:195], v[96:99]\n"
      "v_sub_f32_e64 v38, v38, v50\n"
      "v_cvt_pk_bf16_f32 v21, v21, v25\n"
      "v_mfma_f32_16x16x32_bf16 v[88:91], v[152:155], a[196:199], v[88:91]\n"
      "ds_read_b128 a[212:215], v2 offset:4224\n"
      "ds_write_addtid_b32 v21 offset:46848\n"
      "v_sub_f32 v39, v39, v51\n"
      "v_mfma_f32_16x16x32_bf16 v[92:95], v[144:147], a[196:199], v[92:95]\n"
      "s_nop 0\n"
      "v_fmac_f32_e64 v48, s45, v36\n"
      "v_cvt_pk_bf16_f32 v22, v22, v26\n"
      "v_mfma_f32_16x16x32_bf16 v[96:99], v[136:139], a[196:199], v[96:99]\n"
      "s_waitcnt lgkmcnt(3)\n"
      "ds_write_addtid_b32 v22 offset:50944\n"
      "v_fmac_f32 v49, s45, v37\n"
      "v_mfma_f32_16x16x32_bf16 v[88:91], v[128:131], a[200:203], v[88:91]\n"
      "s_nop 0\n"
      "ds_read_b128 a[192:195], v3 offset:50688\n"
      "v_fmac_f32_e64 v50, s45, v38\n"
      "v_mfma_f32_16x16x32_bf16 v[92:95], v[120:123], a[200:203], v[92:95]\n"
      "s_nop 0\n"
      "v_fmac_f32 v51, s45, v39\n"
      "v_cvt_pk_bf16_f32 v23, v23, v27\n"
      "v_mfma_f32_16x16x32_bf16 v[96:99], v[112:115], a[200:203], v[96:99]\n"
      "s_waitcnt lgkmcnt(2)\n"
      "ds_write_addtid_b32 v23 offset:55040\n"
      "v_fma_f32 v36, s47, v36, v72\n"
      "v_mfma_f32_4x4x4_16b_bf16 v[12:15], a[216:217], a[220:221], 0\n"
      "v_fma_f32 v37, s47, v37, v73\n"
      "v_mfma_f32_4x4x4_16b_bf16 v[16:19], a[216:217], a[222:223], 0\n"
      "v_fma_f32 v38, s47, v38, v74\n"
      "v_fma_f32 v39, s47, v39, v75\n"
      "v_mfma_f32_16x16x32_bf16 v[88:91], v[104:107], a[204:207], v[88:91]\n"
      "ds_read_b128 a[196:199], v3 offset:50752\n"
      "v_cvt_pk_bf16_f32 v48, v48, v49\n"
      "v_cvt_pk_bf16_f32 v12, v12, v16\n"
      "v_mfma_f32_16x16x32_bf16 v[92:95], a[164:167], a[204:207], v[92:95]\n"
      "ds_write_addtid_b32 v48 offset:17408\n"
      "ds_write_addtid_b32 v12 offset:43264\n"
      "v_mfma_f32_16x16x32_bf16 v[96:99], a[156:159], a[204:207], v[96:99]\n"
      "v_cvt_pk_bf16_f32 v13, v13, v17\n"
      "v_cvt_pk_bf16_f32 v14, v14, v18\n"
      "v_mfma_f32_16x16x32_bf16 v[88:91], a[148:151], a[208:211], v[88:91]\n"
      "ds_read_b128 a[200:203], v3 offset:50816\n"
      "ds_write_addtid_b32 v13 offset:47360\n"
      "v_mfma_f32_16x16x32_bf16 v[92:95], a[140:143], a[208:211], v[92:95]\n"
      "ds_read_b64_tr_b16 a[224:225], v7 offset:53248\n"
      "ds_write_addtid_b32 v14 offset:51456\n"
      "v_cvt_pk_bf16_f32 v50, v50, v51\n"
      "v_mfma_f32_16x16x32_bf16 v[96:99], a[132:135], a[208:211], v[96:99]\n"
      "ds_write_addtid_b32 v50 offset:17920\n"
      "ds_read_b64_tr_b16 a[226:227], v7 offset:53256\n"
      "v_mfma_f32_16x16x32_bf16 v[88:91], a[124:127], a[212:215], v[88:91]\n"
      "ds_read_b128 a[204:207], v4 offset:50688\n"
      "v_cvt_pk_bf16_f32 v36, v36, v37\n"
      "v_mfma_f32_16x16x32_bf16 v[92:95], a[116:119], a[212:215], v[92:95]\n"
      "ds_write_addtid_b32 v36 offset:19456\n"
      "v_cvt_pk_bf16_f32 v15, v15, v19\n"
      "v_mfma_f32_16x16x32_bf16 v[96:99], a[108:111], a[212:215], v[96:99]\n"
      "s_waitcnt lgkmcnt(6)\n"
      "ds_write_addtid_b32 v15 offset:55552\n"
      "v_sub_f32 v76, v88, v76\n"
      "v_sub_f32_e64 v77, v89, v77\n"
      "v_cvt_pk_bf16_f32 v38, v38, v39\n"
      "v_mfma_f32_16x16x32_bf16 v[52:55], a[0:3], a[192:195], 0\n"
      "s_nop 0\n"
      "ds_read_b128 a[208:211], v4 offset:50752\n"
      "ds_write_addtid_b32 v38 offset:19968\n"
      "v_mfma_f32_16x16x32_bf16 v[56:59], a[4:7], a[192:195], 0\n"
      "s_waitcnt lgkmcnt(5)\n"
      "ds_write_addtid_b32 v76 offset:0\n"
      "ds_write_addtid_b32 v77 offset:512\n"
      "v_mfma_f32_4x4x4_16b_bf16 v[20:23], a[216:217], a[224:225], 0\n"
      "v_sub_f32 v78, v90, v78\n"
      "v_mfma_f32_4x4x4_16b_bf16 v[24:27], a[216:217], a[226:227], 0\n"
      "ds_write_addtid_b32 v78 offset:1024\n"
      "v_sub_f32 v79, v91, v79\n"
      "v_mfma_f32_16x16x32_bf16 v[60:63], a[8:11], a[192:195], 0\n"
      "ds_write_addtid_b32 v79 offset:1536\n"
      "v_sub_f32 v80, v92, v80\n"
      "v_sub_f32 v81, v93, v81\n"
      "v_mfma_f32_16x16x32_bf16 v[52:55], a[12:15], a[196:199], v[52:55]\n"
      "ds_read_b128 a[212:215], v4 offset:50816\n"
      "ds_write_addtid_b32 v80 offset:2048\n"
      "v_mfma_f32_16x16x32_bf16 v[56:59], a[16:19], a[196:199], v[56:59]\n"
      "ds_write_addtid_b32 v81 offset:2560\n"
      "v_sub_f32 v82, v94, v82\n"
      "v_mfma_f32_16x16x32_bf16 v[60:63], a[20:23], a[196:199], v[60:63]\n"
      "s_nop 0\n"
      "ds_write_addtid_b32 v82 offset:3072\n"
      "v_cvt_pk_bf16_f32 v20, v20, v24\n"
      "v_mfma_f32_16x16x32_bf16 v[52:55], a[24:27], a[200:203], v[52:55]\n"
      "ds_read_b128 a[192:195], v5 offset:50688\n"
      "ds_write_addtid_b32 v20 offset:43776\n"
      "v_sub_f32 v83, v95, v83\n"
      "v_mfma_f32_16x16x32_bf16 v[56:59], a[28:31], a[200:203], v[56:59]\n"
      "ds_write_addtid_b32 v83 offset:3584\n"
      "v_sub_f32_e64 v84, v96, v84\n"
      "v_mfma_f32_16x16x32_bf16 v[60:63], a[32:35], a[200:203], v[60:63]\n"
      "s_waitcnt lgkmcnt(5)\n"
      "ds_write_addtid_b32 v84 offset:4096\n"
      "v_cvt_pk_bf16_f32 v21, v21, v25\n"
      "v_mfma_f32_16x16x32_bf16 v[52:55], a[36:39], a[204:207], v[52:55]\n"
      "ds_read_b128 a[196:199], v5 offset:50752\n"
      "ds_write_addtid_b32 v21 offset:47872\n"
      "v_mfma_f32_16x16x32_bf16 v[56:59], a[40:43], a[204:207], v[56:59]\n"
      "v_sub_f32 v85, v97, v85\n"
      "v_sub_f32 v86, v98, v86\n"
      "v_cvt_pk_bf16_f32 v22, v22, v26\n"
      "v_mfma_f32_16x16x32_bf16 v[60:63], a[44:47], a[204:207], v[60:63]\n"
      "ds_write_addtid_b32 v22 offset:51968\n"
      "ds_write_addtid_b32 v85 offset:4608\n"
      "v_sub_f32 v87, v99, v87\n"
      "v_mfma_f32_16x16x32_bf16 v[52:55], a[48:51], a[208:211], v[52:55]\n"
      "ds_read_b128 a[200:203], v5 offset:50816\n"
      "ds_write_addtid_b32 v86 offset:5120\n"
      "v_mfma_f32_16x16x32_bf16 v[56:59], a[52:55], a[208:211], v[56:59]\n"
      "s_nop 0\n"
      "ds_write_addtid_b32 v87 offset:5632\n"
      "v_cvt_pk_bf16_f32 v23, v23, v27\n"
      "v_mfma_f32_16x16x32_bf16 v[60:63], a[56:59], a[208:211], v[60:63]\n"
      "ds_write_addtid_b32 v23 offset:56064\n"
      "v_fma_f32 v40, 0.5, v76, -v88\n"
      "v_mfma_f32_16x16x32_bf16 v[52:55], a[60:63], a[212:215], v[52:55]\n"
      "v_fma_f32 v41, 0.5, v77, -v89\n"
      "v_fma_f32 v42, 0.5, v78, -v90\n"
      "v_mfma_f32_16x16x32_bf16 v[56:59], a[64:67], a[212:215], v[56:59]\n"
      "v_fma_f32 v43, 0.5, v79, -v91\n"
      "v_fma_f32 v44, 0.5, v80, -v92\n"
      "v_mfma_f32_16x16x32_bf16 v[60:63], a[68:71], a[212:215], v[60:63]\n"
      "s_waitcnt lgkmcnt(0)\n"
      "v_fma_f32 v45, 0.5, v81, -v93\n"
      "s_barrier\n"
      "v_mfma_f32_16x16x32_bf16 v[52:55], a[72:75], a[192:195], v[52:55]\n"
      "ds_read_b128 a[204:207], v0 offset:16384\n"
      "ds_read2st64_b32 v[28:29], v6 offset0:0 offset1:2\n"
      "v_fma_f32 v46, 0.5, v82, -v94\n"
      "v_mfma_f32_16x16x32_bf16 v[56:59], a[76:79], a[192:195], v[56:59]\n"
      "ds_read_b128 a[208:211], v0 offset:16448\n"
      "ds_read2st64_b32 v[30:31], v6 offset0:4 offset1:6\n"
      "v_mfma_f32_16x16x32_bf16 v[60:63], a[80:83], a[192:195], v[60:63]\n"
      "ds_read_b64_tr_b16 a[220:221], v7 offset:0\n"
      "v_fma_f32 v47, 0.5, v83, -v95\n"
      "v_mfma_f32_16x16x32_bf16 v[52:55], a[84:87], a[196:199], v[52:55]\n"
      "ds_read_b128 a[212:215], v0 offset:16512\n"
      "v_fma_f32 v48, 0.5, v84, -v96\n"
      "v_mfma_f32_16x16x32_bf16 v[56:59], a[88:91], a[196:199], v[56:59]\n"
      "ds_read_b64_tr_b16 a[222:223], v7 offset:8\n"
      "ds_read2st64_b32 v[32:33], v6 offset0:8 offset1:10\n"
      "v_fma_f32 v49, 0.5, v85, -v97\n"
      "v_mfma_f32_16x16x32_bf16 v[60:63], a[92:95], a[196:199], v[60:63]\n"
      "ds_read2st64_b32 v[34:35], v6 offset0:12 offset1:14\n"
      "v_fma_f32 v50, 0.5, v86, -v98\n"
      "v_mfma_f32_16x16x32_bf16 v[52:55], a[96:99], a[200:203], v[52:55]\n"
      "ds_read_b128 a[192:195], v1 offset:16384\n"
      "ds_read2st64_b32 v[36:37], v6 offset0:16 offset1:18\n"
      "v_mfma_f32_16x16x32_bf16 v[56:59], a[100:103], a[200:203], v[56:59]\n"
      "ds_read2st64_b32 v[38:39], v6 offset0:20 offset1:22\n"
      "v_fma_f32 v51, 0.5, v87, -v99\n"
      "v_mfma_f32_16x16x32_bf16 v[60:63], a[104:107], a[200:203], v[60:63]\n"
      "s_waitcnt lgkmcnt(8)\n"
      "v_fmac_f32 v28, s46, v64\n"
      "v_fmac_f32 v29, s46, v65\n"
      "v_fmac_f32 v30, s46, v66\n"
      "v_mfma_f32_16x16x32_bf16 v[76:79], v[252:255], a[204:207], v[52:55]\n"
      "s_waitcnt lgkmcnt(5)\n"
      "ds_read_b128 a[196:199], v1 offset:16448\n"
      "v_fmac_f32 v31, s46, v67\n"
      "v_mfma_f32_4x4x4_16b_bf16 v[12:15], a[216:217], a[220:221], 0\n"
      "v_sub_f32_e64 v28, v28, v40\n"
      "v_mfma_f32_4x4x4_16b_bf16 v[16:19], a[216:217], a[222:223], 0\n"
      "v_sub_f32 v29, v29, v41\n"
      "v_sub_f32 v30, v30, v42\n"
      "v_mfma_f32_16x16x32_bf16 v[80:83], v[244:247], a[204:207], v[56:59]\n"
      "v_sub_f32 v31, v31, v43\n"
      "v_fmac_f32 v40, s45, v28\n"
      "v_mfma_f32_16x16x32_bf16 v[84:87], v[236:239], a[204:207], v[60:63]\n"
      "v_fmac_f32 v41, s45, v29\n"
      "v_cvt_pk_bf16_f32 v12, v12, v16\n"
      "v_fmac_f32 v42, s45, v30\n"
      "v_mfma_f32_16x16x32_bf16 v[76:79], v[228:231], a[208:211], v[76:79]\n"
      "ds_read_b128 a[200:203], v1 offset:16512\n"
      "ds_write_addtid_b32 v12 offset:24832\n"
      "v_mfma_f32_16x16x32_bf16 v[80:83], v[220:223], a[208:211], v[80:83]\n"
      "ds_read_b64_tr_b16 a[224:225], v7 offset:512\n"
      "ds_read_b64_tr_b16 a[226:227], v7 offset:520\n"
      "v_mfma_f32_16x16x32_bf16 v[84:87], v[212:215], a[208:211], v[84:87]\n"
      "v_fmac_f32_e64 v43, s45, v31\n"
      "v_cvt_pk_bf16_f32 v13, v13, v17\n"
      "v_mfma_f32_16x16x32_bf16 v[76:79], v[204:207], a[212:215], v[76:79]\n"
      "s_nop 0\n"
      "ds_read_b128 a[204:207], v2 offset:16384\n"
      "ds_write_addtid_b32 v13 offset:28928\n"
      "v_fma_f32 v28, s47, v28, v52\n"
      "v_mfma_f32_16x16x32_bf16 v[80:83], v[196:199], a[212:215], v[80:83]\n"
      "s_nop 0\n"
      "v_fma_f32 v29, s47, v29, v53\n"
      "v_cvt_pk_bf16_f32 v14, v14, v18\n"
      "v_mfma_f32_16x16x32_bf16 v[84:87], v[188:191], a[212:215], v[84:87]\n"
      "s_waitcnt lgkmcnt(5)\n"
      "ds_write_addtid_b32 v14 offset:33024\n"
      "v_fma_f32 v30, s47, v30, v54\n"
      "v_mfma_f32_16x16x32_bf16 v[76:79], v[180:183], a[192:195], v[76:79]\n"
      "s_nop 0\n"
      "ds_read_b128 a[208:211], v2 offset:16448\n"
      "v_fma_f32 v31, s47, v31, v55\n"
      "v_mfma_f32_16x16x32_bf16 v[80:83], v[172:175], a[192:195], v[80:83]\n"
      "s_nop 0\n"
      "v_cvt_pk_bf16_f32 v40, v40, v41\n"
      "v_cvt_pk_bf16_f32 v15, v15, v19\n"
      "v_mfma_f32_16x16x32_bf16 v[84:87], v[164:167], a[192:195], v[84:87]\n"
      "s_waitcnt lgkmcnt(4)\n"
      "ds_write_addtid_b32 v40 offset:12288\n"
      "ds_write_addtid_b32 v15 offset:37120\n"
      "v_cvt_pk_bf16_f32 v42, v42, v43\n"
      "v_mfma_f32_4x4x4_16b_bf16 v[20:23], a[216:217], a[224:225], 0\n"
      "v_cvt_pk_bf16_f32 v28, v28, v29\n"
      "v_mfma_f32_4x4x4_16b_bf16 v[24:27], a[216:217], a[226:227], 0\n"
      "ds_write_addtid_b32 v42 offset:12800\n"
      "ds_write_addtid_b32 v28 offset:14336\n"
      "v_mfma_f32_16x16x32_bf16 v[76:79], v[156:159], a[196:199], v[76:79]\n"
      "ds_read_b128 a[212:215], v2 offset:16512\n"
      "v_cvt_pk_bf16_f32 v30, v30, v31\n"
      "v_mfma_f32_16x16x32_bf16 v[80:83], v[148:151], a[196:199], v[80:83]\n"
      "ds_write_addtid_b32 v30 offset:14848\n"
      "v_fmac_f32 v32, s46, v68\n"
      "v_cvt_pk_bf16_f32 v20, v20, v24\n"
      "v_mfma_f32_16x16x32_bf16 v[84:87], v[140:143], a[196:199], v[84:87]\n"
      "ds_write_addtid_b32 v20 offset:25344\n"
      "v_fmac_f32 v33, s46, v69\n"
      "v_mfma_f32_16x16x32_bf16 v[76:79], v[132:135], a[200:203], v[76:79]\n"
      "ds_read_b128 a[192:195], v0 offset:20480\n"
      "ds_read_b64_tr_b16 a[228:229], v7 offset:1024\n"
      "v_mfma_f32_16x16x32_bf16 v[80:83], v[124:127], a[200:203], v[80:83]\n"
      "ds_read_b64_tr_b16 a[230:231], v7 offset:1032\n"
      "v_fmac_f32 v34, s46, v70\n"
      "v_mfma_f32_16x16x32_bf16 v[84:87], v[116:119], a[200:203], v[84:87]\n"
      "s_waitcnt lgkmcnt(4)\n"
      "v_fmac_f32 v35, s46, v71\n"
      "v_cvt_pk_bf16_f32 v21, v21, v25\n"
      "v_mfma_f32_16x16x32_bf16 v[76:79], v[108:111], a[204:207], v[76:79]\n"
      "s_nop 0\n"
      "ds_read_b128 a[196:199], v0 offset:20544\n"
      "ds_write_addtid_b32 v21 offset:29440\n"
      "v_sub_f32 v32, v32, v44\n"
      "v_mfma_f32_16x16x32_bf16 v[80:83], v[100:103], a[204:207], v[80:83]\n"
      "v_sub_f32 v33, v33, v45\n"
      "v_cvt_pk_bf16_f32 v22, v22, v26\n"
      "v_mfma_f32_16x16x32_bf16 v[84:87], a[160:163], a[204:207], v[84:87]\n"
      "ds_write_addtid_b32 v22 offset:33536\n"
      "v_sub_f32 v34, v34, v46\n"
      "v_mfma_f32_16x16x32_bf16 v[76:79], a[152:155], a[208:211], v[76:79]\n"
      "s_nop 0\n"
      "ds_read_b128 a[200:203], v0 offset:20608\n"
      "v_sub_f32 v35, v35, v47\n"
      "v_mfma_f32_16x16x32_bf16 v[80:83], a[144:147], a[208:211], v[80:83]\n"
      "s_waitcnt lgkmcnt(4)\n"
      "v_fmac_f32 v44, s45, v32\n"
      "v_cvt_pk_bf16_f32 v23, v23, v27\n"
      "v_mfma_f32_4x4x4_16b_bf16 v[12:15], a[216:217], a[228:229], 0\n"
      "v_fmac_f32 v45, s45, v33\n"
      "v_mfma_f32_4x4x4_16b_bf16 v[16:19], a[216:217], a[230:231], 0\n"
      "ds_write_addtid_b32 v23 offset:37632\n"
      "v_fmac_f32 v46, s45, v34\n"
      "v_mfma_f32_16x16x32_bf16 v[84:87], a[136:139], a[208:211], v[84:87]\n"
      "v_fmac_f32 v47, s45, v35\n"
      "v_fma_f32 v32, s47, v32, v56\n"
      "v_cvt_pk_bf16_f32 v12, v12, v16\n"
      "v_mfma_f32_16x16x32_bf16 v[76:79], a[128:131], a[212:215], v[76:79]\n"
      "ds_read_b128 a[204:207], v1 offset:20480\n"
      "ds_write_addtid_b32 v12 offset:25856\n"
      "v_mfma_f32_16x16x32_bf16 v[80:83], a[120:123], a[212:215], v[80:83]\n"
      "v_fma_f32 v33, s47, v33, v57\n"
      "v_cvt_pk_bf16_f32 v13, v13, v17\n"
      "v_mfma_f32_16x16x32_bf16 v[84:87], a[112:115], a[212:215], v[84:87]\n"
      "ds_read_b64_tr_b16 a[232:233], v7 offset:1536\n"
      "ds_write_addtid_b32 v13 offset:29952\n"
      "v_fma_f32 v34, s47, v34, v58\n"
      "v_mfma_f32_16x16x32_bf16 v[88:91], v[248:251], a[192:195], v[52:55]\n"
      "ds_read_b128 a[208:211], v1 offset:20544\n"
      "ds_read_b64_tr_b16 a[234:235], v7 offset:1544\n"
      "v_mfma_f32_16x16x32_bf16 v[92:95], v[240:243], a[192:195], v[56:59]\n"
      "s_nop 0\n"
      "v_fma_f32 v35, s47, v35, v59\n"
      "v_cvt_pk_bf16_f32 v44, v44, v45\n"
      "v_mfma_f32_16x16x32_bf16 v[96:99], v[232:235], a[192:195], v[60:63]\n"
      "s_waitcnt lgkmcnt(4)\n"
      "ds_write_addtid_b32 v44 offset:20484\n"
      "v_cvt_pk_bf16_f32 v14, v14, v18\n"
      "v_mfma_f32_16x16x32_bf16 v[88:91], v[224:227], a[196:199], v[88:91]\n"
      "ds_read_b128 a[212:215], v1 offset:20608\n"
      "ds_write_addtid_b32 v14 offset:34048\n"
      "v_cvt_pk_bf16_f32 v46, v46, v47\n"
      "v_mfma_f32_16x16x32_bf16 v[92:95], v[216:219], a[196:199], v[92:95]\n"
      "ds_write_addtid_b32 v46 offset:20996\n"
      "v_cvt_pk_bf16_f32 v32, v32, v33\n"
      "v_mfma_f32_16x16x32_bf16 v[96:99], v[208:211], a[196:199], v[96:99]\n"
      "ds_write_addtid_b32 v32 offset:22532\n"
      "v_cvt_pk_bf16_f32 v34, v34, v35\n"
      "v_mfma_f32_16x16x32_bf16 v[88:91], v[200:203], a[200:203], v[88:91]\n"
      "s_waitcnt lgkmcnt(4)\n"
      "ds_read_b128 a[192:195], v2 offset:20480\n"
      "ds_write_addtid_b32 v34 offset:23044\n"
      "v_fmac_f32 v36, s46, v72\n"
      "v_mfma_f32_4x4x4_16b_bf16 v[20:23], a[216:217], a[232:233], 0\n"
      "v_fmac_f32_e64 v37, s46, v73\n"
      "v_mfma_f32_4x4x4_16b_bf16 v[24:27], a[216:217], a[234:235], 0\n"
      "v_fmac_f32 v38, s46, v74\n"
      "v_cvt_pk_bf16_f32 v15, v15, v19\n"
      "v_mfma_f32_16x16x32_bf16 v[92:95], v[192:195], a[200:203], v[92:95]\n"
      "ds_write_addtid_b32 v15 offset:38144\n"
      "v_fmac_f32 v39, s46, v75\n"
      "v_cvt_pk_bf16_f32 v20, v20, v24\n"
      "v_mfma_f32_16x16x32_bf16 v[96:99], v[184:187], a[200:203], v[96:99]\n"
      "ds_write_addtid_b32 v20 offset:26368\n"
      "v_sub_f32 v36, v36, v48\n"
      "v_mfma_f32_16x16x32_bf16 v[88:91], v[176:179], a[204:207], v[88:91]\n"
      "ds_read_b128 a[196:199], v2 offset:20544\n"
      "v_sub_f32 v37, v37, v49\n"
      "v_mfma_f32_16x16x32_bf16 v[92:95], v[168:171], a[204:207], v[92:95]\n"
      "ds_read_b64_tr_b16 a[220:221], v7 offset:2048\n"
      "ds_read_b64_tr_b16 a[222:223], v7 offset:2056\n"
      "v_mfma_f32_16x16x32_bf16 v[96:99], v[160:163], a[204:207], v[96:99]\n"
      "v_sub_f32_e64 v38, v38, v50\n"
      "v_cvt_pk_bf16_f32 v21, v21, v25\n"
      "v_mfma_f32_16x16x32_bf16 v[88:91], v[152:155], a[208:211], v[88:91]\n"
      "ds_read_b128 a[200:203], v2 offset:20608\n"
      "ds_write_addtid_b32 v21 offset:30464\n"
      "v_sub_f32 v39, v39, v51\n"
      "v_mfma_f32_16x16x32_bf16 v[92:95], v[144:147], a[208:211], v[92:95]\n"
      "s_nop 0\n"
      "v_fmac_f32_e64 v48, s45, v36\n"
      "v_cvt_pk_bf16_f32 v22, v22, v26\n"
      "v_mfma_f32_16x16x32_bf16 v[96:99], v[136:139], a[208:211], v[96:99]\n"
      "s_waitcnt lgkmcnt(3)\n"
      "ds_write_addtid_b32 v22 offset:34560\n"
      "v_fmac_f32 v49, s45, v37\n"
      "v_mfma_f32_16x16x32_bf16 v[88:91], v[128:131], a[212:215], v[88:91]\n"
      "s_nop 0\n"
      "ds_read_b128 a[204:207], v3 offset:0\n"
      "v_fmac_f32_e64 v50, s45, v38\n"
      "v_mfma_f32_16x16x32_bf16 v[92:95], v[120:123], a[212:215], v[92:95]\n"
      "s_nop 0\n"
      "v_fmac_f32 v51, s45, v39\n"
      "v_cvt_pk_bf16_f32 v23, v23, v27\n"
      "v_mfma_f32_16x16x32_bf16 v[96:99], v[112:115], a[212:215], v[96:99]\n"
      "s_waitcnt lgkmcnt(2)\n"
      "ds_write_addtid_b32 v23 offset:38656\n"
      "v_fma_f32 v36, s47, v36, v60\n"
      "v_mfma_f32_4x4x4_16b_bf16 v[12:15], a[216:217], a[220:221], 0\n"
      "v_fma_f32 v37, s47, v37, v61\n"
      "v_mfma_f32_4x4x4_16b_bf16 v[16:19], a[216:217], a[222:223], 0\n"
      "v_fma_f32 v38, s47, v38, v62\n"
      "v_fma_f32 v39, s47, v39, v63\n"
      "v_mfma_f32_16x16x32_bf16 v[88:91], v[104:107], a[192:195], v[88:91]\n"
      "ds_read_b128 a[208:211], v3 offset:64\n"
      "v_cvt_pk_bf16_f32 v48, v48, v49\n"
      "v_cvt_pk_bf16_f32 v12, v12, v16\n"
      "v_mfma_f32_16x16x32_bf16 v[92:95], a[164:167], a[192:195], v[92:95]\n"
      "ds_write_addtid_b32 v48 offset:16384\n"
      "ds_write_addtid_b32 v12 offset:26880\n"
      "v_mfma_f32_16x16x32_bf16 v[96:99], a[156:159], a[192:195], v[96:99]\n"
      "v_cvt_pk_bf16_f32 v13, v13, v17\n"
      "v_cvt_pk_bf16_f32 v14, v14, v18\n"
      "v_mfma_f32_16x16x32_bf16 v[88:91], a[148:151], a[196:199], v[88:91]\n"
      "ds_read_b128 a[212:215], v3 offset:128\n"
      "ds_write_addtid_b32 v13 offset:30976\n"
      "v_mfma_f32_16x16x32_bf16 v[92:95], a[140:143], a[196:199], v[92:95]\n"
      "ds_read_b64_tr_b16 a[224:225], v7 offset:2560\n"
      "ds_write_addtid_b32 v14 offset:35072\n"
      "v_cvt_pk_bf16_f32 v50, v50, v51\n"
      "v_mfma_f32_16x16x32_bf16 v[96:99], a[132:135], a[196:199], v[96:99]\n"
      "ds_write_addtid_b32 v50 offset:16896\n"
      "ds_read_b64_tr_b16 a[226:227], v7 offset:2568\n"
      "v_mfma_f32_16x16x32_bf16 v[88:91], a[124:127], a[200:203], v[88:91]\n"
      "ds_read_b128 a[192:195], v4 offset:0\n"
      "v_cvt_pk_bf16_f32 v36, v36, v37\n"
      "v_mfma_f32_16x16x32_bf16 v[92:95], a[116:119], a[200:203], v[92:95]\n"
      "ds_write_addtid_b32 v36 offset:18432\n"
      "v_cvt_pk_bf16_f32 v15, v15, v19\n"
      "v_mfma_f32_16x16x32_bf16 v[96:99], a[108:111], a[200:203], v[96:99]\n"
      "s_waitcnt lgkmcnt(6)\n"
      "ds_write_addtid_b32 v15 offset:39168\n"
      "v_sub_f32 v76, v88, v76\n"
      "v_sub_f32_e64 v77, v89, v77\n"
      "v_cvt_pk_bf16_f32 v38, v38, v39\n"
      "v_mfma_f32_16x16x32_bf16 v[64:67], a[0:3], a[204:207], 0\n"
      "s_nop 0\n"
      "ds_read_b128 a[196:199], v4 offset:64\n"
      "ds_write_addtid_b32 v38 offset:18944\n"
      "v_mfma_f32_16x16x32_bf16 v[68:71], a[4:7], a[204:207], 0\n"
      "s_waitcnt lgkmcnt(5)\n"
      "ds_write_addtid_b32 v76 offset:6144\n"
      "ds_write_addtid_b32 v77 offset:6656\n"
      "v_mfma_f32_4x4x4_16b_bf16 v[20:23], a[216:217], a[224:225], 0\n"
      "v_sub_f32 v78, v90, v78\n"
      "v_mfma_f32_4x4x4_16b_bf16 v[24:27], a[216:217], a[226:227], 0\n"
      "ds_write_addtid_b32 v78 offset:7168\n"
      "v_sub_f32 v79, v91, v79\n"
      "v_mfma_f32_16x16x32_bf16 v[72:75], a[8:11], a[204:207], 0\n"
      "ds_write_addtid_b32 v79 offset:7680\n"
      "v_sub_f32 v80, v92, v80\n"
      "v_sub_f32 v81, v93, v81\n"
      "v_mfma_f32_16x16x32_bf16 v[64:67], a[12:15], a[208:211], v[64:67]\n"
      "ds_read_b128 a[200:203], v4 offset:128\n"
      "ds_write_addtid_b32 v80 offset:8192\n"
      "v_mfma_f32_16x16x32_bf16 v[68:71], a[16:19], a[208:211], v[68:71]\n"
      "ds_write_addtid_b32 v81 offset:8704\n"
      "v_sub_f32 v82, v94, v82\n"
      "v_mfma_f32_16x16x32_bf16 v[72:75], a[20:23], a[208:211], v[72:75]\n"
      "s_nop 0\n"
      "ds_write_addtid_b32 v82 offset:9216\n"
      "v_cvt_pk_bf16_f32 v20, v20, v24\n"
      "v_mfma_f32_16x16x32_bf16 v[64:67], a[24:27], a[212:215], v[64:67]\n"
      "ds_read_b128 a[204:207], v5 offset:0\n"
      "ds_write_addtid_b32 v20 offset:27392\n"
      "v_sub_f32 v83, v95, v83\n"
      "v_mfma_f32_16x16x32_bf16 v[68:71], a[28:31], a[212:215], v[68:71]\n"
      "ds_write_addtid_b32 v83 offset:9728\n"
      "v_sub_f32_e64 v84, v96, v84\n"
      "v_mfma_f32_16x16x32_bf16 v[72:75], a[32:35], a[212:215], v[72:75]\n"
      "s_waitcnt lgkmcnt(5)\n"
      "ds_write_addtid_b32 v84 offset:10240\n"
      "v_cvt_pk_bf16_f32 v21, v21, v25\n"
      "v_mfma_f32_16x16x32_bf16 v[64:67], a[36:39], a[192:195], v[64:67]\n"
      "ds_read_b128 a[208:211], v5 offset:64\n"
      "ds_write_addtid_b32 v21 offset:31488\n"
      "v_mfma_f32_16x16x32_bf16 v[68:71], a[40:43], a[192:195], v[68:71]\n"
      "v_sub_f32 v85, v97, v85\n"
      "v_sub_f32 v86, v98, v86\n"
      "v_cvt_pk_bf16_f32 v22, v22, v26\n"
      "v_mfma_f32_16x16x32_bf16 v[72:75], a[44:47], a[192:195], v[72:75]\n"
      "ds_write_addtid_b32 v22 offset:35584\n"
      "ds_write_addtid_b32 v85 offset:10752\n"
      "v_sub_f32 v87, v99, v87\n"
      "v_mfma_f32_16x16x32_bf16 v[64:67], a[48:51], a[196:199], v[64:67]\n"
      "ds_read_b128 a[212:215], v5 offset:128\n"
      "ds_write_addtid_b32 v86 offset:11264\n"
      "v_mfma_f32_16x16x32_bf16 v[68:71], a[52:55], a[196:199], v[68:71]\n"
      "s_nop 0\n"
      "ds_write_addtid_b32 v87 offset:11776\n"
      "v_cvt_pk_bf16_f32 v23, v23, v27\n"
      "v_mfma_f32_16x16x32_bf16 v[72:75], a[56:59], a[196:199], v[72:75]\n"
      "ds_write_addtid_b32 v23 offset:39680\n"
      "v_fma_f32 v40, 0.5, v76, -v88\n"
      "v_mfma_f32_16x16x32_bf16 v[64:67], a[60:63], a[200:203], v[64:67]\n"
      "v_fma_f32 v41, 0.5, v77, -v89\n"
      "v_fma_f32 v42, 0.5, v78, -v90\n"
      "v_mfma_f32_16x16x32_bf16 v[68:71], a[64:67], a[200:203], v[68:71]\n"
      "v_fma_f32 v43, 0.5, v79, -v91\n"
      "v_fma_f32 v44, 0.5, v80, -v92\n"
      "v_mfma_f32_16x16x32_bf16 v[72:75], a[68:71], a[200:203], v[72:75]\n"
      "s_waitcnt lgkmcnt(0)\n"
      "v_fma_f32 v45, 0.5, v81, -v93\n"
      "s_barrier\n"
      "v_mfma_f32_16x16x32_bf16 v[64:67], a[72:75], a[204:207], v[64:67]\n"
      "ds_read_b128 a[192:195], v0 offset:0\n"
      "ds_read2st64_b32 v[28:29], v6 offset0:24 offset1:26\n"
      "v_fma_f32 v46, 0.5, v82, -v94\n"
      "v_mfma_f32_16x16x32_bf16 v[68:71], a[76:79], a[204:207], v[68:71]\n"
      "ds_read_b128 a[196:199], v0 offset:64\n"
      "ds_read2st64_b32 v[30:31], v6 offset0:28 offset1:30\n"
      "v_mfma_f32_16x16x32_bf16 v[72:75], a[80:83], a[204:207], v[72:75]\n"
      "ds_read_b64_tr_b16 a[220:221], v7 offset:16896\n"
      "v_fma_f32 v47, 0.5, v83, -v95\n"
      "v_mfma_f32_16x16x32_bf16 v[64:67], a[84:87], a[208:211], v[64:67]\n"
      "ds_read_b128 a[200:203], v0 offset:128\n"
      "v_fma_f32 v48, 0.5, v84, -v96\n"
      "v_mfma_f32_16x16x32_bf16 v[68:71], a[88:91], a[208:211], v[68:71]\n"
      "ds_read_b64_tr_b16 a[222:223], v7 offset:16904\n"
      "ds_read2st64_b32 v[32:33], v6 offset0:32 offset1:34\n"
      "v_fma_f32 v49, 0.5, v85, -v97\n"
      "v_mfma_f32_16x16x32_bf16 v[72:75], a[92:95], a[208:211], v[72:75]\n"
      "ds_read2st64_b32 v[34:35], v6 offset0:36 offset1:38\n"
      "v_fma_f32 v50, 0.5, v86, -v98\n"
      "v_mfma_f32_16x16x32_bf16 v[64:67], a[96:99], a[212:215], v[64:67]\n"
      "ds_read_b128 a[204:207], v1 offset:0\n"
      "ds_read2st64_b32 v[36:37], v6 offset0:40 offset1:42\n"
      "v_mfma_f32_16x16x32_bf16 v[68:71], a[100:103], a[212:215], v[68:71]\n"
      "ds_read2st64_b32 v[38:39], v6 offset0:44 offset1:46\n"
      "v_fma_f32 v51, 0.5, v87, -v99\n"
      "v_mfma_f32_16x16x32_bf16 v[72:75], a[104:107], a[212:215], v[72:75]\n"
      "s_waitcnt lgkmcnt(8)\n"
      "v_fmac_f32 v28, s46, v52\n"
      "v_fmac_f32 v29, s46, v53\n"
      "v_fmac_f32 v30, s46, v54\n"
      "v_mfma_f32_16x16x32_bf16 v[76:79], v[252:255], a[192:195], v[64:67]\n"
      "s_waitcnt lgkmcnt(5)\n"
      "ds_read_b128 a[208:211], v1 offset:64\n"
      "v_fmac_f32 v31, s46, v55\n"
      "v_mfma_f32_4x4x4_16b_bf16 v[12:15], a[216:217], a[220:221], 0\n"
      "v_sub_f32_e64 v28, v28, v40\n"
      "v_mfma_f32_4x4x4_16b_bf16 v[16:19], a[216:217], a[222:223], 0\n"
      "v_sub_f32 v29, v29, v41\n"
      "v_sub_f32 v30, v30, v42\n"
      "v_mfma_f32_16x16x32_bf16 v[80:83], v[244:247], a[192:195], v[68:71]\n"
      "v_sub_f32 v31, v31, v43\n"
      "v_fmac_f32 v40, s45, v28\n"
      "v_mfma_f32_16x16x32_bf16 v[84:87], v[236:239], a[192:195], v[72:75]\n"
      "v_fmac_f32 v41, s45, v29\n"
      "v_cvt_pk_bf16_f32 v12, v12, v16\n"
      "v_fmac_f32 v42, s45, v30\n"
      "v_mfma_f32_16x16x32_bf16 v[76:79], v[228:231], a[196:199], v[76:79]\n"
      "ds_read_b128 a[212:215], v1 offset:128\n"
      "ds_write_addtid_b32 v12 offset:41216\n"
      "v_mfma_f32_16x16x32_bf16 v[80:83], v[220:223], a[196:199], v[80:83]\n"
      "ds_read_b64_tr_b16 a[224:225], v7 offset:17408\n"
      "ds_read_b64_tr_b16 a[226:227], v7 offset:17416\n"
      "v_mfma_f32_16x16x32_bf16 v[84:87], v[212:215], a[196:199], v[84:87]\n"
      "v_fmac_f32_e64 v43, s45, v31\n"
      "v_cvt_pk_bf16_f32 v13, v13, v17\n"
      "v_mfma_f32_16x16x32_bf16 v[76:79], v[204:207], a[200:203], v[76:79]\n"
      "s_nop 0\n"
      "ds_read_b128 a[192:195], v2 offset:0\n"
      "ds_write_addtid_b32 v13 offset:45312\n"
      "v_fma_f32 v28, s47, v28, v64\n"
      "v_mfma_f32_16x16x32_bf16 v[80:83], v[196:199], a[200:203], v[80:83]\n"
      "s_nop 0\n"
      "v_fma_f32 v29, s47, v29, v65\n"
      "v_cvt_pk_bf16_f32 v14, v14, v18\n"
      "v_mfma_f32_16x16x32_bf16 v[84:87], v[188:191], a[200:203], v[84:87]\n"
      "s_waitcnt lgkmcnt(5)\n"
      "ds_write_addtid_b32 v14 offset:49408\n"
      "v_fma_f32 v30, s47, v30, v66\n"
      "v_mfma_f32_16x16x32_bf16 v[76:79], v[180:183], a[204:207], v[76:79]\n"
      "s_nop 0\n"
      "ds_read_b128 a[196:199], v2 offset:64\n"
      "v_fma_f32 v31, s47, v31, v67\n"
      "v_mfma_f32_16x16x32_bf16 v[80:83], v[172:175], a[204:207], v[80:83]\n"
      "s_nop 0\n"
      "v_cvt_pk_bf16_f32 v40, v40, v41\n"
      "v_cvt_pk_bf16_f32 v15, v15, v19\n"
      "v_mfma_f32_16x16x32_bf16 v[84:87], v[164:167], a[204:207], v[84:87]\n"
      "s_waitcnt lgkmcnt(4)\n"
      "ds_write_addtid_b32 v40 offset:13312\n"
      "ds_write_addtid_b32 v15 offset:53504\n"
      "v_cvt_pk_bf16_f32 v42, v42, v43\n"
      "v_mfma_f32_4x4x4_16b_bf16 v[20:23], a[216:217], a[224:225], 0\n"
      "v_cvt_pk_bf16_f32 v28, v28, v29\n"
      "v_mfma_f32_4x4x4_16b_bf16 v[24:27], a[216:217], a[226:227], 0\n"
      "ds_write_addtid_b32 v42 offset:13824\n"
      "ds_write_addtid_b32 v28 offset:15360\n"
      "v_mfma_f32_16x16x32_bf16 v[76:79], v[156:159], a[208:211], v[76:79]\n"
      "ds_read_b128 a[200:203], v2 offset:128\n"
      "v_cvt_pk_bf16_f32 v30, v30, v31\n"
      "v_mfma_f32_16x16x32_bf16 v[80:83], v[148:151], a[208:211], v[80:83]\n"
      "ds_write_addtid_b32 v30 offset:15872\n"
      "v_fmac_f32 v32, s46, v56\n"
      "v_cvt_pk_bf16_f32 v20, v20, v24\n"
      "v_mfma_f32_16x16x32_bf16 v[84:87], v[140:143], a[208:211], v[84:87]\n"
      "ds_write_addtid_b32 v20 offset:41728\n"
      "v_fmac_f32 v33, s46, v57\n"
      "v_mfma_f32_16x16x32_bf16 v[76:79], v[132:135], a[212:215], v[76:79]\n"
      "ds_read_b128 a[204:207], v0 offset:4096\n"
      "ds_read_b64_tr_b16 a[228:229], v7 offset:17920\n"
      "v_mfma_f32_16x16x32_bf16 v[80:83], v[124:127], a[212:215], v[80:83]\n"
      "ds_read_b64_tr_b16 a[230:231], v7 offset:17928\n"
      "v_fmac_f32 v34, s46, v58\n"
      "v_mfma_f32_16x16x32_bf16 v[84:87], v[116:119], a[212:215], v[84:87]\n"
      "s_waitcnt lgkmcnt(4)\n"
      "v_fmac_f32 v35, s46, v59\n"
      "v_cvt_pk_bf16_f32 v21, v21, v25\n"
      "v_mfma_f32_16x16x32_bf16 v[76:79], v[108:111], a[192:195], v[76:79]\n"
      "s_nop 0\n"
      "ds_read_b128 a[208:211], v0 offset:4160\n"
      "ds_write_addtid_b32 v21 offset:45824\n"
      "v_sub_f32 v32, v32, v44\n"
      "v_mfma_f32_16x16x32_bf16 v[80:83], v[100:103], a[192:195], v[80:83]\n"
      "v_sub_f32 v33, v33, v45\n"
      "v_cvt_pk_bf16_f32 v22, v22, v26\n"
      "v_mfma_f32_16x16x32_bf16 v[84:87], a[160:163], a[192:195], v[84:87]\n"
      "ds_write_addtid_b32 v22 offset:49920\n"
      "v_sub_f32 v34, v34, v46\n"
      "v_mfma_f32_16x16x32_bf16 v[76:79], a[152:155], a[196:199], v[76:79]\n"
      "s_nop 0\n"
      "ds_read_b128 a[212:215], v0 offset:4224\n"
      "v_sub_f32 v35, v35, v47\n"
      "v_mfma_f32_16x16x32_bf16 v[80:83], a[144:147], a[196:199], v[80:83]\n"
      "s_waitcnt lgkmcnt(4)\n"
      "v_fmac_f32 v44, s45, v32\n"
      "v_cvt_pk_bf16_f32 v23, v23, v27\n"
      "v_mfma_f32_4x4x4_16b_bf16 v[12:15], a[216:217], a[228:229], 0\n"
      "v_fmac_f32 v45, s45, v33\n"
      "v_mfma_f32_4x4x4_16b_bf16 v[16:19], a[216:217], a[230:231], 0\n"
      "ds_write_addtid_b32 v23 offset:54016\n"
      "v_fmac_f32 v46, s45, v34\n"
      "v_mfma_f32_16x16x32_bf16 v[84:87], a[136:139], a[196:199], v[84:87]\n"
      "v_fmac_f32 v47, s45, v35\n"
      "v_fma_f32 v32, s47, v32, v68\n"
      "v_cvt_pk_bf16_f32 v12, v12, v16\n"
      "v_mfma_f32_16x16x32_bf16 v[76:79], a[128:131], a[200:203], v[76:79]\n"
      "ds_read_b128 a[192:195], v1 offset:4096\n"
      "ds_write_addtid_b32 v12 offset:42240\n"
      "v_mfma_f32_16x16x32_bf16 v[80:83], a[120:123], a[200:203], v[80:83]\n"
      "v_fma_f32 v33, s47, v33, v69\n"
      "v_cvt_pk_bf16_f32 v13, v13, v17\n"
      "v_mfma_f32_16x16x32_bf16 v[84:87], a[112:115], a[200:203], v[84:87]\n"
      "ds_read_b64_tr_b16 a[232:233], v7 offset:18432\n"
      "ds_write_addtid_b32 v13 offset:46336\n"
      "v_fma_f32 v34, s47, v34, v70\n"
      "v_mfma_f32_16x16x32_bf16 v[88:91], v[248:251], a[204:207], v[64:67]\n"
      "ds_read_b128 a[196:199], v1 offset:4160\n"
      "ds_read_b64_tr_b16 a[234:235], v7 offset:18440\n"
      "v_mfma_f32_16x16x32_bf16 v[92:95], v[240:243], a[204:207], v[68:71]\n"
      "s_nop 0\n"
      "v_fma_f32 v35, s47, v35, v71\n"
      "v_cvt_pk_bf16_f32 v44, v44, v45\n"
      "v_mfma_f32_16x16x32_bf16 v[96:99], v[232:235], a[204:207], v[72:75]\n"
      "s_waitcnt lgkmcnt(4)\n"
      "ds_write_addtid_b32 v44 offset:21508\n"
      "v_cvt_pk_bf16_f32 v14, v14, v18\n"
      "v_mfma_f32_16x16x32_bf16 v[88:91], v[224:227], a[208:211], v[88:91]\n"
      "ds_read_b128 a[200:203], v1 offset:4224\n"
      "ds_write_addtid_b32 v14 offset:50432\n"
      "v_cvt_pk_bf16_f32 v46, v46, v47\n"
      "v_mfma_f32_16x16x32_bf16 v[92:95], v[216:219], a[208:211], v[92:95]\n"
      "ds_write_addtid_b32 v46 offset:22020\n"
      "v_cvt_pk_bf16_f32 v32, v32, v33\n"
      "v_mfma_f32_16x16x32_bf16 v[96:99], v[208:211], a[208:211], v[96:99]\n"
      "ds_write_addtid_b32 v32 offset:23556\n"
      "v_cvt_pk_bf16_f32 v34, v34, v35\n"
      "v_mfma_f32_16x16x32_bf16 v[88:91], v[200:203], a[212:215], v[88:91]\n"
      "s_waitcnt lgkmcnt(4)\n"
      "ds_read_b128 a[204:207], v2 offset:4096\n"
      "ds_write_addtid_b32 v34 offset:24068\n"
      "v_fmac_f32 v36, s46, v60\n"
      "v_mfma_f32_4x4x4_16b_bf16 v[20:23], a[216:217], a[232:233], 0\n"
      "v_fmac_f32_e64 v37, s46, v61\n"
      "v_mfma_f32_4x4x4_16b_bf16 v[24:27], a[216:217], a[234:235], 0\n"
      "v_fmac_f32 v38, s46, v62\n"
      "v_cvt_pk_bf16_f32 v15, v15, v19\n"
      "v_mfma_f32_16x16x32_bf16 v[92:95], v[192:195], a[212:215], v[92:95]\n"
      "ds_write_addtid_b32 v15 offset:54528\n"
      "v_fmac_f32 v39, s46, v63\n"
      "v_cvt_pk_bf16_f32 v20, v20, v24\n"
      "v_mfma_f32_16x16x32_bf16 v[96:99], v[184:187], a[212:215], v[96:99]\n"
      "ds_write_addtid_b32 v20 offset:42752\n"
      "v_sub_f32 v36, v36, v48\n"
      "v_mfma_f32_16x16x32_bf16 v[88:91], v[176:179], a[192:195], v[88:91]\n"
      "ds_read_b128 a[208:211], v2 offset:4160\n"
      "v_sub_f32 v37, v37, v49\n"
      "v_mfma_f32_16x16x32_bf16 v[92:95], v[168:171], a[192:195], v[92:95]\n"
      "ds_read_b64_tr_b16 a[220:221], v7 offset:18944\n"
      "ds_read_b64_tr_b16 a[222:223], v7 offset:18952\n"
      "v_mfma_f32_16x16x32_bf16 v[96:99], v[160:163], a[192:195], v[96:99]\n"
      "v_sub_f32_e64 v38, v38, v50\n"
      "v_cvt_pk_bf16_f32 v21, v21, v25\n"
      "v_mfma_f32_16x16x32_bf16 v[88:91], v[152:155], a[196:199], v[88:91]\n"
      "ds_read_b128 a[212:215], v2 offset:4224\n"
      "ds_write_addtid_b32 v21 offset:46848\n"
      "v_sub_f32 v39, v39, v51\n"
      "v_mfma_f32_16x16x32_bf16 v[92:95], v[144:147], a[196:199], v[92:95]\n"
      "s_nop 0\n"
      "v_fmac_f32_e64 v48, s45, v36\n"
      "v_cvt_pk_bf16_f32 v22, v22, v26\n"
      "v_mfma_f32_16x16x32_bf16 v[96:99], v[136:139], a[196:199], v[96:99]\n"
      "s_waitcnt lgkmcnt(3)\n"
      "ds_write_addtid_b32 v22 offset:50944\n"
      "v_fmac_f32 v49, s45, v37\n"
      "v_mfma_f32_16x16x32_bf16 v[88:91], v[128:131], a[200:203], v[88:91]\n"
      "s_nop 0\n"
      "ds_read_b128 a[192:195], v3 offset:16896\n"
      "v_fmac_f32_e64 v50, s45, v38\n"
      "v_mfma_f32_16x16x32_bf16 v[92:95], v[120:123], a[200:203], v[92:95]\n"
      "s_nop 0\n"
      "v_fmac_f32 v51, s45, v39\n"
      "v_cvt_pk_bf16_f32 v23, v23, v27\n"
      "v_mfma_f32_16x16x32_bf16 v[96:99], v[112:115], a[200:203], v[96:99]\n"
      "s_waitcnt lgkmcnt(2)\n"
      "ds_write_addtid_b32 v23 offset:55040\n"
      "v_fma_f32 v36, s47, v36, v72\n"
      "v_mfma_f32_4x4x4_16b_bf16 v[12:15], a[216:217], a[220:221], 0\n"
      "v_fma_f32 v37, s47, v37, v73\n"
      "v_mfma_f32_4x4x4_16b_bf16 v[16:19], a[216:217], a[222:223], 0\n"
      "v_fma_f32 v38, s47, v38, v74\n"
      "v_fma_f32 v39, s47, v39, v75\n"
      "v_mfma_f32_16x16x32_bf16 v[88:91], v[104:107], a[204:207], v[88:91]\n"
      "ds_read_b128 a[196:199], v3 offset:16960\n"
      "v_cvt_pk_bf16_f32 v48, v48, v49\n"
      "v_cvt_pk_bf16_f32 v12, v12, v16\n"
      "v_mfma_f32_16x16x32_bf16 v[92:95], a[164:167], a[204:207], v[92:95]\n"
      "ds_write_addtid_b32 v48 offset:17408\n"
      "ds_write_addtid_b32 v12 offset:43264\n"
      "v_mfma_f32_16x16x32_bf16 v[96:99], a[156:159], a[204:207], v[96:99]\n"
      "v_cvt_pk_bf16_f32 v13, v13, v17\n"
      "v_cvt_pk_bf16_f32 v14, v14, v18\n"
      "v_mfma_f32_16x16x32_bf16 v[88:91], a[148:151], a[208:211], v[88:91]\n"
      "ds_read_b128 a[200:203], v3 offset:17024\n"
      "ds_write_addtid_b32 v13 offset:47360\n"
      "v_mfma_f32_16x16x32_bf16 v[92:95], a[140:143], a[208:211], v[92:95]\n"
      "ds_read_b64_tr_b16 a[224:225], v7 offset:19456\n"
      "ds_write_addtid_b32 v14 offset:51456\n"
      "v_cvt_pk_bf16_f32 v50, v50, v51\n"
      "v_mfma_f32_16x16x32_bf16 v[96:99], a[132:135], a[208:211], v[96:99]\n"
      "ds_write_addtid_b32 v50 offset:17920\n"
      "ds_read_b64_tr_b16 a[226:227], v7 offset:19464\n"
      "v_mfma_f32_16x16x32_bf16 v[88:91], a[124:127], a[212:215], v[88:91]\n"
      "ds_read_b128 a[204:207], v4 offset:16896\n"
      "v_cvt_pk_bf16_f32 v36, v36, v37\n"
      "v_mfma_f32_16x16x32_bf16 v[92:95], a[116:119], a[212:215], v[92:95]\n"
      "ds_write_addtid_b32 v36 offset:19456\n"
      "v_cvt_pk_bf16_f32 v15, v15, v19\n"
      "v_mfma_f32_16x16x32_bf16 v[96:99], a[108:111], a[212:215], v[96:99]\n"
      "s_waitcnt lgkmcnt(6)\n"
      "ds_write_addtid_b32 v15 offset:55552\n"
      "v_sub_f32 v76, v88, v76\n"
      "v_sub_f32_e64 v77, v89, v77\n"
      "v_cvt_pk_bf16_f32 v38, v38, v39\n"
      "v_mfma_f32_16x16x32_bf16 v[52:55], a[0:3], a[192:195], 0\n"
      "s_nop 0\n"
      "ds_read_b128 a[208:211], v4 offset:16960\n"
      "ds_write_addtid_b32 v38 offset:19968\n"
      "v_mfma_f32_16x16x32_bf16 v[56:59], a[4:7], a[192:195], 0\n"
      "s_waitcnt lgkmcnt(5)\n"
      "ds_write_addtid_b32 v76 offset:0\n"
      "ds_write_addtid_b32 v77 offset:512\n"
      "v_mfma_f32_4x4x4_16b_bf16 v[20:23], a[216:217], a[224:225], 0\n"
      "v_sub_f32 v78, v90, v78\n"
      "v_mfma_f32_4x4x4_16b_bf16 v[24:27], a[216:217], a[226:227], 0\n"
      "ds_write_addtid_b32 v78 offset:1024\n"
      "v_sub_f32 v79, v91, v79\n"
      "v_mfma_f32_16x16x32_bf16 v[60:63], a[8:11], a[192:195], 0\n"
      "ds_write_addtid_b32 v79 offset:1536\n"
      "v_sub_f32 v80, v92, v80\n"
      "v_sub_f32 v81, v93, v81\n"
      "v_mfma_f32_16x16x32_bf16 v[52:55], a[12:15], a[196:199], v[52:55]\n"
      "ds_read_b128 a[212:215], v4 offset:17024\n"
      "ds_write_addtid_b32 v80 offset:2048\n"
      "v_mfma_f32_16x16x32_bf16 v[56:59], a[16:19], a[196:199], v[56:59]\n"
      "ds_write_addtid_b32 v81 offset:2560\n"
      "v_sub_f32 v82, v94, v82\n"
      "v_mfma_f32_16x16x32_bf16 v[60:63], a[20:23], a[196:199], v[60:63]\n"
      "s_nop 0\n"
      "ds_write_addtid_b32 v82 offset:3072\n"
      "v_cvt_pk_bf16_f32 v20, v20, v24\n"
      "v_mfma_f32_16x16x32_bf16 v[52:55], a[24:27], a[200:203], v[52:55]\n"
      "ds_read_b128 a[192:195], v5 offset:16896\n"
      "ds_write_addtid_b32 v20 offset:43776\n"
      "v_sub_f32 v83, v95, v83\n"
      "v_mfma_f32_16x16x32_bf16 v[56:59], a[28:31], a[200:203], v[56:59]\n"
      "ds_write_addtid_b32 v83 offset:3584\n"
      "v_sub_f32_e64 v84, v96, v84\n"
      "v_mfma_f32_16x16x32_bf16 v[60:63], a[32:35], a[200:203], v[60:63]\n"
      "s_waitcnt lgkmcnt(5)\n"
      "ds_write_addtid_b32 v84 offset:4096\n"
      "v_cvt_pk_bf16_f32 v21, v21, v25\n"
      "v_mfma_f32_16x16x32_bf16 v[52:55], a[36:39], a[204:207], v[52:55]\n"
      "ds_read_b128 a[196:199], v5 offset:16960\n"
      "ds_write_addtid_b32 v21 offset:47872\n"
      "v_mfma_f32_16x16x32_bf16 v[56:59], a[40:43], a[204:207], v[56:59]\n"
      "v_sub_f32 v85, v97, v85\n"
      "v_sub_f32 v86, v98, v86\n"
      "v_cvt_pk_bf16_f32 v22, v22, v26\n"
      "v_mfma_f32_16x16x32_bf16 v[60:63], a[44:47], a[204:207], v[60:63]\n"
      "ds_write_addtid_b32 v22 offset:51968\n"
      "ds_write_addtid_b32 v85 offset:4608\n"
      "v_sub_f32 v87, v99, v87\n"
      "v_mfma_f32_16x16x32_bf16 v[52:55], a[48:51], a[208:211], v[52:55]\n"
      "ds_read_b128 a[200:203], v5 offset:17024\n"
      "ds_write_addtid_b32 v86 offset:5120\n"
      "v_mfma_f32_16x16x32_bf16 v[56:59], a[52:55], a[208:211], v[56:59]\n"
      "s_nop 0\n"
      "ds_write_addtid_b32 v87 offset:5632\n"
      "v_cvt_pk_bf16_f32 v23, v23, v27\n"
      "v_mfma_f32_16x16x32_bf16 v[60:63], a[56:59], a[208:211], v[60:63]\n"
      "ds_write_addtid_b32 v23 offset:56064\n"
      "v_fma_f32 v40, 0.5, v76, -v88\n"
      "v_mfma_f32_16x16x32_bf16 v[52:55], a[60:63], a[212:215], v[52:55]\n"
      "v_fma_f32 v41, 0.5, v77, -v89\n"
      "v_fma_f32 v42, 0.5, v78, -v90\n"
      "v_mfma_f32_16x16x32_bf16 v[56:59], a[64:67], a[212:215], v[56:59]\n"
      "s_nop 0\n"
      "v_fma_f32 v43, 0.5, v79, -v91\n"
      "v_fma_f32 v44, 0.5, v80, -v92\n"
      "v_mfma_f32_16x16x32_bf16 v[60:63], a[68:71], a[212:215], v[60:63]\n"
      "s_waitcnt lgkmcnt(0)\n"
      "v_fma_f32 v45, 0.5, v81, -v93\n"
      "s_barrier\n"
      "s_add_u32 s55, s55, 2\n"
      "s_cmp_lt_u32 s55, 6\n"
      "s_cbranch_scc1 LSloop_%=\n"
      "LSlast_%=:\n"
      "v_mfma_f32_16x16x32_bf16 v[52:55], a[72:75], a[192:195], v[52:55]\n"
      "s_add_u32 s59, s62, 8847360\n"
      "ds_read_b128 a[204:207], v0 offset:16384\n"
      "ds_read2st64_b32 v[28:29], v6 offset0:0 offset1:2\n"
      "v_fma_f32 v46, 0.5, v82, -v94\n"
      "v_mfma_f32_16x16x32_bf16 v[56:59], a[76:79], a[192:195], v[56:59]\n"
      "ds_read_b128 a[208:211], v0 offset:16448\n"
      "ds_read2st64_b32 v[30:31], v6 offset0:4 offset1:6\n"
      "v_mfma_f32_16x16x32_bf16 v[60:63], a[80:83], a[192:195], v[60:63]\n"
      "ds_read_b64_tr_b16 a[220:221], v7 offset:33792\n"
      "v_fma_f32 v47, 0.5, v83, -v95\n"
      "v_mfma_f32_16x16x32_bf16 v[52:55], a[84:87], a[196:199], v[52:55]\n"
      "ds_read_b128 a[212:215], v0 offset:16512\n"
      "v_fma_f32 v48, 0.5, v84, -v96\n"
      "v_mfma_f32_16x16x32_bf16 v[56:59], a[88:91], a[196:199], v[56:59]\n"
      "ds_read_b64_tr_b16 a[222:223], v7 offset:33800\n"
      "ds_read2st64_b32 v[32:33], v6 offset0:8 offset1:10\n"
      "v_mfma_f32_16x16x32_bf16 v[60:63], a[92:95], a[196:199], v[60:63]\n"
      "ds_read2st64_b32 v[34:35], v6 offset0:12 offset1:14\n"
      "ds_read2st64_b32 v[36:37], v6 offset0:16 offset1:18\n"
      "v_fma_f32 v49, 0.5, v85, -v97\n"
      "v_mfma_f32_16x16x32_bf16 v[52:55], a[96:99], a[200:203], v[52:55]\n"
      "ds_read_b128 a[192:195], v1 offset:16384\n"
      "v_fma_f32 v50, 0.5, v86, -v98\n"
      "v_mfma_f32_16x16x32_bf16 v[56:59], a[100:103], a[200:203], v[56:59]\n"
      "v_fma_f32 v51, 0.5, v87, -v99\n"
      "v_mfma_f32_16x16x32_bf16 v[60:63], a[104:107], a[200:203], v[60:63]\n"
      "s_waitcnt lgkmcnt(7)\n"
      "ds_read2st64_b32 v[38:39], v6 offset0:20 offset1:22\n"
      "v_fmac_f32 v28, s46, v64\n"
      "v_fmac_f32 v29, s46, v65\n"
      "v_mfma_f32_16x16x32_bf16 v[76:79], v[252:255], a[204:207], v[52:55]\n"
      "s_waitcnt lgkmcnt(5)\n"
      "ds_read_b128 a[196:199], v1 offset:16448\n"
      "ds_read2st64_b32 a[240:241], v8 offset0:4 offset1:6\n"
      "v_mfma_f32_4x4x4_16b_bf16 v[12:15], a[216:217], a[220:221], 0\n"
      "v_fmac_f32 v30, s46, v66\n"
      "v_fmac_f32 v31, s46, v67\n"
      "v_mfma_f32_4x4x4_16b_bf16 v[16:19], a[216:217], a[222:223], 0\n"
      "ds_read2st64_b32 a[242:243], v9 offset0:4 offset1:6\n"
      "v_sub_f32 v28, v28, v40\n"
      "v_mfma_f32_16x16x32_bf16 v[80:83], v[244:247], a[204:207], v[56:59]\n"
      "v_sub_f32 v29, v29, v41\n"
      "v_sub_f32_e64 v30, v30, v42\n"
      "v_mfma_f32_16x16x32_bf16 v[84:87], v[236:239], a[204:207], v[60:63]\n"
      "v_sub_f32 v31, v31, v43\n"
      "v_cvt_pk_bf16_f32 v12, v12, v16\n"
      "v_mfma_f32_16x16x32_bf16 v[76:79], v[228:231], a[208:211], v[76:79]\n"
      "ds_read_b128 a[200:203], v1 offset:16512\n"
      "ds_write_addtid_b32 v12 offset:24832\n"
      "v_mfma_f32_16x16x32_bf16 v[80:83], v[220:223], a[208:211], v[80:83]\n"
      "v_fmac_f32 v40, s45, v28\n"
      "v_fmac_f32_e64 v41, s45, v29\n"
      "v_cvt_pk_bf16_f32 v13, v13, v17\n"
      "v_mfma_f32_16x16x32_bf16 v[84:87], v[212:215], a[208:211], v[84:87]\n"
      "ds_write_addtid_b32 v13 offset:28928\n"
      "v_fmac_f32 v42, s45, v30\n"
      "v_mfma_f32_16x16x32_bf16 v[76:79], v[204:207], a[212:215], v[76:79]\n"
      "ds_read_b128 a[204:207], v2 offset:16384\n"
      "ds_read_b64_tr_b16 a[224:225], v7 offset:34304\n"
      "v_mfma_f32_16x16x32_bf16 v[80:83], v[196:199], a[212:215], v[80:83]\n"
      "s_nop 0\n"
      "ds_read_b64_tr_b16 a[226:227], v7 offset:34312\n"
      "v_fmac_f32 v43, s45, v31\n"
      "v_mfma_f32_16x16x32_bf16 v[84:87], v[188:191], a[212:215], v[84:87]\n"
      "s_waitcnt lgkmcnt(6)\n"
      "v_cvt_pk_bf16_f32 v14, v14, v18\n"
      "v_fma_f32 v28, s47, v28, v52\n"
      "v_mfma_f32_16x16x32_bf16 v[76:79], v[180:183], a[192:195], v[76:79]\n"
      "ds_read_b128 a[208:211], v2 offset:16448\n"
      "ds_write_addtid_b32 v14 offset:33024\n"
      "v_mfma_f32_16x16x32_bf16 v[80:83], v[172:175], a[192:195], v[80:83]\n"
      "s_nop 0\n"
      "v_fma_f32 v29, s47, v29, v53\n"
      "v_cvt_pk_bf16_f32 v15, v15, v19\n"
      "v_mfma_f32_16x16x32_bf16 v[84:87], v[164:167], a[192:195], v[84:87]\n"
      "s_nop 0\n"
      "ds_write_addtid_b32 v15 offset:37120\n"
      "v_fma_f32 v30, s47, v30, v54\n"
      "v_mfma_f32_16x16x32_bf16 v[76:79], v[156:159], a[196:199], v[76:79]\n"
      "s_nop 0\n"
      "ds_read_b128 a[212:215], v2 offset:16512\n"
      "v_fma_f32 v31, s47, v31, v55\n"
      "v_mfma_f32_16x16x32_bf16 v[80:83], v[148:151], a[196:199], v[80:83]\n"
      "s_waitcnt lgkmcnt(4)\n"
      "v_cvt_pk_bf16_f32 v40, v40, v41\n"
      "v_cvt_pk_bf16_f32 v42, v42, v43\n"
      "v_mfma_f32_4x4x4_16b_bf16 v[20:23], a[216:217], a[224:225], 0\n"
      "v_cvt_pk_bf16_f32 v28, v28, v29\n"
      "v_mfma_f32_4x4x4_16b_bf16 v[24:27], a[216:217], a[226:227], 0\n"
      "ds_write_addtid_b32 v40 offset:12288\n"
      "ds_write_addtid_b32 v42 offset:12800\n"
      "v_mfma_f32_16x16x32_bf16 v[84:87], v[140:143], a[196:199], v[84:87]\n"
      "buffer_store_dwordx4 a[240:243], v11, s[8:11], s59 offen offset:256 sc1\n"
      "ds_write_addtid_b32 v28 offset:14336\n"
      "v_mfma_f32_16x16x32_bf16 v[76:79], v[132:135], a[200:203], v[76:79]\n"
      "ds_read_b128 a[192:195], v0 offset:20480\n"
      "v_cvt_pk_bf16_f32 v30, v30, v31\n"
      "v_cvt_pk_bf16_f32 v20, v20, v24\n"
      "v_mfma_f32_16x16x32_bf16 v[80:83], v[124:127], a[200:203], v[80:83]\n"
      "ds_write_addtid_b32 v30 offset:14848\n"
      "ds_write_addtid_b32 v20 offset:25344\n"
      "v_fmac_f32 v32, s46, v68\n"
      "v_mfma_f32_16x16x32_bf16 v[84:87], v[116:119], a[200:203], v[84:87]\n"
      "v_fmac_f32 v33, s46, v69\n"
      "v_cvt_pk_bf16_f32 v21, v21, v25\n"
      "v_mfma_f32_16x16x32_bf16 v[76:79], v[108:111], a[204:207], v[76:79]\n"
      "ds_read_b128 a[196:199], v0 offset:20544\n"
      "ds_read_b64_tr_b16 a[228:229], v7 offset:34816\n"
      "v_mfma_f32_16x16x32_bf16 v[80:83], v[100:103], a[204:207], v[80:83]\n"
      "ds_write_addtid_b32 v21 offset:29440\n"
      "ds_read_b64_tr_b16 a[230:231], v7 offset:34824\n"
      "v_mfma_f32_16x16x32_bf16 v[84:87], a[160:163], a[204:207], v[84:87]\n"
      "s_waitcnt lgkmcnt(6)\n"
      "v_fmac_f32 v34, s46, v70\n"
      "v_fmac_f32 v35, s46, v71\n"
      "v_mfma_f32_16x16x32_bf16 v[76:79], a[152:155], a[208:211], v[76:79]\n"
      "ds_read_b128 a[200:203], v0 offset:20608\n"
      "v_sub_f32 v32, v32, v44\n"
      "v_mfma_f32_16x16x32_bf16 v[80:83], a[144:147], a[208:211], v[80:83]\n"
      "v_sub_f32 v33, v33, v45\n"
      "v_cvt_pk_bf16_f32 v22, v22, v26\n"
      "v_mfma_f32_16x16x32_bf16 v[84:87], a[136:139], a[208:211], v[84:87]\n"
      "ds_write_addtid_b32 v22 offset:33536\n"
      "v_sub_f32 v34, v34, v46\n"
      "v_sub_f32 v35, v35, v47\n"
      "v_mfma_f32_16x16x32_bf16 v[76:79], a[128:131], a[212:215], v[76:79]\n"
      "ds_read_b128 a[204:207], v1 offset:20480\n"
      "v_cvt_pk_bf16_f32 v23, v23, v27\n"
      "v_mfma_f32_16x16x32_bf16 v[80:83], a[120:123], a[212:215], v[80:83]\n"
      "s_waitcnt lgkmcnt(3)\n"
      "ds_write_addtid_b32 v23 offset:37632\n"
      "v_fmac_f32 v44, s45, v32\n"
      "v_mfma_f32_4x4x4_16b_bf16 v[12:15], a[216:217], a[228:229], 0\n"
      "v_fmac_f32 v45, s45, v33\n"
      "v_mfma_f32_4x4x4_16b_bf16 v[16:19], a[216:217], a[230:231], 0\n"
      "v_fmac_f32 v46, s45, v34\n"
      "v_fmac_f32 v47, s45, v35\n"
      "v_mfma_f32_16x16x32_bf16 v[84:87], a[112:115], a[212:215], v[84:87]\n"
      "v_fma_f32 v32, s47, v32, v56\n"
      "v_fma_f32 v33, s47, v33, v57\n"
      "v_cvt_pk_bf16_f32 v12, v12, v16\n"
      "v_mfma_f32_16x16x32_bf16 v[88:91], v[248:251], a[192:195], v[52:55]\n"
      "ds_read_b128 a[208:211], v1 offset:20544\n"
      "ds_write_addtid_b32 v12 offset:25856\n"
      "v_mfma_f32_16x16x32_bf16 v[92:95], v[240:243], a[192:195], v[56:59]\n"
      "v_cvt_pk_bf16_f32 v13, v13, v17\n"
      "v_fma_f32 v34, s47, v34, v58\n"
      "v_mfma_f32_16x16x32_bf16 v[96:99], v[232:235], a[192:195], v[60:63]\n"
      "ds_write_addtid_b32 v13 offset:29952\n"
      "v_fma_f32 v35, s47, v35, v59\n"
      "v_mfma_f32_16x16x32_bf16 v[88:91], v[224:227], a[196:199], v[88:91]\n"
      "ds_read_b128 a[212:215], v1 offset:20608\n"
      "v_cvt_pk_bf16_f32 v14, v14, v18\n"
      "v_mfma_f32_16x16x32_bf16 v[92:95], v[216:219], a[196:199], v[92:95]\n"
      "s_nop 0\n"
      "ds_write_addtid_b32 v14 offset:34048\n"
      "v_cvt_pk_bf16_f32 v44, v44, v45\n"
      "v_mfma_f32_16x16x32_bf16 v[96:99], v[208:211], a[196:199], v[96:99]\n"
      "s_waitcnt lgkmcnt(3)\n"
      "ds_write_addtid_b32 v44 offset:20484\n"
      "v_cvt_pk_bf16_f32 v46, v46, v47\n"
      "v_mfma_f32_16x16x32_bf16 v[88:91], v[200:203], a[200:203], v[88:91]\n"
      "ds_read_b128 a[192:195], v2 offset:20480\n"
      "ds_write_addtid_b32 v46 offset:20996\n"
      "v_cvt_pk_bf16_f32 v32, v32, v33\n"
      "v_mfma_f32_16x16x32_bf16 v[92:95], v[192:195], a[200:203], v[92:95]\n"
      "ds_write_addtid_b32 v32 offset:22532\n"
      "ds_read_b64_tr_b16 a[232:233], v7 offset:35328\n"
      "v_mfma_f32_16x16x32_bf16 v[96:99], v[184:187], a[200:203], v[96:99]\n"
      "ds_read_b64_tr_b16 a[234:235], v7 offset:35336\n"
      "v_cvt_pk_bf16_f32 v34, v34, v35\n"
      "v_cvt_pk_bf16_f32 v15, v15, v19\n"
      "v_mfma_f32_16x16x32_bf16 v[88:91], v[176:179], a[204:207], v[88:91]\n"
      "ds_read_b128 a[196:199], v2 offset:20544\n"
      "ds_write_addtid_b32 v34 offset:23044\n"
      "v_mfma_f32_16x16x32_bf16 v[92:95], v[168:171], a[204:207], v[92:95]\n"
      "ds_write_addtid_b32 v15 offset:38144\n"
      "v_fmac_f32 v36, s46, v72\n"
      "v_mfma_f32_16x16x32_bf16 v[96:99], v[160:163], a[204:207], v[96:99]\n"
      "v_fmac_f32 v37, s46, v73\n"
      "v_fmac_f32 v38, s46, v74\n"
      "v_mfma_f32_16x16x32_bf16 v[88:91], v[152:155], a[208:211], v[88:91]\n"
      "ds_read_b128 a[200:203], v2 offset:20608\n"
      "v_fmac_f32 v39, s46, v75\n"
      "v_mfma_f32_16x16x32_bf16 v[92:95], v[144:147], a[208:211], v[92:95]\n"
      "s_waitcnt lgkmcnt(4)\n"
      "v_sub_f32 v36, v36, v48\n"
      "v_sub_f32 v37, v37, v49\n"
      "v_mfma_f32_4x4x4_16b_bf16 v[20:23], a[216:217], a[232:233], 0\n"
      "v_sub_f32 v38, v38, v50\n"
      "v_mfma_f32_4x4x4_16b_bf16 v[24:27], a[216:217], a[234:235], 0\n"
      "v_sub_f32 v39, v39, v51\n"
      "v_fmac_f32 v48, s45, v36\n"
      "v_mfma_f32_16x16x32_bf16 v[96:99], v[136:139], a[208:211], v[96:99]\n"
      "v_fmac_f32 v49, s45, v37\n"
      "v_fmac_f32 v50, s45, v38\n"
      "v_cvt_pk_bf16_f32 v20, v20, v24\n"
      "v_mfma_f32_16x16x32_bf16 v[88:91], v[128:131], a[212:215], v[88:91]\n"
      "ds_read_b128 a[204:207], v3 offset:33792\n"
      "ds_write_addtid_b32 v20 offset:26368\n"
      "v_mfma_f32_16x16x32_bf16 v[92:95], v[120:123], a[212:215], v[92:95]\n"
      "v_cvt_pk_bf16_f32 v21, v21, v25\n"
      "v_fmac_f32 v51, s45, v39\n"
      "v_mfma_f32_16x16x32_bf16 v[96:99], v[112:115], a[212:215], v[96:99]\n"
      "s_nop 0\n"
      "ds_write_addtid_b32 v21 offset:30464\n"
      "v_fma_f32 v36, s47, v36, v60\n"
      "v_mfma_f32_16x16x32_bf16 v[88:91], v[104:107], a[192:195], v[88:91]\n"
      "ds_read_b128 a[208:211], v3 offset:33856\n"
      "v_fma_f32 v37, s47, v37, v61\n"
      "v_mfma_f32_16x16x32_bf16 v[92:95], a[164:167], a[192:195], v[92:95]\n"
      "ds_read_b64_tr_b16 a[220:221], v7 offset:35840\n"
      "ds_read_b64_tr_b16 a[222:223], v7 offset:35848\n"
      "v_cvt_pk_bf16_f32 v22, v22, v26\n"
      "v_mfma_f32_16x16x32_bf16 v[96:99], a[156:159], a[192:195], v[96:99]\n"
      "s_waitcnt lgkmcnt(4)\n"
      "ds_write_addtid_b32 v22 offset:34560\n"
      "v_fma_f32 v38, s47, v38, v62\n"
      "v_mfma_f32_16x16x32_bf16 v[88:91], a[148:151], a[196:199], v[88:91]\n"
      "ds_read_b128 a[212:215], v3 offset:33920\n"
      "v_fma_f32 v39, s47, v39, v63\n"
      "v_mfma_f32_16x16x32_bf16 v[92:95], a[140:143], a[196:199], v[92:95]\n"
      "s_nop 0\n"
      "v_cvt_pk_bf16_f32 v23, v23, v27\n"
      "v_cvt_pk_bf16_f32 v48, v48, v49\n"
      "v_mfma_f32_16x16x32_bf16 v[96:99], a[132:135], a[196:199], v[96:99]\n"
      "ds_write_addtid_b32 v48 offset:16384\n"
      "ds_write_addtid_b32 v23 offset:38656\n"
      "v_mfma_f32_16x16x32_bf16 v[88:91], a[124:127], a[200:203], v[88:91]\n"
      "ds_read_b128 a[192:195], v4 offset:33792\n"
      "v_cvt_pk_bf16_f32 v50, v50, v51\n"
      "v_mfma_f32_16x16x32_bf16 v[92:95], a[116:119], a[200:203], v[92:95]\n"
      "ds_write_addtid_b32 v50 offset:16896\n"
      "v_cvt_pk_bf16_f32 v36, v36, v37\n"
      "v_mfma_f32_16x16x32_bf16 v[96:99], a[108:111], a[200:203], v[96:99]\n"
      "s_waitcnt lgkmcnt(4)\n"
      "ds_write_addtid_b32 v36 offset:18432\n"
      "v_sub_f32 v76, v88, v76\n"
      "v_sub_f32 v77, v89, v77\n"
      "v_mfma_f32_4x4x4_16b_bf16 v[12:15], a[216:217], a[220:221], 0\n"
      "v_sub_f32 v78, v90, v78\n"
      "v_cvt_pk_bf16_f32 v38, v38, v39\n"
      "v_mfma_f32_4x4x4_16b_bf16 v[16:19], a[216:217], a[222:223], 0\n"
      "ds_write_addtid_b32 v38 offset:18944\n"
      "v_sub_f32 v79, v91, v79\n"
      "v_sub_f32 v80, v92, v80\n"
      "v_mfma_f32_16x16x32_bf16 v[64:67], a[0:3], a[204:207], 0\n"
      "ds_read_b128 a[196:199], v4 offset:33856\n"
      "ds_write_addtid_b32 v76 offset:6144\n"
      "v_mfma_f32_16x16x32_bf16 v[68:71], a[4:7], a[204:207], 0\n"
      "ds_write_addtid_b32 v77 offset:6656\n"
      "ds_write_addtid_b32 v78 offset:7168\n"
      "v_mfma_f32_16x16x32_bf16 v[72:75], a[8:11], a[204:207], 0\n"
      "ds_write_addtid_b32 v79 offset:7680\n"
      "ds_write_addtid_b32 v80 offset:8192\n"
      "v_mfma_f32_16x16x32_bf16 v[64:67], a[12:15], a[208:211], v[64:67]\n"
      "ds_read_b128 a[200:203], v4 offset:33920\n"
      "v_sub_f32 v81, v93, v81\n"
      "v_mfma_f32_16x16x32_bf16 v[68:71], a[16:19], a[208:211], v[68:71]\n"
      "s_nop 0\n"
      "ds_write_addtid_b32 v81 offset:8704\n"
      "v_sub_f32 v82, v94, v82\n"
      "v_cvt_pk_bf16_f32 v12, v12, v16\n"
      "v_mfma_f32_16x16x32_bf16 v[72:75], a[20:23], a[208:211], v[72:75]\n"
      "ds_write_addtid_b32 v12 offset:26880\n"
      "ds_write_addtid_b32 v82 offset:9216\n"
      "v_mfma_f32_16x16x32_bf16 v[64:67], a[24:27], a[212:215], v[64:67]\n"
      "ds_read_b128 a[204:207], v5 offset:33792\n"
      "v_sub_f32 v83, v95, v83\n"
      "v_mfma_f32_16x16x32_bf16 v[68:71], a[28:31], a[212:215], v[68:71]\n"
      "ds_write_addtid_b32 v83 offset:9728\n"
      "v_sub_f32 v84, v96, v84\n"
      "v_mfma_f32_16x16x32_bf16 v[72:75], a[32:35], a[212:215], v[72:75]\n"
      "s_waitcnt lgkmcnt(5)\n"
      "ds_write_addtid_b32 v84 offset:10240\n"
      "v_cvt_pk_bf16_f32 v13, v13, v17\n"
      "v_sub_f32 v85, v97, v85\n"
      "v_mfma_f32_16x16x32_bf16 v[64:67], a[36:39], a[192:195], v[64:67]\n"
      "s_nop 0\n"
      "ds_read_b128 a[208:211], v5 offset:33856\n"
      "ds_write_addtid_b32 v13 offset:30976\n"
      "v_mfma_f32_16x16x32_bf16 v[68:71], a[40:43], a[192:195], v[68:71]\n"
      "ds_write_addtid_b32 v85 offset:10752\n"
      "v_cvt_pk_bf16_f32 v14, v14, v18\n"
      "v_mfma_f32_16x16x32_bf16 v[72:75], a[44:47], a[192:195], v[72:75]\n"
      "ds_write_addtid_b32 v14 offset:35072\n"
      "v_sub_f32 v86, v98, v86\n"
      "v_sub_f32 v87, v99, v87\n"
      "v_mfma_f32_16x16x32_bf16 v[64:67], a[48:51], a[196:199], v[64:67]\n"
      "ds_read_b128 a[212:215], v5 offset:33920\n"
      "ds_write_addtid_b32 v86 offset:11264\n"
      "v_cvt_pk_bf16_f32 v15, v15, v19\n"
      "ds_write_addtid_b32 v15 offset:39168\n"
      "v_mfma_f32_16x16x32_bf16 v[68:71], a[52:55], a[196:199], v[68:71]\n"
      "ds_write_addtid_b32 v87 offset:11776\n"
      "v_fma_f32 v40, 0.5, v76, -v88\n"
      "v_mfma_f32_16x16x32_bf16 v[72:75], a[56:59], a[196:199], v[72:75]\n"
      "v_fma_f32 v41, 0.5, v77, -v89\n"
      "v_fma_f32 v42, 0.5, v78, -v90\n"
      "v_mfma_f32_16x16x32_bf16 v[64:67], a[60:63], a[200:203], v[64:67]\n"
      "v_fma_f32 v43, 0.5, v79, -v91\n"
      "v_fma_f32 v44, 0.5, v80, -v92\n"
      "v_mfma_f32_16x16x32_bf16 v[68:71], a[64:67], a[200:203], v[68:71]\n"
      "v_fma_f32 v45, 0.5, v81, -v93\n"
      "v_mfma_f32_16x16x32_bf16 v[72:75], a[68:71], a[200:203], v[72:75]\n"
      "s_waitcnt lgkmcnt(0)\n"
      "s_barrier\n"
      "v_mfma_f32_16x16x32_bf16 v[64:67], a[72:75], a[204:207], v[64:67]\n"
      "s_add_u32 s59, s62, 9633792\n"
      "ds_read_b128 a[192:195], v0 offset:0\n"
      "ds_read2st64_b32 v[28:29], v6 offset0:24 offset1:26\n"
      "v_fma_f32 v46, 0.5, v82, -v94\n"
      "v_mfma_f32_16x16x32_bf16 v[68:71], a[76:79], a[204:207], v[68:71]\n"
      "ds_read_b128 a[196:199], v0 offset:64\n"
      "ds_read2st64_b32 v[30:31], v6 offset0:28 offset1:30\n"
      "v_mfma_f32_16x16x32_bf16 v[72:75], a[80:83], a[204:207], v[72:75]\n"
      "ds_read_b64_tr_b16 a[220:221], v7 offset:50688\n"
      "v_fma_f32 v47, 0.5, v83, -v95\n"
      "v_mfma_f32_16x16x32_bf16 v[64:67], a[84:87], a[208:211], v[64:67]\n"
      "ds_read_b128 a[200:203], v0 offset:128\n"
      "v_fma_f32 v48, 0.5, v84, -v96\n"
      "v_mfma_f32_16x16x32_bf16 v[68:71], a[88:91], a[208:211], v[68:71]\n"
      "ds_read_b64_tr_b16 a[222:223], v7 offset:50696\n"
      "ds_read2st64_b32 v[32:33], v6 offset0:32 offset1:34\n"
      "v_mfma_f32_16x16x32_bf16 v[72:75], a[92:95], a[208:211], v[72:75]\n"
      "ds_read2st64_b32 v[34:35], v6 offset0:36 offset1:38\n"
      "ds_read2st64_b32 v[36:37], v6 offset0:40 offset1:42\n"
      "v_fma_f32 v49, 0.5, v85, -v97\n"
      "v_mfma_f32_16x16x32_bf16 v[64:67], a[96:99], a[212:215], v[64:67]\n"
      "ds_read_b128 a[204:207], v1 offset:0\n"
      "v_fma_f32 v50, 0.5, v86, -v98\n"
      "v_mfma_f32_16x16x32_bf16 v[68:71], a[100:103], a[212:215], v[68:71]\n"
      "v_fma_f32 v51, 0.5, v87, -v99\n"
      "v_mfma_f32_16x16x32_bf16 v[72:75], a[104:107], a[212:215], v[72:75]\n"
      "s_waitcnt lgkmcnt(7)\n"
      "ds_read2st64_b32 v[38:39], v6 offset0:44 offset1:46\n"
      "v_fmac_f32 v28, s46, v52\n"
      "v_fmac_f32_e64 v29, s46, v53\n"
      "v_mfma_f32_16x16x32_bf16 v[76:79], v[252:255], a[192:195], v[64:67]\n"
      "s_waitcnt lgkmcnt(5)\n"
      "ds_read_b128 a[208:211], v1 offset:64\n"
      "ds_read2st64_b32 a[240:241], v8 offset0:0 offset1:2\n"
      "v_mfma_f32_4x4x4_16b_bf16 v[12:15], a[216:217], a[220:221], 0\n"
      "v_fmac_f32 v30, s46, v54\n"
      "v_fmac_f32 v31, s46, v55\n"
      "v_mfma_f32_4x4x4_16b_bf16 v[16:19], a[216:217], a[222:223], 0\n"
      "ds_read2st64_b32 a[242:243], v9 offset0:0 offset1:2\n"
      "v_sub_f32 v28, v28, v40\n"
      "v_mfma_f32_16x16x32_bf16 v[80:83], v[244:247], a[192:195], v[68:71]\n"
      "v_sub_f32 v29, v29, v41\n"
      "v_sub_f32 v30, v30, v42\n"
      "v_mfma_f32_16x16x32_bf16 v[84:87], v[236:239], a[192:195], v[72:75]\n"
      "v_sub_f32 v31, v31, v43\n"
      "v_cvt_pk_bf16_f32 v12, v12, v16\n"
      "v_mfma_f32_16x16x32_bf16 v[76:79], v[228:231], a[196:199], v[76:79]\n"
      "s_nop 0\n"
      "ds_read_b128 a[212:215], v1 offset:128\n"
      "ds_write_addtid_b32 v12 offset:41216\n"
      "v_mfma_f32_16x16x32_bf16 v[80:83], v[220:223], a[196:199], v[80:83]\n"
      "v_fmac_f32 v40, s45, v28\n"
      "v_fmac_f32 v41, s45, v29\n"
      "v_cvt_pk_bf16_f32 v13, v13, v17\n"
      "v_mfma_f32_16x16x32_bf16 v[84:87], v[212:215], a[196:199], v[84:87]\n"
      "ds_write_addtid_b32 v13 offset:45312\n"
      "v_fmac_f32 v42, s45, v30\n"
      "v_mfma_f32_16x16x32_bf16 v[76:79], v[204:207], a[200:203], v[76:79]\n"
      "s_nop 0\n"
      "ds_read_b128 a[192:195], v2 offset:0\n"
      "ds_read_b64_tr_b16 a[224:225], v7 offset:51200\n"
      "v_mfma_f32_16x16x32_bf16 v[80:83], v[196:199], a[200:203], v[80:83]\n"
      "ds_read_b64_tr_b16 a[226:227], v7 offset:51208\n"
      "v_fmac_f32 v43, s45, v31\n"
      "v_mfma_f32_16x16x32_bf16 v[84:87], v[188:191], a[200:203], v[84:87]\n"
      "s_waitcnt lgkmcnt(6)\n"
      "v_cvt_pk_bf16_f32 v14, v14, v18\n"
      "v_fma_f32 v28, s47, v28, v64\n"
      "v_mfma_f32_16x16x32_bf16 v[76:79], v[180:183], a[204:207], v[76:79]\n"
      "ds_read_b128 a[196:199], v2 offset:64\n"
      "ds_write_addtid_b32 v14 offset:49408\n"
      "v_mfma_f32_16x16x32_bf16 v[80:83], v[172:175], a[204:207], v[80:83]\n"
      "v_fma_f32 v29, s47, v29, v65\n"
      "v_cvt_pk_bf16_f32 v15, v15, v19\n"
      "v_mfma_f32_16x16x32_bf16 v[84:87], v[164:167], a[204:207], v[84:87]\n"
      "ds_write_addtid_b32 v15 offset:53504\n"
      "v_fma_f32 v30, s47, v30, v66\n"
      "v_mfma_f32_16x16x32_bf16 v[76:79], v[156:159], a[208:211], v[76:79]\n"
      "s_nop 0\n"
      "ds_read_b128 a[200:203], v2 offset:128\n"
      "v_fma_f32 v31, s47, v31, v67\n"
      "v_mfma_f32_16x16x32_bf16 v[80:83], v[148:151], a[208:211], v[80:83]\n"
      "s_waitcnt lgkmcnt(4)\n"
      "v_cvt_pk_bf16_f32 v40, v40, v41\n"
      "v_cvt_pk_bf16_f32 v42, v42, v43\n"
      "v_mfma_f32_4x4x4_16b_bf16 v[20:23], a[216:217], a[224:225], 0\n"
      "v_cvt_pk_bf16_f32 v28, v28, v29\n"
      "v_mfma_f32_4x4x4_16b_bf16 v[24:27], a[216:217], a[226:227], 0\n"
      "ds_write_addtid_b32 v40 offset:13312\n"
      "ds_write_addtid_b32 v42 offset:13824\n"
      "v_mfma_f32_16x16x32_bf16 v[84:87], v[140:143], a[208:211], v[84:87]\n"
      "buffer_store_dwordx4 a[240:243], v11, s[8:11], s59 offen offset:256 sc1\n"
      "ds_write_addtid_b32 v28 offset:15360\n"
      "v_mfma_f32_16x16x32_bf16 v[76:79], v[132:135], a[212:215], v[76:79]\n"
      "ds_read_b128 a[204:207], v0 offset:4096\n"
      "v_cvt_pk_bf16_f32 v30, v30, v31\n"
      "v_cvt_pk_bf16_f32 v20, v20, v24\n"
      "v_mfma_f32_16x16x32_bf16 v[80:83], v[124:127], a[212:215], v[80:83]\n"
      "ds_write_addtid_b32 v30 offset:15872\n"
      "ds_write_addtid_b32 v20 offset:41728\n"
      "v_fmac_f32 v32, s46, v56\n"
      "v_mfma_f32_16x16x32_bf16 v[84:87], v[116:119], a[212:215], v[84:87]\n"
      "v_fmac_f32 v33, s46, v57\n"
      "v_cvt_pk_bf16_f32 v21, v21, v25\n"
      "v_mfma_f32_16x16x32_bf16 v[76:79], v[108:111], a[192:195], v[76:79]\n"
      "ds_read_b128 a[208:211], v0 offset:4160\n"
      "ds_read_b64_tr_b16 a[228:229], v7 offset:51712\n"
      "v_mfma_f32_16x16x32_bf16 v[80:83], v[100:103], a[192:195], v[80:83]\n"
      "ds_write_addtid_b32 v21 offset:45824\n"
      "ds_read_b64_tr_b16 a[230:231], v7 offset:51720\n"
      "v_mfma_f32_16x16x32_bf16 v[84:87], a[160:163], a[192:195], v[84:87]\n"
      "s_waitcnt lgkmcnt(6)\n"
      "v_fmac_f32 v34, s46, v58\n"
      "v_fmac_f32 v35, s46, v59\n"
      "v_mfma_f32_16x16x32_bf16 v[76:79], a[152:155], a[196:199], v[76:79]\n"
      "ds_read_b128 a[212:215], v0 offset:4224\n"
      "v_sub_f32 v32, v32, v44\n"
      "v_mfma_f32_16x16x32_bf16 v[80:83], a[144:147], a[196:199], v[80:83]\n"
      "v_sub_f32 v33, v33, v45\n"
      "v_cvt_pk_bf16_f32 v22, v22, v26\n"
      "v_mfma_f32_16x16x32_bf16 v[84:87], a[136:139], a[196:199], v[84:87]\n"
      "s_nop 0\n"
      "ds_write_addtid_b32 v22 offset:49920\n"
      "v_sub_f32 v34, v34, v46\n"
      "v_sub_f32 v35, v35, v47\n"
      "v_mfma_f32_16x16x32_bf16 v[76:79], a[128:131], a[200:203], v[76:79]\n"
      "ds_read_b128 a[192:195], v1 offset:4096\n"
      "v_cvt_pk_bf16_f32 v23, v23, v27\n"
      "v_mfma_f32_16x16x32_bf16 v[80:83], a[120:123], a[200:203], v[80:83]\n"
      "s_waitcnt lgkmcnt(3)\n"
      "ds_write_addtid_b32 v23 offset:54016\n"
      "v_fmac_f32 v44, s45, v32\n"
      "v_mfma_f32_4x4x4_16b_bf16 v[12:15], a[216:217], a[228:229], 0\n"
      "v_fmac_f32 v45, s45, v33\n"
      "v_mfma_f32_4x4x4_16b_bf16 v[16:19], a[216:217], a[230:231], 0\n"
      "v_fmac_f32 v46, s45, v34\n"
      "v_fmac_f32 v47, s45, v35\n"
      "v_mfma_f32_16x16x32_bf16 v[84:87], a[112:115], a[200:203], v[84:87]\n"
      "s_nop 0\n"
      "v_fma_f32 v32, s47, v32, v68\n"
      "v_fma_f32 v33, s47, v33, v69\n"
      "v_cvt_pk_bf16_f32 v12, v12, v16\n"
      "v_mfma_f32_16x16x32_bf16 v[88:91], v[248:251], a[204:207], v[64:67]\n"
      "ds_read_b128 a[196:199], v1 offset:4160\n"
      "ds_write_addtid_b32 v12 offset:42240\n"
      "v_mfma_f32_16x16x32_bf16 v[92:95], v[240:243], a[204:207], v[68:71]\n"
      "v_cvt_pk_bf16_f32 v13, v13, v17\n"
      "v_fma_f32 v34, s47, v34, v70\n"
      "v_mfma_f32_16x16x32_bf16 v[96:99], v[232:235], a[204:207], v[72:75]\n"
      "ds_write_addtid_b32 v13 offset:46336\n"
      "v_fma_f32 v35, s47, v35, v71\n"
      "v_mfma_f32_16x16x32_bf16 v[88:91], v[224:227], a[208:211], v[88:91]\n"
      "ds_read_b128 a[200:203], v1 offset:4224\n"
      "v_cvt_pk_bf16_f32 v14, v14, v18\n"
      "v_mfma_f32_16x16x32_bf16 v[92:95], v[216:219], a[208:211], v[92:95]\n"
      "ds_write_addtid_b32 v14 offset:50432\n"
      "v_cvt_pk_bf16_f32 v44, v44, v45\n"
      "v_mfma_f32_16x16x32_bf16 v[96:99], v[208:211], a[208:211], v[96:99]\n"
      "s_waitcnt lgkmcnt(3)\n"
      "ds_write_addtid_b32 v44 offset:21508\n"
      "v_cvt_pk_bf16_f32 v46, v46, v47\n"
      "v_mfma_f32_16x16x32_bf16 v[88:91], v[200:203], a[212:215], v[88:91]\n"
      "ds_read_b128 a[204:207], v2 offset:4096\n"
      "ds_write_addtid_b32 v46 offset:22020\n"
      "v_cvt_pk_bf16_f32 v32, v32, v33\n"
      "v_mfma_f32_16x16x32_bf16 v[92:95], v[192:195], a[212:215], v[92:95]\n"
      "s_nop 0\n"
      "ds_write_addtid_b32 v32 offset:23556\n"
      "ds_read_b64_tr_b16 a[232:233], v7 offset:52224\n"
      "v_mfma_f32_16x16x32_bf16 v[96:99], v[184:187], a[212:215], v[96:99]\n"
      "ds_read_b64_tr_b16 a[234:235], v7 offset:52232\n"
      "v_cvt_pk_bf16_f32 v34, v34, v35\n"
      "v_cvt_pk_bf16_f32 v15, v15, v19\n"
      "v_mfma_f32_16x16x32_bf16 v[88:91], v[176:179], a[192:195], v[88:91]\n"
      "ds_read_b128 a[208:211], v2 offset:4160\n"
      "ds_write_addtid_b32 v34 offset:24068\n"
      "v_mfma_f32_16x16x32_bf16 v[92:95], v[168:171], a[192:195], v[92:95]\n"
      "ds_write_addtid_b32 v15 offset:54528\n"
      "v_fmac_f32 v36, s46, v60\n"
      "v_mfma_f32_16x16x32_bf16 v[96:99], v[160:163], a[192:195], v[96:99]\n"
      "v_fmac_f32 v37, s46, v61\n"
      "v_fmac_f32 v38, s46, v62\n"
      "v_mfma_f32_16x16x32_bf16 v[88:91], v[152:155], a[196:199], v[88:91]\n"
      "ds_read_b128 a[212:215], v2 offset:4224\n"
      "v_fmac_f32 v39, s46, v63\n"
      "v_mfma_f32_16x16x32_bf16 v[92:95], v[144:147], a[196:199], v[92:95]\n"
      "s_waitcnt lgkmcnt(4)\n"
      "v_sub_f32 v36, v36, v48\n"
      "v_sub_f32 v37, v37, v49\n"
      "v_mfma_f32_4x4x4_16b_bf16 v[20:23], a[216:217], a[232:233], 0\n"
      "v_sub_f32 v38, v38, v50\n"
      "v_mfma_f32_4x4x4_16b_bf16 v[24:27], a[216:217], a[234:235], 0\n"
      "v_sub_f32 v39, v39, v51\n"
      "v_fmac_f32 v48, s45, v36\n"
      "v_mfma_f32_16x16x32_bf16 v[96:99], v[136:139], a[196:199], v[96:99]\n"
      "v_fmac_f32 v49, s45, v37\n"
      "v_fmac_f32 v50, s45, v38\n"
      "v_cvt_pk_bf16_f32 v20, v20, v24\n"
      "v_mfma_f32_16x16x32_bf16 v[88:91], v[128:131], a[200:203], v[88:91]\n"
      "ds_read_b128 a[192:195], v3 offset:50688\n"
      "ds_write_addtid_b32 v20 offset:42752\n"
      "v_mfma_f32_16x16x32_bf16 v[92:95], v[120:123], a[200:203], v[92:95]\n"
      "v_cvt_pk_bf16_f32 v21, v21, v25\n"
      "v_fmac_f32 v51, s45, v39\n"
      "v_mfma_f32_16x16x32_bf16 v[96:99], v[112:115], a[200:203], v[96:99]\n"
      "s_nop 0\n"
      "ds_write_addtid_b32 v21 offset:46848\n"
      "v_fma_f32 v36, s47, v36, v72\n"
      "v_mfma_f32_16x16x32_bf16 v[88:91], v[104:107], a[204:207], v[88:91]\n"
      "ds_read_b128 a[196:199], v3 offset:50752\n"
      "v_fma_f32 v37, s47, v37, v73\n"
      "v_mfma_f32_16x16x32_bf16 v[92:95], a[164:167], a[204:207], v[92:95]\n"
      "ds_read_b64_tr_b16 a[220:221], v7 offset:52736\n"
      "ds_read_b64_tr_b16 a[222:223], v7 offset:52744\n"
      "v_cvt_pk_bf16_f32 v22, v22, v26\n"
      "v_mfma_f32_16x16x32_bf16 v[96:99], a[156:159], a[204:207], v[96:99]\n"
      "s_waitcnt lgkmcnt(4)\n"
      "ds_write_addtid_b32 v22 offset:50944\n"
      "v_fma_f32 v38, s47, v38, v74\n"
      "v_mfma_f32_16x16x32_bf16 v[88:91], a[148:151], a[208:211], v[88:91]\n"
      "s_nop 0\n"
      "ds_read_b128 a[200:203], v3 offset:50816\n"
      "v_fma_f32 v39, s47, v39, v75\n"
      "v_mfma_f32_16x16x32_bf16 v[92:95], a[140:143], a[208:211], v[92:95]\n"
      "v_cvt_pk_bf16_f32 v23, v23, v27\n"
      "v_cvt_pk_bf16_f32 v48, v48, v49\n"
      "v_mfma_f32_16x16x32_bf16 v[96:99], a[132:135], a[208:211], v[96:99]\n"
      "ds_write_addtid_b32 v48 offset:17408\n"
      "ds_write_addtid_b32 v23 offset:55040\n"
      "v_mfma_f32_16x16x32_bf16 v[88:91], a[124:127], a[212:215], v[88:91]\n"
      "ds_read_b128 a[204:207], v4 offset:50688\n"
      "v_cvt_pk_bf16_f32 v50, v50, v51\n"
      "v_mfma_f32_16x16x32_bf16 v[92:95], a[116:119], a[212:215], v[92:95]\n"
      "ds_write_addtid_b32 v50 offset:17920\n"
      "v_cvt_pk_bf16_f32 v36, v36, v37\n"
      "v_mfma_f32_16x16x32_bf16 v[96:99], a[108:111], a[212:215], v[96:99]\n"
      "s_waitcnt lgkmcnt(4)\n"
      "ds_write_addtid_b32 v36 offset:19456\n"
      "v_sub_f32 v76, v88, v76\n"
      "v_sub_f32_e64 v77, v89, v77\n"
      "v_mfma_f32_4x4x4_16b_bf16 v[12:15], a[216:217], a[220:221], 0\n"
      "v_sub_f32 v78, v90, v78\n"
      "v_cvt_pk_bf16_f32 v38, v38, v39\n"
      "v_mfma_f32_4x4x4_16b_bf16 v[16:19], a[216:217], a[222:223], 0\n"
      "ds_write_addtid_b32 v38 offset:19968\n"
      "v_sub_f32 v79, v91, v79\n"
      "v_sub_f32 v80, v92, v80\n"
      "v_mfma_f32_16x16x32_bf16 v[52:55], a[0:3], a[192:195], 0\n"
      "s_nop 0\n"
      "ds_read_b128 a[208:211], v4 offset:50752\n"
      "ds_write_addtid_b32 v76 offset:0\n"
      "v_mfma_f32_16x16x32_bf16 v[56:59], a[4:7], a[192:195], 0\n"
      "ds_write_addtid_b32 v77 offset:512\n"
      "ds_write_addtid_b32 v78 offset:1024\n"
      "v_mfma_f32_16x16x32_bf16 v[60:63], a[8:11], a[192:195], 0\n"
      "ds_write_addtid_b32 v79 offset:1536\n"
      "ds_write_addtid_b32 v80 offset:2048\n"
      "v_mfma_f32_16x16x32_bf16 v[52:55], a[12:15], a[196:199], v[52:55]\n"
      "ds_read_b128 a[212:215], v4 offset:50816\n"
      "v_sub_f32 v81, v93, v81\n"
      "v_mfma_f32_16x16x32_bf16 v[56:59], a[16:19], a[196:199], v[56:59]\n"
      "ds_write_addtid_b32 v81 offset:2560\n"
      "v_sub_f32 v82, v94, v82\n"
      "v_cvt_pk_bf16_f32 v12, v12, v16\n"
      "v_mfma_f32_16x16x32_bf16 v[60:63], a[20:23], a[196:199], v[60:63]\n"
      "ds_write_addtid_b32 v12 offset:43264\n"
      "ds_write_addtid_b32 v82 offset:3072\n"
      "v_mfma_f32_16x16x32_bf16 v[52:55], a[24:27], a[200:203], v[52:55]\n"
      "ds_read_b128 a[192:195], v5 offset:50688\n"
      "v_sub_f32 v83, v95, v83\n"
      "v_mfma_f32_16x16x32_bf16 v[56:59], a[28:31], a[200:203], v[56:59]\n"
      "ds_write_addtid_b32 v83 offset:3584\n"
      "v_sub_f32_e64 v84, v96, v84\n"
      "v_mfma_f32_16x16x32_bf16 v[60:63], a[32:35], a[200:203], v[60:63]\n"
      "s_waitcnt lgkmcnt(5)\n"
      "ds_write_addtid_b32 v84 offset:4096\n"
      "v_cvt_pk_bf16_f32 v13, v13, v17\n"
      "v_sub_f32 v85, v97, v85\n"
      "v_mfma_f32_16x16x32_bf16 v[52:55], a[36:39], a[204:207], v[52:55]\n"
      "ds_read_b128 a[196:199], v5 offset:50752\n"
      "ds_write_addtid_b32 v13 offset:47360\n"
      "v_mfma_f32_16x16x32_bf16 v[56:59], a[40:43], a[204:207], v[56:59]\n"
      "s_nop 0\n"
      "ds_write_addtid_b32 v85 offset:4608\n"
      "v_cvt_pk_bf16_f32 v14, v14, v18\n"
      "v_mfma_f32_16x16x32_bf16 v[60:63], a[44:47], a[204:207], v[60:63]\n"
      "ds_write_addtid_b32 v14 offset:51456\n"
      "v_sub_f32 v86, v98, v86\n"
      "v_sub_f32 v87, v99, v87\n"
      "v_mfma_f32_16x16x32_bf16 v[52:55], a[48:51], a[208:211], v[52:55]\n"
      "ds_read_b128 a[200:203], v5 offset:50816\n"
      "ds_write_addtid_b32 v86 offset:5120\n"
      "v_cvt_pk_bf16_f32 v15, v15, v19\n"
      "ds_write_addtid_b32 v15 offset:55552\n"
      "v_mfma_f32_16x16x32_bf16 v[56:59], a[52:55], a[208:211], v[56:59]\n"
      "ds_write_addtid_b32 v87 offset:5632\n"
      "v_fma_f32 v40, 0.5, v76, -v88\n"
      "v_mfma_f32_16x16x32_bf16 v[60:63], a[56:59], a[208:211], v[60:63]\n"
      "v_fma_f32 v41, 0.5, v77, -v89\n"
      "v_fma_f32 v42, 0.5, v78, -v90\n"
      "v_mfma_f32_16x16x32_bf16 v[52:55], a[60:63], a[212:215], v[52:55]\n"
      "v_fma_f32 v43, 0.5, v79, -v91\n"
      "v_fma_f32 v44, 0.5, v80, -v92\n"
      "v_mfma_f32_16x16x32_bf16 v[56:59], a[64:67], a[212:215], v[56:59]\n"
      "v_fma_f32 v45, 0.5, v81, -v93\n"
      "v_mfma_f32_16x16x32_bf16 v[60:63], a[68:71], a[212:215], v[60:63]\n"
      "s_waitcnt lgkmcnt(0)\n"
      "s_barrier\n"
      "v_mfma_f32_16x16x32_bf16 v[52:55], a[72:75], a[192:195], v[52:55]\n"
      "s_add_u32 s59, s62, 10420224\n"
      "ds_read_b128 a[204:207], v0 offset:16384\n"
      "ds_read2st64_b32 v[28:29], v6 offset0:0 offset1:2\n"
      "v_fma_f32 v46, 0.5, v82, -v94\n"
      "v_mfma_f32_16x16x32_bf16 v[56:59], a[76:79], a[192:195], v[56:59]\n"
      "ds_read_b128 a[208:211], v0 offset:16448\n"
      "ds_read2st64_b32 v[30:31], v6 offset0:4 offset1:6\n"
      "v_mfma_f32_16x16x32_bf16 v[60:63], a[80:83], a[192:195], v[60:63]\n"
      "ds_read2st64_b32 v[32:33], v6 offset0:8 offset1:10\n"
      "v_fma_f32 v47, 0.5, v83, -v95\n"
      "v_mfma_f32_16x16x32_bf16 v[52:55], a[84:87], a[196:199], v[52:55]\n"
      "s_nop 0\n"
      "ds_read_b128 a[212:215], v0 offset:16512\n"
      "v_fma_f32 v48, 0.5, v84, -v96\n"
      "v_mfma_f32_16x16x32_bf16 v[56:59], a[88:91], a[196:199], v[56:59]\n"
      "s_nop 0\n"
      "ds_read2st64_b32 v[34:35], v6 offset0:12 offset1:14\n"
      "ds_read2st64_b32 v[36:37], v6 offset0:16 offset1:18\n"
      "v_mfma_f32_16x16x32_bf16 v[60:63], a[92:95], a[196:199], v[60:63]\n"
      "s_nop 0\n"
      "v_bfe_u32 v16, v11, 6, 1\n"
      "v_mul_i32_i24 v17, 12296, v16\n"
      "s_mov_b32 s50, -8200\n"
      "v_add3_u32 v12, v8, v17, s50\n"
      "v_add_u32 v14, 128, v12\n"
      "v_mul_i32_i24 v17, 8, v16\n"
      "s_mov_b32 s50, -4104\n"
      "v_add3_u32 v13, v8, v17, s50\n"
      "v_add_u32 v15, 128, v13\n"
      "v_mfma_f32_16x16x32_bf16 v[52:55], a[96:99], a[200:203], v[52:55]\n"
      "ds_read_b128 a[192:195], v1 offset:16384\n"
      "ds_read2st64_b32 v[38:39], v6 offset0:20 offset1:22\n"
      "v_mfma_f32_16x16x32_bf16 v[56:59], a[100:103], a[200:203], v[56:59]\n"
      "v_fma_f32 v49, 0.5, v85, -v97\n"
      "v_fma_f32 v50, 0.5, v86, -v98\n"
      "v_fma_f32 v51, 0.5, v87, -v99\n"
      "v_mfma_f32_16x16x32_bf16 v[60:63], a[104:107], a[200:203], v[60:63]\n"
      "s_waitcnt lgkmcnt(5)\n"
      "v_fmac_f32 v28, s46, v64\n"
      "v_fmac_f32 v29, s46, v65\n"
      "v_mfma_f32_16x16x32_bf16 v[76:79], v[252:255], a[204:207], v[52:55]\n"
      "ds_read_b128 a[196:199], v1 offset:16448\n"
      "ds_read2st64_b32 a[232:233], v12 offset0:4 offset1:6\n"
      "v_mfma_f32_16x16x32_bf16 v[80:83], v[244:247], a[204:207], v[56:59]\n"
      "ds_read2st64_b32 a[234:235], v14 offset0:4 offset1:6\n"
      "v_fmac_f32 v30, s46, v66\n"
      "v_mfma_f32_16x16x32_bf16 v[84:87], v[236:239], a[204:207], v[60:63]\n"
      "v_fmac_f32 v31, s46, v67\n"
      "v_sub_f32 v28, v28, v40\n"
      "v_mfma_f32_16x16x32_bf16 v[76:79], v[228:231], a[208:211], v[76:79]\n"
      "ds_read_b128 a[200:203], v1 offset:16512\n"
      "v_sub_f32 v29, v29, v41\n"
      "v_mfma_f32_16x16x32_bf16 v[80:83], v[220:223], a[208:211], v[80:83]\n"
      "v_sub_f32 v30, v30, v42\n"
      "v_sub_f32 v31, v31, v43\n"
      "v_mfma_f32_16x16x32_bf16 v[84:87], v[212:215], a[208:211], v[84:87]\n"
      "s_waitcnt lgkmcnt(3)\n"
      "v_fmac_f32 v40, s45, v28\n"
      "v_fmac_f32 v41, s45, v29\n"
      "v_mfma_f32_16x16x32_bf16 v[76:79], v[204:207], a[212:215], v[76:79]\n"
      "ds_read_b128 a[204:207], v2 offset:16384\n"
      "v_fmac_f32 v42, s45, v30\n"
      "v_mfma_f32_16x16x32_bf16 v[80:83], v[196:199], a[212:215], v[80:83]\n"
      "v_fmac_f32 v43, s45, v31\n"
      "v_fma_f32 v28, s47, v28, v52\n"
      "v_mfma_f32_16x16x32_bf16 v[84:87], v[188:191], a[212:215], v[84:87]\n"
      "ds_read2st64_b32 a[236:237], v13 offset0:4 offset1:6\n"
      "ds_read2st64_b32 a[238:239], v15 offset0:4 offset1:6\n"
      "v_mfma_f32_16x16x32_bf16 v[76:79], v[180:183], a[192:195], v[76:79]\n"
      "ds_read_b128 a[208:211], v2 offset:16448\n"
      "v_fma_f32 v29, s47, v29, v53\n"
      "v_mfma_f32_16x16x32_bf16 v[80:83], v[172:175], a[192:195], v[80:83]\n"
      "v_fma_f32 v30, s47, v30, v54\n"
      "v_fma_f32 v31, s47, v31, v55\n"
      "v_mfma_f32_16x16x32_bf16 v[84:87], v[164:167], a[192:195], v[84:87]\n"
      "v_cvt_pk_bf16_f32 v40, v40, v41\n"
      "v_cvt_pk_bf16_f32 v42, v42, v43\n"
      "v_mfma_f32_16x16x32_bf16 v[76:79], v[156:159], a[196:199], v[76:79]\n"
      "s_nop 0\n"
      "ds_read_b128 a[212:215], v2 offset:16512\n"
      "ds_write_addtid_b32 v40 offset:12288\n"
      "v_mfma_f32_16x16x32_bf16 v[80:83], v[148:151], a[196:199], v[80:83]\n"
      "s_waitcnt lgkmcnt(4)\n"
      "ds_write_addtid_b32 v42 offset:12800\n"
      "v_cvt_pk_bf16_f32 v28, v28, v29\n"
      "v_mfma_f32_16x16x32_bf16 v[84:87], v[140:143], a[196:199], v[84:87]\n"
      "buffer_store_dwordx4 a[232:235], v11, s[8:11], s59 offen offset:0 sc1\n"
      "v_mfma_f32_16x16x32_bf16 v[76:79], v[132:135], a[200:203], v[76:79]\n"
      "ds_read_b128 a[192:195], v0 offset:20480\n"
      "ds_write_addtid_b32 v28 offset:14336\n"
      "v_mfma_f32_16x16x32_bf16 v[80:83], v[124:127], a[200:203], v[80:83]\n"
      "v_cvt_pk_bf16_f32 v30, v30, v31\n"
      "v_fmac_f32 v32, s46, v68\n"
      "v_mfma_f32_16x16x32_bf16 v[84:87], v[116:119], a[200:203], v[84:87]\n"
      "ds_write_addtid_b32 v30 offset:14848\n"
      "v_fmac_f32 v33, s46, v69\n"
      "v_mfma_f32_16x16x32_bf16 v[76:79], v[108:111], a[204:207], v[76:79]\n"
      "ds_read_b128 a[196:199], v0 offset:20544\n"
      "v_fmac_f32 v34, s46, v70\n"
      "v_mfma_f32_16x16x32_bf16 v[80:83], v[100:103], a[204:207], v[80:83]\n"
      "v_fmac_f32 v35, s46, v71\n"
      "v_sub_f32_e64 v32, v32, v44\n"
      "v_mfma_f32_16x16x32_bf16 v[84:87], a[160:163], a[204:207], v[84:87]\n"
      "s_waitcnt lgkmcnt(4)\n"
      "v_sub_f32 v33, v33, v45\n"
      "v_sub_f32 v34, v34, v46\n"
      "v_mfma_f32_16x16x32_bf16 v[76:79], a[152:155], a[208:211], v[76:79]\n"
      "ds_read_b128 a[200:203], v0 offset:20608\n"
      "v_sub_f32 v35, v35, v47\n"
      "v_mfma_f32_16x16x32_bf16 v[80:83], a[144:147], a[208:211], v[80:83]\n"
      "buffer_store_dwordx4 a[236:239], v11, s[8:11], s59 offen offset:128 sc1\n"
      "v_mfma_f32_16x16x32_bf16 v[84:87], a[136:139], a[208:211], v[84:87]\n"
      "v_fmac_f32 v44, s45, v32\n"
      "v_fmac_f32 v45, s45, v33\n"
      "v_mfma_f32_16x16x32_bf16 v[76:79], a[128:131], a[212:215], v[76:79]\n"
      "ds_read_b128 a[204:207], v1 offset:20480\n"
      "v_fmac_f32 v46, s45, v34\n"
      "v_mfma_f32_16x16x32_bf16 v[80:83], a[120:123], a[212:215], v[80:83]\n"
      "v_fmac_f32_e64 v47, s45, v35\n"
      "v_fma_f32 v32, s47, v32, v56\n"
      "v_mfma_f32_16x16x32_bf16 v[84:87], a[112:115], a[212:215], v[84:87]\n"
      "s_waitcnt lgkmcnt(1)\n"
      "ds_read2st64_b32 a[240:241], v8 offset0:4 offset1:6\n"
      "ds_read2st64_b32 a[242:243], v9 offset0:4 offset1:6\n"
      "v_mfma_f32_16x16x32_bf16 v[88:91], v[248:251], a[192:195], v[52:55]\n"
      "ds_read_b128 a[208:211], v1 offset:20544\n"
      "v_fma_f32 v33, s47, v33, v57\n"
      "v_mfma_f32_16x16x32_bf16 v[92:95], v[240:243], a[192:195], v[56:59]\n"
      "v_fma_f32 v34, s47, v34, v58\n"
      "v_fma_f32 v35, s47, v35, v59\n"
      "v_mfma_f32_16x16x32_bf16 v[96:99], v[232:235], a[192:195], v[60:63]\n"
      "v_cvt_pk_bf16_f32 v44, v44, v45\n"
      "v_cvt_pk_bf16_f32 v46, v46, v47\n"
      "v_mfma_f32_16x16x32_bf16 v[88:91], v[224:227], a[196:199], v[88:91]\n"
      "ds_read_b128 a[212:215], v1 offset:20608\n"
      "ds_write_addtid_b32 v44 offset:20484\n"
      "v_mfma_f32_16x16x32_bf16 v[92:95], v[216:219], a[196:199], v[92:95]\n"
      "ds_write_addtid_b32 v46 offset:20996\n"
      "v_cvt_pk_bf16_f32 v32, v32, v33\n"
      "v_mfma_f32_16x16x32_bf16 v[96:99], v[208:211], a[196:199], v[96:99]\n"
      "ds_write_addtid_b32 v32 offset:22532\n"
      "v_cvt_pk_bf16_f32 v34, v34, v35\n"
      "v_mfma_f32_16x16x32_bf16 v[88:91], v[200:203], a[200:203], v[88:91]\n"
      "ds_read_b128 a[192:195], v2 offset:20480\n"
      "ds_write_addtid_b32 v34 offset:23044\n"
      "v_mfma_f32_16x16x32_bf16 v[92:95], v[192:195], a[200:203], v[92:95]\n"
      "v_fmac_f32 v36, s46, v72\n"
      "v_fmac_f32 v37, s46, v73\n"
      "v_mfma_f32_16x16x32_bf16 v[96:99], v[184:187], a[200:203], v[96:99]\n"
      "s_waitcnt lgkmcnt(4)\n"
      "v_fmac_f32 v38, s46, v74\n"
      "v_fmac_f32 v39, s46, v75\n"
      "v_mfma_f32_16x16x32_bf16 v[88:91], v[176:179], a[204:207], v[88:91]\n"
      "ds_read_b128 a[196:199], v2 offset:20544\n"
      "v_sub_f32 v36, v36, v48\n"
      "v_mfma_f32_16x16x32_bf16 v[92:95], v[168:171], a[204:207], v[92:95]\n"
      "v_sub_f32 v37, v37, v49\n"
      "v_sub_f32 v38, v38, v50\n"
      "v_mfma_f32_16x16x32_bf16 v[96:99], v[160:163], a[204:207], v[96:99]\n"
      "v_sub_f32 v39, v39, v51\n"
      "v_fmac_f32 v48, s45, v36\n"
      "v_mfma_f32_16x16x32_bf16 v[88:91], v[152:155], a[208:211], v[88:91]\n"
      "ds_read_b128 a[200:203], v2 offset:20608\n"
      "v_fmac_f32 v49, s45, v37\n"
      "v_mfma_f32_16x16x32_bf16 v[92:95], v[144:147], a[208:211], v[92:95]\n"
      "buffer_store_dwordx4 a[240:243], v11, s[8:11], s59 offen offset:256 sc1\n"
      "v_mfma_f32_16x16x32_bf16 v[96:99], v[136:139], a[208:211], v[96:99]\n"
      "v_fmac_f32 v50, s45, v38\n"
      "v_fmac_f32 v51, s45, v39\n"
      "v_mfma_f32_16x16x32_bf16 v[88:91], v[128:131], a[212:215], v[88:91]\n"
      "ds_read_b128 a[204:207], v3 offset:0\n"
      "v_fma_f32 v36, s47, v36, v60\n"
      "v_mfma_f32_16x16x32_bf16 v[92:95], v[120:123], a[212:215], v[92:95]\n"
      "v_fma_f32 v37, s47, v37, v61\n"
      "v_fma_f32 v38, s47, v38, v62\n"
      "v_mfma_f32_16x16x32_bf16 v[96:99], v[112:115], a[212:215], v[96:99]\n"
      "s_waitcnt lgkmcnt(1)\n"
      "v_fma_f32 v39, s47, v39, v63\n"
      "v_cvt_pk_bf16_f32 v48, v48, v49\n"
      "v_mfma_f32_16x16x32_bf16 v[88:91], v[104:107], a[192:195], v[88:91]\n"
      "ds_read_b128 a[208:211], v3 offset:64\n"
      "ds_write_addtid_b32 v48 offset:16384\n"
      "v_mfma_f32_16x16x32_bf16 v[92:95], a[164:167], a[192:195], v[92:95]\n"
      "v_cvt_pk_bf16_f32 v50, v50, v51\n"
      "v_cvt_pk_bf16_f32 v36, v36, v37\n"
      "v_mfma_f32_16x16x32_bf16 v[96:99], a[156:159], a[192:195], v[96:99]\n"
      "ds_write_addtid_b32 v50 offset:16896\n"
      "ds_write_addtid_b32 v36 offset:18432\n"
      "v_mfma_f32_16x16x32_bf16 v[88:91], a[148:151], a[196:199], v[88:91]\n"
      "ds_read_b128 a[212:215], v3 offset:128\n"
      "v_cvt_pk_bf16_f32 v38, v38, v39\n"
      "v_mfma_f32_16x16x32_bf16 v[92:95], a[140:143], a[196:199], v[92:95]\n"
      "ds_write_addtid_b32 v38 offset:18944\n"
      "v_mfma_f32_16x16x32_bf16 v[96:99], a[132:135], a[196:199], v[96:99]\n"
      "v_mfma_f32_16x16x32_bf16 v[88:91], a[124:127], a[200:203], v[88:91]\n"
      "ds_read_b128 a[192:195], v4 offset:0\n"
      "v_mfma_f32_16x16x32_bf16 v[92:95], a[116:119], a[200:203], v[92:95]\n"
      "v_mfma_f32_16x16x32_bf16 v[96:99], a[108:111], a[200:203], v[96:99]\n"
      "s_waitcnt lgkmcnt(2)\n"
      "v_mfma_f32_16x16x32_bf16 v[64:67], a[0:3], a[204:207], 0\n"
      "s_nop 0\n"
      "ds_read_b128 a[196:199], v4 offset:64\n"
      "v_mfma_f32_16x16x32_bf16 v[68:71], a[4:7], a[204:207], 0\n"
      "v_mfma_f32_16x16x32_bf16 v[72:75], a[8:11], a[204:207], 0\n"
      "v_sub_f32 v76, v88, v76\n"
      "v_sub_f32 v77, v89, v77\n"
      "v_sub_f32 v78, v90, v78\n"
      "v_sub_f32 v79, v91, v79\n"
      "v_sub_f32 v80, v92, v80\n"
      "v_sub_f32 v81, v93, v81\n"
      "v_sub_f32 v82, v94, v82\n"
      "v_mfma_f32_16x16x32_bf16 v[64:67], a[12:15], a[208:211], v[64:67]\n"
      "s_nop 0\n"
      "ds_read_b128 a[200:203], v4 offset:128\n"
      "ds_write_addtid_b32 v76 offset:6144\n"
      "ds_write_addtid_b32 v77 offset:6656\n"
      "v_sub_f32 v83, v95, v83\n"
      "v_sub_f32 v84, v96, v84\n"
      "v_sub_f32 v85, v97, v85\n"
      "v_sub_f32 v86, v98, v86\n"
      "v_mfma_f32_16x16x32_bf16 v[68:71], a[16:19], a[208:211], v[68:71]\n"
      "ds_write_addtid_b32 v78 offset:7168\n"
      "ds_write_addtid_b32 v79 offset:7680\n"
      "ds_write_addtid_b32 v80 offset:8192\n"
      "v_mfma_f32_16x16x32_bf16 v[72:75], a[20:23], a[208:211], v[72:75]\n"
      "ds_write_addtid_b32 v81 offset:8704\n"
      "ds_write_addtid_b32 v82 offset:9216\n"
      "ds_write_addtid_b32 v83 offset:9728\n"
      "v_sub_f32 v87, v99, v87\n"
      "v_mfma_f32_16x16x32_bf16 v[64:67], a[24:27], a[212:215], v[64:67]\n"
      "s_nop 0\n"
      "ds_write_addtid_b32 v84 offset:10240\n"
      "ds_write_addtid_b32 v85 offset:10752\n"
      "v_mfma_f32_16x16x32_bf16 v[68:71], a[28:31], a[212:215], v[68:71]\n"
      "ds_write_addtid_b32 v86 offset:11264\n"
      "ds_write_addtid_b32 v87 offset:11776\n"
      "v_mfma_f32_16x16x32_bf16 v[72:75], a[32:35], a[212:215], v[72:75]\n"
      "s_waitcnt lgkmcnt(0)\n"
      "s_barrier\n"
      "v_mfma_f32_16x16x32_bf16 v[64:67], a[36:39], a[192:195], v[64:67]\n"
      "s_add_u32 s59, s59, 786432\n"
      "ds_read_b128 a[204:207], v5 offset:0\n"
      "ds_read2st64_b32 v[28:29], v6 offset0:24 offset1:26\n"
      "v_mfma_f32_16x16x32_bf16 v[68:71], a[40:43], a[192:195], v[68:71]\n"
      "ds_read2st64_b32 v[30:31], v6 offset0:28 offset1:30\n"
      "ds_read2st64_b32 v[32:33], v6 offset0:32 offset1:34\n"
      "v_mfma_f32_16x16x32_bf16 v[72:75], a[44:47], a[192:195], v[72:75]\n"
      "ds_read2st64_b32 v[34:35], v6 offset0:36 offset1:38\n"
      "ds_read2st64_b32 v[36:37], v6 offset0:40 offset1:42\n"
      "v_mfma_f32_16x16x32_bf16 v[64:67], a[48:51], a[196:199], v[64:67]\n"
      "ds_read_b128 a[208:211], v5 offset:64\n"
      "ds_read2st64_b32 v[38:39], v6 offset0:44 offset1:46\n"
      "v_mfma_f32_16x16x32_bf16 v[68:71], a[52:55], a[196:199], v[68:71]\n"
      "v_fma_f32 v40, 0.5, v76, -v88\n"
      "v_fma_f32 v41, 0.5, v77, -v89\n"
      "v_mfma_f32_16x16x32_bf16 v[72:75], a[56:59], a[196:199], v[72:75]\n"
      "ds_read2st64_b32 a[232:233], v12 offset0:0 offset1:2\n"
      "ds_read2st64_b32 a[234:235], v14 offset0:0 offset1:2\n"
      "v_mfma_f32_16x16x32_bf16 v[64:67], a[60:63], a[200:203], v[64:67]\n"
      "ds_read_b128 a[212:215], v5 offset:128\n"
      "v_fma_f32 v42, 0.5, v78, -v90\n"
      "v_mfma_f32_16x16x32_bf16 v[68:71], a[64:67], a[200:203], v[68:71]\n"
      "s_nop 0\n"
      "ds_read2st64_b32 a[236:237], v13 offset0:0 offset1:2\n"
      "ds_read2st64_b32 a[238:239], v15 offset0:0 offset1:2\n"
      "v_mfma_f32_16x16x32_bf16 v[72:75], a[68:71], a[200:203], v[72:75]\n"
      "s_waitcnt lgkmcnt(9)\n"
      "v_fma_f32 v43, 0.5, v79, -v91\n"
      "v_fma_f32 v44, 0.5, v80, -v92\n"
      "v_mfma_f32_16x16x32_bf16 v[64:67], a[72:75], a[204:207], v[64:67]\n"
      "s_waitcnt lgkmcnt(3)\n"
      "buffer_store_dwordx4 a[232:235], v11, s[8:11], s59 offen offset:0 sc1\n"
      "v_mfma_f32_16x16x32_bf16 v[68:71], a[76:79], a[204:207], v[68:71]\n"
      "s_nop 0\n"
      "v_fma_f32 v45, 0.5, v81, -v93\n"
      "v_fma_f32 v46, 0.5, v82, -v94\n"
      "v_mfma_f32_16x16x32_bf16 v[72:75], a[80:83], a[204:207], v[72:75]\n"
      "ds_read2st64_b32 a[240:241], v8 offset0:0 offset1:2\n"
      "ds_read2st64_b32 a[242:243], v9 offset0:0 offset1:2\n"
      "v_mfma_f32_16x16x32_bf16 v[64:67], a[84:87], a[208:211], v[64:67]\n"
      "s_waitcnt lgkmcnt(2)\n"
      "buffer_store_dwordx4 a[236:239], v11, s[8:11], s59 offen offset:128 sc1\n"
      "v_mfma_f32_16x16x32_bf16 v[68:71], a[88:91], a[208:211], v[68:71]\n"
      "s_nop 0\n"
      "v_fma_f32 v47, 0.5, v83, -v95\n"
      "v_fma_f32 v48, 0.5, v84, -v96\n"
      "v_mfma_f32_16x16x32_bf16 v[72:75], a[92:95], a[208:211], v[72:75]\n"
      "v_fma_f32 v49, 0.5, v85, -v97\n"
      "v_fma_f32 v50, 0.5, v86, -v98\n"
      "v_mfma_f32_16x16x32_bf16 v[64:67], a[96:99], a[212:215], v[64:67]\n"
      "s_waitcnt lgkmcnt(0)\n"
      "buffer_store_dwordx4 a[240:243], v11, s[8:11], s59 offen offset:256 sc1\n"
      "v_mfma_f32_16x16x32_bf16 v[68:71], a[100:103], a[212:215], v[68:71]\n"
      "s_nop 0\n"
      "v_fma_f32 v51, 0.5, v87, -v99\n"
      "v_fmac_f32 v28, s46, v52\n"
      "v_mfma_f32_16x16x32_bf16 v[72:75], a[104:107], a[212:215], v[72:75]\n"
      "v_fmac_f32 v29, s46, v53\n"
      "v_fmac_f32 v30, s46, v54\n"
      "v_fmac_f32 v31, s46, v55\n"
      "v_sub_f32 v28, v28, v40\n"
      "v_sub_f32 v29, v29, v41\n"
      "v_sub_f32 v30, v30, v42\n"
      "v_sub_f32 v31, v31, v43\n"
      "v_fmac_f32 v40, s45, v28\n"
      "v_fmac_f32 v41, s45, v29\n"
      "v_pk_fma_f32 v[42:43], v[30:31], s[44:45], v[42:43] op_sel:[0,1,0] op_sel_hi:[1,1,1]\n"
      "v_cvt_pk_bf16_f32 v40, v40, v41\n"
      "v_cvt_pk_bf16_f32 v42, v42, v43\n"
      "v_pk_fma_f32 v[32:33], v[56:57], s[46:47], v[32:33] op_sel:[0,0,0] op_sel_hi:[1,0,1]\n"
      "v_pk_fma_f32 v[34:35], v[58:59], s[46:47], v[34:35] op_sel:[0,0,0] op_sel_hi:[1,0,1]\n"
      "v_pk_add_f32 v[32:33], v[32:33], v[44:45] neg_lo:[0,1] neg_hi:[0,1]\n"
      "v_pk_add_f32 v[34:35], v[34:35], v[46:47] neg_lo:[0,1] neg_hi:[0,1]\n"
      "v_pk_fma_f32 v[44:45], v[32:33], s[44:45], v[44:45] op_sel:[0,1,0] op_sel_hi:[1,1,1]\n"
      "v_pk_fma_f32 v[46:47], v[34:35], s[44:45], v[46:47] op_sel:[0,1,0] op_sel_hi:[1,1,1]\n"
      "v_cvt_pk_bf16_f32 v44, v44, v45\n"
      "v_cvt_pk_bf16_f32 v46, v46, v47\n"
      "v_pk_fma_f32 v[36:37], v[60:61], s[46:47], v[36:37] op_sel:[0,0,0] op_sel_hi:[1,0,1]\n"
      "v_pk_fma_f32 v[38:39], v[62:63], s[46:47], v[38:39] op_sel:[0,0,0] op_sel_hi:[1,0,1]\n"
      "v_pk_add_f32 v[36:37], v[36:37], v[48:49] neg_lo:[0,1] neg_hi:[0,1]\n"
      "v_pk_add_f32 v[38:39], v[38:39], v[50:51] neg_lo:[0,1] neg_hi:[0,1]\n"
      "v_pk_fma_f32 v[48:49], v[36:37], s[44:45], v[48:49] op_sel:[0,1,0] op_sel_hi:[1,1,1]\n"
      "v_pk_fma_f32 v[50:51], v[38:39], s[44:45], v[50:51] op_sel:[0,1,0] op_sel_hi:[1,1,1]\n"
      "v_cvt_pk_bf16_f32 v48, v48, v49\n"
      "v_cvt_pk_bf16_f32 v50, v50, v51\n"
      "ds_write_addtid_b32 v40 offset:13312\n"
      "ds_write_addtid_b32 v42 offset:13824\n"
      "ds_write_addtid_b32 v44 offset:21508\n"
      "ds_write_addtid_b32 v46 offset:22020\n"
      "ds_write_addtid_b32 v48 offset:17408\n"
      "ds_write_addtid_b32 v50 offset:17920\n"
      "v_pk_fma_f32 v[28:29], v[28:29], s[46:47], v[64:65] op_sel:[0,1,0] op_sel_hi:[1,1,1]\n"
      "v_pk_fma_f32 v[30:31], v[30:31], s[46:47], v[66:67] op_sel:[0,1,0] op_sel_hi:[1,1,1]\n"
      "v_cvt_pk_bf16_f32 v28, v28, v29\n"
      "v_cvt_pk_bf16_f32 v30, v30, v31\n"
      "v_pk_fma_f32 v[32:33], v[32:33], s[46:47], v[68:69] op_sel:[0,1,0] op_sel_hi:[1,1,1]\n"
      "v_pk_fma_f32 v[34:35], v[34:35], s[46:47], v[70:71] op_sel:[0,1,0] op_sel_hi:[1,1,1]\n"
      "v_cvt_pk_bf16_f32 v32, v32, v33\n"
      "v_cvt_pk_bf16_f32 v34, v34, v35\n"
      "v_pk_fma_f32 v[36:37], v[36:37], s[46:47], v[72:73] op_sel:[0,1,0] op_sel_hi:[1,1,1]\n"
      "v_pk_fma_f32 v[38:39], v[38:39], s[46:47], v[74:75] op_sel:[0,1,0] op_sel_hi:[1,1,1]\n"
      "v_cvt_pk_bf16_f32 v36, v36, v37\n"
      "v_cvt_pk_bf16_f32 v38, v38, v39\n"
      "ds_write_addtid_b32 v28 offset:15360\n"
      "ds_write_addtid_b32 v30 offset:15872\n"
      "ds_write_addtid_b32 v32 offset:23556\n"
      "ds_write_addtid_b32 v34 offset:24068\n"
      "ds_write_addtid_b32 v36 offset:19456\n"
      "ds_write_addtid_b32 v38 offset:19968\n"
      "s_waitcnt lgkmcnt(0)\n"
      "s_barrier\n"
      "ds_read2st64_b32 a[232:233], v12 offset0:4 offset1:6\n"
      "ds_read2st64_b32 a[234:235], v14 offset0:4 offset1:6\n"
      "ds_read2st64_b32 a[236:237], v13 offset0:4 offset1:6\n"
      "ds_read2st64_b32 a[238:239], v15 offset0:4 offset1:6\n"
      "ds_read2st64_b32 a[240:241], v8 offset0:4 offset1:6\n"
      "ds_read2st64_b32 a[242:243], v9 offset0:4 offset1:6\n"
      "s_add_u32 s59, s59, 786432\n"
      "s_waitcnt lgkmcnt(4)\n"
      "buffer_store_dwordx4 a[232:235], v11, s[8:11], s59 offen offset:0 sc1\n"
      "s_waitcnt lgkmcnt(2)\n"
      "buffer_store_dwordx4 a[236:239], v11, s[8:11], s59 offen offset:128 sc1\n"
      "s_waitcnt lgkmcnt(0)\n"
      "buffer_store_dwordx4 a[240:243], v11, s[8:11], s59 offen offset:256 sc1\n"
      "LSend_%=:\n"
      "Lfin_%=:\n"
      "s_waitcnt vmcnt(0) lgkmcnt(0)\n"
      "s_endpgm\n"
      ".rept 512\n"
      "s_nop 0\n"
      ".endr\n"
      ".Lwg_end:\n"
      : "+{v0}"(tid)
      : "{s[0:1]}"(__builtin_amdgcn_kernarg_segment_ptr()), "{s2}"(blockIdx.x)
      : "v1", "v2", "v3", "v4", "v5", "v6", "v7", "v8", "v9", "v10", "v11", "v12", "v13", "v14", "v15", "v16", "v17", "v18", "v19", "v20", "v21", "v22", "v23", "v24", "v25", "v26", "v27", "v28", "v29", "v30", "v31", "v32", "v33", "v34", "v35", "v36", "v37", "v38", "v39", "v40", "v41", "v42", "v43", "v44", "v45", "v46", "v47", "v48", "v49", "v50", "v51", "v52", "v53", "v54", "v55", "v56", "v57", "v58", "v59", "v60", "v61", "v62", "v63", "v64", "v65", "v66", "v67", "v68", "v69", "v70", "v71", "v72", "v73", "v74", "v75", "v76", "v77", "v78", "v79", "v80", "v81", "v82", "v83", "v84", "v85", "v86", "v87", "v88", "v89", "v90", "v91", "v92", "v93", "v94", "v95", "v96", "v97", "v98", "v99", "v100", "v101", "v102", "v103", "v104", "v105", "v106", "v107", "v108", "v109", "v110", "v111", "v112", "v113", "v114", "v115", "v116", "v117", "v118", "v119", "v120", "v121", "v122", "v123", "v124", "v125", "v126", "v127", "v128", "v129", "v130", "v131", "v132", "v133", "v134", "v135", "v136", "v137", "v138", "v139", "v140", "v141", "v142", "v143", "v144", "v145", "v146", "v147", "v148", "v149", "v150", "v151", "v152", "v153", "v154", "v155", "v156", "v157", "v158", "v159", "v160", "v161", "v162", "v163", "v164", "v165", "v166", "v167", "v168", "v169", "v170", "v171", "v172", "v173", "v174", "v175", "v176", "v177", "v178", "v179", "v180", "v181", "v182", "v183", "v184", "v185", "v186", "v187", "v188", "v189", "v190", "v191", "v192", "v193", "v194", "v195", "v196", "v197", "v198", "v199", "v200", "v201", "v202", "v203", "v204", "v205", "v206", "v207", "v208", "v209", "v210", "v211", "v212", "v213", "v214", "v215", "v216", "v217", "v218", "v219", "v220", "v221", "v222", "v223", "v224", "v225", "v226", "v227", "v228", "v229", "v230", "v231", "v232", "v233", "v234", "v235", "v236", "v237", "v238", "v239", "v240", "v241", "v242", "v243", "v244", "v245", "v246", "v247", "v248", "v249", "v250", "v251", "v252", "v253", "v254", "v255", "a0", "a1", "a2", "a3", "a4", "a5", "a6", "a7", "a8", "a9", "a10", "a11", "a12", "a13", "a14", "a15", "a16", "a17", "a18", "a19", "a20", "a21", "a22", "a23", "a24", "a25", "a26", "a27", "a28", "a29", "a30", "a31", "a32", "a33", "a34", "a35", "a36", "a37", "a38", "a39", "a40", "a41", "a42", "a43", "a44", "a45", "a46", "a47", "a48", "a49", "a50", "a51", "a52", "a53", "a54", "a55", "a56", "a57", "a58", "a59", "a60", "a61", "a62", "a63", "a64", "a65", "a66", "a67", "a68", "a69", "a70", "a71", "a72", "a73", "a74", "a75", "a76", "a77", "a78", "a79", "a80", "a81", "a82", "a83", "a84", "a85", "a86", "a87", "a88", "a89", "a90", "a91", "a92", "a93", "a94", "a95", "a96", "a97", "a98", "a99", "a100", "a101", "a102", "a103", "a104", "a105", "a106", "a107", "a108", "a109", "a110", "a111", "a112", "a113", "a114", "a115", "a116", "a117", "a118", "a119", "a120", "a121", "a122", "a123", "a124", "a125", "a126", "a127", "a128", "a129", "a130", "a131", "a132", "a133", "a134", "a135", "a136", "a137", "a138", "a139", "a140", "a141", "a142", "a143", "a144", "a145", "a146", "a147", "a148", "a149", "a150", "a151", "a152", "a153", "a154", "a155", "a156", "a157", "a158", "a159", "a160", "a161", "a162", "a163", "a164", "a165", "a166", "a167", "a168", "a169", "a170", "a171", "a172", "a173", "a174", "a175", "a176", "a177", "a178", "a179", "a180", "a181", "a182", "a183", "a184", "a185", "a186", "a187", "a188", "a189", "a190", "a191", "a192", "a193", "a194", "a195", "a196", "a197", "a198", "a199", "a200", "a201", "a202", "a203", "a204", "a205", "a206", "a207", "a208", "a209", "a210", "a211", "a212", "a213", "a214", "a215", "a216", "a217", "a218", "a219", "a220", "a221", "a222", "a223", "a224", "a225", "a226", "a227", "a228", "a229", "a230", "a231", "a232", "a233", "a234", "a235", "a236", "a237", "a238", "a239", "a240", "a241", "a242", "a243", "a244", "a245", "a246", "a247", "a248", "a249", "a250", "a251", "a252", "a253", "a254", "a255", "s3", "s4", "s5", "s6", "s7", "s8", "s9", "s10", "s11", "s12", "s13", "s14", "s15", "s16", "s17", "s18", "s19", "s20", "s21", "s22", "s23", "s24", "s25", "s26", "s27", "s28", "s29", "s30", "s31", "s33", "s34", "s35", "s36", "s37", "s38", "s39", "s40", "s41", "s42", "s43", "s44", "s45", "s46", "s47", "s48", "s49", "s50", "s51", "s52", "s53", "s54", "s55", "s56", "s57", "s58", "s59", "s60", "s61", "s62", "s63", "s64", "s65", "s66", "s67", "s68", "s69", "s70", "s71", "s72", "s73", "s74", "s75", "s76", "s77", "s78", "s79", "s80", "s81", "s82", "s83", "s84", "s85", "s86", "s87", "s88", "s89", "s90", "s91", "s92", "s93", "s94", "s95", "s96", "s97", "s98", "s99", "scc", "vcc", "memory");
  asm volatile("" :: "v"((unsigned)(uintptr_t)smem) : "memory");
}

extern "C" void kernel_launch(void* const* d_in, const int* in_sizes, int n_in, void* d_out,
                              int out_size, void* d_ws, size_t ws_size, hipStream_t stream) {
  conv_kernel<<<256, 256, 0, stream>>>((const char*)d_in[0], (const char*)d_in[1], (char*)d_out, (char*)d_ws);
}
```

```cpp
#include <hip/hip_runtime.h>
#include <hip/hip_bf16.h>
#include <stdint.h>

__global__ void __launch_bounds__(256, 1)
conv_kernel(const char* __restrict__ x, const char* __restrict__ w, char* __restrict__ out, char* __restrict__ dbg) {
  __shared__ __attribute__((aligned(1024))) char smem[163840];
  unsigned tid = threadIdx.x;
  asm volatile(
      "s_load_dwordx2 s[12:13], s[0:1], 8\n"
      "s_load_dwordx2 s[4:5], s[0:1], 0\n"
      "s_load_dwordx2 s[8:9], s[0:1], 16\n"
      "v_readfirstlane_b32 s16, v0\n"
      "s_getpc_b64 s[26:27]\n"
      ".Lwg_pc:\n"
      "v_and_b32 v37, 3, v0\n"
      "s_lshr_b32 s23, s2, 3\n"
      "s_lshl_b32 s28, s16, 3\n"
      "v_lshlrev_b32 v40, 7, v37\n"
      "s_lshl_b32 s29, s23, 11\n"
      "s_mov_b32 m0, 150016\n"
      "s_add_u32 s28, s28, s29\n"
      "v_and_b32 v36, 63, v0\n"
      "v_add_u32 v40, s28, v40\n"
      "s_lshr_b32 s16, s16, 6\n"
      "v_min_u32 v40, .Lwg_end-.Lwg_pc-4, v40\n"
      "s_and_b32 s18, s16, 1\n"
      "global_load_lds_dword v40, s[26:27]\n"
      "v_lshlrev_b32 v17, 4, v36\n"
      "s_lshr_b32 s17, s16, 1\n"
      "s_bfe_u32 s28, s2, 0x20001\n"
      "s_mul_i32 s19, s28, 12582912\n"
      "s_and_b32 s28, s23, 1\n"
      "s_mul_i32 s20, s28, 98304\n"
      "s_lshr_b32 s28, s23, 1\n"
      "s_mul_i32 s29, s28, 3072\n"
      "s_bitcmp1_b32 s2, 0\n"
      "s_cselect_b32 s28, 49152, 0\n"
      "s_add_u32 s21, s20, s29\n"
      "s_add_u32 s21, s21, s28\n"
      "s_add_u32 s6, s19, 12783616\n"
      "s_min_u32 s6, s6, 50335744\n"
      "s_mov_b32 s7, 0x20000\n"
      "s_mov_b32 s10, 50331648\n"
      "s_mov_b32 s11, 0x20000\n"
      "s_mov_b32 s14, 165888\n"
      "s_mov_b32 s15, 0x20000\n"
      "s_mul_i32 s63, s18, 3072\n"
      "s_add_u32 s54, s19, s20\n"
      "s_sub_u32 s54, s54, 196608\n"
      "s_lshl_b32 s64, s18, 8\n"
      "s_mul_i32 s28, s17, 24832\n"
      "s_add_u32 s64, s64, s28\n"
      "s_mov_b32 s55, 0\n"
      "v_bitop3_b32 v18, v36, v17, 32 bitop3:0x6c\n"
      "v_xor_b32 v19, 32, v17\n"
      "v_bfe_u32 v38, v36, 2, 2\n"
      "v_lshrrev_b32 v39, 4, v36\n"
      "s_cmp_eq_u32 s17, 1\n"
      "s_cbranch_scc1 LBD_%=\n"
      "v_lshlrev_b32 v40, 3, v17\n"
      "s_mul_i32 s53, s18, 2688\n"
      "s_mul_i32 s34, s23, 5184\n"
      "v_min_u32 v40, 2560, v40\n"
      "s_add_u32 s53, s53, s34\n"
      "v_lshlrev_b32 v43, 4, v37\n"
      "s_mul_i32 s26, s17, 0x8080\n"
      "s_sub_u32 s26, 0xbf80, s26\n"
      "s_mov_b32 s27, 0\n"
      "s_lshl_b32 s34, s17, 7\n"
      "s_mov_b32 s29, 0\n"
      "s_sub_u32 s28, s26, s34\n"
      "s_mov_b32 s31, 0\n"
      "s_sub_u32 s30, s28, s34\n"
      "v_lshlrev_b64 v[44:45], v43, s[26:27]\n"
      "v_lshlrev_b64 v[46:47], v43, s[28:29]\n"
      "s_xor_b32 s28, s28, 0x8000\n"
      "v_lshlrev_b64 v[48:49], v43, s[30:31]\n"
      "v_lshlrev_b64 v[50:51], v43, s[28:29]\n"
      "v_accvgpr_write_b32 a244, v44\n"
      "v_accvgpr_write_b32 a245, v45\n"
      "v_accvgpr_write_b32 a246, v46\n"
      "v_accvgpr_write_b32 a247, v47\n"
      "v_accvgpr_write_b32 a248, v48\n"
      "v_accvgpr_write_b32 a249, v49\n"
      "v_accvgpr_write_b32 a250, v50\n"
      "v_accvgpr_write_b32 a251, v51\n"
      "s_waitcnt lgkmcnt(0)\n"
      "s_and_b32 s13, s13, 0xffff\n"
      "buffer_load_dword v40, s[12:15], s53 offen lds\n"
      "s_add_u32 m0, s63, 30976\n"
      "s_add_u32 s52, s63, 55296\n"
      "buffer_load_dwordx4 v17, s[12:15], s52 offen offset:0  lds\n"
      "buffer_load_dwordx4 v18, s[12:15], s52 offen offset:1024  lds\n"
      "buffer_load_dwordx4 v19, s[12:15], s52 offen offset:2048  lds\n"
      "s_add_u32 m0, s63, 140800\n"
      "s_add_u32 s52, s63, 110592\n"
      "buffer_load_dwordx4 v17, s[12:15], s52 offen offset:0  lds\n"
      "buffer_load_dwordx4 v18, s[12:15], s52 offen offset:1024  lds\n"
      "buffer_load_dwordx4 v19, s[12:15], s52 offen offset:2048  lds\n"
      "s_branch LS_%=\n"
      "LBD_%=:\n"
      "s_sub_u32 s26, s21, s20\n"
      "s_lshl_b32 s37, s18, 11\n"
      "s_add_u32 s26, s26, s37\n"
      "s_add_u32 s27, s26, 3712\n"
      "v_add_u32 v44, s27, v17\n"
      "s_cmp_eq_u32 s18, 1\n"
      "s_cselect_b32 s28, 16, 48\n"
      "s_cselect_b32 s29, 0, 32\n"
      "s_cselect_b32 s27, 48, 64\n"
      "v_cmp_le_u32 vcc, s28, v36\n"
      "v_cmp_gt_u32 s[30:31], s29, v36\n"
      "v_xor_b32 v45, 32, v44\n"
      "s_cmp_eq_u32 s26, 0\n"
      "s_cselect_b32 s28, 24, 0\n"
      "v_cndmask_b32 v47, v44, v45, vcc\n"
      "v_cndmask_b32 v46, v44, v45, s[30:31]\n"
      "v_cmp_le_u32 vcc, s28, v36\n"
      "v_cmp_gt_u32 s[30:31], s27, v36\n"
      "s_cmp_eq_u32 s26, 97280\n"
      "s_cselect_b32 s27, 24, 64\n"
      "v_cmp_gt_u32 s[34:35], s27, v36\n"
      "v_subrev_u32 v45, 256, v44\n"
      "v_cndmask_b32 v8, 2.0, v47, vcc\n"
      "v_cndmask_b32 v48, v45, v46, s[30:31]\n"
      "v_cndmask_b32 v9, 2.0, v48, s[34:35]\n"
      "s_add_u32 s34, s19, s21\n"
      "s_cmp_eq_u32 s19, 0\n"
      "s_cselect_b32 s36, 0x80000000, s54\n"
      "s_waitcnt lgkmcnt(0)\n"
      "s_load_dword s44, s[4:5], s34\n"
      "s_and_b32 s13, s13, 0xffff\n"
      "s_add_u32 m0, s63, 24832\n"
      "s_add_u32 s52, s63, 0\n"
      "buffer_load_dwordx4 v17, s[12:15], s52 offen offset:0  lds\n"
      "buffer_load_dwordx4 v18, s[12:15], s52 offen offset:1024  lds\n"
      "buffer_load_dwordx4 v19, s[12:15], s52 offen offset:2048  lds\n"
      "s_add_u32 s4, s4, -4096\n"
      "s_addc_u32 s5, s5, -1\n"
      "s_and_b32 s5, s5, 0xffff\n"
      "s_add_u32 s53, s54, 196608\n"
      "s_add_u32 m0, s37, 82432\n"
      "s_nop 0\n"
      "buffer_load_dwordx4 v8, s[4:7], s53 offen offset:0 nt lds\n"
      "buffer_load_dwordx4 v9, s[4:7], s53 offen offset:1024 nt lds\n"
      "s_add_u32 s53, s54, 393216\n"
      "s_add_u32 m0, s37, 86536\n"
      "s_nop 0\n"
      "buffer_load_dwordx4 v8, s[4:7], s53 offen offset:0 nt lds\n"
      "buffer_load_dwordx4 v9, s[4:7], s53 offen offset:1024 nt lds\n"
      "s_add_u32 s53, s54, 589824\n"
      "s_add_u32 m0, s37, 90760\n"
      "s_nop 0\n"
      "buffer_load_dwordx4 v8, s[4:7], s53 offen offset:0 nt lds\n"
      "buffer_load_dwordx4 v9, s[4:7], s53 offen offset:1024 nt lds\n"
      "s_add_u32 s53, s54, 786432\n"
      "s_add_u32 m0, s37, 95104\n"
      "s_nop 0\n"
      "buffer_load_dwordx4 v8, s[4:7], s53 offen offset:0 nt lds\n"
      "buffer_load_dwordx4 v9, s[4:7], s53 offen offset:1024 nt lds\n"
      "s_add_u32 s53, s36, 0\n"
      "s_add_u32 m0, s37, 150272\n"
      "s_nop 0\n"
      "buffer_load_dwordx4 v8, s[4:7], s53 offen offset:0 nt lds\n"
      "buffer_load_dwordx4 v9, s[4:7], s53 offen offset:1024 nt lds\n"
      "v_lshlrev_b32 v43, 4, v37\n"
      "v_lshlrev_b32 v44, 2, v37\n"
      "v_and_b32 v43, 16, v43\n"
      "v_and_or_b32 v43, v44, 8, v43\n"
      "v_mul_u32_u24 v44, 192, v38\n"
      "v_mul_u32_u24 v45, 1536, v39\n"
      "s_lshl_b32 s27, s18, 5\n"
      "v_lshlrev_b32 v46, 1, v36\n"
      "v_add3_u32 v43, v43, v44, v45\n"
      "v_bitop3_b32 v46, v46, s27, 32 bitop3:0x6c\n"
      "v_add_u32 v60, v43, v46\n"
      "v_add_u32 v61, 57344, v60\n"
      "v_add_u32 v62, 114688, v60\n"
      "v_lshlrev_b32 v43, 4, v37\n"
      "s_mul_i32 s26, s17, 0x8080\n"
      "s_sub_u32 s26, 0xbf80, s26\n"
      "s_mov_b32 s27, 0\n"
      "s_lshl_b32 s34, s17, 7\n"
      "s_mov_b32 s29, 0\n"
      "s_sub_u32 s28, s26, s34\n"
      "s_mov_b32 s31, 0\n"
      "s_sub_u32 s30, s28, s34\n"
      "v_lshlrev_b64 v[44:45], v43, s[26:27]\n"
      "v_lshlrev_b64 v[46:47], v43, s[28:29]\n"
      "s_xor_b32 s28, s28, 0x8000\n"
      "v_lshlrev_b64 v[48:49], v43, s[30:31]\n"
      "v_lshlrev_b64 v[50:51], v43, s[28:29]\n"
      "v_accvgpr_write_b32 a244, v44\n"
      "v_accvgpr_write_b32 a245, v45\n"
      "v_accvgpr_write_b32 a246, v46\n"
      "v_accvgpr_write_b32 a247, v47\n"
      "v_accvgpr_write_b32 a248, v48\n"
      "v_accvgpr_write_b32 a249, v49\n"
      "v_accvgpr_write_b32 a250, v50\n"
      "v_accvgpr_write_b32 a251, v51\n"
      "s_lshl_b32 s26, s18, 3\n"
      "v_and_b32 v42, 15, v36\n"
      "v_xor_b32 v42, s26, v42\n"
      "s_lshl_b32 s26, s17, 13\n"
      "s_add_u32 s26, s26, 24832\n"
      "s_mul_i32 s27, s17, 12672\n"
      "v_mul_u32_u24 v44, 192, v42\n"
      "v_lshlrev_b32 v43, 3, v42\n"
      "v_and_b32 v45, 48, v36\n"
      "s_add_u32 s27, s27, 82432\n"
      "v_bitop3_b32 v46, v43, v45, 32 bitop3:0x6c\n"
      "v_add3_u32 v0, v46, v44, s26\n"
      "v_add3_u32 v3, v46, v44, s27\n"
      "v_add_u32 v43, 16, v43\n"
      "v_add_u32 v44, 384, v44\n"
      "v_bitop3_b32 v46, v43, v45, 32 bitop3:0x6c\n"
      "v_add3_u32 v1, v46, v44, s26\n"
      "v_add3_u32 v4, v46, v44, s27\n"
      "v_add_u32 v43, 16, v43\n"
      "v_add_u32 v44, 384, v44\n"
      "v_bitop3_b32 v46, v43, v45, 32 bitop3:0x6c\n"
      "v_add3_u32 v2, v46, v44, s26\n"
      "v_add3_u32 v5, v46, v44, s27\n"
      "v_lshlrev_b32 v43, 31, v36\n"
      "v_and_b32 v44, 2, v36\n"
      "v_or_b32 v43, 0x3f803f80, v43\n"
      "v_mul_u32_u24 v45, 0xc00080, v44\n"
      "v_lshl_add_u32 v44, v44, 22, v43\n"
      "v_add_u32 v45, v45, v43\n"
      "v_accvgpr_write_b32 a216, v44\n"
      "v_accvgpr_write_b32 a217, v45\n"
      "v_lshlrev_b32 v43, 3, v38\n"
      "s_mov_b32 s26, 806420736\n"
      "v_bfe_u32 v43, s26, v43, 8\n"
      "v_lshlrev_b32 v44, 12, v38\n"
      "s_lshl_b32 s28, s18, 8\n"
      "s_add_u32 s28, s28, 82432\n"
      "v_lshl_add_u32 v43, v43, 3, v44\n"
      "v_lshl_add_u32 v44, v39, 2, v37\n"
      "v_lshl_add_u32 v43, v44, 4, v43\n"
      "v_add_u32 v7, s28, v43\n"
      "s_mul_i32 s26, s17, 70144\n"
      "s_lshl_b32 s27, s18, 8\n"
      "s_sub_u32 s26, 70144, s26\n"
      "s_add_u32 s26, s26, s27\n"
      "v_lshl_add_u32 v6, v36, 2, s26\n"
      "v_lshrrev_b32 v43, 3, v36\n"
      "v_and_b32 v44, 7, v36\n"
      "v_mul_u32_u24 v45, 384, v43\n"
      "v_lshl_add_u32 v16, v44, 4, v45\n"
      "v_and_b32 v44, 2, v36\n"
      "v_lshlrev_b32 v43, 3, v43\n"
      "v_lshlrev_b32 v45, 4, v44\n"
      "v_xor_b32 v43, v43, v45\n"
      "v_lshl_add_u32 v43, v44, 7, v43\n"
      "v_and_b32 v44, 1, v36\n"
      "s_lshl_b32 s26, s18, 11\n"
      "v_lshl_add_u32 v43, v44, 6, v43\n"
      "s_add_u32 s26, s26, 12288\n"
      "v_bfe_u32 v44, v36, 2, 1\n"
      "v_add_u32 v43, s26, v43\n"
      "v_mul_i32_i24 v45, 8196, v44\n"
      "s_mov_b32 s27, 0\n"
      "v_add3_u32 v10, v43, v45, s27\n"
      "v_add_u32 v13, 128, v10\n"
      "v_mul_i32_i24 v45, -4092, v44\n"
      "s_mov_b32 s27, 4096\n"
      "v_add3_u32 v11, v43, v45, s27\n"
      "v_add_u32 v14, 128, v11\n"
      "v_mul_i32_i24 v45, -4100, v44\n"
      "s_mov_b32 s27, 8200\n"
      "v_add3_u32 v12, v43, v45, s27\n"
      "v_add_u32 v15, 128, v12\n"
      "s_mul_i32 s62, s18, 393216\n"
      "s_add_u32 s62, s62, s19\n"
      "s_add_u32 s62, s62, s21\n"
      "s_mov_b32 s40, 0x80000000\n"
      "v_lshlrev_b32 v43, 31, v36\n"
      "s_lshl_b32 s26, s18, 1\n"
      "v_bitop3_b32 v44, v36, s26, 2 bitop3:0x28\n"
      "v_or_b32 v43, 0x3f803f80, v43\n"
      "v_mul_u32_u24 v45, 0xc00080, v44\n"
      "v_lshl_add_u32 v44, v44, 22, v43\n"
      "v_add_u32 v45, v45, v43\n"
      "v_accvgpr_write_b32 a218, v44\n"
      "v_accvgpr_write_b32 a219, v45\n"
      "s_lshl_b32 s41, s18, 13\n"
      "s_add_u32 s41, s41, 24832\n"
      "s_and_b32 s9, s9, 0xffff\n"
      "s_mov_b32 s45, 0xbf2aaaab\n"
      "s_mov_b32 s46, 0xbe800000\n"
      "s_mov_b32 s47, 0x3f2aaaab\n"
      "s_mov_b32 s48, 0xc0c00000\n"
      "s_mov_b32 s49, 0x3f000000\n"
      "s_add_u32 m0, s63, 0\n"
      "s_add_u32 s52, s63, 6144\n"
      "buffer_load_dwordx4 v17, s[12:15], s52 offen offset:0  lds\n"
      "buffer_load_dwordx4 v18, s[12:15], s52 offen offset:1024  lds\n"
      "buffer_load_dwordx4 v19, s[12:15], s52 offen offset:2048  lds\n"
      "LDpro_%=:\n"
      "s_waitcnt vmcnt(13)\n"
      "s_barrier\n"
      "ds_read_b64_tr_b16 a[0:1], v60 offset:24832\n"
      "ds_read_b64_tr_b16 a[2:3], v60 offset:25600\n"
      "ds_read_b64_tr_b16 a[224:225], v62 offset:26112\n"
      "ds_read_b64_tr_b16 a[226:227], v62 offset:26880\n"
      "ds_read_b64_tr_b16 a[220:221], v60 offset:30976\n"
      "ds_read_b64_tr_b16 a[222:223], v60 offset:31744\n"
      "ds_read_b64_tr_b16 a[4:5], v60 offset:24896\n"
      "ds_read_b64_tr_b16 a[6:7], v60 offset:25664\n"
      "ds_read_b64_tr_b16 a[232:233], v62 offset:26176\n"
      "ds_read_b64_tr_b16 a[234:235], v62 offset:26944\n"
      "ds_read_b64_tr_b16 a[228:229], v60 offset:31040\n"
      "ds_read_b64_tr_b16 a[230:231], v60 offset:31808\n"
      "s_add_u32 m0, s63, 63744\n"
      "s_add_u32 s52, s63, 122880\n"
      "s_waitcnt lgkmcnt(10)\n"
      "v_mfma_f32_4x4x4_16b_bf16 v[28:31], a[244:245], a[0:1], 0\n"
      "v_mfma_f32_4x4x4_16b_bf16 v[32:35], a[244:245], a[2:3], 0\n"
      "buffer_load_dwordx4 v17, s[12:15], s52 offen offset:0  lds\n"
      "s_waitcnt lgkmcnt(8)\n"
      "v_mfma_f32_4x4x4_16b_bf16 v[28:31], a[248:249], a[224:225], v[28:31]\n"
      "ds_read_b64_tr_b16 a[8:9], v60 offset:24960\n"
      "v_mfma_f32_4x4x4_16b_bf16 v[32:35], a[248:249], a[226:227], v[32:35]\n"
      "buffer_load_dwordx4 v18, s[12:15], s52 offen offset:1024  lds\n"
      "s_waitcnt lgkmcnt(7)\n"
      "v_mfma_f32_4x4x4_16b_bf16 v[36:39], a[246:247], a[220:221], v[28:31]\n"
      "ds_read_b64_tr_b16 a[10:11], v60 offset:25728\n"
      "v_mfma_f32_4x4x4_16b_bf16 v[40:43], a[246:247], a[222:223], v[32:35]\n"
      "buffer_load_dwordx4 v19, s[12:15], s52 offen offset:2048  lds\n"
      "v_mfma_f32_4x4x4_16b_bf16 v[28:31], a[250:251], a[220:221], v[28:31]\n"
      "ds_read_b64_tr_b16 a[240:241], v62 offset:26240\n"
      "v_mfma_f32_4x4x4_16b_bf16 v[32:35], a[250:251], a[222:223], v[32:35]\n"
      "ds_read_b64_tr_b16 a[242:243], v62 offset:27008\n"
      "ds_read_b64_tr_b16 a[236:237], v60 offset:31104\n"
      "ds_read_b64_tr_b16 a[238:239], v60 offset:31872\n"
      "s_add_u32 m0, s63, 154368\n"
      "s_add_u32 s52, s63, 129024\n"
      "s_nop 0\n"
      "s_waitcnt lgkmcnt(5)\n"
      "v_mfma_f32_4x4x4_16b_bf16 v[44:47], a[244:245], a[4:5], 0\n"
      "v_mfma_f32_4x4x4_16b_bf16 v[48:51], a[244:245], a[6:7], 0\n"
      "buffer_load_dwordx4 v17, s[12:15], s52 offen offset:0  lds\n"
      "v_mfma_f32_4x4x4_16b_bf16 v[44:47], a[248:249], a[232:233], v[44:47]\n"
      "buffer_load_dwordx4 v18, s[12:15], s52 offen offset:1024  lds\n"
      "v_mfma_f32_4x4x4_16b_bf16 v[48:51], a[248:249], a[234:235], v[48:51]\n"
      "buffer_load_dwordx4 v19, s[12:15], s52 offen offset:2048  lds\n"
      "v_mfma_f32_4x4x4_16b_bf16 v[52:55], a[246:247], a[228:229], v[44:47]\n"
      "v_mfma_f32_4x4x4_16b_bf16 v[56:59], a[246:247], a[230:231], v[48:51]\n"
      "v_mfma_f32_4x4x4_16b_bf16 v[44:47], a[250:251], a[228:229], v[44:47]\n"
      "v_mfma_f32_4x4x4_16b_bf16 v[48:51], a[250:251], a[230:231], v[48:51]\n"
      "v_cvt_pk_bf16_f32 v252, v36, v40\n"
      "v_cvt_pk_bf16_f32 v253, v37, v41\n"
      "v_cvt_pk_bf16_f32 v254, v38, v42\n"
      "v_cvt_pk_bf16_f32 v255, v39, v43\n"
      "v_cvt_pk_bf16_f32 v248, v28, v32\n"
      "v_cvt_pk_bf16_f32 v249, v29, v33\n"
      "v_cvt_pk_bf16_f32 v250, v30, v34\n"
      "v_cvt_pk_bf16_f32 v251, v31, v35\n"
      "s_waitcnt lgkmcnt(0)\n"
      "v_mfma_f32_4x4x4_16b_bf16 v[28:31], a[244:245], a[8:9], 0\n"
      "v_mfma_f32_4x4x4_16b_bf16 v[32:35], a[244:245], a[10:11], 0\n"
      "s_nop 0\n"
      "v_mfma_f32_4x4x4_16b_bf16 v[28:31], a[248:249], a[240:241], v[28:31]\n"
      "v_mfma_f32_4x4x4_16b_bf16 v[32:35], a[248:249], a[242:243], v[32:35]\n"
      "s_nop 0\n"
      "s_nop 0\n"
      "v_mfma_f32_4x4x4_16b_bf16 v[36:39], a[246:247], a[236:237], v[28:31]\n"
      "v_mfma_f32_4x4x4_16b_bf16 v[40:43], a[246:247], a[238:239], v[32:35]\n"
      "v_mfma_f32_4x4x4_16b_bf16 v[28:31], a[250:251], a[236:237], v[28:31]\n"
      "v_mfma_f32_4x4x4_16b_bf16 v[32:35], a[250:251], a[238:239], v[32:35]\n"
      "v_cvt_pk_bf16_f32 v244, v52, v56\n"
      "v_cvt_pk_bf16_f32 v245, v53, v57\n"
      "v_cvt_pk_bf16_f32 v246, v54, v58\n"
      "v_cvt_pk_bf16_f32 v247, v55, v59\n"
      "v_cvt_pk_bf16_f32 v240, v44, v48\n"
      "v_cvt_pk_bf16_f32 v241, v45, v49\n"
      "v_cvt_pk_bf16_f32 v242, v46, v50\n"
      "v_cvt_pk_bf16_f32 v243, v47, v51\n"
      "s_waitcnt vmcnt(6)\n"
      "s_barrier\n"
      "ds_read_b64_tr_b16 a[12:13], v60 offset:0\n"
      "ds_read_b64_tr_b16 a[14:15], v60 offset:768\n"
      "ds_read_b64_tr_b16 a[224:225], v60 offset:12288\n"
      "ds_read_b64_tr_b16 a[226:227], v60 offset:13056\n"
      "ds_read_b64_tr_b16 a[220:221], v60 offset:6144\n"
      "ds_read_b64_tr_b16 a[222:223], v60 offset:6912\n"
      "s_add_u32 m0, s63, 128512\n"
      "s_add_u32 s52, s63, 135168\n"
      "s_waitcnt lgkmcnt(4)\n"
      "v_mfma_f32_4x4x4_16b_bf16 v[44:47], a[244:245], a[12:13], 0\n"
      "v_mfma_f32_4x4x4_16b_bf16 v[48:51], a[244:245], a[14:15], 0\n"
      "buffer_load_dwordx4 v17, s[12:15], s52 offen offset:0  lds\n"
      "s_waitcnt lgkmcnt(2)\n"
      "v_mfma_f32_4x4x4_16b_bf16 v[44:47], a[248:249], a[224:225], v[44:47]\n"
      "ds_read_b64_tr_b16 a[16:17], v60 offset:64\n"
      "v_mfma_f32_4x4x4_16b_bf16 v[48:51], a[248:249], a[226:227], v[48:51]\n"
      "buffer_load_dwordx4 v18, s[12:15], s52 offen offset:1024  lds\n"
      "s_nop 0\n"
      "s_waitcnt lgkmcnt(1)\n"
      "v_mfma_f32_4x4x4_16b_bf16 v[52:55], a[246:247], a[220:221], v[44:47]\n"
      "ds_read_b64_tr_b16 a[18:19], v60 offset:832\n"
      "v_mfma_f32_4x4x4_16b_bf16 v[56:59], a[246:247], a[222:223], v[48:51]\n"
      "buffer_load_dwordx4 v19, s[12:15], s52 offen offset:2048  lds\n"
      "v_mfma_f32_4x4x4_16b_bf16 v[44:47], a[250:251], a[220:221], v[44:47]\n"
      "ds_read_b64_tr_b16 a[232:233], v60 offset:12352\n"
      "v_mfma_f32_4x4x4_16b_bf16 v[48:51], a[250:251], a[222:223], v[48:51]\n"
      "ds_read_b64_tr_b16 a[234:235], v60 offset:13120\n"
      "ds_read_b64_tr_b16 a[228:229], v60 offset:6208\n"
      "ds_read_b64_tr_b16 a[230:231], v60 offset:6976\n"
      "ds_read_b64_tr_b16 a[20:21], v60 offset:128\n"
      "ds_read_b64_tr_b16 a[22:23], v60 offset:896\n"
      "ds_read_b64_tr_b16 a[240:241], v60 offset:12416\n"
      "ds_read_b64_tr_b16 a[242:243], v60 offset:13184\n"
      "ds_read_b64_tr_b16 a[236:237], v60 offset:6272\n"
      "ds_read_b64_tr_b16 a[238:239], v60 offset:7040\n"
      "ds_read_b64_tr_b16 a[168:169], v7 offset:2048\n"
      "ds_read_b64_tr_b16 a[170:171], v7 offset:2056\n"
      "ds_read_b128 a[204:207], v3 offset:55168\n"
      "v_cvt_pk_bf16_f32 v236, v36, v40\n"
      "v_cvt_pk_bf16_f32 v237, v37, v41\n"
      "v_cvt_pk_bf16_f32 v238, v38, v42\n"
      "v_cvt_pk_bf16_f32 v239, v39, v43\n"
      "v_cvt_pk_bf16_f32 v232, v28, v32\n"
      "v_cvt_pk_bf16_f32 v233, v29, v33\n"
      "v_cvt_pk_bf16_f32 v234, v30, v34\n"
      "v_cvt_pk_bf16_f32 v235, v31, v35\n"
      "s_waitcnt vmcnt(6)\n"
      "s_barrier\n"
      "s_waitcnt lgkmcnt(8)\n"
      "v_mfma_f32_4x4x4_16b_bf16 v[28:31], a[244:245], a[16:17], 0\n"
      "v_mfma_f32_4x4x4_16b_bf16 v[32:35], a[244:245], a[18:19], 0\n"
      "ds_read_b64_tr_b16 a[24:25], v60 offset:18432\n"
      "v_mfma_f32_4x4x4_16b_bf16 v[28:31], a[248:249], a[232:233], v[28:31]\n"
      "ds_read_b64_tr_b16 a[26:27], v60 offset:19200\n"
      "v_mfma_f32_4x4x4_16b_bf16 v[32:35], a[248:249], a[234:235], v[32:35]\n"
      "ds_read_b64_tr_b16 a[224:225], v61 offset:6400\n"
      "v_mfma_f32_4x4x4_16b_bf16 v[36:39], a[246:247], a[228:229], v[28:31]\n"
      "ds_read_b64_tr_b16 a[226:227], v61 offset:7168\n"
      "v_mfma_f32_4x4x4_16b_bf16 v[40:43], a[246:247], a[230:231], v[32:35]\n"
      "ds_read_b64_tr_b16 a[220:221], v61 offset:256\n"
      "v_mfma_f32_4x4x4_16b_bf16 v[28:31], a[250:251], a[228:229], v[28:31]\n"
      "ds_read_b64_tr_b16 a[222:223], v61 offset:1024\n"
      "v_mfma_f32_4x4x4_16b_bf16 v[32:35], a[250:251], a[230:231], v[32:35]\n"
      "ds_read_b64_tr_b16 a[172:173], v7 offset:2560\n"
      "s_waitcnt lgkmcnt(7)\n"
      "v_mfma_f32_4x4x4_16b_bf16 v[20:23], a[216:217], a[168:169], 0\n"
      "ds_read_b64_tr_b16 a[174:175], v7 offset:2568\n"
      "v_mfma_f32_4x4x4_16b_bf16 v[24:27], a[216:217], a[170:171], 0\n"
      "ds_read_b128 a[208:211], v3 offset:55232\n"
      "v_cvt_pk_bf16_f32 v228, v52, v56\n"
      "v_cvt_pk_bf16_f32 v229, v53, v57\n"
      "v_cvt_pk_bf16_f32 v230, v54, v58\n"
      "v_cvt_pk_bf16_f32 v231, v55, v59\n"
      "v_cvt_pk_bf16_f32 v224, v44, v48\n"
      "v_cvt_pk_bf16_f32 v225, v45, v49\n"
      "v_cvt_pk_bf16_f32 v226, v46, v50\n"
      "v_cvt_pk_bf16_f32 v227, v47, v51\n"
      "s_add_u32 m0, s63, 134656\n"
      "s_add_u32 s52, s63, 141312\n"
      "v_mfma_f32_4x4x4_16b_bf16 v[44:47], a[244:245], a[20:21], 0\n"
      "v_mfma_f32_4x4x4_16b_bf16 v[48:51], a[244:245], a[22:23], 0\n"
      "buffer_load_dwordx4 v17, s[12:15], s52 offen offset:0  lds\n"
      "v_mfma_f32_4x4x4_16b_bf16 v[44:47], a[248:249], a[240:241], v[44:47]\n"
      "ds_read_b64_tr_b16 a[28:29], v60 offset:18496\n"
      "v_mfma_f32_4x4x4_16b_bf16 v[48:51], a[248:249], a[242:243], v[48:51]\n"
      "buffer_load_dwordx4 v18, s[12:15], s52 offen offset:1024  lds\n"
      "v_mfma_f32_4x4x4_16b_bf16 v[52:55], a[246:247], a[236:237], v[44:47]\n"
      "ds_read_b64_tr_b16 a[30:31], v60 offset:19264\n"
      "v_mfma_f32_4x4x4_16b_bf16 v[56:59], a[246:247], a[238:239], v[48:51]\n"
      "buffer_load_dwordx4 v19, s[12:15], s52 offen offset:2048  lds\n"
      "v_mfma_f32_4x4x4_16b_bf16 v[44:47], a[250:251], a[236:237], v[44:47]\n"
      "ds_read_b64_tr_b16 a[232:233], v61 offset:6464\n"
      "v_mfma_f32_4x4x4_16b_bf16 v[48:51], a[250:251], a[238:239], v[48:51]\n"
      "s_add_u32 s53, s54, 983040\n"
      "s_nop 0\n"
      "s_waitcnt lgkmcnt(3)\n"
      "v_mfma_f32_4x4x4_16b_bf16 v[100:103], a[216:217], a[172:173], 0\n"
      "ds_read_b64_tr_b16 a[234:235], v61 offset:7232\n"
      "v_mfma_f32_4x4x4_16b_bf16 v[104:107], a[216:217], a[174:175], 0\n"
      "s_add_u32 m0, s37, 99328\n"
      "ds_read_b64_tr_b16 a[228:229], v61 offset:320\n"
      "buffer_load_dwordx4 v8, s[4:7], s53 offen offset:0 nt lds\n"
      "buffer_load_dwordx4 v9, s[4:7], s53 offen offset:1024 nt lds\n"
      "ds_read_b64_tr_b16 a[230:231], v61 offset:1088\n"
      "s_add_u32 s53, s54, 1179648\n"
      "ds_read_b64_tr_b16 a[176:177], v7 offset:3072\n"
      "s_add_u32 m0, s37, 103432\n"
      "ds_read_b64_tr_b16 a[178:179], v7 offset:3080\n"
      "buffer_load_dwordx4 v8, s[4:7], s53 offen offset:0 nt lds\n"
      "buffer_load_dwordx4 v9, s[4:7], s53 offen offset:1024 nt lds\n"
      "v_cvt_pk_bf16_f32 v220, v36, v40\n"
      "v_cvt_pk_bf16_f32 v221, v37, v41\n"
      "v_cvt_pk_bf16_f32 v222, v38, v42\n"
      "v_cvt_pk_bf16_f32 v223, v39, v43\n"
      "v_cvt_pk_bf16_f32 v216, v28, v32\n"
      "v_cvt_pk_bf16_f32 v217, v29, v33\n"
      "v_mfma_f32_16x16x32_bf16 v[64:67], a[0:3], a[204:207], 0\n"
      "v_cvt_pk_bf16_f32 v218, v30, v34\n"
      "v_cvt_pk_bf16_f32 v219, v31, v35\n"
      "v_mfma_f32_16x16x32_bf16 v[68:71], a[4:7], a[204:207], 0\n"
      "s_add_u32 m0, s37, 107656\n"
      "v_cvt_pk_bf16_f32 v20, v20, v24\n"
      "v_cvt_pk_bf16_f32 v21, v21, v25\n"
      "v_mfma_f32_16x16x32_bf16 v[72:75], a[8:11], a[204:207], 0\n"
      "s_add_u32 s53, s54, 1376256\n"
      "v_cvt_pk_bf16_f32 v22, v22, v26\n"
      "v_cvt_pk_bf16_f32 v23, v23, v27\n"
      "v_mfma_f32_4x4x4_16b_bf16 v[28:31], a[244:245], a[24:25], 0\n"
      "v_mfma_f32_4x4x4_16b_bf16 v[32:35], a[244:245], a[26:27], 0\n"
      "buffer_load_dwordx4 v8, s[4:7], s53 offen offset:0 nt lds\n"
      "buffer_load_dwordx4 v9, s[4:7], s53 offen offset:1024 nt lds\n"
      "v_mfma_f32_4x4x4_16b_bf16 v[28:31], a[248:249], a[224:225], v[28:31]\n"
      "ds_read_b64_tr_b16 a[32:33], v60 offset:18560\n"
      "v_mfma_f32_4x4x4_16b_bf16 v[32:35], a[248:249], a[226:227], v[32:35]\n"
      "s_add_u32 s53, s54, 1572864\n"
      "v_mfma_f32_4x4x4_16b_bf16 v[36:39], a[246:247], a[220:221], v[28:31]\n"
      "ds_read_b64_tr_b16 a[34:35], v60 offset:19328\n"
      "v_mfma_f32_4x4x4_16b_bf16 v[40:43], a[246:247], a[222:223], v[32:35]\n"
      "s_add_u32 m0, s37, 112000\n"
      "v_mfma_f32_4x4x4_16b_bf16 v[28:31], a[250:251], a[220:221], v[28:31]\n"
      "ds_read_b64_tr_b16 a[240:241], v61 offset:6528\n"
      "v_mfma_f32_4x4x4_16b_bf16 v[32:35], a[250:251], a[222:223], v[32:35]\n"
      "buffer_load_dwordx4 v8, s[4:7], s53 offen offset:0 nt lds\n"
      "buffer_load_dwordx4 v9, s[4:7], s53 offen offset:1024 nt lds\n"
      "s_nop 0\n"
      "s_waitcnt lgkmcnt(3)\n"
      "v_mfma_f32_4x4x4_16b_bf16 v[108:111], a[216:217], a[176:177], 0\n"
      "ds_read_b64_tr_b16 a[242:243], v61 offset:7296\n"
      "v_mfma_f32_4x4x4_16b_bf16 v[112:115], a[216:217], a[178:179], 0\n"
      "ds_read_b64_tr_b16 a[236:237], v61 offset:384\n"
      "ds_read_b64_tr_b16 a[238:239], v61 offset:1152\n"
      "ds_read_b64_tr_b16 a[180:181], v7 offset:3584\n"
      "ds_read_b64_tr_b16 a[182:183], v7 offset:3592\n"
      "s_mov_b32 m0, s64\n"
      "s_nop 0\n"
      "ds_write_addtid_b32 v20 offset:2048\n"
      "ds_write_addtid_b32 v21 offset:6144\n"
      "ds_write_addtid_b32 v22 offset:10240\n"
      "ds_write_addtid_b32 v23 offset:14336\n"
      "v_cvt_pk_bf16_f32 v212, v52, v56\n"
      "v_cvt_pk_bf16_f32 v213, v53, v57\n"
      "v_cvt_pk_bf16_f32 v214, v54, v58\n"
      "v_cvt_pk_bf16_f32 v215, v55, v59\n"
      "v_cvt_pk_bf16_f32 v208, v44, v48\n"
      "v_cvt_pk_bf16_f32 v209, v45, v49\n"
      "v_mfma_f32_16x16x32_bf16 v[64:67], a[12:15], a[208:211], v[64:67]\n"
      "v_cvt_pk_bf16_f32 v210, v46, v50\n"
      "v_cvt_pk_bf16_f32 v211, v47, v51\n"
      "v_mfma_f32_16x16x32_bf16 v[68:71], a[16:19], a[208:211], v[68:71]\n"
      "v_cvt_pk_bf16_f32 v100, v100, v104\n"
      "v_cvt_pk_bf16_f32 v101, v101, v105\n"
      "v_mfma_f32_16x16x32_bf16 v[72:75], a[20:23], a[208:211], v[72:75]\n"
      "s_waitcnt vmcnt(14)\n"
      "v_cvt_pk_bf16_f32 v102, v102, v106\n"
      "v_cvt_pk_bf16_f32 v103, v103, v107\n"
      "s_barrier\n"
      "v_mfma_f32_4x4x4_16b_bf16 v[44:47], a[244:245], a[28:29], 0\n"
      "v_mfma_f32_4x4x4_16b_bf16 v[48:51], a[244:245], a[30:31], 0\n"
      "ds_read_b64_tr_b16 a[36:37], v61 offset:12544\n"
      "v_mfma_f32_4x4x4_16b_bf16 v[44:47], a[248:249], a[232:233], v[44:47]\n"
      "ds_read_b64_tr_b16 a[38:39], v61 offset:13312\n"
      "v_mfma_f32_4x4x4_16b_bf16 v[48:51], a[248:249], a[234:235], v[48:51]\n"
      "ds_read_b64_tr_b16 a[224:225], v62 offset:39680\n"
      "v_mfma_f32_4x4x4_16b_bf16 v[52:55], a[246:247], a[228:229], v[44:47]\n"
      "ds_read_b64_tr_b16 a[226:227], v62 offset:40448\n"
      "v_mfma_f32_4x4x4_16b_bf16 v[56:59], a[246:247], a[230:231], v[48:51]\n"
      "ds_read_b64_tr_b16 a[220:221], v61 offset:18688\n"
      "v_mfma_f32_4x4x4_16b_bf16 v[44:47], a[250:251], a[228:229], v[44:47]\n"
      "ds_read_b64_tr_b16 a[222:223], v61 offset:19456\n"
      "v_mfma_f32_4x4x4_16b_bf16 v[48:51], a[250:251], a[230:231], v[48:51]\n"
      "ds_write_addtid_b32 v100 offset:2560\n"
      "s_nop 0\n"
      "s_waitcnt lgkmcnt(7)\n"
      "v_mfma_f32_4x4x4_16b_bf16 v[116:119], a[216:217], a[180:181], 0\n"
      "ds_write_addtid_b32 v101 offset:6656\n"
      "v_mfma_f32_4x4x4_16b_bf16 v[120:123], a[216:217], a[182:183], 0\n"
      "ds_write_addtid_b32 v102 offset:10752\n"
      "ds_write_addtid_b32 v103 offset:14848\n"
      "ds_read_b128 a[212:215], v3 offset:55296\n"
      "v_cvt_pk_bf16_f32 v204, v36, v40\n"
      "v_cvt_pk_bf16_f32 v205, v37, v41\n"
      "v_cvt_pk_bf16_f32 v206, v38, v42\n"
      "v_cvt_pk_bf16_f32 v207, v39, v43\n"
      "v_cvt_pk_bf16_f32 v200, v28, v32\n"
      "v_cvt_pk_bf16_f32 v201, v29, v33\n"
      "v_cvt_pk_bf16_f32 v202, v30, v34\n"
      "v_cvt_pk_bf16_f32 v203, v31, v35\n"
      "v_cvt_pk_bf16_f32 v108, v108, v112\n"
      "v_cvt_pk_bf16_f32 v109, v109, v113\n"
      "v_cvt_pk_bf16_f32 v110, v110, v114\n"
      "v_cvt_pk_bf16_f32 v111, v111, v115\n"
      "v_mfma_f32_4x4x4_16b_bf16 v[28:31], a[244:245], a[32:33], 0\n"
      "v_mfma_f32_4x4x4_16b_bf16 v[32:35], a[244:245], a[34:35], 0\n"
      "ds_read_b64_tr_b16 a[40:41], v61 offset:12608\n"
      "v_mfma_f32_4x4x4_16b_bf16 v[28:31], a[248:249], a[240:241], v[28:31]\n"
      "ds_read_b64_tr_b16 a[42:43], v61 offset:13376\n"
      "v_mfma_f32_4x4x4_16b_bf16 v[32:35], a[248:249], a[242:243], v[32:35]\n"
      "ds_read_b64_tr_b16 a[232:233], v62 offset:39744\n"
      "v_mfma_f32_4x4x4_16b_bf16 v[36:39], a[246:247], a[236:237], v[28:31]\n"
      "ds_read_b64_tr_b16 a[234:235], v62 offset:40512\n"
      "v_mfma_f32_4x4x4_16b_bf16 v[40:43], a[246:247], a[238:239], v[32:35]\n"
      "ds_read_b64_tr_b16 a[228:229], v61 offset:18752\n"
      "v_mfma_f32_4x4x4_16b_bf16 v[28:31], a[250:251], a[236:237], v[28:31]\n"
      "ds_read_b64_tr_b16 a[230:231], v61 offset:19520\n"
      "v_mfma_f32_4x4x4_16b_bf16 v[32:35], a[250:251], a[238:239], v[32:35]\n"
      "ds_write_addtid_b32 v108 offset:3072\n"
      "ds_write_addtid_b32 v109 offset:7168\n"
      "ds_write_addtid_b32 v110 offset:11264\n"
      "ds_write_addtid_b32 v111 offset:15360\n"
      "v_cvt_pk_bf16_f32 v196, v52, v56\n"
      "v_cvt_pk_bf16_f32 v197, v53, v57\n"
      "v_cvt_pk_bf16_f32 v198, v54, v58\n"
      "v_cvt_pk_bf16_f32 v199, v55, v59\n"
      "v_cvt_pk_bf16_f32 v192, v44, v48\n"
      "v_cvt_pk_bf16_f32 v193, v45, v49\n"
      "v_cvt_pk_bf16_f32 v194, v46, v50\n"
      "v_cvt_pk_bf16_f32 v195, v47, v51\n"
      "v_cvt_pk_bf16_f32 v116, v116, v120\n"
      "v_cvt_pk_bf16_f32 v117, v117, v121\n"
      "v_cvt_pk_bf16_f32 v118, v118, v122\n"
      "v_cvt_pk_bf16_f32 v119, v119, v123\n"
      "s_add_u32 m0, s63, 12288\n"
      "s_add_u32 s52, s63, 147456\n"
      "s_nop 0\n"
      "s_waitcnt lgkmcnt(3)\n"
      "v_mfma_f32_4x4x4_16b_bf16 v[44:47], a[244:245], a[36:37], 0\n"
      "v_mfma_f32_4x4x4_16b_bf16 v[48:51], a[244:245], a[38:39], 0\n"
      "buffer_load_dwordx4 v17, s[12:15], s52 offen offset:0  lds\n"
      "v_mfma_f32_4x4x4_16b_bf16 v[44:47], a[248:249], a[224:225], v[44:47]\n"
      "ds_read_b64_tr_b16 a[44:45], v61 offset:12672\n"
      "v_mfma_f32_4x4x4_16b_bf16 v[48:51], a[248:249], a[226:227], v[48:51]\n"
      "buffer_load_dwordx4 v18, s[12:15], s52 offen offset:1024  lds\n"
      "v_mfma_f32_4x4x4_16b_bf16 v[52:55], a[246:247], a[220:221], v[44:47]\n"
      "ds_read_b64_tr_b16 a[46:47], v61 offset:13440\n"
      "v_mfma_f32_4x4x4_16b_bf16 v[56:59], a[246:247], a[222:223], v[48:51]\n"
      "buffer_load_dwordx4 v19, s[12:15], s52 offen offset:2048  lds\n"
      "v_mfma_f32_4x4x4_16b_bf16 v[44:47], a[250:251], a[220:221], v[44:47]\n"
      "ds_read_b64_tr_b16 a[240:241], v62 offset:39808\n"
      "v_mfma_f32_4x4x4_16b_bf16 v[48:51], a[250:251], a[222:223], v[48:51]\n"
      "ds_read_b64_tr_b16 a[242:243], v62 offset:40576\n"
      "ds_read_b64_tr_b16 a[236:237], v61 offset:18816\n"
      "ds_read_b64_tr_b16 a[238:239], v61 offset:19584\n"
      "s_mov_b32 m0, s64\n"
      "s_nop 0\n"
      "ds_write_addtid_b32 v116 offset:3584\n"
      "ds_write_addtid_b32 v117 offset:7680\n"
      "ds_write_addtid_b32 v118 offset:11776\n"
      "ds_write_addtid_b32 v119 offset:15872\n"
      "v_cvt_pk_bf16_f32 v188, v36, v40\n"
      "v_cvt_pk_bf16_f32 v189, v37, v41\n"
      "v_mfma_f32_16x16x32_bf16 v[64:67], a[24:27], a[212:215], v[64:67]\n"
      "v_cvt_pk_bf16_f32 v190, v38, v42\n"
      "v_cvt_pk_bf16_f32 v191, v39, v43\n"
      "v_mfma_f32_16x16x32_bf16 v[68:71], a[28:31], a[212:215], v[68:71]\n"
      "s_waitcnt vmcnt(14)\n"
      "v_cvt_pk_bf16_f32 v184, v28, v32\n"
      "v_cvt_pk_bf16_f32 v185, v29, v33\n"
      "v_mfma_f32_16x16x32_bf16 v[72:75], a[32:35], a[212:215], v[72:75]\n"
      "s_waitcnt lgkmcnt(0)\n"
      "v_cvt_pk_bf16_f32 v186, v30, v34\n"
      "v_cvt_pk_bf16_f32 v187, v31, v35\n"
      "s_nop 0\n"
      "s_barrier\n"
      "v_mfma_f32_4x4x4_16b_bf16 v[28:31], a[244:245], a[40:41], 0\n"
      "v_mfma_f32_4x4x4_16b_bf16 v[32:35], a[244:245], a[42:43], 0\n"
      "ds_read_b64_tr_b16 a[48:49], v62 offset:1536\n"
      "v_mfma_f32_4x4x4_16b_bf16 v[28:31], a[248:249], a[232:233], v[28:31]\n"
      "ds_read_b64_tr_b16 a[50:51], v62 offset:2304\n"
      "v_mfma_f32_4x4x4_16b_bf16 v[32:35], a[248:249], a[234:235], v[32:35]\n"
      "ds_read_b64_tr_b16 a[224:225], v62 offset:13824\n"
      "v_mfma_f32_4x4x4_16b_bf16 v[36:39], a[246:247], a[228:229], v[28:31]\n"
      "ds_read_b64_tr_b16 a[226:227], v62 offset:14592\n"
      "v_mfma_f32_4x4x4_16b_bf16 v[40:43], a[246:247], a[230:231], v[32:35]\n"
      "ds_read_b64_tr_b16 a[220:221], v62 offset:7680\n"
      "v_mfma_f32_4x4x4_16b_bf16 v[28:31], a[250:251], a[228:229], v[28:31]\n"
      "ds_read_b64_tr_b16 a[222:223], v62 offset:8448\n"
      "v_mfma_f32_4x4x4_16b_bf16 v[32:35], a[250:251], a[230:231], v[32:35]\n"
      "ds_read_b128 a[192:195], v4 offset:55168\n"
      "ds_read_b128 a[196:199], v0 offset:0\n"
      "ds_read_b128 a[200:203], v0 offset:4096\n"
      "v_cvt_pk_bf16_f32 v180, v52, v56\n"
      "v_cvt_pk_bf16_f32 v181, v53, v57\n"
      "v_cvt_pk_bf16_f32 v182, v54, v58\n"
      "v_cvt_pk_bf16_f32 v183, v55, v59\n"
      "v_cvt_pk_bf16_f32 v176, v44, v48\n"
      "v_cvt_pk_bf16_f32 v177, v45, v49\n"
      "v_cvt_pk_bf16_f32 v178, v46, v50\n"
      "v_cvt_pk_bf16_f32 v179, v47, v51\n"
      "v_mfma_f32_4x4x4_16b_bf16 v[44:47], a[244:245], a[44:45], 0\n"
      "v_mfma_f32_4x4x4_16b_bf16 v[48:51], a[244:245], a[46:47], 0\n"
      "ds_read_b64_tr_b16 a[52:53], v62 offset:1600\n"
      "v_mfma_f32_4x4x4_16b_bf16 v[44:47], a[248:249], a[240:241], v[44:47]\n"
      "ds_read_b64_tr_b16 a[54:55], v62 offset:2368\n"
      "v_mfma_f32_4x4x4_16b_bf16 v[48:51], a[248:249], a[242:243], v[48:51]\n"
      "ds_read_b64_tr_b16 a[232:233], v62 offset:13888\n"
      "v_mfma_f32_4x4x4_16b_bf16 v[52:55], a[246:247], a[236:237], v[44:47]\n"
      "ds_read_b64_tr_b16 a[234:235], v62 offset:14656\n"
      "v_mfma_f32_4x4x4_16b_bf16 v[56:59], a[246:247], a[238:239], v[48:51]\n"
      "ds_read_b64_tr_b16 a[228:229], v62 offset:7744\n"
      "v_mfma_f32_4x4x4_16b_bf16 v[44:47], a[250:251], a[236:237], v[44:47]\n"
      "ds_read_b64_tr_b16 a[230:231], v62 offset:8512\n"
      "v_mfma_f32_4x4x4_16b_bf16 v[48:51], a[250:251], a[238:239], v[48:51]\n"
      "ds_read_b128 a[204:207], v0 offset:64\n"
      "ds_read_b128 a[208:211], v0 offset:4160\n"
      "ds_read_b128 a[212:215], v0 offset:128\n"
      "v_cvt_pk_bf16_f32 v172, v36, v40\n"
      "v_cvt_pk_bf16_f32 v173, v37, v41\n"
      "v_cvt_pk_bf16_f32 v174, v38, v42\n"
      "v_cvt_pk_bf16_f32 v175, v39, v43\n"
      "v_cvt_pk_bf16_f32 v168, v28, v32\n"
      "v_cvt_pk_bf16_f32 v169, v29, v33\n"
      "v_cvt_pk_bf16_f32 v170, v30, v34\n"
      "v_cvt_pk_bf16_f32 v171, v31, v35\n"
      "s_add_u32 m0, s63, 63744\n"
      "s_add_u32 s52, s63, 153600\n"
      "s_nop 0\n"
      "s_waitcnt lgkmcnt(4)\n"
      "v_mfma_f32_4x4x4_16b_bf16 v[28:31], a[244:245], a[48:49], 0\n"
      "v_mfma_f32_4x4x4_16b_bf16 v[32:35], a[244:245], a[50:51], 0\n"
      "buffer_load_dwordx4 v17, s[12:15], s52 offen offset:0  lds\n"
      "v_mfma_f32_4x4x4_16b_bf16 v[28:31], a[248:249], a[224:225], v[28:31]\n"
      "ds_read_b64_tr_b16 a[56:57], v62 offset:1664\n"
      "v_mfma_f32_4x4x4_16b_bf16 v[32:35], a[248:249], a[226:227], v[32:35]\n"
      "buffer_load_dwordx4 v18, s[12:15], s52 offen offset:1024  lds\n"
      "v_mfma_f32_4x4x4_16b_bf16 v[36:39], a[246:247], a[220:221], v[28:31]\n"
      "ds_read_b64_tr_b16 a[58:59], v62 offset:2432\n"
      "v_mfma_f32_4x4x4_16b_bf16 v[40:43], a[246:247], a[222:223], v[32:35]\n"
      "buffer_load_dwordx4 v19, s[12:15], s52 offen offset:2048  lds\n"
      "v_mfma_f32_4x4x4_16b_bf16 v[28:31], a[250:251], a[220:221], v[28:31]\n"
      "ds_read_b64_tr_b16 a[240:241], v62 offset:13952\n"
      "v_mfma_f32_4x4x4_16b_bf16 v[32:35], a[250:251], a[222:223], v[32:35]\n"
      "ds_read_b64_tr_b16 a[242:243], v62 offset:14720\n"
      "ds_read_b64_tr_b16 a[236:237], v62 offset:7808\n"
      "ds_read_b64_tr_b16 a[238:239], v62 offset:8576\n"
      "v_mfma_f32_16x16x32_bf16 v[64:67], a[36:39], a[192:195], v[64:67]\n"
      "v_cvt_pk_bf16_f32 v164, v52, v56\n"
      "v_cvt_pk_bf16_f32 v165, v53, v57\n"
      "v_mfma_f32_16x16x32_bf16 v[68:71], a[40:43], a[192:195], v[68:71]\n"
      "v_cvt_pk_bf16_f32 v166, v54, v58\n"
      "v_cvt_pk_bf16_f32 v167, v55, v59\n"
      "v_mfma_f32_16x16x32_bf16 v[72:75], a[44:47], a[192:195], v[72:75]\n"
      "v_cvt_pk_bf16_f32 v160, v44, v48\n"
      "v_cvt_pk_bf16_f32 v161, v45, v49\n"
      "v_mfma_f32_16x16x32_bf16 v[76:79], v[252:255], a[196:199], 0\n"
      "s_waitcnt vmcnt(14)\n"
      "v_cvt_pk_bf16_f32 v162, v46, v50\n"
      "v_cvt_pk_bf16_f32 v163, v47, v51\n"
      "s_barrier\n"
      "s_add_u32 m0, s63, 154368\n"
      "s_add_u32 s52, s63, 159744\n"
      "v_mfma_f32_4x4x4_16b_bf16 v[44:47], a[244:245], a[52:53], 0\n"
      "v_mfma_f32_4x4x4_16b_bf16 v[48:51], a[244:245], a[54:55], 0\n"
      "buffer_load_dwordx4 v17, s[12:15], s52 offen offset:0  lds\n"
      "v_mfma_f32_4x4x4_16b_bf16 v[44:47], a[248:249], a[232:233], v[44:47]\n"
      "ds_read_b64_tr_b16 a[60:61], v60 offset:41216\n"
      "v_mfma_f32_4x4x4_16b_bf16 v[48:51], a[248:249], a[234:235], v[48:51]\n"
      "buffer_load_dwordx4 v18, s[12:15], s52 offen offset:1024  lds\n"
      "s_nop 0\n"
      "s_waitcnt lgkmcnt(1)\n"
      "v_mfma_f32_4x4x4_16b_bf16 v[52:55], a[246:247], a[228:229], v[44:47]\n"
      "ds_read_b64_tr_b16 a[62:63], v60 offset:41984\n"
      "v_mfma_f32_4x4x4_16b_bf16 v[56:59], a[246:247], a[230:231], v[48:51]\n"
      "buffer_load_dwordx4 v19, s[12:15], s52 offen offset:2048  lds\n"
      "v_mfma_f32_4x4x4_16b_bf16 v[44:47], a[250:251], a[228:229], v[44:47]\n"
      "ds_read_b64_tr_b16 a[224:225], v62 offset:19968\n"
      "v_mfma_f32_4x4x4_16b_bf16 v[48:51], a[250:251], a[230:231], v[48:51]\n"
      "ds_read_b64_tr_b16 a[226:227], v62 offset:20736\n"
      "ds_read_b64_tr_b16 a[220:221], v60 offset:47360\n"
      "ds_read_b64_tr_b16 a[222:223], v60 offset:48128\n"
      "ds_read_b128 a[192:195], v0 offset:4224\n"
      "v_mfma_f32_16x16x32_bf16 v[80:83], v[244:247], a[196:199], 0\n"
      "v_cvt_pk_bf16_f32 v156, v36, v40\n"
      "v_cvt_pk_bf16_f32 v157, v37, v41\n"
      "v_mfma_f32_16x16x32_bf16 v[84:87], v[236:239], a[196:199], 0\n"
      "v_cvt_pk_bf16_f32 v158, v38, v42\n"
      "v_cvt_pk_bf16_f32 v159, v39, v43\n"
      "v_mfma_f32_16x16x32_bf16 v[88:91], v[248:251], a[200:203], 0\n"
      "v_cvt_pk_bf16_f32 v152, v28, v32\n"
      "v_cvt_pk_bf16_f32 v153, v29, v33\n"
      "v_mfma_f32_16x16x32_bf16 v[92:95], v[240:243], a[200:203], 0\n"
      "v_cvt_pk_bf16_f32 v154, v30, v34\n"
      "v_cvt_pk_bf16_f32 v155, v31, v35\n"
      "v_mfma_f32_4x4x4_16b_bf16 v[28:31], a[244:245], a[56:57], 0\n"
      "v_mfma_f32_4x4x4_16b_bf16 v[32:35], a[244:245], a[58:59], 0\n"
      "ds_read_b64_tr_b16 a[64:65], v60 offset:41280\n"
      "v_mfma_f32_4x4x4_16b_bf16 v[28:31], a[248:249], a[240:241], v[28:31]\n"
      "ds_read_b64_tr_b16 a[66:67], v60 offset:42048\n"
      "v_mfma_f32_4x4x4_16b_bf16 v[32:35], a[248:249], a[242:243], v[32:35]\n"
      "ds_read_b64_tr_b16 a[232:233], v62 offset:20032\n"
      "v_mfma_f32_4x4x4_16b_bf16 v[36:39], a[246:247], a[236:237], v[28:31]\n"
      "ds_read_b64_tr_b16 a[234:235], v62 offset:20800\n"
      "v_mfma_f32_4x4x4_16b_bf16 v[40:43], a[246:247], a[238:239], v[32:35]\n"
      "ds_read_b64_tr_b16 a[228:229], v60 offset:47424\n"
      "v_mfma_f32_4x4x4_16b_bf16 v[28:31], a[250:251], a[236:237], v[28:31]\n"
      "ds_read_b64_tr_b16 a[230:231], v60 offset:48192\n"
      "v_mfma_f32_4x4x4_16b_bf16 v[32:35], a[250:251], a[238:239], v[32:35]\n"
      "ds_read_b128 a[196:199], v1 offset:0\n"
      "v_mfma_f32_16x16x32_bf16 v[96:99], v[232:235], a[200:203], 0\n"
      "v_cvt_pk_bf16_f32 v148, v52, v56\n"
      "v_cvt_pk_bf16_f32 v149, v53, v57\n"
      "v_mfma_f32_16x16x32_bf16 v[76:79], v[228:231], a[204:207], v[76:79]\n"
      "v_cvt_pk_bf16_f32 v150, v54, v58\n"
      "v_cvt_pk_bf16_f32 v151, v55, v59\n"
      "v_mfma_f32_16x16x32_bf16 v[80:83], v[220:223], a[204:207], v[80:83]\n"
      "s_nop 0\n"
      "v_cvt_pk_bf16_f32 v144, v44, v48\n"
      "v_cvt_pk_bf16_f32 v145, v45, v49\n"
      "v_mfma_f32_16x16x32_bf16 v[84:87], v[212:215], a[204:207], v[84:87]\n"
      "s_waitcnt lgkmcnt(1)\n"
      "v_cvt_pk_bf16_f32 v146, v46, v50\n"
      "v_cvt_pk_bf16_f32 v147, v47, v51\n"
      "v_mfma_f32_4x4x4_16b_bf16 v[44:47], a[244:245], a[60:61], 0\n"
      "v_mfma_f32_4x4x4_16b_bf16 v[48:51], a[244:245], a[62:63], 0\n"
      "ds_read_b64_tr_b16 a[68:69], v60 offset:41344\n"
      "v_mfma_f32_4x4x4_16b_bf16 v[44:47], a[248:249], a[224:225], v[44:47]\n"
      "ds_read_b64_tr_b16 a[70:71], v60 offset:42112\n"
      "v_mfma_f32_4x4x4_16b_bf16 v[48:51], a[248:249], a[226:227], v[48:51]\n"
      "ds_read_b64_tr_b16 a[240:241], v62 offset:20096\n"
      "v_mfma_f32_4x4x4_16b_bf16 v[52:55], a[246:247], a[220:221], v[44:47]\n"
      "ds_read_b64_tr_b16 a[242:243], v62 offset:20864\n"
      "v_mfma_f32_4x4x4_16b_bf16 v[56:59], a[246:247], a[222:223], v[48:51]\n"
      "ds_read_b64_tr_b16 a[236:237], v60 offset:47488\n"
      "v_mfma_f32_4x4x4_16b_bf16 v[44:47], a[250:251], a[220:221], v[44:47]\n"
      "ds_read_b64_tr_b16 a[238:239], v60 offset:48256\n"
      "v_mfma_f32_4x4x4_16b_bf16 v[48:51], a[250:251], a[222:223], v[48:51]\n"
      "ds_read_b128 a[200:203], v1 offset:4096\n"
      "ds_read_b128 a[204:207], v4 offset:55232\n"
      "v_mfma_f32_16x16x32_bf16 v[88:91], v[224:227], a[208:211], v[88:91]\n"
      "v_cvt_pk_bf16_f32 v140, v36, v40\n"
      "v_cvt_pk_bf16_f32 v141, v37, v41\n"
      "v_mfma_f32_16x16x32_bf16 v[92:95], v[216:219], a[208:211], v[92:95]\n"
      "v_cvt_pk_bf16_f32 v142, v38, v42\n"
      "v_cvt_pk_bf16_f32 v143, v39, v43\n"
      "v_mfma_f32_16x16x32_bf16 v[96:99], v[208:211], a[208:211], v[96:99]\n"
      "v_cvt_pk_bf16_f32 v136, v28, v32\n"
      "v_cvt_pk_bf16_f32 v137, v29, v33\n"
      "v_mfma_f32_16x16x32_bf16 v[76:79], v[204:207], a[212:215], v[76:79]\n"
      "s_waitcnt vmcnt(6)\n"
      "v_cvt_pk_bf16_f32 v138, v30, v34\n"
      "v_cvt_pk_bf16_f32 v139, v31, v35\n"
      "s_barrier\n"
      "s_add_u32 s53, s54, 1769472\n"
      "s_add_u32 m0, s37, 116224\n"
      "v_mfma_f32_4x4x4_16b_bf16 v[28:31], a[244:245], a[64:65], 0\n"
      "v_mfma_f32_4x4x4_16b_bf16 v[32:35], a[244:245], a[66:67], 0\n"
      "buffer_load_dwordx4 v8, s[4:7], s53 offen offset:0 nt lds\n"
      "buffer_load_dwordx4 v9, s[4:7], s53 offen offset:1024 nt lds\n"
      "v_mfma_f32_4x4x4_16b_bf16 v[28:31], a[248:249], a[232:233], v[28:31]\n"
      "ds_read_b64_tr_b16 a[72:73], v60 offset:0\n"
      "v_mfma_f32_4x4x4_16b_bf16 v[32:35], a[248:249], a[234:235], v[32:35]\n"
      "ds_read_b64_tr_b16 a[74:75], v60 offset:768\n"
      "v_mfma_f32_4x4x4_16b_bf16 v[36:39], a[246:247], a[228:229], v[28:31]\n"
      "ds_read_b64_tr_b16 a[224:225], v60 offset:12288\n"
      "v_mfma_f32_4x4x4_16b_bf16 v[40:43], a[246:247], a[230:231], v[32:35]\n"
      "ds_read_b64_tr_b16 a[226:227], v60 offset:13056\n"
      "v_mfma_f32_4x4x4_16b_bf16 v[28:31], a[250:251], a[228:229], v[28:31]\n"
      "ds_read_b64_tr_b16 a[220:221], v60 offset:6144\n"
      "v_mfma_f32_4x4x4_16b_bf16 v[32:35], a[250:251], a[230:231], v[32:35]\n"
      "ds_read_b64_tr_b16 a[222:223], v60 offset:6912\n"
      "ds_read_b128 a[208:211], v1 offset:64\n"
      "v_mfma_f32_16x16x32_bf16 v[80:83], v[196:199], a[212:215], v[80:83]\n"
      "v_cvt_pk_bf16_f32 v132, v52, v56\n"
      "v_cvt_pk_bf16_f32 v133, v53, v57\n"
      "v_mfma_f32_16x16x32_bf16 v[84:87], v[188:191], a[212:215], v[84:87]\n"
      "v_cvt_pk_bf16_f32 v134, v54, v58\n"
      "v_cvt_pk_bf16_f32 v135, v55, v59\n"
      "v_mfma_f32_16x16x32_bf16 v[88:91], v[200:203], a[192:195], v[88:91]\n"
      "s_add_u32 m0, s37, 120328\n"
      "v_cvt_pk_bf16_f32 v128, v44, v48\n"
      "v_cvt_pk_bf16_f32 v129, v45, v49\n"
      "v_mfma_f32_16x16x32_bf16 v[92:95], v[192:195], a[192:195], v[92:95]\n"
      "s_add_u32 s53, s54, 1966080\n"
      "v_cvt_pk_bf16_f32 v130, v46, v50\n"
      "v_cvt_pk_bf16_f32 v131, v47, v51\n"
      "s_nop 0\n"
      "s_waitcnt lgkmcnt(1)\n"
      "v_mfma_f32_4x4x4_16b_bf16 v[44:47], a[244:245], a[68:69], 0\n"
      "v_mfma_f32_4x4x4_16b_bf16 v[48:51], a[244:245], a[70:71], 0\n"
      "buffer_load_dwordx4 v8, s[4:7], s53 offen offset:0 nt lds\n"
      "buffer_load_dwordx4 v9, s[4:7], s53 offen offset:1024 nt lds\n"
      "v_mfma_f32_4x4x4_16b_bf16 v[44:47], a[248:249], a[240:241], v[44:47]\n"
      "ds_read_b64_tr_b16 a[76:77], v60 offset:64\n"
      "v_mfma_f32_4x4x4_16b_bf16 v[48:51], a[248:249], a[242:243], v[48:51]\n"
      "ds_read_b64_tr_b16 a[78:79], v60 offset:832\n"
      "v_mfma_f32_4x4x4_16b_bf16 v[52:55], a[246:247], a[236:237], v[44:47]\n"
      "ds_read_b64_tr_b16 a[232:233], v60 offset:12352\n"
      "v_mfma_f32_4x4x4_16b_bf16 v[56:59], a[246:247], a[238:239], v[48:51]\n"
      "ds_read_b64_tr_b16 a[234:235], v60 offset:13120\n"
      "v_mfma_f32_4x4x4_16b_bf16 v[44:47], a[250:251], a[236:237], v[44:47]\n"
      "ds_read_b64_tr_b16 a[228:229], v60 offset:6208\n"
      "v_mfma_f32_4x4x4_16b_bf16 v[48:51], a[250:251], a[238:239], v[48:51]\n"
      "ds_read_b64_tr_b16 a[230:231], v60 offset:6976\n"
      "ds_read_b128 a[212:215], v1 offset:4160\n"
      "v_mfma_f32_16x16x32_bf16 v[96:99], v[184:187], a[192:195], v[96:99]\n"
      "s_nop 0\n"
      "v_cvt_pk_bf16_f32 v124, v36, v40\n"
      "v_cvt_pk_bf16_f32 v125, v37, v41\n"
      "v_mfma_f32_16x16x32_bf16 v[76:79], v[180:183], a[196:199], v[76:79]\n"
      "s_nop 0\n"
      "v_cvt_pk_bf16_f32 v126, v38, v42\n"
      "v_cvt_pk_bf16_f32 v127, v39, v43\n"
      "v_mfma_f32_16x16x32_bf16 v[80:83], v[172:175], a[196:199], v[80:83]\n"
      "s_add_u32 m0, s37, 124552\n"
      "v_cvt_pk_bf16_f32 v120, v28, v32\n"
      "v_cvt_pk_bf16_f32 v121, v29, v33\n"
      "v_mfma_f32_16x16x32_bf16 v[84:87], v[164:167], a[196:199], v[84:87]\n"
      "s_add_u32 s53, s54, 2162688\n"
      "v_cvt_pk_bf16_f32 v122, v30, v34\n"
      "v_cvt_pk_bf16_f32 v123, v31, v35\n"
      "v_mfma_f32_4x4x4_16b_bf16 v[28:31], a[244:245], a[72:73], 0\n"
      "v_mfma_f32_4x4x4_16b_bf16 v[32:35], a[244:245], a[74:75], 0\n"
      "buffer_load_dwordx4 v8, s[4:7], s53 offen offset:0 nt lds\n"
      "buffer_load_dwordx4 v9, s[4:7], s53 offen offset:1024 nt lds\n"
      "v_mfma_f32_4x4x4_16b_bf16 v[28:31], a[248:249], a[224:225], v[28:31]\n"
      "ds_read_b64_tr_b16 a[80:81], v60 offset:128\n"
      "v_mfma_f32_4x4x4_16b_bf16 v[32:35], a[248:249], a[226:227], v[32:35]\n"
      "ds_read_b64_tr_b16 a[82:83], v60 offset:896\n"
      "v_mfma_f32_4x4x4_16b_bf16 v[36:39], a[246:247], a[220:221], v[28:31]\n"
      "ds_read_b64_tr_b16 a[240:241], v60 offset:12416\n"
      "v_mfma_f32_4x4x4_16b_bf16 v[40:43], a[246:247], a[222:223], v[32:35]\n"
      "ds_read_b64_tr_b16 a[242:243], v60 offset:13184\n"
      "v_mfma_f32_4x4x4_16b_bf16 v[28:31], a[250:251], a[220:221], v[28:31]\n"
      "ds_read_b64_tr_b16 a[236:237], v60 offset:6272\n"
      "v_mfma_f32_4x4x4_16b_bf16 v[32:35], a[250:251], a[222:223], v[32:35]\n"
      "ds_read_b64_tr_b16 a[238:239], v60 offset:7040\n"
      "ds_read_b128 a[192:195], v4 offset:55296\n"
      "ds_read_b128 a[196:199], v1 offset:128\n"
      "v_mfma_f32_16x16x32_bf16 v[88:91], v[176:179], a[200:203], v[88:91]\n"
      "s_nop 0\n"
      "v_cvt_pk_bf16_f32 v116, v52, v56\n"
      "v_cvt_pk_bf16_f32 v117, v53, v57\n"
      "v_mfma_f32_16x16x32_bf16 v[92:95], v[168:171], a[200:203], v[92:95]\n"
      "s_nop 0\n"
      "v_cvt_pk_bf16_f32 v118, v54, v58\n"
      "v_cvt_pk_bf16_f32 v119, v55, v59\n"
      "v_mfma_f32_16x16x32_bf16 v[96:99], v[160:163], a[200:203], v[96:99]\n"
      "s_nop 0\n"
      "v_cvt_pk_bf16_f32 v112, v44, v48\n"
      "v_cvt_pk_bf16_f32 v113, v45, v49\n"
      "v_mfma_f32_16x16x32_bf16 v[64:67], a[48:51], a[204:207], v[64:67]\n"
      "s_waitcnt vmcnt(9)\n"
      "v_cvt_pk_bf16_f32 v114, v46, v50\n"
      "v_cvt_pk_bf16_f32 v115, v47, v51\n"
      "s_barrier\n"
      "s_add_u32 s53, s54, 2359296\n"
      "s_add_u32 m0, s37, 128896\n"
      "s_waitcnt lgkmcnt(0)\n"
      "v_mfma_f32_4x4x4_16b_bf16 v[44:47], a[244:245], a[76:77], 0\n"
      "v_mfma_f32_4x4x4_16b_bf16 v[48:51], a[244:245], a[78:79], 0\n"
      "buffer_load_dwordx4 v8, s[4:7], s53 offen offset:0 nt lds\n"
      "buffer_load_dwordx4 v9, s[4:7], s53 offen offset:1024 nt lds\n"
      "v_mfma_f32_4x4x4_16b_bf16 v[44:47], a[248:249], a[232:233], v[44:47]\n"
      "ds_read_b64_tr_b16 a[84:85], v60 offset:18432\n"
      "v_mfma_f32_4x4x4_16b_bf16 v[48:51], a[248:249], a[234:235], v[48:51]\n"
      "ds_read_b64_tr_b16 a[86:87], v60 offset:19200\n"
      "v_mfma_f32_4x4x4_16b_bf16 v[52:55], a[246:247], a[228:229], v[44:47]\n"
      "ds_read_b64_tr_b16 a[224:225], v61 offset:6400\n"
      "v_mfma_f32_4x4x4_16b_bf16 v[56:59], a[246:247], a[230:231], v[48:51]\n"
      "ds_read_b64_tr_b16 a[226:227], v61 offset:7168\n"
      "v_mfma_f32_4x4x4_16b_bf16 v[44:47], a[250:251], a[228:229], v[44:47]\n"
      "ds_read_b64_tr_b16 a[220:221], v61 offset:256\n"
      "v_mfma_f32_4x4x4_16b_bf16 v[48:51], a[250:251], a[230:231], v[48:51]\n"
      "ds_read_b64_tr_b16 a[222:223], v61 offset:1024\n"
      "ds_read_b128 a[200:203], v1 offset:4224\n"
      "v_mfma_f32_16x16x32_bf16 v[68:71], a[52:55], a[204:207], v[68:71]\n"
      "v_cvt_pk_bf16_f32 v108, v36, v40\n"
      "v_cvt_pk_bf16_f32 v109, v37, v41\n"
      "v_mfma_f32_16x16x32_bf16 v[72:75], a[56:59], a[204:207], v[72:75]\n"
      "v_cvt_pk_bf16_f32 v110, v38, v42\n"
      "v_cvt_pk_bf16_f32 v111, v39, v43\n"
      "v_mfma_f32_16x16x32_bf16 v[76:79], v[156:159], a[208:211], v[76:79]\n"
      "s_add_u32 m0, s37, 133120\n"
      "v_cvt_pk_bf16_f32 v104, v28, v32\n"
      "v_cvt_pk_bf16_f32 v105, v29, v33\n"
      "v_mfma_f32_16x16x32_bf16 v[80:83], v[148:151], a[208:211], v[80:83]\n"
      "s_add_u32 s53, s54, 2555904\n"
      "v_cvt_pk_bf16_f32 v106, v30, v34\n"
      "v_cvt_pk_bf16_f32 v107, v31, v35\n"
      "v_mfma_f32_4x4x4_16b_bf16 v[28:31], a[244:245], a[80:81], 0\n"
      "v_mfma_f32_4x4x4_16b_bf16 v[32:35], a[244:245], a[82:83], 0\n"
      "buffer_load_dwordx4 v8, s[4:7], s53 offen offset:0 nt lds\n"
      "buffer_load_dwordx4 v9, s[4:7], s53 offen offset:1024 nt lds\n"
      "v_mfma_f32_4x4x4_16b_bf16 v[28:31], a[248:249], a[240:241], v[28:31]\n"
      "ds_read_b64_tr_b16 a[88:89], v60 offset:18496\n"
      "v_mfma_f32_4x4x4_16b_bf16 v[32:35], a[248:249], a[242:243], v[32:35]\n"
      "ds_read_b64_tr_b16 a[90:91], v60 offset:19264\n"
      "v_mfma_f32_4x4x4_16b_bf16 v[36:39], a[246:247], a[236:237], v[28:31]\n"
      "ds_read_b64_tr_b16 a[232:233], v61 offset:6464\n"
      "v_mfma_f32_4x4x4_16b_bf16 v[40:43], a[246:247], a[238:239], v[32:35]\n"
      "ds_read_b64_tr_b16 a[234:235], v61 offset:7232\n"
      "v_mfma_f32_4x4x4_16b_bf16 v[28:31], a[250:251], a[236:237], v[28:31]\n"
      "ds_read_b64_tr_b16 a[228:229], v61 offset:320\n"
      "v_mfma_f32_4x4x4_16b_bf16 v[32:35], a[250:251], a[238:239], v[32:35]\n"
      "ds_read_b64_tr_b16 a[230:231], v61 offset:1088\n"
      "ds_read_b128 a[204:207], v5 offset:55168\n"
      "v_mfma_f32_16x16x32_bf16 v[84:87], v[140:143], a[208:211], v[84:87]\n"
      "v_cvt_pk_bf16_f32 v100, v52, v56\n"
      "v_cvt_pk_bf16_f32 v101, v53, v57\n"
      "v_mfma_f32_16x16x32_bf16 v[88:91], v[152:155], a[212:215], v[88:91]\n"
      "v_cvt_pk_bf16_f32 v102, v54, v58\n"
      "v_cvt_pk_bf16_f32 v103, v55, v59\n"
      "v_mfma_f32_16x16x32_bf16 v[92:95], v[144:147], a[212:215], v[92:95]\n"
      "v_cvt_pk_bf16_f32 v44, v44, v48\n"
      "v_cvt_pk_bf16_f32 v45, v45, v49\n"
      "v_mfma_f32_16x16x32_bf16 v[96:99], v[136:139], a[212:215], v[96:99]\n"
      "v_cvt_pk_bf16_f32 v46, v46, v50\n"
      "v_cvt_pk_bf16_f32 v47, v47, v51\n"
      "v_mfma_f32_16x16x32_bf16 v[64:67], a[60:63], a[192:195], v[64:67]\n"
      "s_add_u32 m0, s37, 137224\n"
      "v_accvgpr_write_b32 a164, v44\n"
      "v_accvgpr_write_b32 a165, v45\n"
      "v_mfma_f32_16x16x32_bf16 v[68:71], a[64:67], a[192:195], v[68:71]\n"
      "s_add_u32 s53, s54, 2752512\n"
      "v_accvgpr_write_b32 a166, v46\n"
      "v_accvgpr_write_b32 a167, v47\n"
      "s_nop 0\n"
      "s_waitcnt lgkmcnt(0)\n"
      "v_mfma_f32_4x4x4_16b_bf16 v[44:47], a[244:245], a[84:85], 0\n"
      "v_mfma_f32_4x4x4_16b_bf16 v[48:51], a[244:245], a[86:87], 0\n"
      "buffer_load_dwordx4 v8, s[4:7], s53 offen offset:0 nt lds\n"
      "buffer_load_dwordx4 v9, s[4:7], s53 offen offset:1024 nt lds\n"
      "v_mfma_f32_4x4x4_16b_bf16 v[44:47], a[248:249], a[224:225], v[44:47]\n"
      "ds_read_b64_tr_b16 a[92:93], v60 offset:18560\n"
      "v_mfma_f32_4x4x4_16b_bf16 v[48:51], a[248:249], a[226:227], v[48:51]\n"
      "ds_read_b64_tr_b16 a[94:95], v60 offset:19328\n"
      "v_mfma_f32_4x4x4_16b_bf16 v[52:55], a[246:247], a[220:221], v[44:47]\n"
      "ds_read_b64_tr_b16 a[240:241], v61 offset:6528\n"
      "v_mfma_f32_4x4x4_16b_bf16 v[56:59], a[246:247], a[222:223], v[48:51]\n"
      "ds_read_b64_tr_b16 a[242:243], v61 offset:7296\n"
      "v_mfma_f32_4x4x4_16b_bf16 v[44:47], a[250:251], a[220:221], v[44:47]\n"
      "ds_read_b64_tr_b16 a[236:237], v61 offset:384\n"
      "v_mfma_f32_4x4x4_16b_bf16 v[48:51], a[250:251], a[222:223], v[48:51]\n"
      "ds_read_b64_tr_b16 a[238:239], v61 offset:1152\n"
      "ds_read_b128 a[208:211], v2 offset:0\n"
      "ds_read_b128 a[212:215], v2 offset:4096\n"
      "v_cvt_pk_bf16_f32 v36, v36, v40\n"
      "v_cvt_pk_bf16_f32 v37, v37, v41\n"
      "v_cvt_pk_bf16_f32 v38, v38, v42\n"
      "v_cvt_pk_bf16_f32 v39, v39, v43\n"
      "v_mfma_f32_16x16x32_bf16 v[72:75], a[68:71], a[192:195], v[72:75]\n"
      "v_accvgpr_write_b32 a160, v36\n"
      "v_accvgpr_write_b32 a161, v37\n"
      "v_mfma_f32_16x16x32_bf16 v[76:79], v[132:135], a[196:199], v[76:79]\n"
      "s_nop 0\n"
      "v_accvgpr_write_b32 a162, v38\n"
      "v_accvgpr_write_b32 a163, v39\n"
      "v_mfma_f32_16x16x32_bf16 v[80:83], v[124:127], a[196:199], v[80:83]\n"
      "s_nop 0\n"
      "v_cvt_pk_bf16_f32 v28, v28, v32\n"
      "v_cvt_pk_bf16_f32 v29, v29, v33\n"
      "v_mfma_f32_16x16x32_bf16 v[84:87], v[116:119], a[196:199], v[84:87]\n"
      "s_nop 0\n"
      "v_cvt_pk_bf16_f32 v30, v30, v34\n"
      "v_cvt_pk_bf16_f32 v31, v31, v35\n"
      "v_mfma_f32_16x16x32_bf16 v[88:91], v[128:131], a[200:203], v[88:91]\n"
      "s_nop 0\n"
      "v_accvgpr_write_b32 a156, v28\n"
      "v_accvgpr_write_b32 a157, v29\n"
      "v_mfma_f32_16x16x32_bf16 v[92:95], v[120:123], a[200:203], v[92:95]\n"
      "s_waitcnt vmcnt(12)\n"
      "v_accvgpr_write_b32 a158, v30\n"
      "v_accvgpr_write_b32 a159, v31\n"
      "s_barrier\n"
      "s_add_u32 s53, s54, 2949120\n"
      "s_add_u32 m0, s37, 141448\n"
      "v_mfma_f32_4x4x4_16b_bf16 v[28:31], a[244:245], a[88:89], 0\n"
      "v_mfma_f32_4x4x4_16b_bf16 v[32:35], a[244:245], a[90:91], 0\n"
      "buffer_load_dwordx4 v8, s[4:7], s53 offen offset:0 nt lds\n"
      "buffer_load_dwordx4 v9, s[4:7], s53 offen offset:1024 nt lds\n"
      "v_mfma_f32_4x4x4_16b_bf16 v[28:31], a[248:249], a[232:233], v[28:31]\n"
      "ds_read_b64_tr_b16 a[96:97], v61 offset:12544\n"
      "v_mfma_f32_4x4x4_16b_bf16 v[32:35], a[248:249], a[234:235], v[32:35]\n"
      "ds_read_b64_tr_b16 a[98:99], v61 offset:13312\n"
      "v_mfma_f32_4x4x4_16b_bf16 v[36:39], a[246:247], a[228:229], v[28:31]\n"
      "ds_read_b64_tr_b16 a[224:225], v62 offset:39680\n"
      "v_mfma_f32_4x4x4_16b_bf16 v[40:43], a[246:247], a[230:231], v[32:35]\n"
      "ds_read_b64_tr_b16 a[226:227], v62 offset:40448\n"
      "v_mfma_f32_4x4x4_16b_bf16 v[28:31], a[250:251], a[228:229], v[28:31]\n"
      "ds_read_b64_tr_b16 a[220:221], v61 offset:18688\n"
      "v_mfma_f32_4x4x4_16b_bf16 v[32:35], a[250:251], a[230:231], v[32:35]\n"
      "ds_read_b64_tr_b16 a[222:223], v61 offset:19456\n"
      "ds_read_b128 a[192:195], v5 offset:55232\n"
      "v_cvt_pk_bf16_f32 v52, v52, v56\n"
      "v_cvt_pk_bf16_f32 v53, v53, v57\n"
      "v_cvt_pk_bf16_f32 v54, v54, v58\n"
      "v_cvt_pk_bf16_f32 v55, v55, v59\n"
      "v_mfma_f32_16x16x32_bf16 v[96:99], v[112:115], a[200:203], v[96:99]\n"
      "s_nop 0\n"
      "v_accvgpr_write_b32 a152, v52\n"
      "v_accvgpr_write_b32 a153, v53\n"
      "v_mfma_f32_16x16x32_bf16 v[64:67], a[72:75], a[204:207], v[64:67]\n"
      "s_nop 0\n"
      "v_accvgpr_write_b32 a154, v54\n"
      "v_accvgpr_write_b32 a155, v55\n"
      "v_mfma_f32_16x16x32_bf16 v[68:71], a[76:79], a[204:207], v[68:71]\n"
      "s_nop 0\n"
      "v_cvt_pk_bf16_f32 v44, v44, v48\n"
      "v_cvt_pk_bf16_f32 v45, v45, v49\n"
      "v_mfma_f32_16x16x32_bf16 v[72:75], a[80:83], a[204:207], v[72:75]\n"
      "s_waitcnt lgkmcnt(0)\n"
      "v_cvt_pk_bf16_f32 v46, v46, v50\n"
      "v_cvt_pk_bf16_f32 v47, v47, v51\n"
      "v_mfma_f32_16x16x32_bf16 v[76:79], v[108:111], a[208:211], v[76:79]\n"
      "s_add_u32 m0, s37, 145792\n"
      "v_accvgpr_write_b32 a148, v44\n"
      "v_accvgpr_write_b32 a149, v45\n"
      "v_mfma_f32_16x16x32_bf16 v[80:83], v[100:103], a[208:211], v[80:83]\n"
      "s_add_u32 s53, s54, 3145728\n"
      "v_accvgpr_write_b32 a150, v46\n"
      "v_accvgpr_write_b32 a151, v47\n"
      "v_mfma_f32_4x4x4_16b_bf16 v[44:47], a[244:245], a[92:93], 0\n"
      "v_mfma_f32_4x4x4_16b_bf16 v[48:51], a[244:245], a[94:95], 0\n"
      "buffer_load_dwordx4 v8, s[4:7], s53 offen offset:0 nt lds\n"
      "buffer_load_dwordx4 v9, s[4:7], s53 offen offset:1024 nt lds\n"
      "v_mfma_f32_4x4x4_16b_bf16 v[44:47], a[248:249], a[240:241], v[44:47]\n"
      "ds_read_b64_tr_b16 a[100:101], v61 offset:12608\n"
      "v_mfma_f32_4x4x4_16b_bf16 v[48:51], a[248:249], a[242:243], v[48:51]\n"
      "ds_read_b64_tr_b16 a[102:103], v61 offset:13376\n"
      "v_mfma_f32_4x4x4_16b_bf16 v[52:55], a[246:247], a[236:237], v[44:47]\n"
      "ds_read_b64_tr_b16 a[232:233], v62 offset:39744\n"
      "v_mfma_f32_4x4x4_16b_bf16 v[56:59], a[246:247], a[238:239], v[48:51]\n"
      "ds_read_b64_tr_b16 a[234:235], v62 offset:40512\n"
      "v_mfma_f32_4x4x4_16b_bf16 v[44:47], a[250:251], a[236:237], v[44:47]\n"
      "ds_read_b64_tr_b16 a[228:229], v61 offset:18752\n"
      "v_mfma_f32_4x4x4_16b_bf16 v[48:51], a[250:251], a[238:239], v[48:51]\n"
      "ds_read_b64_tr_b16 a[230:231], v61 offset:19520\n"
      "ds_read_b128 a[196:199], v2 offset:64\n"
      "ds_read_b128 a[200:203], v2 offset:4160\n"
      "v_cvt_pk_bf16_f32 v36, v36, v40\n"
      "v_cvt_pk_bf16_f32 v37, v37, v41\n"
      "v_cvt_pk_bf16_f32 v38, v38, v42\n"
      "v_cvt_pk_bf16_f32 v39, v39, v43\n"
      "v_accvgpr_write_b32 a144, v36\n"
      "v_accvgpr_write_b32 a145, v37\n"
      "v_accvgpr_write_b32 a146, v38\n"
      "v_accvgpr_write_b32 a147, v39\n"
      "v_mfma_f32_16x16x32_bf16 v[84:87], a[160:163], a[208:211], v[84:87]\n"
      "s_nop 0\n"
      "v_cvt_pk_bf16_f32 v28, v28, v32\n"
      "v_cvt_pk_bf16_f32 v29, v29, v33\n"
      "v_mfma_f32_16x16x32_bf16 v[88:91], v[104:107], a[212:215], v[88:91]\n"
      "s_nop 0\n"
      "v_cvt_pk_bf16_f32 v30, v30, v34\n"
      "v_cvt_pk_bf16_f32 v31, v31, v35\n"
      "v_mfma_f32_16x16x32_bf16 v[92:95], a[164:167], a[212:215], v[92:95]\n"
      "s_nop 0\n"
      "v_accvgpr_write_b32 a140, v28\n"
      "v_accvgpr_write_b32 a141, v29\n"
      "v_mfma_f32_16x16x32_bf16 v[96:99], a[156:159], a[212:215], v[96:99]\n"
      "s_nop 0\n"
      "v_accvgpr_write_b32 a142, v30\n"
      "v_accvgpr_write_b32 a143, v31\n"
      "v_mfma_f32_4x4x4_16b_bf16 v[28:31], a[244:245], a[96:97], 0\n"
      "v_mfma_f32_4x4x4_16b_bf16 v[32:35], a[244:245], a[98:99], 0\n"
      "ds_read_b64_tr_b16 a[104:105], v61 offset:12672\n"
      "v_mfma_f32_4x4x4_16b_bf16 v[28:31], a[248:249], a[224:225], v[28:31]\n"
      "ds_read_b64_tr_b16 a[106:107], v61 offset:13440\n"
      "v_mfma_f32_4x4x4_16b_bf16 v[32:35], a[248:249], a[226:227], v[32:35]\n"
      "ds_read_b64_tr_b16 a[240:241], v62 offset:39808\n"
      "v_mfma_f32_4x4x4_16b_bf16 v[36:39], a[246:247], a[220:221], v[28:31]\n"
      "ds_read_b64_tr_b16 a[242:243], v62 offset:40576\n"
      "v_mfma_f32_4x4x4_16b_bf16 v[40:43], a[246:247], a[222:223], v[32:35]\n"
      "ds_read_b64_tr_b16 a[236:237], v61 offset:18816\n"
      "v_mfma_f32_4x4x4_16b_bf16 v[28:31], a[250:251], a[220:221], v[28:31]\n"
      "ds_read_b64_tr_b16 a[238:239], v61 offset:19584\n"
      "v_mfma_f32_4x4x4_16b_bf16 v[32:35], a[250:251], a[222:223], v[32:35]\n"
      "v_cvt_pk_bf16_f32 v52, v52, v56\n"
      "v_cvt_pk_bf16_f32 v53, v53, v57\n"
      "v_cvt_pk_bf16_f32 v54, v54, v58\n"
      "v_cvt_pk_bf16_f32 v55, v55, v59\n"
      "v_accvgpr_write_b32 a136, v52\n"
      "v_accvgpr_write_b32 a137, v53\n"
      "v_accvgpr_write_b32 a138, v54\n"
      "v_accvgpr_write_b32 a139, v55\n"
      "v_cvt_pk_bf16_f32 v44, v44, v48\n"
      "v_cvt_pk_bf16_f32 v45, v45, v49\n"
      "v_mfma_f32_16x16x32_bf16 v[64:67], a[84:87], a[192:195], v[64:67]\n"
      "s_nop 0\n"
      "v_cvt_pk_bf16_f32 v46, v46, v50\n"
      "v_cvt_pk_bf16_f32 v47, v47, v51\n"
      "v_mfma_f32_16x16x32_bf16 v[68:71], a[88:91], a[192:195], v[68:71]\n"
      "s_nop 0\n"
      "v_accvgpr_write_b32 a132, v44\n"
      "v_accvgpr_write_b32 a133, v45\n"
      "v_mfma_f32_16x16x32_bf16 v[72:75], a[92:95], a[192:195], v[72:75]\n"
      "s_waitcnt lgkmcnt(0)\n"
      "v_accvgpr_write_b32 a134, v46\n"
      "v_accvgpr_write_b32 a135, v47\n"
      "v_mfma_f32_4x4x4_16b_bf16 v[44:47], a[244:245], a[100:101], 0\n"
      "v_mfma_f32_4x4x4_16b_bf16 v[48:51], a[244:245], a[102:103], 0\n"
      "ds_read_b128 a[204:207], v5 offset:55296\n"
      "v_mfma_f32_4x4x4_16b_bf16 v[44:47], a[248:249], a[232:233], v[44:47]\n"
      "v_mfma_f32_4x4x4_16b_bf16 v[48:51], a[248:249], a[234:235], v[48:51]\n"
      "s_nop 0\n"
      "v_mfma_f32_4x4x4_16b_bf16 v[52:55], a[246:247], a[228:229], v[44:47]\n"
      "v_mfma_f32_4x4x4_16b_bf16 v[56:59], a[246:247], a[230:231], v[48:51]\n"
      "v_mfma_f32_4x4x4_16b_bf16 v[44:47], a[250:251], a[228:229], v[44:47]\n"
      "v_mfma_f32_4x4x4_16b_bf16 v[48:51], a[250:251], a[230:231], v[48:51]\n"
      "v_cvt_pk_bf16_f32 v36, v36, v40\n"
      "v_cvt_pk_bf16_f32 v37, v37, v41\n"
      "v_cvt_pk_bf16_f32 v38, v38, v42\n"
      "v_cvt_pk_bf16_f32 v39, v39, v43\n"
      "v_mfma_f32_16x16x32_bf16 v[76:79], a[152:155], a[196:199], v[76:79]\n"
      "v_accvgpr_write_b32 a128, v36\n"
      "v_accvgpr_write_b32 a129, v37\n"
      "v_mfma_f32_16x16x32_bf16 v[80:83], a[144:147], a[196:199], v[80:83]\n"
      "v_accvgpr_write_b32 a130, v38\n"
      "v_accvgpr_write_b32 a131, v39\n"
      "v_mfma_f32_16x16x32_bf16 v[84:87], a[136:139], a[196:199], v[84:87]\n"
      "v_cvt_pk_bf16_f32 v28, v28, v32\n"
      "v_cvt_pk_bf16_f32 v29, v29, v33\n"
      "v_mfma_f32_16x16x32_bf16 v[88:91], a[148:151], a[200:203], v[88:91]\n"
      "v_cvt_pk_bf16_f32 v30, v30, v34\n"
      "v_cvt_pk_bf16_f32 v31, v31, v35\n"
      "v_mfma_f32_16x16x32_bf16 v[92:95], a[140:143], a[200:203], v[92:95]\n"
      "v_accvgpr_write_b32 a124, v28\n"
      "v_accvgpr_write_b32 a125, v29\n"
      "v_mfma_f32_16x16x32_bf16 v[96:99], a[132:135], a[200:203], v[96:99]\n"
      "v_accvgpr_write_b32 a126, v30\n"
      "v_accvgpr_write_b32 a127, v31\n"
      "v_mfma_f32_4x4x4_16b_bf16 v[28:31], a[244:245], a[104:105], 0\n"
      "v_mfma_f32_4x4x4_16b_bf16 v[32:35], a[244:245], a[106:107], 0\n"
      "ds_read_b128 a[208:211], v2 offset:128\n"
      "v_mfma_f32_4x4x4_16b_bf16 v[28:31], a[248:249], a[240:241], v[28:31]\n"
      "ds_read_b128 a[212:215], v2 offset:4224\n"
      "v_mfma_f32_4x4x4_16b_bf16 v[32:35], a[248:249], a[242:243], v[32:35]\n"
      "v_mfma_f32_4x4x4_16b_bf16 v[36:39], a[246:247], a[236:237], v[28:31]\n"
      "s_nop 0\n"
      "s_nop 0\n"
      "v_mfma_f32_4x4x4_16b_bf16 v[40:43], a[246:247], a[238:239], v[32:35]\n"
      "v_mfma_f32_4x4x4_16b_bf16 v[28:31], a[250:251], a[236:237], v[28:31]\n"
      "v_mfma_f32_4x4x4_16b_bf16 v[32:35], a[250:251], a[238:239], v[32:35]\n"
      "v_cvt_pk_bf16_f32 v52, v52, v56\n"
      "v_cvt_pk_bf16_f32 v53, v53, v57\n"
      "v_cvt_pk_bf16_f32 v54, v54, v58\n"
      "v_cvt_pk_bf16_f32 v55, v55, v59\n"
      "v_accvgpr_write_b32 a120, v52\n"
      "v_accvgpr_write_b32 a121, v53\n"
      "v_accvgpr_write_b32 a122, v54\n"
      "v_accvgpr_write_b32 a123, v55\n"
      "v_cvt_pk_bf16_f32 v44, v44, v48\n"
      "v_cvt_pk_bf16_f32 v45, v45, v49\n"
      "v_cvt_pk_bf16_f32 v46, v46, v50\n"
      "v_cvt_pk_bf16_f32 v47, v47, v51\n"
      "v_accvgpr_write_b32 a116, v44\n"
      "v_accvgpr_write_b32 a117, v45\n"
      "v_accvgpr_write_b32 a118, v46\n"
      "v_accvgpr_write_b32 a119, v47\n"
      "s_waitcnt lgkmcnt(0)\n"
      "s_barrier\n"
      "s_mov_b32 m0, s64\n"
      "s_nop 0\n"
      "v_mfma_f32_16x16x32_bf16 v[64:67], a[96:99], a[204:207], v[64:67]\n"
      "s_add_u32 s53, s54, 3342336\n"
      "ds_read_b128 a[192:195], v3 offset:0\n"
      "ds_read_b64_tr_b16 a[220:221], v7 offset:18944\n"
      "ds_read_b64_tr_b16 a[222:223], v7 offset:18952\n"
      "v_cvt_pk_bf16_f32 v36, v36, v40\n"
      "v_cvt_pk_bf16_f32 v37, v37, v41\n"
      "v_cvt_pk_bf16_f32 v38, v38, v42\n"
      "v_cvt_pk_bf16_f32 v39, v39, v43\n"
      "v_mfma_f32_16x16x32_bf16 v[68:71], a[100:103], a[204:207], v[68:71]\n"
      "s_add_u32 m0, s37, 82432\n"
      "ds_read_b128 a[196:199], v3 offset:64\n"
      "v_accvgpr_write_b32 a112, v36\n"
      "v_accvgpr_write_b32 a113, v37\n"
      "v_accvgpr_write_b32 a114, v38\n"
      "v_accvgpr_write_b32 a115, v39\n"
      "v_cvt_pk_bf16_f32 v28, v28, v32\n"
      "v_cvt_pk_bf16_f32 v29, v29, v33\n"
      "v_mfma_f32_16x16x32_bf16 v[72:75], a[104:107], a[204:207], v[72:75]\n"
      "s_nop 0\n"
      "buffer_load_dwordx4 v8, s[4:7], s53 offen offset:0 nt lds\n"
      "buffer_load_dwordx4 v9, s[4:7], s53 offen offset:1024 nt lds\n"
      "v_cvt_pk_bf16_f32 v30, v30, v34\n"
      "v_mfma_f32_16x16x32_bf16 v[76:79], a[128:131], a[208:211], v[76:79]\n"
      "s_mov_b32 m0, s64\n"
      "ds_read_b128 a[200:203], v3 offset:128\n"
      "ds_read_b64_tr_b16 a[224:225], v7 offset:19456\n"
      "ds_read_b64_tr_b16 a[226:227], v7 offset:19464\n"
      "v_cvt_pk_bf16_f32 v31, v31, v35\n"
      "v_accvgpr_write_b32 a108, v28\n"
      "v_accvgpr_write_b32 a109, v29\n"
      "v_accvgpr_write_b32 a110, v30\n"
      "v_accvgpr_write_b32 a111, v31\n"
      "v_mfma_f32_16x16x32_bf16 v[80:83], a[120:123], a[208:211], v[80:83]\n"
      "s_nop 0\n"
      "buffer_load_dword a252, v8, s[4:7], s40 offen\n"
      "v_mfma_f32_16x16x32_bf16 v[84:87], a[112:115], a[208:211], v[84:87]\n"
      "s_waitcnt lgkmcnt(3)\n"
      "buffer_load_dword a252, v8, s[4:7], s40 offen\n"
      "buffer_load_dword a252, v8, s[4:7], s40 offen\n"
      "v_mfma_f32_4x4x4_16b_bf16 v[20:23], a[216:217], a[220:221], 0\n"
      "buffer_load_dword a252, v8, s[4:7], s40 offen\n"
      "v_mfma_f32_4x4x4_16b_bf16 v[24:27], a[216:217], a[222:223], 0\n"
      "buffer_load_dword a252, v8, s[4:7], s40 offen\n"
      "v_mfma_f32_16x16x32_bf16 v[88:91], a[124:127], a[212:215], v[88:91]\n"
      "s_add_u32 s53, s54, 3538944\n"
      "ds_read_b128 a[204:207], v4 offset:0\n"
      "v_mfma_f32_16x16x32_bf16 v[92:95], a[116:119], a[212:215], v[92:95]\n"
      "s_add_u32 m0, s37, 86536\n"
      "v_cvt_pk_bf16_f32 v20, v20, v24\n"
      "v_cvt_pk_bf16_f32 v21, v21, v25\n"
      "v_cvt_pk_bf16_f32 v22, v22, v26\n"
      "v_mfma_f32_16x16x32_bf16 v[96:99], a[108:111], a[212:215], v[96:99]\n"
      "s_nop 0\n"
      "buffer_load_dwordx4 v8, s[4:7], s53 offen offset:0 nt lds\n"
      "buffer_load_dwordx4 v9, s[4:7], s53 offen offset:1024 nt lds\n"
      "ds_read_b64_tr_b16 a[228:229], v7 offset:19968\n"
      "ds_read_b64_tr_b16 a[230:231], v7 offset:19976\n"
      "v_mfma_f32_16x16x32_bf16 v[52:55], a[0:3], a[192:195], 0\n"
      "s_mov_b32 m0, s64\n"
      "ds_read_b128 a[208:211], v4 offset:64\n"
      "ds_write_addtid_b32 v20 offset:18432\n"
      "ds_write_addtid_b32 v21 offset:22528\n"
      "v_mfma_f32_16x16x32_bf16 v[56:59], a[4:7], a[192:195], 0\n"
      "s_waitcnt lgkmcnt(5)\n"
      "ds_write_addtid_b32 v22 offset:26624\n"
      "v_add_f32 v88, v76, v88\n"
      "v_add_f32 v89, v77, v89\n"
      "v_mfma_f32_4x4x4_16b_bf16 v[28:31], a[216:217], a[224:225], 0\n"
      "v_add_f32 v90, v78, v90\n"
      "v_mfma_f32_4x4x4_16b_bf16 v[32:35], a[216:217], a[226:227], 0\n"
      "ds_write_addtid_b32 v88 offset:45312\n"
      "ds_write_addtid_b32 v89 offset:45824\n"
      "ds_write_addtid_b32 v90 offset:46336\n"
      "v_add_f32 v91, v79, v91\n"
      "v_mfma_f32_16x16x32_bf16 v[60:63], a[8:11], a[192:195], 0\n"
      "s_nop 0\n"
      "ds_write_addtid_b32 v91 offset:46848\n"
      "v_cvt_pk_bf16_f32 v23, v23, v27\n"
      "v_cvt_pk_bf16_f32 v28, v28, v32\n"
      "v_mfma_f32_16x16x32_bf16 v[52:55], a[12:15], a[196:199], v[52:55]\n"
      "s_add_u32 s53, s54, 3735552\n"
      "ds_read_b128 a[212:215], v4 offset:128\n"
      "ds_write_addtid_b32 v28 offset:18944\n"
      "ds_write_addtid_b32 v23 offset:30720\n"
      "v_mfma_f32_16x16x32_bf16 v[56:59], a[16:19], a[196:199], v[56:59]\n"
      "s_add_u32 m0, s37, 90760\n"
      "ds_read_b64_tr_b16 a[232:233], v7 offset:20480\n"
      "ds_read_b64_tr_b16 a[234:235], v7 offset:20488\n"
      "v_add_f32_e64 v92, v80, v92\n"
      "v_mfma_f32_16x16x32_bf16 v[60:63], a[20:23], a[196:199], v[60:63]\n"
      "s_waitcnt lgkmcnt(6)\n"
      "buffer_load_dwordx4 v8, s[4:7], s53 offen offset:0 nt lds\n"
      "buffer_load_dwordx4 v9, s[4:7], s53 offen offset:1024 nt lds\n"
      "v_mfma_f32_16x16x32_bf16 v[52:55], a[24:27], a[200:203], v[52:55]\n"
      "s_mov_b32 m0, s64\n"
      "ds_read_b128 a[192:195], v5 offset:0\n"
      "ds_write_addtid_b32 v92 offset:47360\n"
      "v_add_f32 v93, v81, v93\n"
      "v_mfma_f32_4x4x4_16b_bf16 v[20:23], a[216:217], a[228:229], 0\n"
      "v_cvt_pk_bf16_f32 v29, v29, v33\n"
      "v_add_f32 v94, v82, v94\n"
      "v_mfma_f32_4x4x4_16b_bf16 v[24:27], a[216:217], a[230:231], 0\n"
      "ds_write_addtid_b32 v29 offset:23040\n"
      "ds_write_addtid_b32 v93 offset:47872\n"
      "ds_write_addtid_b32 v94 offset:48384\n"
      "v_mfma_f32_16x16x32_bf16 v[56:59], a[28:31], a[200:203], v[56:59]\n"
      "v_add_f32 v95, v83, v95\n"
      "v_cvt_pk_bf16_f32 v30, v30, v34\n"
      "v_add_f32 v96, v84, v96\n"
      "v_mfma_f32_16x16x32_bf16 v[60:63], a[32:35], a[200:203], v[60:63]\n"
      "ds_write_addtid_b32 v30 offset:27136\n"
      "ds_write_addtid_b32 v95 offset:48896\n"
      "ds_write_addtid_b32 v96 offset:49408\n"
      "v_mfma_f32_16x16x32_bf16 v[52:55], a[36:39], a[204:207], v[52:55]\n"
      "ds_read_b128 a[196:199], v5 offset:64\n"
      "v_cvt_pk_bf16_f32 v31, v31, v35\n"
      "v_cvt_pk_bf16_f32 v20, v20, v24\n"
      "v_mfma_f32_16x16x32_bf16 v[56:59], a[40:43], a[204:207], v[56:59]\n"
      "ds_write_addtid_b32 v20 offset:19456\n"
      "ds_write_addtid_b32 v31 offset:31232\n"
      "v_add_f32_e64 v97, v85, v97\n"
      "v_mfma_f32_16x16x32_bf16 v[60:63], a[44:47], a[204:207], v[60:63]\n"
      "s_waitcnt lgkmcnt(6)\n"
      "ds_write_addtid_b32 v97 offset:49920\n"
      "v_add_f32 v98, v86, v98\n"
      "v_add_f32_e64 v99, v87, v99\n"
      "v_mfma_f32_4x4x4_16b_bf16 v[28:31], a[216:217], a[232:233], 0\n"
      "v_cvt_pk_bf16_f32 v21, v21, v25\n"
      "v_cvt_pk_bf16_f32 v22, v22, v26\n"
      "v_mfma_f32_4x4x4_16b_bf16 v[32:35], a[216:217], a[234:235], 0\n"
      "ds_write_addtid_b32 v21 offset:23552\n"
      "ds_write_addtid_b32 v98 offset:50432\n"
      "ds_write_addtid_b32 v22 offset:27648\n"
      "v_mfma_f32_16x16x32_bf16 v[52:55], a[48:51], a[208:211], v[52:55]\n"
      "ds_read_b128 a[200:203], v5 offset:128\n"
      "ds_write_addtid_b32 v99 offset:50944\n"
      "v_cvt_pk_bf16_f32 v28, v28, v32\n"
      "v_mfma_f32_16x16x32_bf16 v[56:59], a[52:55], a[208:211], v[56:59]\n"
      "ds_write_addtid_b32 v28 offset:19968\n"
      "v_cvt_pk_bf16_f32 v29, v29, v33\n"
      "v_cvt_pk_bf16_f32 v30, v30, v34\n"
      "v_cvt_pk_bf16_f32 v31, v31, v35\n"
      "v_cvt_pk_bf16_f32 v23, v23, v27\n"
      "v_mfma_f32_16x16x32_bf16 v[60:63], a[56:59], a[208:211], v[60:63]\n"
      "ds_write_addtid_b32 v29 offset:24064\n"
      "ds_write_addtid_b32 v23 offset:31744\n"
      "v_mfma_f32_16x16x32_bf16 v[52:55], a[60:63], a[212:215], v[52:55]\n"
      "s_nop 0\n"
      "ds_write_addtid_b32 v30 offset:28160\n"
      "ds_write_addtid_b32 v31 offset:32256\n"
      "v_fma_f32 v40, 2.0, v76, -v88\n"
      "v_mfma_f32_16x16x32_bf16 v[56:59], a[64:67], a[212:215], v[56:59]\n"
      "s_waitcnt vmcnt(19)\n"
      "v_fma_f32 v41, 2.0, v77, -v89\n"
      "v_fma_f32 v42, 2.0, v78, -v90\n"
      "v_fma_f32 v43, 2.0, v79, -v91\n"
      "v_mfma_f32_16x16x32_bf16 v[60:63], a[68:71], a[212:215], v[60:63]\n"
      "s_waitcnt lgkmcnt(0)\n"
      "v_fma_f32 v44, 2.0, v80, -v92\n"
      "v_fma_f32 v45, 2.0, v81, -v93\n"
      "s_barrier\n"
      "s_lshl_b32 s50, s18, 8\n"
      "v_subrev_u32 v17, s50, v7\n"
      "s_add_u32 s54, s54, 3735552\n"
      "LDloop_%=:\n"
      "v_mfma_f32_16x16x32_bf16 v[52:55], a[72:75], a[192:195], v[52:55]\n"
      "s_add_u32 s58, s40, 0\n"
      "ds_read_b128 a[204:207], v0 offset:16384\n"
      "ds_read2st64_b32 a[220:221], v10 offset0:181 offset1:183\n"
      "ds_read2st64_b32 a[222:223], v13 offset0:181 offset1:183\n"
      "ds_read2st64_b32 v[28:29], v6 offset0:0 offset1:2\n"
      "v_mfma_f32_16x16x32_bf16 v[56:59], a[76:79], a[192:195], v[56:59]\n"
      "s_mov_b32 s40, s62\n"
      "ds_read_b128 a[208:211], v0 offset:16448\n"
      "v_fma_f32 v46, 2.0, v82, -v94\n"
      "v_mfma_f32_16x16x32_bf16 v[60:63], a[80:83], a[192:195], v[60:63]\n"
      "s_add_u32 s62, s62, 786432\n"
      "ds_read2st64_b32 v[30:31], v6 offset0:4 offset1:6\n"
      "ds_read2st64_b32 v[32:33], v6 offset0:8 offset1:10\n"
      "v_mfma_f32_16x16x32_bf16 v[52:55], a[84:87], a[196:199], v[52:55]\n"
      "s_add_u32 s59, s58, 196608\n"
      "ds_read_b128 a[212:215], v0 offset:16512\n"
      "v_fma_f32 v47, 2.0, v83, -v95\n"
      "v_mfma_f32_16x16x32_bf16 v[56:59], a[88:91], a[196:199], v[56:59]\n"
      "s_nop 0\n"
      "s_waitcnt lgkmcnt(5)\n"
      "ds_read2st64_b32 a[224:225], v11 offset0:181 offset1:183\n"
      "v_fma_f32 v48, 2.0, v84, -v96\n"
      "v_mfma_f32_16x16x32_bf16 v[60:63], a[92:95], a[196:199], v[60:63]\n"
      "s_add_u32 s54, s54, 196608\n"
      "v_fma_f32 v49, 2.0, v85, -v97\n"
      "v_fma_f32 v50, 2.0, v86, -v98\n"
      "v_mfma_f32_16x16x32_bf16 v[52:55], a[96:99], a[200:203], v[52:55]\n"
      "s_add_u32 m0, s37, 95104\n"
      "buffer_store_dwordx4 a[220:223], v16, s[8:11], s58 offen offset:0 sc1\n"
      "ds_read_b128 a[192:195], v1 offset:16384\n"
      "ds_read2st64_b32 a[226:227], v14 offset0:181 offset1:183\n"
      "v_mfma_f32_16x16x32_bf16 v[56:59], a[100:103], a[200:203], v[56:59]\n"
      "buffer_load_dwordx4 v8, s[4:7], s54 offen offset:0 nt lds\n"
      "buffer_load_dwordx4 v9, s[4:7], s54 offen offset:1024 nt lds\n"
      "v_fma_f32 v51, 2.0, v87, -v99\n"
      "v_mfma_f32_16x16x32_bf16 v[60:63], a[104:107], a[200:203], v[60:63]\n"
      "s_mov_b32 m0, s64\n"
      "ds_read2st64_b32 v[34:35], v6 offset0:12 offset1:14\n"
      "ds_read2st64_b32 v[36:37], v6 offset0:16 offset1:18\n"
      "v_mfma_f32_16x16x32_bf16 v[76:79], v[252:255], a[204:207], 0\n"
      "s_nop 0\n"
      "ds_read_b128 a[196:199], v1 offset:16448\n"
      "ds_read2st64_b32 v[38:39], v6 offset0:20 offset1:22\n"
      "v_mfma_f32_16x16x32_bf16 v[80:83], v[244:247], a[204:207], 0\n"
      "ds_read_b64_tr_b16 a[244:245], v7 offset:36864\n"
      "ds_read_b64_tr_b16 a[246:247], v7 offset:36872\n"
      "v_mfma_f32_16x16x32_bf16 v[84:87], v[236:239], a[204:207], 0\n"
      "s_waitcnt lgkmcnt(8)\n"
      "v_fmac_f32 v40, s46, v28\n"
      "v_fmac_f32 v41, s46, v29\n"
      "v_mfma_f32_16x16x32_bf16 v[76:79], v[228:231], a[208:211], v[76:79]\n"
      "s_waitcnt lgkmcnt(6)\n"
      "ds_read_b128 a[200:203], v1 offset:16512\n"
      "v_fmac_f32 v42, s46, v30\n"
      "v_mfma_f32_16x16x32_bf16 v[80:83], v[220:223], a[208:211], v[80:83]\n"
      "buffer_store_dwordx4 a[224:227], v16, s[8:11], s58 offen offset:128 sc1\n"
      "v_mfma_f32_16x16x32_bf16 v[84:87], v[212:215], a[208:211], v[84:87]\n"
      "ds_read2st64_b32 a[228:229], v12 offset0:181 offset1:183\n"
      "ds_read2st64_b32 a[230:231], v15 offset0:181 offset1:183\n"
      "v_mfma_f32_16x16x32_bf16 v[76:79], v[204:207], a[212:215], v[76:79]\n"
      "ds_read_b128 a[204:207], v2 offset:16384\n"
      "v_fmac_f32 v43, s46, v31\n"
      "v_fma_f32 v28, 0.5, v28, v64\n"
      "v_mfma_f32_16x16x32_bf16 v[80:83], v[196:199], a[212:215], v[80:83]\n"
      "v_fma_f32 v29, 0.5, v29, v65\n"
      "v_fma_f32 v30, 0.5, v30, v66\n"
      "v_mfma_f32_16x16x32_bf16 v[84:87], v[188:191], a[212:215], v[84:87]\n"
      "s_waitcnt lgkmcnt(3)\n"
      "v_fma_f32 v31, 0.5, v31, v67\n"
      "v_fmac_f32 v40, s48, v52\n"
      "v_mfma_f32_4x4x4_16b_bf16 v[20:23], a[216:217], a[244:245], 0\n"
      "v_fmac_f32 v41, s48, v53\n"
      "v_mfma_f32_4x4x4_16b_bf16 v[24:27], a[216:217], a[246:247], 0\n"
      "v_fmac_f32 v42, s48, v54\n"
      "v_fmac_f32 v43, s48, v55\n"
      "v_mfma_f32_16x16x32_bf16 v[76:79], v[180:183], a[192:195], v[76:79]\n"
      "ds_read_b128 a[208:211], v2 offset:16448\n"
      "v_fmac_f32 v28, s45, v40\n"
      "v_mfma_f32_16x16x32_bf16 v[80:83], v[172:175], a[192:195], v[80:83]\n"
      "v_cvt_pk_bf16_f32 v20, v20, v24\n"
      "v_cvt_pk_bf16_f32 v21, v21, v25\n"
      "v_mfma_f32_16x16x32_bf16 v[84:87], v[164:167], a[192:195], v[84:87]\n"
      "s_add_u32 s54, s54, 196608\n"
      "ds_write_addtid_b32 v20 offset:3072\n"
      "ds_write_addtid_b32 v21 offset:7168\n"
      "v_fmac_f32 v29, s45, v41\n"
      "v_mfma_f32_16x16x32_bf16 v[76:79], v[156:159], a[196:199], v[76:79]\n"
      "s_add_u32 m0, s37, 99328\n"
      "ds_read_b128 a[212:215], v2 offset:16512\n"
      "v_fmac_f32 v30, s45, v42\n"
      "v_mfma_f32_16x16x32_bf16 v[80:83], v[148:151], a[196:199], v[80:83]\n"
      "s_waitcnt lgkmcnt(4)\n"
      "buffer_load_dwordx4 v8, s[4:7], s54 offen offset:0 nt lds\n"
      "buffer_load_dwordx4 v9, s[4:7], s54 offen offset:1024 nt lds\n"
      "v_mfma_f32_16x16x32_bf16 v[84:87], v[140:143], a[196:199], v[84:87]\n"
      "s_mov_b32 m0, s64\n"
      "v_fmac_f32 v31, s45, v43\n"
      "v_cvt_pk_bf16_f32 v22, v22, v26\n"
      "v_mfma_f32_16x16x32_bf16 v[76:79], v[132:135], a[200:203], v[76:79]\n"
      "s_nop 0\n"
      "buffer_store_dwordx4 a[228:231], v16, s[8:11], s58 offen offset:256 sc1\n"
      "ds_read_b128 a[192:195], v0 offset:20480\n"
      "v_mfma_f32_16x16x32_bf16 v[80:83], v[124:127], a[200:203], v[80:83]\n"
      "ds_write_addtid_b32 v22 offset:11264\n"
      "v_fma_f32 v40, s47, v40, -v52\n"
      "v_mfma_f32_16x16x32_bf16 v[84:87], v[116:119], a[200:203], v[84:87]\n"
      "v_fma_f32 v41, s47, v41, -v53\n"
      "v_cvt_pk_bf16_f32 v23, v23, v27\n"
      "v_mfma_f32_16x16x32_bf16 v[76:79], v[108:111], a[204:207], v[76:79]\n"
      "ds_read_b128 a[196:199], v0 offset:20544\n"
      "ds_write_addtid_b32 v23 offset:15360\n"
      "v_mfma_f32_16x16x32_bf16 v[80:83], v[100:103], a[204:207], v[80:83]\n"
      "ds_read2st64_b32 a[232:233], v10 offset0:4 offset1:6\n"
      "ds_read2st64_b32 a[234:235], v13 offset0:4 offset1:6\n"
      "v_fma_f32 v42, s47, v42, -v54\n"
      "v_mfma_f32_16x16x32_bf16 v[84:87], a[160:163], a[204:207], v[84:87]\n"
      "s_waitcnt lgkmcnt(6)\n"
      "v_fma_f32 v43, s47, v43, -v55\n"
      "v_cvt_pk_bf16_f32 v28, v28, v29\n"
      "v_mfma_f32_16x16x32_bf16 v[76:79], a[152:155], a[208:211], v[76:79]\n"
      "ds_read_b128 a[200:203], v0 offset:20608\n"
      "ds_write_addtid_b32 v28 offset:32768\n"
      "v_mfma_f32_16x16x32_bf16 v[80:83], a[144:147], a[208:211], v[80:83]\n"
      "s_nop 0\n"
      "v_cvt_pk_bf16_f32 v30, v30, v31\n"
      "v_cvt_pk_bf16_f32 v40, v40, v41\n"
      "v_mfma_f32_16x16x32_bf16 v[84:87], a[136:139], a[208:211], v[84:87]\n"
      "ds_write_addtid_b32 v30 offset:33280\n"
      "ds_write_addtid_b32 v40 offset:34816\n"
      "v_mfma_f32_16x16x32_bf16 v[76:79], a[128:131], a[212:215], v[76:79]\n"
      "ds_read_b128 a[204:207], v1 offset:20480\n"
      "ds_read_b64_tr_b16 a[248:249], v17 offset:37376\n"
      "v_cvt_pk_bf16_f32 v42, v42, v43\n"
      "v_mfma_f32_16x16x32_bf16 v[80:83], a[120:123], a[212:215], v[80:83]\n"
      "ds_write_addtid_b32 v42 offset:35328\n"
      "ds_read_b64_tr_b16 a[250:251], v17 offset:37384\n"
      "v_fmac_f32 v44, s46, v32\n"
      "v_mfma_f32_16x16x32_bf16 v[84:87], a[112:115], a[212:215], v[84:87]\n"
      "s_waitcnt lgkmcnt(6)\n"
      "v_fmac_f32 v45, s46, v33\n"
      "v_fmac_f32 v46, s46, v34\n"
      "v_mfma_f32_16x16x32_bf16 v[88:91], v[248:251], a[192:195], v[76:79]\n"
      "ds_read_b128 a[208:211], v1 offset:20544\n"
      "v_fmac_f32 v47, s46, v35\n"
      "v_mfma_f32_16x16x32_bf16 v[92:95], v[240:243], a[192:195], v[80:83]\n"
      "v_fma_f32 v32, 0.5, v32, v68\n"
      "v_fma_f32 v33, 0.5, v33, v69\n"
      "v_mfma_f32_16x16x32_bf16 v[96:99], v[232:235], a[192:195], v[84:87]\n"
      "buffer_store_dwordx4 a[232:235], v16, s[8:11], s59 offen offset:0 sc1\n"
      "v_mfma_f32_16x16x32_bf16 v[88:91], v[224:227], a[196:199], v[88:91]\n"
      "s_waitcnt lgkmcnt(1)\n"
      "ds_read_b128 a[212:215], v1 offset:20608\n"
      "v_fma_f32 v34, 0.5, v34, v70\n"
      "v_mfma_f32_4x4x4_16b_bf16 v[20:23], a[218:219], a[248:249], 0\n"
      "v_fma_f32 v35, 0.5, v35, v71\n"
      "v_mfma_f32_4x4x4_16b_bf16 v[24:27], a[218:219], a[250:251], 0\n"
      "v_fmac_f32 v44, s48, v56\n"
      "v_fmac_f32 v45, s48, v57\n"
      "v_mfma_f32_16x16x32_bf16 v[92:95], v[216:219], a[196:199], v[92:95]\n"
      "v_fmac_f32 v46, s48, v58\n"
      "v_fmac_f32 v47, s48, v59\n"
      "v_cvt_pk_bf16_f32 v20, v20, v24\n"
      "v_mfma_f32_16x16x32_bf16 v[96:99], v[208:211], a[196:199], v[96:99]\n"
      "v_cvt_pk_bf16_f32 v21, v21, v25\n"
      "v_fmac_f32 v32, s45, v44\n"
      "v_mfma_f32_16x16x32_bf16 v[88:91], v[200:203], a[200:203], v[88:91]\n"
      "ds_read_b128 a[192:195], v2 offset:20480\n"
      "v_fmac_f32 v33, s45, v45\n"
      "v_mfma_f32_16x16x32_bf16 v[92:95], v[192:195], a[200:203], v[92:95]\n"
      "s_mov_b32 m0, s41\n"
      "v_fmac_f32 v34, s45, v46\n"
      "v_fmac_f32_e64 v35, s45, v47\n"
      "v_mfma_f32_16x16x32_bf16 v[96:99], v[184:187], a[200:203], v[96:99]\n"
      "ds_write_addtid_b32 v20 offset:3584\n"
      "ds_write_addtid_b32 v21 offset:7680\n"
      "v_mfma_f32_16x16x32_bf16 v[88:91], v[176:179], a[204:207], v[88:91]\n"
      "s_mov_b32 m0, s64\n"
      "ds_read_b128 a[196:199], v2 offset:20544\n"
      "v_fma_f32 v44, s47, v44, -v56\n"
      "v_mfma_f32_16x16x32_bf16 v[92:95], v[168:171], a[204:207], v[92:95]\n"
      "s_waitcnt lgkmcnt(4)\n"
      "v_fma_f32 v45, s47, v45, -v57\n"
      "v_fma_f32 v46, s47, v46, -v58\n"
      "v_mfma_f32_16x16x32_bf16 v[96:99], v[160:163], a[204:207], v[96:99]\n"
      "s_add_u32 s54, s54, 196608\n"
      "v_fma_f32 v47, s47, v47, -v59\n"
      "v_cvt_pk_bf16_f32 v32, v32, v33\n"
      "v_mfma_f32_16x16x32_bf16 v[88:91], v[152:155], a[208:211], v[88:91]\n"
      "s_add_u32 m0, s37, 103432\n"
      "ds_read_b128 a[200:203], v2 offset:20608\n"
      "v_cvt_pk_bf16_f32 v34, v34, v35\n"
      "v_mfma_f32_16x16x32_bf16 v[92:95], v[144:147], a[208:211], v[92:95]\n"
      "s_nop 0\n"
      "buffer_load_dwordx4 v8, s[4:7], s54 offen offset:0 nt lds\n"
      "buffer_load_dwordx4 v9, s[4:7], s54 offen offset:1024 nt lds\n"
      "v_mfma_f32_16x16x32_bf16 v[96:99], v[136:139], a[208:211], v[96:99]\n"
      "s_mov_b32 m0, s64\n"
      "v_cvt_pk_bf16_f32 v44, v44, v45\n"
      "v_cvt_pk_bf16_f32 v46, v46, v47\n"
      "v_mfma_f32_16x16x32_bf16 v[88:91], v[128:131], a[212:215], v[88:91]\n"
      "ds_read_b128 a[204:207], v3 offset:16896\n"
      "ds_write_addtid_b32 v32 offset:40964\n"
      "v_mfma_f32_16x16x32_bf16 v[92:95], v[120:123], a[212:215], v[92:95]\n"
      "ds_write_addtid_b32 v34 offset:41476\n"
      "ds_write_addtid_b32 v44 offset:43012\n"
      "v_fmac_f32_e64 v48, s46, v36\n"
      "v_mfma_f32_16x16x32_bf16 v[96:99], v[112:115], a[212:215], v[96:99]\n"
      "s_waitcnt lgkmcnt(4)\n"
      "ds_read2st64_b32 a[236:237], v11 offset0:4 offset1:6\n"
      "ds_write_addtid_b32 v46 offset:43524\n"
      "v_fmac_f32 v49, s46, v37\n"
      "v_fmac_f32 v50, s46, v38\n"
      "v_mfma_f32_16x16x32_bf16 v[88:91], v[104:107], a[192:195], v[88:91]\n"
      "s_nop 0\n"
      "ds_read_b128 a[208:211], v3 offset:16960\n"
      "ds_read2st64_b32 a[238:239], v14 offset0:4 offset1:6\n"
      "v_mfma_f32_16x16x32_bf16 v[92:95], a[164:167], a[192:195], v[92:95]\n"
      "v_fmac_f32 v51, s46, v39\n"
      "v_fma_f32 v36, 0.5, v36, v72\n"
      "v_mfma_f32_16x16x32_bf16 v[96:99], a[156:159], a[192:195], v[96:99]\n"
      "v_fma_f32 v37, 0.5, v37, v73\n"
      "v_fma_f32 v38, 0.5, v38, v74\n"
      "v_mfma_f32_16x16x32_bf16 v[88:91], a[148:151], a[196:199], v[88:91]\n"
      "s_nop 0\n"
      "ds_read_b128 a[212:215], v3 offset:17024\n"
      "v_fma_f32 v39, 0.5, v39, v75\n"
      "v_mfma_f32_16x16x32_bf16 v[92:95], a[140:143], a[196:199], v[92:95]\n"
      "v_fmac_f32 v48, s48, v60\n"
      "v_fmac_f32 v49, s48, v61\n"
      "v_mfma_f32_16x16x32_bf16 v[96:99], a[132:135], a[196:199], v[96:99]\n"
      "s_waitcnt lgkmcnt(1)\n"
      "v_fmac_f32 v50, s48, v62\n"
      "v_fmac_f32_e64 v51, s48, v63\n"
      "v_mfma_f32_16x16x32_bf16 v[88:91], a[124:127], a[200:203], v[88:91]\n"
      "s_add_u32 s54, s54, 196608\n"
      "ds_read_b128 a[192:195], v4 offset:16896\n"
      "ds_read2st64_b32 a[240:241], v12 offset0:4 offset1:6\n"
      "v_mfma_f32_16x16x32_bf16 v[92:95], a[116:119], a[200:203], v[92:95]\n"
      "s_add_u32 m0, s37, 107656\n"
      "ds_read2st64_b32 a[242:243], v15 offset0:4 offset1:6\n"
      "v_fmac_f32 v36, s45, v48\n"
      "v_mfma_f32_16x16x32_bf16 v[96:99], a[108:111], a[200:203], v[96:99]\n"
      "s_nop 0\n"
      "buffer_store_dwordx4 a[236:239], v16, s[8:11], s59 offen offset:128 sc1\n"
      "buffer_load_dwordx4 v8, s[4:7], s54 offen offset:0 nt lds\n"
      "buffer_load_dwordx4 v9, s[4:7], s54 offen offset:1024 nt lds\n"
      "v_mfma_f32_16x16x32_bf16 v[64:67], a[0:3], a[204:207], 0\n"
      "s_mov_b32 m0, s64\n"
      "ds_read_b128 a[196:199], v4 offset:16960\n"
      "ds_write_addtid_b32 v88 offset:51456\n"
      "v_fmac_f32 v37, s45, v49\n"
      "v_mfma_f32_16x16x32_bf16 v[68:71], a[4:7], a[204:207], 0\n"
      "ds_write_addtid_b32 v89 offset:51968\n"
      "ds_write_addtid_b32 v90 offset:52480\n"
      "v_fmac_f32 v38, s45, v50\n"
      "v_mfma_f32_16x16x32_bf16 v[72:75], a[8:11], a[204:207], 0\n"
      "ds_write_addtid_b32 v91 offset:52992\n"
      "ds_write_addtid_b32 v92 offset:53504\n"
      "v_mfma_f32_16x16x32_bf16 v[64:67], a[12:15], a[208:211], v[64:67]\n"
      "ds_read_b128 a[200:203], v4 offset:17024\n"
      "ds_write_addtid_b32 v93 offset:54016\n"
      "v_fmac_f32 v39, s45, v51\n"
      "v_mfma_f32_16x16x32_bf16 v[68:71], a[16:19], a[208:211], v[68:71]\n"
      "ds_write_addtid_b32 v94 offset:54528\n"
      "ds_write_addtid_b32 v95 offset:55040\n"
      "v_mfma_f32_16x16x32_bf16 v[72:75], a[20:23], a[208:211], v[72:75]\n"
      "s_waitcnt lgkmcnt(7)\n"
      "ds_write_addtid_b32 v96 offset:55552\n"
      "ds_write_addtid_b32 v97 offset:56064\n"
      "v_mfma_f32_16x16x32_bf16 v[64:67], a[24:27], a[212:215], v[64:67]\n"
      "s_nop 0\n"
      "ds_read_b128 a[204:207], v5 offset:16896\n"
      "ds_write_addtid_b32 v98 offset:56576\n"
      "v_mfma_f32_16x16x32_bf16 v[68:71], a[28:31], a[212:215], v[68:71]\n"
      "buffer_store_dwordx4 a[240:243], v16, s[8:11], s59 offen offset:256 sc1\n"
      "v_mfma_f32_16x16x32_bf16 v[72:75], a[32:35], a[212:215], v[72:75]\n"
      "ds_write_addtid_b32 v99 offset:57088\n"
      "v_fma_f32 v48, s47, v48, -v60\n"
      "v_fma_f32 v49, s47, v49, -v61\n"
      "v_mfma_f32_16x16x32_bf16 v[64:67], a[36:39], a[192:195], v[64:67]\n"
      "s_nop 0\n"
      "ds_read_b128 a[208:211], v5 offset:16960\n"
      "v_fma_f32 v50, s47, v50, -v62\n"
      "v_mfma_f32_16x16x32_bf16 v[68:71], a[40:43], a[192:195], v[68:71]\n"
      "s_nop 0\n"
      "v_fma_f32 v51, s47, v51, -v63\n"
      "v_cvt_pk_bf16_f32 v36, v36, v37\n"
      "v_mfma_f32_16x16x32_bf16 v[72:75], a[44:47], a[192:195], v[72:75]\n"
      "s_nop 0\n"
      "ds_write_addtid_b32 v36 offset:36864\n"
      "v_cvt_pk_bf16_f32 v38, v38, v39\n"
      "v_cvt_pk_bf16_f32 v48, v48, v49\n"
      "v_cvt_pk_bf16_f32 v50, v50, v51\n"
      "v_mfma_f32_16x16x32_bf16 v[64:67], a[48:51], a[196:199], v[64:67]\n"
      "s_nop 0\n"
      "ds_read_b128 a[212:215], v5 offset:17024\n"
      "ds_write_addtid_b32 v38 offset:37376\n"
      "v_mfma_f32_16x16x32_bf16 v[68:71], a[52:55], a[196:199], v[68:71]\n"
      "s_nop 0\n"
      "ds_write_addtid_b32 v48 offset:38912\n"
      "ds_write_addtid_b32 v50 offset:39424\n"
      "v_mfma_f32_16x16x32_bf16 v[72:75], a[56:59], a[196:199], v[72:75]\n"
      "s_waitcnt lgkmcnt(5)\n"
      "v_fma_f32 v40, 2.0, v76, -v88\n"
      "v_fma_f32 v41, 2.0, v77, -v89\n"
      "v_mfma_f32_16x16x32_bf16 v[64:67], a[60:63], a[200:203], v[64:67]\n"
      "s_nop 0\n"
      "v_fma_f32 v42, 2.0, v78, -v90\n"
      "v_fma_f32 v43, 2.0, v79, -v91\n"
      "v_mfma_f32_16x16x32_bf16 v[68:71], a[64:67], a[200:203], v[68:71]\n"
      "s_waitcnt vmcnt(25)\n"
      "v_fma_f32 v44, 2.0, v80, -v92\n"
      "v_fma_f32 v45, 2.0, v81, -v93\n"
      "v_mfma_f32_16x16x32_bf16 v[72:75], a[68:71], a[200:203], v[72:75]\n"
      "s_waitcnt lgkmcnt(0)\n"
      "s_barrier\n"
      "LDmid_%=:\n"
      "v_mfma_f32_16x16x32_bf16 v[64:67], a[72:75], a[204:207], v[64:67]\n"
      "s_add_u32 s58, s40, 0\n"
      "ds_read_b128 a[192:195], v0 offset:0\n"
      "ds_read2st64_b32 a[220:221], v10 offset0:177 offset1:179\n"
      "ds_read2st64_b32 a[222:223], v13 offset0:177 offset1:179\n"
      "ds_read2st64_b32 v[28:29], v6 offset0:24 offset1:26\n"
      "v_mfma_f32_16x16x32_bf16 v[68:71], a[76:79], a[204:207], v[68:71]\n"
      "s_mov_b32 s40, s62\n"
      "ds_read_b128 a[196:199], v0 offset:64\n"
      "v_fma_f32 v46, 2.0, v82, -v94\n"
      "v_mfma_f32_16x16x32_bf16 v[72:75], a[80:83], a[204:207], v[72:75]\n"
      "s_add_u32 s62, s62, 786432\n"
      "ds_read2st64_b32 v[30:31], v6 offset0:28 offset1:30\n"
      "ds_read2st64_b32 v[32:33], v6 offset0:32 offset1:34\n"
      "v_mfma_f32_16x16x32_bf16 v[64:67], a[84:87], a[208:211], v[64:67]\n"
      "s_add_u32 s59, s58, 196608\n"
      "ds_read_b128 a[200:203], v0 offset:128\n"
      "v_fma_f32 v47, 2.0, v83, -v95\n"
      "v_mfma_f32_16x16x32_bf16 v[68:71], a[88:91], a[208:211], v[68:71]\n"
      "s_nop 0\n"
      "s_waitcnt lgkmcnt(5)\n"
      "ds_read2st64_b32 a[224:225], v11 offset0:177 offset1:179\n"
      "v_fma_f32 v48, 2.0, v84, -v96\n"
      "v_mfma_f32_16x16x32_bf16 v[72:75], a[92:95], a[208:211], v[72:75]\n"
      "s_add_u32 s54, s54, 196608\n"
      "v_fma_f32 v49, 2.0, v85, -v97\n"
      "v_fma_f32 v50, 2.0, v86, -v98\n"
      "v_mfma_f32_16x16x32_bf16 v[64:67], a[96:99], a[212:215], v[64:67]\n"
      "s_add_u32 m0, s37, 112000\n"
      "buffer_store_dwordx4 a[220:223], v16, s[8:11], s58 offen offset:0 sc1\n"
      "ds_read_b128 a[204:207], v1 offset:0\n"
      "ds_read2st64_b32 a[226:227], v14 offset0:177 offset1:179\n"
      "v_mfma_f32_16x16x32_bf16 v[68:71], a[100:103], a[212:215], v[68:71]\n"
      "buffer_load_dwordx4 v8, s[4:7], s54 offen offset:0 nt lds\n"
      "buffer_load_dwordx4 v9, s[4:7], s54 offen offset:1024 nt lds\n"
      "v_fma_f32 v51, 2.0, v87, -v99\n"
      "v_mfma_f32_16x16x32_bf16 v[72:75], a[104:107], a[212:215], v[72:75]\n"
      "s_mov_b32 m0, s64\n"
      "ds_read2st64_b32 v[34:35], v6 offset0:36 offset1:38\n"
      "ds_read2st64_b32 v[36:37], v6 offset0:40 offset1:42\n"
      "v_mfma_f32_16x16x32_bf16 v[76:79], v[252:255], a[192:195], 0\n"
      "s_nop 0\n"
      "ds_read_b128 a[208:211], v1 offset:64\n"
      "ds_read2st64_b32 v[38:39], v6 offset0:44 offset1:46\n"
      "v_mfma_f32_16x16x32_bf16 v[80:83], v[244:247], a[192:195], 0\n"
      "ds_read_b64_tr_b16 a[244:245], v7 offset:53760\n"
      "ds_read_b64_tr_b16 a[246:247], v7 offset:53768\n"
      "v_mfma_f32_16x16x32_bf16 v[84:87], v[236:239], a[192:195], 0\n"
      "s_waitcnt lgkmcnt(8)\n"
      "v_fmac_f32 v40, s46, v28\n"
      "v_fmac_f32 v41, s46, v29\n"
      "v_mfma_f32_16x16x32_bf16 v[76:79], v[228:231], a[196:199], v[76:79]\n"
      "s_waitcnt lgkmcnt(6)\n"
      "ds_read_b128 a[212:215], v1 offset:128\n"
      "v_fmac_f32 v42, s46, v30\n"
      "v_mfma_f32_16x16x32_bf16 v[80:83], v[220:223], a[196:199], v[80:83]\n"
      "buffer_store_dwordx4 a[224:227], v16, s[8:11], s58 offen offset:128 sc1\n"
      "v_mfma_f32_16x16x32_bf16 v[84:87], v[212:215], a[196:199], v[84:87]\n"
      "ds_read2st64_b32 a[228:229], v12 offset0:177 offset1:179\n"
      "ds_read2st64_b32 a[230:231], v15 offset0:177 offset1:179\n"
      "v_mfma_f32_16x16x32_bf16 v[76:79], v[204:207], a[200:203], v[76:79]\n"
      "s_nop 0\n"
      "ds_read_b128 a[192:195], v2 offset:0\n"
      "v_fmac_f32 v43, s46, v31\n"
      "v_fma_f32 v28, 0.5, v28, v52\n"
      "v_mfma_f32_16x16x32_bf16 v[80:83], v[196:199], a[200:203], v[80:83]\n"
      "v_fma_f32 v29, 0.5, v29, v53\n"
      "v_fma_f32 v30, 0.5, v30, v54\n"
      "v_mfma_f32_16x16x32_bf16 v[84:87], v[188:191], a[200:203], v[84:87]\n"
      "s_waitcnt lgkmcnt(3)\n"
      "v_fma_f32 v31, 0.5, v31, v55\n"
      "v_fmac_f32 v40, s48, v64\n"
      "v_mfma_f32_4x4x4_16b_bf16 v[20:23], a[216:217], a[244:245], 0\n"
      "v_fmac_f32 v41, s48, v65\n"
      "v_mfma_f32_4x4x4_16b_bf16 v[24:27], a[216:217], a[246:247], 0\n"
      "v_fmac_f32 v42, s48, v66\n"
      "v_fmac_f32 v43, s48, v67\n"
      "v_mfma_f32_16x16x32_bf16 v[76:79], v[180:183], a[204:207], v[76:79]\n"
      "ds_read_b128 a[196:199], v2 offset:64\n"
      "v_fmac_f32 v28, s45, v40\n"
      "v_mfma_f32_16x16x32_bf16 v[80:83], v[172:175], a[204:207], v[80:83]\n"
      "s_nop 0\n"
      "v_cvt_pk_bf16_f32 v20, v20, v24\n"
      "v_cvt_pk_bf16_f32 v21, v21, v25\n"
      "v_mfma_f32_16x16x32_bf16 v[84:87], v[164:167], a[204:207], v[84:87]\n"
      "s_add_u32 s54, s54, 196608\n"
      "ds_write_addtid_b32 v20 offset:19456\n"
      "ds_write_addtid_b32 v21 offset:23552\n"
      "v_fmac_f32_e64 v29, s45, v41\n"
      "v_mfma_f32_16x16x32_bf16 v[76:79], v[156:159], a[208:211], v[76:79]\n"
      "s_add_u32 m0, s37, 116224\n"
      "ds_read_b128 a[200:203], v2 offset:128\n"
      "v_fmac_f32 v30, s45, v42\n"
      "v_mfma_f32_16x16x32_bf16 v[80:83], v[148:151], a[208:211], v[80:83]\n"
      "s_waitcnt lgkmcnt(4)\n"
      "buffer_load_dwordx4 v8, s[4:7], s54 offen offset:0 nt lds\n"
      "buffer_load_dwordx4 v9, s[4:7], s54 offen offset:1024 nt lds\n"
      "v_mfma_f32_16x16x32_bf16 v[84:87], v[140:143], a[208:211], v[84:87]\n"
      "s_mov_b32 m0, s64\n"
      "v_fmac_f32 v31, s45, v43\n"
      "v_cvt_pk_bf16_f32 v22, v22, v26\n"
      "v_mfma_f32_16x16x32_bf16 v[76:79], v[132:135], a[212:215], v[76:79]\n"
      "buffer_store_dwordx4 a[228:231], v16, s[8:11], s58 offen offset:256 sc1\n"
      "ds_read_b128 a[204:207], v0 offset:4096\n"
      "v_mfma_f32_16x16x32_bf16 v[80:83], v[124:127], a[212:215], v[80:83]\n"
      "ds_write_addtid_b32 v22 offset:27648\n"
      "v_fma_f32 v40, s47, v40, -v64\n"
      "v_mfma_f32_16x16x32_bf16 v[84:87], v[116:119], a[212:215], v[84:87]\n"
      "v_fma_f32 v41, s47, v41, -v65\n"
      "v_cvt_pk_bf16_f32 v23, v23, v27\n"
      "v_mfma_f32_16x16x32_bf16 v[76:79], v[108:111], a[192:195], v[76:79]\n"
      "ds_read_b128 a[208:211], v0 offset:4160\n"
      "ds_write_addtid_b32 v23 offset:31744\n"
      "v_mfma_f32_16x16x32_bf16 v[80:83], v[100:103], a[192:195], v[80:83]\n"
      "ds_read2st64_b32 a[232:233], v10 offset0:0 offset1:2\n"
      "ds_read2st64_b32 a[234:235], v13 offset0:0 offset1:2\n"
      "v_fma_f32 v42, s47, v42, -v66\n"
      "v_mfma_f32_16x16x32_bf16 v[84:87], a[160:163], a[192:195], v[84:87]\n"
      "s_waitcnt lgkmcnt(6)\n"
      "v_fma_f32 v43, s47, v43, -v67\n"
      "v_cvt_pk_bf16_f32 v28, v28, v29\n"
      "v_mfma_f32_16x16x32_bf16 v[76:79], a[152:155], a[196:199], v[76:79]\n"
      "s_nop 0\n"
      "ds_read_b128 a[212:215], v0 offset:4224\n"
      "ds_write_addtid_b32 v28 offset:33792\n"
      "v_mfma_f32_16x16x32_bf16 v[80:83], a[144:147], a[196:199], v[80:83]\n"
      "v_cvt_pk_bf16_f32 v30, v30, v31\n"
      "v_cvt_pk_bf16_f32 v40, v40, v41\n"
      "v_mfma_f32_16x16x32_bf16 v[84:87], a[136:139], a[196:199], v[84:87]\n"
      "ds_write_addtid_b32 v30 offset:34304\n"
      "ds_write_addtid_b32 v40 offset:35840\n"
      "v_mfma_f32_16x16x32_bf16 v[76:79], a[128:131], a[200:203], v[76:79]\n"
      "ds_read_b128 a[192:195], v1 offset:4096\n"
      "ds_read_b64_tr_b16 a[248:249], v17 offset:54272\n"
      "v_cvt_pk_bf16_f32 v42, v42, v43\n"
      "v_mfma_f32_16x16x32_bf16 v[80:83], a[120:123], a[200:203], v[80:83]\n"
      "ds_write_addtid_b32 v42 offset:36352\n"
      "ds_read_b64_tr_b16 a[250:251], v17 offset:54280\n"
      "v_fmac_f32 v44, s46, v32\n"
      "v_mfma_f32_16x16x32_bf16 v[84:87], a[112:115], a[200:203], v[84:87]\n"
      "s_waitcnt lgkmcnt(6)\n"
      "v_fmac_f32 v45, s46, v33\n"
      "v_fmac_f32 v46, s46, v34\n"
      "v_mfma_f32_16x16x32_bf16 v[88:91], v[248:251], a[204:207], v[76:79]\n"
      "ds_read_b128 a[196:199], v1 offset:4160\n"
      "v_fmac_f32 v47, s46, v35\n"
      "v_mfma_f32_16x16x32_bf16 v[92:95], v[240:243], a[204:207], v[80:83]\n"
      "v_fma_f32 v32, 0.5, v32, v56\n"
      "v_fma_f32 v33, 0.5, v33, v57\n"
      "v_mfma_f32_16x16x32_bf16 v[96:99], v[232:235], a[204:207], v[84:87]\n"
      "s_nop 0\n"
      "buffer_store_dwordx4 a[232:235], v16, s[8:11], s59 offen offset:0 sc1\n"
      "v_mfma_f32_16x16x32_bf16 v[88:91], v[224:227], a[208:211], v[88:91]\n"
      "s_waitcnt lgkmcnt(1)\n"
      "ds_read_b128 a[200:203], v1 offset:4224\n"
      "v_fma_f32 v34, 0.5, v34, v58\n"
      "v_mfma_f32_4x4x4_16b_bf16 v[20:23], a[218:219], a[248:249], 0\n"
      "v_fma_f32 v35, 0.5, v35, v59\n"
      "v_mfma_f32_4x4x4_16b_bf16 v[24:27], a[218:219], a[250:251], 0\n"
      "v_fmac_f32 v44, s48, v68\n"
      "v_fmac_f32_e64 v45, s48, v69\n"
      "v_mfma_f32_16x16x32_bf16 v[92:95], v[216:219], a[208:211], v[92:95]\n"
      "v_fmac_f32 v46, s48, v70\n"
      "v_fmac_f32 v47, s48, v71\n"
      "v_cvt_pk_bf16_f32 v20, v20, v24\n"
      "v_mfma_f32_16x16x32_bf16 v[96:99], v[208:211], a[208:211], v[96:99]\n"
      "v_cvt_pk_bf16_f32 v21, v21, v25\n"
      "v_fmac_f32 v32, s45, v44\n"
      "v_mfma_f32_16x16x32_bf16 v[88:91], v[200:203], a[212:215], v[88:91]\n"
      "ds_read_b128 a[204:207], v2 offset:4096\n"
      "v_fmac_f32 v33, s45, v45\n"
      "v_mfma_f32_16x16x32_bf16 v[92:95], v[192:195], a[212:215], v[92:95]\n"
      "s_mov_b32 m0, s41\n"
      "v_fmac_f32_e64 v34, s45, v46\n"
      "v_fmac_f32_e64 v35, s45, v47\n"
      "v_mfma_f32_16x16x32_bf16 v[96:99], v[184:187], a[212:215], v[96:99]\n"
      "s_nop 0\n"
      "ds_write_addtid_b32 v20 offset:19968\n"
      "ds_write_addtid_b32 v21 offset:24064\n"
      "v_mfma_f32_16x16x32_bf16 v[88:91], v[176:179], a[192:195], v[88:91]\n"
      "s_mov_b32 m0, s64\n"
      "ds_read_b128 a[208:211], v2 offset:4160\n"
      "v_fma_f32 v44, s47, v44, -v68\n"
      "v_mfma_f32_16x16x32_bf16 v[92:95], v[168:171], a[192:195], v[92:95]\n"
      "s_waitcnt lgkmcnt(4)\n"
      "v_fma_f32 v45, s47, v45, -v69\n"
      "v_fma_f32 v46, s47, v46, -v70\n"
      "v_mfma_f32_16x16x32_bf16 v[96:99], v[160:163], a[192:195], v[96:99]\n"
      "s_add_u32 s54, s54, 196608\n"
      "v_fma_f32 v47, s47, v47, -v71\n"
      "v_cvt_pk_bf16_f32 v32, v32, v33\n"
      "v_mfma_f32_16x16x32_bf16 v[88:91], v[152:155], a[196:199], v[88:91]\n"
      "s_add_u32 m0, s37, 120328\n"
      "ds_read_b128 a[212:215], v2 offset:4224\n"
      "v_cvt_pk_bf16_f32 v34, v34, v35\n"
      "v_mfma_f32_16x16x32_bf16 v[92:95], v[144:147], a[196:199], v[92:95]\n"
      "buffer_load_dwordx4 v8, s[4:7], s54 offen offset:0 nt lds\n"
      "buffer_load_dwordx4 v9, s[4:7], s54 offen offset:1024 nt lds\n"
      "v_mfma_f32_16x16x32_bf16 v[96:99], v[136:139], a[196:199], v[96:99]\n"
      "s_mov_b32 m0, s64\n"
      "v_cvt_pk_bf16_f32 v44, v44, v45\n"
      "v_cvt_pk_bf16_f32 v46, v46, v47\n"
      "v_mfma_f32_16x16x32_bf16 v[88:91], v[128:131], a[200:203], v[88:91]\n"
      "s_nop 0\n"
      "ds_read_b128 a[192:195], v3 offset:33792\n"
      "ds_write_addtid_b32 v32 offset:41988\n"
      "v_mfma_f32_16x16x32_bf16 v[92:95], v[120:123], a[200:203], v[92:95]\n"
      "ds_write_addtid_b32 v34 offset:42500\n"
      "ds_write_addtid_b32 v44 offset:44036\n"
      "v_fmac_f32 v48, s46, v36\n"
      "v_mfma_f32_16x16x32_bf16 v[96:99], v[112:115], a[200:203], v[96:99]\n"
      "s_waitcnt lgkmcnt(4)\n"
      "ds_read2st64_b32 a[236:237], v11 offset0:0 offset1:2\n"
      "ds_write_addtid_b32 v46 offset:44548\n"
      "v_fmac_f32 v49, s46, v37\n"
      "v_fmac_f32 v50, s46, v38\n"
      "v_mfma_f32_16x16x32_bf16 v[88:91], v[104:107], a[204:207], v[88:91]\n"
      "ds_read_b128 a[196:199], v3 offset:33856\n"
      "ds_read2st64_b32 a[238:239], v14 offset0:0 offset1:2\n"
      "v_mfma_f32_16x16x32_bf16 v[92:95], a[164:167], a[204:207], v[92:95]\n"
      "v_fmac_f32 v51, s46, v39\n"
      "v_fma_f32 v36, 0.5, v36, v60\n"
      "v_mfma_f32_16x16x32_bf16 v[96:99], a[156:159], a[204:207], v[96:99]\n"
      "s_nop 0\n"
      "v_fma_f32 v37, 0.5, v37, v61\n"
      "v_fma_f32 v38, 0.5, v38, v62\n"
      "v_mfma_f32_16x16x32_bf16 v[88:91], a[148:151], a[208:211], v[88:91]\n"
      "ds_read_b128 a[200:203], v3 offset:33920\n"
      "v_fma_f32 v39, 0.5, v39, v63\n"
      "v_mfma_f32_16x16x32_bf16 v[92:95], a[140:143], a[208:211], v[92:95]\n"
      "v_fmac_f32 v48, s48, v72\n"
      "v_fmac_f32 v49, s48, v73\n"
      "v_mfma_f32_16x16x32_bf16 v[96:99], a[132:135], a[208:211], v[96:99]\n"
      "s_waitcnt lgkmcnt(1)\n"
      "v_fmac_f32 v50, s48, v74\n"
      "v_fmac_f32 v51, s48, v75\n"
      "v_mfma_f32_16x16x32_bf16 v[88:91], a[124:127], a[212:215], v[88:91]\n"
      "s_add_u32 s54, s54, 196608\n"
      "ds_read_b128 a[204:207], v4 offset:33792\n"
      "ds_read2st64_b32 a[240:241], v12 offset0:0 offset1:2\n"
      "v_mfma_f32_16x16x32_bf16 v[92:95], a[116:119], a[212:215], v[92:95]\n"
      "s_add_u32 m0, s37, 124552\n"
      "ds_read2st64_b32 a[242:243], v15 offset0:0 offset1:2\n"
      "v_fmac_f32 v36, s45, v48\n"
      "v_mfma_f32_16x16x32_bf16 v[96:99], a[108:111], a[212:215], v[96:99]\n"
      "buffer_store_dwordx4 a[236:239], v16, s[8:11], s59 offen offset:128 sc1\n"
      "buffer_load_dwordx4 v8, s[4:7], s54 offen offset:0 nt lds\n"
      "buffer_load_dwordx4 v9, s[4:7], s54 offen offset:1024 nt lds\n"
      "v_mfma_f32_16x16x32_bf16 v[52:55], a[0:3], a[192:195], 0\n"
      "s_mov_b32 m0, s64\n"
      "ds_read_b128 a[208:211], v4 offset:33856\n"
      "ds_write_addtid_b32 v88 offset:45312\n"
      "v_fmac_f32 v37, s45, v49\n"
      "v_mfma_f32_16x16x32_bf16 v[56:59], a[4:7], a[192:195], 0\n"
      "ds_write_addtid_b32 v89 offset:45824\n"
      "ds_write_addtid_b32 v90 offset:46336\n"
      "v_fmac_f32 v38, s45, v50\n"
      "v_mfma_f32_16x16x32_bf16 v[60:63], a[8:11], a[192:195], 0\n"
      "ds_write_addtid_b32 v91 offset:46848\n"
      "ds_write_addtid_b32 v92 offset:47360\n"
      "v_mfma_f32_16x16x32_bf16 v[52:55], a[12:15], a[196:199], v[52:55]\n"
      "s_nop 0\n"
      "ds_read_b128 a[212:215], v4 offset:33920\n"
      "ds_write_addtid_b32 v93 offset:47872\n"
      "v_fmac_f32 v39, s45, v51\n"
      "v_mfma_f32_16x16x32_bf16 v[56:59], a[16:19], a[196:199], v[56:59]\n"
      "ds_write_addtid_b32 v94 offset:48384\n"
      "ds_write_addtid_b32 v95 offset:48896\n"
      "v_mfma_f32_16x16x32_bf16 v[60:63], a[20:23], a[196:199], v[60:63]\n"
      "s_waitcnt lgkmcnt(7)\n"
      "ds_write_addtid_b32 v96 offset:49408\n"
      "ds_write_addtid_b32 v97 offset:49920\n"
      "v_mfma_f32_16x16x32_bf16 v[52:55], a[24:27], a[200:203], v[52:55]\n"
      "ds_read_b128 a[192:195], v5 offset:33792\n"
      "ds_write_addtid_b32 v98 offset:50432\n"
      "v_mfma_f32_16x16x32_bf16 v[56:59], a[28:31], a[200:203], v[56:59]\n"
      "buffer_store_dwordx4 a[240:243], v16, s[8:11], s59 offen offset:256 sc1\n"
      "v_mfma_f32_16x16x32_bf16 v[60:63], a[32:35], a[200:203], v[60:63]\n"
      "ds_write_addtid_b32 v99 offset:50944\n"
      "v_fma_f32 v48, s47, v48, -v72\n"
      "v_fma_f32 v49, s47, v49, -v73\n"
      "v_mfma_f32_16x16x32_bf16 v[52:55], a[36:39], a[204:207], v[52:55]\n"
      "ds_read_b128 a[196:199], v5 offset:33856\n"
      "v_fma_f32 v50, s47, v50, -v74\n"
      "v_mfma_f32_16x16x32_bf16 v[56:59], a[40:43], a[204:207], v[56:59]\n"
      "v_fma_f32 v51, s47, v51, -v75\n"
      "v_cvt_pk_bf16_f32 v36, v36, v37\n"
      "v_mfma_f32_16x16x32_bf16 v[60:63], a[44:47], a[204:207], v[60:63]\n"
      "ds_write_addtid_b32 v36 offset:37888\n"
      "v_cvt_pk_bf16_f32 v38, v38, v39\n"
      "v_cvt_pk_bf16_f32 v48, v48, v49\n"
      "v_cvt_pk_bf16_f32 v50, v50, v51\n"
      "v_mfma_f32_16x16x32_bf16 v[52:55], a[48:51], a[208:211], v[52:55]\n"
      "ds_read_b128 a[200:203], v5 offset:33920\n"
      "ds_write_addtid_b32 v38 offset:38400\n"
      "v_mfma_f32_16x16x32_bf16 v[56:59], a[52:55], a[208:211], v[56:59]\n"
      "s_nop 0\n"
      "ds_write_addtid_b32 v48 offset:39936\n"
      "ds_write_addtid_b32 v50 offset:40448\n"
      "v_mfma_f32_16x16x32_bf16 v[60:63], a[56:59], a[208:211], v[60:63]\n"
      "s_waitcnt lgkmcnt(5)\n"
      "v_fma_f32 v40, 2.0, v76, -v88\n"
      "v_fma_f32 v41, 2.0, v77, -v89\n"
      "v_mfma_f32_16x16x32_bf16 v[52:55], a[60:63], a[212:215], v[52:55]\n"
      "s_nop 0\n"
      "v_fma_f32 v42, 2.0, v78, -v90\n"
      "v_fma_f32 v43, 2.0, v79, -v91\n"
      "v_mfma_f32_16x16x32_bf16 v[56:59], a[64:67], a[212:215], v[56:59]\n"
      "s_waitcnt vmcnt(25)\n"
      "v_fma_f32 v44, 2.0, v80, -v92\n"
      "v_fma_f32 v45, 2.0, v81, -v93\n"
      "v_mfma_f32_16x16x32_bf16 v[60:63], a[68:71], a[212:215], v[60:63]\n"
      "s_waitcnt lgkmcnt(0)\n"
      "s_barrier\n"
      "v_mfma_f32_16x16x32_bf16 v[52:55], a[72:75], a[192:195], v[52:55]\n"
      "s_add_u32 s58, s40, 0\n"
      "ds_read_b128 a[204:207], v0 offset:16384\n"
      "ds_read2st64_b32 a[220:221], v10 offset0:181 offset1:183\n"
      "ds_read2st64_b32 a[222:223], v13 offset0:181 offset1:183\n"
      "ds_read2st64_b32 v[28:29], v6 offset0:0 offset1:2\n"
      "v_mfma_f32_16x16x32_bf16 v[56:59], a[76:79], a[192:195], v[56:59]\n"
      "s_mov_b32 s40, s62\n"
      "ds_read_b128 a[208:211], v0 offset:16448\n"
      "v_fma_f32 v46, 2.0, v82, -v94\n"
      "v_mfma_f32_16x16x32_bf16 v[60:63], a[80:83], a[192:195], v[60:63]\n"
      "s_add_u32 s62, s62, 786432\n"
      "ds_read2st64_b32 v[30:31], v6 offset0:4 offset1:6\n"
      "ds_read2st64_b32 v[32:33], v6 offset0:8 offset1:10\n"
      "v_mfma_f32_16x16x32_bf16 v[52:55], a[84:87], a[196:199], v[52:55]\n"
      "s_add_u32 s59, s58, 196608\n"
      "ds_read_b128 a[212:215], v0 offset:16512\n"
      "v_fma_f32 v47, 2.0, v83, -v95\n"
      "v_mfma_f32_16x16x32_bf16 v[56:59], a[88:91], a[196:199], v[56:59]\n"
      "s_nop 0\n"
      "s_waitcnt lgkmcnt(5)\n"
      "ds_read2st64_b32 a[224:225], v11 offset0:181 offset1:183\n"
      "v_fma_f32 v48, 2.0, v84, -v96\n"
      "v_mfma_f32_16x16x32_bf16 v[60:63], a[92:95], a[196:199], v[60:63]\n"
      "s_add_u32 s54, s54, 196608\n"
      "v_fma_f32 v49, 2.0, v85, -v97\n"
      "v_fma_f32 v50, 2.0, v86, -v98\n"
      "v_mfma_f32_16x16x32_bf16 v[52:55], a[96:99], a[200:203], v[52:55]\n"
      "s_add_u32 m0, s37, 128896\n"
      "buffer_store_dwordx4 a[220:223], v16, s[8:11], s58 offen offset:0 sc1\n"
      "ds_read_b128 a[192:195], v1 offset:16384\n"
      "ds_read2st64_b32 a[226:227], v14 offset0:181 offset1:183\n"
      "v_mfma_f32_16x16x32_bf16 v[56:59], a[100:103], a[200:203], v[56:59]\n"
      "buffer_load_dwordx4 v8, s[4:7], s54 offen offset:0 nt lds\n"
      "buffer_load_dwordx4 v9, s[4:7], s54 offen offset:1024 nt lds\n"
      "v_fma_f32 v51, 2.0, v87, -v99\n"
      "v_mfma_f32_16x16x32_bf16 v[60:63], a[104:107], a[200:203], v[60:63]\n"
      "s_mov_b32 m0, s64\n"
      "ds_read2st64_b32 v[34:35], v6 offset0:12 offset1:14\n"
      "ds_read2st64_b32 v[36:37], v6 offset0:16 offset1:18\n"
      "v_mfma_f32_16x16x32_bf16 v[76:79], v[252:255], a[204:207], 0\n"
      "s_nop 0\n"
      "ds_read_b128 a[196:199], v1 offset:16448\n"
      "ds_read2st64_b32 v[38:39], v6 offset0:20 offset1:22\n"
      "v_mfma_f32_16x16x32_bf16 v[80:83], v[244:247], a[204:207], 0\n"
      "ds_read_b64_tr_b16 a[244:245], v7 offset:3072\n"
      "ds_read_b64_tr_b16 a[246:247], v7 offset:3080\n"
      "v_mfma_f32_16x16x32_bf16 v[84:87], v[236:239], a[204:207], 0\n"
      "s_waitcnt lgkmcnt(8)\n"
      "v_fmac_f32 v40, s46, v28\n"
      "v_fmac_f32 v41, s46, v29\n"
      "v_mfma_f32_16x16x32_bf16 v[76:79], v[228:231], a[208:211], v[76:79]\n"
      "s_waitcnt lgkmcnt(6)\n"
      "ds_read_b128 a[200:203], v1 offset:16512\n"
      "v_fmac_f32 v42, s46, v30\n"
      "v_mfma_f32_16x16x32_bf16 v[80:83], v[220:223], a[208:211], v[80:83]\n"
      "buffer_store_dwordx4 a[224:227], v16, s[8:11], s58 offen offset:128 sc1\n"
      "v_mfma_f32_16x16x32_bf16 v[84:87], v[212:215], a[208:211], v[84:87]\n"
      "ds_read2st64_b32 a[228:229], v12 offset0:181 offset1:183\n"
      "ds_read2st64_b32 a[230:231], v15 offset0:181 offset1:183\n"
      "v_mfma_f32_16x16x32_bf16 v[76:79], v[204:207], a[212:215], v[76:79]\n"
      "s_nop 0\n"
      "ds_read_b128 a[204:207], v2 offset:16384\n"
      "v_fmac_f32 v43, s46, v31\n"
      "v_fma_f32 v28, 0.5, v28, v64\n"
      "v_mfma_f32_16x16x32_bf16 v[80:83], v[196:199], a[212:215], v[80:83]\n"
      "v_fma_f32 v29, 0.5, v29, v65\n"
      "v_fma_f32 v30, 0.5, v30, v66\n"
      "v_mfma_f32_16x16x32_bf16 v[84:87], v[188:191], a[212:215], v[84:87]\n"
      "s_waitcnt lgkmcnt(3)\n"
      "v_fma_f32 v31, 0.5, v31, v67\n"
      "v_fmac_f32 v40, s48, v52\n"
      "v_mfma_f32_4x4x4_16b_bf16 v[20:23], a[216:217], a[244:245], 0\n"
      "v_fmac_f32 v41, s48, v53\n"
      "v_mfma_f32_4x4x4_16b_bf16 v[24:27], a[216:217], a[246:247], 0\n"
      "v_fmac_f32 v42, s48, v54\n"
      "v_fmac_f32 v43, s48, v55\n"
      "v_mfma_f32_16x16x32_bf16 v[76:79], v[180:183], a[192:195], v[76:79]\n"
      "ds_read_b128 a[208:211], v2 offset:16448\n"
      "v_fmac_f32 v28, s45, v40\n"
      "v_mfma_f32_16x16x32_bf16 v[80:83], v[172:175], a[192:195], v[80:83]\n"
      "s_nop 0\n"
      "v_cvt_pk_bf16_f32 v20, v20, v24\n"
      "v_cvt_pk_bf16_f32 v21, v21, v25\n"
      "v_mfma_f32_16x16x32_bf16 v[84:87], v[164:167], a[192:195], v[84:87]\n"
      "s_add_u32 s54, s54, 196608\n"
      "ds_write_addtid_b32 v20 offset:3072\n"
      "ds_write_addtid_b32 v21 offset:7168\n"
      "v_fmac_f32_e64 v29, s45, v41\n"
      "v_mfma_f32_16x16x32_bf16 v[76:79], v[156:159], a[196:199], v[76:79]\n"
      "s_add_u32 m0, s37, 133120\n"
      "ds_read_b128 a[212:215], v2 offset:16512\n"
      "v_fmac_f32 v30, s45, v42\n"
      "v_mfma_f32_16x16x32_bf16 v[80:83], v[148:151], a[196:199], v[80:83]\n"
      "s_waitcnt lgkmcnt(4)\n"
      "buffer_load_dwordx4 v8, s[4:7], s54 offen offset:0 nt lds\n"
      "buffer_load_dwordx4 v9, s[4:7], s54 offen offset:1024 nt lds\n"
      "v_mfma_f32_16x16x32_bf16 v[84:87], v[140:143], a[196:199], v[84:87]\n"
      "s_mov_b32 m0, s64\n"
      "v_fmac_f32 v31, s45, v43\n"
      "v_cvt_pk_bf16_f32 v22, v22, v26\n"
      "v_mfma_f32_16x16x32_bf16 v[76:79], v[132:135], a[200:203], v[76:79]\n"
      "buffer_store_dwordx4 a[228:231], v16, s[8:11], s58 offen offset:256 sc1\n"
      "ds_read_b128 a[192:195], v0 offset:20480\n"
      "v_mfma_f32_16x16x32_bf16 v[80:83], v[124:127], a[200:203], v[80:83]\n"
      "ds_write_addtid_b32 v22 offset:11264\n"
      "v_fma_f32 v40, s47, v40, -v52\n"
      "v_mfma_f32_16x16x32_bf16 v[84:87], v[116:119], a[200:203], v[84:87]\n"
      "v_fma_f32 v41, s47, v41, -v53\n"
      "v_cvt_pk_bf16_f32 v23, v23, v27\n"
      "v_mfma_f32_16x16x32_bf16 v[76:79], v[108:111], a[204:207], v[76:79]\n"
      "ds_read_b128 a[196:199], v0 offset:20544\n"
      "ds_write_addtid_b32 v23 offset:15360\n"
      "v_mfma_f32_16x16x32_bf16 v[80:83], v[100:103], a[204:207], v[80:83]\n"
      "ds_read2st64_b32 a[232:233], v10 offset0:4 offset1:6\n"
      "ds_read2st64_b32 a[234:235], v13 offset0:4 offset1:6\n"
      "v_fma_f32 v42, s47, v42, -v54\n"
      "v_mfma_f32_16x16x32_bf16 v[84:87], a[160:163], a[204:207], v[84:87]\n"
      "s_waitcnt lgkmcnt(6)\n"
      "v_fma_f32 v43, s47, v43, -v55\n"
      "v_cvt_pk_bf16_f32 v28, v28, v29\n"
      "v_mfma_f32_16x16x32_bf16 v[76:79], a[152:155], a[208:211], v[76:79]\n"
      "s_nop 0\n"
      "ds_read_b128 a[200:203], v0 offset:20608\n"
      "ds_write_addtid_b32 v28 offset:32768\n"
      "v_mfma_f32_16x16x32_bf16 v[80:83], a[144:147], a[208:211], v[80:83]\n"
      "v_cvt_pk_bf16_f32 v30, v30, v31\n"
      "v_cvt_pk_bf16_f32 v40, v40, v41\n"
      "v_mfma_f32_16x16x32_bf16 v[84:87], a[136:139], a[208:211], v[84:87]\n"
      "ds_write_addtid_b32 v30 offset:33280\n"
      "ds_write_addtid_b32 v40 offset:34816\n"
      "v_mfma_f32_16x16x32_bf16 v[76:79], a[128:131], a[212:215], v[76:79]\n"
      "ds_read_b128 a[204:207], v1 offset:20480\n"
      "ds_read_b64_tr_b16 a[248:249], v17 offset:3584\n"
      "v_cvt_pk_bf16_f32 v42, v42, v43\n"
      "v_mfma_f32_16x16x32_bf16 v[80:83], a[120:123], a[212:215], v[80:83]\n"
      "ds_write_addtid_b32 v42 offset:35328\n"
      "ds_read_b64_tr_b16 a[250:251], v17 offset:3592\n"
      "v_fmac_f32 v44, s46, v32\n"
      "v_mfma_f32_16x16x32_bf16 v[84:87], a[112:115], a[212:215], v[84:87]\n"
      "s_waitcnt lgkmcnt(6)\n"
      "v_fmac_f32 v45, s46, v33\n"
      "v_fmac_f32 v46, s46, v34\n"
      "v_mfma_f32_16x16x32_bf16 v[88:91], v[248:251], a[192:195], v[76:79]\n"
      "ds_read_b128 a[208:211], v1 offset:20544\n"
      "v_fmac_f32 v47, s46, v35\n"
      "v_mfma_f32_16x16x32_bf16 v[92:95], v[240:243], a[192:195], v[80:83]\n"
      "v_fma_f32 v32, 0.5, v32, v68\n"
      "v_fma_f32 v33, 0.5, v33, v69\n"
      "v_mfma_f32_16x16x32_bf16 v[96:99], v[232:235], a[192:195], v[84:87]\n"
      "s_nop 0\n"
      "buffer_store_dwordx4 a[232:235], v16, s[8:11], s59 offen offset:0 sc1\n"
      "v_mfma_f32_16x16x32_bf16 v[88:91], v[224:227], a[196:199], v[88:91]\n"
      "s_waitcnt lgkmcnt(1)\n"
      "ds_read_b128 a[212:215], v1 offset:20608\n"
      "v_fma_f32 v34, 0.5, v34, v70\n"
      "v_mfma_f32_4x4x4_16b_bf16 v[20:23], a[218:219], a[248:249], 0\n"
      "v_fma_f32 v35, 0.5, v35, v71\n"
      "v_mfma_f32_4x4x4_16b_bf16 v[24:27], a[218:219], a[250:251], 0\n"
      "v_fmac_f32 v44, s48, v56\n"
      "v_fmac_f32_e64 v45, s48, v57\n"
      "v_mfma_f32_16x16x32_bf16 v[92:95], v[216:219], a[196:199], v[92:95]\n"
      "v_fmac_f32 v46, s48, v58\n"
      "v_fmac_f32 v47, s48, v59\n"
      "v_cvt_pk_bf16_f32 v20, v20, v24\n"
      "v_mfma_f32_16x16x32_bf16 v[96:99], v[208:211], a[196:199], v[96:99]\n"
      "v_cvt_pk_bf16_f32 v21, v21, v25\n"
      "v_fmac_f32 v32, s45, v44\n"
      "v_mfma_f32_16x16x32_bf16 v[88:91], v[200:203], a[200:203], v[88:91]\n"
      "ds_read_b128 a[192:195], v2 offset:20480\n"
      "v_fmac_f32 v33, s45, v45\n"
      "v_mfma_f32_16x16x32_bf16 v[92:95], v[192:195], a[200:203], v[92:95]\n"
      "s_mov_b32 m0, s41\n"
      "v_fmac_f32_e64 v34, s45, v46\n"
      "v_fmac_f32_e64 v35, s45, v47\n"
      "v_mfma_f32_16x16x32_bf16 v[96:99], v[184:187], a[200:203], v[96:99]\n"
      "s_nop 0\n"
      "ds_write_addtid_b32 v20 offset:3584\n"
      "ds_write_addtid_b32 v21 offset:7680\n"
      "v_mfma_f32_16x16x32_bf16 v[88:91], v[176:179], a[204:207], v[88:91]\n"
      "s_mov_b32 m0, s64\n"
      "ds_read_b128 a[196:199], v2 offset:20544\n"
      "v_fma_f32 v44, s47, v44, -v56\n"
      "v_mfma_f32_16x16x32_bf16 v[92:95], v[168:171], a[204:207], v[92:95]\n"
      "s_waitcnt lgkmcnt(4)\n"
      "v_fma_f32 v45, s47, v45, -v57\n"
      "v_fma_f32 v46, s47, v46, -v58\n"
      "v_mfma_f32_16x16x32_bf16 v[96:99], v[160:163], a[204:207], v[96:99]\n"
      "s_add_u32 s54, s54, 196608\n"
      "v_fma_f32 v47, s47, v47, -v59\n"
      "v_cvt_pk_bf16_f32 v32, v32, v33\n"
      "v_mfma_f32_16x16x32_bf16 v[88:91], v[152:155], a[208:211], v[88:91]\n"
      "s_add_u32 m0, s37, 137224\n"
      "ds_read_b128 a[200:203], v2 offset:20608\n"
      "v_cvt_pk_bf16_f32 v34, v34, v35\n"
      "v_mfma_f32_16x16x32_bf16 v[92:95], v[144:147], a[208:211], v[92:95]\n"
      "buffer_load_dwordx4 v8, s[4:7], s54 offen offset:0 nt lds\n"
      "buffer_load_dwordx4 v9, s[4:7], s54 offen offset:1024 nt lds\n"
      "v_mfma_f32_16x16x32_bf16 v[96:99], v[136:139], a[208:211], v[96:99]\n"
      "s_mov_b32 m0, s64\n"
      "v_cvt_pk_bf16_f32 v44, v44, v45\n"
      "v_cvt_pk_bf16_f32 v46, v46, v47\n"
      "v_mfma_f32_16x16x32_bf16 v[88:91], v[128:131], a[212:215], v[88:91]\n"
      "s_nop 0\n"
      "ds_read_b128 a[204:207], v3 offset:50688\n"
      "ds_write_addtid_b32 v32 offset:40964\n"
      "v_mfma_f32_16x16x32_bf16 v[92:95], v[120:123], a[212:215], v[92:95]\n"
      "ds_write_addtid_b32 v34 offset:41476\n"
      "ds_write_addtid_b32 v44 offset:43012\n"
      "v_fmac_f32 v48, s46, v36\n"
      "v_mfma_f32_16x16x32_bf16 v[96:99], v[112:115], a[212:215], v[96:99]\n"
      "s_waitcnt lgkmcnt(4)\n"
      "ds_read2st64_b32 a[236:237], v11 offset0:4 offset1:6\n"
      "ds_write_addtid_b32 v46 offset:43524\n"
      "v_fmac_f32 v49, s46, v37\n"
      "v_fmac_f32 v50, s46, v38\n"
      "v_mfma_f32_16x16x32_bf16 v[88:91], v[104:107], a[192:195], v[88:91]\n"
      "ds_read_b128 a[208:211], v3 offset:50752\n"
      "ds_read2st64_b32 a[238:239], v14 offset0:4 offset1:6\n"
      "v_mfma_f32_16x16x32_bf16 v[92:95], a[164:167], a[192:195], v[92:95]\n"
      "v_fmac_f32 v51, s46, v39\n"
      "v_fma_f32 v36, 0.5, v36, v72\n"
      "v_mfma_f32_16x16x32_bf16 v[96:99], a[156:159], a[192:195], v[96:99]\n"
      "s_nop 0\n"
      "v_fma_f32 v37, 0.5, v37, v73\n"
      "v_fma_f32 v38, 0.5, v38, v74\n"
      "v_mfma_f32_16x16x32_bf16 v[88:91], a[148:151], a[196:199], v[88:91]\n"
      "ds_read_b128 a[212:215], v3 offset:50816\n"
      "v_fma_f32 v39, 0.5, v39, v75\n"
      "v_mfma_f32_16x16x32_bf16 v[92:95], a[140:143], a[196:199], v[92:95]\n"
      "v_fmac_f32 v48, s48, v60\n"
      "v_fmac_f32 v49, s48, v61\n"
      "v_mfma_f32_16x16x32_bf16 v[96:99], a[132:135], a[196:199], v[96:99]\n"
      "s_waitcnt lgkmcnt(1)\n"
      "v_fmac_f32 v50, s48, v62\n"
      "v_fmac_f32 v51, s48, v63\n"
      "v_mfma_f32_16x16x32_bf16 v[88:91], a[124:127], a[200:203], v[88:91]\n"
      "s_add_u32 s54, s54, 196608\n"
      "ds_read_b128 a[192:195], v4 offset:50688\n"
      "ds_read2st64_b32 a[240:241], v12 offset0:4 offset1:6\n"
      "v_mfma_f32_16x16x32_bf16 v[92:95], a[116:119], a[200:203], v[92:95]\n"
      "s_add_u32 m0, s37, 141448\n"
      "ds_read2st64_b32 a[242:243], v15 offset0:4 offset1:6\n"
      "v_fmac_f32 v36, s45, v48\n"
      "v_mfma_f32_16x16x32_bf16 v[96:99], a[108:111], a[200:203], v[96:99]\n"
      "buffer_store_dwordx4 a[236:239], v16, s[8:11], s59 offen offset:128 sc1\n"
      "buffer_load_dwordx4 v8, s[4:7], s54 offen offset:0 nt lds\n"
      "buffer_load_dwordx4 v9, s[4:7], s54 offen offset:1024 nt lds\n"
      "v_mfma_f32_16x16x32_bf16 v[64:67], a[0:3], a[204:207], 0\n"
      "s_mov_b32 m0, s64\n"
      "ds_read_b128 a[196:199], v4 offset:50752\n"
      "ds_write_addtid_b32 v88 offset:51456\n"
      "v_fmac_f32 v37, s45, v49\n"
      "v_mfma_f32_16x16x32_bf16 v[68:71], a[4:7], a[204:207], 0\n"
      "ds_write_addtid_b32 v89 offset:51968\n"
      "ds_write_addtid_b32 v90 offset:52480\n"
      "v_fmac_f32 v38, s45, v50\n"
      "v_mfma_f32_16x16x32_bf16 v[72:75], a[8:11], a[204:207], 0\n"
      "ds_write_addtid_b32 v91 offset:52992\n"
      "ds_write_addtid_b32 v92 offset:53504\n"
      "v_mfma_f32_16x16x32_bf16 v[64:67], a[12:15], a[208:211], v[64:67]\n"
      "s_nop 0\n"
      "ds_read_b128 a[200:203], v4 offset:50816\n"
      "ds_write_addtid_b32 v93 offset:54016\n"
      "v_fmac_f32 v39, s45, v51\n"
      "v_mfma_f32_16x16x32_bf16 v[68:71], a[16:19], a[208:211], v[68:71]\n"
      "ds_write_addtid_b32 v94 offset:54528\n"
      "ds_write_addtid_b32 v95 offset:55040\n"
      "v_mfma_f32_16x16x32_bf16 v[72:75], a[20:23], a[208:211], v[72:75]\n"
      "s_waitcnt lgkmcnt(7)\n"
      "ds_write_addtid_b32 v96 offset:55552\n"
      "ds_write_addtid_b32 v97 offset:56064\n"
      "v_mfma_f32_16x16x32_bf16 v[64:67], a[24:27], a[212:215], v[64:67]\n"
      "ds_read_b128 a[204:207], v5 offset:50688\n"
      "ds_write_addtid_b32 v98 offset:56576\n"
      "v_mfma_f32_16x16x32_bf16 v[68:71], a[28:31], a[212:215], v[68:71]\n"
      "buffer_store_dwordx4 a[240:243], v16, s[8:11], s59 offen offset:256 sc1\n"
      "v_mfma_f32_16x16x32_bf16 v[72:75], a[32:35], a[212:215], v[72:75]\n"
      "ds_write_addtid_b32 v99 offset:57088\n"
      "v_fma_f32 v48, s47, v48, -v60\n"
      "v_fma_f32 v49, s47, v49, -v61\n"
      "v_mfma_f32_16x16x32_bf16 v[64:67], a[36:39], a[192:195], v[64:67]\n"
      "ds_read_b128 a[208:211], v5 offset:50752\n"
      "v_fma_f32 v50, s47, v50, -v62\n"
      "v_mfma_f32_16x16x32_bf16 v[68:71], a[40:43], a[192:195], v[68:71]\n"
      "v_fma_f32 v51, s47, v51, -v63\n"
      "v_cvt_pk_bf16_f32 v36, v36, v37\n"
      "v_mfma_f32_16x16x32_bf16 v[72:75], a[44:47], a[192:195], v[72:75]\n"
      "ds_write_addtid_b32 v36 offset:36864\n"
      "v_cvt_pk_bf16_f32 v38, v38, v39\n"
      "v_cvt_pk_bf16_f32 v48, v48, v49\n"
      "v_cvt_pk_bf16_f32 v50, v50, v51\n"
      "v_mfma_f32_16x16x32_bf16 v[64:67], a[48:51], a[196:199], v[64:67]\n"
      "ds_read_b128 a[212:215], v5 offset:50816\n"
      "ds_write_addtid_b32 v38 offset:37376\n"
      "v_mfma_f32_16x16x32_bf16 v[68:71], a[52:55], a[196:199], v[68:71]\n"
      "s_nop 0\n"
      "ds_write_addtid_b32 v48 offset:38912\n"
      "ds_write_addtid_b32 v50 offset:39424\n"
      "v_mfma_f32_16x16x32_bf16 v[72:75], a[56:59], a[196:199], v[72:75]\n"
      "s_waitcnt lgkmcnt(5)\n"
      "v_fma_f32 v40, 2.0, v76, -v88\n"
      "v_fma_f32 v41, 2.0, v77, -v89\n"
      "v_mfma_f32_16x16x32_bf16 v[64:67], a[60:63], a[200:203], v[64:67]\n"
      "s_nop 0\n"
      "v_fma_f32 v42, 2.0, v78, -v90\n"
      "v_fma_f32 v43, 2.0, v79, -v91\n"
      "v_mfma_f32_16x16x32_bf16 v[68:71], a[64:67], a[200:203], v[68:71]\n"
      "s_waitcnt vmcnt(25)\n"
      "v_fma_f32 v44, 2.0, v80, -v92\n"
      "v_fma_f32 v45, 2.0, v81, -v93\n"
      "v_mfma_f32_16x16x32_bf16 v[72:75], a[68:71], a[200:203], v[72:75]\n"
      "s_waitcnt lgkmcnt(0)\n"
      "s_barrier\n"
      "v_mfma_f32_16x16x32_bf16 v[64:67], a[72:75], a[204:207], v[64:67]\n"
      "s_add_u32 s58, s40, 0\n"
      "ds_read_b128 a[192:195], v0 offset:0\n"
      "ds_read2st64_b32 a[220:221], v10 offset0:177 offset1:179\n"
      "ds_read2st64_b32 a[222:223], v13 offset0:177 offset1:179\n"
      "ds_read2st64_b32 v[28:29], v6 offset0:24 offset1:26\n"
      "v_mfma_f32_16x16x32_bf16 v[68:71], a[76:79], a[204:207], v[68:71]\n"
      "s_mov_b32 s40, s62\n"
      "ds_read_b128 a[196:199], v0 offset:64\n"
      "v_fma_f32 v46, 2.0, v82, -v94\n"
      "v_mfma_f32_16x16x32_bf16 v[72:75], a[80:83], a[204:207], v[72:75]\n"
      "s_add_u32 s62, s62, 786432\n"
      "ds_read2st64_b32 v[30:31], v6 offset0:28 offset1:30\n"
      "ds_read2st64_b32 v[32:33], v6 offset0:32 offset1:34\n"
      "v_mfma_f32_16x16x32_bf16 v[64:67], a[84:87], a[208:211], v[64:67]\n"
      "s_add_u32 s59, s58, 196608\n"
      "ds_read_b128 a[200:203], v0 offset:128\n"
      "v_fma_f32 v47, 2.0, v83, -v95\n"
      "v_mfma_f32_16x16x32_bf16 v[68:71], a[88:91], a[208:211], v[68:71]\n"
      "s_nop 0\n"
      "s_waitcnt lgkmcnt(5)\n"
      "ds_read2st64_b32 a[224:225], v11 offset0:177 offset1:179\n"
      "v_fma_f32 v48, 2.0, v84, -v96\n"
      "v_mfma_f32_16x16x32_bf16 v[72:75], a[92:95], a[208:211], v[72:75]\n"
      "s_add_u32 s54, s54, 196608\n"
      "v_fma_f32 v49, 2.0, v85, -v97\n"
      "v_fma_f32 v50, 2.0, v86, -v98\n"
      "v_mfma_f32_16x16x32_bf16 v[64:67], a[96:99], a[212:215], v[64:67]\n"
      "s_add_u32 m0, s37, 145792\n"
      "buffer_store_dwordx4 a[220:223], v16, s[8:11], s58 offen offset:0 sc1\n"
      "ds_read_b128 a[204:207], v1 offset:0\n"
      "ds_read2st64_b32 a[226:227], v14 offset0:177 offset1:179\n"
      "v_mfma_f32_16x16x32_bf16 v[68:71], a[100:103], a[212:215], v[68:71]\n"
      "buffer_load_dwordx4 v8, s[4:7], s54 offen offset:0 nt lds\n"
      "buffer_load_dwordx4 v9, s[4:7], s54 offen offset:1024 nt lds\n"
      "v_fma_f32 v51, 2.0, v87, -v99\n"
      "v_mfma_f32_16x16x32_bf16 v[72:75], a[104:107], a[212:215], v[72:75]\n"
      "s_mov_b32 m0, s64\n"
      "ds_read2st64_b32 v[34:35], v6 offset0:36 offset1:38\n"
      "ds_read2st64_b32 v[36:37], v6 offset0:40 offset1:42\n"
      "v_mfma_f32_16x16x32_bf16 v[76:79], v[252:255], a[192:195], 0\n"
      "s_nop 0\n"
      "ds_read_b128 a[208:211], v1 offset:64\n"
      "ds_read2st64_b32 v[38:39], v6 offset0:44 offset1:46\n"
      "v_mfma_f32_16x16x32_bf16 v[80:83], v[244:247], a[192:195], 0\n"
      "ds_read_b64_tr_b16 a[244:245], v7 offset:19968\n"
      "ds_read_b64_tr_b16 a[246:247], v7 offset:19976\n"
      "v_mfma_f32_16x16x32_bf16 v[84:87], v[236:239], a[192:195], 0\n"
      "s_waitcnt lgkmcnt(8)\n"
      "v_fmac_f32 v40, s46, v28\n"
      "v_fmac_f32 v41, s46, v29\n"
      "v_mfma_f32_16x16x32_bf16 v[76:79], v[228:231], a[196:199], v[76:79]\n"
      "s_waitcnt lgkmcnt(6)\n"
      "ds_read_b128 a[212:215], v1 offset:128\n"
      "v_fmac_f32 v42, s46, v30\n"
      "v_mfma_f32_16x16x32_bf16 v[80:83], v[220:223], a[196:199], v[80:83]\n"
      "buffer_store_dwordx4 a[224:227], v16, s[8:11], s58 offen offset:128 sc1\n"
      "v_mfma_f32_16x16x32_bf16 v[84:87], v[212:215], a[196:199], v[84:87]\n"
      "ds_read2st64_b32 a[228:229], v12 offset0:177 offset1:179\n"
      "ds_read2st64_b32 a[230:231], v15 offset0:177 offset1:179\n"
      "v_mfma_f32_16x16x32_bf16 v[76:79], v[204:207], a[200:203], v[76:79]\n"
      "s_nop 0\n"
      "ds_read_b128 a[192:195], v2 offset:0\n"
      "v_fmac_f32 v43, s46, v31\n"
      "v_fma_f32 v28, 0.5, v28, v52\n"
      "v_mfma_f32_16x16x32_bf16 v[80:83], v[196:199], a[200:203], v[80:83]\n"
      "v_fma_f32 v29, 0.5, v29, v53\n"
      "v_fma_f32 v30, 0.5, v30, v54\n"
      "v_mfma_f32_16x16x32_bf16 v[84:87], v[188:191], a[200:203], v[84:87]\n"
      "s_waitcnt lgkmcnt(3)\n"
      "v_fma_f32 v31, 0.5, v31, v55\n"
      "v_fmac_f32 v40, s48, v64\n"
      "v_mfma_f32_4x4x4_16b_bf16 v[20:23], a[216:217], a[244:245], 0\n"
      "v_fmac_f32 v41, s48, v65\n"
      "v_mfma_f32_4x4x4_16b_bf16 v[24:27], a[216:217], a[246:247], 0\n"
      "v_fmac_f32 v42, s48, v66\n"
      "v_fmac_f32 v43, s48, v67\n"
      "v_mfma_f32_16x16x32_bf16 v[76:79], v[180:183], a[204:207], v[76:79]\n"
      "ds_read_b128 a[196:199], v2 offset:64\n"
      "v_fmac_f32 v28, s45, v40\n"
      "v_mfma_f32_16x16x32_bf16 v[80:83], v[172:175], a[204:207], v[80:83]\n"
      "s_nop 0\n"
      "v_cvt_pk_bf16_f32 v20, v20, v24\n"
      "v_cvt_pk_bf16_f32 v21, v21, v25\n"
      "v_mfma_f32_16x16x32_bf16 v[84:87], v[164:167], a[204:207], v[84:87]\n"
      "s_add_u32 s54, s54, 196608\n"
      "ds_write_addtid_b32 v20 offset:19456\n"
      "ds_write_addtid_b32 v21 offset:23552\n"
      "v_fmac_f32_e64 v29, s45, v41\n"
      "v_mfma_f32_16x16x32_bf16 v[76:79], v[156:159], a[208:211], v[76:79]\n"
      "s_add_u32 m0, s37, 82432\n"
      "ds_read_b128 a[200:203], v2 offset:128\n"
      "v_fmac_f32 v30, s45, v42\n"
      "v_mfma_f32_16x16x32_bf16 v[80:83], v[148:151], a[208:211], v[80:83]\n"
      "s_waitcnt lgkmcnt(4)\n"
      "buffer_load_dwordx4 v8, s[4:7], s54 offen offset:0 nt lds\n"
      "buffer_load_dwordx4 v9, s[4:7], s54 offen offset:1024 nt lds\n"
      "v_mfma_f32_16x16x32_bf16 v[84:87], v[140:143], a[208:211], v[84:87]\n"
      "s_mov_b32 m0, s64\n"
      "v_fmac_f32 v31, s45, v43\n"
      "v_cvt_pk_bf16_f32 v22, v22, v26\n"
      "v_mfma_f32_16x16x32_bf16 v[76:79], v[132:135], a[212:215], v[76:79]\n"
      "buffer_store_dwordx4 a[228:231], v16, s[8:11], s58 offen offset:256 sc1\n"
      "ds_read_b128 a[204:207], v0 offset:4096\n"
      "v_mfma_f32_16x16x32_bf16 v[80:83], v[124:127], a[212:215], v[80:83]\n"
      "ds_write_addtid_b32 v22 offset:27648\n"
      "v_fma_f32 v40, s47, v40, -v64\n"
      "v_mfma_f32_16x16x32_bf16 v[84:87], v[116:119], a[212:215], v[84:87]\n"
      "v_fma_f32 v41, s47, v41, -v65\n"
      "v_cvt_pk_bf16_f32 v23, v23, v27\n"
      "v_mfma_f32_16x16x32_bf16 v[76:79], v[108:111], a[192:195], v[76:79]\n"
      "ds_read_b128 a[208:211], v0 offset:4160\n"
      "ds_write_addtid_b32 v23 offset:31744\n"
      "v_mfma_f32_16x16x32_bf16 v[80:83], v[100:103], a[192:195], v[80:83]\n"
      "ds_read2st64_b32 a[232:233], v10 offset0:0 offset1:2\n"
      "ds_read2st64_b32 a[234:235], v13 offset0:0 offset1:2\n"
      "v_fma_f32 v42, s47, v42, -v66\n"
      "v_mfma_f32_16x16x32_bf16 v[84:87], a[160:163], a[192:195], v[84:87]\n"
      "s_waitcnt lgkmcnt(6)\n"
      "v_fma_f32 v43, s47, v43, -v67\n"
      "v_cvt_pk_bf16_f32 v28, v28, v29\n"
      "v_mfma_f32_16x16x32_bf16 v[76:79], a[152:155], a[196:199], v[76:79]\n"
      "s_nop 0\n"
      "ds_read_b128 a[212:215], v0 offset:4224\n"
      "ds_write_addtid_b32 v28 offset:33792\n"
      "v_mfma_f32_16x16x32_bf16 v[80:83], a[144:147], a[196:199], v[80:83]\n"
      "v_cvt_pk_bf16_f32 v30, v30, v31\n"
      "v_cvt_pk_bf16_f32 v40, v40, v41\n"
      "v_mfma_f32_16x16x32_bf16 v[84:87], a[136:139], a[196:199], v[84:87]\n"
      "ds_write_addtid_b32 v30 offset:34304\n"
      "ds_write_addtid_b32 v40 offset:35840\n"
      "v_mfma_f32_16x16x32_bf16 v[76:79], a[128:131], a[200:203], v[76:79]\n"
      "ds_read_b128 a[192:195], v1 offset:4096\n"
      "ds_read_b64_tr_b16 a[248:249], v17 offset:20480\n"
      "v_cvt_pk_bf16_f32 v42, v42, v43\n"
      "v_mfma_f32_16x16x32_bf16 v[80:83], a[120:123], a[200:203], v[80:83]\n"
      "ds_write_addtid_b32 v42 offset:36352\n"
      "ds_read_b64_tr_b16 a[250:251], v17 offset:20488\n"
      "v_fmac_f32 v44, s46, v32\n"
      "v_mfma_f32_16x16x32_bf16 v[84:87], a[112:115], a[200:203], v[84:87]\n"
      "s_waitcnt lgkmcnt(6)\n"
      "v_fmac_f32 v45, s46, v33\n"
      "v_fmac_f32 v46, s46, v34\n"
      "v_mfma_f32_16x16x32_bf16 v[88:91], v[248:251], a[204:207], v[76:79]\n"
      "ds_read_b128 a[196:199], v1 offset:4160\n"
      "v_fmac_f32 v47, s46, v35\n"
      "v_mfma_f32_16x16x32_bf16 v[92:95], v[240:243], a[204:207], v[80:83]\n"
      "v_fma_f32 v32, 0.5, v32, v56\n"
      "v_fma_f32 v33, 0.5, v33, v57\n"
      "v_mfma_f32_16x16x32_bf16 v[96:99], v[232:235], a[204:207], v[84:87]\n"
      "s_nop 0\n"
      "buffer_store_dwordx4 a[232:235], v16, s[8:11], s59 offen offset:0 sc1\n"
      "v_mfma_f32_16x16x32_bf16 v[88:91], v[224:227], a[208:211], v[88:91]\n"
      "s_waitcnt lgkmcnt(1)\n"
      "ds_read_b128 a[200:203], v1 offset:4224\n"
      "v_fma_f32 v34, 0.5, v34, v58\n"
      "v_mfma_f32_4x4x4_16b_bf16 v[20:23], a[218:219], a[248:249], 0\n"
      "v_fma_f32 v35, 0.5, v35, v59\n"
      "v_mfma_f32_4x4x4_16b_bf16 v[24:27], a[218:219], a[250:251], 0\n"
      "v_fmac_f32 v44, s48, v68\n"
      "v_fmac_f32_e64 v45, s48, v69\n"
      "v_mfma_f32_16x16x32_bf16 v[92:95], v[216:219], a[208:211], v[92:95]\n"
      "v_fmac_f32 v46, s48, v70\n"
      "v_fmac_f32 v47, s48, v71\n"
      "v_cvt_pk_bf16_f32 v20, v20, v24\n"
      "v_mfma_f32_16x16x32_bf16 v[96:99], v[208:211], a[208:211], v[96:99]\n"
      "v_cvt_pk_bf16_f32 v21, v21, v25\n"
      "v_fmac_f32 v32, s45, v44\n"
      "v_mfma_f32_16x16x32_bf16 v[88:91], v[200:203], a[212:215], v[88:91]\n"
      "ds_read_b128 a[204:207], v2 offset:4096\n"
      "v_fmac_f32 v33, s45, v45\n"
      "v_mfma_f32_16x16x32_bf16 v[92:95], v[192:195], a[212:215], v[92:95]\n"
      "s_mov_b32 m0, s41\n"
      "v_fmac_f32_e64 v34, s45, v46\n"
      "v_fmac_f32_e64 v35, s45, v47\n"
      "v_mfma_f32_16x16x32_bf16 v[96:99], v[184:187], a[212:215], v[96:99]\n"
      "s_nop 0\n"
      "ds_write_addtid_b32 v20 offset:19968\n"
      "ds_write_addtid_b32 v21 offset:24064\n"
      "v_mfma_f32_16x16x32_bf16 v[88:91], v[176:179], a[192:195], v[88:91]\n"
      "s_mov_b32 m0, s64\n"
      "ds_read_b128 a[208:211], v2 offset:4160\n"
      "v_fma_f32 v44, s47, v44, -v68\n"
      "v_mfma_f32_16x16x32_bf16 v[92:95], v[168:171], a[192:195], v[92:95]\n"
      "s_waitcnt lgkmcnt(4)\n"
      "v_fma_f32 v45, s47, v45, -v69\n"
      "v_fma_f32 v46, s47, v46, -v70\n"
      "v_mfma_f32_16x16x32_bf16 v[96:99], v[160:163], a[192:195], v[96:99]\n"
      "s_add_u32 s54, s54, 196608\n"
      "v_fma_f32 v47, s47, v47, -v71\n"
      "v_cvt_pk_bf16_f32 v32, v32, v33\n"
      "v_mfma_f32_16x16x32_bf16 v[88:91], v[152:155], a[196:199], v[88:91]\n"
      "s_add_u32 m0, s37, 86536\n"
      "ds_read_b128 a[212:215], v2 offset:4224\n"
      "v_cvt_pk_bf16_f32 v34, v34, v35\n"
      "v_mfma_f32_16x16x32_bf16 v[92:95], v[144:147], a[196:199], v[92:95]\n"
      "buffer_load_dwordx4 v8, s[4:7], s54 offen offset:0 nt lds\n"
      "buffer_load_dwordx4 v9, s[4:7], s54 offen offset:1024 nt lds\n"
      "v_mfma_f32_16x16x32_bf16 v[96:99], v[136:139], a[196:199], v[96:99]\n"
      "s_mov_b32 m0, s64\n"
      "v_cvt_pk_bf16_f32 v44, v44, v45\n"
      "v_cvt_pk_bf16_f32 v46, v46, v47\n"
      "v_mfma_f32_16x16x32_bf16 v[88:91], v[128:131], a[200:203], v[88:91]\n"
      "s_nop 0\n"
      "ds_read_b128 a[192:195], v3 offset:0\n"
      "ds_write_addtid_b32 v32 offset:41988\n"
      "v_mfma_f32_16x16x32_bf16 v[92:95], v[120:123], a[200:203], v[92:95]\n"
      "ds_write_addtid_b32 v34 offset:42500\n"
      "ds_write_addtid_b32 v44 offset:44036\n"
      "v_fmac_f32 v48, s46, v36\n"
      "v_mfma_f32_16x16x32_bf16 v[96:99], v[112:115], a[200:203], v[96:99]\n"
      "s_waitcnt lgkmcnt(4)\n"
      "ds_read2st64_b32 a[236:237], v11 offset0:0 offset1:2\n"
      "ds_write_addtid_b32 v46 offset:44548\n"
      "v_fmac_f32 v49, s46, v37\n"
      "v_fmac_f32 v50, s46, v38\n"
      "v_mfma_f32_16x16x32_bf16 v[88:91], v[104:107], a[204:207], v[88:91]\n"
      "ds_read_b128 a[196:199], v3 offset:64\n"
      "ds_read2st64_b32 a[238:239], v14 offset0:0 offset1:2\n"
      "v_mfma_f32_16x16x32_bf16 v[92:95], a[164:167], a[204:207], v[92:95]\n"
      "v_fmac_f32 v51, s46, v39\n"
      "v_fma_f32 v36, 0.5, v36, v60\n"
      "v_mfma_f32_16x16x32_bf16 v[96:99], a[156:159], a[204:207], v[96:99]\n"
      "s_nop 0\n"
      "v_fma_f32 v37, 0.5, v37, v61\n"
      "v_fma_f32 v38, 0.5, v38, v62\n"
      "v_mfma_f32_16x16x32_bf16 v[88:91], a[148:151], a[208:211], v[88:91]\n"
      "ds_read_b128 a[200:203], v3 offset:128\n"
      "v_fma_f32 v39, 0.5, v39, v63\n"
      "v_mfma_f32_16x16x32_bf16 v[92:95], a[140:143], a[208:211], v[92:95]\n"
      "v_fmac_f32 v48, s48, v72\n"
      "v_fmac_f32 v49, s48, v73\n"
      "v_mfma_f32_16x16x32_bf16 v[96:99], a[132:135], a[208:211], v[96:99]\n"
      "s_waitcnt lgkmcnt(1)\n"
      "v_fmac_f32 v50, s48, v74\n"
      "v_fmac_f32 v51, s48, v75\n"
      "v_mfma_f32_16x16x32_bf16 v[88:91], a[124:127], a[212:215], v[88:91]\n"
      "s_add_u32 s54, s54, 196608\n"
      "ds_read_b128 a[204:207], v4 offset:0\n"
      "ds_read2st64_b32 a[240:241], v12 offset0:0 offset1:2\n"
      "v_mfma_f32_16x16x32_bf16 v[92:95], a[116:119], a[212:215], v[92:95]\n"
      "s_add_u32 m0, s37, 90760\n"
      "ds_read2st64_b32 a[242:243], v15 offset0:0 offset1:2\n"
      "v_fmac_f32 v36, s45, v48\n"
      "v_mfma_f32_16x16x32_bf16 v[96:99], a[108:111], a[212:215], v[96:99]\n"
      "buffer_store_dwordx4 a[236:239], v16, s[8:11], s59 offen offset:128 sc1\n"
      "buffer_load_dwordx4 v8, s[4:7], s54 offen offset:0 nt lds\n"
      "buffer_load_dwordx4 v9, s[4:7], s54 offen offset:1024 nt lds\n"
      "v_mfma_f32_16x16x32_bf16 v[52:55], a[0:3], a[192:195], 0\n"
      "s_mov_b32 m0, s64\n"
      "ds_read_b128 a[208:211], v4 offset:64\n"
      "ds_write_addtid_b32 v88 offset:45312\n"
      "v_fmac_f32 v37, s45, v49\n"
      "v_mfma_f32_16x16x32_bf16 v[56:59], a[4:7], a[192:195], 0\n"
      "ds_write_addtid_b32 v89 offset:45824\n"
      "ds_write_addtid_b32 v90 offset:46336\n"
      "v_fmac_f32 v38, s45, v50\n"
      "v_mfma_f32_16x16x32_bf16 v[60:63], a[8:11], a[192:195], 0\n"
      "ds_write_addtid_b32 v91 offset:46848\n"
      "ds_write_addtid_b32 v92 offset:47360\n"
      "v_mfma_f32_16x16x32_bf16 v[52:55], a[12:15], a[196:199], v[52:55]\n"
      "s_nop 0\n"
      "ds_read_b128 a[212:215], v4 offset:128\n"
      "ds_write_addtid_b32 v93 offset:47872\n"
      "v_fmac_f32 v39, s45, v51\n"
      "v_mfma_f32_16x16x32_bf16 v[56:59], a[16:19], a[196:199], v[56:59]\n"
      "ds_write_addtid_b32 v94 offset:48384\n"
      "ds_write_addtid_b32 v95 offset:48896\n"
      "v_mfma_f32_16x16x32_bf16 v[60:63], a[20:23], a[196:199], v[60:63]\n"
      "s_waitcnt lgkmcnt(7)\n"
      "ds_write_addtid_b32 v96 offset:49408\n"
      "ds_write_addtid_b32 v97 offset:49920\n"
      "v_mfma_f32_16x16x32_bf16 v[52:55], a[24:27], a[200:203], v[52:55]\n"
      "ds_read_b128 a[192:195], v5 offset:0\n"
      "ds_write_addtid_b32 v98 offset:50432\n"
      "v_mfma_f32_16x16x32_bf16 v[56:59], a[28:31], a[200:203], v[56:59]\n"
      "buffer_store_dwordx4 a[240:243], v16, s[8:11], s59 offen offset:256 sc1\n"
      "v_mfma_f32_16x16x32_bf16 v[60:63], a[32:35], a[200:203], v[60:63]\n"
      "ds_write_addtid_b32 v99 offset:50944\n"
      "v_fma_f32 v48, s47, v48, -v72\n"
      "v_fma_f32 v49, s47, v49, -v73\n"
      "v_mfma_f32_16x16x32_bf16 v[52:55], a[36:39], a[204:207], v[52:55]\n"
      "ds_read_b128 a[196:199], v5 offset:64\n"
      "v_fma_f32 v50, s47, v50, -v74\n"
      "v_mfma_f32_16x16x32_bf16 v[56:59], a[40:43], a[204:207], v[56:59]\n"
      "v_fma_f32 v51, s47, v51, -v75\n"
      "v_cvt_pk_bf16_f32 v36, v36, v37\n"
      "v_mfma_f32_16x16x32_bf16 v[60:63], a[44:47], a[204:207], v[60:63]\n"
      "ds_write_addtid_b32 v36 offset:37888\n"
      "v_cvt_pk_bf16_f32 v38, v38, v39\n"
      "v_cvt_pk_bf16_f32 v48, v48, v49\n"
      "v_cvt_pk_bf16_f32 v50, v50, v51\n"
      "v_mfma_f32_16x16x32_bf16 v[52:55], a[48:51], a[208:211], v[52:55]\n"
      "ds_read_b128 a[200:203], v5 offset:128\n"
      "ds_write_addtid_b32 v38 offset:38400\n"
      "v_mfma_f32_16x16x32_bf16 v[56:59], a[52:55], a[208:211], v[56:59]\n"
      "ds_write_addtid_b32 v48 offset:39936\n"
      "ds_write_addtid_b32 v50 offset:40448\n"
      "v_mfma_f32_16x16x32_bf16 v[60:63], a[56:59], a[208:211], v[60:63]\n"
      "s_waitcnt lgkmcnt(5)\n"
      "v_fma_f32 v40, 2.0, v76, -v88\n"
      "v_fma_f32 v41, 2.0, v77, -v89\n"
      "v_mfma_f32_16x16x32_bf16 v[52:55], a[60:63], a[212:215], v[52:55]\n"
      "s_nop 0\n"
      "v_fma_f32 v42, 2.0, v78, -v90\n"
      "v_fma_f32 v43, 2.0, v79, -v91\n"
      "v_mfma_f32_16x16x32_bf16 v[56:59], a[64:67], a[212:215], v[56:59]\n"
      "s_waitcnt vmcnt(25)\n"
      "v_fma_f32 v44, 2.0, v80, -v92\n"
      "v_fma_f32 v45, 2.0, v81, -v93\n"
      "v_mfma_f32_16x16x32_bf16 v[60:63], a[68:71], a[212:215], v[60:63]\n"
      "s_waitcnt lgkmcnt(0)\n"
      "s_barrier\n"
      "s_add_u32 s55, s55, 2\n"
      "s_cmp_lt_u32 s55, 6\n"
      "s_cbranch_scc1 LDloop_%=\n"
      "LDlast_%=:\n"
      "v_mfma_f32_16x16x32_bf16 v[52:55], a[72:75], a[192:195], v[52:55]\n"
      "s_add_u32 s58, s40, 0\n"
      "ds_read_b128 a[204:207], v0 offset:16384\n"
      "ds_read2st64_b32 v[28:29], v6 offset0:0 offset1:2\n"
      "v_fma_f32 v46, 2.0, v82, -v94\n"
      "v_mfma_f32_16x16x32_bf16 v[56:59], a[76:79], a[192:195], v[56:59]\n"
      "s_mov_b32 s40, s62\n"
      "ds_read_b128 a[208:211], v0 offset:16448\n"
      "ds_read2st64_b32 v[30:31], v6 offset0:4 offset1:6\n"
      "v_mfma_f32_16x16x32_bf16 v[60:63], a[80:83], a[192:195], v[60:63]\n"
      "s_add_u32 s62, s62, 786432\n"
      "ds_read2st64_b32 v[32:33], v6 offset0:8 offset1:10\n"
      "ds_read2st64_b32 v[34:35], v6 offset0:12 offset1:14\n"
      "v_mfma_f32_16x16x32_bf16 v[52:55], a[84:87], a[196:199], v[52:55]\n"
      "s_add_u32 s59, s58, 196608\n"
      "ds_read_b128 a[212:215], v0 offset:16512\n"
      "v_fma_f32 v47, 2.0, v83, -v95\n"
      "v_mfma_f32_16x16x32_bf16 v[56:59], a[88:91], a[196:199], v[56:59]\n"
      "ds_read2st64_b32 a[220:221], v10 offset0:181 offset1:183\n"
      "v_fma_f32 v48, 2.0, v84, -v96\n"
      "v_mfma_f32_16x16x32_bf16 v[60:63], a[92:95], a[196:199], v[60:63]\n"
      "v_fma_f32 v49, 2.0, v85, -v97\n"
      "v_fma_f32 v50, 2.0, v86, -v98\n"
      "v_mfma_f32_16x16x32_bf16 v[52:55], a[96:99], a[200:203], v[52:55]\n"
      "ds_read_b128 a[192:195], v1 offset:16384\n"
      "ds_read2st64_b32 a[222:223], v13 offset0:181 offset1:183\n"
      "v_mfma_f32_16x16x32_bf16 v[56:59], a[100:103], a[200:203], v[56:59]\n"
      "ds_read2st64_b32 v[36:37], v6 offset0:16 offset1:18\n"
      "v_fma_f32 v51, 2.0, v87, -v99\n"
      "v_mfma_f32_16x16x32_bf16 v[60:63], a[104:107], a[200:203], v[60:63]\n"
      "s_waitcnt lgkmcnt(7)\n"
      "v_fmac_f32 v40, s46, v28\n"
      "v_fmac_f32 v41, s46, v29\n"
      "v_fmac_f32 v42, s46, v30\n"
      "v_mfma_f32_16x16x32_bf16 v[76:79], v[252:255], a[204:207], 0\n"
      "ds_read_b128 a[196:199], v1 offset:16448\n"
      "ds_read2st64_b32 v[38:39], v6 offset0:20 offset1:22\n"
      "v_mfma_f32_16x16x32_bf16 v[80:83], v[244:247], a[204:207], 0\n"
      "v_fmac_f32 v43, s46, v31\n"
      "v_fma_f32 v28, 0.5, v28, v64\n"
      "v_mfma_f32_16x16x32_bf16 v[84:87], v[236:239], a[204:207], 0\n"
      "s_nop 0\n"
      "v_fma_f32 v29, 0.5, v29, v65\n"
      "v_fma_f32 v30, 0.5, v30, v66\n"
      "v_mfma_f32_16x16x32_bf16 v[76:79], v[228:231], a[208:211], v[76:79]\n"
      "s_waitcnt lgkmcnt(3)\n"
      "ds_read_b128 a[200:203], v1 offset:16512\n"
      "v_fma_f32 v31, 0.5, v31, v67\n"
      "v_mfma_f32_16x16x32_bf16 v[80:83], v[220:223], a[208:211], v[80:83]\n"
      "buffer_store_dwordx4 a[220:223], v16, s[8:11], s58 offen offset:0 sc1\n"
      "v_fmac_f32 v40, s48, v52\n"
      "v_mfma_f32_16x16x32_bf16 v[84:87], v[212:215], a[208:211], v[84:87]\n"
      "ds_read2st64_b32 a[224:225], v11 offset0:181 offset1:183\n"
      "ds_read2st64_b32 a[226:227], v14 offset0:181 offset1:183\n"
      "v_mfma_f32_16x16x32_bf16 v[76:79], v[204:207], a[212:215], v[76:79]\n"
      "ds_read_b128 a[204:207], v2 offset:16384\n"
      "v_fmac_f32 v41, s48, v53\n"
      "v_mfma_f32_16x16x32_bf16 v[80:83], v[196:199], a[212:215], v[80:83]\n"
      "s_nop 0\n"
      "ds_read_b64_tr_b16 a[244:245], v7 offset:36352\n"
      "ds_read_b64_tr_b16 a[246:247], v7 offset:36360\n"
      "v_mfma_f32_16x16x32_bf16 v[84:87], v[188:191], a[212:215], v[84:87]\n"
      "v_fmac_f32 v42, s48, v54\n"
      "v_fmac_f32 v43, s48, v55\n"
      "v_mfma_f32_16x16x32_bf16 v[76:79], v[180:183], a[192:195], v[76:79]\n"
      "ds_read_b128 a[208:211], v2 offset:16448\n"
      "v_fmac_f32 v28, s45, v40\n"
      "v_mfma_f32_16x16x32_bf16 v[80:83], v[172:175], a[192:195], v[80:83]\n"
      "s_nop 0\n"
      "v_fmac_f32_e64 v29, s45, v41\n"
      "v_fmac_f32_e64 v30, s45, v42\n"
      "v_mfma_f32_16x16x32_bf16 v[84:87], v[164:167], a[192:195], v[84:87]\n"
      "s_waitcnt lgkmcnt(4)\n"
      "v_fmac_f32_e64 v31, s45, v43\n"
      "v_fma_f32 v40, s47, v40, -v52\n"
      "v_mfma_f32_16x16x32_bf16 v[76:79], v[156:159], a[196:199], v[76:79]\n"
      "s_nop 0\n"
      "ds_read_b128 a[212:215], v2 offset:16512\n"
      "v_fma_f32 v41, s47, v41, -v53\n"
      "v_mfma_f32_16x16x32_bf16 v[80:83], v[148:151], a[196:199], v[80:83]\n"
      "s_nop 0\n"
      "v_fma_f32 v42, s47, v42, -v54\n"
      "v_fma_f32 v43, s47, v43, -v55\n"
      "v_mfma_f32_16x16x32_bf16 v[84:87], v[140:143], a[196:199], v[84:87]\n"
      "s_waitcnt lgkmcnt(1)\n"
      "v_cvt_pk_bf16_f32 v28, v28, v29\n"
      "v_cvt_pk_bf16_f32 v30, v30, v31\n"
      "v_mfma_f32_4x4x4_16b_bf16 v[20:23], a[216:217], a[244:245], 0\n"
      "v_cvt_pk_bf16_f32 v40, v40, v41\n"
      "v_mfma_f32_4x4x4_16b_bf16 v[24:27], a[216:217], a[246:247], 0\n"
      "ds_write_addtid_b32 v28 offset:32768\n"
      "ds_write_addtid_b32 v30 offset:33280\n"
      "v_mfma_f32_16x16x32_bf16 v[76:79], v[132:135], a[200:203], v[76:79]\n"
      "buffer_store_dwordx4 a[224:227], v16, s[8:11], s58 offen offset:128 sc1\n"
      "ds_read_b128 a[192:195], v0 offset:20480\n"
      "v_mfma_f32_16x16x32_bf16 v[80:83], v[124:127], a[200:203], v[80:83]\n"
      "ds_write_addtid_b32 v40 offset:34816\n"
      "v_cvt_pk_bf16_f32 v42, v42, v43\n"
      "v_cvt_pk_bf16_f32 v20, v20, v24\n"
      "v_mfma_f32_16x16x32_bf16 v[84:87], v[116:119], a[200:203], v[84:87]\n"
      "ds_write_addtid_b32 v20 offset:2560\n"
      "ds_write_addtid_b32 v42 offset:35328\n"
      "v_fmac_f32 v44, s46, v32\n"
      "v_mfma_f32_16x16x32_bf16 v[76:79], v[108:111], a[204:207], v[76:79]\n"
      "ds_read_b128 a[196:199], v0 offset:20544\n"
      "v_fmac_f32 v45, s46, v33\n"
      "v_mfma_f32_16x16x32_bf16 v[80:83], v[100:103], a[204:207], v[80:83]\n"
      "ds_read2st64_b32 a[228:229], v12 offset0:181 offset1:183\n"
      "ds_read2st64_b32 a[230:231], v15 offset0:181 offset1:183\n"
      "v_cvt_pk_bf16_f32 v21, v21, v25\n"
      "v_mfma_f32_16x16x32_bf16 v[84:87], a[160:163], a[204:207], v[84:87]\n"
      "ds_write_addtid_b32 v21 offset:6656\n"
      "v_fmac_f32 v46, s46, v34\n"
      "v_mfma_f32_16x16x32_bf16 v[76:79], a[152:155], a[208:211], v[76:79]\n"
      "s_nop 0\n"
      "ds_read_b128 a[200:203], v0 offset:20608\n"
      "v_fmac_f32 v47, s46, v35\n"
      "v_mfma_f32_16x16x32_bf16 v[80:83], a[144:147], a[208:211], v[80:83]\n"
      "v_cvt_pk_bf16_f32 v22, v22, v26\n"
      "v_fma_f32 v32, 0.5, v32, v68\n"
      "v_mfma_f32_16x16x32_bf16 v[84:87], a[136:139], a[208:211], v[84:87]\n"
      "s_waitcnt lgkmcnt(4)\n"
      "ds_write_addtid_b32 v22 offset:10752\n"
      "ds_read_b64_tr_b16 a[248:249], v7 offset:36864\n"
      "v_mfma_f32_16x16x32_bf16 v[76:79], a[128:131], a[212:215], v[76:79]\n"
      "ds_read_b128 a[204:207], v1 offset:20480\n"
      "ds_read_b64_tr_b16 a[250:251], v7 offset:36872\n"
      "v_mfma_f32_16x16x32_bf16 v[80:83], a[120:123], a[212:215], v[80:83]\n"
      "v_fma_f32 v33, 0.5, v33, v69\n"
      "v_cvt_pk_bf16_f32 v23, v23, v27\n"
      "v_fma_f32 v34, 0.5, v34, v70\n"
      "v_mfma_f32_16x16x32_bf16 v[84:87], a[112:115], a[212:215], v[84:87]\n"
      "ds_write_addtid_b32 v23 offset:14848\n"
      "v_fma_f32 v35, 0.5, v35, v71\n"
      "v_mfma_f32_16x16x32_bf16 v[88:91], v[248:251], a[192:195], v[76:79]\n"
      "ds_read_b128 a[208:211], v1 offset:20544\n"
      "v_fmac_f32 v44, s48, v56\n"
      "v_mfma_f32_16x16x32_bf16 v[92:95], v[240:243], a[192:195], v[80:83]\n"
      "s_waitcnt lgkmcnt(4)\n"
      "v_fmac_f32 v45, s48, v57\n"
      "v_fmac_f32 v46, s48, v58\n"
      "v_mfma_f32_16x16x32_bf16 v[96:99], v[232:235], a[192:195], v[84:87]\n"
      "buffer_store_dwordx4 a[228:231], v16, s[8:11], s58 offen offset:256 sc1\n"
      "v_mfma_f32_16x16x32_bf16 v[88:91], v[224:227], a[196:199], v[88:91]\n"
      "s_waitcnt lgkmcnt(1)\n"
      "ds_read_b128 a[212:215], v1 offset:20608\n"
      "v_fmac_f32 v47, s48, v59\n"
      "v_mfma_f32_4x4x4_16b_bf16 v[20:23], a[216:217], a[248:249], 0\n"
      "v_fmac_f32 v32, s45, v44\n"
      "v_mfma_f32_4x4x4_16b_bf16 v[24:27], a[216:217], a[250:251], 0\n"
      "v_fmac_f32 v33, s45, v45\n"
      "v_fmac_f32_e64 v34, s45, v46\n"
      "v_mfma_f32_16x16x32_bf16 v[92:95], v[216:219], a[196:199], v[92:95]\n"
      "v_fmac_f32 v35, s45, v47\n"
      "v_fma_f32 v44, s47, v44, -v56\n"
      "v_cvt_pk_bf16_f32 v20, v20, v24\n"
      "v_mfma_f32_16x16x32_bf16 v[96:99], v[208:211], a[196:199], v[96:99]\n"
      "ds_write_addtid_b32 v20 offset:3072\n"
      "v_cvt_pk_bf16_f32 v21, v21, v25\n"
      "v_mfma_f32_16x16x32_bf16 v[88:91], v[200:203], a[200:203], v[88:91]\n"
      "s_nop 0\n"
      "ds_read_b128 a[192:195], v2 offset:20480\n"
      "ds_write_addtid_b32 v21 offset:7168\n"
      "v_mfma_f32_16x16x32_bf16 v[92:95], v[192:195], a[200:203], v[92:95]\n"
      "v_fma_f32 v45, s47, v45, -v57\n"
      "v_fma_f32 v46, s47, v46, -v58\n"
      "v_mfma_f32_16x16x32_bf16 v[96:99], v[184:187], a[200:203], v[96:99]\n"
      "v_fma_f32 v47, s47, v47, -v59\n"
      "v_cvt_pk_bf16_f32 v22, v22, v26\n"
      "v_mfma_f32_16x16x32_bf16 v[88:91], v[176:179], a[204:207], v[88:91]\n"
      "ds_read_b128 a[196:199], v2 offset:20544\n"
      "ds_write_addtid_b32 v22 offset:11264\n"
      "v_mfma_f32_16x16x32_bf16 v[92:95], v[168:171], a[204:207], v[92:95]\n"
      "s_nop 0\n"
      "v_cvt_pk_bf16_f32 v32, v32, v33\n"
      "v_cvt_pk_bf16_f32 v34, v34, v35\n"
      "v_mfma_f32_16x16x32_bf16 v[96:99], v[160:163], a[204:207], v[96:99]\n"
      "s_waitcnt lgkmcnt(3)\n"
      "ds_write_addtid_b32 v32 offset:40964\n"
      "ds_write_addtid_b32 v34 offset:41476\n"
      "v_cvt_pk_bf16_f32 v44, v44, v45\n"
      "v_mfma_f32_16x16x32_bf16 v[88:91], v[152:155], a[208:211], v[88:91]\n"
      "ds_read_b128 a[200:203], v2 offset:20608\n"
      "ds_write_addtid_b32 v44 offset:43012\n"
      "v_mfma_f32_16x16x32_bf16 v[92:95], v[144:147], a[208:211], v[92:95]\n"
      "v_cvt_pk_bf16_f32 v46, v46, v47\n"
      "v_cvt_pk_bf16_f32 v23, v23, v27\n"
      "v_mfma_f32_16x16x32_bf16 v[96:99], v[136:139], a[208:211], v[96:99]\n"
      "ds_write_addtid_b32 v46 offset:43524\n"
      "ds_write_addtid_b32 v23 offset:15360\n"
      "v_fmac_f32 v48, s46, v36\n"
      "v_mfma_f32_16x16x32_bf16 v[88:91], v[128:131], a[212:215], v[88:91]\n"
      "ds_read_b128 a[204:207], v3 offset:16896\n"
      "v_fmac_f32 v49, s46, v37\n"
      "v_mfma_f32_16x16x32_bf16 v[92:95], v[120:123], a[212:215], v[92:95]\n"
      "ds_read2st64_b32 a[232:233], v10 offset0:4 offset1:6\n"
      "ds_read2st64_b32 a[234:235], v13 offset0:4 offset1:6\n"
      "v_mfma_f32_16x16x32_bf16 v[96:99], v[112:115], a[212:215], v[96:99]\n"
      "v_fmac_f32 v50, s46, v38\n"
      "v_fmac_f32 v51, s46, v39\n"
      "v_mfma_f32_16x16x32_bf16 v[88:91], v[104:107], a[192:195], v[88:91]\n"
      "ds_read_b128 a[208:211], v3 offset:16960\n"
      "ds_read_b64_tr_b16 a[244:245], v17 offset:37376\n"
      "v_mfma_f32_16x16x32_bf16 v[92:95], a[164:167], a[192:195], v[92:95]\n"
      "ds_read_b64_tr_b16 a[246:247], v17 offset:37384\n"
      "v_fma_f32 v36, 0.5, v36, v72\n"
      "v_fma_f32 v37, 0.5, v37, v73\n"
      "v_mfma_f32_16x16x32_bf16 v[96:99], a[156:159], a[192:195], v[96:99]\n"
      "s_waitcnt lgkmcnt(5)\n"
      "v_fma_f32 v38, 0.5, v38, v74\n"
      "v_fma_f32 v39, 0.5, v39, v75\n"
      "v_mfma_f32_16x16x32_bf16 v[88:91], a[148:151], a[196:199], v[88:91]\n"
      "s_nop 0\n"
      "ds_read_b128 a[212:215], v3 offset:17024\n"
      "v_fmac_f32 v48, s48, v60\n"
      "v_mfma_f32_16x16x32_bf16 v[92:95], a[140:143], a[196:199], v[92:95]\n"
      "v_fmac_f32 v49, s48, v61\n"
      "v_fmac_f32 v50, s48, v62\n"
      "v_mfma_f32_16x16x32_bf16 v[96:99], a[132:135], a[196:199], v[96:99]\n"
      "v_fmac_f32 v51, s48, v63\n"
      "v_fmac_f32 v36, s45, v48\n"
      "v_mfma_f32_16x16x32_bf16 v[88:91], a[124:127], a[200:203], v[88:91]\n"
      "s_nop 0\n"
      "ds_read_b128 a[192:195], v4 offset:16896\n"
      "ds_read2st64_b32 a[236:237], v11 offset0:4 offset1:6\n"
      "v_mfma_f32_16x16x32_bf16 v[92:95], a[116:119], a[200:203], v[92:95]\n"
      "s_waitcnt lgkmcnt(3)\n"
      "ds_read2st64_b32 a[238:239], v14 offset0:4 offset1:6\n"
      "v_fmac_f32 v37, s45, v49\n"
      "v_mfma_f32_4x4x4_16b_bf16 v[20:23], a[218:219], a[244:245], 0\n"
      "v_fmac_f32 v38, s45, v50\n"
      "v_mfma_f32_4x4x4_16b_bf16 v[24:27], a[218:219], a[246:247], 0\n"
      "v_fmac_f32 v39, s45, v51\n"
      "v_fma_f32 v48, s47, v48, -v60\n"
      "v_mfma_f32_16x16x32_bf16 v[96:99], a[108:111], a[200:203], v[96:99]\n"
      "buffer_store_dwordx4 a[232:235], v16, s[8:11], s59 offen offset:0 sc1\n"
      "v_mfma_f32_16x16x32_bf16 v[64:67], a[0:3], a[204:207], 0\n"
      "ds_read_b128 a[196:199], v4 offset:16960\n"
      "ds_write_addtid_b32 v88 offset:51456\n"
      "v_cvt_pk_bf16_f32 v20, v20, v24\n"
      "v_mfma_f32_16x16x32_bf16 v[68:71], a[4:7], a[204:207], 0\n"
      "ds_write_addtid_b32 v89 offset:51968\n"
      "ds_write_addtid_b32 v90 offset:52480\n"
      "v_mfma_f32_16x16x32_bf16 v[72:75], a[8:11], a[204:207], 0\n"
      "s_mov_b32 m0, s41\n"
      "v_cvt_pk_bf16_f32 v21, v21, v25\n"
      "v_fma_f32 v49, s47, v49, -v61\n"
      "v_mfma_f32_16x16x32_bf16 v[64:67], a[12:15], a[208:211], v[64:67]\n"
      "s_nop 0\n"
      "ds_read_b128 a[200:203], v4 offset:17024\n"
      "ds_write_addtid_b32 v20 offset:3584\n"
      "ds_write_addtid_b32 v21 offset:7680\n"
      "v_mfma_f32_16x16x32_bf16 v[68:71], a[16:19], a[208:211], v[68:71]\n"
      "s_mov_b32 m0, s64\n"
      "v_fma_f32 v50, s47, v50, -v62\n"
      "v_fma_f32 v51, s47, v51, -v63\n"
      "v_mfma_f32_16x16x32_bf16 v[72:75], a[20:23], a[208:211], v[72:75]\n"
      "s_waitcnt lgkmcnt(4)\n"
      "ds_write_addtid_b32 v91 offset:52992\n"
      "ds_write_addtid_b32 v92 offset:53504\n"
      "v_mfma_f32_16x16x32_bf16 v[64:67], a[24:27], a[212:215], v[64:67]\n"
      "ds_read_b128 a[204:207], v5 offset:16896\n"
      "ds_write_addtid_b32 v93 offset:54016\n"
      "v_mfma_f32_16x16x32_bf16 v[68:71], a[28:31], a[212:215], v[68:71]\n"
      "buffer_store_dwordx4 a[236:239], v16, s[8:11], s59 offen offset:128 sc1\n"
      "ds_write_addtid_b32 v94 offset:54528\n"
      "v_mfma_f32_16x16x32_bf16 v[72:75], a[32:35], a[212:215], v[72:75]\n"
      "ds_write_addtid_b32 v95 offset:55040\n"
      "ds_write_addtid_b32 v96 offset:55552\n"
      "v_cvt_pk_bf16_f32 v36, v36, v37\n"
      "v_mfma_f32_16x16x32_bf16 v[64:67], a[36:39], a[192:195], v[64:67]\n"
      "ds_read_b128 a[208:211], v5 offset:16960\n"
      "ds_write_addtid_b32 v36 offset:36864\n"
      "v_mfma_f32_16x16x32_bf16 v[68:71], a[40:43], a[192:195], v[68:71]\n"
      "ds_write_addtid_b32 v97 offset:56064\n"
      "ds_write_addtid_b32 v98 offset:56576\n"
      "v_mfma_f32_16x16x32_bf16 v[72:75], a[44:47], a[192:195], v[72:75]\n"
      "ds_write_addtid_b32 v99 offset:57088\n"
      "v_cvt_pk_bf16_f32 v38, v38, v39\n"
      "v_cvt_pk_bf16_f32 v48, v48, v49\n"
      "v_cvt_pk_bf16_f32 v50, v50, v51\n"
      "v_mfma_f32_16x16x32_bf16 v[64:67], a[48:51], a[196:199], v[64:67]\n"
      "ds_read_b128 a[212:215], v5 offset:17024\n"
      "ds_write_addtid_b32 v38 offset:37376\n"
      "v_mfma_f32_16x16x32_bf16 v[68:71], a[52:55], a[196:199], v[68:71]\n"
      "s_nop 0\n"
      "ds_write_addtid_b32 v48 offset:38912\n"
      "ds_write_addtid_b32 v50 offset:39424\n"
      "v_mfma_f32_16x16x32_bf16 v[72:75], a[56:59], a[196:199], v[72:75]\n"
      "s_waitcnt lgkmcnt(7)\n"
      "v_fma_f32 v40, 2.0, v76, -v88\n"
      "v_fma_f32 v41, 2.0, v77, -v89\n"
      "v_mfma_f32_16x16x32_bf16 v[64:67], a[60:63], a[200:203], v[64:67]\n"
      "s_nop 0\n"
      "v_fma_f32 v42, 2.0, v78, -v90\n"
      "v_fma_f32 v43, 2.0, v79, -v91\n"
      "v_mfma_f32_16x16x32_bf16 v[68:71], a[64:67], a[200:203], v[68:71]\n"
      "s_waitcnt vmcnt(16)\n"
      "v_fma_f32 v44, 2.0, v80, -v92\n"
      "v_fma_f32 v45, 2.0, v81, -v93\n"
      "v_mfma_f32_16x16x32_bf16 v[72:75], a[68:71], a[200:203], v[72:75]\n"
      "s_waitcnt lgkmcnt(0)\n"
      "s_barrier\n"
      "v_mfma_f32_16x16x32_bf16 v[64:67], a[72:75], a[204:207], v[64:67]\n"
      "s_add_u32 s58, s40, 0\n"
      "ds_read_b128 a[192:195], v0 offset:0\n"
      "ds_read2st64_b32 v[28:29], v6 offset0:24 offset1:26\n"
      "v_fma_f32 v46, 2.0, v82, -v94\n"
      "v_mfma_f32_16x16x32_bf16 v[68:71], a[76:79], a[204:207], v[68:71]\n"
      "s_mov_b32 s40, s62\n"
      "ds_read_b128 a[196:199], v0 offset:64\n"
      "ds_read2st64_b32 v[30:31], v6 offset0:28 offset1:30\n"
      "v_mfma_f32_16x16x32_bf16 v[72:75], a[80:83], a[204:207], v[72:75]\n"
      "s_add_u32 s62, s62, 786432\n"
      "ds_read2st64_b32 v[32:33], v6 offset0:32 offset1:34\n"
      "ds_read2st64_b32 v[34:35], v6 offset0:36 offset1:38\n"
      "v_mfma_f32_16x16x32_bf16 v[64:67], a[84:87], a[208:211], v[64:67]\n"
      "s_add_u32 s59, s58, 196608\n"
      "ds_read_b128 a[200:203], v0 offset:128\n"
      "v_fma_f32 v47, 2.0, v83, -v95\n"
      "v_mfma_f32_16x16x32_bf16 v[68:71], a[88:91], a[208:211], v[68:71]\n"
      "ds_read2st64_b32 a[220:221], v10 offset0:177 offset1:179\n"
      "v_fma_f32 v48, 2.0, v84, -v96\n"
      "v_mfma_f32_16x16x32_bf16 v[72:75], a[92:95], a[208:211], v[72:75]\n"
      "v_fma_f32 v49, 2.0, v85, -v97\n"
      "v_fma_f32 v50, 2.0, v86, -v98\n"
      "v_mfma_f32_16x16x32_bf16 v[64:67], a[96:99], a[212:215], v[64:67]\n"
      "ds_read_b128 a[204:207], v1 offset:0\n"
      "ds_read2st64_b32 a[222:223], v13 offset0:177 offset1:179\n"
      "v_mfma_f32_16x16x32_bf16 v[68:71], a[100:103], a[212:215], v[68:71]\n"
      "ds_read2st64_b32 v[36:37], v6 offset0:40 offset1:42\n"
      "v_fma_f32 v51, 2.0, v87, -v99\n"
      "v_mfma_f32_16x16x32_bf16 v[72:75], a[104:107], a[212:215], v[72:75]\n"
      "s_waitcnt lgkmcnt(7)\n"
      "v_fmac_f32 v40, s46, v28\n"
      "v_fmac_f32 v41, s46, v29\n"
      "v_fmac_f32 v42, s46, v30\n"
      "v_mfma_f32_16x16x32_bf16 v[76:79], v[252:255], a[192:195], 0\n"
      "ds_read_b128 a[208:211], v1 offset:64\n"
      "ds_read2st64_b32 v[38:39], v6 offset0:44 offset1:46\n"
      "v_mfma_f32_16x16x32_bf16 v[80:83], v[244:247], a[192:195], 0\n"
      "v_fmac_f32 v43, s46, v31\n"
      "v_fma_f32 v28, 0.5, v28, v52\n"
      "v_mfma_f32_16x16x32_bf16 v[84:87], v[236:239], a[192:195], 0\n"
      "s_nop 0\n"
      "v_fma_f32 v29, 0.5, v29, v53\n"
      "v_fma_f32 v30, 0.5, v30, v54\n"
      "v_mfma_f32_16x16x32_bf16 v[76:79], v[228:231], a[196:199], v[76:79]\n"
      "s_waitcnt lgkmcnt(3)\n"
      "ds_read_b128 a[212:215], v1 offset:128\n"
      "v_fma_f32 v31, 0.5, v31, v55\n"
      "v_mfma_f32_16x16x32_bf16 v[80:83], v[220:223], a[196:199], v[80:83]\n"
      "buffer_store_dwordx4 a[220:223], v16, s[8:11], s58 offen offset:0 sc1\n"
      "v_fmac_f32 v40, s48, v64\n"
      "v_mfma_f32_16x16x32_bf16 v[84:87], v[212:215], a[196:199], v[84:87]\n"
      "ds_read2st64_b32 a[224:225], v11 offset0:177 offset1:179\n"
      "ds_read2st64_b32 a[226:227], v14 offset0:177 offset1:179\n"
      "v_mfma_f32_16x16x32_bf16 v[76:79], v[204:207], a[200:203], v[76:79]\n"
      "ds_read_b128 a[192:195], v2 offset:0\n"
      "v_fmac_f32 v41, s48, v65\n"
      "v_mfma_f32_16x16x32_bf16 v[80:83], v[196:199], a[200:203], v[80:83]\n"
      "ds_read_b64_tr_b16 a[244:245], v7 offset:53248\n"
      "ds_read_b64_tr_b16 a[246:247], v7 offset:53256\n"
      "v_mfma_f32_16x16x32_bf16 v[84:87], v[188:191], a[200:203], v[84:87]\n"
      "v_fmac_f32 v42, s48, v66\n"
      "v_fmac_f32 v43, s48, v67\n"
      "v_mfma_f32_16x16x32_bf16 v[76:79], v[180:183], a[204:207], v[76:79]\n"
      "ds_read_b128 a[196:199], v2 offset:64\n"
      "v_fmac_f32 v28, s45, v40\n"
      "v_mfma_f32_16x16x32_bf16 v[80:83], v[172:175], a[204:207], v[80:83]\n"
      "v_fmac_f32 v29, s45, v41\n"
      "v_fmac_f32 v30, s45, v42\n"
      "v_mfma_f32_16x16x32_bf16 v[84:87], v[164:167], a[204:207], v[84:87]\n"
      "s_waitcnt lgkmcnt(4)\n"
      "v_fmac_f32 v31, s45, v43\n"
      "v_fma_f32 v40, s47, v40, -v64\n"
      "v_mfma_f32_16x16x32_bf16 v[76:79], v[156:159], a[208:211], v[76:79]\n"
      "ds_read_b128 a[200:203], v2 offset:128\n"
      "v_fma_f32 v41, s47, v41, -v65\n"
      "v_mfma_f32_16x16x32_bf16 v[80:83], v[148:151], a[208:211], v[80:83]\n"
      "s_nop 0\n"
      "v_fma_f32 v42, s47, v42, -v66\n"
      "v_fma_f32 v43, s47, v43, -v67\n"
      "v_mfma_f32_16x16x32_bf16 v[84:87], v[140:143], a[208:211], v[84:87]\n"
      "s_waitcnt lgkmcnt(1)\n"
      "v_cvt_pk_bf16_f32 v28, v28, v29\n"
      "v_cvt_pk_bf16_f32 v30, v30, v31\n"
      "v_mfma_f32_4x4x4_16b_bf16 v[20:23], a[216:217], a[244:245], 0\n"
      "v_cvt_pk_bf16_f32 v40, v40, v41\n"
      "v_mfma_f32_4x4x4_16b_bf16 v[24:27], a[216:217], a[246:247], 0\n"
      "ds_write_addtid_b32 v28 offset:33792\n"
      "ds_write_addtid_b32 v30 offset:34304\n"
      "v_mfma_f32_16x16x32_bf16 v[76:79], v[132:135], a[212:215], v[76:79]\n"
      "buffer_store_dwordx4 a[224:227], v16, s[8:11], s58 offen offset:128 sc1\n"
      "ds_read_b128 a[204:207], v0 offset:4096\n"
      "v_mfma_f32_16x16x32_bf16 v[80:83], v[124:127], a[212:215], v[80:83]\n"
      "ds_write_addtid_b32 v40 offset:35840\n"
      "v_cvt_pk_bf16_f32 v42, v42, v43\n"
      "v_cvt_pk_bf16_f32 v20, v20, v24\n"
      "v_mfma_f32_16x16x32_bf16 v[84:87], v[116:119], a[212:215], v[84:87]\n"
      "ds_write_addtid_b32 v20 offset:18944\n"
      "ds_write_addtid_b32 v42 offset:36352\n"
      "v_fmac_f32 v44, s46, v32\n"
      "v_mfma_f32_16x16x32_bf16 v[76:79], v[108:111], a[192:195], v[76:79]\n"
      "ds_read_b128 a[208:211], v0 offset:4160\n"
      "v_fmac_f32 v45, s46, v33\n"
      "v_mfma_f32_16x16x32_bf16 v[80:83], v[100:103], a[192:195], v[80:83]\n"
      "ds_read2st64_b32 a[228:229], v12 offset0:177 offset1:179\n"
      "ds_read2st64_b32 a[230:231], v15 offset0:177 offset1:179\n"
      "v_cvt_pk_bf16_f32 v21, v21, v25\n"
      "v_mfma_f32_16x16x32_bf16 v[84:87], a[160:163], a[192:195], v[84:87]\n"
      "ds_write_addtid_b32 v21 offset:23040\n"
      "v_fmac_f32 v46, s46, v34\n"
      "v_mfma_f32_16x16x32_bf16 v[76:79], a[152:155], a[196:199], v[76:79]\n"
      "ds_read_b128 a[212:215], v0 offset:4224\n"
      "v_fmac_f32 v47, s46, v35\n"
      "v_mfma_f32_16x16x32_bf16 v[80:83], a[144:147], a[196:199], v[80:83]\n"
      "v_cvt_pk_bf16_f32 v22, v22, v26\n"
      "v_fma_f32 v32, 0.5, v32, v56\n"
      "v_mfma_f32_16x16x32_bf16 v[84:87], a[136:139], a[196:199], v[84:87]\n"
      "s_waitcnt lgkmcnt(4)\n"
      "ds_write_addtid_b32 v22 offset:27136\n"
      "ds_read_b64_tr_b16 a[248:249], v7 offset:53760\n"
      "v_mfma_f32_16x16x32_bf16 v[76:79], a[128:131], a[200:203], v[76:79]\n"
      "ds_read_b128 a[192:195], v1 offset:4096\n"
      "ds_read_b64_tr_b16 a[250:251], v7 offset:53768\n"
      "v_mfma_f32_16x16x32_bf16 v[80:83], a[120:123], a[200:203], v[80:83]\n"
      "s_nop 0\n"
      "v_fma_f32 v33, 0.5, v33, v57\n"
      "v_cvt_pk_bf16_f32 v23, v23, v27\n"
      "v_fma_f32 v34, 0.5, v34, v58\n"
      "v_mfma_f32_16x16x32_bf16 v[84:87], a[112:115], a[200:203], v[84:87]\n"
      "ds_write_addtid_b32 v23 offset:31232\n"
      "v_fma_f32 v35, 0.5, v35, v59\n"
      "v_mfma_f32_16x16x32_bf16 v[88:91], v[248:251], a[204:207], v[76:79]\n"
      "ds_read_b128 a[196:199], v1 offset:4160\n"
      "v_fmac_f32 v44, s48, v68\n"
      "v_mfma_f32_16x16x32_bf16 v[92:95], v[240:243], a[204:207], v[80:83]\n"
      "s_waitcnt lgkmcnt(4)\n"
      "v_fmac_f32 v45, s48, v69\n"
      "v_fmac_f32 v46, s48, v70\n"
      "v_mfma_f32_16x16x32_bf16 v[96:99], v[232:235], a[204:207], v[84:87]\n"
      "buffer_store_dwordx4 a[228:231], v16, s[8:11], s58 offen offset:256 sc1\n"
      "v_mfma_f32_16x16x32_bf16 v[88:91], v[224:227], a[208:211], v[88:91]\n"
      "s_waitcnt lgkmcnt(1)\n"
      "ds_read_b128 a[200:203], v1 offset:4224\n"
      "v_fmac_f32 v47, s48, v71\n"
      "v_mfma_f32_4x4x4_16b_bf16 v[20:23], a[216:217], a[248:249], 0\n"
      "v_fmac_f32 v32, s45, v44\n"
      "v_mfma_f32_4x4x4_16b_bf16 v[24:27], a[216:217], a[250:251], 0\n"
      "v_fmac_f32 v33, s45, v45\n"
      "v_fmac_f32 v34, s45, v46\n"
      "v_mfma_f32_16x16x32_bf16 v[92:95], v[216:219], a[208:211], v[92:95]\n"
      "v_fmac_f32 v35, s45, v47\n"
      "v_fma_f32 v44, s47, v44, -v68\n"
      "v_cvt_pk_bf16_f32 v20, v20, v24\n"
      "v_mfma_f32_16x16x32_bf16 v[96:99], v[208:211], a[208:211], v[96:99]\n"
      "ds_write_addtid_b32 v20 offset:19456\n"
      "v_cvt_pk_bf16_f32 v21, v21, v25\n"
      "v_mfma_f32_16x16x32_bf16 v[88:91], v[200:203], a[212:215], v[88:91]\n"
      "ds_read_b128 a[204:207], v2 offset:4096\n"
      "ds_write_addtid_b32 v21 offset:23552\n"
      "v_mfma_f32_16x16x32_bf16 v[92:95], v[192:195], a[212:215], v[92:95]\n"
      "v_fma_f32 v45, s47, v45, -v69\n"
      "v_fma_f32 v46, s47, v46, -v70\n"
      "v_mfma_f32_16x16x32_bf16 v[96:99], v[184:187], a[212:215], v[96:99]\n"
      "v_fma_f32 v47, s47, v47, -v71\n"
      "v_cvt_pk_bf16_f32 v22, v22, v26\n"
      "v_mfma_f32_16x16x32_bf16 v[88:91], v[176:179], a[192:195], v[88:91]\n"
      "ds_read_b128 a[208:211], v2 offset:4160\n"
      "ds_write_addtid_b32 v22 offset:27648\n"
      "v_mfma_f32_16x16x32_bf16 v[92:95], v[168:171], a[192:195], v[92:95]\n"
      "v_cvt_pk_bf16_f32 v32, v32, v33\n"
      "v_cvt_pk_bf16_f32 v34, v34, v35\n"
      "v_mfma_f32_16x16x32_bf16 v[96:99], v[160:163], a[192:195], v[96:99]\n"
      "s_waitcnt lgkmcnt(3)\n"
      "ds_write_addtid_b32 v32 offset:41988\n"
      "ds_write_addtid_b32 v34 offset:42500\n"
      "v_cvt_pk_bf16_f32 v44, v44, v45\n"
      "v_mfma_f32_16x16x32_bf16 v[88:91], v[152:155], a[196:199], v[88:91]\n"
      "s_nop 0\n"
      "ds_read_b128 a[212:215], v2 offset:4224\n"
      "ds_write_addtid_b32 v44 offset:44036\n"
      "v_mfma_f32_16x16x32_bf16 v[92:95], v[144:147], a[196:199], v[92:95]\n"
      "v_cvt_pk_bf16_f32 v46, v46, v47\n"
      "v_cvt_pk_bf16_f32 v23, v23, v27\n"
      "v_mfma_f32_16x16x32_bf16 v[96:99], v[136:139], a[196:199], v[96:99]\n"
      "ds_write_addtid_b32 v46 offset:44548\n"
      "ds_write_addtid_b32 v23 offset:31744\n"
      "v_fmac_f32_e64 v48, s46, v36\n"
      "v_mfma_f32_16x16x32_bf16 v[88:91], v[128:131], a[200:203], v[88:91]\n"
      "ds_read_b128 a[192:195], v3 offset:33792\n"
      "v_fmac_f32 v49, s46, v37\n"
      "v_mfma_f32_16x16x32_bf16 v[92:95], v[120:123], a[200:203], v[92:95]\n"
      "ds_read2st64_b32 a[232:233], v10 offset0:0 offset1:2\n"
      "ds_read2st64_b32 a[234:235], v13 offset0:0 offset1:2\n"
      "v_mfma_f32_16x16x32_bf16 v[96:99], v[112:115], a[200:203], v[96:99]\n"
      "v_fmac_f32 v50, s46, v38\n"
      "v_fmac_f32_e64 v51, s46, v39\n"
      "v_mfma_f32_16x16x32_bf16 v[88:91], v[104:107], a[204:207], v[88:91]\n"
      "ds_read_b128 a[196:199], v3 offset:33856\n"
      "ds_read_b64_tr_b16 a[244:245], v17 offset:54272\n"
      "v_mfma_f32_16x16x32_bf16 v[92:95], a[164:167], a[204:207], v[92:95]\n"
      "ds_read_b64_tr_b16 a[246:247], v17 offset:54280\n"
      "v_fma_f32 v36, 0.5, v36, v60\n"
      "v_fma_f32 v37, 0.5, v37, v61\n"
      "v_mfma_f32_16x16x32_bf16 v[96:99], a[156:159], a[204:207], v[96:99]\n"
      "s_waitcnt lgkmcnt(5)\n"
      "v_fma_f32 v38, 0.5, v38, v62\n"
      "v_fma_f32 v39, 0.5, v39, v63\n"
      "v_mfma_f32_16x16x32_bf16 v[88:91], a[148:151], a[208:211], v[88:91]\n"
      "ds_read_b128 a[200:203], v3 offset:33920\n"
      "v_fmac_f32 v48, s48, v72\n"
      "v_mfma_f32_16x16x32_bf16 v[92:95], a[140:143], a[208:211], v[92:95]\n"
      "v_fmac_f32 v49, s48, v73\n"
      "v_fmac_f32 v50, s48, v74\n"
      "v_mfma_f32_16x16x32_bf16 v[96:99], a[132:135], a[208:211], v[96:99]\n"
      "v_fmac_f32 v51, s48, v75\n"
      "v_fmac_f32 v36, s45, v48\n"
      "v_mfma_f32_16x16x32_bf16 v[88:91], a[124:127], a[212:215], v[88:91]\n"
      "ds_read_b128 a[204:207], v4 offset:33792\n"
      "ds_read2st64_b32 a[236:237], v11 offset0:0 offset1:2\n"
      "v_mfma_f32_16x16x32_bf16 v[92:95], a[116:119], a[212:215], v[92:95]\n"
      "s_waitcnt lgkmcnt(3)\n"
      "ds_read2st64_b32 a[238:239], v14 offset0:0 offset1:2\n"
      "v_fmac_f32 v37, s45, v49\n"
      "v_mfma_f32_4x4x4_16b_bf16 v[20:23], a[218:219], a[244:245], 0\n"
      "v_fmac_f32 v38, s45, v50\n"
      "v_mfma_f32_4x4x4_16b_bf16 v[24:27], a[218:219], a[246:247], 0\n"
      "v_fmac_f32 v39, s45, v51\n"
      "v_fma_f32 v48, s47, v48, -v72\n"
      "v_mfma_f32_16x16x32_bf16 v[96:99], a[108:111], a[212:215], v[96:99]\n"
      "s_nop 0\n"
      "buffer_store_dwordx4 a[232:235], v16, s[8:11], s59 offen offset:0 sc1\n"
      "v_mfma_f32_16x16x32_bf16 v[52:55], a[0:3], a[192:195], 0\n"
      "s_nop 0\n"
      "ds_read_b128 a[208:211], v4 offset:33856\n"
      "ds_write_addtid_b32 v88 offset:45312\n"
      "v_cvt_pk_bf16_f32 v20, v20, v24\n"
      "v_mfma_f32_16x16x32_bf16 v[56:59], a[4:7], a[192:195], 0\n"
      "s_nop 0\n"
      "ds_write_addtid_b32 v89 offset:45824\n"
      "ds_write_addtid_b32 v90 offset:46336\n"
      "v_mfma_f32_16x16x32_bf16 v[60:63], a[8:11], a[192:195], 0\n"
      "s_mov_b32 m0, s41\n"
      "v_cvt_pk_bf16_f32 v21, v21, v25\n"
      "v_fma_f32 v49, s47, v49, -v73\n"
      "v_mfma_f32_16x16x32_bf16 v[52:55], a[12:15], a[196:199], v[52:55]\n"
      "s_nop 0\n"
      "ds_read_b128 a[212:215], v4 offset:33920\n"
      "ds_write_addtid_b32 v20 offset:19968\n"
      "ds_write_addtid_b32 v21 offset:24064\n"
      "v_mfma_f32_16x16x32_bf16 v[56:59], a[16:19], a[196:199], v[56:59]\n"
      "s_mov_b32 m0, s64\n"
      "v_fma_f32 v50, s47, v50, -v74\n"
      "v_fma_f32 v51, s47, v51, -v75\n"
      "v_mfma_f32_16x16x32_bf16 v[60:63], a[20:23], a[196:199], v[60:63]\n"
      "s_waitcnt lgkmcnt(4)\n"
      "ds_write_addtid_b32 v91 offset:46848\n"
      "ds_write_addtid_b32 v92 offset:47360\n"
      "v_mfma_f32_16x16x32_bf16 v[52:55], a[24:27], a[200:203], v[52:55]\n"
      "s_nop 0\n"
      "ds_read_b128 a[192:195], v5 offset:33792\n"
      "ds_write_addtid_b32 v93 offset:47872\n"
      "v_mfma_f32_16x16x32_bf16 v[56:59], a[28:31], a[200:203], v[56:59]\n"
      "s_nop 0\n"
      "buffer_store_dwordx4 a[236:239], v16, s[8:11], s59 offen offset:128 sc1\n"
      "ds_write_addtid_b32 v94 offset:48384\n"
      "v_mfma_f32_16x16x32_bf16 v[60:63], a[32:35], a[200:203], v[60:63]\n"
      "s_nop 0\n"
      "ds_write_addtid_b32 v95 offset:48896\n"
      "ds_write_addtid_b32 v96 offset:49408\n"
      "v_cvt_pk_bf16_f32 v36, v36, v37\n"
      "v_mfma_f32_16x16x32_bf16 v[52:55], a[36:39], a[204:207], v[52:55]\n"
      "s_nop 0\n"
      "ds_read_b128 a[196:199], v5 offset:33856\n"
      "ds_write_addtid_b32 v36 offset:37888\n"
      "v_mfma_f32_16x16x32_bf16 v[56:59], a[40:43], a[204:207], v[56:59]\n"
      "s_nop 0\n"
      "ds_write_addtid_b32 v97 offset:49920\n"
      "ds_write_addtid_b32 v98 offset:50432\n"
      "v_mfma_f32_16x16x32_bf16 v[60:63], a[44:47], a[204:207], v[60:63]\n"
      "s_nop 0\n"
      "ds_write_addtid_b32 v99 offset:50944\n"
      "v_cvt_pk_bf16_f32 v38, v38, v39\n"
      "v_cvt_pk_bf16_f32 v48, v48, v49\n"
      "v_cvt_pk_bf16_f32 v50, v50, v51\n"
      "v_mfma_f32_16x16x32_bf16 v[52:55], a[48:51], a[208:211], v[52:55]\n"
      "s_nop 0\n"
      "ds_read_b128 a[200:203], v5 offset:33920\n"
      "ds_write_addtid_b32 v38 offset:38400\n"
      "v_mfma_f32_16x16x32_bf16 v[56:59], a[52:55], a[208:211], v[56:59]\n"
      "s_nop 0\n"
      "ds_write_addtid_b32 v48 offset:39936\n"
      "ds_write_addtid_b32 v50 offset:40448\n"
      "v_mfma_f32_16x16x32_bf16 v[60:63], a[56:59], a[208:211], v[60:63]\n"
      "s_waitcnt lgkmcnt(7)\n"
      "v_fma_f32 v40, 2.0, v76, -v88\n"
      "v_fma_f32 v41, 2.0, v77, -v89\n"
      "v_mfma_f32_16x16x32_bf16 v[52:55], a[60:63], a[212:215], v[52:55]\n"
      "s_nop 0\n"
      "v_fma_f32 v42, 2.0, v78, -v90\n"
      "v_fma_f32 v43, 2.0, v79, -v91\n"
      "v_mfma_f32_16x16x32_bf16 v[56:59], a[64:67], a[212:215], v[56:59]\n"
      "s_waitcnt vmcnt(11)\n"
      "v_fma_f32 v44, 2.0, v80, -v92\n"
      "v_fma_f32 v45, 2.0, v81, -v93\n"
      "v_mfma_f32_16x16x32_bf16 v[60:63], a[68:71], a[212:215], v[60:63]\n"
      "s_waitcnt lgkmcnt(0)\n"
      "s_barrier\n"
      "v_mfma_f32_16x16x32_bf16 v[52:55], a[72:75], a[192:195], v[52:55]\n"
      "s_add_u32 s58, s40, 0\n"
      "ds_read_b128 a[204:207], v0 offset:16384\n"
      "ds_read2st64_b32 v[28:29], v6 offset0:0 offset1:2\n"
      "v_fma_f32 v46, 2.0, v82, -v94\n"
      "v_mfma_f32_16x16x32_bf16 v[56:59], a[76:79], a[192:195], v[56:59]\n"
      "s_mov_b32 s40, s62\n"
      "ds_read_b128 a[208:211], v0 offset:16448\n"
      "ds_read2st64_b32 v[30:31], v6 offset0:4 offset1:6\n"
      "v_mfma_f32_16x16x32_bf16 v[60:63], a[80:83], a[192:195], v[60:63]\n"
      "s_add_u32 s62, s62, 786432\n"
      "ds_read2st64_b32 v[32:33], v6 offset0:8 offset1:10\n"
      "ds_read2st64_b32 v[34:35], v6 offset0:12 offset1:14\n"
      "v_mfma_f32_16x16x32_bf16 v[52:55], a[84:87], a[196:199], v[52:55]\n"
      "ds_read_b128 a[212:215], v0 offset:16512\n"
      "v_fma_f32 v47, 2.0, v83, -v95\n"
      "v_mfma_f32_16x16x32_bf16 v[56:59], a[88:91], a[196:199], v[56:59]\n"
      "v_fma_f32 v48, 2.0, v84, -v96\n"
      "v_fma_f32 v49, 2.0, v85, -v97\n"
      "v_mfma_f32_16x16x32_bf16 v[60:63], a[92:95], a[196:199], v[60:63]\n"
      "ds_read2st64_b32 v[36:37], v6 offset0:16 offset1:18\n"
      "ds_read2st64_b32 v[38:39], v6 offset0:20 offset1:22\n"
      "v_mfma_f32_16x16x32_bf16 v[52:55], a[96:99], a[200:203], v[52:55]\n"
      "ds_read_b128 a[192:195], v1 offset:16384\n"
      "v_fma_f32 v50, 2.0, v86, -v98\n"
      "v_mfma_f32_16x16x32_bf16 v[56:59], a[100:103], a[200:203], v[56:59]\n"
      "v_fma_f32 v51, 2.0, v87, -v99\n"
      "v_mfma_f32_16x16x32_bf16 v[60:63], a[104:107], a[200:203], v[60:63]\n"
      "s_waitcnt lgkmcnt(6)\n"
      "v_fmac_f32 v40, s46, v28\n"
      "v_fmac_f32 v41, s46, v29\n"
      "v_fmac_f32 v42, s46, v30\n"
      "v_fmac_f32 v43, s46, v31\n"
      "v_mfma_f32_16x16x32_bf16 v[76:79], v[252:255], a[204:207], 0\n"
      "s_nop 0\n"
      "ds_read_b128 a[196:199], v1 offset:16448\n"
      "v_fma_f32 v28, 0.5, v28, v64\n"
      "v_mfma_f32_16x16x32_bf16 v[80:83], v[244:247], a[204:207], 0\n"
      "v_fma_f32 v29, 0.5, v29, v65\n"
      "v_fma_f32 v30, 0.5, v30, v66\n"
      "v_mfma_f32_16x16x32_bf16 v[84:87], v[236:239], a[204:207], 0\n"
      "v_fma_f32 v31, 0.5, v31, v67\n"
      "v_fmac_f32 v40, s48, v52\n"
      "v_mfma_f32_16x16x32_bf16 v[76:79], v[228:231], a[208:211], v[76:79]\n"
      "s_nop 0\n"
      "ds_read_b128 a[200:203], v1 offset:16512\n"
      "v_fmac_f32 v41, s48, v53\n"
      "v_mfma_f32_16x16x32_bf16 v[80:83], v[220:223], a[208:211], v[80:83]\n"
      "ds_read2st64_b32 a[220:221], v10 offset0:181 offset1:183\n"
      "ds_read2st64_b32 a[222:223], v13 offset0:181 offset1:183\n"
      "v_mfma_f32_16x16x32_bf16 v[84:87], v[212:215], a[208:211], v[84:87]\n"
      "s_waitcnt lgkmcnt(3)\n"
      "v_fmac_f32 v42, s48, v54\n"
      "v_fmac_f32 v43, s48, v55\n"
      "v_fmac_f32_e64 v28, s45, v40\n"
      "v_mfma_f32_16x16x32_bf16 v[76:79], v[204:207], a[212:215], v[76:79]\n"
      "ds_read_b128 a[204:207], v2 offset:16384\n"
      "v_fmac_f32 v29, s45, v41\n"
      "v_mfma_f32_16x16x32_bf16 v[80:83], v[196:199], a[212:215], v[80:83]\n"
      "v_fmac_f32 v30, s45, v42\n"
      "v_fmac_f32 v31, s45, v43\n"
      "v_mfma_f32_16x16x32_bf16 v[84:87], v[188:191], a[212:215], v[84:87]\n"
      "s_nop 0\n"
      "v_fma_f32 v40, s47, v40, -v52\n"
      "v_fma_f32 v41, s47, v41, -v53\n"
      "v_mfma_f32_16x16x32_bf16 v[76:79], v[180:183], a[192:195], v[76:79]\n"
      "ds_read_b128 a[208:211], v2 offset:16448\n"
      "v_fma_f32 v42, s47, v42, -v54\n"
      "v_mfma_f32_16x16x32_bf16 v[80:83], v[172:175], a[192:195], v[80:83]\n"
      "v_fma_f32 v43, s47, v43, -v55\n"
      "v_cvt_pk_bf16_f32 v28, v28, v29\n"
      "v_mfma_f32_16x16x32_bf16 v[84:87], v[164:167], a[192:195], v[84:87]\n"
      "ds_write_addtid_b32 v28 offset:32768\n"
      "v_cvt_pk_bf16_f32 v30, v30, v31\n"
      "v_mfma_f32_16x16x32_bf16 v[76:79], v[156:159], a[196:199], v[76:79]\n"
      "s_nop 0\n"
      "ds_read_b128 a[212:215], v2 offset:16512\n"
      "ds_write_addtid_b32 v30 offset:33280\n"
      "v_mfma_f32_16x16x32_bf16 v[80:83], v[148:151], a[196:199], v[80:83]\n"
      "s_waitcnt lgkmcnt(4)\n"
      "ds_read2st64_b32 a[224:225], v11 offset0:181 offset1:183\n"
      "ds_read2st64_b32 a[226:227], v14 offset0:181 offset1:183\n"
      "v_cvt_pk_bf16_f32 v40, v40, v41\n"
      "v_mfma_f32_16x16x32_bf16 v[84:87], v[140:143], a[196:199], v[84:87]\n"
      "buffer_store_dwordx4 a[220:223], v16, s[8:11], s58 offen offset:0 sc1\n"
      "v_mfma_f32_16x16x32_bf16 v[76:79], v[132:135], a[200:203], v[76:79]\n"
      "ds_read_b128 a[192:195], v0 offset:20480\n"
      "ds_write_addtid_b32 v40 offset:34816\n"
      "v_mfma_f32_16x16x32_bf16 v[80:83], v[124:127], a[200:203], v[80:83]\n"
      "v_cvt_pk_bf16_f32 v42, v42, v43\n"
      "v_fmac_f32 v44, s46, v32\n"
      "v_mfma_f32_16x16x32_bf16 v[84:87], v[116:119], a[200:203], v[84:87]\n"
      "ds_write_addtid_b32 v42 offset:35328\n"
      "v_fmac_f32 v45, s46, v33\n"
      "v_fmac_f32 v46, s46, v34\n"
      "v_mfma_f32_16x16x32_bf16 v[76:79], v[108:111], a[204:207], v[76:79]\n"
      "ds_read_b128 a[196:199], v0 offset:20544\n"
      "v_fmac_f32 v47, s46, v35\n"
      "v_mfma_f32_16x16x32_bf16 v[80:83], v[100:103], a[204:207], v[80:83]\n"
      "v_fma_f32 v32, 0.5, v32, v68\n"
      "v_fma_f32 v33, 0.5, v33, v69\n"
      "v_mfma_f32_16x16x32_bf16 v[84:87], a[160:163], a[204:207], v[84:87]\n"
      "s_waitcnt lgkmcnt(4)\n"
      "v_fma_f32 v34, 0.5, v34, v70\n"
      "v_fma_f32 v35, 0.5, v35, v71\n"
      "v_mfma_f32_16x16x32_bf16 v[76:79], a[152:155], a[208:211], v[76:79]\n"
      "ds_read_b128 a[200:203], v0 offset:20608\n"
      "v_fmac_f32 v44, s48, v56\n"
      "v_mfma_f32_16x16x32_bf16 v[80:83], a[144:147], a[208:211], v[80:83]\n"
      "buffer_store_dwordx4 a[224:227], v16, s[8:11], s58 offen offset:128 sc1\n"
      "v_mfma_f32_16x16x32_bf16 v[84:87], a[136:139], a[208:211], v[84:87]\n"
      "v_fmac_f32 v45, s48, v57\n"
      "v_fmac_f32 v46, s48, v58\n"
      "v_fmac_f32 v47, s48, v59\n"
      "v_mfma_f32_16x16x32_bf16 v[76:79], a[128:131], a[212:215], v[76:79]\n"
      "ds_read_b128 a[204:207], v1 offset:20480\n"
      "v_fmac_f32 v32, s45, v44\n"
      "v_mfma_f32_16x16x32_bf16 v[80:83], a[120:123], a[212:215], v[80:83]\n"
      "v_fmac_f32 v33, s45, v45\n"
      "v_fmac_f32 v34, s45, v46\n"
      "v_mfma_f32_16x16x32_bf16 v[84:87], a[112:115], a[212:215], v[84:87]\n"
      "s_waitcnt lgkmcnt(1)\n"
      "v_fmac_f32 v35, s45, v47\n"
      "v_fma_f32 v44, s47, v44, -v56\n"
      "v_mfma_f32_16x16x32_bf16 v[88:91], v[248:251], a[192:195], v[76:79]\n"
      "ds_read_b128 a[208:211], v1 offset:20544\n"
      "v_fma_f32 v45, s47, v45, -v57\n"
      "v_mfma_f32_16x16x32_bf16 v[92:95], v[240:243], a[192:195], v[80:83]\n"
      "v_fma_f32 v46, s47, v46, -v58\n"
      "v_fma_f32 v47, s47, v47, -v59\n"
      "v_mfma_f32_16x16x32_bf16 v[96:99], v[232:235], a[192:195], v[84:87]\n"
      "v_cvt_pk_bf16_f32 v32, v32, v33\n"
      "v_cvt_pk_bf16_f32 v34, v34, v35\n"
      "v_mfma_f32_16x16x32_bf16 v[88:91], v[224:227], a[196:199], v[88:91]\n"
      "ds_read_b128 a[212:215], v1 offset:20608\n"
      "ds_write_addtid_b32 v32 offset:40964\n"
      "v_mfma_f32_16x16x32_bf16 v[92:95], v[216:219], a[196:199], v[92:95]\n"
      "ds_read2st64_b32 a[228:229], v12 offset0:181 offset1:183\n"
      "ds_read2st64_b32 a[230:231], v15 offset0:181 offset1:183\n"
      "v_cvt_pk_bf16_f32 v44, v44, v45\n"
      "v_mfma_f32_16x16x32_bf16 v[96:99], v[208:211], a[196:199], v[96:99]\n"
      "ds_write_addtid_b32 v34 offset:41476\n"
      "ds_write_addtid_b32 v44 offset:43012\n"
      "v_mfma_f32_16x16x32_bf16 v[88:91], v[200:203], a[200:203], v[88:91]\n"
      "ds_read_b128 a[192:195], v2 offset:20480\n"
      "v_cvt_pk_bf16_f32 v46, v46, v47\n"
      "v_mfma_f32_16x16x32_bf16 v[92:95], v[192:195], a[200:203], v[92:95]\n"
      "ds_write_addtid_b32 v46 offset:43524\n"
      "v_fmac_f32 v48, s46, v36\n"
      "v_mfma_f32_16x16x32_bf16 v[96:99], v[184:187], a[200:203], v[96:99]\n"
      "s_waitcnt lgkmcnt(6)\n"
      "v_fmac_f32 v49, s46, v37\n"
      "v_fmac_f32 v50, s46, v38\n"
      "v_mfma_f32_16x16x32_bf16 v[88:91], v[176:179], a[204:207], v[88:91]\n"
      "ds_read_b128 a[196:199], v2 offset:20544\n"
      "v_fmac_f32 v51, s46, v39\n"
      "v_mfma_f32_16x16x32_bf16 v[92:95], v[168:171], a[204:207], v[92:95]\n"
      "v_fma_f32 v36, 0.5, v36, v72\n"
      "v_fma_f32 v37, 0.5, v37, v73\n"
      "v_fma_f32 v38, 0.5, v38, v74\n"
      "v_mfma_f32_16x16x32_bf16 v[96:99], v[160:163], a[204:207], v[96:99]\n"
      "v_fma_f32 v39, 0.5, v39, v75\n"
      "v_fmac_f32 v48, s48, v60\n"
      "v_mfma_f32_16x16x32_bf16 v[88:91], v[152:155], a[208:211], v[88:91]\n"
      "s_waitcnt lgkmcnt(1)\n"
      "ds_read_b128 a[200:203], v2 offset:20608\n"
      "v_fmac_f32 v49, s48, v61\n"
      "v_mfma_f32_16x16x32_bf16 v[92:95], v[144:147], a[208:211], v[92:95]\n"
      "buffer_store_dwordx4 a[228:231], v16, s[8:11], s58 offen offset:256 sc1\n"
      "v_mfma_f32_16x16x32_bf16 v[96:99], v[136:139], a[208:211], v[96:99]\n"
      "v_fmac_f32 v50, s48, v62\n"
      "v_fmac_f32 v51, s48, v63\n"
      "v_mfma_f32_16x16x32_bf16 v[88:91], v[128:131], a[212:215], v[88:91]\n"
      "ds_read_b128 a[204:207], v3 offset:50688\n"
      "v_fmac_f32 v36, s45, v48\n"
      "v_mfma_f32_16x16x32_bf16 v[92:95], v[120:123], a[212:215], v[92:95]\n"
      "v_fmac_f32 v37, s45, v49\n"
      "v_fmac_f32 v38, s45, v50\n"
      "v_fmac_f32 v39, s45, v51\n"
      "v_mfma_f32_16x16x32_bf16 v[96:99], v[112:115], a[212:215], v[96:99]\n"
      "v_fma_f32 v48, s47, v48, -v60\n"
      "v_fma_f32 v49, s47, v49, -v61\n"
      "v_mfma_f32_16x16x32_bf16 v[88:91], v[104:107], a[192:195], v[88:91]\n"
      "ds_read_b128 a[208:211], v3 offset:50752\n"
      "v_fma_f32 v50, s47, v50, -v62\n"
      "v_mfma_f32_16x16x32_bf16 v[92:95], a[164:167], a[192:195], v[92:95]\n"
      "s_nop 0\n"
      "v_fma_f32 v51, s47, v51, -v63\n"
      "v_cvt_pk_bf16_f32 v36, v36, v37\n"
      "v_mfma_f32_16x16x32_bf16 v[96:99], a[156:159], a[192:195], v[96:99]\n"
      "s_waitcnt lgkmcnt(1)\n"
      "ds_write_addtid_b32 v36 offset:36864\n"
      "v_cvt_pk_bf16_f32 v38, v38, v39\n"
      "v_mfma_f32_16x16x32_bf16 v[88:91], a[148:151], a[196:199], v[88:91]\n"
      "ds_read_b128 a[212:215], v3 offset:50816\n"
      "ds_write_addtid_b32 v38 offset:37376\n"
      "v_cvt_pk_bf16_f32 v48, v48, v49\n"
      "v_cvt_pk_bf16_f32 v50, v50, v51\n"
      "v_mfma_f32_16x16x32_bf16 v[92:95], a[140:143], a[196:199], v[92:95]\n"
      "ds_write_addtid_b32 v48 offset:38912\n"
      "ds_write_addtid_b32 v50 offset:39424\n"
      "v_mfma_f32_16x16x32_bf16 v[96:99], a[132:135], a[196:199], v[96:99]\n"
      "v_mfma_f32_16x16x32_bf16 v[88:91], a[124:127], a[200:203], v[88:91]\n"
      "ds_read_b128 a[192:195], v4 offset:50688\n"
      "v_mfma_f32_16x16x32_bf16 v[92:95], a[116:119], a[200:203], v[92:95]\n"
      "v_mfma_f32_16x16x32_bf16 v[96:99], a[108:111], a[200:203], v[96:99]\n"
      "v_mfma_f32_16x16x32_bf16 v[64:67], a[0:3], a[204:207], 0\n"
      "ds_read_b128 a[196:199], v4 offset:50752\n"
      "v_mfma_f32_16x16x32_bf16 v[68:71], a[4:7], a[204:207], 0\n"
      "v_mfma_f32_16x16x32_bf16 v[72:75], a[8:11], a[204:207], 0\n"
      "s_waitcnt lgkmcnt(1)\n"
      "ds_write_addtid_b32 v88 offset:51456\n"
      "ds_write_addtid_b32 v89 offset:51968\n"
      "v_mfma_f32_16x16x32_bf16 v[64:67], a[12:15], a[208:211], v[64:67]\n"
      "ds_read_b128 a[200:203], v4 offset:50816\n"
      "ds_write_addtid_b32 v90 offset:52480\n"
      "v_mfma_f32_16x16x32_bf16 v[68:71], a[16:19], a[208:211], v[68:71]\n"
      "s_nop 0\n"
      "ds_write_addtid_b32 v91 offset:52992\n"
      "ds_write_addtid_b32 v92 offset:53504\n"
      "v_mfma_f32_16x16x32_bf16 v[72:75], a[20:23], a[208:211], v[72:75]\n"
      "ds_write_addtid_b32 v93 offset:54016\n"
      "ds_write_addtid_b32 v94 offset:54528\n"
      "v_mfma_f32_16x16x32_bf16 v[64:67], a[24:27], a[212:215], v[64:67]\n"
      "ds_write_addtid_b32 v95 offset:55040\n"
      "ds_write_addtid_b32 v96 offset:55552\n"
      "v_mfma_f32_16x16x32_bf16 v[68:71], a[28:31], a[212:215], v[68:71]\n"
      "ds_write_addtid_b32 v97 offset:56064\n"
      "ds_write_addtid_b32 v98 offset:56576\n"
      "ds_write_addtid_b32 v99 offset:57088\n"
      "v_mfma_f32_16x16x32_bf16 v[72:75], a[32:35], a[212:215], v[72:75]\n"
      "s_waitcnt lgkmcnt(0)\n"
      "s_barrier\n"
      "v_mfma_f32_16x16x32_bf16 v[64:67], a[36:39], a[192:195], v[64:67]\n"
      "s_add_u32 s58, s58, 786432\n"
      "ds_read_b128 a[204:207], v5 offset:50688\n"
      "ds_read2st64_b32 v[28:29], v6 offset0:24 offset1:26\n"
      "v_mfma_f32_16x16x32_bf16 v[68:71], a[40:43], a[192:195], v[68:71]\n"
      "ds_read2st64_b32 v[30:31], v6 offset0:28 offset1:30\n"
      "ds_read2st64_b32 v[32:33], v6 offset0:32 offset1:34\n"
      "v_mfma_f32_16x16x32_bf16 v[72:75], a[44:47], a[192:195], v[72:75]\n"
      "ds_read2st64_b32 v[34:35], v6 offset0:36 offset1:38\n"
      "ds_read2st64_b32 v[36:37], v6 offset0:40 offset1:42\n"
      "v_mfma_f32_16x16x32_bf16 v[64:67], a[48:51], a[196:199], v[64:67]\n"
      "ds_read_b128 a[208:211], v5 offset:50752\n"
      "ds_read2st64_b32 v[38:39], v6 offset0:44 offset1:46\n"
      "v_mfma_f32_16x16x32_bf16 v[68:71], a[52:55], a[196:199], v[68:71]\n"
      "v_fma_f32 v40, 2.0, v76, -v88\n"
      "v_fma_f32 v41, 2.0, v77, -v89\n"
      "v_mfma_f32_16x16x32_bf16 v[72:75], a[56:59], a[196:199], v[72:75]\n"
      "ds_read2st64_b32 a[220:221], v10 offset0:177 offset1:179\n"
      "ds_read2st64_b32 a[222:223], v13 offset0:177 offset1:179\n"
      "v_mfma_f32_16x16x32_bf16 v[64:67], a[60:63], a[200:203], v[64:67]\n"
      "ds_read_b128 a[212:215], v5 offset:50816\n"
      "v_fma_f32 v42, 2.0, v78, -v90\n"
      "v_mfma_f32_16x16x32_bf16 v[68:71], a[64:67], a[200:203], v[68:71]\n"
      "ds_read2st64_b32 a[224:225], v11 offset0:177 offset1:179\n"
      "ds_read2st64_b32 a[226:227], v14 offset0:177 offset1:179\n"
      "v_mfma_f32_16x16x32_bf16 v[72:75], a[68:71], a[200:203], v[72:75]\n"
      "s_waitcnt lgkmcnt(9)\n"
      "v_fma_f32 v43, 2.0, v79, -v91\n"
      "v_fma_f32 v44, 2.0, v80, -v92\n"
      "v_mfma_f32_16x16x32_bf16 v[64:67], a[72:75], a[204:207], v[64:67]\n"
      "s_waitcnt lgkmcnt(3)\n"
      "buffer_store_dwordx4 a[220:223], v16, s[8:11], s58 offen offset:0 sc1\n"
      "v_mfma_f32_16x16x32_bf16 v[68:71], a[76:79], a[204:207], v[68:71]\n"
      "v_fma_f32 v45, 2.0, v81, -v93\n"
      "v_fma_f32 v46, 2.0, v82, -v94\n"
      "v_mfma_f32_16x16x32_bf16 v[72:75], a[80:83], a[204:207], v[72:75]\n"
      "ds_read2st64_b32 a[228:229], v12 offset0:177 offset1:179\n"
      "ds_read2st64_b32 a[230:231], v15 offset0:177 offset1:179\n"
      "v_mfma_f32_16x16x32_bf16 v[64:67], a[84:87], a[208:211], v[64:67]\n"
      "s_waitcnt lgkmcnt(2)\n"
      "buffer_store_dwordx4 a[224:227], v16, s[8:11], s58 offen offset:128 sc1\n"
      "v_mfma_f32_16x16x32_bf16 v[68:71], a[88:91], a[208:211], v[68:71]\n"
      "s_nop 0\n"
      "v_fma_f32 v47, 2.0, v83, -v95\n"
      "v_fma_f32 v48, 2.0, v84, -v96\n"
      "v_mfma_f32_16x16x32_bf16 v[72:75], a[92:95], a[208:211], v[72:75]\n"
      "v_fma_f32 v49, 2.0, v85, -v97\n"
      "v_fma_f32 v50, 2.0, v86, -v98\n"
      "v_mfma_f32_16x16x32_bf16 v[64:67], a[96:99], a[212:215], v[64:67]\n"
      "s_waitcnt lgkmcnt(0)\n"
      "buffer_store_dwordx4 a[228:231], v16, s[8:11], s58 offen offset:256 sc1\n"
      "v_mfma_f32_16x16x32_bf16 v[68:71], a[100:103], a[212:215], v[68:71]\n"
      "s_nop 0\n"
      "v_fma_f32 v51, 2.0, v87, -v99\n"
      "v_fmac_f32 v40, s46, v28\n"
      "v_mfma_f32_16x16x32_bf16 v[72:75], a[104:107], a[212:215], v[72:75]\n"
      "v_fmac_f32 v41, s46, v29\n"
      "v_fmac_f32 v42, s46, v30\n"
      "v_fmac_f32 v43, s46, v31\n"
      "v_fma_f32 v28, 0.5, v28, v52\n"
      "v_fma_f32 v29, 0.5, v29, v53\n"
      "v_fma_f32 v30, 0.5, v30, v54\n"
      "v_fma_f32 v31, 0.5, v31, v55\n"
      "v_fmac_f32 v44, s46, v32\n"
      "v_fmac_f32 v45, s46, v33\n"
      "v_pk_fma_f32 v[46:47], v[34:35], s[46:47], v[46:47] op_sel:[0,0,0] op_sel_hi:[1,0,1]\n"
      "v_pk_fma_f32 v[32:33], v[32:33], s[48:49], v[56:57] op_sel:[0,1,0] op_sel_hi:[1,1,1]\n"
      "v_pk_fma_f32 v[34:35], v[34:35], s[48:49], v[58:59] op_sel:[0,1,0] op_sel_hi:[1,1,1]\n"
      "v_pk_fma_f32 v[48:49], v[36:37], s[46:47], v[48:49] op_sel:[0,0,0] op_sel_hi:[1,0,1]\n"
      "v_pk_fma_f32 v[50:51], v[38:39], s[46:47], v[50:51] op_sel:[0,0,0] op_sel_hi:[1,0,1]\n"
      "v_pk_fma_f32 v[36:37], v[36:37], s[48:49], v[60:61] op_sel:[0,1,0] op_sel_hi:[1,1,1]\n"
      "v_pk_fma_f32 v[38:39], v[38:39], s[48:49], v[62:63] op_sel:[0,1,0] op_sel_hi:[1,1,1]\n"
      "v_pk_fma_f32 v[40:41], v[64:65], s[48:49], v[40:41] op_sel:[0,0,0] op_sel_hi:[1,0,1]\n"
      "v_pk_fma_f32 v[42:43], v[66:67], s[48:49], v[42:43] op_sel:[0,0,0] op_sel_hi:[1,0,1]\n"
      "v_pk_fma_f32 v[28:29], v[40:41], s[44:45], v[28:29] op_sel:[0,1,0] op_sel_hi:[1,1,1]\n"
      "v_pk_fma_f32 v[30:31], v[42:43], s[44:45], v[30:31] op_sel:[0,1,0] op_sel_hi:[1,1,1]\n"
      "v_pk_fma_f32 v[40:41], v[40:41], s[46:47], v[64:65] op_sel:[0,1,0] op_sel_hi:[1,1,1] neg_lo:[0,0,1] neg_hi:[0,0,1]\n"
      "v_pk_fma_f32 v[42:43], v[42:43], s[46:47], v[66:67] op_sel:[0,1,0] op_sel_hi:[1,1,1] neg_lo:[0,0,1] neg_hi:[0,0,1]\n"
      "v_cvt_pk_bf16_f32 v28, v28, v29\n"
      "v_cvt_pk_bf16_f32 v30, v30, v31\n"
      "v_cvt_pk_bf16_f32 v40, v40, v41\n"
      "v_cvt_pk_bf16_f32 v42, v42, v43\n"
      "v_pk_fma_f32 v[44:45], v[68:69], s[48:49], v[44:45] op_sel:[0,0,0] op_sel_hi:[1,0,1]\n"
      "v_pk_fma_f32 v[46:47], v[70:71], s[48:49], v[46:47] op_sel:[0,0,0] op_sel_hi:[1,0,1]\n"
      "v_pk_fma_f32 v[32:33], v[44:45], s[44:45], v[32:33] op_sel:[0,1,0] op_sel_hi:[1,1,1]\n"
      "v_pk_fma_f32 v[34:35], v[46:47], s[44:45], v[34:35] op_sel:[0,1,0] op_sel_hi:[1,1,1]\n"
      "v_pk_fma_f32 v[44:45], v[44:45], s[46:47], v[68:69] op_sel:[0,1,0] op_sel_hi:[1,1,1] neg_lo:[0,0,1] neg_hi:[0,0,1]\n"
      "v_pk_fma_f32 v[46:47], v[46:47], s[46:47], v[70:71] op_sel:[0,1,0] op_sel_hi:[1,1,1] neg_lo:[0,0,1] neg_hi:[0,0,1]\n"
      "v_cvt_pk_bf16_f32 v32, v32, v33\n"
      "v_cvt_pk_bf16_f32 v34, v34, v35\n"
      "ds_write_addtid_b32 v28 offset:33792\n"
      "ds_write_addtid_b32 v30 offset:34304\n"
      "ds_write_addtid_b32 v40 offset:35840\n"
      "ds_write_addtid_b32 v42 offset:36352\n"
      "ds_write_addtid_b32 v32 offset:41988\n"
      "ds_write_addtid_b32 v34 offset:42500\n"
      "v_cvt_pk_bf16_f32 v44, v44, v45\n"
      "v_cvt_pk_bf16_f32 v46, v46, v47\n"
      "v_pk_fma_f32 v[48:49], v[72:73], s[48:49], v[48:49] op_sel:[0,0,0] op_sel_hi:[1,0,1]\n"
      "v_pk_fma_f32 v[50:51], v[74:75], s[48:49], v[50:51] op_sel:[0,0,0] op_sel_hi:[1,0,1]\n"
      "v_pk_fma_f32 v[36:37], v[48:49], s[44:45], v[36:37] op_sel:[0,1,0] op_sel_hi:[1,1,1]\n"
      "v_pk_fma_f32 v[38:39], v[50:51], s[44:45], v[38:39] op_sel:[0,1,0] op_sel_hi:[1,1,1]\n"
      "v_pk_fma_f32 v[48:49], v[48:49], s[46:47], v[72:73] op_sel:[0,1,0] op_sel_hi:[1,1,1] neg_lo:[0,0,1] neg_hi:[0,0,1]\n"
      "v_pk_fma_f32 v[50:51], v[50:51], s[46:47], v[74:75] op_sel:[0,1,0] op_sel_hi:[1,1,1] neg_lo:[0,0,1] neg_hi:[0,0,1]\n"
      "v_cvt_pk_bf16_f32 v36, v36, v37\n"
      "v_cvt_pk_bf16_f32 v38, v38, v39\n"
      "v_cvt_pk_bf16_f32 v48, v48, v49\n"
      "v_cvt_pk_bf16_f32 v50, v50, v51\n"
      "ds_write_addtid_b32 v44 offset:44036\n"
      "ds_write_addtid_b32 v46 offset:44548\n"
      "ds_write_addtid_b32 v36 offset:37888\n"
      "ds_write_addtid_b32 v38 offset:38400\n"
      "ds_write_addtid_b32 v48 offset:39936\n"
      "ds_write_addtid_b32 v50 offset:40448\n"
      "s_waitcnt lgkmcnt(0)\n"
      "s_barrier\n"
      "ds_read2st64_b32 a[220:221], v10 offset0:181 offset1:183\n"
      "ds_read2st64_b32 a[222:223], v13 offset0:181 offset1:183\n"
      "ds_read2st64_b32 a[224:225], v11 offset0:181 offset1:183\n"
      "ds_read2st64_b32 a[226:227], v14 offset0:181 offset1:183\n"
      "ds_read2st64_b32 a[228:229], v12 offset0:181 offset1:183\n"
      "ds_read2st64_b32 a[230:231], v15 offset0:181 offset1:183\n"
      "s_add_u32 s58, s58, 786432\n"
      "s_waitcnt lgkmcnt(4)\n"
      "buffer_store_dwordx4 a[220:223], v16, s[8:11], s58 offen offset:0 sc1\n"
      "s_waitcnt lgkmcnt(2)\n"
      "buffer_store_dwordx4 a[224:227], v16, s[8:11], s58 offen offset:128 sc1\n"
      "s_waitcnt lgkmcnt(0)\n"
      "buffer_store_dwordx4 a[228:231], v16, s[8:11], s58 offen offset:256 sc1\n"
      "LDend_%=:\n"
      "s_branch Lfin_%=\n"
      ".p2align 6\n"
      "LS_%=:\n"
      "v_lshlrev_b32 v43, 4, v37\n"
      "v_lshlrev_b32 v44, 2, v37\n"
      "v_and_b32 v43, 16, v43\n"
      "v_and_or_b32 v43, v44, 8, v43\n"
      "v_mul_u32_u24 v44, 192, v38\n"
      "v_mul_u32_u24 v45, 1536, v39\n"
      "s_lshl_b32 s27, s18, 5\n"
      "v_lshlrev_b32 v46, 1, v36\n"
      "v_add3_u32 v43, v43, v44, v45\n"
      "v_bitop3_b32 v46, v46, s27, 32 bitop3:0x6c\n"
      "v_add_u32 v60, v43, v46\n"
      "v_add_u32 v61, 57344, v60\n"
      "v_add_u32 v62, 114688, v60\n"
      "s_lshl_b32 s26, s18, 3\n"
      "v_and_b32 v42, 15, v36\n"
      "v_xor_b32 v42, s26, v42\n"
      "s_lshl_b32 s26, s17, 13\n"
      "s_add_u32 s26, s26, 24832\n"
      "s_mul_i32 s27, s17, 12672\n"
      "v_mul_u32_u24 v44, 192, v42\n"
      "v_lshlrev_b32 v43, 3, v42\n"
      "v_and_b32 v45, 48, v36\n"
      "s_add_u32 s27, s27, 82432\n"
      "v_bitop3_b32 v46, v43, v45, 32 bitop3:0x6c\n"
      "v_add3_u32 v0, v46, v44, s26\n"
      "v_add3_u32 v3, v46, v44, s27\n"
      "v_add_u32 v43, 16, v43\n"
      "v_add_u32 v44, 384, v44\n"
      "v_bitop3_b32 v46, v43, v45, 32 bitop3:0x6c\n"
      "v_add3_u32 v1, v46, v44, s26\n"
      "v_add3_u32 v4, v46, v44, s27\n"
      "v_add_u32 v43, 16, v43\n"
      "v_add_u32 v44, 384, v44\n"
      "v_bitop3_b32 v46, v43, v45, 32 bitop3:0x6c\n"
      "v_add3_u32 v2, v46, v44, s26\n"
      "v_add3_u32 v5, v46, v44, s27\n"
      "v_lshlrev_b32 v43, 31, v36\n"
      "v_and_b32 v44, 2, v36\n"
      "v_or_b32 v43, 0x3f803f80, v43\n"
      "v_mul_u32_u24 v45, 0xc00080, v44\n"
      "v_lshl_add_u32 v44, v44, 22, v43\n"
      "v_add_u32 v45, v45, v43\n"
      "v_accvgpr_write_b32 a216, v44\n"
      "v_accvgpr_write_b32 a217, v45\n"
      "v_lshlrev_b32 v43, 3, v38\n"
      "s_mov_b32 s26, 806420736\n"
      "v_bfe_u32 v43, s26, v43, 8\n"
      "v_lshlrev_b32 v44, 12, v38\n"
      "s_lshl_b32 s28, s18, 8\n"
      "s_add_u32 s28, s28, 82432\n"
      "v_lshl_add_u32 v43, v43, 3, v44\n"
      "v_lshl_add_u32 v44, v39, 2, v37\n"
      "v_lshl_add_u32 v43, v44, 4, v43\n"
      "v_add_u32 v7, s28, v43\n"
      "s_mul_i32 s26, s17, 70144\n"
      "s_lshl_b32 s27, s18, 8\n"
      "s_sub_u32 s26, 70144, s26\n"
      "s_add_u32 s26, s26, s27\n"
      "v_lshl_add_u32 v6, v36, 2, s26\n"
      "v_lshrrev_b32 v43, 3, v36\n"
      "v_and_b32 v44, 7, v36\n"
      "v_mul_u32_u24 v45, 384, v43\n"
      "v_lshl_add_u32 v11, v44, 4, v45\n"
      "v_and_b32 v44, 2, v36\n"
      "v_lshlrev_b32 v43, 3, v43\n"
      "v_lshlrev_b32 v45, 4, v44\n"
      "v_xor_b32 v43, v43, v45\n"
      "v_lshl_add_u32 v43, v44, 7, v43\n"
      "v_and_b32 v44, 1, v36\n"
      "s_lshl_b32 s26, s18, 11\n"
      "v_lshl_add_u32 v43, v44, 6, v43\n"
      "s_add_u32 s26, s26, 12288\n"
      "v_bfe_u32 v44, v36, 2, 1\n"
      "v_add_u32 v43, s26, v43\n"
      "v_mul_i32_i24 v45, -4100, v44\n"
      "s_mov_b32 s27, 8200\n"
      "v_add3_u32 v8, v43, v45, s27\n"
      "v_add_u32 v9, 128, v8\n"
      "s_mul_i32 s62, s18, 393216\n"
      "s_add_u32 s62, s62, s19\n"
      "s_add_u32 s62, s62, s21\n"
      "s_mov_b32 s40, 0x80000000\n"
      "s_and_b32 s9, s9, 0xffff\n"
      "s_mov_b32 s45, 0xbeaaaaab\n"
      "s_mov_b32 s46, 0xbe800000\n"
      "s_mov_b32 s47, 0x3eaaaaab\n"
      "s_add_u32 m0, s63, 6144\n"
      "s_add_u32 s52, s63, 61440\n"
      "buffer_load_dwordx4 v17, s[12:15], s52 offen offset:0  lds\n"
      "buffer_load_dwordx4 v18, s[12:15], s52 offen offset:1024  lds\n"
      "buffer_load_dwordx4 v19, s[12:15], s52 offen offset:2048  lds\n"
      "s_add_u32 m0, s63, 12288\n"
      "s_add_u32 s52, s63, 116736\n"
      "buffer_load_dwordx4 v17, s[12:15], s52 offen offset:0  lds\n"
      "buffer_load_dwordx4 v18, s[12:15], s52 offen offset:1024  lds\n"
      "buffer_load_dwordx4 v19, s[12:15], s52 offen offset:2048  lds\n"
      "LSpro_%=:\n"
      "s_waitcnt vmcnt(6)\n"
      "s_barrier\n"
      "ds_read_b64_tr_b16 a[224:225], v60 offset:24832\n"
      "ds_read_b64_tr_b16 a[226:227], v60 offset:25600\n"
      "ds_read_b64_tr_b16 a[0:1], v62 offset:26112\n"
      "ds_read_b64_tr_b16 a[2:3], v62 offset:26880\n"
      "ds_read_b64_tr_b16 a[220:221], v60 offset:30976\n"
      "ds_read_b64_tr_b16 a[222:223], v60 offset:31744\n"
      "ds_read_b64_tr_b16 a[232:233], v60 offset:24896\n"
      "ds_read_b64_tr_b16 a[234:235], v60 offset:25664\n"
      "ds_read_b64_tr_b16 a[4:5], v62 offset:26176\n"
      "ds_read_b64_tr_b16 a[6:7], v62 offset:26944\n"
      "ds_read_b64_tr_b16 a[228:229], v60 offset:31040\n"
      "ds_read_b64_tr_b16 a[230:231], v60 offset:31808\n"
      "s_add_u32 m0, s63, 18432\n"
      "s_add_u32 s52, s63, 12288\n"
      "s_nop 0\n"
      "s_waitcnt lgkmcnt(10)\n"
      "v_mfma_f32_4x4x4_16b_bf16 v[28:31], a[244:245], a[224:225], 0\n"
      "v_mfma_f32_4x4x4_16b_bf16 v[32:35], a[244:245], a[226:227], 0\n"
      "buffer_load_dwordx4 v17, s[12:15], s52 offen offset:0  lds\n"
      "s_waitcnt lgkmcnt(8)\n"
      "v_mfma_f32_4x4x4_16b_bf16 v[28:31], a[248:249], a[0:1], v[28:31]\n"
      "ds_read_b64_tr_b16 a[240:241], v60 offset:24960\n"
      "v_mfma_f32_4x4x4_16b_bf16 v[32:35], a[248:249], a[2:3], v[32:35]\n"
      "buffer_load_dwordx4 v18, s[12:15], s52 offen offset:1024  lds\n"
      "s_waitcnt lgkmcnt(7)\n"
      "v_mfma_f32_4x4x4_16b_bf16 v[36:39], a[246:247], a[220:221], v[28:31]\n"
      "ds_read_b64_tr_b16 a[242:243], v60 offset:25728\n"
      "v_mfma_f32_4x4x4_16b_bf16 v[40:43], a[246:247], a[222:223], v[32:35]\n"
      "buffer_load_dwordx4 v19, s[12:15], s52 offen offset:2048  lds\n"
      "v_mfma_f32_4x4x4_16b_bf16 v[28:31], a[250:251], a[220:221], v[28:31]\n"
      "ds_read_b64_tr_b16 a[8:9], v62 offset:26240\n"
      "v_mfma_f32_4x4x4_16b_bf16 v[32:35], a[250:251], a[222:223], v[32:35]\n"
      "ds_read_b64_tr_b16 a[10:11], v62 offset:27008\n"
      "ds_read_b64_tr_b16 a[236:237], v60 offset:31104\n"
      "ds_read_b64_tr_b16 a[238:239], v60 offset:31872\n"
      "s_add_u32 m0, s63, 57600\n"
      "s_add_u32 s52, s63, 67584\n"
      "s_nop 0\n"
      "s_waitcnt lgkmcnt(5)\n"
      "v_mfma_f32_4x4x4_16b_bf16 v[44:47], a[244:245], a[232:233], 0\n"
      "v_mfma_f32_4x4x4_16b_bf16 v[48:51], a[244:245], a[234:235], 0\n"
      "buffer_load_dwordx4 v17, s[12:15], s52 offen offset:0  lds\n"
      "v_mfma_f32_4x4x4_16b_bf16 v[44:47], a[248:249], a[4:5], v[44:47]\n"
      "buffer_load_dwordx4 v18, s[12:15], s52 offen offset:1024  lds\n"
      "v_mfma_f32_4x4x4_16b_bf16 v[48:51], a[248:249], a[6:7], v[48:51]\n"
      "buffer_load_dwordx4 v19, s[12:15], s52 offen offset:2048  lds\n"
      "v_mfma_f32_4x4x4_16b_bf16 v[52:55], a[246:247], a[228:229], v[44:47]\n"
      "v_mfma_f32_4x4x4_16b_bf16 v[56:59], a[246:247], a[230:231], v[48:51]\n"
      "v_mfma_f32_4x4x4_16b_bf16 v[44:47], a[250:251], a[228:229], v[44:47]\n"
      "v_mfma_f32_4x4x4_16b_bf16 v[48:51], a[250:251], a[230:231], v[48:51]\n"
      "v_cvt_pk_bf16_f32 v252, v36, v40\n"
      "v_cvt_pk_bf16_f32 v253, v37, v41\n"
      "v_cvt_pk_bf16_f32 v254, v38, v42\n"
      "v_cvt_pk_bf16_f32 v255, v39, v43\n"
      "v_cvt_pk_bf16_f32 v248, v28, v32\n"
      "v_cvt_pk_bf16_f32 v249, v29, v33\n"
      "v_cvt_pk_bf16_f32 v250, v30, v34\n"
      "v_cvt_pk_bf16_f32 v251, v31, v35\n"
      "s_add_u32 m0, s63, 69888\n"
      "s_add_u32 s52, s63, 18432\n"
      "s_nop 0\n"
      "s_waitcnt lgkmcnt(0)\n"
      "v_mfma_f32_4x4x4_16b_bf16 v[28:31], a[244:245], a[240:241], 0\n"
      "v_mfma_f32_4x4x4_16b_bf16 v[32:35], a[244:245], a[242:243], 0\n"
      "buffer_load_dwordx4 v17, s[12:15], s52 offen offset:0  lds\n"
      "v_mfma_f32_4x4x4_16b_bf16 v[28:31], a[248:249], a[8:9], v[28:31]\n"
      "buffer_load_dwordx4 v18, s[12:15], s52 offen offset:1024  lds\n"
      "v_mfma_f32_4x4x4_16b_bf16 v[32:35], a[248:249], a[10:11], v[32:35]\n"
      "buffer_load_dwordx4 v19, s[12:15], s52 offen offset:2048  lds\n"
      "v_mfma_f32_4x4x4_16b_bf16 v[36:39], a[246:247], a[236:237], v[28:31]\n"
      "v_mfma_f32_4x4x4_16b_bf16 v[40:43], a[246:247], a[238:239], v[32:35]\n"
      "v_mfma_f32_4x4x4_16b_bf16 v[28:31], a[250:251], a[236:237], v[28:31]\n"
      "v_mfma_f32_4x4x4_16b_bf16 v[32:35], a[250:251], a[238:239], v[32:35]\n"
      "v_cvt_pk_bf16_f32 v244, v52, v56\n"
      "v_cvt_pk_bf16_f32 v245, v53, v57\n"
      "v_cvt_pk_bf16_f32 v246, v54, v58\n"
      "v_cvt_pk_bf16_f32 v247, v55, v59\n"
      "v_cvt_pk_bf16_f32 v240, v44, v48\n"
      "v_cvt_pk_bf16_f32 v241, v45, v49\n"
      "v_cvt_pk_bf16_f32 v242, v46, v50\n"
      "v_cvt_pk_bf16_f32 v243, v47, v51\n"
      "s_waitcnt vmcnt(9)\n"
      "s_barrier\n"
      "ds_read_b64_tr_b16 a[224:225], v60 offset:0\n"
      "ds_read_b64_tr_b16 a[226:227], v60 offset:768\n"
      "ds_read_b64_tr_b16 a[12:13], v60 offset:12288\n"
      "ds_read_b64_tr_b16 a[14:15], v60 offset:13056\n"
      "ds_read_b64_tr_b16 a[220:221], v60 offset:6144\n"
      "ds_read_b64_tr_b16 a[222:223], v60 offset:6912\n"
      "s_add_u32 m0, s63, 76032\n"
      "s_add_u32 s52, s63, 73728\n"
      "s_nop 0\n"
      "s_waitcnt lgkmcnt(4)\n"
      "v_mfma_f32_4x4x4_16b_bf16 v[44:47], a[244:245], a[224:225], 0\n"
      "v_mfma_f32_4x4x4_16b_bf16 v[48:51], a[244:245], a[226:227], 0\n"
      "buffer_load_dwordx4 v17, s[12:15], s52 offen offset:0  lds\n"
      "s_waitcnt lgkmcnt(2)\n"
      "v_mfma_f32_4x4x4_16b_bf16 v[44:47], a[248:249], a[12:13], v[44:47]\n"
      "ds_read_b64_tr_b16 a[232:233], v60 offset:64\n"
      "v_mfma_f32_4x4x4_16b_bf16 v[48:51], a[248:249], a[14:15], v[48:51]\n"
      "buffer_load_dwordx4 v18, s[12:15], s52 offen offset:1024  lds\n"
      "s_waitcnt lgkmcnt(1)\n"
      "v_mfma_f32_4x4x4_16b_bf16 v[52:55], a[246:247], a[220:221], v[44:47]\n"
      "ds_read_b64_tr_b16 a[234:235], v60 offset:832\n"
      "v_mfma_f32_4x4x4_16b_bf16 v[56:59], a[246:247], a[222:223], v[48:51]\n"
      "buffer_load_dwordx4 v19, s[12:15], s52 offen offset:2048  lds\n"
      "v_mfma_f32_4x4x4_16b_bf16 v[44:47], a[250:251], a[220:221], v[44:47]\n"
      "ds_read_b64_tr_b16 a[16:17], v60 offset:12352\n"
      "v_mfma_f32_4x4x4_16b_bf16 v[48:51], a[250:251], a[222:223], v[48:51]\n"
      "ds_read_b64_tr_b16 a[18:19], v60 offset:13120\n"
      "ds_read_b64_tr_b16 a[228:229], v60 offset:6208\n"
      "ds_read_b64_tr_b16 a[230:231], v60 offset:6976\n"
      "ds_read_b64_tr_b16 a[240:241], v60 offset:128\n"
      "ds_read_b64_tr_b16 a[242:243], v60 offset:896\n"
      "ds_read_b64_tr_b16 a[20:21], v60 offset:12416\n"
      "ds_read_b64_tr_b16 a[22:23], v60 offset:13184\n"
      "ds_read_b64_tr_b16 a[236:237], v60 offset:6272\n"
      "ds_read_b64_tr_b16 a[238:239], v60 offset:7040\n"
      "ds_read_b64_tr_b16 a[168:169], v7 offset:0\n"
      "ds_read_b64_tr_b16 a[170:171], v7 offset:8\n"
      "ds_read_b128 a[204:207], v3 offset:0\n"
      "v_cvt_pk_bf16_f32 v236, v36, v40\n"
      "v_cvt_pk_bf16_f32 v237, v37, v41\n"
      "v_cvt_pk_bf16_f32 v238, v38, v42\n"
      "v_cvt_pk_bf16_f32 v239, v39, v43\n"
      "v_cvt_pk_bf16_f32 v232, v28, v32\n"
      "v_cvt_pk_bf16_f32 v233, v29, v33\n"
      "v_cvt_pk_bf16_f32 v234, v30, v34\n"
      "v_cvt_pk_bf16_f32 v235, v31, v35\n"
      "s_waitcnt vmcnt(6)\n"
      "s_barrier\n"
      "s_add_u32 m0, s63, 116224\n"
      "s_add_u32 s52, s63, 24576\n"
      "s_waitcnt lgkmcnt(7)\n"
      "v_mfma_f32_4x4x4_16b_bf16 v[28:31], a[244:245], a[232:233], 0\n"
      "v_mfma_f32_4x4x4_16b_bf16 v[32:35], a[244:245], a[234:235], 0\n"
      "buffer_load_dwordx4 v17, s[12:15], s52 offen offset:0  lds\n"
      "v_mfma_f32_4x4x4_16b_bf16 v[28:31], a[248:249], a[16:17], v[28:31]\n"
      "ds_read_b64_tr_b16 a[224:225], v60 offset:18432\n"
      "v_mfma_f32_4x4x4_16b_bf16 v[32:35], a[248:249], a[18:19], v[32:35]\n"
      "buffer_load_dwordx4 v18, s[12:15], s52 offen offset:1024  lds\n"
      "v_mfma_f32_4x4x4_16b_bf16 v[36:39], a[246:247], a[228:229], v[28:31]\n"
      "ds_read_b64_tr_b16 a[226:227], v60 offset:19200\n"
      "v_mfma_f32_4x4x4_16b_bf16 v[40:43], a[246:247], a[230:231], v[32:35]\n"
      "buffer_load_dwordx4 v19, s[12:15], s52 offen offset:2048  lds\n"
      "v_mfma_f32_4x4x4_16b_bf16 v[28:31], a[250:251], a[228:229], v[28:31]\n"
      "ds_read_b64_tr_b16 a[24:25], v61 offset:6400\n"
      "v_mfma_f32_4x4x4_16b_bf16 v[32:35], a[250:251], a[230:231], v[32:35]\n"
      "ds_read_b64_tr_b16 a[26:27], v61 offset:7168\n"
      "s_waitcnt lgkmcnt(4)\n"
      "v_mfma_f32_4x4x4_16b_bf16 v[20:23], a[216:217], a[168:169], 0\n"
      "ds_read_b64_tr_b16 a[220:221], v61 offset:256\n"
      "v_mfma_f32_4x4x4_16b_bf16 v[24:27], a[216:217], a[170:171], 0\n"
      "ds_read_b64_tr_b16 a[222:223], v61 offset:1024\n"
      "ds_read_b64_tr_b16 a[172:173], v7 offset:512\n"
      "ds_read_b64_tr_b16 a[174:175], v7 offset:520\n"
      "ds_read_b128 a[208:211], v3 offset:64\n"
      "v_cvt_pk_bf16_f32 v228, v52, v56\n"
      "v_cvt_pk_bf16_f32 v229, v53, v57\n"
      "v_cvt_pk_bf16_f32 v230, v54, v58\n"
      "v_cvt_pk_bf16_f32 v231, v55, v59\n"
      "v_cvt_pk_bf16_f32 v224, v44, v48\n"
      "v_cvt_pk_bf16_f32 v225, v45, v49\n"
      "v_cvt_pk_bf16_f32 v226, v46, v50\n"
      "v_cvt_pk_bf16_f32 v227, v47, v51\n"
      "s_add_u32 m0, s63, 122368\n"
      "s_add_u32 s52, s63, 79872\n"
      "v_mfma_f32_4x4x4_16b_bf16 v[44:47], a[244:245], a[240:241], 0\n"
      "v_mfma_f32_4x4x4_16b_bf16 v[48:51], a[244:245], a[242:243], 0\n"
      "buffer_load_dwordx4 v17, s[12:15], s52 offen offset:0  lds\n"
      "v_mfma_f32_4x4x4_16b_bf16 v[44:47], a[248:249], a[20:21], v[44:47]\n"
      "ds_read_b64_tr_b16 a[232:233], v60 offset:18496\n"
      "v_mfma_f32_4x4x4_16b_bf16 v[48:51], a[248:249], a[22:23], v[48:51]\n"
      "buffer_load_dwordx4 v18, s[12:15], s52 offen offset:1024  lds\n"
      "v_mfma_f32_4x4x4_16b_bf16 v[52:55], a[246:247], a[236:237], v[44:47]\n"
      "ds_read_b64_tr_b16 a[234:235], v60 offset:19264\n"
      "v_mfma_f32_4x4x4_16b_bf16 v[56:59], a[246:247], a[238:239], v[48:51]\n"
      "buffer_load_dwordx4 v19, s[12:15], s52 offen offset:2048  lds\n"
      "v_mfma_f32_4x4x4_16b_bf16 v[44:47], a[250:251], a[236:237], v[44:47]\n"
      "ds_read_b64_tr_b16 a[28:29], v61 offset:6464\n"
      "v_mfma_f32_4x4x4_16b_bf16 v[48:51], a[250:251], a[238:239], v[48:51]\n"
      "ds_read_b64_tr_b16 a[30:31], v61 offset:7232\n"
      "s_nop 0\n"
      "s_waitcnt lgkmcnt(4)\n"
      "v_mfma_f32_4x4x4_16b_bf16 v[100:103], a[216:217], a[172:173], 0\n"
      "ds_read_b64_tr_b16 a[228:229], v61 offset:320\n"
      "v_mfma_f32_4x4x4_16b_bf16 v[104:107], a[216:217], a[174:175], 0\n"
      "ds_read_b64_tr_b16 a[230:231], v61 offset:1088\n"
      "ds_read_b64_tr_b16 a[176:177], v7 offset:1024\n"
      "ds_read_b64_tr_b16 a[178:179], v7 offset:1032\n"
      "v_cvt_pk_bf16_f32 v220, v36, v40\n"
      "v_cvt_pk_bf16_f32 v221, v37, v41\n"
      "v_cvt_pk_bf16_f32 v222, v38, v42\n"
      "v_cvt_pk_bf16_f32 v223, v39, v43\n"
      "v_cvt_pk_bf16_f32 v216, v28, v32\n"
      "v_cvt_pk_bf16_f32 v217, v29, v33\n"
      "v_mfma_f32_16x16x32_bf16 v[64:67], a[0:3], a[204:207], 0\n"
      "v_cvt_pk_bf16_f32 v218, v30, v34\n"
      "v_cvt_pk_bf16_f32 v219, v31, v35\n"
      "v_mfma_f32_16x16x32_bf16 v[68:71], a[4:7], a[204:207], 0\n"
      "v_cvt_pk_bf16_f32 v20, v20, v24\n"
      "v_cvt_pk_bf16_f32 v21, v21, v25\n"
      "v_mfma_f32_16x16x32_bf16 v[72:75], a[8:11], a[204:207], 0\n"
      "s_add_u32 m0, s63, 41216\n"
      "v_cvt_pk_bf16_f32 v22, v22, v26\n"
      "v_cvt_pk_bf16_f32 v23, v23, v27\n"
      "s_add_u32 s52, s63, 30720\n"
      "v_mfma_f32_4x4x4_16b_bf16 v[28:31], a[244:245], a[224:225], 0\n"
      "v_mfma_f32_4x4x4_16b_bf16 v[32:35], a[244:245], a[226:227], 0\n"
      "buffer_load_dwordx4 v17, s[12:15], s52 offen offset:0  lds\n"
      "v_mfma_f32_4x4x4_16b_bf16 v[28:31], a[248:249], a[24:25], v[28:31]\n"
      "ds_read_b64_tr_b16 a[240:241], v60 offset:18560\n"
      "v_mfma_f32_4x4x4_16b_bf16 v[32:35], a[248:249], a[26:27], v[32:35]\n"
      "buffer_load_dwordx4 v18, s[12:15], s52 offen offset:1024  lds\n"
      "v_mfma_f32_4x4x4_16b_bf16 v[36:39], a[246:247], a[220:221], v[28:31]\n"
      "ds_read_b64_tr_b16 a[242:243], v60 offset:19328\n"
      "v_mfma_f32_4x4x4_16b_bf16 v[40:43], a[246:247], a[222:223], v[32:35]\n"
      "buffer_load_dwordx4 v19, s[12:15], s52 offen offset:2048  lds\n"
      "v_mfma_f32_4x4x4_16b_bf16 v[28:31], a[250:251], a[220:221], v[28:31]\n"
      "ds_read_b64_tr_b16 a[32:33], v61 offset:6528\n"
      "v_mfma_f32_4x4x4_16b_bf16 v[32:35], a[250:251], a[222:223], v[32:35]\n"
      "ds_read_b64_tr_b16 a[34:35], v61 offset:7296\n"
      "s_nop 0\n"
      "s_waitcnt lgkmcnt(4)\n"
      "v_mfma_f32_4x4x4_16b_bf16 v[108:111], a[216:217], a[176:177], 0\n"
      "ds_read_b64_tr_b16 a[236:237], v61 offset:384\n"
      "v_mfma_f32_4x4x4_16b_bf16 v[112:115], a[216:217], a[178:179], 0\n"
      "ds_read_b64_tr_b16 a[238:239], v61 offset:1152\n"
      "ds_read_b64_tr_b16 a[180:181], v7 offset:1536\n"
      "ds_read_b64_tr_b16 a[182:183], v7 offset:1544\n"
      "s_mov_b32 m0, s64\n"
      "s_nop 0\n"
      "ds_write_addtid_b32 v20 offset:24832\n"
      "ds_write_addtid_b32 v21 offset:28928\n"
      "ds_write_addtid_b32 v22 offset:33024\n"
      "ds_write_addtid_b32 v23 offset:37120\n"
      "v_cvt_pk_bf16_f32 v212, v52, v56\n"
      "v_cvt_pk_bf16_f32 v213, v53, v57\n"
      "v_cvt_pk_bf16_f32 v214, v54, v58\n"
      "v_cvt_pk_bf16_f32 v215, v55, v59\n"
      "v_cvt_pk_bf16_f32 v208, v44, v48\n"
      "v_cvt_pk_bf16_f32 v209, v45, v49\n"
      "v_mfma_f32_16x16x32_bf16 v[64:67], a[12:15], a[208:211], v[64:67]\n"
      "v_cvt_pk_bf16_f32 v210, v46, v50\n"
      "v_cvt_pk_bf16_f32 v211, v47, v51\n"
      "v_mfma_f32_16x16x32_bf16 v[68:71], a[16:19], a[208:211], v[68:71]\n"
      "v_cvt_pk_bf16_f32 v100, v100, v104\n"
      "v_cvt_pk_bf16_f32 v101, v101, v105\n"
      "v_mfma_f32_16x16x32_bf16 v[72:75], a[20:23], a[208:211], v[72:75]\n"
      "s_waitcnt vmcnt(9)\n"
      "v_cvt_pk_bf16_f32 v102, v102, v106\n"
      "v_cvt_pk_bf16_f32 v103, v103, v107\n"
      "s_barrier\n"
      "s_add_u32 m0, s63, 47360\n"
      "s_add_u32 s52, s63, 86016\n"
      "v_mfma_f32_4x4x4_16b_bf16 v[44:47], a[244:245], a[232:233], 0\n"
      "v_mfma_f32_4x4x4_16b_bf16 v[48:51], a[244:245], a[234:235], 0\n"
      "buffer_load_dwordx4 v17, s[12:15], s52 offen offset:0  lds\n"
      "v_mfma_f32_4x4x4_16b_bf16 v[44:47], a[248:249], a[28:29], v[44:47]\n"
      "ds_read_b64_tr_b16 a[224:225], v61 offset:12544\n"
      "v_mfma_f32_4x4x4_16b_bf16 v[48:51], a[248:249], a[30:31], v[48:51]\n"
      "buffer_load_dwordx4 v18, s[12:15], s52 offen offset:1024  lds\n"
      "v_mfma_f32_4x4x4_16b_bf16 v[52:55], a[246:247], a[228:229], v[44:47]\n"
      "ds_read_b64_tr_b16 a[226:227], v61 offset:13312\n"
      "v_mfma_f32_4x4x4_16b_bf16 v[56:59], a[246:247], a[230:231], v[48:51]\n"
      "buffer_load_dwordx4 v19, s[12:15], s52 offen offset:2048  lds\n"
      "v_mfma_f32_4x4x4_16b_bf16 v[44:47], a[250:251], a[228:229], v[44:47]\n"
      "ds_read_b64_tr_b16 a[36:37], v62 offset:39680\n"
      "v_mfma_f32_4x4x4_16b_bf16 v[48:51], a[250:251], a[230:231], v[48:51]\n"
      "ds_read_b64_tr_b16 a[38:39], v62 offset:40448\n"
      "s_waitcnt lgkmcnt(4)\n"
      "v_mfma_f32_4x4x4_16b_bf16 v[116:119], a[216:217], a[180:181], 0\n"
      "ds_read_b64_tr_b16 a[220:221], v61 offset:18688\n"
      "v_mfma_f32_4x4x4_16b_bf16 v[120:123], a[216:217], a[182:183], 0\n"
      "ds_read_b64_tr_b16 a[222:223], v61 offset:19456\n"
      "s_mov_b32 m0, s64\n"
      "s_nop 0\n"
      "ds_write_addtid_b32 v100 offset:25344\n"
      "ds_write_addtid_b32 v101 offset:29440\n"
      "ds_write_addtid_b32 v102 offset:33536\n"
      "ds_write_addtid_b32 v103 offset:37632\n"
      "ds_read_b128 a[212:215], v3 offset:128\n"
      "v_cvt_pk_bf16_f32 v204, v36, v40\n"
      "v_cvt_pk_bf16_f32 v205, v37, v41\n"
      "v_cvt_pk_bf16_f32 v206, v38, v42\n"
      "v_cvt_pk_bf16_f32 v207, v39, v43\n"
      "v_cvt_pk_bf16_f32 v200, v28, v32\n"
      "v_cvt_pk_bf16_f32 v201, v29, v33\n"
      "v_cvt_pk_bf16_f32 v202, v30, v34\n"
      "v_cvt_pk_bf16_f32 v203, v31, v35\n"
      "v_cvt_pk_bf16_f32 v108, v108, v112\n"
      "v_cvt_pk_bf16_f32 v109, v109, v113\n"
      "v_cvt_pk_bf16_f32 v110, v110, v114\n"
      "v_cvt_pk_bf16_f32 v111, v111, v115\n"
      "s_add_u32 m0, s63, 0\n"
      "s_add_u32 s52, s63, 36864\n"
      "v_mfma_f32_4x4x4_16b_bf16 v[28:31], a[244:245], a[240:241], 0\n"
      "v_mfma_f32_4x4x4_16b_bf16 v[32:35], a[244:245], a[242:243], 0\n"
      "buffer_load_dwordx4 v17, s[12:15], s52 offen offset:0  lds\n"
      "v_mfma_f32_4x4x4_16b_bf16 v[28:31], a[248:249], a[32:33], v[28:31]\n"
      "ds_read_b64_tr_b16 a[232:233], v61 offset:12608\n"
      "v_mfma_f32_4x4x4_16b_bf16 v[32:35], a[248:249], a[34:35], v[32:35]\n"
      "buffer_load_dwordx4 v18, s[12:15], s52 offen offset:1024  lds\n"
      "v_mfma_f32_4x4x4_16b_bf16 v[36:39], a[246:247], a[236:237], v[28:31]\n"
      "ds_read_b64_tr_b16 a[234:235], v61 offset:13376\n"
      "v_mfma_f32_4x4x4_16b_bf16 v[40:43], a[246:247], a[238:239], v[32:35]\n"
      "ds_read_b64_tr_b16 a[40:41], v62 offset:39744\n"
      "v_mfma_f32_4x4x4_16b_bf16 v[28:31], a[250:251], a[236:237], v[28:31]\n"
      "ds_read_b64_tr_b16 a[42:43], v62 offset:40512\n"
      "v_mfma_f32_4x4x4_16b_bf16 v[32:35], a[250:251], a[238:239], v[32:35]\n"
      "ds_read_b64_tr_b16 a[228:229], v61 offset:18752\n"
      "ds_read_b64_tr_b16 a[230:231], v61 offset:19520\n"
      "s_mov_b32 m0, s64\n"
      "s_nop 0\n"
      "ds_write_addtid_b32 v108 offset:25856\n"
      "ds_write_addtid_b32 v109 offset:29952\n"
      "ds_write_addtid_b32 v110 offset:34048\n"
      "ds_write_addtid_b32 v111 offset:38144\n"
      "v_cvt_pk_bf16_f32 v196, v52, v56\n"
      "v_cvt_pk_bf16_f32 v197, v53, v57\n"
      "v_cvt_pk_bf16_f32 v198, v54, v58\n"
      "v_cvt_pk_bf16_f32 v199, v55, v59\n"
      "v_cvt_pk_bf16_f32 v192, v44, v48\n"
      "v_cvt_pk_bf16_f32 v193, v45, v49\n"
      "v_cvt_pk_bf16_f32 v194, v46, v50\n"
      "v_cvt_pk_bf16_f32 v195, v47, v51\n"
      "v_cvt_pk_bf16_f32 v116, v116, v120\n"
      "v_cvt_pk_bf16_f32 v117, v117, v121\n"
      "v_cvt_pk_bf16_f32 v118, v118, v122\n"
      "v_cvt_pk_bf16_f32 v119, v119, v123\n"
      "s_waitcnt lgkmcnt(4)\n"
      "v_mfma_f32_4x4x4_16b_bf16 v[44:47], a[244:245], a[224:225], 0\n"
      "v_mfma_f32_4x4x4_16b_bf16 v[48:51], a[244:245], a[226:227], 0\n"
      "s_add_u32 m0, s63, 0\n"
      "s_nop 0\n"
      "s_nop 0\n"
      "buffer_load_dwordx4 v19, s[12:15], s52 offen offset:2048  lds\n"
      "v_mfma_f32_4x4x4_16b_bf16 v[44:47], a[248:249], a[36:37], v[44:47]\n"
      "ds_read_b64_tr_b16 a[240:241], v61 offset:12672\n"
      "v_mfma_f32_4x4x4_16b_bf16 v[48:51], a[248:249], a[38:39], v[48:51]\n"
      "s_add_u32 m0, s63, 6144\n"
      "v_mfma_f32_4x4x4_16b_bf16 v[52:55], a[246:247], a[220:221], v[44:47]\n"
      "ds_read_b64_tr_b16 a[242:243], v61 offset:13440\n"
      "v_mfma_f32_4x4x4_16b_bf16 v[56:59], a[246:247], a[222:223], v[48:51]\n"
      "s_add_u32 s52, s63, 92160\n"
      "v_mfma_f32_4x4x4_16b_bf16 v[44:47], a[250:251], a[220:221], v[44:47]\n"
      "ds_read_b64_tr_b16 a[44:45], v62 offset:39808\n"
      "v_mfma_f32_4x4x4_16b_bf16 v[48:51], a[250:251], a[222:223], v[48:51]\n"
      "buffer_load_dwordx4 v17, s[12:15], s52 offen offset:0  lds\n"
      "ds_read_b64_tr_b16 a[46:47], v62 offset:40576\n"
      "ds_read_b64_tr_b16 a[236:237], v61 offset:18816\n"
      "ds_read_b64_tr_b16 a[238:239], v61 offset:19584\n"
      "s_mov_b32 m0, s64\n"
      "s_nop 0\n"
      "ds_write_addtid_b32 v116 offset:26368\n"
      "ds_write_addtid_b32 v117 offset:30464\n"
      "ds_write_addtid_b32 v118 offset:34560\n"
      "ds_write_addtid_b32 v119 offset:38656\n"
      "v_cvt_pk_bf16_f32 v188, v36, v40\n"
      "v_cvt_pk_bf16_f32 v189, v37, v41\n"
      "v_mfma_f32_16x16x32_bf16 v[64:67], a[24:27], a[212:215], v[64:67]\n"
      "v_cvt_pk_bf16_f32 v190, v38, v42\n"
      "v_cvt_pk_bf16_f32 v191, v39, v43\n"
      "v_mfma_f32_16x16x32_bf16 v[68:71], a[28:31], a[212:215], v[68:71]\n"
      "s_waitcnt vmcnt(10)\n"
      "v_cvt_pk_bf16_f32 v184, v28, v32\n"
      "v_cvt_pk_bf16_f32 v185, v29, v33\n"
      "v_mfma_f32_16x16x32_bf16 v[72:75], a[32:35], a[212:215], v[72:75]\n"
      "s_waitcnt lgkmcnt(0)\n"
      "v_cvt_pk_bf16_f32 v186, v30, v34\n"
      "v_cvt_pk_bf16_f32 v187, v31, v35\n"
      "s_barrier\n"
      "v_mfma_f32_4x4x4_16b_bf16 v[28:31], a[244:245], a[232:233], 0\n"
      "v_mfma_f32_4x4x4_16b_bf16 v[32:35], a[244:245], a[234:235], 0\n"
      "s_add_u32 m0, s63, 6144\n"
      "s_nop 0\n"
      "buffer_load_dwordx4 v18, s[12:15], s52 offen offset:1024  lds\n"
      "v_mfma_f32_4x4x4_16b_bf16 v[28:31], a[248:249], a[40:41], v[28:31]\n"
      "ds_read_b64_tr_b16 a[224:225], v62 offset:1536\n"
      "v_mfma_f32_4x4x4_16b_bf16 v[32:35], a[248:249], a[42:43], v[32:35]\n"
      "buffer_load_dwordx4 v19, s[12:15], s52 offen offset:2048  lds\n"
      "v_mfma_f32_4x4x4_16b_bf16 v[36:39], a[246:247], a[228:229], v[28:31]\n"
      "ds_read_b64_tr_b16 a[226:227], v62 offset:2304\n"
      "v_mfma_f32_4x4x4_16b_bf16 v[40:43], a[246:247], a[230:231], v[32:35]\n"
      "ds_read_b64_tr_b16 a[48:49], v62 offset:13824\n"
      "v_mfma_f32_4x4x4_16b_bf16 v[28:31], a[250:251], a[228:229], v[28:31]\n"
      "ds_read_b64_tr_b16 a[50:51], v62 offset:14592\n"
      "v_mfma_f32_4x4x4_16b_bf16 v[32:35], a[250:251], a[230:231], v[32:35]\n"
      "ds_read_b64_tr_b16 a[220:221], v62 offset:7680\n"
      "ds_read_b64_tr_b16 a[222:223], v62 offset:8448\n"
      "ds_read_b128 a[192:195], v4 offset:0\n"
      "ds_read_b128 a[196:199], v0 offset:0\n"
      "ds_read_b128 a[200:203], v0 offset:4096\n"
      "v_cvt_pk_bf16_f32 v180, v52, v56\n"
      "v_cvt_pk_bf16_f32 v181, v53, v57\n"
      "v_cvt_pk_bf16_f32 v182, v54, v58\n"
      "v_cvt_pk_bf16_f32 v183, v55, v59\n"
      "v_cvt_pk_bf16_f32 v176, v44, v48\n"
      "v_cvt_pk_bf16_f32 v177, v45, v49\n"
      "v_cvt_pk_bf16_f32 v178, v46, v50\n"
      "v_cvt_pk_bf16_f32 v179, v47, v51\n"
      "s_add_u32 m0, s63, 18432\n"
      "s_add_u32 s52, s63, 43008\n"
      "v_mfma_f32_4x4x4_16b_bf16 v[44:47], a[244:245], a[240:241], 0\n"
      "v_mfma_f32_4x4x4_16b_bf16 v[48:51], a[244:245], a[242:243], 0\n"
      "buffer_load_dwordx4 v17, s[12:15], s52 offen offset:0  lds\n"
      "v_mfma_f32_4x4x4_16b_bf16 v[44:47], a[248:249], a[44:45], v[44:47]\n"
      "ds_read_b64_tr_b16 a[232:233], v62 offset:1600\n"
      "v_mfma_f32_4x4x4_16b_bf16 v[48:51], a[248:249], a[46:47], v[48:51]\n"
      "buffer_load_dwordx4 v18, s[12:15], s52 offen offset:1024  lds\n"
      "v_mfma_f32_4x4x4_16b_bf16 v[52:55], a[246:247], a[236:237], v[44:47]\n"
      "ds_read_b64_tr_b16 a[234:235], v62 offset:2368\n"
      "v_mfma_f32_4x4x4_16b_bf16 v[56:59], a[246:247], a[238:239], v[48:51]\n"
      "ds_read_b64_tr_b16 a[52:53], v62 offset:13888\n"
      "v_mfma_f32_4x4x4_16b_bf16 v[44:47], a[250:251], a[236:237], v[44:47]\n"
      "ds_read_b64_tr_b16 a[54:55], v62 offset:14656\n"
      "v_mfma_f32_4x4x4_16b_bf16 v[48:51], a[250:251], a[238:239], v[48:51]\n"
      "ds_read_b64_tr_b16 a[228:229], v62 offset:7744\n"
      "ds_read_b64_tr_b16 a[230:231], v62 offset:8512\n"
      "ds_read_b128 a[204:207], v0 offset:64\n"
      "ds_read_b128 a[208:211], v0 offset:4160\n"
      "ds_read_b128 a[212:215], v0 offset:128\n"
      "v_cvt_pk_bf16_f32 v172, v36, v40\n"
      "v_cvt_pk_bf16_f32 v173, v37, v41\n"
      "v_cvt_pk_bf16_f32 v174, v38, v42\n"
      "v_cvt_pk_bf16_f32 v175, v39, v43\n"
      "v_cvt_pk_bf16_f32 v168, v28, v32\n"
      "v_cvt_pk_bf16_f32 v169, v29, v33\n"
      "v_cvt_pk_bf16_f32 v170, v30, v34\n"
      "v_cvt_pk_bf16_f32 v171, v31, v35\n"
      "s_nop 0\n"
      "s_waitcnt lgkmcnt(6)\n"
      "v_mfma_f32_4x4x4_16b_bf16 v[28:31], a[244:245], a[224:225], 0\n"
      "v_mfma_f32_4x4x4_16b_bf16 v[32:35], a[244:245], a[226:227], 0\n"
      "buffer_load_dwordx4 v19, s[12:15], s52 offen offset:2048  lds\n"
      "v_mfma_f32_4x4x4_16b_bf16 v[28:31], a[248:249], a[48:49], v[28:31]\n"
      "ds_read_b64_tr_b16 a[240:241], v62 offset:1664\n"
      "v_mfma_f32_4x4x4_16b_bf16 v[32:35], a[248:249], a[50:51], v[32:35]\n"
      "s_add_u32 m0, s63, 57600\n"
      "v_mfma_f32_4x4x4_16b_bf16 v[36:39], a[246:247], a[220:221], v[28:31]\n"
      "ds_read_b64_tr_b16 a[242:243], v62 offset:2432\n"
      "v_mfma_f32_4x4x4_16b_bf16 v[40:43], a[246:247], a[222:223], v[32:35]\n"
      "s_add_u32 s52, s63, 98304\n"
      "v_mfma_f32_4x4x4_16b_bf16 v[28:31], a[250:251], a[220:221], v[28:31]\n"
      "ds_read_b64_tr_b16 a[56:57], v62 offset:13952\n"
      "v_mfma_f32_4x4x4_16b_bf16 v[32:35], a[250:251], a[222:223], v[32:35]\n"
      "buffer_load_dwordx4 v17, s[12:15], s52 offen offset:0  lds\n"
      "ds_read_b64_tr_b16 a[58:59], v62 offset:14720\n"
      "ds_read_b64_tr_b16 a[236:237], v62 offset:7808\n"
      "ds_read_b64_tr_b16 a[238:239], v62 offset:8576\n"
      "v_mfma_f32_16x16x32_bf16 v[64:67], a[36:39], a[192:195], v[64:67]\n"
      "v_cvt_pk_bf16_f32 v164, v52, v56\n"
      "v_cvt_pk_bf16_f32 v165, v53, v57\n"
      "v_mfma_f32_16x16x32_bf16 v[68:71], a[40:43], a[192:195], v[68:71]\n"
      "v_cvt_pk_bf16_f32 v166, v54, v58\n"
      "v_cvt_pk_bf16_f32 v167, v55, v59\n"
      "v_mfma_f32_16x16x32_bf16 v[72:75], a[44:47], a[192:195], v[72:75]\n"
      "s_nop 0\n"
      "v_cvt_pk_bf16_f32 v160, v44, v48\n"
      "v_cvt_pk_bf16_f32 v161, v45, v49\n"
      "v_mfma_f32_16x16x32_bf16 v[76:79], v[252:255], a[196:199], 0\n"
      "s_waitcnt vmcnt(10)\n"
      "v_cvt_pk_bf16_f32 v162, v46, v50\n"
      "v_cvt_pk_bf16_f32 v163, v47, v51\n"
      "s_barrier\n"
      "v_mfma_f32_4x4x4_16b_bf16 v[44:47], a[244:245], a[232:233], 0\n"
      "v_mfma_f32_4x4x4_16b_bf16 v[48:51], a[244:245], a[234:235], 0\n"
      "buffer_load_dwordx4 v18, s[12:15], s52 offen offset:1024  lds\n"
      "s_waitcnt lgkmcnt(0)\n"
      "v_mfma_f32_4x4x4_16b_bf16 v[44:47], a[248:249], a[52:53], v[44:47]\n"
      "ds_read_b64_tr_b16 a[224:225], v60 offset:41216\n"
      "v_mfma_f32_4x4x4_16b_bf16 v[48:51], a[248:249], a[54:55], v[48:51]\n"
      "buffer_load_dwordx4 v19, s[12:15], s52 offen offset:2048  lds\n"
      "v_mfma_f32_4x4x4_16b_bf16 v[52:55], a[246:247], a[228:229], v[44:47]\n"
      "ds_read_b64_tr_b16 a[226:227], v60 offset:41984\n"
      "v_mfma_f32_4x4x4_16b_bf16 v[56:59], a[246:247], a[230:231], v[48:51]\n"
      "ds_read_b64_tr_b16 a[60:61], v62 offset:19968\n"
      "v_mfma_f32_4x4x4_16b_bf16 v[44:47], a[250:251], a[228:229], v[44:47]\n"
      "ds_read_b64_tr_b16 a[62:63], v62 offset:20736\n"
      "v_mfma_f32_4x4x4_16b_bf16 v[48:51], a[250:251], a[230:231], v[48:51]\n"
      "ds_read_b64_tr_b16 a[220:221], v60 offset:47360\n"
      "ds_read_b64_tr_b16 a[222:223], v60 offset:48128\n"
      "ds_read_b128 a[192:195], v0 offset:4224\n"
      "v_mfma_f32_16x16x32_bf16 v[80:83], v[244:247], a[196:199], 0\n"
      "v_cvt_pk_bf16_f32 v156, v36, v40\n"
      "v_cvt_pk_bf16_f32 v157, v37, v41\n"
      "v_mfma_f32_16x16x32_bf16 v[84:87], v[236:239], a[196:199], 0\n"
      "v_cvt_pk_bf16_f32 v158, v38, v42\n"
      "v_cvt_pk_bf16_f32 v159, v39, v43\n"
      "v_mfma_f32_16x16x32_bf16 v[88:91], v[248:251], a[200:203], 0\n"
      "v_cvt_pk_bf16_f32 v152, v28, v32\n"
      "v_cvt_pk_bf16_f32 v153, v29, v33\n"
      "v_mfma_f32_16x16x32_bf16 v[92:95], v[240:243], a[200:203], 0\n"
      "s_add_u32 m0, s63, 69888\n"
      "v_cvt_pk_bf16_f32 v154, v30, v34\n"
      "v_cvt_pk_bf16_f32 v155, v31, v35\n"
      "s_add_u32 s52, s63, 49152\n"
      "v_mfma_f32_4x4x4_16b_bf16 v[28:31], a[244:245], a[240:241], 0\n"
      "v_mfma_f32_4x4x4_16b_bf16 v[32:35], a[244:245], a[242:243], 0\n"
      "buffer_load_dwordx4 v17, s[12:15], s52 offen offset:0  lds\n"
      "v_mfma_f32_4x4x4_16b_bf16 v[28:31], a[248:249], a[56:57], v[28:31]\n"
      "ds_read_b64_tr_b16 a[232:233], v60 offset:41280\n"
      "v_mfma_f32_4x4x4_16b_bf16 v[32:35], a[248:249], a[58:59], v[32:35]\n"
      "buffer_load_dwordx4 v18, s[12:15], s52 offen offset:1024  lds\n"
      "v_mfma_f32_4x4x4_16b_bf16 v[36:39], a[246:247], a[236:237], v[28:31]\n"
      "ds_read_b64_tr_b16 a[234:235], v60 offset:42048\n"
      "v_mfma_f32_4x4x4_16b_bf16 v[40:43], a[246:247], a[238:239], v[32:35]\n"
      "ds_read_b64_tr_b16 a[64:65], v62 offset:20032\n"
      "v_mfma_f32_4x4x4_16b_bf16 v[28:31], a[250:251], a[236:237], v[28:31]\n"
      "ds_read_b64_tr_b16 a[66:67], v62 offset:20800\n"
      "v_mfma_f32_4x4x4_16b_bf16 v[32:35], a[250:251], a[238:239], v[32:35]\n"
      "ds_read_b64_tr_b16 a[228:229], v60 offset:47424\n"
      "ds_read_b64_tr_b16 a[230:231], v60 offset:48192\n"
      "ds_read_b128 a[196:199], v1 offset:0\n"
      "v_mfma_f32_16x16x32_bf16 v[96:99], v[232:235], a[200:203], 0\n"
      "v_cvt_pk_bf16_f32 v148, v52, v56\n"
      "v_cvt_pk_bf16_f32 v149, v53, v57\n"
      "v_mfma_f32_16x16x32_bf16 v[76:79], v[228:231], a[204:207], v[76:79]\n"
      "v_cvt_pk_bf16_f32 v150, v54, v58\n"
      "v_cvt_pk_bf16_f32 v151, v55, v59\n"
      "v_mfma_f32_16x16x32_bf16 v[80:83], v[220:223], a[204:207], v[80:83]\n"
      "s_nop 0\n"
      "v_cvt_pk_bf16_f32 v144, v44, v48\n"
      "v_cvt_pk_bf16_f32 v145, v45, v49\n"
      "v_mfma_f32_16x16x32_bf16 v[84:87], v[212:215], a[204:207], v[84:87]\n"
      "s_waitcnt lgkmcnt(2)\n"
      "v_cvt_pk_bf16_f32 v146, v46, v50\n"
      "v_cvt_pk_bf16_f32 v147, v47, v51\n"
      "v_mfma_f32_4x4x4_16b_bf16 v[44:47], a[244:245], a[224:225], 0\n"
      "v_mfma_f32_4x4x4_16b_bf16 v[48:51], a[244:245], a[226:227], 0\n"
      "buffer_load_dwordx4 v19, s[12:15], s52 offen offset:2048  lds\n"
      "v_mfma_f32_4x4x4_16b_bf16 v[44:47], a[248:249], a[60:61], v[44:47]\n"
      "ds_read_b64_tr_b16 a[240:241], v60 offset:41344\n"
      "v_mfma_f32_4x4x4_16b_bf16 v[48:51], a[248:249], a[62:63], v[48:51]\n"
      "s_add_u32 m0, s63, 76032\n"
      "v_mfma_f32_4x4x4_16b_bf16 v[52:55], a[246:247], a[220:221], v[44:47]\n"
      "ds_read_b64_tr_b16 a[242:243], v60 offset:42112\n"
      "v_mfma_f32_4x4x4_16b_bf16 v[56:59], a[246:247], a[222:223], v[48:51]\n"
      "s_add_u32 s52, s63, 104448\n"
      "v_mfma_f32_4x4x4_16b_bf16 v[44:47], a[250:251], a[220:221], v[44:47]\n"
      "ds_read_b64_tr_b16 a[68:69], v62 offset:20096\n"
      "v_mfma_f32_4x4x4_16b_bf16 v[48:51], a[250:251], a[222:223], v[48:51]\n"
      "buffer_load_dwordx4 v17, s[12:15], s52 offen offset:0  lds\n"
      "ds_read_b64_tr_b16 a[70:71], v62 offset:20864\n"
      "ds_read_b64_tr_b16 a[236:237], v60 offset:47488\n"
      "ds_read_b64_tr_b16 a[238:239], v60 offset:48256\n"
      "ds_read_b128 a[200:203], v1 offset:4096\n"
      "ds_read_b128 a[204:207], v4 offset:64\n"
      "v_mfma_f32_16x16x32_bf16 v[88:91], v[224:227], a[208:211], v[88:91]\n"
      "v_cvt_pk_bf16_f32 v140, v36, v40\n"
      "v_cvt_pk_bf16_f32 v141, v37, v41\n"
      "v_mfma_f32_16x16x32_bf16 v[92:95], v[216:219], a[208:211], v[92:95]\n"
      "v_cvt_pk_bf16_f32 v142, v38, v42\n"
      "v_cvt_pk_bf16_f32 v143, v39, v43\n"
      "v_mfma_f32_16x16x32_bf16 v[96:99], v[208:211], a[208:211], v[96:99]\n"
      "s_nop 0\n"
      "v_cvt_pk_bf16_f32 v136, v28, v32\n"
      "v_cvt_pk_bf16_f32 v137, v29, v33\n"
      "v_mfma_f32_16x16x32_bf16 v[76:79], v[204:207], a[212:215], v[76:79]\n"
      "s_waitcnt vmcnt(10)\n"
      "v_cvt_pk_bf16_f32 v138, v30, v34\n"
      "v_cvt_pk_bf16_f32 v139, v31, v35\n"
      "s_barrier\n"
      "v_mfma_f32_4x4x4_16b_bf16 v[28:31], a[244:245], a[232:233], 0\n"
      "v_mfma_f32_4x4x4_16b_bf16 v[32:35], a[244:245], a[234:235], 0\n"
      "buffer_load_dwordx4 v18, s[12:15], s52 offen offset:1024  lds\n"
      "v_mfma_f32_4x4x4_16b_bf16 v[28:31], a[248:249], a[64:65], v[28:31]\n"
      "ds_read_b64_tr_b16 a[224:225], v60 offset:0\n"
      "v_mfma_f32_4x4x4_16b_bf16 v[32:35], a[248:249], a[66:67], v[32:35]\n"
      "buffer_load_dwordx4 v19, s[12:15], s52 offen offset:2048  lds\n"
      "s_waitcnt lgkmcnt(1)\n"
      "v_mfma_f32_4x4x4_16b_bf16 v[36:39], a[246:247], a[228:229], v[28:31]\n"
      "ds_read_b64_tr_b16 a[226:227], v60 offset:768\n"
      "v_mfma_f32_4x4x4_16b_bf16 v[40:43], a[246:247], a[230:231], v[32:35]\n"
      "ds_read_b64_tr_b16 a[72:73], v60 offset:12288\n"
      "v_mfma_f32_4x4x4_16b_bf16 v[28:31], a[250:251], a[228:229], v[28:31]\n"
      "ds_read_b64_tr_b16 a[74:75], v60 offset:13056\n"
      "v_mfma_f32_4x4x4_16b_bf16 v[32:35], a[250:251], a[230:231], v[32:35]\n"
      "ds_read_b64_tr_b16 a[220:221], v60 offset:6144\n"
      "ds_read_b64_tr_b16 a[222:223], v60 offset:6912\n"
      "ds_read_b128 a[208:211], v1 offset:64\n"
      "v_mfma_f32_16x16x32_bf16 v[80:83], v[196:199], a[212:215], v[80:83]\n"
      "v_cvt_pk_bf16_f32 v132, v52, v56\n"
      "v_cvt_pk_bf16_f32 v133, v53, v57\n"
      "v_mfma_f32_16x16x32_bf16 v[84:87], v[188:191], a[212:215], v[84:87]\n"
      "v_cvt_pk_bf16_f32 v134, v54, v58\n"
      "v_cvt_pk_bf16_f32 v135, v55, v59\n"
      "v_mfma_f32_16x16x32_bf16 v[88:91], v[200:203], a[192:195], v[88:91]\n"
      "v_cvt_pk_bf16_f32 v128, v44, v48\n"
      "v_cvt_pk_bf16_f32 v129, v45, v49\n"
      "v_mfma_f32_16x16x32_bf16 v[92:95], v[192:195], a[192:195], v[92:95]\n"
      "v_cvt_pk_bf16_f32 v130, v46, v50\n"
      "v_cvt_pk_bf16_f32 v131, v47, v51\n"
      "v_mfma_f32_4x4x4_16b_bf16 v[44:47], a[244:245], a[240:241], 0\n"
      "v_mfma_f32_4x4x4_16b_bf16 v[48:51], a[244:245], a[242:243], 0\n"
      "ds_read_b64_tr_b16 a[232:233], v60 offset:64\n"
      "v_mfma_f32_4x4x4_16b_bf16 v[44:47], a[248:249], a[68:69], v[44:47]\n"
      "ds_read_b64_tr_b16 a[234:235], v60 offset:832\n"
      "v_mfma_f32_4x4x4_16b_bf16 v[48:51], a[248:249], a[70:71], v[48:51]\n"
      "ds_read_b64_tr_b16 a[76:77], v60 offset:12352\n"
      "v_mfma_f32_4x4x4_16b_bf16 v[52:55], a[246:247], a[236:237], v[44:47]\n"
      "ds_read_b64_tr_b16 a[78:79], v60 offset:13120\n"
      "v_mfma_f32_4x4x4_16b_bf16 v[56:59], a[246:247], a[238:239], v[48:51]\n"
      "ds_read_b64_tr_b16 a[228:229], v60 offset:6208\n"
      "v_mfma_f32_4x4x4_16b_bf16 v[44:47], a[250:251], a[236:237], v[44:47]\n"
      "ds_read_b64_tr_b16 a[230:231], v60 offset:6976\n"
      "v_mfma_f32_4x4x4_16b_bf16 v[48:51], a[250:251], a[238:239], v[48:51]\n"
      "ds_read_b128 a[212:215], v1 offset:4160\n"
      "v_mfma_f32_16x16x32_bf16 v[96:99], v[184:187], a[192:195], v[96:99]\n"
      "v_cvt_pk_bf16_f32 v124, v36, v40\n"
      "v_cvt_pk_bf16_f32 v125, v37, v41\n"
      "v_mfma_f32_16x16x32_bf16 v[76:79], v[180:183], a[196:199], v[76:79]\n"
      "v_cvt_pk_bf16_f32 v126, v38, v42\n"
      "v_cvt_pk_bf16_f32 v127, v39, v43\n"
      "v_mfma_f32_16x16x32_bf16 v[80:83], v[172:175], a[196:199], v[80:83]\n"
      "s_nop 0\n"
      "v_cvt_pk_bf16_f32 v120, v28, v32\n"
      "v_cvt_pk_bf16_f32 v121, v29, v33\n"
      "v_mfma_f32_16x16x32_bf16 v[84:87], v[164:167], a[196:199], v[84:87]\n"
      "s_waitcnt lgkmcnt(1)\n"
      "v_cvt_pk_bf16_f32 v122, v30, v34\n"
      "v_cvt_pk_bf16_f32 v123, v31, v35\n"
      "v_mfma_f32_4x4x4_16b_bf16 v[28:31], a[244:245], a[224:225], 0\n"
      "v_mfma_f32_4x4x4_16b_bf16 v[32:35], a[244:245], a[226:227], 0\n"
      "ds_read_b64_tr_b16 a[240:241], v60 offset:128\n"
      "v_mfma_f32_4x4x4_16b_bf16 v[28:31], a[248:249], a[72:73], v[28:31]\n"
      "ds_read_b64_tr_b16 a[242:243], v60 offset:896\n"
      "v_mfma_f32_4x4x4_16b_bf16 v[32:35], a[248:249], a[74:75], v[32:35]\n"
      "ds_read_b64_tr_b16 a[80:81], v60 offset:12416\n"
      "v_mfma_f32_4x4x4_16b_bf16 v[36:39], a[246:247], a[220:221], v[28:31]\n"
      "ds_read_b64_tr_b16 a[82:83], v60 offset:13184\n"
      "v_mfma_f32_4x4x4_16b_bf16 v[40:43], a[246:247], a[222:223], v[32:35]\n"
      "ds_read_b64_tr_b16 a[236:237], v60 offset:6272\n"
      "v_mfma_f32_4x4x4_16b_bf16 v[28:31], a[250:251], a[220:221], v[28:31]\n"
      "ds_read_b64_tr_b16 a[238:239], v60 offset:7040\n"
      "v_mfma_f32_4x4x4_16b_bf16 v[32:35], a[250:251], a[222:223], v[32:35]\n"
      "ds_read_b128 a[192:195], v4 offset:128\n"
      "ds_read_b128 a[196:199], v1 offset:128\n"
      "v_mfma_f32_16x16x32_bf16 v[88:91], v[176:179], a[200:203], v[88:91]\n"
      "v_cvt_pk_bf16_f32 v116, v52, v56\n"
      "v_cvt_pk_bf16_f32 v117, v53, v57\n"
      "v_mfma_f32_16x16x32_bf16 v[92:95], v[168:171], a[200:203], v[92:95]\n"
      "v_cvt_pk_bf16_f32 v118, v54, v58\n"
      "v_cvt_pk_bf16_f32 v119, v55, v59\n"
      "v_mfma_f32_16x16x32_bf16 v[96:99], v[160:163], a[200:203], v[96:99]\n"
      "v_cvt_pk_bf16_f32 v112, v44, v48\n"
      "v_cvt_pk_bf16_f32 v113, v45, v49\n"
      "v_mfma_f32_16x16x32_bf16 v[64:67], a[48:51], a[204:207], v[64:67]\n"
      "s_waitcnt vmcnt(6)\n"
      "v_cvt_pk_bf16_f32 v114, v46, v50\n"
      "v_cvt_pk_bf16_f32 v115, v47, v51\n"
      "s_barrier\n"
      "v_mfma_f32_4x4x4_16b_bf16 v[44:47], a[244:245], a[232:233], 0\n"
      "v_mfma_f32_4x4x4_16b_bf16 v[48:51], a[244:245], a[234:235], 0\n"
      "ds_read_b64_tr_b16 a[224:225], v60 offset:18432\n"
      "v_mfma_f32_4x4x4_16b_bf16 v[44:47], a[248:249], a[76:77], v[44:47]\n"
      "ds_read_b64_tr_b16 a[226:227], v60 offset:19200\n"
      "v_mfma_f32_4x4x4_16b_bf16 v[48:51], a[248:249], a[78:79], v[48:51]\n"
      "ds_read_b64_tr_b16 a[84:85], v61 offset:6400\n"
      "v_mfma_f32_4x4x4_16b_bf16 v[52:55], a[246:247], a[228:229], v[44:47]\n"
      "ds_read_b64_tr_b16 a[86:87], v61 offset:7168\n"
      "v_mfma_f32_4x4x4_16b_bf16 v[56:59], a[246:247], a[230:231], v[48:51]\n"
      "ds_read_b64_tr_b16 a[220:221], v61 offset:256\n"
      "v_mfma_f32_4x4x4_16b_bf16 v[44:47], a[250:251], a[228:229], v[44:47]\n"
      "ds_read_b64_tr_b16 a[222:223], v61 offset:1024\n"
      "v_mfma_f32_4x4x4_16b_bf16 v[48:51], a[250:251], a[230:231], v[48:51]\n"
      "ds_read_b128 a[200:203], v1 offset:4224\n"
      "v_mfma_f32_16x16x32_bf16 v[68:71], a[52:55], a[204:207], v[68:71]\n"
      "v_cvt_pk_bf16_f32 v108, v36, v40\n"
      "v_cvt_pk_bf16_f32 v109, v37, v41\n"
      "v_mfma_f32_16x16x32_bf16 v[72:75], a[56:59], a[204:207], v[72:75]\n"
      "v_cvt_pk_bf16_f32 v110, v38, v42\n"
      "v_cvt_pk_bf16_f32 v111, v39, v43\n"
      "v_mfma_f32_16x16x32_bf16 v[76:79], v[156:159], a[208:211], v[76:79]\n"
      "v_cvt_pk_bf16_f32 v104, v28, v32\n"
      "v_cvt_pk_bf16_f32 v105, v29, v33\n"
      "v_mfma_f32_16x16x32_bf16 v[80:83], v[148:151], a[208:211], v[80:83]\n"
      "s_nop 0\n"
      "s_waitcnt lgkmcnt(1)\n"
      "v_cvt_pk_bf16_f32 v106, v30, v34\n"
      "v_cvt_pk_bf16_f32 v107, v31, v35\n"
      "v_mfma_f32_4x4x4_16b_bf16 v[28:31], a[244:245], a[240:241], 0\n"
      "v_mfma_f32_4x4x4_16b_bf16 v[32:35], a[244:245], a[242:243], 0\n"
      "ds_read_b64_tr_b16 a[232:233], v60 offset:18496\n"
      "v_mfma_f32_4x4x4_16b_bf16 v[28:31], a[248:249], a[80:81], v[28:31]\n"
      "ds_read_b64_tr_b16 a[234:235], v60 offset:19264\n"
      "v_mfma_f32_4x4x4_16b_bf16 v[32:35], a[248:249], a[82:83], v[32:35]\n"
      "ds_read_b64_tr_b16 a[88:89], v61 offset:6464\n"
      "v_mfma_f32_4x4x4_16b_bf16 v[36:39], a[246:247], a[236:237], v[28:31]\n"
      "ds_read_b64_tr_b16 a[90:91], v61 offset:7232\n"
      "v_mfma_f32_4x4x4_16b_bf16 v[40:43], a[246:247], a[238:239], v[32:35]\n"
      "ds_read_b64_tr_b16 a[228:229], v61 offset:320\n"
      "v_mfma_f32_4x4x4_16b_bf16 v[28:31], a[250:251], a[236:237], v[28:31]\n"
      "ds_read_b64_tr_b16 a[230:231], v61 offset:1088\n"
      "v_mfma_f32_4x4x4_16b_bf16 v[32:35], a[250:251], a[238:239], v[32:35]\n"
      "ds_read_b128 a[204:207], v5 offset:0\n"
      "v_mfma_f32_16x16x32_bf16 v[84:87], v[140:143], a[208:211], v[84:87]\n"
      "v_cvt_pk_bf16_f32 v100, v52, v56\n"
      "v_cvt_pk_bf16_f32 v101, v53, v57\n"
      "v_mfma_f32_16x16x32_bf16 v[88:91], v[152:155], a[212:215], v[88:91]\n"
      "v_cvt_pk_bf16_f32 v102, v54, v58\n"
      "v_cvt_pk_bf16_f32 v103, v55, v59\n"
      "v_mfma_f32_16x16x32_bf16 v[92:95], v[144:147], a[212:215], v[92:95]\n"
      "v_cvt_pk_bf16_f32 v44, v44, v48\n"
      "v_cvt_pk_bf16_f32 v45, v45, v49\n"
      "v_mfma_f32_16x16x32_bf16 v[96:99], v[136:139], a[212:215], v[96:99]\n"
      "v_cvt_pk_bf16_f32 v46, v46, v50\n"
      "v_cvt_pk_bf16_f32 v47, v47, v51\n"
      "v_mfma_f32_16x16x32_bf16 v[64:67], a[60:63], a[192:195], v[64:67]\n"
      "v_accvgpr_write_b32 a164, v44\n"
      "v_accvgpr_write_b32 a165, v45\n"
      "v_mfma_f32_16x16x32_bf16 v[68:71], a[64:67], a[192:195], v[68:71]\n"
      "v_accvgpr_write_b32 a166, v46\n"
      "v_accvgpr_write_b32 a167, v47\n"
      "v_mfma_f32_4x4x4_16b_bf16 v[44:47], a[244:245], a[224:225], 0\n"
      "v_mfma_f32_4x4x4_16b_bf16 v[48:51], a[244:245], a[226:227], 0\n"
      "ds_read_b64_tr_b16 a[240:241], v60 offset:18560\n"
      "v_mfma_f32_4x4x4_16b_bf16 v[44:47], a[248:249], a[84:85], v[44:47]\n"
      "ds_read_b64_tr_b16 a[242:243], v60 offset:19328\n"
      "v_mfma_f32_4x4x4_16b_bf16 v[48:51], a[248:249], a[86:87], v[48:51]\n"
      "ds_read_b64_tr_b16 a[92:93], v61 offset:6528\n"
      "v_mfma_f32_4x4x4_16b_bf16 v[52:55], a[246:247], a[220:221], v[44:47]\n"
      "ds_read_b64_tr_b16 a[94:95], v61 offset:7296\n"
      "v_mfma_f32_4x4x4_16b_bf16 v[56:59], a[246:247], a[222:223], v[48:51]\n"
      "ds_read_b64_tr_b16 a[236:237], v61 offset:384\n"
      "v_mfma_f32_4x4x4_16b_bf16 v[44:47], a[250:251], a[220:221], v[44:47]\n"
      "ds_read_b64_tr_b16 a[238:239], v61 offset:1152\n"
      "v_mfma_f32_4x4x4_16b_bf16 v[48:51], a[250:251], a[222:223], v[48:51]\n"
      "ds_read_b128 a[208:211], v2 offset:0\n"
      "ds_read_b128 a[212:215], v2 offset:4096\n"
      "v_cvt_pk_bf16_f32 v36, v36, v40\n"
      "v_cvt_pk_bf16_f32 v37, v37, v41\n"
      "v_cvt_pk_bf16_f32 v38, v38, v42\n"
      "v_cvt_pk_bf16_f32 v39, v39, v43\n"
      "v_mfma_f32_16x16x32_bf16 v[72:75], a[68:71], a[192:195], v[72:75]\n"
      "v_accvgpr_write_b32 a160, v36\n"
      "v_accvgpr_write_b32 a161, v37\n"
      "v_mfma_f32_16x16x32_bf16 v[76:79], v[132:135], a[196:199], v[76:79]\n"
      "v_accvgpr_write_b32 a162, v38\n"
      "v_accvgpr_write_b32 a163, v39\n"
      "v_mfma_f32_16x16x32_bf16 v[80:83], v[124:127], a[196:199], v[80:83]\n"
      "v_cvt_pk_bf16_f32 v28, v28, v32\n"
      "v_cvt_pk_bf16_f32 v29, v29, v33\n"
      "v_mfma_f32_16x16x32_bf16 v[84:87], v[116:119], a[196:199], v[84:87]\n"
      "s_waitcnt lgkmcnt(1)\n"
      "v_cvt_pk_bf16_f32 v30, v30, v34\n"
      "v_cvt_pk_bf16_f32 v31, v31, v35\n"
      "v_mfma_f32_16x16x32_bf16 v[88:91], v[128:131], a[200:203], v[88:91]\n"
      "s_nop 0\n"
      "v_accvgpr_write_b32 a156, v28\n"
      "v_accvgpr_write_b32 a157, v29\n"
      "v_mfma_f32_16x16x32_bf16 v[92:95], v[120:123], a[200:203], v[92:95]\n"
      "s_waitcnt vmcnt(0)\n"
      "v_accvgpr_write_b32 a158, v30\n"
      "v_accvgpr_write_b32 a159, v31\n"
      "s_barrier\n"
      "v_mfma_f32_4x4x4_16b_bf16 v[28:31], a[244:245], a[232:233], 0\n"
      "v_mfma_f32_4x4x4_16b_bf16 v[32:35], a[244:245], a[234:235], 0\n"
      "ds_read_b64_tr_b16 a[224:225], v61 offset:12544\n"
      "v_mfma_f32_4x4x4_16b_bf16 v[28:31], a[248:249], a[88:89], v[28:31]\n"
      "ds_read_b64_tr_b16 a[226:227], v61 offset:13312\n"
      "v_mfma_f32_4x4x4_16b_bf16 v[32:35], a[248:249], a[90:91], v[32:35]\n"
      "ds_read_b64_tr_b16 a[96:97], v62 offset:39680\n"
      "v_mfma_f32_4x4x4_16b_bf16 v[36:39], a[246:247], a[228:229], v[28:31]\n"
      "ds_read_b64_tr_b16 a[98:99], v62 offset:40448\n"
      "v_mfma_f32_4x4x4_16b_bf16 v[40:43], a[246:247], a[230:231], v[32:35]\n"
      "ds_read_b64_tr_b16 a[220:221], v61 offset:18688\n"
      "v_mfma_f32_4x4x4_16b_bf16 v[28:31], a[250:251], a[228:229], v[28:31]\n"
      "ds_read_b64_tr_b16 a[222:223], v61 offset:19456\n"
      "v_mfma_f32_4x4x4_16b_bf16 v[32:35], a[250:251], a[230:231], v[32:35]\n"
      "ds_read_b128 a[192:195], v5 offset:64\n"
      "v_cvt_pk_bf16_f32 v52, v52, v56\n"
      "v_cvt_pk_bf16_f32 v53, v53, v57\n"
      "v_cvt_pk_bf16_f32 v54, v54, v58\n"
      "v_cvt_pk_bf16_f32 v55, v55, v59\n"
      "v_mfma_f32_16x16x32_bf16 v[96:99], v[112:115], a[200:203], v[96:99]\n"
      "v_accvgpr_write_b32 a152, v52\n"
      "v_accvgpr_write_b32 a153, v53\n"
      "v_mfma_f32_16x16x32_bf16 v[64:67], a[72:75], a[204:207], v[64:67]\n"
      "v_accvgpr_write_b32 a154, v54\n"
      "v_accvgpr_write_b32 a155, v55\n"
      "v_mfma_f32_16x16x32_bf16 v[68:71], a[76:79], a[204:207], v[68:71]\n"
      "v_cvt_pk_bf16_f32 v44, v44, v48\n"
      "v_cvt_pk_bf16_f32 v45, v45, v49\n"
      "v_mfma_f32_16x16x32_bf16 v[72:75], a[80:83], a[204:207], v[72:75]\n"
      "v_cvt_pk_bf16_f32 v46, v46, v50\n"
      "v_cvt_pk_bf16_f32 v47, v47, v51\n"
      "v_mfma_f32_16x16x32_bf16 v[76:79], v[108:111], a[208:211], v[76:79]\n"
      "s_nop 0\n"
      "v_accvgpr_write_b32 a148, v44\n"
      "v_accvgpr_write_b32 a149, v45\n"
      "v_mfma_f32_16x16x32_bf16 v[80:83], v[100:103], a[208:211], v[80:83]\n"
      "s_nop 0\n"
      "v_accvgpr_write_b32 a150, v46\n"
      "v_accvgpr_write_b32 a151, v47\n"
      "v_mfma_f32_4x4x4_16b_bf16 v[44:47], a[244:245], a[240:241], 0\n"
      "v_mfma_f32_4x4x4_16b_bf16 v[48:51], a[244:245], a[242:243], 0\n"
      "ds_read_b64_tr_b16 a[232:233], v61 offset:12608\n"
      "v_mfma_f32_4x4x4_16b_bf16 v[44:47], a[248:249], a[92:93], v[44:47]\n"
      "ds_read_b64_tr_b16 a[234:235], v61 offset:13376\n"
      "v_mfma_f32_4x4x4_16b_bf16 v[48:51], a[248:249], a[94:95], v[48:51]\n"
      "ds_read_b64_tr_b16 a[100:101], v62 offset:39744\n"
      "v_mfma_f32_4x4x4_16b_bf16 v[52:55], a[246:247], a[236:237], v[44:47]\n"
      "ds_read_b64_tr_b16 a[102:103], v62 offset:40512\n"
      "v_mfma_f32_4x4x4_16b_bf16 v[56:59], a[246:247], a[238:239], v[48:51]\n"
      "ds_read_b64_tr_b16 a[228:229], v61 offset:18752\n"
      "v_mfma_f32_4x4x4_16b_bf16 v[44:47], a[250:251], a[236:237], v[44:47]\n"
      "ds_read_b64_tr_b16 a[230:231], v61 offset:19520\n"
      "v_mfma_f32_4x4x4_16b_bf16 v[48:51], a[250:251], a[238:239], v[48:51]\n"
      "ds_read_b128 a[196:199], v2 offset:64\n"
      "ds_read_b128 a[200:203], v2 offset:4160\n"
      "v_cvt_pk_bf16_f32 v36, v36, v40\n"
      "v_cvt_pk_bf16_f32 v37, v37, v41\n"
      "v_cvt_pk_bf16_f32 v38, v38, v42\n"
      "v_cvt_pk_bf16_f32 v39, v39, v43\n"
      "v_accvgpr_write_b32 a144, v36\n"
      "v_accvgpr_write_b32 a145, v37\n"
      "v_accvgpr_write_b32 a146, v38\n"
      "v_accvgpr_write_b32 a147, v39\n"
      "v_mfma_f32_16x16x32_bf16 v[84:87], a[160:163], a[208:211], v[84:87]\n"
      "s_waitcnt lgkmcnt(3)\n"
      "v_cvt_pk_bf16_f32 v28, v28, v32\n"
      "v_cvt_pk_bf16_f32 v29, v29, v33\n"
      "v_mfma_f32_16x16x32_bf16 v[88:91], v[104:107], a[212:215], v[88:91]\n"
      "s_nop 0\n"
      "v_cvt_pk_bf16_f32 v30, v30, v34\n"
      "v_cvt_pk_bf16_f32 v31, v31, v35\n"
      "v_mfma_f32_16x16x32_bf16 v[92:95], a[164:167], a[212:215], v[92:95]\n"
      "s_nop 0\n"
      "v_accvgpr_write_b32 a140, v28\n"
      "v_accvgpr_write_b32 a141, v29\n"
      "v_mfma_f32_16x16x32_bf16 v[96:99], a[156:159], a[212:215], v[96:99]\n"
      "s_nop 0\n"
      "v_accvgpr_write_b32 a142, v30\n"
      "v_accvgpr_write_b32 a143, v31\n"
      "v_mfma_f32_4x4x4_16b_bf16 v[28:31], a[244:245], a[224:225], 0\n"
      "v_mfma_f32_4x4x4_16b_bf16 v[32:35], a[244:245], a[226:227], 0\n"
      "ds_read_b64_tr_b16 a[240:241], v61 offset:12672\n"
      "v_mfma_f32_4x4x4_16b_bf16 v[28:31], a[248:249], a[96:97], v[28:31]\n"
      "ds_read_b64_tr_b16 a[242:243], v61 offset:13440\n"
      "v_mfma_f32_4x4x4_16b_bf16 v[32:35], a[248:249], a[98:99], v[32:35]\n"
      "ds_read_b64_tr_b16 a[104:105], v62 offset:39808\n"
      "v_mfma_f32_4x4x4_16b_bf16 v[36:39], a[246:247], a[220:221], v[28:31]\n"
      "ds_read_b64_tr_b16 a[106:107], v62 offset:40576\n"
      "v_mfma_f32_4x4x4_16b_bf16 v[40:43], a[246:247], a[222:223], v[32:35]\n"
      "ds_read_b64_tr_b16 a[236:237], v61 offset:18816\n"
      "v_mfma_f32_4x4x4_16b_bf16 v[28:31], a[250:251], a[220:221], v[28:31]\n"
      "ds_read_b64_tr_b16 a[238:239], v61 offset:19584\n"
      "v_mfma_f32_4x4x4_16b_bf16 v[32:35], a[250:251], a[222:223], v[32:35]\n"
      "v_cvt_pk_bf16_f32 v52, v52, v56\n"
      "v_cvt_pk_bf16_f32 v53, v53, v57\n"
      "v_cvt_pk_bf16_f32 v54, v54, v58\n"
      "v_cvt_pk_bf16_f32 v55, v55, v59\n"
      "v_accvgpr_write_b32 a136, v52\n"
      "v_accvgpr_write_b32 a137, v53\n"
      "v_accvgpr_write_b32 a138, v54\n"
      "v_accvgpr_write_b32 a139, v55\n"
      "v_cvt_pk_bf16_f32 v44, v44, v48\n"
      "v_cvt_pk_bf16_f32 v45, v45, v49\n"
      "v_mfma_f32_16x16x32_bf16 v[64:67], a[84:87], a[192:195], v[64:67]\n"
      "s_nop 0\n"
      "v_cvt_pk_bf16_f32 v46, v46, v50\n"
      "v_cvt_pk_bf16_f32 v47, v47, v51\n"
      "v_mfma_f32_16x16x32_bf16 v[68:71], a[88:91], a[192:195], v[68:71]\n"
      "s_nop 0\n"
      "v_accvgpr_write_b32 a132, v44\n"
      "v_accvgpr_write_b32 a133, v45\n"
      "v_mfma_f32_16x16x32_bf16 v[72:75], a[92:95], a[192:195], v[72:75]\n"
      "s_nop 0\n"
      "v_accvgpr_write_b32 a134, v46\n"
      "v_accvgpr_write_b32 a135, v47\n"
      "v_mfma_f32_4x4x4_16b_bf16 v[44:47], a[244:245], a[232:233], 0\n"
      "v_mfma_f32_4x4x4_16b_bf16 v[48:51], a[244:245], a[234:235], 0\n"
      "ds_read_b128 a[204:207], v5 offset:128\n"
      "v_mfma_f32_4x4x4_16b_bf16 v[44:47], a[248:249], a[100:101], v[44:47]\n"
      "v_mfma_f32_4x4x4_16b_bf16 v[48:51], a[248:249], a[102:103], v[48:51]\n"
      "s_waitcnt lgkmcnt(1)\n"
      "v_mfma_f32_4x4x4_16b_bf16 v[52:55], a[246:247], a[228:229], v[44:47]\n"
      "v_mfma_f32_4x4x4_16b_bf16 v[56:59], a[246:247], a[230:231], v[48:51]\n"
      "v_mfma_f32_4x4x4_16b_bf16 v[44:47], a[250:251], a[228:229], v[44:47]\n"
      "v_mfma_f32_4x4x4_16b_bf16 v[48:51], a[250:251], a[230:231], v[48:51]\n"
      "v_cvt_pk_bf16_f32 v36, v36, v40\n"
      "v_cvt_pk_bf16_f32 v37, v37, v41\n"
      "v_cvt_pk_bf16_f32 v38, v38, v42\n"
      "v_cvt_pk_bf16_f32 v39, v39, v43\n"
      "v_mfma_f32_16x16x32_bf16 v[76:79], a[152:155], a[196:199], v[76:79]\n"
      "v_accvgpr_write_b32 a128, v36\n"
      "v_accvgpr_write_b32 a129, v37\n"
      "v_mfma_f32_16x16x32_bf16 v[80:83], a[144:147], a[196:199], v[80:83]\n"
      "v_accvgpr_write_b32 a130, v38\n"
      "v_accvgpr_write_b32 a131, v39\n"
      "v_mfma_f32_16x16x32_bf16 v[84:87], a[136:139], a[196:199], v[84:87]\n"
      "v_cvt_pk_bf16_f32 v28, v28, v32\n"
      "v_cvt_pk_bf16_f32 v29, v29, v33\n"
      "v_mfma_f32_16x16x32_bf16 v[88:91], a[148:151], a[200:203], v[88:91]\n"
      "v_cvt_pk_bf16_f32 v30, v30, v34\n"
      "v_cvt_pk_bf16_f32 v31, v31, v35\n"
      "v_mfma_f32_16x16x32_bf16 v[92:95], a[140:143], a[200:203], v[92:95]\n"
      "v_accvgpr_write_b32 a124, v28\n"
      "v_accvgpr_write_b32 a125, v29\n"
      "v_mfma_f32_16x16x32_bf16 v[96:99], a[132:135], a[200:203], v[96:99]\n"
      "v_accvgpr_write_b32 a126, v30\n"
      "v_accvgpr_write_b32 a127, v31\n"
      "v_mfma_f32_4x4x4_16b_bf16 v[28:31], a[244:245], a[240:241], 0\n"
      "v_mfma_f32_4x4x4_16b_bf16 v[32:35], a[244:245], a[242:243], 0\n"
      "ds_read_b128 a[208:211], v2 offset:128\n"
      "v_mfma_f32_4x4x4_16b_bf16 v[28:31], a[248:249], a[104:105], v[28:31]\n"
      "ds_read_b128 a[212:215], v2 offset:4224\n"
      "v_mfma_f32_4x4x4_16b_bf16 v[32:35], a[248:249], a[106:107], v[32:35]\n"
      "v_mfma_f32_4x4x4_16b_bf16 v[36:39], a[246:247], a[236:237], v[28:31]\n"
      "s_nop 0\n"
      "s_nop 0\n"
      "v_mfma_f32_4x4x4_16b_bf16 v[40:43], a[246:247], a[238:239], v[32:35]\n"
      "v_mfma_f32_4x4x4_16b_bf16 v[28:31], a[250:251], a[236:237], v[28:31]\n"
      "v_mfma_f32_4x4x4_16b_bf16 v[32:35], a[250:251], a[238:239], v[32:35]\n"
      "v_cvt_pk_bf16_f32 v52, v52, v56\n"
      "v_cvt_pk_bf16_f32 v53, v53, v57\n"
      "v_cvt_pk_bf16_f32 v54, v54, v58\n"
      "v_cvt_pk_bf16_f32 v55, v55, v59\n"
      "v_accvgpr_write_b32 a120, v52\n"
      "v_accvgpr_write_b32 a121, v53\n"
      "v_accvgpr_write_b32 a122, v54\n"
      "v_accvgpr_write_b32 a123, v55\n"
      "v_cvt_pk_bf16_f32 v44, v44, v48\n"
      "v_cvt_pk_bf16_f32 v45, v45, v49\n"
      "v_cvt_pk_bf16_f32 v46, v46, v50\n"
      "v_cvt_pk_bf16_f32 v47, v47, v51\n"
      "v_accvgpr_write_b32 a116, v44\n"
      "v_accvgpr_write_b32 a117, v45\n"
      "v_accvgpr_write_b32 a118, v46\n"
      "v_accvgpr_write_b32 a119, v47\n"
      "s_waitcnt lgkmcnt(0)\n"
      "s_barrier\n"
      "s_mov_b32 m0, s64\n"
      "s_nop 0\n"
      "v_mfma_f32_16x16x32_bf16 v[64:67], a[96:99], a[204:207], v[64:67]\n"
      "ds_read_b128 a[192:195], v3 offset:16896\n"
      "ds_read_b64_tr_b16 a[220:221], v7 offset:16896\n"
      "ds_read_b64_tr_b16 a[222:223], v7 offset:16904\n"
      "v_cvt_pk_bf16_f32 v36, v36, v40\n"
      "v_cvt_pk_bf16_f32 v37, v37, v41\n"
      "v_mfma_f32_16x16x32_bf16 v[68:71], a[100:103], a[204:207], v[68:71]\n"
      "ds_read_b128 a[196:199], v3 offset:16960\n"
      "v_cvt_pk_bf16_f32 v38, v38, v42\n"
      "v_cvt_pk_bf16_f32 v39, v39, v43\n"
      "v_accvgpr_write_b32 a112, v36\n"
      "v_accvgpr_write_b32 a113, v37\n"
      "v_mfma_f32_16x16x32_bf16 v[72:75], a[104:107], a[204:207], v[72:75]\n"
      "v_accvgpr_write_b32 a114, v38\n"
      "v_accvgpr_write_b32 a115, v39\n"
      "v_cvt_pk_bf16_f32 v28, v28, v32\n"
      "v_cvt_pk_bf16_f32 v29, v29, v33\n"
      "v_cvt_pk_bf16_f32 v30, v30, v34\n"
      "v_cvt_pk_bf16_f32 v31, v31, v35\n"
      "v_accvgpr_write_b32 a108, v28\n"
      "v_mfma_f32_16x16x32_bf16 v[76:79], a[128:131], a[208:211], v[76:79]\n"
      "ds_read_b128 a[200:203], v3 offset:17024\n"
      "ds_read_b64_tr_b16 a[224:225], v7 offset:17408\n"
      "ds_read_b64_tr_b16 a[226:227], v7 offset:17416\n"
      "v_accvgpr_write_b32 a109, v29\n"
      "v_accvgpr_write_b32 a110, v30\n"
      "v_accvgpr_write_b32 a111, v31\n"
      "v_mfma_f32_16x16x32_bf16 v[80:83], a[120:123], a[208:211], v[80:83]\n"
      "v_mfma_f32_16x16x32_bf16 v[84:87], a[112:115], a[208:211], v[84:87]\n"
      "s_waitcnt lgkmcnt(3)\n"
      "v_mfma_f32_4x4x4_16b_bf16 v[20:23], a[216:217], a[220:221], 0\n"
      "v_mfma_f32_4x4x4_16b_bf16 v[24:27], a[216:217], a[222:223], 0\n"
      "v_mfma_f32_16x16x32_bf16 v[88:91], a[124:127], a[212:215], v[88:91]\n"
      "s_nop 0\n"
      "ds_read_b128 a[204:207], v4 offset:16896\n"
      "v_mfma_f32_16x16x32_bf16 v[92:95], a[116:119], a[212:215], v[92:95]\n"
      "v_mfma_f32_16x16x32_bf16 v[96:99], a[108:111], a[212:215], v[96:99]\n"
      "ds_read_b64_tr_b16 a[228:229], v7 offset:17920\n"
      "ds_read_b64_tr_b16 a[230:231], v7 offset:17928\n"
      "v_cvt_pk_bf16_f32 v20, v20, v24\n"
      "v_cvt_pk_bf16_f32 v21, v21, v25\n"
      "v_cvt_pk_bf16_f32 v22, v22, v26\n"
      "v_sub_f32 v76, v88, v76\n"
      "v_sub_f32 v77, v89, v77\n"
      "v_cvt_pk_bf16_f32 v23, v23, v27\n"
      "v_mfma_f32_16x16x32_bf16 v[52:55], a[0:3], a[192:195], 0\n"
      "ds_read_b128 a[208:211], v4 offset:16960\n"
      "ds_write_addtid_b32 v20 offset:41216\n"
      "ds_write_addtid_b32 v21 offset:45312\n"
      "ds_write_addtid_b32 v22 offset:49408\n"
      "v_mfma_f32_16x16x32_bf16 v[56:59], a[4:7], a[192:195], 0\n"
      "s_waitcnt lgkmcnt(6)\n"
      "ds_write_addtid_b32 v76 offset:0\n"
      "ds_write_addtid_b32 v77 offset:512\n"
      "ds_write_addtid_b32 v23 offset:53504\n"
      "v_sub_f32 v78, v90, v78\n"
      "v_mfma_f32_4x4x4_16b_bf16 v[28:31], a[216:217], a[224:225], 0\n"
      "v_add_f32 v88, v88, v64\n"
      "v_mfma_f32_4x4x4_16b_bf16 v[32:35], a[216:217], a[226:227], 0\n"
      "ds_write_addtid_b32 v78 offset:1024\n"
      "v_sub_f32 v79, v91, v79\n"
      "v_add_f32 v89, v89, v65\n"
      "v_sub_f32 v80, v92, v80\n"
      "v_mfma_f32_16x16x32_bf16 v[60:63], a[8:11], a[192:195], 0\n"
      "ds_write_addtid_b32 v79 offset:1536\n"
      "ds_write_addtid_b32 v80 offset:2048\n"
      "v_add_f32 v90, v90, v66\n"
      "v_mfma_f32_16x16x32_bf16 v[52:55], a[12:15], a[196:199], v[52:55]\n"
      "ds_read_b128 a[212:215], v4 offset:17024\n"
      "v_sub_f32 v81, v93, v81\n"
      "v_cvt_pk_bf16_f32 v28, v28, v32\n"
      "v_mfma_f32_16x16x32_bf16 v[56:59], a[16:19], a[196:199], v[56:59]\n"
      "ds_write_addtid_b32 v28 offset:41728\n"
      "ds_read_b64_tr_b16 a[232:233], v7 offset:18432\n"
      "ds_write_addtid_b32 v81 offset:2560\n"
      "ds_read_b64_tr_b16 a[234:235], v7 offset:18440\n"
      "v_mfma_f32_16x16x32_bf16 v[60:63], a[20:23], a[196:199], v[60:63]\n"
      "v_add_f32 v91, v91, v67\n"
      "v_sub_f32 v82, v94, v82\n"
      "v_add_f32_e64 v92, v92, v68\n"
      "v_mfma_f32_16x16x32_bf16 v[52:55], a[24:27], a[200:203], v[52:55]\n"
      "s_waitcnt lgkmcnt(5)\n"
      "ds_read_b128 a[192:195], v5 offset:16896\n"
      "ds_write_addtid_b32 v82 offset:3072\n"
      "v_cvt_pk_bf16_f32 v29, v29, v33\n"
      "v_mfma_f32_4x4x4_16b_bf16 v[20:23], a[216:217], a[228:229], 0\n"
      "v_sub_f32 v83, v95, v83\n"
      "v_add_f32_e64 v93, v93, v69\n"
      "v_mfma_f32_4x4x4_16b_bf16 v[24:27], a[216:217], a[230:231], 0\n"
      "ds_write_addtid_b32 v29 offset:45824\n"
      "ds_write_addtid_b32 v83 offset:3584\n"
      "v_sub_f32 v84, v96, v84\n"
      "v_mfma_f32_16x16x32_bf16 v[56:59], a[28:31], a[200:203], v[56:59]\n"
      "ds_write_addtid_b32 v84 offset:4096\n"
      "v_cvt_pk_bf16_f32 v30, v30, v34\n"
      "v_add_f32 v94, v94, v70\n"
      "v_sub_f32 v85, v97, v85\n"
      "v_mfma_f32_16x16x32_bf16 v[60:63], a[32:35], a[200:203], v[60:63]\n"
      "s_nop 0\n"
      "ds_write_addtid_b32 v30 offset:49920\n"
      "ds_write_addtid_b32 v85 offset:4608\n"
      "v_cvt_pk_bf16_f32 v20, v20, v24\n"
      "v_mfma_f32_16x16x32_bf16 v[52:55], a[36:39], a[204:207], v[52:55]\n"
      "ds_read_b128 a[196:199], v5 offset:16960\n"
      "ds_write_addtid_b32 v20 offset:42240\n"
      "v_cvt_pk_bf16_f32 v31, v31, v35\n"
      "v_mfma_f32_16x16x32_bf16 v[56:59], a[40:43], a[204:207], v[56:59]\n"
      "ds_write_addtid_b32 v31 offset:54016\n"
      "v_sub_f32 v86, v98, v86\n"
      "v_add_f32 v95, v95, v71\n"
      "v_mfma_f32_16x16x32_bf16 v[60:63], a[44:47], a[204:207], v[60:63]\n"
      "s_waitcnt lgkmcnt(5)\n"
      "ds_write_addtid_b32 v86 offset:5120\n"
      "v_sub_f32 v87, v99, v87\n"
      "v_cvt_pk_bf16_f32 v21, v21, v25\n"
      "v_add_f32_e64 v96, v96, v72\n"
      "v_mfma_f32_4x4x4_16b_bf16 v[28:31], a[216:217], a[232:233], 0\n"
      "v_cvt_pk_bf16_f32 v22, v22, v26\n"
      "v_add_f32 v97, v97, v73\n"
      "v_mfma_f32_4x4x4_16b_bf16 v[32:35], a[216:217], a[234:235], 0\n"
      "ds_write_addtid_b32 v21 offset:46336\n"
      "ds_write_addtid_b32 v22 offset:50432\n"
      "ds_write_addtid_b32 v87 offset:5632\n"
      "v_mfma_f32_16x16x32_bf16 v[52:55], a[48:51], a[208:211], v[52:55]\n"
      "s_nop 0\n"
      "ds_read_b128 a[200:203], v5 offset:17024\n"
      "v_cvt_pk_bf16_f32 v28, v28, v32\n"
      "v_cvt_pk_bf16_f32 v23, v23, v27\n"
      "v_mfma_f32_16x16x32_bf16 v[56:59], a[52:55], a[208:211], v[56:59]\n"
      "ds_write_addtid_b32 v28 offset:42752\n"
      "ds_write_addtid_b32 v23 offset:54528\n"
      "v_cvt_pk_bf16_f32 v29, v29, v33\n"
      "v_cvt_pk_bf16_f32 v30, v30, v34\n"
      "v_cvt_pk_bf16_f32 v31, v31, v35\n"
      "v_mfma_f32_16x16x32_bf16 v[60:63], a[56:59], a[208:211], v[60:63]\n"
      "ds_write_addtid_b32 v29 offset:46848\n"
      "ds_write_addtid_b32 v30 offset:50944\n"
      "v_mfma_f32_16x16x32_bf16 v[52:55], a[60:63], a[212:215], v[52:55]\n"
      "ds_write_addtid_b32 v31 offset:55040\n"
      "v_add_f32 v98, v98, v74\n"
      "v_add_f32 v99, v99, v75\n"
      "v_fma_f32 v40, 0.5, v76, -v88\n"
      "v_mfma_f32_16x16x32_bf16 v[56:59], a[64:67], a[212:215], v[56:59]\n"
      "v_fma_f32 v41, 0.5, v77, -v89\n"
      "v_fma_f32 v42, 0.5, v78, -v90\n"
      "v_fma_f32 v43, 0.5, v79, -v91\n"
      "v_mfma_f32_16x16x32_bf16 v[60:63], a[68:71], a[212:215], v[60:63]\n"
      "s_waitcnt lgkmcnt(0)\n"
      "v_fma_f32 v44, 0.5, v80, -v92\n"
      "v_fma_f32 v45, 0.5, v81, -v93\n"
      "s_barrier\n"
      "LSloop_%=:\n"
      "v_mfma_f32_16x16x32_bf16 v[52:55], a[72:75], a[192:195], v[52:55]\n"
      "ds_read_b128 a[204:207], v0 offset:16384\n"
      "ds_read2st64_b32 v[28:29], v6 offset0:0 offset1:2\n"
      "v_fma_f32 v46, 0.5, v82, -v94\n"
      "v_mfma_f32_16x16x32_bf16 v[56:59], a[76:79], a[192:195], v[56:59]\n"
      "ds_read_b128 a[208:211], v0 offset:16448\n"
      "ds_read2st64_b32 v[30:31], v6 offset0:4 offset1:6\n"
      "v_mfma_f32_16x16x32_bf16 v[60:63], a[80:83], a[192:195], v[60:63]\n"
      "ds_read_b64_tr_b16 a[220:221], v7 offset:33792\n"
      "v_fma_f32 v47, 0.5, v83, -v95\n"
      "v_mfma_f32_16x16x32_bf16 v[52:55], a[84:87], a[196:199], v[52:55]\n"
      "ds_read_b128 a[212:215], v0 offset:16512\n"
      "v_fma_f32 v48, 0.5, v84, -v96\n"
      "v_mfma_f32_16x16x32_bf16 v[56:59], a[88:91], a[196:199], v[56:59]\n"
      "ds_read_b64_tr_b16 a[222:223], v7 offset:33800\n"
      "ds_read2st64_b32 v[32:33], v6 offset0:8 offset1:10\n"
      "v_fma_f32 v49, 0.5, v85, -v97\n"
      "v_mfma_f32_16x16x32_bf16 v[60:63], a[92:95], a[196:199], v[60:63]\n"
      "ds_read2st64_b32 v[34:35], v6 offset0:12 offset1:14\n"
      "v_fma_f32 v50, 0.5, v86, -v98\n"
      "v_mfma_f32_16x16x32_bf16 v[52:55], a[96:99], a[200:203], v[52:55]\n"
      "ds_read_b128 a[192:195], v1 offset:16384\n"
      "ds_read2st64_b32 v[36:37], v6 offset0:16 offset1:18\n"
      "v_mfma_f32_16x16x32_bf16 v[56:59], a[100:103], a[200:203], v[56:59]\n"
      "ds_read2st64_b32 v[38:39], v6 offset0:20 offset1:22\n"
      "v_fma_f32 v51, 0.5, v87, -v99\n"
      "v_mfma_f32_16x16x32_bf16 v[60:63], a[104:107], a[200:203], v[60:63]\n"
      "s_waitcnt lgkmcnt(8)\n"
      "v_fmac_f32 v28, s46, v64\n"
      "v_fmac_f32 v29, s46, v65\n"
      "v_fmac_f32 v30, s46, v66\n"
      "v_mfma_f32_16x16x32_bf16 v[76:79], v[252:255], a[204:207], v[52:55]\n"
      "s_waitcnt lgkmcnt(5)\n"
      "ds_read_b128 a[196:199], v1 offset:16448\n"
      "v_fmac_f32 v31, s46, v67\n"
      "v_mfma_f32_4x4x4_16b_bf16 v[12:15], a[216:217], a[220:221], 0\n"
      "v_sub_f32 v28, v28, v40\n"
      "v_mfma_f32_4x4x4_16b_bf16 v[16:19], a[216:217], a[222:223], 0\n"
      "v_sub_f32 v29, v29, v41\n"
      "v_sub_f32 v30, v30, v42\n"
      "v_mfma_f32_16x16x32_bf16 v[80:83], v[244:247], a[204:207], v[56:59]\n"
      "v_sub_f32 v31, v31, v43\n"
      "v_fmac_f32 v40, s45, v28\n"
      "v_mfma_f32_16x16x32_bf16 v[84:87], v[236:239], a[204:207], v[60:63]\n"
      "v_fmac_f32 v41, s45, v29\n"
      "v_cvt_pk_bf16_f32 v12, v12, v16\n"
      "v_fmac_f32_e64 v42, s45, v30\n"
      "v_mfma_f32_16x16x32_bf16 v[76:79], v[228:231], a[208:211], v[76:79]\n"
      "ds_read_b128 a[200:203], v1 offset:16512\n"
      "ds_write_addtid_b32 v12 offset:24832\n"
      "v_mfma_f32_16x16x32_bf16 v[80:83], v[220:223], a[208:211], v[80:83]\n"
      "ds_read_b64_tr_b16 a[224:225], v7 offset:34304\n"
      "ds_read_b64_tr_b16 a[226:227], v7 offset:34312\n"
      "v_mfma_f32_16x16x32_bf16 v[84:87], v[212:215], a[208:211], v[84:87]\n"
      "v_fmac_f32_e64 v43, s45, v31\n"
      "v_cvt_pk_bf16_f32 v13, v13, v17\n"
      "v_mfma_f32_16x16x32_bf16 v[76:79], v[204:207], a[212:215], v[76:79]\n"
      "ds_read_b128 a[204:207], v2 offset:16384\n"
      "ds_write_addtid_b32 v13 offset:28928\n"
      "v_fma_f32 v28, s47, v28, v52\n"
      "v_mfma_f32_16x16x32_bf16 v[80:83], v[196:199], a[212:215], v[80:83]\n"
      "v_fma_f32 v29, s47, v29, v53\n"
      "v_cvt_pk_bf16_f32 v14, v14, v18\n"
      "v_mfma_f32_16x16x32_bf16 v[84:87], v[188:191], a[212:215], v[84:87]\n"
      "s_waitcnt lgkmcnt(4)\n"
      "ds_write_addtid_b32 v14 offset:33024\n"
      "v_fma_f32 v30, s47, v30, v54\n"
      "v_mfma_f32_16x16x32_bf16 v[76:79], v[180:183], a[192:195], v[76:79]\n"
      "s_nop 0\n"
      "ds_read_b128 a[208:211], v2 offset:16448\n"
      "v_fma_f32 v31, s47, v31, v55\n"
      "v_mfma_f32_16x16x32_bf16 v[80:83], v[172:175], a[192:195], v[80:83]\n"
      "s_nop 0\n"
      "v_cvt_pk_bf16_f32 v40, v40, v41\n"
      "v_cvt_pk_bf16_f32 v15, v15, v19\n"
      "v_mfma_f32_16x16x32_bf16 v[84:87], v[164:167], a[192:195], v[84:87]\n"
      "s_waitcnt lgkmcnt(2)\n"
      "ds_write_addtid_b32 v40 offset:12288\n"
      "ds_write_addtid_b32 v15 offset:37120\n"
      "v_cvt_pk_bf16_f32 v42, v42, v43\n"
      "v_mfma_f32_4x4x4_16b_bf16 v[20:23], a[216:217], a[224:225], 0\n"
      "v_cvt_pk_bf16_f32 v28, v28, v29\n"
      "v_mfma_f32_4x4x4_16b_bf16 v[24:27], a[216:217], a[226:227], 0\n"
      "ds_write_addtid_b32 v42 offset:12800\n"
      "ds_write_addtid_b32 v28 offset:14336\n"
      "v_mfma_f32_16x16x32_bf16 v[76:79], v[156:159], a[196:199], v[76:79]\n"
      "ds_read_b128 a[212:215], v2 offset:16512\n"
      "v_cvt_pk_bf16_f32 v30, v30, v31\n"
      "v_mfma_f32_16x16x32_bf16 v[80:83], v[148:151], a[196:199], v[80:83]\n"
      "ds_write_addtid_b32 v30 offset:14848\n"
      "v_fmac_f32 v32, s46, v68\n"
      "v_cvt_pk_bf16_f32 v20, v20, v24\n"
      "v_mfma_f32_16x16x32_bf16 v[84:87], v[140:143], a[196:199], v[84:87]\n"
      "ds_write_addtid_b32 v20 offset:25344\n"
      "v_fmac_f32 v33, s46, v69\n"
      "v_mfma_f32_16x16x32_bf16 v[76:79], v[132:135], a[200:203], v[76:79]\n"
      "ds_read_b128 a[192:195], v0 offset:20480\n"
      "ds_read_b64_tr_b16 a[228:229], v7 offset:34816\n"
      "v_mfma_f32_16x16x32_bf16 v[80:83], v[124:127], a[200:203], v[80:83]\n"
      "ds_read_b64_tr_b16 a[230:231], v7 offset:34824\n"
      "v_fmac_f32 v34, s46, v70\n"
      "v_mfma_f32_16x16x32_bf16 v[84:87], v[116:119], a[200:203], v[84:87]\n"
      "v_fmac_f32 v35, s46, v71\n"
      "v_cvt_pk_bf16_f32 v21, v21, v25\n"
      "v_mfma_f32_16x16x32_bf16 v[76:79], v[108:111], a[204:207], v[76:79]\n"
      "ds_read_b128 a[196:199], v0 offset:20544\n"
      "ds_write_addtid_b32 v21 offset:29440\n"
      "v_sub_f32 v32, v32, v44\n"
      "v_mfma_f32_16x16x32_bf16 v[80:83], v[100:103], a[204:207], v[80:83]\n"
      "v_sub_f32 v33, v33, v45\n"
      "v_cvt_pk_bf16_f32 v22, v22, v26\n"
      "v_mfma_f32_16x16x32_bf16 v[84:87], a[160:163], a[204:207], v[84:87]\n"
      "s_waitcnt lgkmcnt(2)\n"
      "ds_write_addtid_b32 v22 offset:33536\n"
      "v_sub_f32 v34, v34, v46\n"
      "v_mfma_f32_16x16x32_bf16 v[76:79], a[152:155], a[208:211], v[76:79]\n"
      "ds_read_b128 a[200:203], v0 offset:20608\n"
      "v_sub_f32 v35, v35, v47\n"
      "v_mfma_f32_16x16x32_bf16 v[80:83], a[144:147], a[208:211], v[80:83]\n"
      "v_fmac_f32 v44, s45, v32\n"
      "v_cvt_pk_bf16_f32 v23, v23, v27\n"
      "v_mfma_f32_4x4x4_16b_bf16 v[12:15], a[216:217], a[228:229], 0\n"
      "v_fmac_f32 v45, s45, v33\n"
      "v_mfma_f32_4x4x4_16b_bf16 v[16:19], a[216:217], a[230:231], 0\n"
      "ds_write_addtid_b32 v23 offset:37632\n"
      "v_fmac_f32 v46, s45, v34\n"
      "v_mfma_f32_16x16x32_bf16 v[84:87], a[136:139], a[208:211], v[84:87]\n"
      "v_fmac_f32 v47, s45, v35\n"
      "v_fma_f32 v32, s47, v32, v56\n"
      "v_cvt_pk_bf16_f32 v12, v12, v16\n"
      "v_mfma_f32_16x16x32_bf16 v[76:79], a[128:131], a[212:215], v[76:79]\n"
      "ds_read_b128 a[204:207], v1 offset:20480\n"
      "ds_write_addtid_b32 v12 offset:25856\n"
      "v_mfma_f32_16x16x32_bf16 v[80:83], a[120:123], a[212:215], v[80:83]\n"
      "s_nop 0\n"
      "v_fma_f32 v33, s47, v33, v57\n"
      "v_cvt_pk_bf16_f32 v13, v13, v17\n"
      "v_mfma_f32_16x16x32_bf16 v[84:87], a[112:115], a[212:215], v[84:87]\n"
      "ds_read_b64_tr_b16 a[232:233], v7 offset:35328\n"
      "ds_write_addtid_b32 v13 offset:29952\n"
      "v_fma_f32 v34, s47, v34, v58\n"
      "v_mfma_f32_16x16x32_bf16 v[88:91], v[248:251], a[192:195], v[52:55]\n"
      "ds_read_b128 a[208:211], v1 offset:20544\n"
      "ds_read_b64_tr_b16 a[234:235], v7 offset:35336\n"
      "v_mfma_f32_16x16x32_bf16 v[92:95], v[240:243], a[192:195], v[56:59]\n"
      "v_fma_f32 v35, s47, v35, v59\n"
      "v_cvt_pk_bf16_f32 v44, v44, v45\n"
      "v_mfma_f32_16x16x32_bf16 v[96:99], v[232:235], a[192:195], v[60:63]\n"
      "s_waitcnt lgkmcnt(4)\n"
      "ds_write_addtid_b32 v44 offset:20484\n"
      "v_cvt_pk_bf16_f32 v14, v14, v18\n"
      "v_mfma_f32_16x16x32_bf16 v[88:91], v[224:227], a[196:199], v[88:91]\n"
      "s_nop 0\n"
      "ds_read_b128 a[212:215], v1 offset:20608\n"
      "ds_write_addtid_b32 v14 offset:34048\n"
      "v_cvt_pk_bf16_f32 v46, v46, v47\n"
      "v_mfma_f32_16x16x32_bf16 v[92:95], v[216:219], a[196:199], v[92:95]\n"
      "ds_write_addtid_b32 v46 offset:20996\n"
      "v_cvt_pk_bf16_f32 v32, v32, v33\n"
      "v_mfma_f32_16x16x32_bf16 v[96:99], v[208:211], a[196:199], v[96:99]\n"
      "ds_write_addtid_b32 v32 offset:22532\n"
      "v_cvt_pk_bf16_f32 v34, v34, v35\n"
      "v_mfma_f32_16x16x32_bf16 v[88:91], v[200:203], a[200:203], v[88:91]\n"
      "s_waitcnt lgkmcnt(4)\n"
      "ds_read_b128 a[192:195], v2 offset:20480\n"
      "ds_write_addtid_b32 v34 offset:23044\n"
      "v_fmac_f32 v36, s46, v72\n"
      "v_mfma_f32_4x4x4_16b_bf16 v[20:23], a[216:217], a[232:233], 0\n"
      "v_fmac_f32 v37, s46, v73\n"
      "v_mfma_f32_4x4x4_16b_bf16 v[24:27], a[216:217], a[234:235], 0\n"
      "v_fmac_f32 v38, s46, v74\n"
      "v_cvt_pk_bf16_f32 v15, v15, v19\n"
      "v_mfma_f32_16x16x32_bf16 v[92:95], v[192:195], a[200:203], v[92:95]\n"
      "ds_write_addtid_b32 v15 offset:38144\n"
      "v_fmac_f32 v39, s46, v75\n"
      "v_cvt_pk_bf16_f32 v20, v20, v24\n"
      "v_mfma_f32_16x16x32_bf16 v[96:99], v[184:187], a[200:203], v[96:99]\n"
      "ds_write_addtid_b32 v20 offset:26368\n"
      "v_sub_f32 v36, v36, v48\n"
      "v_mfma_f32_16x16x32_bf16 v[88:91], v[176:179], a[204:207], v[88:91]\n"
      "ds_read_b128 a[196:199], v2 offset:20544\n"
      "v_sub_f32 v37, v37, v49\n"
      "v_mfma_f32_16x16x32_bf16 v[92:95], v[168:171], a[204:207], v[92:95]\n"
      "ds_read_b64_tr_b16 a[220:221], v7 offset:35840\n"
      "ds_read_b64_tr_b16 a[222:223], v7 offset:35848\n"
      "v_mfma_f32_16x16x32_bf16 v[96:99], v[160:163], a[204:207], v[96:99]\n"
      "v_sub_f32 v38, v38, v50\n"
      "v_cvt_pk_bf16_f32 v21, v21, v25\n"
      "v_mfma_f32_16x16x32_bf16 v[88:91], v[152:155], a[208:211], v[88:91]\n"
      "ds_read_b128 a[200:203], v2 offset:20608\n"
      "ds_write_addtid_b32 v21 offset:30464\n"
      "v_sub_f32 v39, v39, v51\n"
      "v_mfma_f32_16x16x32_bf16 v[92:95], v[144:147], a[208:211], v[92:95]\n"
      "v_fmac_f32 v48, s45, v36\n"
      "v_cvt_pk_bf16_f32 v22, v22, v26\n"
      "v_mfma_f32_16x16x32_bf16 v[96:99], v[136:139], a[208:211], v[96:99]\n"
      "s_waitcnt lgkmcnt(3)\n"
      "ds_write_addtid_b32 v22 offset:34560\n"
      "v_fmac_f32 v49, s45, v37\n"
      "v_mfma_f32_16x16x32_bf16 v[88:91], v[128:131], a[212:215], v[88:91]\n"
      "ds_read_b128 a[204:207], v3 offset:33792\n"
      "v_fmac_f32 v50, s45, v38\n"
      "v_mfma_f32_16x16x32_bf16 v[92:95], v[120:123], a[212:215], v[92:95]\n"
      "v_fmac_f32_e64 v51, s45, v39\n"
      "v_cvt_pk_bf16_f32 v23, v23, v27\n"
      "v_mfma_f32_16x16x32_bf16 v[96:99], v[112:115], a[212:215], v[96:99]\n"
      "s_waitcnt lgkmcnt(2)\n"
      "ds_write_addtid_b32 v23 offset:38656\n"
      "v_fma_f32 v36, s47, v36, v60\n"
      "v_mfma_f32_4x4x4_16b_bf16 v[12:15], a[216:217], a[220:221], 0\n"
      "v_fma_f32 v37, s47, v37, v61\n"
      "v_mfma_f32_4x4x4_16b_bf16 v[16:19], a[216:217], a[222:223], 0\n"
      "v_fma_f32 v38, s47, v38, v62\n"
      "v_fma_f32 v39, s47, v39, v63\n"
      "v_mfma_f32_16x16x32_bf16 v[88:91], v[104:107], a[192:195], v[88:91]\n"
      "ds_read_b128 a[208:211], v3 offset:33856\n"
      "v_cvt_pk_bf16_f32 v48, v48, v49\n"
      "v_cvt_pk_bf16_f32 v12, v12, v16\n"
      "v_mfma_f32_16x16x32_bf16 v[92:95], a[164:167], a[192:195], v[92:95]\n"
      "ds_write_addtid_b32 v48 offset:16384\n"
      "ds_write_addtid_b32 v12 offset:26880\n"
      "v_mfma_f32_16x16x32_bf16 v[96:99], a[156:159], a[192:195], v[96:99]\n"
      "v_cvt_pk_bf16_f32 v13, v13, v17\n"
      "v_cvt_pk_bf16_f32 v14, v14, v18\n"
      "v_mfma_f32_16x16x32_bf16 v[88:91], a[148:151], a[196:199], v[88:91]\n"
      "ds_read_b128 a[212:215], v3 offset:33920\n"
      "ds_write_addtid_b32 v13 offset:30976\n"
      "v_mfma_f32_16x16x32_bf16 v[92:95], a[140:143], a[196:199], v[92:95]\n"
      "ds_read_b64_tr_b16 a[224:225], v7 offset:36352\n"
      "ds_write_addtid_b32 v14 offset:35072\n"
      "v_cvt_pk_bf16_f32 v50, v50, v51\n"
      "v_mfma_f32_16x16x32_bf16 v[96:99], a[132:135], a[196:199], v[96:99]\n"
      "ds_write_addtid_b32 v50 offset:16896\n"
      "ds_read_b64_tr_b16 a[226:227], v7 offset:36360\n"
      "v_mfma_f32_16x16x32_bf16 v[88:91], a[124:127], a[200:203], v[88:91]\n"
      "ds_read_b128 a[192:195], v4 offset:33792\n"
      "v_cvt_pk_bf16_f32 v36, v36, v37\n"
      "v_mfma_f32_16x16x32_bf16 v[92:95], a[116:119], a[200:203], v[92:95]\n"
      "ds_write_addtid_b32 v36 offset:18432\n"
      "v_cvt_pk_bf16_f32 v15, v15, v19\n"
      "v_mfma_f32_16x16x32_bf16 v[96:99], a[108:111], a[200:203], v[96:99]\n"
      "s_waitcnt lgkmcnt(6)\n"
      "ds_write_addtid_b32 v15 offset:39168\n"
      "v_sub_f32 v76, v88, v76\n"
      "v_sub_f32 v77, v89, v77\n"
      "v_cvt_pk_bf16_f32 v38, v38, v39\n"
      "v_mfma_f32_16x16x32_bf16 v[64:67], a[0:3], a[204:207], 0\n"
      "s_nop 0\n"
      "ds_read_b128 a[196:199], v4 offset:33856\n"
      "ds_write_addtid_b32 v38 offset:18944\n"
      "v_mfma_f32_16x16x32_bf16 v[68:71], a[4:7], a[204:207], 0\n"
      "s_waitcnt lgkmcnt(4)\n"
      "ds_write_addtid_b32 v76 offset:6144\n"
      "ds_write_addtid_b32 v77 offset:6656\n"
      "v_mfma_f32_4x4x4_16b_bf16 v[20:23], a[216:217], a[224:225], 0\n"
      "v_sub_f32 v78, v90, v78\n"
      "v_mfma_f32_4x4x4_16b_bf16 v[24:27], a[216:217], a[226:227], 0\n"
      "ds_write_addtid_b32 v78 offset:7168\n"
      "v_sub_f32_e64 v79, v91, v79\n"
      "v_mfma_f32_16x16x32_bf16 v[72:75], a[8:11], a[204:207], 0\n"
      "ds_write_addtid_b32 v79 offset:7680\n"
      "v_sub_f32 v80, v92, v80\n"
      "v_sub_f32 v81, v93, v81\n"
      "v_mfma_f32_16x16x32_bf16 v[64:67], a[12:15], a[208:211], v[64:67]\n"
      "ds_read_b128 a[200:203], v4 offset:33920\n"
      "ds_write_addtid_b32 v80 offset:8192\n"
      "v_mfma_f32_16x16x32_bf16 v[68:71], a[16:19], a[208:211], v[68:71]\n"
      "ds_write_addtid_b32 v81 offset:8704\n"
      "v_sub_f32 v82, v94, v82\n"
      "v_mfma_f32_16x16x32_bf16 v[72:75], a[20:23], a[208:211], v[72:75]\n"
      "ds_write_addtid_b32 v82 offset:9216\n"
      "v_cvt_pk_bf16_f32 v20, v20, v24\n"
      "v_mfma_f32_16x16x32_bf16 v[64:67], a[24:27], a[212:215], v[64:67]\n"
      "ds_read_b128 a[204:207], v5 offset:33792\n"
      "ds_write_addtid_b32 v20 offset:27392\n"
      "v_sub_f32 v83, v95, v83\n"
      "v_mfma_f32_16x16x32_bf16 v[68:71], a[28:31], a[212:215], v[68:71]\n"
      "ds_write_addtid_b32 v83 offset:9728\n"
      "v_sub_f32 v84, v96, v84\n"
      "v_mfma_f32_16x16x32_bf16 v[72:75], a[32:35], a[212:215], v[72:75]\n"
      "ds_write_addtid_b32 v84 offset:10240\n"
      "v_cvt_pk_bf16_f32 v21, v21, v25\n"
      "v_mfma_f32_16x16x32_bf16 v[64:67], a[36:39], a[192:195], v[64:67]\n"
      "s_nop 0\n"
      "ds_read_b128 a[208:211], v5 offset:33856\n"
      "ds_write_addtid_b32 v21 offset:31488\n"
      "v_mfma_f32_16x16x32_bf16 v[68:71], a[40:43], a[192:195], v[68:71]\n"
      "v_sub_f32 v85, v97, v85\n"
      "v_sub_f32 v86, v98, v86\n"
      "v_cvt_pk_bf16_f32 v22, v22, v26\n"
      "v_mfma_f32_16x16x32_bf16 v[72:75], a[44:47], a[192:195], v[72:75]\n"
      "s_waitcnt lgkmcnt(4)\n"
      "ds_write_addtid_b32 v22 offset:35584\n"
      "ds_write_addtid_b32 v85 offset:10752\n"
      "v_sub_f32 v87, v99, v87\n"
      "v_mfma_f32_16x16x32_bf16 v[64:67], a[48:51], a[196:199], v[64:67]\n"
      "ds_read_b128 a[212:215], v5 offset:33920\n"
      "ds_write_addtid_b32 v86 offset:11264\n"
      "v_mfma_f32_16x16x32_bf16 v[68:71], a[52:55], a[196:199], v[68:71]\n"
      "ds_write_addtid_b32 v87 offset:11776\n"
      "v_cvt_pk_bf16_f32 v23, v23, v27\n"
      "v_mfma_f32_16x16x32_bf16 v[72:75], a[56:59], a[196:199], v[72:75]\n"
      "ds_write_addtid_b32 v23 offset:39680\n"
      "v_fma_f32 v40, 0.5, v76, -v88\n"
      "v_mfma_f32_16x16x32_bf16 v[64:67], a[60:63], a[200:203], v[64:67]\n"
      "v_fma_f32 v41, 0.5, v77, -v89\n"
      "v_fma_f32 v42, 0.5, v78, -v90\n"
      "v_mfma_f32_16x16x32_bf16 v[68:71], a[64:67], a[200:203], v[68:71]\n"
      "s_nop 0\n"
      "v_fma_f32 v43, 0.5, v79, -v91\n"
      "v_fma_f32 v44, 0.5, v80, -v92\n"
      "v_mfma_f32_16x16x32_bf16 v[72:75], a[68:71], a[200:203], v[72:75]\n"
      "s_waitcnt lgkmcnt(0)\n"
      "v_fma_f32 v45, 0.5, v81, -v93\n"
      "s_barrier\n"
      "LSmid_%=:\n"
      "v_mfma_f32_16x16x32_bf16 v[64:67], a[72:75], a[204:207], v[64:67]\n"
      "ds_read_b128 a[192:195], v0 offset:0\n"
      "ds_read2st64_b32 v[28:29], v6 offset0:24 offset1:26\n"
      "v_fma_f32 v46, 0.5, v82, -v94\n"
      "v_mfma_f32_16x16x32_bf16 v[68:71], a[76:79], a[204:207], v[68:71]\n"
      "s_nop 0\n"
      "ds_read_b128 a[196:199], v0 offset:64\n"
      "ds_read2st64_b32 v[30:31], v6 offset0:28 offset1:30\n"
      "v_mfma_f32_16x16x32_bf16 v[72:75], a[80:83], a[204:207], v[72:75]\n"
      "ds_read_b64_tr_b16 a[220:221], v7 offset:50688\n"
      "v_fma_f32 v47, 0.5, v83, -v95\n"
      "v_mfma_f32_16x16x32_bf16 v[64:67], a[84:87], a[208:211], v[64:67]\n"
      "ds_read_b128 a[200:203], v0 offset:128\n"
      "v_fma_f32 v48, 0.5, v84, -v96\n"
      "v_mfma_f32_16x16x32_bf16 v[68:71], a[88:91], a[208:211], v[68:71]\n"
      "ds_read_b64_tr_b16 a[222:223], v7 offset:50696\n"
      "ds_read2st64_b32 v[32:33], v6 offset0:32 offset1:34\n"
      "v_fma_f32 v49, 0.5, v85, -v97\n"
      "v_mfma_f32_16x16x32_bf16 v[72:75], a[92:95], a[208:211], v[72:75]\n"
      "ds_read2st64_b32 v[34:35], v6 offset0:36 offset1:38\n"
      "v_fma_f32 v50, 0.5, v86, -v98\n"
      "v_mfma_f32_16x16x32_bf16 v[64:67], a[96:99], a[212:215], v[64:67]\n"
      "ds_read_b128 a[204:207], v1 offset:0\n"
      "ds_read2st64_b32 v[36:37], v6 offset0:40 offset1:42\n"
      "v_mfma_f32_16x16x32_bf16 v[68:71], a[100:103], a[212:215], v[68:71]\n"
      "ds_read2st64_b32 v[38:39], v6 offset0:44 offset1:46\n"
      "v_fma_f32 v51, 0.5, v87, -v99\n"
      "v_mfma_f32_16x16x32_bf16 v[72:75], a[104:107], a[212:215], v[72:75]\n"
      "s_waitcnt lgkmcnt(8)\n"
      "v_fmac_f32 v28, s46, v52\n"
      "v_fmac_f32 v29, s46, v53\n"
      "v_fmac_f32 v30, s46, v54\n"
      "v_mfma_f32_16x16x32_bf16 v[76:79], v[252:255], a[192:195], v[64:67]\n"
      "s_waitcnt lgkmcnt(5)\n"
      "ds_read_b128 a[208:211], v1 offset:64\n"
      "v_fmac_f32 v31, s46, v55\n"
      "v_mfma_f32_4x4x4_16b_bf16 v[12:15], a[216:217], a[220:221], 0\n"
      "v_sub_f32 v28, v28, v40\n"
      "v_mfma_f32_4x4x4_16b_bf16 v[16:19], a[216:217], a[222:223], 0\n"
      "v_sub_f32 v29, v29, v41\n"
      "v_sub_f32 v30, v30, v42\n"
      "v_mfma_f32_16x16x32_bf16 v[80:83], v[244:247], a[192:195], v[68:71]\n"
      "v_sub_f32 v31, v31, v43\n"
      "v_fmac_f32_e64 v40, s45, v28\n"
      "v_mfma_f32_16x16x32_bf16 v[84:87], v[236:239], a[192:195], v[72:75]\n"
      "v_fmac_f32 v41, s45, v29\n"
      "v_cvt_pk_bf16_f32 v12, v12, v16\n"
      "v_fmac_f32 v42, s45, v30\n"
      "v_mfma_f32_16x16x32_bf16 v[76:79], v[228:231], a[196:199], v[76:79]\n"
      "ds_read_b128 a[212:215], v1 offset:128\n"
      "ds_write_addtid_b32 v12 offset:41216\n"
      "v_mfma_f32_16x16x32_bf16 v[80:83], v[220:223], a[196:199], v[80:83]\n"
      "ds_read_b64_tr_b16 a[224:225], v7 offset:51200\n"
      "ds_read_b64_tr_b16 a[226:227], v7 offset:51208\n"
      "v_mfma_f32_16x16x32_bf16 v[84:87], v[212:215], a[196:199], v[84:87]\n"
      "v_fmac_f32 v43, s45, v31\n"
      "v_cvt_pk_bf16_f32 v13, v13, v17\n"
      "v_mfma_f32_16x16x32_bf16 v[76:79], v[204:207], a[200:203], v[76:79]\n"
      "s_nop 0\n"
      "ds_read_b128 a[192:195], v2 offset:0\n"
      "ds_write_addtid_b32 v13 offset:45312\n"
      "v_fma_f32 v28, s47, v28, v64\n"
      "v_mfma_f32_16x16x32_bf16 v[80:83], v[196:199], a[200:203], v[80:83]\n"
      "v_fma_f32 v29, s47, v29, v65\n"
      "v_cvt_pk_bf16_f32 v14, v14, v18\n"
      "v_mfma_f32_16x16x32_bf16 v[84:87], v[188:191], a[200:203], v[84:87]\n"
      "s_waitcnt lgkmcnt(4)\n"
      "ds_write_addtid_b32 v14 offset:49408\n"
      "v_fma_f32 v30, s47, v30, v66\n"
      "v_mfma_f32_16x16x32_bf16 v[76:79], v[180:183], a[204:207], v[76:79]\n"
      "s_nop 0\n"
      "ds_read_b128 a[196:199], v2 offset:64\n"
      "v_fma_f32 v31, s47, v31, v67\n"
      "v_mfma_f32_16x16x32_bf16 v[80:83], v[172:175], a[204:207], v[80:83]\n"
      "s_nop 0\n"
      "v_cvt_pk_bf16_f32 v40, v40, v41\n"
      "v_cvt_pk_bf16_f32 v15, v15, v19\n"
      "v_mfma_f32_16x16x32_bf16 v[84:87], v[164:167], a[204:207], v[84:87]\n"
      "s_waitcnt lgkmcnt(2)\n"
      "ds_write_addtid_b32 v40 offset:13312\n"
      "ds_write_addtid_b32 v15 offset:53504\n"
      "v_cvt_pk_bf16_f32 v42, v42, v43\n"
      "v_mfma_f32_4x4x4_16b_bf16 v[20:23], a[216:217], a[224:225], 0\n"
      "v_cvt_pk_bf16_f32 v28, v28, v29\n"
      "v_mfma_f32_4x4x4_16b_bf16 v[24:27], a[216:217], a[226:227], 0\n"
      "ds_write_addtid_b32 v42 offset:13824\n"
      "ds_write_addtid_b32 v28 offset:15360\n"
      "v_mfma_f32_16x16x32_bf16 v[76:79], v[156:159], a[208:211], v[76:79]\n"
      "ds_read_b128 a[200:203], v2 offset:128\n"
      "v_cvt_pk_bf16_f32 v30, v30, v31\n"
      "v_mfma_f32_16x16x32_bf16 v[80:83], v[148:151], a[208:211], v[80:83]\n"
      "ds_write_addtid_b32 v30 offset:15872\n"
      "v_fmac_f32 v32, s46, v56\n"
      "v_cvt_pk_bf16_f32 v20, v20, v24\n"
      "v_mfma_f32_16x16x32_bf16 v[84:87], v[140:143], a[208:211], v[84:87]\n"
      "ds_write_addtid_b32 v20 offset:41728\n"
      "v_fmac_f32 v33, s46, v57\n"
      "v_mfma_f32_16x16x32_bf16 v[76:79], v[132:135], a[212:215], v[76:79]\n"
      "ds_read_b128 a[204:207], v0 offset:4096\n"
      "ds_read_b64_tr_b16 a[228:229], v7 offset:51712\n"
      "v_mfma_f32_16x16x32_bf16 v[80:83], v[124:127], a[212:215], v[80:83]\n"
      "ds_read_b64_tr_b16 a[230:231], v7 offset:51720\n"
      "v_fmac_f32 v34, s46, v58\n"
      "v_mfma_f32_16x16x32_bf16 v[84:87], v[116:119], a[212:215], v[84:87]\n"
      "v_fmac_f32 v35, s46, v59\n"
      "v_cvt_pk_bf16_f32 v21, v21, v25\n"
      "v_mfma_f32_16x16x32_bf16 v[76:79], v[108:111], a[192:195], v[76:79]\n"
      "ds_read_b128 a[208:211], v0 offset:4160\n"
      "ds_write_addtid_b32 v21 offset:45824\n"
      "v_sub_f32 v32, v32, v44\n"
      "v_mfma_f32_16x16x32_bf16 v[80:83], v[100:103], a[192:195], v[80:83]\n"
      "v_sub_f32_e64 v33, v33, v45\n"
      "v_cvt_pk_bf16_f32 v22, v22, v26\n"
      "v_mfma_f32_16x16x32_bf16 v[84:87], a[160:163], a[192:195], v[84:87]\n"
      "s_waitcnt lgkmcnt(2)\n"
      "ds_write_addtid_b32 v22 offset:49920\n"
      "v_sub_f32 v34, v34, v46\n"
      "v_mfma_f32_16x16x32_bf16 v[76:79], a[152:155], a[196:199], v[76:79]\n"
      "ds_read_b128 a[212:215], v0 offset:4224\n"
      "v_sub_f32 v35, v35, v47\n"
      "v_mfma_f32_16x16x32_bf16 v[80:83], a[144:147], a[196:199], v[80:83]\n"
      "v_fmac_f32 v44, s45, v32\n"
      "v_cvt_pk_bf16_f32 v23, v23, v27\n"
      "v_mfma_f32_4x4x4_16b_bf16 v[12:15], a[216:217], a[228:229], 0\n"
      "v_fmac_f32 v45, s45, v33\n"
      "v_mfma_f32_4x4x4_16b_bf16 v[16:19], a[216:217], a[230:231], 0\n"
      "ds_write_addtid_b32 v23 offset:54016\n"
      "v_fmac_f32 v46, s45, v34\n"
      "v_mfma_f32_16x16x32_bf16 v[84:87], a[136:139], a[196:199], v[84:87]\n"
      "v_fmac_f32 v47, s45, v35\n"
      "v_fma_f32 v32, s47, v32, v68\n"
      "v_cvt_pk_bf16_f32 v12, v12, v16\n"
      "v_mfma_f32_16x16x32_bf16 v[76:79], a[128:131], a[200:203], v[76:79]\n"
      "ds_read_b128 a[192:195], v1 offset:4096\n"
      "ds_write_addtid_b32 v12 offset:42240\n"
      "v_mfma_f32_16x16x32_bf16 v[80:83], a[120:123], a[200:203], v[80:83]\n"
      "v_fma_f32 v33, s47, v33, v69\n"
      "v_cvt_pk_bf16_f32 v13, v13, v17\n"
      "v_mfma_f32_16x16x32_bf16 v[84:87], a[112:115], a[200:203], v[84:87]\n"
      "ds_read_b64_tr_b16 a[232:233], v7 offset:52224\n"
      "ds_write_addtid_b32 v13 offset:46336\n"
      "v_fma_f32 v34, s47, v34, v70\n"
      "v_mfma_f32_16x16x32_bf16 v[88:91], v[248:251], a[204:207], v[64:67]\n"
      "ds_read_b128 a[196:199], v1 offset:4160\n"
      "ds_read_b64_tr_b16 a[234:235], v7 offset:52232\n"
      "v_mfma_f32_16x16x32_bf16 v[92:95], v[240:243], a[204:207], v[68:71]\n"
      "s_nop 0\n"
      "v_fma_f32 v35, s47, v35, v71\n"
      "v_cvt_pk_bf16_f32 v44, v44, v45\n"
      "v_mfma_f32_16x16x32_bf16 v[96:99], v[232:235], a[204:207], v[72:75]\n"
      "s_waitcnt lgkmcnt(4)\n"
      "ds_write_addtid_b32 v44 offset:21508\n"
      "v_cvt_pk_bf16_f32 v14, v14, v18\n"
      "v_mfma_f32_16x16x32_bf16 v[88:91], v[224:227], a[208:211], v[88:91]\n"
      "ds_read_b128 a[200:203], v1 offset:4224\n"
      "ds_write_addtid_b32 v14 offset:50432\n"
      "v_cvt_pk_bf16_f32 v46, v46, v47\n"
      "v_mfma_f32_16x16x32_bf16 v[92:95], v[216:219], a[208:211], v[92:95]\n"
      "ds_write_addtid_b32 v46 offset:22020\n"
      "v_cvt_pk_bf16_f32 v32, v32, v33\n"
      "v_mfma_f32_16x16x32_bf16 v[96:99], v[208:211], a[208:211], v[96:99]\n"
      "ds_write_addtid_b32 v32 offset:23556\n"
      "v_cvt_pk_bf16_f32 v34, v34, v35\n"
      "v_mfma_f32_16x16x32_bf16 v[88:91], v[200:203], a[212:215], v[88:91]\n"
      "s_waitcnt lgkmcnt(4)\n"
      "ds_read_b128 a[204:207], v2 offset:4096\n"
      "ds_write_addtid_b32 v34 offset:24068\n"
      "v_fmac_f32 v36, s46, v60\n"
      "v_mfma_f32_4x4x4_16b_bf16 v[20:23], a[216:217], a[232:233], 0\n"
      "v_fmac_f32 v37, s46, v61\n"
      "v_mfma_f32_4x4x4_16b_bf16 v[24:27], a[216:217], a[234:235], 0\n"
      "v_fmac_f32 v38, s46, v62\n"
      "v_cvt_pk_bf16_f32 v15, v15, v19\n"
      "v_mfma_f32_16x16x32_bf16 v[92:95], v[192:195], a[212:215], v[92:95]\n"
      "ds_write_addtid_b32 v15 offset:54528\n"
      "v_fmac_f32_e64 v39, s46, v63\n"
      "v_cvt_pk_bf16_f32 v20, v20, v24\n"
      "v_mfma_f32_16x16x32_bf16 v[96:99], v[184:187], a[212:215], v[96:99]\n"
      "ds_write_addtid_b32 v20 offset:42752\n"
      "v_sub_f32 v36, v36, v48\n"
      "v_mfma_f32_16x16x32_bf16 v[88:91], v[176:179], a[192:195], v[88:91]\n"
      "ds_read_b128 a[208:211], v2 offset:4160\n"
      "v_sub_f32 v37, v37, v49\n"
      "v_mfma_f32_16x16x32_bf16 v[92:95], v[168:171], a[192:195], v[92:95]\n"
      "ds_read_b64_tr_b16 a[220:221], v7 offset:52736\n"
      "ds_read_b64_tr_b16 a[222:223], v7 offset:52744\n"
      "v_mfma_f32_16x16x32_bf16 v[96:99], v[160:163], a[192:195], v[96:99]\n"
      "v_sub_f32 v38, v38, v50\n"
      "v_cvt_pk_bf16_f32 v21, v21, v25\n"
      "v_mfma_f32_16x16x32_bf16 v[88:91], v[152:155], a[196:199], v[88:91]\n"
      "ds_read_b128 a[212:215], v2 offset:4224\n"
      "ds_write_addtid_b32 v21 offset:46848\n"
      "v_sub_f32 v39, v39, v51\n"
      "v_mfma_f32_16x16x32_bf16 v[92:95], v[144:147], a[196:199], v[92:95]\n"
      "v_fmac_f32_e64 v48, s45, v36\n"
      "v_cvt_pk_bf16_f32 v22, v22, v26\n"
      "v_mfma_f32_16x16x32_bf16 v[96:99], v[136:139], a[196:199], v[96:99]\n"
      "s_waitcnt lgkmcnt(3)\n"
      "ds_write_addtid_b32 v22 offset:50944\n"
      "v_fmac_f32 v49, s45, v37\n"
      "v_mfma_f32_16x16x32_bf16 v[88:91], v[128:131], a[200:203], v[88:91]\n"
      "ds_read_b128 a[192:195], v3 offset:50688\n"
      "v_fmac_f32 v50, s45, v38\n"
      "v_mfma_f32_16x16x32_bf16 v[92:95], v[120:123], a[200:203], v[92:95]\n"
      "s_nop 0\n"
      "v_fmac_f32 v51, s45, v39\n"
      "v_cvt_pk_bf16_f32 v23, v23, v27\n"
      "v_mfma_f32_16x16x32_bf16 v[96:99], v[112:115], a[200:203], v[96:99]\n"
      "s_waitcnt lgkmcnt(2)\n"
      "ds_write_addtid_b32 v23 offset:55040\n"
      "v_fma_f32 v36, s47, v36, v72\n"
      "v_mfma_f32_4x4x4_16b_bf16 v[12:15], a[216:217], a[220:221], 0\n"
      "v_fma_f32 v37, s47, v37, v73\n"
      "v_mfma_f32_4x4x4_16b_bf16 v[16:19], a[216:217], a[222:223], 0\n"
      "v_fma_f32 v38, s47, v38, v74\n"
      "v_fma_f32 v39, s47, v39, v75\n"
      "v_mfma_f32_16x16x32_bf16 v[88:91], v[104:107], a[204:207], v[88:91]\n"
      "ds_read_b128 a[196:199], v3 offset:50752\n"
      "v_cvt_pk_bf16_f32 v48, v48, v49\n"
      "v_cvt_pk_bf16_f32 v12, v12, v16\n"
      "v_mfma_f32_16x16x32_bf16 v[92:95], a[164:167], a[204:207], v[92:95]\n"
      "ds_write_addtid_b32 v48 offset:17408\n"
      "ds_write_addtid_b32 v12 offset:43264\n"
      "v_mfma_f32_16x16x32_bf16 v[96:99], a[156:159], a[204:207], v[96:99]\n"
      "v_cvt_pk_bf16_f32 v13, v13, v17\n"
      "v_cvt_pk_bf16_f32 v14, v14, v18\n"
      "v_mfma_f32_16x16x32_bf16 v[88:91], a[148:151], a[208:211], v[88:91]\n"
      "ds_read_b128 a[200:203], v3 offset:50816\n"
      "ds_write_addtid_b32 v13 offset:47360\n"
      "v_mfma_f32_16x16x32_bf16 v[92:95], a[140:143], a[208:211], v[92:95]\n"
      "ds_read_b64_tr_b16 a[224:225], v7 offset:53248\n"
      "ds_write_addtid_b32 v14 offset:51456\n"
      "v_cvt_pk_bf16_f32 v50, v50, v51\n"
      "v_mfma_f32_16x16x32_bf16 v[96:99], a[132:135], a[208:211], v[96:99]\n"
      "ds_write_addtid_b32 v50 offset:17920\n"
      "ds_read_b64_tr_b16 a[226:227], v7 offset:53256\n"
      "v_mfma_f32_16x16x32_bf16 v[88:91], a[124:127], a[212:215], v[88:91]\n"
      "ds_read_b128 a[204:207], v4 offset:50688\n"
      "v_cvt_pk_bf16_f32 v36, v36, v37\n"
      "v_mfma_f32_16x16x32_bf16 v[92:95], a[116:119], a[212:215], v[92:95]\n"
      "ds_write_addtid_b32 v36 offset:19456\n"
      "v_cvt_pk_bf16_f32 v15, v15, v19\n"
      "v_mfma_f32_16x16x32_bf16 v[96:99], a[108:111], a[212:215], v[96:99]\n"
      "s_waitcnt lgkmcnt(6)\n"
      "ds_write_addtid_b32 v15 offset:55552\n"
      "v_sub_f32 v76, v88, v76\n"
      "v_sub_f32_e64 v77, v89, v77\n"
      "v_cvt_pk_bf16_f32 v38, v38, v39\n"
      "v_mfma_f32_16x16x32_bf16 v[52:55], a[0:3], a[192:195], 0\n"
      "s_nop 0\n"
      "ds_read_b128 a[208:211], v4 offset:50752\n"
      "ds_write_addtid_b32 v38 offset:19968\n"
      "v_mfma_f32_16x16x32_bf16 v[56:59], a[4:7], a[192:195], 0\n"
      "s_waitcnt lgkmcnt(4)\n"
      "ds_write_addtid_b32 v76 offset:0\n"
      "ds_write_addtid_b32 v77 offset:512\n"
      "v_mfma_f32_4x4x4_16b_bf16 v[20:23], a[216:217], a[224:225], 0\n"
      "v_sub_f32 v78, v90, v78\n"
      "v_mfma_f32_4x4x4_16b_bf16 v[24:27], a[216:217], a[226:227], 0\n"
      "ds_write_addtid_b32 v78 offset:1024\n"
      "v_sub_f32 v79, v91, v79\n"
      "v_mfma_f32_16x16x32_bf16 v[60:63], a[8:11], a[192:195], 0\n"
      "ds_write_addtid_b32 v79 offset:1536\n"
      "v_sub_f32 v80, v92, v80\n"
      "v_sub_f32 v81, v93, v81\n"
      "v_mfma_f32_16x16x32_bf16 v[52:55], a[12:15], a[196:199], v[52:55]\n"
      "ds_read_b128 a[212:215], v4 offset:50816\n"
      "ds_write_addtid_b32 v80 offset:2048\n"
      "v_mfma_f32_16x16x32_bf16 v[56:59], a[16:19], a[196:199], v[56:59]\n"
      "ds_write_addtid_b32 v81 offset:2560\n"
      "v_sub_f32 v82, v94, v82\n"
      "v_mfma_f32_16x16x32_bf16 v[60:63], a[20:23], a[196:199], v[60:63]\n"
      "s_nop 0\n"
      "ds_write_addtid_b32 v82 offset:3072\n"
      "v_cvt_pk_bf16_f32 v20, v20, v24\n"
      "v_mfma_f32_16x16x32_bf16 v[52:55], a[24:27], a[200:203], v[52:55]\n"
      "ds_read_b128 a[192:195], v5 offset:50688\n"
      "ds_write_addtid_b32 v20 offset:43776\n"
      "v_sub_f32 v83, v95, v83\n"
      "v_mfma_f32_16x16x32_bf16 v[56:59], a[28:31], a[200:203], v[56:59]\n"
      "ds_write_addtid_b32 v83 offset:3584\n"
      "v_sub_f32 v84, v96, v84\n"
      "v_mfma_f32_16x16x32_bf16 v[60:63], a[32:35], a[200:203], v[60:63]\n"
      "ds_write_addtid_b32 v84 offset:4096\n"
      "v_cvt_pk_bf16_f32 v21, v21, v25\n"
      "v_mfma_f32_16x16x32_bf16 v[52:55], a[36:39], a[204:207], v[52:55]\n"
      "ds_read_b128 a[196:199], v5 offset:50752\n"
      "ds_write_addtid_b32 v21 offset:47872\n"
      "v_mfma_f32_16x16x32_bf16 v[56:59], a[40:43], a[204:207], v[56:59]\n"
      "v_sub_f32 v85, v97, v85\n"
      "v_sub_f32 v86, v98, v86\n"
      "v_cvt_pk_bf16_f32 v22, v22, v26\n"
      "v_mfma_f32_16x16x32_bf16 v[60:63], a[44:47], a[204:207], v[60:63]\n"
      "s_waitcnt lgkmcnt(4)\n"
      "ds_write_addtid_b32 v22 offset:51968\n"
      "ds_write_addtid_b32 v85 offset:4608\n"
      "v_sub_f32 v87, v99, v87\n"
      "v_mfma_f32_16x16x32_bf16 v[52:55], a[48:51], a[208:211], v[52:55]\n"
      "ds_read_b128 a[200:203], v5 offset:50816\n"
      "ds_write_addtid_b32 v86 offset:5120\n"
      "v_mfma_f32_16x16x32_bf16 v[56:59], a[52:55], a[208:211], v[56:59]\n"
      "ds_write_addtid_b32 v87 offset:5632\n"
      "v_cvt_pk_bf16_f32 v23, v23, v27\n"
      "v_mfma_f32_16x16x32_bf16 v[60:63], a[56:59], a[208:211], v[60:63]\n"
      "ds_write_addtid_b32 v23 offset:56064\n"
      "v_fma_f32 v40, 0.5, v76, -v88\n"
      "v_mfma_f32_16x16x32_bf16 v[52:55], a[60:63], a[212:215], v[52:55]\n"
      "v_fma_f32 v41, 0.5, v77, -v89\n"
      "v_fma_f32 v42, 0.5, v78, -v90\n"
      "v_mfma_f32_16x16x32_bf16 v[56:59], a[64:67], a[212:215], v[56:59]\n"
      "v_fma_f32 v43, 0.5, v79, -v91\n"
      "v_fma_f32 v44, 0.5, v80, -v92\n"
      "v_mfma_f32_16x16x32_bf16 v[60:63], a[68:71], a[212:215], v[60:63]\n"
      "s_waitcnt lgkmcnt(0)\n"
      "v_fma_f32 v45, 0.5, v81, -v93\n"
      "s_barrier\n"
      "v_mfma_f32_16x16x32_bf16 v[52:55], a[72:75], a[192:195], v[52:55]\n"
      "ds_read_b128 a[204:207], v0 offset:16384\n"
      "ds_read2st64_b32 v[28:29], v6 offset0:0 offset1:2\n"
      "v_fma_f32 v46, 0.5, v82, -v94\n"
      "v_mfma_f32_16x16x32_bf16 v[56:59], a[76:79], a[192:195], v[56:59]\n"
      "ds_read_b128 a[208:211], v0 offset:16448\n"
      "ds_read2st64_b32 v[30:31], v6 offset0:4 offset1:6\n"
      "v_mfma_f32_16x16x32_bf16 v[60:63], a[80:83], a[192:195], v[60:63]\n"
      "ds_read_b64_tr_b16 a[220:221], v7 offset:0\n"
      "v_fma_f32 v47, 0.5, v83, -v95\n"
      "v_mfma_f32_16x16x32_bf16 v[52:55], a[84:87], a[196:199], v[52:55]\n"
      "ds_read_b128 a[212:215], v0 offset:16512\n"
      "v_fma_f32 v48, 0.5, v84, -v96\n"
      "v_mfma_f32_16x16x32_bf16 v[56:59], a[88:91], a[196:199], v[56:59]\n"
      "ds_read_b64_tr_b16 a[222:223], v7 offset:8\n"
      "ds_read2st64_b32 v[32:33], v6 offset0:8 offset1:10\n"
      "v_fma_f32 v49, 0.5, v85, -v97\n"
      "v_mfma_f32_16x16x32_bf16 v[60:63], a[92:95], a[196:199], v[60:63]\n"
      "ds_read2st64_b32 v[34:35], v6 offset0:12 offset1:14\n"
      "v_fma_f32 v50, 0.5, v86, -v98\n"
      "v_mfma_f32_16x16x32_bf16 v[52:55], a[96:99], a[200:203], v[52:55]\n"
      "ds_read_b128 a[192:195], v1 offset:16384\n"
      "ds_read2st64_b32 v[36:37], v6 offset0:16 offset1:18\n"
      "v_mfma_f32_16x16x32_bf16 v[56:59], a[100:103], a[200:203], v[56:59]\n"
      "ds_read2st64_b32 v[38:39], v6 offset0:20 offset1:22\n"
      "v_fma_f32 v51, 0.5, v87, -v99\n"
      "v_mfma_f32_16x16x32_bf16 v[60:63], a[104:107], a[200:203], v[60:63]\n"
      "s_waitcnt lgkmcnt(8)\n"
      "v_fmac_f32 v28, s46, v64\n"
      "v_fmac_f32 v29, s46, v65\n"
      "v_fmac_f32 v30, s46, v66\n"
      "v_mfma_f32_16x16x32_bf16 v[76:79], v[252:255], a[204:207], v[52:55]\n"
      "s_waitcnt lgkmcnt(5)\n"
      "ds_read_b128 a[196:199], v1 offset:16448\n"
      "v_fmac_f32 v31, s46, v67\n"
      "v_mfma_f32_4x4x4_16b_bf16 v[12:15], a[216:217], a[220:221], 0\n"
      "v_sub_f32 v28, v28, v40\n"
      "v_mfma_f32_4x4x4_16b_bf16 v[16:19], a[216:217], a[222:223], 0\n"
      "v_sub_f32 v29, v29, v41\n"
      "v_sub_f32 v30, v30, v42\n"
      "v_mfma_f32_16x16x32_bf16 v[80:83], v[244:247], a[204:207], v[56:59]\n"
      "v_sub_f32 v31, v31, v43\n"
      "v_fmac_f32 v40, s45, v28\n"
      "v_mfma_f32_16x16x32_bf16 v[84:87], v[236:239], a[204:207], v[60:63]\n"
      "v_fmac_f32 v41, s45, v29\n"
      "v_cvt_pk_bf16_f32 v12, v12, v16\n"
      "v_fmac_f32 v42, s45, v30\n"
      "v_mfma_f32_16x16x32_bf16 v[76:79], v[228:231], a[208:211], v[76:79]\n"
      "s_nop 0\n"
      "ds_read_b128 a[200:203], v1 offset:16512\n"
      "ds_write_addtid_b32 v12 offset:24832\n"
      "v_mfma_f32_16x16x32_bf16 v[80:83], v[220:223], a[208:211], v[80:83]\n"
      "s_nop 0\n"
      "ds_read_b64_tr_b16 a[224:225], v7 offset:512\n"
      "ds_read_b64_tr_b16 a[226:227], v7 offset:520\n"
      "v_mfma_f32_16x16x32_bf16 v[84:87], v[212:215], a[208:211], v[84:87]\n"
      "s_nop 0\n"
      "v_fmac_f32_e64 v43, s45, v31\n"
      "v_cvt_pk_bf16_f32 v13, v13, v17\n"
      "v_mfma_f32_16x16x32_bf16 v[76:79], v[204:207], a[212:215], v[76:79]\n"
      "s_nop 0\n"
      "ds_read_b128 a[204:207], v2 offset:16384\n"
      "ds_write_addtid_b32 v13 offset:28928\n"
      "v_fma_f32 v28, s47, v28, v52\n"
      "v_mfma_f32_16x16x32_bf16 v[80:83], v[196:199], a[212:215], v[80:83]\n"
      "s_nop 0\n"
      "v_fma_f32 v29, s47, v29, v53\n"
      "v_cvt_pk_bf16_f32 v14, v14, v18\n"
      "v_mfma_f32_16x16x32_bf16 v[84:87], v[188:191], a[212:215], v[84:87]\n"
      "s_waitcnt lgkmcnt(4)\n"
      "ds_write_addtid_b32 v14 offset:33024\n"
      "v_fma_f32 v30, s47, v30, v54\n"
      "v_mfma_f32_16x16x32_bf16 v[76:79], v[180:183], a[192:195], v[76:79]\n"
      "s_nop 0\n"
      "ds_read_b128 a[208:211], v2 offset:16448\n"
      "v_fma_f32 v31, s47, v31, v55\n"
      "v_mfma_f32_16x16x32_bf16 v[80:83], v[172:175], a[192:195], v[80:83]\n"
      "s_nop 0\n"
      "v_cvt_pk_bf16_f32 v40, v40, v41\n"
      "v_cvt_pk_bf16_f32 v15, v15, v19\n"
      "v_mfma_f32_16x16x32_bf16 v[84:87], v[164:167], a[192:195], v[84:87]\n"
      "s_waitcnt lgkmcnt(2)\n"
      "ds_write_addtid_b32 v40 offset:12288\n"
      "ds_write_addtid_b32 v15 offset:37120\n"
      "v_cvt_pk_bf16_f32 v42, v42, v43\n"
      "v_mfma_f32_4x4x4_16b_bf16 v[20:23], a[216:217], a[224:225], 0\n"
      "v_cvt_pk_bf16_f32 v28, v28, v29\n"
      "v_mfma_f32_4x4x4_16b_bf16 v[24:27], a[216:217], a[226:227], 0\n"
      "ds_write_addtid_b32 v42 offset:12800\n"
      "ds_write_addtid_b32 v28 offset:14336\n"
      "v_mfma_f32_16x16x32_bf16 v[76:79], v[156:159], a[196:199], v[76:79]\n"
      "ds_read_b128 a[212:215], v2 offset:16512\n"
      "v_cvt_pk_bf16_f32 v30, v30, v31\n"
      "v_mfma_f32_16x16x32_bf16 v[80:83], v[148:151], a[196:199], v[80:83]\n"
      "ds_write_addtid_b32 v30 offset:14848\n"
      "v_fmac_f32 v32, s46, v68\n"
      "v_cvt_pk_bf16_f32 v20, v20, v24\n"
      "v_mfma_f32_16x16x32_bf16 v[84:87], v[140:143], a[196:199], v[84:87]\n"
      "ds_write_addtid_b32 v20 offset:25344\n"
      "v_fmac_f32 v33, s46, v69\n"
      "v_mfma_f32_16x16x32_bf16 v[76:79], v[132:135], a[200:203], v[76:79]\n"
      "ds_read_b128 a[192:195], v0 offset:20480\n"
      "ds_read_b64_tr_b16 a[228:229], v7 offset:1024\n"
      "v_mfma_f32_16x16x32_bf16 v[80:83], v[124:127], a[200:203], v[80:83]\n"
      "ds_read_b64_tr_b16 a[230:231], v7 offset:1032\n"
      "v_fmac_f32 v34, s46, v70\n"
      "v_mfma_f32_16x16x32_bf16 v[84:87], v[116:119], a[200:203], v[84:87]\n"
      "v_fmac_f32 v35, s46, v71\n"
      "v_cvt_pk_bf16_f32 v21, v21, v25\n"
      "v_mfma_f32_16x16x32_bf16 v[76:79], v[108:111], a[204:207], v[76:79]\n"
      "ds_read_b128 a[196:199], v0 offset:20544\n"
      "ds_write_addtid_b32 v21 offset:29440\n"
      "v_sub_f32 v32, v32, v44\n"
      "v_mfma_f32_16x16x32_bf16 v[80:83], v[100:103], a[204:207], v[80:83]\n"
      "v_sub_f32 v33, v33, v45\n"
      "v_cvt_pk_bf16_f32 v22, v22, v26\n"
      "v_mfma_f32_16x16x32_bf16 v[84:87], a[160:163], a[204:207], v[84:87]\n"
      "s_waitcnt lgkmcnt(2)\n"
      "ds_write_addtid_b32 v22 offset:33536\n"
      "v_sub_f32 v34, v34, v46\n"
      "v_mfma_f32_16x16x32_bf16 v[76:79], a[152:155], a[208:211], v[76:79]\n"
      "ds_read_b128 a[200:203], v0 offset:20608\n"
      "v_sub_f32 v35, v35, v47\n"
      "v_mfma_f32_16x16x32_bf16 v[80:83], a[144:147], a[208:211], v[80:83]\n"
      "v_fmac_f32 v44, s45, v32\n"
      "v_cvt_pk_bf16_f32 v23, v23, v27\n"
      "v_mfma_f32_4x4x4_16b_bf16 v[12:15], a[216:217], a[228:229], 0\n"
      "v_fmac_f32 v45, s45, v33\n"
      "v_mfma_f32_4x4x4_16b_bf16 v[16:19], a[216:217], a[230:231], 0\n"
      "ds_write_addtid_b32 v23 offset:37632\n"
      "v_fmac_f32 v46, s45, v34\n"
      "v_mfma_f32_16x16x32_bf16 v[84:87], a[136:139], a[208:211], v[84:87]\n"
      "v_fmac_f32 v47, s45, v35\n"
      "v_fma_f32 v32, s47, v32, v56\n"
      "v_cvt_pk_bf16_f32 v12, v12, v16\n"
      "v_mfma_f32_16x16x32_bf16 v[76:79], a[128:131], a[212:215], v[76:79]\n"
      "ds_read_b128 a[204:207], v1 offset:20480\n"
      "ds_write_addtid_b32 v12 offset:25856\n"
      "v_mfma_f32_16x16x32_bf16 v[80:83], a[120:123], a[212:215], v[80:83]\n"
      "s_nop 0\n"
      "v_fma_f32 v33, s47, v33, v57\n"
      "v_cvt_pk_bf16_f32 v13, v13, v17\n"
      "v_mfma_f32_16x16x32_bf16 v[84:87], a[112:115], a[212:215], v[84:87]\n"
      "ds_read_b64_tr_b16 a[232:233], v7 offset:1536\n"
      "ds_write_addtid_b32 v13 offset:29952\n"
      "v_fma_f32 v34, s47, v34, v58\n"
      "v_mfma_f32_16x16x32_bf16 v[88:91], v[248:251], a[192:195], v[52:55]\n"
      "ds_read_b128 a[208:211], v1 offset:20544\n"
      "ds_read_b64_tr_b16 a[234:235], v7 offset:1544\n"
      "v_mfma_f32_16x16x32_bf16 v[92:95], v[240:243], a[192:195], v[56:59]\n"
      "v_fma_f32 v35, s47, v35, v59\n"
      "v_cvt_pk_bf16_f32 v44, v44, v45\n"
      "v_mfma_f32_16x16x32_bf16 v[96:99], v[232:235], a[192:195], v[60:63]\n"
      "s_waitcnt lgkmcnt(4)\n"
      "ds_write_addtid_b32 v44 offset:20484\n"
      "v_cvt_pk_bf16_f32 v14, v14, v18\n"
      "v_mfma_f32_16x16x32_bf16 v[88:91], v[224:227], a[196:199], v[88:91]\n"
      "s_nop 0\n"
      "ds_read_b128 a[212:215], v1 offset:20608\n"
      "ds_write_addtid_b32 v14 offset:34048\n"
      "v_cvt_pk_bf16_f32 v46, v46, v47\n"
      "v_mfma_f32_16x16x32_bf16 v[92:95], v[216:219], a[196:199], v[92:95]\n"
      "ds_write_addtid_b32 v46 offset:20996\n"
      "v_cvt_pk_bf16_f32 v32, v32, v33\n"
      "v_mfma_f32_16x16x32_bf16 v[96:99], v[208:211], a[196:199], v[96:99]\n"
      "ds_write_addtid_b32 v32 offset:22532\n"
      "v_cvt_pk_bf16_f32 v34, v34, v35\n"
      "v_mfma_f32_16x16x32_bf16 v[88:91], v[200:203], a[200:203], v[88:91]\n"
      "s_waitcnt lgkmcnt(4)\n"
      "ds_read_b128 a[192:195], v2 offset:20480\n"
      "ds_write_addtid_b32 v34 offset:23044\n"
      "v_fmac_f32 v36, s46, v72\n"
      "v_mfma_f32_4x4x4_16b_bf16 v[20:23], a[216:217], a[232:233], 0\n"
      "v_fmac_f32 v37, s46, v73\n"
      "v_mfma_f32_4x4x4_16b_bf16 v[24:27], a[216:217], a[234:235], 0\n"
      "v_fmac_f32 v38, s46, v74\n"
      "v_cvt_pk_bf16_f32 v15, v15, v19\n"
      "v_mfma_f32_16x16x32_bf16 v[92:95], v[192:195], a[200:203], v[92:95]\n"
      "ds_write_addtid_b32 v15 offset:38144\n"
      "v_fmac_f32 v39, s46, v75\n"
      "v_cvt_pk_bf16_f32 v20, v20, v24\n"
      "v_mfma_f32_16x16x32_bf16 v[96:99], v[184:187], a[200:203], v[96:99]\n"
      "ds_write_addtid_b32 v20 offset:26368\n"
      "v_sub_f32 v36, v36, v48\n"
      "v_mfma_f32_16x16x32_bf16 v[88:91], v[176:179], a[204:207], v[88:91]\n"
      "ds_read_b128 a[196:199], v2 offset:20544\n"
      "v_sub_f32 v37, v37, v49\n"
      "v_mfma_f32_16x16x32_bf16 v[92:95], v[168:171], a[204:207], v[92:95]\n"
      "ds_read_b64_tr_b16 a[220:221], v7 offset:2048\n"
      "ds_read_b64_tr_b16 a[222:223], v7 offset:2056\n"
      "v_mfma_f32_16x16x32_bf16 v[96:99], v[160:163], a[204:207], v[96:99]\n"
      "v_sub_f32 v38, v38, v50\n"
      "v_cvt_pk_bf16_f32 v21, v21, v25\n"
      "v_mfma_f32_16x16x32_bf16 v[88:91], v[152:155], a[208:211], v[88:91]\n"
      "ds_read_b128 a[200:203], v2 offset:20608\n"
      "ds_write_addtid_b32 v21 offset:30464\n"
      "v_sub_f32 v39, v39, v51\n"
      "v_mfma_f32_16x16x32_bf16 v[92:95], v[144:147], a[208:211], v[92:95]\n"
      "v_fmac_f32 v48, s45, v36\n"
      "v_cvt_pk_bf16_f32 v22, v22, v26\n"
      "v_mfma_f32_16x16x32_bf16 v[96:99], v[136:139], a[208:211], v[96:99]\n"
      "s_waitcnt lgkmcnt(3)\n"
      "ds_write_addtid_b32 v22 offset:34560\n"
      "v_fmac_f32 v49, s45, v37\n"
      "v_mfma_f32_16x16x32_bf16 v[88:91], v[128:131], a[212:215], v[88:91]\n"
      "ds_read_b128 a[204:207], v3 offset:0\n"
      "v_fmac_f32 v50, s45, v38\n"
      "v_mfma_f32_16x16x32_bf16 v[92:95], v[120:123], a[212:215], v[92:95]\n"
      "v_fmac_f32_e64 v51, s45, v39\n"
      "v_cvt_pk_bf16_f32 v23, v23, v27\n"
      "v_mfma_f32_16x16x32_bf16 v[96:99], v[112:115], a[212:215], v[96:99]\n"
      "s_waitcnt lgkmcnt(2)\n"
      "ds_write_addtid_b32 v23 offset:38656\n"
      "v_fma_f32 v36, s47, v36, v60\n"
      "v_mfma_f32_4x4x4_16b_bf16 v[12:15], a[216:217], a[220:221], 0\n"
      "v_fma_f32 v37, s47, v37, v61\n"
      "v_mfma_f32_4x4x4_16b_bf16 v[16:19], a[216:217], a[222:223], 0\n"
      "v_fma_f32 v38, s47, v38, v62\n"
      "v_fma_f32 v39, s47, v39, v63\n"
      "v_mfma_f32_16x16x32_bf16 v[88:91], v[104:107], a[192:195], v[88:91]\n"
      "ds_read_b128 a[208:211], v3 offset:64\n"
      "v_cvt_pk_bf16_f32 v48, v48, v49\n"
      "v_cvt_pk_bf16_f32 v12, v12, v16\n"
      "v_mfma_f32_16x16x32_bf16 v[92:95], a[164:167], a[192:195], v[92:95]\n"
      "ds_write_addtid_b32 v48 offset:16384\n"
      "ds_write_addtid_b32 v12 offset:26880\n"
      "v_mfma_f32_16x16x32_bf16 v[96:99], a[156:159], a[192:195], v[96:99]\n"
      "v_cvt_pk_bf16_f32 v13, v13, v17\n"
      "v_cvt_pk_bf16_f32 v14, v14, v18\n"
      "v_mfma_f32_16x16x32_bf16 v[88:91], a[148:151], a[196:199], v[88:91]\n"
      "ds_read_b128 a[212:215], v3 offset:128\n"
      "ds_write_addtid_b32 v13 offset:30976\n"
      "v_mfma_f32_16x16x32_bf16 v[92:95], a[140:143], a[196:199], v[92:95]\n"
      "ds_read_b64_tr_b16 a[224:225], v7 offset:2560\n"
      "ds_write_addtid_b32 v14 offset:35072\n"
      "v_cvt_pk_bf16_f32 v50, v50, v51\n"
      "v_mfma_f32_16x16x32_bf16 v[96:99], a[132:135], a[196:199], v[96:99]\n"
      "ds_write_addtid_b32 v50 offset:16896\n"
      "ds_read_b64_tr_b16 a[226:227], v7 offset:2568\n"
      "v_mfma_f32_16x16x32_bf16 v[88:91], a[124:127], a[200:203], v[88:91]\n"
      "ds_read_b128 a[192:195], v4 offset:0\n"
      "v_cvt_pk_bf16_f32 v36, v36, v37\n"
      "v_mfma_f32_16x16x32_bf16 v[92:95], a[116:119], a[200:203], v[92:95]\n"
      "ds_write_addtid_b32 v36 offset:18432\n"
      "v_cvt_pk_bf16_f32 v15, v15, v19\n"
      "v_mfma_f32_16x16x32_bf16 v[96:99], a[108:111], a[200:203], v[96:99]\n"
      "s_waitcnt lgkmcnt(6)\n"
      "ds_write_addtid_b32 v15 offset:39168\n"
      "v_sub_f32 v76, v88, v76\n"
      "v_sub_f32 v77, v89, v77\n"
      "v_cvt_pk_bf16_f32 v38, v38, v39\n"
      "v_mfma_f32_16x16x32_bf16 v[64:67], a[0:3], a[204:207], 0\n"
      "s_nop 0\n"
      "ds_read_b128 a[196:199], v4 offset:64\n"
      "ds_write_addtid_b32 v38 offset:18944\n"
      "v_mfma_f32_16x16x32_bf16 v[68:71], a[4:7], a[204:207], 0\n"
      "s_waitcnt lgkmcnt(4)\n"
      "ds_write_addtid_b32 v76 offset:6144\n"
      "ds_write_addtid_b32 v77 offset:6656\n"
      "v_mfma_f32_4x4x4_16b_bf16 v[20:23], a[216:217], a[224:225], 0\n"
      "v_sub_f32 v78, v90, v78\n"
      "v_mfma_f32_4x4x4_16b_bf16 v[24:27], a[216:217], a[226:227], 0\n"
      "ds_write_addtid_b32 v78 offset:7168\n"
      "v_sub_f32_e64 v79, v91, v79\n"
      "v_mfma_f32_16x16x32_bf16 v[72:75], a[8:11], a[204:207], 0\n"
      "ds_write_addtid_b32 v79 offset:7680\n"
      "v_sub_f32 v80, v92, v80\n"
      "v_sub_f32 v81, v93, v81\n"
      "v_mfma_f32_16x16x32_bf16 v[64:67], a[12:15], a[208:211], v[64:67]\n"
      "ds_read_b128 a[200:203], v4 offset:128\n"
      "ds_write_addtid_b32 v80 offset:8192\n"
      "v_mfma_f32_16x16x32_bf16 v[68:71], a[16:19], a[208:211], v[68:71]\n"
      "ds_write_addtid_b32 v81 offset:8704\n"
      "v_sub_f32 v82, v94, v82\n"
      "v_mfma_f32_16x16x32_bf16 v[72:75], a[20:23], a[208:211], v[72:75]\n"
      "ds_write_addtid_b32 v82 offset:9216\n"
      "v_cvt_pk_bf16_f32 v20, v20, v24\n"
      "v_mfma_f32_16x16x32_bf16 v[64:67], a[24:27], a[212:215], v[64:67]\n"
      "ds_read_b128 a[204:207], v5 offset:0\n"
      "ds_write_addtid_b32 v20 offset:27392\n"
      "v_sub_f32 v83, v95, v83\n"
      "v_mfma_f32_16x16x32_bf16 v[68:71], a[28:31], a[212:215], v[68:71]\n"
      "ds_write_addtid_b32 v83 offset:9728\n"
      "v_sub_f32 v84, v96, v84\n"
      "v_mfma_f32_16x16x32_bf16 v[72:75], a[32:35], a[212:215], v[72:75]\n"
      "ds_write_addtid_b32 v84 offset:10240\n"
      "v_cvt_pk_bf16_f32 v21, v21, v25\n"
      "v_mfma_f32_16x16x32_bf16 v[64:67], a[36:39], a[192:195], v[64:67]\n"
      "s_nop 0\n"
      "ds_read_b128 a[208:211], v5 offset:64\n"
      "ds_write_addtid_b32 v21 offset:31488\n"
      "v_mfma_f32_16x16x32_bf16 v[68:71], a[40:43], a[192:195], v[68:71]\n"
      "v_sub_f32 v85, v97, v85\n"
      "v_sub_f32 v86, v98, v86\n"
      "v_cvt_pk_bf16_f32 v22, v22, v26\n"
      "v_mfma_f32_16x16x32_bf16 v[72:75], a[44:47], a[192:195], v[72:75]\n"
      "s_waitcnt lgkmcnt(4)\n"
      "ds_write_addtid_b32 v22 offset:35584\n"
      "ds_write_addtid_b32 v85 offset:10752\n"
      "v_sub_f32 v87, v99, v87\n"
      "v_mfma_f32_16x16x32_bf16 v[64:67], a[48:51], a[196:199], v[64:67]\n"
      "ds_read_b128 a[212:215], v5 offset:128\n"
      "ds_write_addtid_b32 v86 offset:11264\n"
      "v_mfma_f32_16x16x32_bf16 v[68:71], a[52:55], a[196:199], v[68:71]\n"
      "ds_write_addtid_b32 v87 offset:11776\n"
      "v_cvt_pk_bf16_f32 v23, v23, v27\n"
      "v_mfma_f32_16x16x32_bf16 v[72:75], a[56:59], a[196:199], v[72:75]\n"
      "ds_write_addtid_b32 v23 offset:39680\n"
      "v_fma_f32 v40, 0.5, v76, -v88\n"
      "v_mfma_f32_16x16x32_bf16 v[64:67], a[60:63], a[200:203], v[64:67]\n"
      "v_fma_f32 v41, 0.5, v77, -v89\n"
      "v_fma_f32 v42, 0.5, v78, -v90\n"
      "v_mfma_f32_16x16x32_bf16 v[68:71], a[64:67], a[200:203], v[68:71]\n"
      "s_nop 0\n"
      "v_fma_f32 v43, 0.5, v79, -v91\n"
      "v_fma_f32 v44, 0.5, v80, -v92\n"
      "v_mfma_f32_16x16x32_bf16 v[72:75], a[68:71], a[200:203], v[72:75]\n"
      "s_waitcnt lgkmcnt(0)\n"
      "v_fma_f32 v45, 0.5, v81, -v93\n"
      "s_barrier\n"
      "v_mfma_f32_16x16x32_bf16 v[64:67], a[72:75], a[204:207], v[64:67]\n"
      "ds_read_b128 a[192:195], v0 offset:0\n"
      "ds_read2st64_b32 v[28:29], v6 offset0:24 offset1:26\n"
      "v_fma_f32 v46, 0.5, v82, -v94\n"
      "v_mfma_f32_16x16x32_bf16 v[68:71], a[76:79], a[204:207], v[68:71]\n"
      "s_nop 0\n"
      "ds_read_b128 a[196:199], v0 offset:64\n"
      "ds_read2st64_b32 v[30:31], v6 offset0:28 offset1:30\n"
      "v_mfma_f32_16x16x32_bf16 v[72:75], a[80:83], a[204:207], v[72:75]\n"
      "ds_read_b64_tr_b16 a[220:221], v7 offset:16896\n"
      "v_fma_f32 v47, 0.5, v83, -v95\n"
      "v_mfma_f32_16x16x32_bf16 v[64:67], a[84:87], a[208:211], v[64:67]\n"
      "ds_read_b128 a[200:203], v0 offset:128\n"
      "v_fma_f32 v48, 0.5, v84, -v96\n"
      "v_mfma_f32_16x16x32_bf16 v[68:71], a[88:91], a[208:211], v[68:71]\n"
      "ds_read_b64_tr_b16 a[222:223], v7 offset:16904\n"
      "ds_read2st64_b32 v[32:33], v6 offset0:32 offset1:34\n"
      "v_fma_f32 v49, 0.5, v85, -v97\n"
      "v_mfma_f32_16x16x32_bf16 v[72:75], a[92:95], a[208:211], v[72:75]\n"
      "ds_read2st64_b32 v[34:35], v6 offset0:36 offset1:38\n"
      "v_fma_f32 v50, 0.5, v86, -v98\n"
      "v_mfma_f32_16x16x32_bf16 v[64:67], a[96:99], a[212:215], v[64:67]\n"
      "ds_read_b128 a[204:207], v1 offset:0\n"
      "ds_read2st64_b32 v[36:37], v6 offset0:40 offset1:42\n"
      "v_mfma_f32_16x16x32_bf16 v[68:71], a[100:103], a[212:215], v[68:71]\n"
      "ds_read2st64_b32 v[38:39], v6 offset0:44 offset1:46\n"
      "v_fma_f32 v51, 0.5, v87, -v99\n"
      "v_mfma_f32_16x16x32_bf16 v[72:75], a[104:107], a[212:215], v[72:75]\n"
      "s_waitcnt lgkmcnt(8)\n"
      "v_fmac_f32 v28, s46, v52\n"
      "v_fmac_f32 v29, s46, v53\n"
      "v_fmac_f32 v30, s46, v54\n"
      "v_mfma_f32_16x16x32_bf16 v[76:79], v[252:255], a[192:195], v[64:67]\n"
      "s_waitcnt lgkmcnt(5)\n"
      "ds_read_b128 a[208:211], v1 offset:64\n"
      "v_fmac_f32 v31, s46, v55\n"
      "v_mfma_f32_4x4x4_16b_bf16 v[12:15], a[216:217], a[220:221], 0\n"
      "v_sub_f32 v28, v28, v40\n"
      "v_mfma_f32_4x4x4_16b_bf16 v[16:19], a[216:217], a[222:223], 0\n"
      "v_sub_f32 v29, v29, v41\n"
      "v_sub_f32 v30, v30, v42\n"
      "v_mfma_f32_16x16x32_bf16 v[80:83], v[244:247], a[192:195], v[68:71]\n"
      "v_sub_f32 v31, v31, v43\n"
      "v_fmac_f32_e64 v40, s45, v28\n"
      "v_mfma_f32_16x16x32_bf16 v[84:87], v[236:239], a[192:195], v[72:75]\n"
      "v_fmac_f32 v41, s45, v29\n"
      "v_cvt_pk_bf16_f32 v12, v12, v16\n"
      "v_fmac_f32 v42, s45, v30\n"
      "v_mfma_f32_16x16x32_bf16 v[76:79], v[228:231], a[196:199], v[76:79]\n"
      "ds_read_b128 a[212:215], v1 offset:128\n"
      "ds_write_addtid_b32 v12 offset:41216\n"
      "v_mfma_f32_16x16x32_bf16 v[80:83], v[220:223], a[196:199], v[80:83]\n"
      "ds_read_b64_tr_b16 a[224:225], v7 offset:17408\n"
      "ds_read_b64_tr_b16 a[226:227], v7 offset:17416\n"
      "v_mfma_f32_16x16x32_bf16 v[84:87], v[212:215], a[196:199], v[84:87]\n"
      "v_fmac_f32 v43, s45, v31\n"
      "v_cvt_pk_bf16_f32 v13, v13, v17\n"
      "v_mfma_f32_16x16x32_bf16 v[76:79], v[204:207], a[200:203], v[76:79]\n"
      "s_nop 0\n"
      "ds_read_b128 a[192:195], v2 offset:0\n"
      "ds_write_addtid_b32 v13 offset:45312\n"
      "v_fma_f32 v28, s47, v28, v64\n"
      "v_mfma_f32_16x16x32_bf16 v[80:83], v[196:199], a[200:203], v[80:83]\n"
      "v_fma_f32 v29, s47, v29, v65\n"
      "v_cvt_pk_bf16_f32 v14, v14, v18\n"
      "v_mfma_f32_16x16x32_bf16 v[84:87], v[188:191], a[200:203], v[84:87]\n"
      "s_waitcnt lgkmcnt(4)\n"
      "ds_write_addtid_b32 v14 offset:49408\n"
      "v_fma_f32 v30, s47, v30, v66\n"
      "v_mfma_f32_16x16x32_bf16 v[76:79], v[180:183], a[204:207], v[76:79]\n"
      "s_nop 0\n"
      "ds_read_b128 a[196:199], v2 offset:64\n"
      "v_fma_f32 v31, s47, v31, v67\n"
      "v_mfma_f32_16x16x32_bf16 v[80:83], v[172:175], a[204:207], v[80:83]\n"
      "s_nop 0\n"
      "v_cvt_pk_bf16_f32 v40, v40, v41\n"
      "v_cvt_pk_bf16_f32 v15, v15, v19\n"
      "v_mfma_f32_16x16x32_bf16 v[84:87], v[164:167], a[204:207], v[84:87]\n"
      "s_waitcnt lgkmcnt(2)\n"
      "ds_write_addtid_b32 v40 offset:13312\n"
      "ds_write_addtid_b32 v15 offset:53504\n"
      "v_cvt_pk_bf16_f32 v42, v42, v43\n"
      "v_mfma_f32_4x4x4_16b_bf16 v[20:23], a[216:217], a[224:225], 0\n"
      "v_cvt_pk_bf16_f32 v28, v28, v29\n"
      "v_mfma_f32_4x4x4_16b_bf16 v[24:27], a[216:217], a[226:227], 0\n"
      "ds_write_addtid_b32 v42 offset:13824\n"
      "ds_write_addtid_b32 v28 offset:15360\n"
      "v_mfma_f32_16x16x32_bf16 v[76:79], v[156:159], a[208:211], v[76:79]\n"
      "ds_read_b128 a[200:203], v2 offset:128\n"
      "v_cvt_pk_bf16_f32 v30, v30, v31\n"
      "v_mfma_f32_16x16x32_bf16 v[80:83], v[148:151], a[208:211], v[80:83]\n"
      "ds_write_addtid_b32 v30 offset:15872\n"
      "v_fmac_f32 v32, s46, v56\n"
      "v_cvt_pk_bf16_f32 v20, v20, v24\n"
      "v_mfma_f32_16x16x32_bf16 v[84:87], v[140:143], a[208:211], v[84:87]\n"
      "ds_write_addtid_b32 v20 offset:41728\n"
      "v_fmac_f32 v33, s46, v57\n"
      "v_mfma_f32_16x16x32_bf16 v[76:79], v[132:135], a[212:215], v[76:79]\n"
      "ds_read_b128 a[204:207], v0 offset:4096\n"
      "ds_read_b64_tr_b16 a[228:229], v7 offset:17920\n"
      "v_mfma_f32_16x16x32_bf16 v[80:83], v[124:127], a[212:215], v[80:83]\n"
      "ds_read_b64_tr_b16 a[230:231], v7 offset:17928\n"
      "v_fmac_f32 v34, s46, v58\n"
      "v_mfma_f32_16x16x32_bf16 v[84:87], v[116:119], a[212:215], v[84:87]\n"
      "v_fmac_f32 v35, s46, v59\n"
      "v_cvt_pk_bf16_f32 v21, v21, v25\n"
      "v_mfma_f32_16x16x32_bf16 v[76:79], v[108:111], a[192:195], v[76:79]\n"
      "ds_read_b128 a[208:211], v0 offset:4160\n"
      "ds_write_addtid_b32 v21 offset:45824\n"
      "v_sub_f32 v32, v32, v44\n"
      "v_mfma_f32_16x16x32_bf16 v[80:83], v[100:103], a[192:195], v[80:83]\n"
      "v_sub_f32_e64 v33, v33, v45\n"
      "v_cvt_pk_bf16_f32 v22, v22, v26\n"
      "v_mfma_f32_16x16x32_bf16 v[84:87], a[160:163], a[192:195], v[84:87]\n"
      "s_waitcnt lgkmcnt(2)\n"
      "ds_write_addtid_b32 v22 offset:49920\n"
      "v_sub_f32 v34, v34, v46\n"
      "v_mfma_f32_16x16x32_bf16 v[76:79], a[152:155], a[196:199], v[76:79]\n"
      "ds_read_b128 a[212:215], v0 offset:4224\n"
      "v_sub_f32 v35, v35, v47\n"
      "v_mfma_f32_16x16x32_bf16 v[80:83], a[144:147], a[196:199], v[80:83]\n"
      "v_fmac_f32 v44, s45, v32\n"
      "v_cvt_pk_bf16_f32 v23, v23, v27\n"
      "v_mfma_f32_4x4x4_16b_bf16 v[12:15], a[216:217], a[228:229], 0\n"
      "v_fmac_f32 v45, s45, v33\n"
      "v_mfma_f32_4x4x4_16b_bf16 v[16:19], a[216:217], a[230:231], 0\n"
      "ds_write_addtid_b32 v23 offset:54016\n"
      "v_fmac_f32 v46, s45, v34\n"
      "v_mfma_f32_16x16x32_bf16 v[84:87], a[136:139], a[196:199], v[84:87]\n"
      "v_fmac_f32 v47, s45, v35\n"
      "v_fma_f32 v32, s47, v32, v68\n"
      "v_cvt_pk_bf16_f32 v12, v12, v16\n"
      "v_mfma_f32_16x16x32_bf16 v[76:79], a[128:131], a[200:203], v[76:79]\n"
      "ds_read_b128 a[192:195], v1 offset:4096\n"
      "ds_write_addtid_b32 v12 offset:42240\n"
      "v_mfma_f32_16x16x32_bf16 v[80:83], a[120:123], a[200:203], v[80:83]\n"
      "v_fma_f32 v33, s47, v33, v69\n"
      "v_cvt_pk_bf16_f32 v13, v13, v17\n"
      "v_mfma_f32_16x16x32_bf16 v[84:87], a[112:115], a[200:203], v[84:87]\n"
      "ds_read_b64_tr_b16 a[232:233], v7 offset:18432\n"
      "ds_write_addtid_b32 v13 offset:46336\n"
      "v_fma_f32 v34, s47, v34, v70\n"
      "v_mfma_f32_16x16x32_bf16 v[88:91], v[248:251], a[204:207], v[64:67]\n"
      "ds_read_b128 a[196:199], v1 offset:4160\n"
      "ds_read_b64_tr_b16 a[234:235], v7 offset:18440\n"
      "v_mfma_f32_16x16x32_bf16 v[92:95], v[240:243], a[204:207], v[68:71]\n"
      "s_nop 0\n"
      "v_fma_f32 v35, s47, v35, v71\n"
      "v_cvt_pk_bf16_f32 v44, v44, v45\n"
      "v_mfma_f32_16x16x32_bf16 v[96:99], v[232:235], a[204:207], v[72:75]\n"
      "s_waitcnt lgkmcnt(4)\n"
      "ds_write_addtid_b32 v44 offset:21508\n"
      "v_cvt_pk_bf16_f32 v14, v14, v18\n"
      "v_mfma_f32_16x16x32_bf16 v[88:91], v[224:227], a[208:211], v[88:91]\n"
      "ds_read_b128 a[200:203], v1 offset:4224\n"
      "ds_write_addtid_b32 v14 offset:50432\n"
      "v_cvt_pk_bf16_f32 v46, v46, v47\n"
      "v_mfma_f32_16x16x32_bf16 v[92:95], v[216:219], a[208:211], v[92:95]\n"
      "ds_write_addtid_b32 v46 offset:22020\n"
      "v_cvt_pk_bf16_f32 v32, v32, v33\n"
      "v_mfma_f32_16x16x32_bf16 v[96:99], v[208:211], a[208:211], v[96:99]\n"
      "ds_write_addtid_b32 v32 offset:23556\n"
      "v_cvt_pk_bf16_f32 v34, v34, v35\n"
      "v_mfma_f32_16x16x32_bf16 v[88:91], v[200:203], a[212:215], v[88:91]\n"
      "s_waitcnt lgkmcnt(4)\n"
      "ds_read_b128 a[204:207], v2 offset:4096\n"
      "ds_write_addtid_b32 v34 offset:24068\n"
      "v_fmac_f32 v36, s46, v60\n"
      "v_mfma_f32_4x4x4_16b_bf16 v[20:23], a[216:217], a[232:233], 0\n"
      "v_fmac_f32 v37, s46, v61\n"
      "v_mfma_f32_4x4x4_16b_bf16 v[24:27], a[216:217], a[234:235], 0\n"
      "v_fmac_f32 v38, s46, v62\n"
      "v_cvt_pk_bf16_f32 v15, v15, v19\n"
      "v_mfma_f32_16x16x32_bf16 v[92:95], v[192:195], a[212:215], v[92:95]\n"
      "ds_write_addtid_b32 v15 offset:54528\n"
      "v_fmac_f32_e64 v39, s46, v63\n"
      "v_cvt_pk_bf16_f32 v20, v20, v24\n"
      "v_mfma_f32_16x16x32_bf16 v[96:99], v[184:187], a[212:215], v[96:99]\n"
      "ds_write_addtid_b32 v20 offset:42752\n"
      "v_sub_f32 v36, v36, v48\n"
      "v_mfma_f32_16x16x32_bf16 v[88:91], v[176:179], a[192:195], v[88:91]\n"
      "ds_read_b128 a[208:211], v2 offset:4160\n"
      "v_sub_f32 v37, v37, v49\n"
      "v_mfma_f32_16x16x32_bf16 v[92:95], v[168:171], a[192:195], v[92:95]\n"
      "ds_read_b64_tr_b16 a[220:221], v7 offset:18944\n"
      "ds_read_b64_tr_b16 a[222:223], v7 offset:18952\n"
      "v_mfma_f32_16x16x32_bf16 v[96:99], v[160:163], a[192:195], v[96:99]\n"
      "v_sub_f32 v38, v38, v50\n"
      "v_cvt_pk_bf16_f32 v21, v21, v25\n"
      "v_mfma_f32_16x16x32_bf16 v[88:91], v[152:155], a[196:199], v[88:91]\n"
      "ds_read_b128 a[212:215], v2 offset:4224\n"
      "ds_write_addtid_b32 v21 offset:46848\n"
      "v_sub_f32 v39, v39, v51\n"
      "v_mfma_f32_16x16x32_bf16 v[92:95], v[144:147], a[196:199], v[92:95]\n"
      "v_fmac_f32_e64 v48, s45, v36\n"
      "v_cvt_pk_bf16_f32 v22, v22, v26\n"
      "v_mfma_f32_16x16x32_bf16 v[96:99], v[136:139], a[196:199], v[96:99]\n"
      "s_waitcnt lgkmcnt(3)\n"
      "ds_write_addtid_b32 v22 offset:50944\n"
      "v_fmac_f32 v49, s45, v37\n"
      "v_mfma_f32_16x16x32_bf16 v[88:91], v[128:131], a[200:203], v[88:91]\n"
      "ds_read_b128 a[192:195], v3 offset:16896\n"
      "v_fmac_f32 v50, s45, v38\n"
      "v_mfma_f32_16x16x32_bf16 v[92:95], v[120:123], a[200:203], v[92:95]\n"
      "s_nop 0\n"
      "v_fmac_f32 v51, s45, v39\n"
      "v_cvt_pk_bf16_f32 v23, v23, v27\n"
      "v_mfma_f32_16x16x32_bf16 v[96:99], v[112:115], a[200:203], v[96:99]\n"
      "s_waitcnt lgkmcnt(2)\n"
      "ds_write_addtid_b32 v23 offset:55040\n"
      "v_fma_f32 v36, s47, v36, v72\n"
      "v_mfma_f32_4x4x4_16b_bf16 v[12:15], a[216:217], a[220:221], 0\n"
      "v_fma_f32 v37, s47, v37, v73\n"
      "v_mfma_f32_4x4x4_16b_bf16 v[16:19], a[216:217], a[222:223], 0\n"
      "v_fma_f32 v38, s47, v38, v74\n"
      "v_fma_f32 v39, s47, v39, v75\n"
      "v_mfma_f32_16x16x32_bf16 v[88:91], v[104:107], a[204:207], v[88:91]\n"
      "ds_read_b128 a[196:199], v3 offset:16960\n"
      "v_cvt_pk_bf16_f32 v48, v48, v49\n"
      "v_cvt_pk_bf16_f32 v12, v12, v16\n"
      "v_mfma_f32_16x16x32_bf16 v[92:95], a[164:167], a[204:207], v[92:95]\n"
      "ds_write_addtid_b32 v48 offset:17408\n"
      "ds_write_addtid_b32 v12 offset:43264\n"
      "v_mfma_f32_16x16x32_bf16 v[96:99], a[156:159], a[204:207], v[96:99]\n"
      "v_cvt_pk_bf16_f32 v13, v13, v17\n"
      "v_cvt_pk_bf16_f32 v14, v14, v18\n"
      "v_mfma_f32_16x16x32_bf16 v[88:91], a[148:151], a[208:211], v[88:91]\n"
      "ds_read_b128 a[200:203], v3 offset:17024\n"
      "ds_write_addtid_b32 v13 offset:47360\n"
      "v_mfma_f32_16x16x32_bf16 v[92:95], a[140:143], a[208:211], v[92:95]\n"
      "ds_read_b64_tr_b16 a[224:225], v7 offset:19456\n"
      "ds_write_addtid_b32 v14 offset:51456\n"
      "v_cvt_pk_bf16_f32 v50, v50, v51\n"
      "v_mfma_f32_16x16x32_bf16 v[96:99], a[132:135], a[208:211], v[96:99]\n"
      "ds_write_addtid_b32 v50 offset:17920\n"
      "ds_read_b64_tr_b16 a[226:227], v7 offset:19464\n"
      "v_mfma_f32_16x16x32_bf16 v[88:91], a[124:127], a[212:215], v[88:91]\n"
      "ds_read_b128 a[204:207], v4 offset:16896\n"
      "v_cvt_pk_bf16_f32 v36, v36, v37\n"
      "v_mfma_f32_16x16x32_bf16 v[92:95], a[116:119], a[212:215], v[92:95]\n"
      "ds_write_addtid_b32 v36 offset:19456\n"
      "v_cvt_pk_bf16_f32 v15, v15, v19\n"
      "v_mfma_f32_16x16x32_bf16 v[96:99], a[108:111], a[212:215], v[96:99]\n"
      "s_waitcnt lgkmcnt(6)\n"
      "ds_write_addtid_b32 v15 offset:55552\n"
      "v_sub_f32 v76, v88, v76\n"
      "v_sub_f32_e64 v77, v89, v77\n"
      "v_cvt_pk_bf16_f32 v38, v38, v39\n"
      "v_mfma_f32_16x16x32_bf16 v[52:55], a[0:3], a[192:195], 0\n"
      "s_nop 0\n"
      "ds_read_b128 a[208:211], v4 offset:16960\n"
      "ds_write_addtid_b32 v38 offset:19968\n"
      "v_mfma_f32_16x16x32_bf16 v[56:59], a[4:7], a[192:195], 0\n"
      "s_waitcnt lgkmcnt(4)\n"
      "ds_write_addtid_b32 v76 offset:0\n"
      "ds_write_addtid_b32 v77 offset:512\n"
      "v_mfma_f32_4x4x4_16b_bf16 v[20:23], a[216:217], a[224:225], 0\n"
      "v_sub_f32 v78, v90, v78\n"
      "v_mfma_f32_4x4x4_16b_bf16 v[24:27], a[216:217], a[226:227], 0\n"
      "ds_write_addtid_b32 v78 offset:1024\n"
      "v_sub_f32 v79, v91, v79\n"
      "v_mfma_f32_16x16x32_bf16 v[60:63], a[8:11], a[192:195], 0\n"
      "ds_write_addtid_b32 v79 offset:1536\n"
      "v_sub_f32 v80, v92, v80\n"
      "v_sub_f32 v81, v93, v81\n"
      "v_mfma_f32_16x16x32_bf16 v[52:55], a[12:15], a[196:199], v[52:55]\n"
      "ds_read_b128 a[212:215], v4 offset:17024\n"
      "ds_write_addtid_b32 v80 offset:2048\n"
      "v_mfma_f32_16x16x32_bf16 v[56:59], a[16:19], a[196:199], v[56:59]\n"
      "ds_write_addtid_b32 v81 offset:2560\n"
      "v_sub_f32 v82, v94, v82\n"
      "v_mfma_f32_16x16x32_bf16 v[60:63], a[20:23], a[196:199], v[60:63]\n"
      "s_nop 0\n"
      "ds_write_addtid_b32 v82 offset:3072\n"
      "v_cvt_pk_bf16_f32 v20, v20, v24\n"
      "v_mfma_f32_16x16x32_bf16 v[52:55], a[24:27], a[200:203], v[52:55]\n"
      "ds_read_b128 a[192:195], v5 offset:16896\n"
      "ds_write_addtid_b32 v20 offset:43776\n"
      "v_sub_f32 v83, v95, v83\n"
      "v_mfma_f32_16x16x32_bf16 v[56:59], a[28:31], a[200:203], v[56:59]\n"
      "ds_write_addtid_b32 v83 offset:3584\n"
      "v_sub_f32 v84, v96, v84\n"
      "v_mfma_f32_16x16x32_bf16 v[60:63], a[32:35], a[200:203], v[60:63]\n"
      "ds_write_addtid_b32 v84 offset:4096\n"
      "v_cvt_pk_bf16_f32 v21, v21, v25\n"
      "v_mfma_f32_16x16x32_bf16 v[52:55], a[36:39], a[204:207], v[52:55]\n"
      "ds_read_b128 a[196:199], v5 offset:16960\n"
      "ds_write_addtid_b32 v21 offset:47872\n"
      "v_mfma_f32_16x16x32_bf16 v[56:59], a[40:43], a[204:207], v[56:59]\n"
      "v_sub_f32 v85, v97, v85\n"
      "v_sub_f32 v86, v98, v86\n"
      "v_cvt_pk_bf16_f32 v22, v22, v26\n"
      "v_mfma_f32_16x16x32_bf16 v[60:63], a[44:47], a[204:207], v[60:63]\n"
      "s_waitcnt lgkmcnt(4)\n"
      "ds_write_addtid_b32 v22 offset:51968\n"
      "ds_write_addtid_b32 v85 offset:4608\n"
      "v_sub_f32 v87, v99, v87\n"
      "v_mfma_f32_16x16x32_bf16 v[52:55], a[48:51], a[208:211], v[52:55]\n"
      "ds_read_b128 a[200:203], v5 offset:17024\n"
      "ds_write_addtid_b32 v86 offset:5120\n"
      "v_mfma_f32_16x16x32_bf16 v[56:59], a[52:55], a[208:211], v[56:59]\n"
      "ds_write_addtid_b32 v87 offset:5632\n"
      "v_cvt_pk_bf16_f32 v23, v23, v27\n"
      "v_mfma_f32_16x16x32_bf16 v[60:63], a[56:59], a[208:211], v[60:63]\n"
      "ds_write_addtid_b32 v23 offset:56064\n"
      "v_fma_f32 v40, 0.5, v76, -v88\n"
      "v_mfma_f32_16x16x32_bf16 v[52:55], a[60:63], a[212:215], v[52:55]\n"
      "v_fma_f32 v41, 0.5, v77, -v89\n"
      "v_fma_f32 v42, 0.5, v78, -v90\n"
      "v_mfma_f32_16x16x32_bf16 v[56:59], a[64:67], a[212:215], v[56:59]\n"
      "s_nop 0\n"
      "v_fma_f32 v43, 0.5, v79, -v91\n"
      "v_fma_f32 v44, 0.5, v80, -v92\n"
      "v_mfma_f32_16x16x32_bf16 v[60:63], a[68:71], a[212:215], v[60:63]\n"
      "s_waitcnt lgkmcnt(0)\n"
      "v_fma_f32 v45, 0.5, v81, -v93\n"
      "s_barrier\n"
      "s_add_u32 s55, s55, 2\n"
      "s_cmp_lt_u32 s55, 6\n"
      "s_cbranch_scc1 LSloop_%=\n"
      "LSlast_%=:\n"
      "v_mfma_f32_16x16x32_bf16 v[52:55], a[72:75], a[192:195], v[52:55]\n"
      "s_add_u32 s59, s62, 8847360\n"
      "ds_read_b128 a[204:207], v0 offset:16384\n"
      "ds_read2st64_b32 v[28:29], v6 offset0:0 offset1:2\n"
      "v_fma_f32 v46, 0.5, v82, -v94\n"
      "v_mfma_f32_16x16x32_bf16 v[56:59], a[76:79], a[192:195], v[56:59]\n"
      "ds_read_b128 a[208:211], v0 offset:16448\n"
      "ds_read2st64_b32 v[30:31], v6 offset0:4 offset1:6\n"
      "v_mfma_f32_16x16x32_bf16 v[60:63], a[80:83], a[192:195], v[60:63]\n"
      "ds_read_b64_tr_b16 a[220:221], v7 offset:33792\n"
      "v_fma_f32 v47, 0.5, v83, -v95\n"
      "v_mfma_f32_16x16x32_bf16 v[52:55], a[84:87], a[196:199], v[52:55]\n"
      "ds_read_b128 a[212:215], v0 offset:16512\n"
      "v_fma_f32 v48, 0.5, v84, -v96\n"
      "v_mfma_f32_16x16x32_bf16 v[56:59], a[88:91], a[196:199], v[56:59]\n"
      "ds_read_b64_tr_b16 a[222:223], v7 offset:33800\n"
      "ds_read2st64_b32 v[32:33], v6 offset0:8 offset1:10\n"
      "v_mfma_f32_16x16x32_bf16 v[60:63], a[92:95], a[196:199], v[60:63]\n"
      "ds_read2st64_b32 v[34:35], v6 offset0:12 offset1:14\n"
      "ds_read2st64_b32 v[36:37], v6 offset0:16 offset1:18\n"
      "v_fma_f32 v49, 0.5, v85, -v97\n"
      "v_mfma_f32_16x16x32_bf16 v[52:55], a[96:99], a[200:203], v[52:55]\n"
      "ds_read_b128 a[192:195], v1 offset:16384\n"
      "v_fma_f32 v50, 0.5, v86, -v98\n"
      "v_mfma_f32_16x16x32_bf16 v[56:59], a[100:103], a[200:203], v[56:59]\n"
      "v_fma_f32 v51, 0.5, v87, -v99\n"
      "v_mfma_f32_16x16x32_bf16 v[60:63], a[104:107], a[200:203], v[60:63]\n"
      "s_waitcnt lgkmcnt(7)\n"
      "ds_read2st64_b32 v[38:39], v6 offset0:20 offset1:22\n"
      "v_fmac_f32 v28, s46, v64\n"
      "v_fmac_f32 v29, s46, v65\n"
      "v_mfma_f32_16x16x32_bf16 v[76:79], v[252:255], a[204:207], v[52:55]\n"
      "s_waitcnt lgkmcnt(5)\n"
      "ds_read_b128 a[196:199], v1 offset:16448\n"
      "v_fmac_f32_e64 v30, s46, v66\n"
      "v_mfma_f32_4x4x4_16b_bf16 v[12:15], a[216:217], a[220:221], 0\n"
      "v_fmac_f32 v31, s46, v67\n"
      "v_sub_f32 v28, v28, v40\n"
      "v_mfma_f32_4x4x4_16b_bf16 v[16:19], a[216:217], a[222:223], 0\n"
      "v_sub_f32 v29, v29, v41\n"
      "v_sub_f32 v30, v30, v42\n"
      "v_mfma_f32_16x16x32_bf16 v[80:83], v[244:247], a[204:207], v[56:59]\n"
      "v_sub_f32 v31, v31, v43\n"
      "v_fmac_f32 v40, s45, v28\n"
      "v_mfma_f32_16x16x32_bf16 v[84:87], v[236:239], a[204:207], v[60:63]\n"
      "v_cvt_pk_bf16_f32 v12, v12, v16\n"
      "v_fmac_f32 v41, s45, v29\n"
      "v_mfma_f32_16x16x32_bf16 v[76:79], v[228:231], a[208:211], v[76:79]\n"
      "ds_read_b128 a[200:203], v1 offset:16512\n"
      "ds_write_addtid_b32 v12 offset:24832\n"
      "v_mfma_f32_16x16x32_bf16 v[80:83], v[220:223], a[208:211], v[80:83]\n"
      "s_nop 0\n"
      "ds_read2st64_b32 a[240:241], v8 offset0:4 offset1:6\n"
      "ds_read2st64_b32 a[242:243], v9 offset0:4 offset1:6\n"
      "v_cvt_pk_bf16_f32 v13, v13, v17\n"
      "v_mfma_f32_16x16x32_bf16 v[84:87], v[212:215], a[208:211], v[84:87]\n"
      "ds_write_addtid_b32 v13 offset:28928\n"
      "v_fmac_f32 v42, s45, v30\n"
      "v_mfma_f32_16x16x32_bf16 v[76:79], v[204:207], a[212:215], v[76:79]\n"
      "ds_read_b128 a[204:207], v2 offset:16384\n"
      "ds_read_b64_tr_b16 a[224:225], v7 offset:34304\n"
      "v_mfma_f32_16x16x32_bf16 v[80:83], v[196:199], a[212:215], v[80:83]\n"
      "s_nop 0\n"
      "ds_read_b64_tr_b16 a[226:227], v7 offset:34312\n"
      "v_fmac_f32_e64 v43, s45, v31\n"
      "v_mfma_f32_16x16x32_bf16 v[84:87], v[188:191], a[212:215], v[84:87]\n"
      "s_waitcnt lgkmcnt(6)\n"
      "v_cvt_pk_bf16_f32 v14, v14, v18\n"
      "v_fma_f32 v28, s47, v28, v52\n"
      "v_mfma_f32_16x16x32_bf16 v[76:79], v[180:183], a[192:195], v[76:79]\n"
      "s_nop 0\n"
      "ds_read_b128 a[208:211], v2 offset:16448\n"
      "ds_write_addtid_b32 v14 offset:33024\n"
      "v_mfma_f32_16x16x32_bf16 v[80:83], v[172:175], a[192:195], v[80:83]\n"
      "s_nop 0\n"
      "v_fma_f32 v29, s47, v29, v53\n"
      "v_cvt_pk_bf16_f32 v15, v15, v19\n"
      "v_mfma_f32_16x16x32_bf16 v[84:87], v[164:167], a[192:195], v[84:87]\n"
      "s_nop 0\n"
      "ds_write_addtid_b32 v15 offset:37120\n"
      "v_fma_f32 v30, s47, v30, v54\n"
      "v_mfma_f32_16x16x32_bf16 v[76:79], v[156:159], a[196:199], v[76:79]\n"
      "s_nop 0\n"
      "ds_read_b128 a[212:215], v2 offset:16512\n"
      "v_fma_f32 v31, s47, v31, v55\n"
      "v_mfma_f32_16x16x32_bf16 v[80:83], v[148:151], a[196:199], v[80:83]\n"
      "s_waitcnt lgkmcnt(4)\n"
      "v_cvt_pk_bf16_f32 v40, v40, v41\n"
      "v_cvt_pk_bf16_f32 v42, v42, v43\n"
      "v_mfma_f32_4x4x4_16b_bf16 v[20:23], a[216:217], a[224:225], 0\n"
      "v_cvt_pk_bf16_f32 v28, v28, v29\n"
      "v_mfma_f32_4x4x4_16b_bf16 v[24:27], a[216:217], a[226:227], 0\n"
      "ds_write_addtid_b32 v40 offset:12288\n"
      "ds_write_addtid_b32 v42 offset:12800\n"
      "v_mfma_f32_16x16x32_bf16 v[84:87], v[140:143], a[196:199], v[84:87]\n"
      "buffer_store_dwordx4 a[240:243], v11, s[8:11], s59 offen offset:256 sc1\n"
      "ds_write_addtid_b32 v28 offset:14336\n"
      "v_mfma_f32_16x16x32_bf16 v[76:79], v[132:135], a[200:203], v[76:79]\n"
      "ds_read_b128 a[192:195], v0 offset:20480\n"
      "v_cvt_pk_bf16_f32 v30, v30, v31\n"
      "v_cvt_pk_bf16_f32 v20, v20, v24\n"
      "v_mfma_f32_16x16x32_bf16 v[80:83], v[124:127], a[200:203], v[80:83]\n"
      "ds_write_addtid_b32 v30 offset:14848\n"
      "ds_write_addtid_b32 v20 offset:25344\n"
      "v_fmac_f32 v32, s46, v68\n"
      "v_mfma_f32_16x16x32_bf16 v[84:87], v[116:119], a[200:203], v[84:87]\n"
      "v_fmac_f32 v33, s46, v69\n"
      "v_cvt_pk_bf16_f32 v21, v21, v25\n"
      "v_mfma_f32_16x16x32_bf16 v[76:79], v[108:111], a[204:207], v[76:79]\n"
      "ds_read_b128 a[196:199], v0 offset:20544\n"
      "ds_read_b64_tr_b16 a[228:229], v7 offset:34816\n"
      "v_mfma_f32_16x16x32_bf16 v[80:83], v[100:103], a[204:207], v[80:83]\n"
      "ds_write_addtid_b32 v21 offset:29440\n"
      "ds_read_b64_tr_b16 a[230:231], v7 offset:34824\n"
      "v_mfma_f32_16x16x32_bf16 v[84:87], a[160:163], a[204:207], v[84:87]\n"
      "s_waitcnt lgkmcnt(6)\n"
      "v_fmac_f32 v34, s46, v70\n"
      "v_fmac_f32 v35, s46, v71\n"
      "v_mfma_f32_16x16x32_bf16 v[76:79], a[152:155], a[208:211], v[76:79]\n"
      "ds_read_b128 a[200:203], v0 offset:20608\n"
      "v_sub_f32 v32, v32, v44\n"
      "v_mfma_f32_16x16x32_bf16 v[80:83], a[144:147], a[208:211], v[80:83]\n"
      "v_sub_f32 v33, v33, v45\n"
      "v_cvt_pk_bf16_f32 v22, v22, v26\n"
      "v_mfma_f32_16x16x32_bf16 v[84:87], a[136:139], a[208:211], v[84:87]\n"
      "ds_write_addtid_b32 v22 offset:33536\n"
      "v_sub_f32 v34, v34, v46\n"
      "v_sub_f32 v35, v35, v47\n"
      "v_mfma_f32_16x16x32_bf16 v[76:79], a[128:131], a[212:215], v[76:79]\n"
      "ds_read_b128 a[204:207], v1 offset:20480\n"
      "v_cvt_pk_bf16_f32 v23, v23, v27\n"
      "v_mfma_f32_16x16x32_bf16 v[80:83], a[120:123], a[212:215], v[80:83]\n"
      "s_waitcnt lgkmcnt(3)\n"
      "ds_write_addtid_b32 v23 offset:37632\n"
      "v_fmac_f32 v44, s45, v32\n"
      "v_mfma_f32_4x4x4_16b_bf16 v[12:15], a[216:217], a[228:229], 0\n"
      "v_fmac_f32 v45, s45, v33\n"
      "v_mfma_f32_4x4x4_16b_bf16 v[16:19], a[216:217], a[230:231], 0\n"
      "v_fmac_f32 v46, s45, v34\n"
      "v_fmac_f32 v47, s45, v35\n"
      "v_mfma_f32_16x16x32_bf16 v[84:87], a[112:115], a[212:215], v[84:87]\n"
      "v_fma_f32 v32, s47, v32, v56\n"
      "v_fma_f32 v33, s47, v33, v57\n"
      "v_cvt_pk_bf16_f32 v12, v12, v16\n"
      "v_mfma_f32_16x16x32_bf16 v[88:91], v[248:251], a[192:195], v[52:55]\n"
      "ds_read_b128 a[208:211], v1 offset:20544\n"
      "ds_write_addtid_b32 v12 offset:25856\n"
      "v_mfma_f32_16x16x32_bf16 v[92:95], v[240:243], a[192:195], v[56:59]\n"
      "v_cvt_pk_bf16_f32 v13, v13, v17\n"
      "v_fma_f32 v34, s47, v34, v58\n"
      "v_mfma_f32_16x16x32_bf16 v[96:99], v[232:235], a[192:195], v[60:63]\n"
      "ds_write_addtid_b32 v13 offset:29952\n"
      "v_fma_f32 v35, s47, v35, v59\n"
      "v_mfma_f32_16x16x32_bf16 v[88:91], v[224:227], a[196:199], v[88:91]\n"
      "ds_read_b128 a[212:215], v1 offset:20608\n"
      "v_cvt_pk_bf16_f32 v14, v14, v18\n"
      "v_mfma_f32_16x16x32_bf16 v[92:95], v[216:219], a[196:199], v[92:95]\n"
      "s_nop 0\n"
      "ds_write_addtid_b32 v14 offset:34048\n"
      "v_cvt_pk_bf16_f32 v44, v44, v45\n"
      "v_mfma_f32_16x16x32_bf16 v[96:99], v[208:211], a[196:199], v[96:99]\n"
      "s_waitcnt lgkmcnt(3)\n"
      "ds_write_addtid_b32 v44 offset:20484\n"
      "v_cvt_pk_bf16_f32 v46, v46, v47\n"
      "v_mfma_f32_16x16x32_bf16 v[88:91], v[200:203], a[200:203], v[88:91]\n"
      "ds_read_b128 a[192:195], v2 offset:20480\n"
      "ds_write_addtid_b32 v46 offset:20996\n"
      "v_cvt_pk_bf16_f32 v32, v32, v33\n"
      "v_mfma_f32_16x16x32_bf16 v[92:95], v[192:195], a[200:203], v[92:95]\n"
      "ds_write_addtid_b32 v32 offset:22532\n"
      "ds_read_b64_tr_b16 a[232:233], v7 offset:35328\n"
      "v_mfma_f32_16x16x32_bf16 v[96:99], v[184:187], a[200:203], v[96:99]\n"
      "ds_read_b64_tr_b16 a[234:235], v7 offset:35336\n"
      "v_cvt_pk_bf16_f32 v34, v34, v35\n"
      "v_cvt_pk_bf16_f32 v15, v15, v19\n"
      "v_mfma_f32_16x16x32_bf16 v[88:91], v[176:179], a[204:207], v[88:91]\n"
      "ds_read_b128 a[196:199], v2 offset:20544\n"
      "ds_write_addtid_b32 v34 offset:23044\n"
      "v_mfma_f32_16x16x32_bf16 v[92:95], v[168:171], a[204:207], v[92:95]\n"
      "ds_write_addtid_b32 v15 offset:38144\n"
      "v_fmac_f32 v36, s46, v72\n"
      "v_mfma_f32_16x16x32_bf16 v[96:99], v[160:163], a[204:207], v[96:99]\n"
      "v_fmac_f32 v37, s46, v73\n"
      "v_fmac_f32 v38, s46, v74\n"
      "v_mfma_f32_16x16x32_bf16 v[88:91], v[152:155], a[208:211], v[88:91]\n"
      "ds_read_b128 a[200:203], v2 offset:20608\n"
      "v_fmac_f32 v39, s46, v75\n"
      "v_mfma_f32_16x16x32_bf16 v[92:95], v[144:147], a[208:211], v[92:95]\n"
      "s_waitcnt lgkmcnt(4)\n"
      "v_sub_f32 v36, v36, v48\n"
      "v_sub_f32 v37, v37, v49\n"
      "v_mfma_f32_4x4x4_16b_bf16 v[20:23], a[216:217], a[232:233], 0\n"
      "v_sub_f32 v38, v38, v50\n"
      "v_mfma_f32_4x4x4_16b_bf16 v[24:27], a[216:217], a[234:235], 0\n"
      "v_sub_f32 v39, v39, v51\n"
      "v_fmac_f32 v48, s45, v36\n"
      "v_mfma_f32_16x16x32_bf16 v[96:99], v[136:139], a[208:211], v[96:99]\n"
      "v_fmac_f32 v49, s45, v37\n"
      "v_fmac_f32 v50, s45, v38\n"
      "v_cvt_pk_bf16_f32 v20, v20, v24\n"
      "v_mfma_f32_16x16x32_bf16 v[88:91], v[128:131], a[212:215], v[88:91]\n"
      "ds_read_b128 a[204:207], v3 offset:33792\n"
      "ds_write_addtid_b32 v20 offset:26368\n"
      "v_mfma_f32_16x16x32_bf16 v[92:95], v[120:123], a[212:215], v[92:95]\n"
      "v_cvt_pk_bf16_f32 v21, v21, v25\n"
      "v_fmac_f32 v51, s45, v39\n"
      "v_mfma_f32_16x16x32_bf16 v[96:99], v[112:115], a[212:215], v[96:99]\n"
      "s_nop 0\n"
      "ds_write_addtid_b32 v21 offset:30464\n"
      "v_fma_f32 v36, s47, v36, v60\n"
      "v_mfma_f32_16x16x32_bf16 v[88:91], v[104:107], a[192:195], v[88:91]\n"
      "ds_read_b128 a[208:211], v3 offset:33856\n"
      "v_fma_f32 v37, s47, v37, v61\n"
      "v_mfma_f32_16x16x32_bf16 v[92:95], a[164:167], a[192:195], v[92:95]\n"
      "ds_read_b64_tr_b16 a[220:221], v7 offset:35840\n"
      "ds_read_b64_tr_b16 a[222:223], v7 offset:35848\n"
      "v_cvt_pk_bf16_f32 v22, v22, v26\n"
      "v_mfma_f32_16x16x32_bf16 v[96:99], a[156:159], a[192:195], v[96:99]\n"
      "s_waitcnt lgkmcnt(4)\n"
      "ds_write_addtid_b32 v22 offset:34560\n"
      "v_fma_f32 v38, s47, v38, v62\n"
      "v_mfma_f32_16x16x32_bf16 v[88:91], a[148:151], a[196:199], v[88:91]\n"
      "ds_read_b128 a[212:215], v3 offset:33920\n"
      "v_fma_f32 v39, s47, v39, v63\n"
      "v_mfma_f32_16x16x32_bf16 v[92:95], a[140:143], a[196:199], v[92:95]\n"
      "s_nop 0\n"
      "v_cvt_pk_bf16_f32 v23, v23, v27\n"
      "v_cvt_pk_bf16_f32 v48, v48, v49\n"
      "v_mfma_f32_16x16x32_bf16 v[96:99], a[132:135], a[196:199], v[96:99]\n"
      "ds_write_addtid_b32 v48 offset:16384\n"
      "ds_write_addtid_b32 v23 offset:38656\n"
      "v_mfma_f32_16x16x32_bf16 v[88:91], a[124:127], a[200:203], v[88:91]\n"
      "ds_read_b128 a[192:195], v4 offset:33792\n"
      "v_cvt_pk_bf16_f32 v50, v50, v51\n"
      "v_mfma_f32_16x16x32_bf16 v[92:95], a[116:119], a[200:203], v[92:95]\n"
      "ds_write_addtid_b32 v50 offset:16896\n"
      "v_cvt_pk_bf16_f32 v36, v36, v37\n"
      "v_mfma_f32_16x16x32_bf16 v[96:99], a[108:111], a[200:203], v[96:99]\n"
      "s_waitcnt lgkmcnt(4)\n"
      "ds_write_addtid_b32 v36 offset:18432\n"
      "v_sub_f32 v76, v88, v76\n"
      "v_sub_f32 v77, v89, v77\n"
      "v_mfma_f32_4x4x4_16b_bf16 v[12:15], a[216:217], a[220:221], 0\n"
      "v_sub_f32 v78, v90, v78\n"
      "v_cvt_pk_bf16_f32 v38, v38, v39\n"
      "v_mfma_f32_4x4x4_16b_bf16 v[16:19], a[216:217], a[222:223], 0\n"
      "ds_write_addtid_b32 v38 offset:18944\n"
      "v_sub_f32 v79, v91, v79\n"
      "v_sub_f32 v80, v92, v80\n"
      "v_mfma_f32_16x16x32_bf16 v[64:67], a[0:3], a[204:207], 0\n"
      "ds_read_b128 a[196:199], v4 offset:33856\n"
      "ds_write_addtid_b32 v76 offset:6144\n"
      "v_mfma_f32_16x16x32_bf16 v[68:71], a[4:7], a[204:207], 0\n"
      "ds_write_addtid_b32 v77 offset:6656\n"
      "ds_write_addtid_b32 v78 offset:7168\n"
      "v_mfma_f32_16x16x32_bf16 v[72:75], a[8:11], a[204:207], 0\n"
      "ds_write_addtid_b32 v79 offset:7680\n"
      "ds_write_addtid_b32 v80 offset:8192\n"
      "v_mfma_f32_16x16x32_bf16 v[64:67], a[12:15], a[208:211], v[64:67]\n"
      "ds_read_b128 a[200:203], v4 offset:33920\n"
      "v_sub_f32 v81, v93, v81\n"
      "v_mfma_f32_16x16x32_bf16 v[68:71], a[16:19], a[208:211], v[68:71]\n"
      "s_nop 0\n"
      "ds_write_addtid_b32 v81 offset:8704\n"
      "v_sub_f32 v82, v94, v82\n"
      "v_cvt_pk_bf16_f32 v12, v12, v16\n"
      "v_mfma_f32_16x16x32_bf16 v[72:75], a[20:23], a[208:211], v[72:75]\n"
      "ds_write_addtid_b32 v12 offset:26880\n"
      "ds_write_addtid_b32 v82 offset:9216\n"
      "v_mfma_f32_16x16x32_bf16 v[64:67], a[24:27], a[212:215], v[64:67]\n"
      "ds_read_b128 a[204:207], v5 offset:33792\n"
      "v_sub_f32 v83, v95, v83\n"
      "v_mfma_f32_16x16x32_bf16 v[68:71], a[28:31], a[212:215], v[68:71]\n"
      "ds_write_addtid_b32 v83 offset:9728\n"
      "v_sub_f32 v84, v96, v84\n"
      "v_mfma_f32_16x16x32_bf16 v[72:75], a[32:35], a[212:215], v[72:75]\n"
      "s_waitcnt lgkmcnt(4)\n"
      "ds_write_addtid_b32 v84 offset:10240\n"
      "v_cvt_pk_bf16_f32 v13, v13, v17\n"
      "v_sub_f32 v85, v97, v85\n"
      "v_mfma_f32_16x16x32_bf16 v[64:67], a[36:39], a[192:195], v[64:67]\n"
      "s_nop 0\n"
      "ds_read_b128 a[208:211], v5 offset:33856\n"
      "ds_write_addtid_b32 v13 offset:30976\n"
      "v_mfma_f32_16x16x32_bf16 v[68:71], a[40:43], a[192:195], v[68:71]\n"
      "ds_write_addtid_b32 v85 offset:10752\n"
      "v_cvt_pk_bf16_f32 v14, v14, v18\n"
      "v_mfma_f32_16x16x32_bf16 v[72:75], a[44:47], a[192:195], v[72:75]\n"
      "ds_write_addtid_b32 v14 offset:35072\n"
      "v_sub_f32 v86, v98, v86\n"
      "v_sub_f32 v87, v99, v87\n"
      "v_mfma_f32_16x16x32_bf16 v[64:67], a[48:51], a[196:199], v[64:67]\n"
      "ds_read_b128 a[212:215], v5 offset:33920\n"
      "ds_write_addtid_b32 v86 offset:11264\n"
      "v_cvt_pk_bf16_f32 v15, v15, v19\n"
      "ds_write_addtid_b32 v15 offset:39168\n"
      "v_mfma_f32_16x16x32_bf16 v[68:71], a[52:55], a[196:199], v[68:71]\n"
      "ds_write_addtid_b32 v87 offset:11776\n"
      "v_fma_f32 v40, 0.5, v76, -v88\n"
      "v_mfma_f32_16x16x32_bf16 v[72:75], a[56:59], a[196:199], v[72:75]\n"
      "v_fma_f32 v41, 0.5, v77, -v89\n"
      "v_fma_f32 v42, 0.5, v78, -v90\n"
      "v_mfma_f32_16x16x32_bf16 v[64:67], a[60:63], a[200:203], v[64:67]\n"
      "v_fma_f32 v43, 0.5, v79, -v91\n"
      "v_fma_f32 v44, 0.5, v80, -v92\n"
      "v_mfma_f32_16x16x32_bf16 v[68:71], a[64:67], a[200:203], v[68:71]\n"
      "v_fma_f32 v45, 0.5, v81, -v93\n"
      "v_mfma_f32_16x16x32_bf16 v[72:75], a[68:71], a[200:203], v[72:75]\n"
      "s_waitcnt lgkmcnt(0)\n"
      "s_barrier\n"
      "v_mfma_f32_16x16x32_bf16 v[64:67], a[72:75], a[204:207], v[64:67]\n"
      "s_add_u32 s59, s62, 9633792\n"
      "ds_read_b128 a[192:195], v0 offset:0\n"
      "ds_read2st64_b32 v[28:29], v6 offset0:24 offset1:26\n"
      "v_fma_f32 v46, 0.5, v82, -v94\n"
      "v_mfma_f32_16x16x32_bf16 v[68:71], a[76:79], a[204:207], v[68:71]\n"
      "ds_read_b128 a[196:199], v0 offset:64\n"
      "ds_read2st64_b32 v[30:31], v6 offset0:28 offset1:30\n"
      "v_mfma_f32_16x16x32_bf16 v[72:75], a[80:83], a[204:207], v[72:75]\n"
      "ds_read_b64_tr_b16 a[220:221], v7 offset:50688\n"
      "v_fma_f32 v47, 0.5, v83, -v95\n"
      "v_mfma_f32_16x16x32_bf16 v[64:67], a[84:87], a[208:211], v[64:67]\n"
      "ds_read_b128 a[200:203], v0 offset:128\n"
      "v_fma_f32 v48, 0.5, v84, -v96\n"
      "v_mfma_f32_16x16x32_bf16 v[68:71], a[88:91], a[208:211], v[68:71]\n"
      "ds_read_b64_tr_b16 a[222:223], v7 offset:50696\n"
      "ds_read2st64_b32 v[32:33], v6 offset0:32 offset1:34\n"
      "v_mfma_f32_16x16x32_bf16 v[72:75], a[92:95], a[208:211], v[72:75]\n"
      "ds_read2st64_b32 v[34:35], v6 offset0:36 offset1:38\n"
      "ds_read2st64_b32 v[36:37], v6 offset0:40 offset1:42\n"
      "v_fma_f32 v49, 0.5, v85, -v97\n"
      "v_mfma_f32_16x16x32_bf16 v[64:67], a[96:99], a[212:215], v[64:67]\n"
      "ds_read_b128 a[204:207], v1 offset:0\n"
      "v_fma_f32 v50, 0.5, v86, -v98\n"
      "v_mfma_f32_16x16x32_bf16 v[68:71], a[100:103], a[212:215], v[68:71]\n"
      "v_fma_f32 v51, 0.5, v87, -v99\n"
      "v_mfma_f32_16x16x32_bf16 v[72:75], a[104:107], a[212:215], v[72:75]\n"
      "s_waitcnt lgkmcnt(7)\n"
      "ds_read2st64_b32 v[38:39], v6 offset0:44 offset1:46\n"
      "v_fmac_f32 v28, s46, v52\n"
      "v_fmac_f32_e64 v29, s46, v53\n"
      "v_mfma_f32_16x16x32_bf16 v[76:79], v[252:255], a[192:195], v[64:67]\n"
      "s_waitcnt lgkmcnt(5)\n"
      "ds_read_b128 a[208:211], v1 offset:64\n"
      "v_fmac_f32 v30, s46, v54\n"
      "v_mfma_f32_4x4x4_16b_bf16 v[12:15], a[216:217], a[220:221], 0\n"
      "v_fmac_f32 v31, s46, v55\n"
      "v_sub_f32 v28, v28, v40\n"
      "v_mfma_f32_4x4x4_16b_bf16 v[16:19], a[216:217], a[222:223], 0\n"
      "v_sub_f32 v29, v29, v41\n"
      "v_sub_f32 v30, v30, v42\n"
      "v_mfma_f32_16x16x32_bf16 v[80:83], v[244:247], a[192:195], v[68:71]\n"
      "v_sub_f32 v31, v31, v43\n"
      "v_fmac_f32 v40, s45, v28\n"
      "v_mfma_f32_16x16x32_bf16 v[84:87], v[236:239], a[192:195], v[72:75]\n"
      "v_cvt_pk_bf16_f32 v12, v12, v16\n"
      "v_fmac_f32 v41, s45, v29\n"
      "v_mfma_f32_16x16x32_bf16 v[76:79], v[228:231], a[196:199], v[76:79]\n"
      "ds_read_b128 a[212:215], v1 offset:128\n"
      "ds_write_addtid_b32 v12 offset:41216\n"
      "v_mfma_f32_16x16x32_bf16 v[80:83], v[220:223], a[196:199], v[80:83]\n"
      "s_nop 0\n"
      "ds_read2st64_b32 a[240:241], v8 offset0:0 offset1:2\n"
      "ds_read2st64_b32 a[242:243], v9 offset0:0 offset1:2\n"
      "v_cvt_pk_bf16_f32 v13, v13, v17\n"
      "v_mfma_f32_16x16x32_bf16 v[84:87], v[212:215], a[196:199], v[84:87]\n"
      "ds_write_addtid_b32 v13 offset:45312\n"
      "v_fmac_f32 v42, s45, v30\n"
      "v_mfma_f32_16x16x32_bf16 v[76:79], v[204:207], a[200:203], v[76:79]\n"
      "s_nop 0\n"
      "ds_read_b128 a[192:195], v2 offset:0\n"
      "ds_read_b64_tr_b16 a[224:225], v7 offset:51200\n"
      "v_mfma_f32_16x16x32_bf16 v[80:83], v[196:199], a[200:203], v[80:83]\n"
      "ds_read_b64_tr_b16 a[226:227], v7 offset:51208\n"
      "v_fmac_f32 v43, s45, v31\n"
      "v_mfma_f32_16x16x32_bf16 v[84:87], v[188:191], a[200:203], v[84:87]\n"
      "s_waitcnt lgkmcnt(6)\n"
      "v_cvt_pk_bf16_f32 v14, v14, v18\n"
      "v_fma_f32 v28, s47, v28, v64\n"
      "v_mfma_f32_16x16x32_bf16 v[76:79], v[180:183], a[204:207], v[76:79]\n"
      "ds_read_b128 a[196:199], v2 offset:64\n"
      "ds_write_addtid_b32 v14 offset:49408\n"
      "v_mfma_f32_16x16x32_bf16 v[80:83], v[172:175], a[204:207], v[80:83]\n"
      "v_fma_f32 v29, s47, v29, v65\n"
      "v_cvt_pk_bf16_f32 v15, v15, v19\n"
      "v_mfma_f32_16x16x32_bf16 v[84:87], v[164:167], a[204:207], v[84:87]\n"
      "ds_write_addtid_b32 v15 offset:53504\n"
      "v_fma_f32 v30, s47, v30, v66\n"
      "v_mfma_f32_16x16x32_bf16 v[76:79], v[156:159], a[208:211], v[76:79]\n"
      "s_nop 0\n"
      "ds_read_b128 a[200:203], v2 offset:128\n"
      "v_fma_f32 v31, s47, v31, v67\n"
      "v_mfma_f32_16x16x32_bf16 v[80:83], v[148:151], a[208:211], v[80:83]\n"
      "s_waitcnt lgkmcnt(4)\n"
      "v_cvt_pk_bf16_f32 v40, v40, v41\n"
      "v_cvt_pk_bf16_f32 v42, v42, v43\n"
      "v_mfma_f32_4x4x4_16b_bf16 v[20:23], a[216:217], a[224:225], 0\n"
      "v_cvt_pk_bf16_f32 v28, v28, v29\n"
      "v_mfma_f32_4x4x4_16b_bf16 v[24:27], a[216:217], a[226:227], 0\n"
      "ds_write_addtid_b32 v40 offset:13312\n"
      "ds_write_addtid_b32 v42 offset:13824\n"
      "v_mfma_f32_16x16x32_bf16 v[84:87], v[140:143], a[208:211], v[84:87]\n"
      "buffer_store_dwordx4 a[240:243], v11, s[8:11], s59 offen offset:256 sc1\n"
      "ds_write_addtid_b32 v28 offset:15360\n"
      "v_mfma_f32_16x16x32_bf16 v[76:79], v[132:135], a[212:215], v[76:79]\n"
      "ds_read_b128 a[204:207], v0 offset:4096\n"
      "v_cvt_pk_bf16_f32 v30, v30, v31\n"
      "v_cvt_pk_bf16_f32 v20, v20, v24\n"
      "v_mfma_f32_16x16x32_bf16 v[80:83], v[124:127], a[212:215], v[80:83]\n"
      "ds_write_addtid_b32 v30 offset:15872\n"
      "ds_write_addtid_b32 v20 offset:41728\n"
      "v_fmac_f32 v32, s46, v56\n"
      "v_mfma_f32_16x16x32_bf16 v[84:87], v[116:119], a[212:215], v[84:87]\n"
      "v_fmac_f32 v33, s46, v57\n"
      "v_cvt_pk_bf16_f32 v21, v21, v25\n"
      "v_mfma_f32_16x16x32_bf16 v[76:79], v[108:111], a[192:195], v[76:79]\n"
      "ds_read_b128 a[208:211], v0 offset:4160\n"
      "ds_read_b64_tr_b16 a[228:229], v7 offset:51712\n"
      "v_mfma_f32_16x16x32_bf16 v[80:83], v[100:103], a[192:195], v[80:83]\n"
      "ds_write_addtid_b32 v21 offset:45824\n"
      "ds_read_b64_tr_b16 a[230:231], v7 offset:51720\n"
      "v_mfma_f32_16x16x32_bf16 v[84:87], a[160:163], a[192:195], v[84:87]\n"
      "s_waitcnt lgkmcnt(6)\n"
      "v_fmac_f32 v34, s46, v58\n"
      "v_fmac_f32 v35, s46, v59\n"
      "v_mfma_f32_16x16x32_bf16 v[76:79], a[152:155], a[196:199], v[76:79]\n"
      "ds_read_b128 a[212:215], v0 offset:4224\n"
      "v_sub_f32 v32, v32, v44\n"
      "v_mfma_f32_16x16x32_bf16 v[80:83], a[144:147], a[196:199], v[80:83]\n"
      "v_sub_f32 v33, v33, v45\n"
      "v_cvt_pk_bf16_f32 v22, v22, v26\n"
      "v_mfma_f32_16x16x32_bf16 v[84:87], a[136:139], a[196:199], v[84:87]\n"
      "s_nop 0\n"
      "ds_write_addtid_b32 v22 offset:49920\n"
      "v_sub_f32 v34, v34, v46\n"
      "v_sub_f32 v35, v35, v47\n"
      "v_mfma_f32_16x16x32_bf16 v[76:79], a[128:131], a[200:203], v[76:79]\n"
      "ds_read_b128 a[192:195], v1 offset:4096\n"
      "v_cvt_pk_bf16_f32 v23, v23, v27\n"
      "v_mfma_f32_16x16x32_bf16 v[80:83], a[120:123], a[200:203], v[80:83]\n"
      "s_waitcnt lgkmcnt(3)\n"
      "ds_write_addtid_b32 v23 offset:54016\n"
      "v_fmac_f32 v44, s45, v32\n"
      "v_mfma_f32_4x4x4_16b_bf16 v[12:15], a[216:217], a[228:229], 0\n"
      "v_fmac_f32 v45, s45, v33\n"
      "v_mfma_f32_4x4x4_16b_bf16 v[16:19], a[216:217], a[230:231], 0\n"
      "v_fmac_f32 v46, s45, v34\n"
      "v_fmac_f32 v47, s45, v35\n"
      "v_mfma_f32_16x16x32_bf16 v[84:87], a[112:115], a[200:203], v[84:87]\n"
      "s_nop 0\n"
      "v_fma_f32 v32, s47, v32, v68\n"
      "v_fma_f32 v33, s47, v33, v69\n"
      "v_cvt_pk_bf16_f32 v12, v12, v16\n"
      "v_mfma_f32_16x16x32_bf16 v[88:91], v[248:251], a[204:207], v[64:67]\n"
      "ds_read_b128 a[196:199], v1 offset:4160\n"
      "ds_write_addtid_b32 v12 offset:42240\n"
      "v_mfma_f32_16x16x32_bf16 v[92:95], v[240:243], a[204:207], v[68:71]\n"
      "v_cvt_pk_bf16_f32 v13, v13, v17\n"
      "v_fma_f32 v34, s47, v34, v70\n"
      "v_mfma_f32_16x16x32_bf16 v[96:99], v[232:235], a[204:207], v[72:75]\n"
      "ds_write_addtid_b32 v13 offset:46336\n"
      "v_fma_f32 v35, s47, v35, v71\n"
      "v_mfma_f32_16x16x32_bf16 v[88:91], v[224:227], a[208:211], v[88:91]\n"
      "ds_read_b128 a[200:203], v1 offset:4224\n"
      "v_cvt_pk_bf16_f32 v14, v14, v18\n"
      "v_mfma_f32_16x16x32_bf16 v[92:95], v[216:219], a[208:211], v[92:95]\n"
      "ds_write_addtid_b32 v14 offset:50432\n"
      "v_cvt_pk_bf16_f32 v44, v44, v45\n"
      "v_mfma_f32_16x16x32_bf16 v[96:99], v[208:211], a[208:211], v[96:99]\n"
      "s_waitcnt lgkmcnt(3)\n"
      "ds_write_addtid_b32 v44 offset:21508\n"
      "v_cvt_pk_bf16_f32 v46, v46, v47\n"
      "v_mfma_f32_16x16x32_bf16 v[88:91], v[200:203], a[212:215], v[88:91]\n"
      "ds_read_b128 a[204:207], v2 offset:4096\n"
      "ds_write_addtid_b32 v46 offset:22020\n"
      "v_cvt_pk_bf16_f32 v32, v32, v33\n"
      "v_mfma_f32_16x16x32_bf16 v[92:95], v[192:195], a[212:215], v[92:95]\n"
      "s_nop 0\n"
      "ds_write_addtid_b32 v32 offset:23556\n"
      "ds_read_b64_tr_b16 a[232:233], v7 offset:52224\n"
      "v_mfma_f32_16x16x32_bf16 v[96:99], v[184:187], a[212:215], v[96:99]\n"
      "ds_read_b64_tr_b16 a[234:235], v7 offset:52232\n"
      "v_cvt_pk_bf16_f32 v34, v34, v35\n"
      "v_cvt_pk_bf16_f32 v15, v15, v19\n"
      "v_mfma_f32_16x16x32_bf16 v[88:91], v[176:179], a[192:195], v[88:91]\n"
      "ds_read_b128 a[208:211], v2 offset:4160\n"
      "ds_write_addtid_b32 v34 offset:24068\n"
      "v_mfma_f32_16x16x32_bf16 v[92:95], v[168:171], a[192:195], v[92:95]\n"
      "ds_write_addtid_b32 v15 offset:54528\n"
      "v_fmac_f32 v36, s46, v60\n"
      "v_mfma_f32_16x16x32_bf16 v[96:99], v[160:163], a[192:195], v[96:99]\n"
      "v_fmac_f32 v37, s46, v61\n"
      "v_fmac_f32 v38, s46, v62\n"
      "v_mfma_f32_16x16x32_bf16 v[88:91], v[152:155], a[196:199], v[88:91]\n"
      "ds_read_b128 a[212:215], v2 offset:4224\n"
      "v_fmac_f32 v39, s46, v63\n"
      "v_mfma_f32_16x16x32_bf16 v[92:95], v[144:147], a[196:199], v[92:95]\n"
      "s_waitcnt lgkmcnt(4)\n"
      "v_sub_f32 v36, v36, v48\n"
      "v_sub_f32 v37, v37, v49\n"
      "v_mfma_f32_4x4x4_16b_bf16 v[20:23], a[216:217], a[232:233], 0\n"
      "v_sub_f32 v38, v38, v50\n"
      "v_mfma_f32_4x4x4_16b_bf16 v[24:27], a[216:217], a[234:235], 0\n"
      "v_sub_f32 v39, v39, v51\n"
      "v_fmac_f32 v48, s45, v36\n"
      "v_mfma_f32_16x16x32_bf16 v[96:99], v[136:139], a[196:199], v[96:99]\n"
      "v_fmac_f32 v49, s45, v37\n"
      "v_fmac_f32 v50, s45, v38\n"
      "v_cvt_pk_bf16_f32 v20, v20, v24\n"
      "v_mfma_f32_16x16x32_bf16 v[88:91], v[128:131], a[200:203], v[88:91]\n"
      "ds_read_b128 a[192:195], v3 offset:50688\n"
      "ds_write_addtid_b32 v20 offset:42752\n"
      "v_mfma_f32_16x16x32_bf16 v[92:95], v[120:123], a[200:203], v[92:95]\n"
      "v_cvt_pk_bf16_f32 v21, v21, v25\n"
      "v_fmac_f32 v51, s45, v39\n"
      "v_mfma_f32_16x16x32_bf16 v[96:99], v[112:115], a[200:203], v[96:99]\n"
      "s_nop 0\n"
      "ds_write_addtid_b32 v21 offset:46848\n"
      "v_fma_f32 v36, s47, v36, v72\n"
      "v_mfma_f32_16x16x32_bf16 v[88:91], v[104:107], a[204:207], v[88:91]\n"
      "ds_read_b128 a[196:199], v3 offset:50752\n"
      "v_fma_f32 v37, s47, v37, v73\n"
      "v_mfma_f32_16x16x32_bf16 v[92:95], a[164:167], a[204:207], v[92:95]\n"
      "ds_read_b64_tr_b16 a[220:221], v7 offset:52736\n"
      "ds_read_b64_tr_b16 a[222:223], v7 offset:52744\n"
      "v_cvt_pk_bf16_f32 v22, v22, v26\n"
      "v_mfma_f32_16x16x32_bf16 v[96:99], a[156:159], a[204:207], v[96:99]\n"
      "s_waitcnt lgkmcnt(4)\n"
      "ds_write_addtid_b32 v22 offset:50944\n"
      "v_fma_f32 v38, s47, v38, v74\n"
      "v_mfma_f32_16x16x32_bf16 v[88:91], a[148:151], a[208:211], v[88:91]\n"
      "s_nop 0\n"
      "ds_read_b128 a[200:203], v3 offset:50816\n"
      "v_fma_f32 v39, s47, v39, v75\n"
      "v_mfma_f32_16x16x32_bf16 v[92:95], a[140:143], a[208:211], v[92:95]\n"
      "v_cvt_pk_bf16_f32 v23, v23, v27\n"
      "v_cvt_pk_bf16_f32 v48, v48, v49\n"
      "v_mfma_f32_16x16x32_bf16 v[96:99], a[132:135], a[208:211], v[96:99]\n"
      "ds_write_addtid_b32 v48 offset:17408\n"
      "ds_write_addtid_b32 v23 offset:55040\n"
      "v_mfma_f32_16x16x32_bf16 v[88:91], a[124:127], a[212:215], v[88:91]\n"
      "ds_read_b128 a[204:207], v4 offset:50688\n"
      "v_cvt_pk_bf16_f32 v50, v50, v51\n"
      "v_mfma_f32_16x16x32_bf16 v[92:95], a[116:119], a[212:215], v[92:95]\n"
      "ds_write_addtid_b32 v50 offset:17920\n"
      "v_cvt_pk_bf16_f32 v36, v36, v37\n"
      "v_mfma_f32_16x16x32_bf16 v[96:99], a[108:111], a[212:215], v[96:99]\n"
      "s_waitcnt lgkmcnt(4)\n"
      "ds_write_addtid_b32 v36 offset:19456\n"
      "v_sub_f32 v76, v88, v76\n"
      "v_sub_f32_e64 v77, v89, v77\n"
      "v_mfma_f32_4x4x4_16b_bf16 v[12:15], a[216:217], a[220:221], 0\n"
      "v_sub_f32 v78, v90, v78\n"
      "v_cvt_pk_bf16_f32 v38, v38, v39\n"
      "v_mfma_f32_4x4x4_16b_bf16 v[16:19], a[216:217], a[222:223], 0\n"
      "ds_write_addtid_b32 v38 offset:19968\n"
      "v_sub_f32 v79, v91, v79\n"
      "v_sub_f32 v80, v92, v80\n"
      "v_mfma_f32_16x16x32_bf16 v[52:55], a[0:3], a[192:195], 0\n"
      "s_nop 0\n"
      "ds_read_b128 a[208:211], v4 offset:50752\n"
      "ds_write_addtid_b32 v76 offset:0\n"
      "v_mfma_f32_16x16x32_bf16 v[56:59], a[4:7], a[192:195], 0\n"
      "ds_write_addtid_b32 v77 offset:512\n"
      "ds_write_addtid_b32 v78 offset:1024\n"
      "v_mfma_f32_16x16x32_bf16 v[60:63], a[8:11], a[192:195], 0\n"
      "ds_write_addtid_b32 v79 offset:1536\n"
      "ds_write_addtid_b32 v80 offset:2048\n"
      "v_mfma_f32_16x16x32_bf16 v[52:55], a[12:15], a[196:199], v[52:55]\n"
      "ds_read_b128 a[212:215], v4 offset:50816\n"
      "v_sub_f32 v81, v93, v81\n"
      "v_mfma_f32_16x16x32_bf16 v[56:59], a[16:19], a[196:199], v[56:59]\n"
      "ds_write_addtid_b32 v81 offset:2560\n"
      "v_sub_f32 v82, v94, v82\n"
      "v_cvt_pk_bf16_f32 v12, v12, v16\n"
      "v_mfma_f32_16x16x32_bf16 v[60:63], a[20:23], a[196:199], v[60:63]\n"
      "ds_write_addtid_b32 v12 offset:43264\n"
      "ds_write_addtid_b32 v82 offset:3072\n"
      "v_mfma_f32_16x16x32_bf16 v[52:55], a[24:27], a[200:203], v[52:55]\n"
      "ds_read_b128 a[192:195], v5 offset:50688\n"
      "v_sub_f32 v83, v95, v83\n"
      "v_mfma_f32_16x16x32_bf16 v[56:59], a[28:31], a[200:203], v[56:59]\n"
      "ds_write_addtid_b32 v83 offset:3584\n"
      "v_sub_f32_e64 v84, v96, v84\n"
      "v_mfma_f32_16x16x32_bf16 v[60:63], a[32:35], a[200:203], v[60:63]\n"
      "s_waitcnt lgkmcnt(4)\n"
      "ds_write_addtid_b32 v84 offset:4096\n"
      "v_cvt_pk_bf16_f32 v13, v13, v17\n"
      "v_sub_f32 v85, v97, v85\n"
      "v_mfma_f32_16x16x32_bf16 v[52:55], a[36:39], a[204:207], v[52:55]\n"
      "ds_read_b128 a[196:199], v5 offset:50752\n"
      "ds_write_addtid_b32 v13 offset:47360\n"
      "v_mfma_f32_16x16x32_bf16 v[56:59], a[40:43], a[204:207], v[56:59]\n"
      "s_nop 0\n"
      "ds_write_addtid_b32 v85 offset:4608\n"
      "v_cvt_pk_bf16_f32 v14, v14, v18\n"
      "v_mfma_f32_16x16x32_bf16 v[60:63], a[44:47], a[204:207], v[60:63]\n"
      "ds_write_addtid_b32 v14 offset:51456\n"
      "v_sub_f32 v86, v98, v86\n"
      "v_sub_f32 v87, v99, v87\n"
      "v_mfma_f32_16x16x32_bf16 v[52:55], a[48:51], a[208:211], v[52:55]\n"
      "ds_read_b128 a[200:203], v5 offset:50816\n"
      "ds_write_addtid_b32 v86 offset:5120\n"
      "v_cvt_pk_bf16_f32 v15, v15, v19\n"
      "ds_write_addtid_b32 v15 offset:55552\n"
      "v_mfma_f32_16x16x32_bf16 v[56:59], a[52:55], a[208:211], v[56:59]\n"
      "ds_write_addtid_b32 v87 offset:5632\n"
      "v_fma_f32 v40, 0.5, v76, -v88\n"
      "v_mfma_f32_16x16x32_bf16 v[60:63], a[56:59], a[208:211], v[60:63]\n"
      "v_fma_f32 v41, 0.5, v77, -v89\n"
      "v_fma_f32 v42, 0.5, v78, -v90\n"
      "v_mfma_f32_16x16x32_bf16 v[52:55], a[60:63], a[212:215], v[52:55]\n"
      "v_fma_f32 v43, 0.5, v79, -v91\n"
      "v_fma_f32 v44, 0.5, v80, -v92\n"
      "v_mfma_f32_16x16x32_bf16 v[56:59], a[64:67], a[212:215], v[56:59]\n"
      "v_fma_f32 v45, 0.5, v81, -v93\n"
      "v_mfma_f32_16x16x32_bf16 v[60:63], a[68:71], a[212:215], v[60:63]\n"
      "s_waitcnt lgkmcnt(0)\n"
      "s_barrier\n"
      "v_mfma_f32_16x16x32_bf16 v[52:55], a[72:75], a[192:195], v[52:55]\n"
      "s_add_u32 s59, s62, 10420224\n"
      "ds_read_b128 a[204:207], v0 offset:16384\n"
      "ds_read2st64_b32 v[28:29], v6 offset0:0 offset1:2\n"
      "v_fma_f32 v46, 0.5, v82, -v94\n"
      "v_mfma_f32_16x16x32_bf16 v[56:59], a[76:79], a[192:195], v[56:59]\n"
      "ds_read_b128 a[208:211], v0 offset:16448\n"
      "ds_read2st64_b32 v[30:31], v6 offset0:4 offset1:6\n"
      "v_mfma_f32_16x16x32_bf16 v[60:63], a[80:83], a[192:195], v[60:63]\n"
      "ds_read2st64_b32 v[32:33], v6 offset0:8 offset1:10\n"
      "v_fma_f32 v47, 0.5, v83, -v95\n"
      "v_mfma_f32_16x16x32_bf16 v[52:55], a[84:87], a[196:199], v[52:55]\n"
      "s_nop 0\n"
      "ds_read_b128 a[212:215], v0 offset:16512\n"
      "v_fma_f32 v48, 0.5, v84, -v96\n"
      "v_mfma_f32_16x16x32_bf16 v[56:59], a[88:91], a[196:199], v[56:59]\n"
      "s_nop 0\n"
      "ds_read2st64_b32 v[34:35], v6 offset0:12 offset1:14\n"
      "ds_read2st64_b32 v[36:37], v6 offset0:16 offset1:18\n"
      "v_mfma_f32_16x16x32_bf16 v[60:63], a[92:95], a[196:199], v[60:63]\n"
      "s_nop 0\n"
      "v_bfe_u32 v16, v11, 6, 1\n"
      "v_mul_i32_i24 v17, 12296, v16\n"
      "s_mov_b32 s50, -8200\n"
      "v_add3_u32 v12, v8, v17, s50\n"
      "v_add_u32 v14, 128, v12\n"
      "v_mul_i32_i24 v17, 8, v16\n"
      "s_mov_b32 s50, -4104\n"
      "v_add3_u32 v13, v8, v17, s50\n"
      "v_add_u32 v15, 128, v13\n"
      "v_mfma_f32_16x16x32_bf16 v[52:55], a[96:99], a[200:203], v[52:55]\n"
      "ds_read_b128 a[192:195], v1 offset:16384\n"
      "ds_read2st64_b32 v[38:39], v6 offset0:20 offset1:22\n"
      "v_mfma_f32_16x16x32_bf16 v[56:59], a[100:103], a[200:203], v[56:59]\n"
      "v_fma_f32 v49, 0.5, v85, -v97\n"
      "v_fma_f32 v50, 0.5, v86, -v98\n"
      "v_fma_f32 v51, 0.5, v87, -v99\n"
      "v_mfma_f32_16x16x32_bf16 v[60:63], a[104:107], a[200:203], v[60:63]\n"
      "s_waitcnt lgkmcnt(5)\n"
      "v_fmac_f32 v28, s46, v64\n"
      "v_fmac_f32 v29, s46, v65\n"
      "v_mfma_f32_16x16x32_bf16 v[76:79], v[252:255], a[204:207], v[52:55]\n"
      "ds_read_b128 a[196:199], v1 offset:16448\n"
      "v_fmac_f32 v30, s46, v66\n"
      "v_mfma_f32_16x16x32_bf16 v[80:83], v[244:247], a[204:207], v[56:59]\n"
      "v_fmac_f32 v31, s46, v67\n"
      "v_sub_f32 v28, v28, v40\n"
      "v_mfma_f32_16x16x32_bf16 v[84:87], v[236:239], a[204:207], v[60:63]\n"
      "v_sub_f32 v29, v29, v41\n"
      "v_sub_f32 v30, v30, v42\n"
      "v_mfma_f32_16x16x32_bf16 v[76:79], v[228:231], a[208:211], v[76:79]\n"
      "ds_read_b128 a[200:203], v1 offset:16512\n"
      "v_sub_f32 v31, v31, v43\n"
      "v_mfma_f32_16x16x32_bf16 v[80:83], v[220:223], a[208:211], v[80:83]\n"
      "ds_read2st64_b32 a[232:233], v12 offset0:4 offset1:6\n"
      "ds_read2st64_b32 a[234:235], v14 offset0:4 offset1:6\n"
      "v_mfma_f32_16x16x32_bf16 v[84:87], v[212:215], a[208:211], v[84:87]\n"
      "s_waitcnt lgkmcnt(3)\n"
      "v_fmac_f32 v40, s45, v28\n"
      "v_fmac_f32 v41, s45, v29\n"
      "v_mfma_f32_16x16x32_bf16 v[76:79], v[204:207], a[212:215], v[76:79]\n"
      "ds_read_b128 a[204:207], v2 offset:16384\n"
      "v_fmac_f32 v42, s45, v30\n"
      "v_mfma_f32_16x16x32_bf16 v[80:83], v[196:199], a[212:215], v[80:83]\n"
      "v_fmac_f32 v43, s45, v31\n"
      "v_fma_f32 v28, s47, v28, v52\n"
      "v_mfma_f32_16x16x32_bf16 v[84:87], v[188:191], a[212:215], v[84:87]\n"
      "v_fma_f32 v29, s47, v29, v53\n"
      "v_fma_f32 v30, s47, v30, v54\n"
      "v_mfma_f32_16x16x32_bf16 v[76:79], v[180:183], a[192:195], v[76:79]\n"
      "ds_read_b128 a[208:211], v2 offset:16448\n"
      "v_fma_f32 v31, s47, v31, v55\n"
      "v_mfma_f32_16x16x32_bf16 v[80:83], v[172:175], a[192:195], v[80:83]\n"
      "v_cvt_pk_bf16_f32 v40, v40, v41\n"
      "v_cvt_pk_bf16_f32 v42, v42, v43\n"
      "v_mfma_f32_16x16x32_bf16 v[84:87], v[164:167], a[192:195], v[84:87]\n"
      "ds_write_addtid_b32 v40 offset:12288\n"
      "ds_write_addtid_b32 v42 offset:12800\n"
      "v_mfma_f32_16x16x32_bf16 v[76:79], v[156:159], a[196:199], v[76:79]\n"
      "s_nop 0\n"
      "ds_read_b128 a[212:215], v2 offset:16512\n"
      "ds_read2st64_b32 a[236:237], v13 offset0:4 offset1:6\n"
      "v_mfma_f32_16x16x32_bf16 v[80:83], v[148:151], a[196:199], v[80:83]\n"
      "s_waitcnt lgkmcnt(5)\n"
      "ds_read2st64_b32 a[238:239], v15 offset0:4 offset1:6\n"
      "v_cvt_pk_bf16_f32 v28, v28, v29\n"
      "v_mfma_f32_16x16x32_bf16 v[84:87], v[140:143], a[196:199], v[84:87]\n"
      "buffer_store_dwordx4 a[232:235], v11, s[8:11], s59 offen offset:0 sc1\n"
      "v_mfma_f32_16x16x32_bf16 v[76:79], v[132:135], a[200:203], v[76:79]\n"
      "ds_read_b128 a[192:195], v0 offset:20480\n"
      "ds_write_addtid_b32 v28 offset:14336\n"
      "v_mfma_f32_16x16x32_bf16 v[80:83], v[124:127], a[200:203], v[80:83]\n"
      "v_cvt_pk_bf16_f32 v30, v30, v31\n"
      "v_fmac_f32 v32, s46, v68\n"
      "v_mfma_f32_16x16x32_bf16 v[84:87], v[116:119], a[200:203], v[84:87]\n"
      "ds_write_addtid_b32 v30 offset:14848\n"
      "v_fmac_f32 v33, s46, v69\n"
      "v_mfma_f32_16x16x32_bf16 v[76:79], v[108:111], a[204:207], v[76:79]\n"
      "ds_read_b128 a[196:199], v0 offset:20544\n"
      "v_fmac_f32 v34, s46, v70\n"
      "v_mfma_f32_16x16x32_bf16 v[80:83], v[100:103], a[204:207], v[80:83]\n"
      "v_fmac_f32 v35, s46, v71\n"
      "v_sub_f32_e64 v32, v32, v44\n"
      "v_mfma_f32_16x16x32_bf16 v[84:87], a[160:163], a[204:207], v[84:87]\n"
      "s_waitcnt lgkmcnt(4)\n"
      "v_sub_f32 v33, v33, v45\n"
      "v_sub_f32 v34, v34, v46\n"
      "v_mfma_f32_16x16x32_bf16 v[76:79], a[152:155], a[208:211], v[76:79]\n"
      "ds_read_b128 a[200:203], v0 offset:20608\n"
      "v_sub_f32 v35, v35, v47\n"
      "v_mfma_f32_16x16x32_bf16 v[80:83], a[144:147], a[208:211], v[80:83]\n"
      "buffer_store_dwordx4 a[236:239], v11, s[8:11], s59 offen offset:128 sc1\n"
      "v_mfma_f32_16x16x32_bf16 v[84:87], a[136:139], a[208:211], v[84:87]\n"
      "v_fmac_f32 v44, s45, v32\n"
      "v_fmac_f32 v45, s45, v33\n"
      "v_mfma_f32_16x16x32_bf16 v[76:79], a[128:131], a[212:215], v[76:79]\n"
      "ds_read_b128 a[204:207], v1 offset:20480\n"
      "v_fmac_f32 v46, s45, v34\n"
      "v_mfma_f32_16x16x32_bf16 v[80:83], a[120:123], a[212:215], v[80:83]\n"
      "v_fmac_f32_e64 v47, s45, v35\n"
      "v_fma_f32 v32, s47, v32, v56\n"
      "v_mfma_f32_16x16x32_bf16 v[84:87], a[112:115], a[212:215], v[84:87]\n"
      "s_waitcnt lgkmcnt(1)\n"
      "v_fma_f32 v33, s47, v33, v57\n"
      "v_fma_f32 v34, s47, v34, v58\n"
      "v_mfma_f32_16x16x32_bf16 v[88:91], v[248:251], a[192:195], v[52:55]\n"
      "ds_read_b128 a[208:211], v1 offset:20544\n"
      "v_fma_f32 v35, s47, v35, v59\n"
      "v_mfma_f32_16x16x32_bf16 v[92:95], v[240:243], a[192:195], v[56:59]\n"
      "v_cvt_pk_bf16_f32 v44, v44, v45\n"
      "v_cvt_pk_bf16_f32 v46, v46, v47\n"
      "v_mfma_f32_16x16x32_bf16 v[96:99], v[232:235], a[192:195], v[60:63]\n"
      "ds_write_addtid_b32 v44 offset:20484\n"
      "ds_write_addtid_b32 v46 offset:20996\n"
      "v_mfma_f32_16x16x32_bf16 v[88:91], v[224:227], a[196:199], v[88:91]\n"
      "ds_read_b128 a[212:215], v1 offset:20608\n"
      "ds_read2st64_b32 a[240:241], v8 offset0:4 offset1:6\n"
      "v_mfma_f32_16x16x32_bf16 v[92:95], v[216:219], a[196:199], v[92:95]\n"
      "ds_read2st64_b32 a[242:243], v9 offset0:4 offset1:6\n"
      "v_cvt_pk_bf16_f32 v32, v32, v33\n"
      "v_mfma_f32_16x16x32_bf16 v[96:99], v[208:211], a[196:199], v[96:99]\n"
      "ds_write_addtid_b32 v32 offset:22532\n"
      "v_cvt_pk_bf16_f32 v34, v34, v35\n"
      "v_mfma_f32_16x16x32_bf16 v[88:91], v[200:203], a[200:203], v[88:91]\n"
      "ds_read_b128 a[192:195], v2 offset:20480\n"
      "ds_write_addtid_b32 v34 offset:23044\n"
      "v_mfma_f32_16x16x32_bf16 v[92:95], v[192:195], a[200:203], v[92:95]\n"
      "v_fmac_f32 v36, s46, v72\n"
      "v_fmac_f32 v37, s46, v73\n"
      "v_mfma_f32_16x16x32_bf16 v[96:99], v[184:187], a[200:203], v[96:99]\n"
      "s_waitcnt lgkmcnt(5)\n"
      "v_fmac_f32 v38, s46, v74\n"
      "v_fmac_f32 v39, s46, v75\n"
      "v_mfma_f32_16x16x32_bf16 v[88:91], v[176:179], a[204:207], v[88:91]\n"
      "ds_read_b128 a[196:199], v2 offset:20544\n"
      "v_sub_f32 v36, v36, v48\n"
      "v_mfma_f32_16x16x32_bf16 v[92:95], v[168:171], a[204:207], v[92:95]\n"
      "v_sub_f32 v37, v37, v49\n"
      "v_sub_f32 v38, v38, v50\n"
      "v_mfma_f32_16x16x32_bf16 v[96:99], v[160:163], a[204:207], v[96:99]\n"
      "v_sub_f32 v39, v39, v51\n"
      "v_fmac_f32 v48, s45, v36\n"
      "v_mfma_f32_16x16x32_bf16 v[88:91], v[152:155], a[208:211], v[88:91]\n"
      "s_waitcnt lgkmcnt(1)\n"
      "ds_read_b128 a[200:203], v2 offset:20608\n"
      "v_fmac_f32 v49, s45, v37\n"
      "v_mfma_f32_16x16x32_bf16 v[92:95], v[144:147], a[208:211], v[92:95]\n"
      "buffer_store_dwordx4 a[240:243], v11, s[8:11], s59 offen offset:256 sc1\n"
      "v_mfma_f32_16x16x32_bf16 v[96:99], v[136:139], a[208:211], v[96:99]\n"
      "v_fmac_f32 v50, s45, v38\n"
      "v_fmac_f32 v51, s45, v39\n"
      "v_mfma_f32_16x16x32_bf16 v[88:91], v[128:131], a[212:215], v[88:91]\n"
      "ds_read_b128 a[204:207], v3 offset:0\n"
      "v_fma_f32 v36, s47, v36, v60\n"
      "v_mfma_f32_16x16x32_bf16 v[92:95], v[120:123], a[212:215], v[92:95]\n"
      "v_fma_f32 v37, s47, v37, v61\n"
      "v_fma_f32 v38, s47, v38, v62\n"
      "v_mfma_f32_16x16x32_bf16 v[96:99], v[112:115], a[212:215], v[96:99]\n"
      "v_fma_f32 v39, s47, v39, v63\n"
      "v_cvt_pk_bf16_f32 v48, v48, v49\n"
      "v_mfma_f32_16x16x32_bf16 v[88:91], v[104:107], a[192:195], v[88:91]\n"
      "ds_read_b128 a[208:211], v3 offset:64\n"
      "ds_write_addtid_b32 v48 offset:16384\n"
      "v_mfma_f32_16x16x32_bf16 v[92:95], a[164:167], a[192:195], v[92:95]\n"
      "v_cvt_pk_bf16_f32 v50, v50, v51\n"
      "v_cvt_pk_bf16_f32 v36, v36, v37\n"
      "v_mfma_f32_16x16x32_bf16 v[96:99], a[156:159], a[192:195], v[96:99]\n"
      "s_waitcnt lgkmcnt(2)\n"
      "ds_write_addtid_b32 v50 offset:16896\n"
      "ds_write_addtid_b32 v36 offset:18432\n"
      "v_mfma_f32_16x16x32_bf16 v[88:91], a[148:151], a[196:199], v[88:91]\n"
      "ds_read_b128 a[212:215], v3 offset:128\n"
      "v_cvt_pk_bf16_f32 v38, v38, v39\n"
      "v_mfma_f32_16x16x32_bf16 v[92:95], a[140:143], a[196:199], v[92:95]\n"
      "s_nop 0\n"
      "ds_write_addtid_b32 v38 offset:18944\n"
      "v_mfma_f32_16x16x32_bf16 v[96:99], a[132:135], a[196:199], v[96:99]\n"
      "v_mfma_f32_16x16x32_bf16 v[88:91], a[124:127], a[200:203], v[88:91]\n"
      "ds_read_b128 a[192:195], v4 offset:0\n"
      "v_mfma_f32_16x16x32_bf16 v[92:95], a[116:119], a[200:203], v[92:95]\n"
      "v_mfma_f32_16x16x32_bf16 v[96:99], a[108:111], a[200:203], v[96:99]\n"
      "v_mfma_f32_16x16x32_bf16 v[64:67], a[0:3], a[204:207], 0\n"
      "ds_read_b128 a[196:199], v4 offset:64\n"
      "v_mfma_f32_16x16x32_bf16 v[68:71], a[4:7], a[204:207], 0\n"
      "v_mfma_f32_16x16x32_bf16 v[72:75], a[8:11], a[204:207], 0\n"
      "s_waitcnt lgkmcnt(1)\n"
      "v_sub_f32 v76, v88, v76\n"
      "v_sub_f32 v77, v89, v77\n"
      "v_sub_f32 v78, v90, v78\n"
      "v_sub_f32 v79, v91, v79\n"
      "v_sub_f32 v80, v92, v80\n"
      "v_sub_f32 v81, v93, v81\n"
      "v_sub_f32 v82, v94, v82\n"
      "v_mfma_f32_16x16x32_bf16 v[64:67], a[12:15], a[208:211], v[64:67]\n"
      "ds_read_b128 a[200:203], v4 offset:128\n"
      "ds_write_addtid_b32 v76 offset:6144\n"
      "ds_write_addtid_b32 v77 offset:6656\n"
      "v_sub_f32 v83, v95, v83\n"
      "v_sub_f32 v84, v96, v84\n"
      "v_sub_f32 v85, v97, v85\n"
      "v_mfma_f32_16x16x32_bf16 v[68:71], a[16:19], a[208:211], v[68:71]\n"
      "s_nop 0\n"
      "ds_write_addtid_b32 v78 offset:7168\n"
      "ds_write_addtid_b32 v79 offset:7680\n"
      "ds_write_addtid_b32 v80 offset:8192\n"
      "v_mfma_f32_16x16x32_bf16 v[72:75], a[20:23], a[208:211], v[72:75]\n"
      "ds_write_addtid_b32 v81 offset:8704\n"
      "ds_write_addtid_b32 v82 offset:9216\n"
      "ds_write_addtid_b32 v83 offset:9728\n"
      "v_sub_f32 v86, v98, v86\n"
      "v_sub_f32 v87, v99, v87\n"
      "v_mfma_f32_16x16x32_bf16 v[64:67], a[24:27], a[212:215], v[64:67]\n"
      "ds_write_addtid_b32 v84 offset:10240\n"
      "ds_write_addtid_b32 v85 offset:10752\n"
      "v_mfma_f32_16x16x32_bf16 v[68:71], a[28:31], a[212:215], v[68:71]\n"
      "ds_write_addtid_b32 v86 offset:11264\n"
      "ds_write_addtid_b32 v87 offset:11776\n"
      "v_mfma_f32_16x16x32_bf16 v[72:75], a[32:35], a[212:215], v[72:75]\n"
      "s_waitcnt lgkmcnt(0)\n"
      "s_barrier\n"
      "v_mfma_f32_16x16x32_bf16 v[64:67], a[36:39], a[192:195], v[64:67]\n"
      "s_add_u32 s59, s59, 786432\n"
      "ds_read_b128 a[204:207], v5 offset:0\n"
      "ds_read2st64_b32 v[28:29], v6 offset0:24 offset1:26\n"
      "v_mfma_f32_16x16x32_bf16 v[68:71], a[40:43], a[192:195], v[68:71]\n"
      "ds_read2st64_b32 v[30:31], v6 offset0:28 offset1:30\n"
      "ds_read2st64_b32 v[32:33], v6 offset0:32 offset1:34\n"
      "v_mfma_f32_16x16x32_bf16 v[72:75], a[44:47], a[192:195], v[72:75]\n"
      "ds_read2st64_b32 v[34:35], v6 offset0:36 offset1:38\n"
      "ds_read2st64_b32 v[36:37], v6 offset0:40 offset1:42\n"
      "v_mfma_f32_16x16x32_bf16 v[64:67], a[48:51], a[196:199], v[64:67]\n"
      "ds_read_b128 a[208:211], v5 offset:64\n"
      "ds_read2st64_b32 v[38:39], v6 offset0:44 offset1:46\n"
      "v_mfma_f32_16x16x32_bf16 v[68:71], a[52:55], a[196:199], v[68:71]\n"
      "v_fma_f32 v40, 0.5, v76, -v88\n"
      "v_fma_f32 v41, 0.5, v77, -v89\n"
      "v_mfma_f32_16x16x32_bf16 v[72:75], a[56:59], a[196:199], v[72:75]\n"
      "ds_read2st64_b32 a[232:233], v12 offset0:0 offset1:2\n"
      "ds_read2st64_b32 a[234:235], v14 offset0:0 offset1:2\n"
      "v_mfma_f32_16x16x32_bf16 v[64:67], a[60:63], a[200:203], v[64:67]\n"
      "ds_read_b128 a[212:215], v5 offset:128\n"
      "v_fma_f32 v42, 0.5, v78, -v90\n"
      "v_mfma_f32_16x16x32_bf16 v[68:71], a[64:67], a[200:203], v[68:71]\n"
      "s_nop 0\n"
      "ds_read2st64_b32 a[236:237], v13 offset0:0 offset1:2\n"
      "ds_read2st64_b32 a[238:239], v15 offset0:0 offset1:2\n"
      "v_mfma_f32_16x16x32_bf16 v[72:75], a[68:71], a[200:203], v[72:75]\n"
      "s_waitcnt lgkmcnt(9)\n"
      "v_fma_f32 v43, 0.5, v79, -v91\n"
      "v_fma_f32 v44, 0.5, v80, -v92\n"
      "v_mfma_f32_16x16x32_bf16 v[64:67], a[72:75], a[204:207], v[64:67]\n"
      "s_waitcnt lgkmcnt(3)\n"
      "buffer_store_dwordx4 a[232:235], v11, s[8:11], s59 offen offset:0 sc1\n"
      "v_mfma_f32_16x16x32_bf16 v[68:71], a[76:79], a[204:207], v[68:71]\n"
      "s_nop 0\n"
      "v_fma_f32 v45, 0.5, v81, -v93\n"
      "v_fma_f32 v46, 0.5, v82, -v94\n"
      "v_mfma_f32_16x16x32_bf16 v[72:75], a[80:83], a[204:207], v[72:75]\n"
      "ds_read2st64_b32 a[240:241], v8 offset0:0 offset1:2\n"
      "ds_read2st64_b32 a[242:243], v9 offset0:0 offset1:2\n"
      "v_mfma_f32_16x16x32_bf16 v[64:67], a[84:87], a[208:211], v[64:67]\n"
      "s_waitcnt lgkmcnt(2)\n"
      "buffer_store_dwordx4 a[236:239], v11, s[8:11], s59 offen offset:128 sc1\n"
      "v_mfma_f32_16x16x32_bf16 v[68:71], a[88:91], a[208:211], v[68:71]\n"
      "s_nop 0\n"
      "v_fma_f32 v47, 0.5, v83, -v95\n"
      "v_fma_f32 v48, 0.5, v84, -v96\n"
      "v_mfma_f32_16x16x32_bf16 v[72:75], a[92:95], a[208:211], v[72:75]\n"
      "v_fma_f32 v49, 0.5, v85, -v97\n"
      "v_fma_f32 v50, 0.5, v86, -v98\n"
      "v_mfma_f32_16x16x32_bf16 v[64:67], a[96:99], a[212:215], v[64:67]\n"
      "s_waitcnt lgkmcnt(0)\n"
      "buffer_store_dwordx4 a[240:243], v11, s[8:11], s59 offen offset:256 sc1\n"
      "v_mfma_f32_16x16x32_bf16 v[68:71], a[100:103], a[212:215], v[68:71]\n"
      "s_nop 0\n"
      "v_fma_f32 v51, 0.5, v87, -v99\n"
      "v_fmac_f32 v28, s46, v52\n"
      "v_mfma_f32_16x16x32_bf16 v[72:75], a[104:107], a[212:215], v[72:75]\n"
      "v_fmac_f32 v29, s46, v53\n"
      "v_fmac_f32 v30, s46, v54\n"
      "v_fmac_f32 v31, s46, v55\n"
      "v_sub_f32 v28, v28, v40\n"
      "v_sub_f32 v29, v29, v41\n"
      "v_sub_f32 v30, v30, v42\n"
      "v_sub_f32 v31, v31, v43\n"
      "v_fmac_f32 v40, s45, v28\n"
      "v_fmac_f32 v41, s45, v29\n"
      "v_pk_fma_f32 v[42:43], v[30:31], s[44:45], v[42:43] op_sel:[0,1,0] op_sel_hi:[1,1,1]\n"
      "v_cvt_pk_bf16_f32 v40, v40, v41\n"
      "v_cvt_pk_bf16_f32 v42, v42, v43\n"
      "v_pk_fma_f32 v[32:33], v[56:57], s[46:47], v[32:33] op_sel:[0,0,0] op_sel_hi:[1,0,1]\n"
      "v_pk_fma_f32 v[34:35], v[58:59], s[46:47], v[34:35] op_sel:[0,0,0] op_sel_hi:[1,0,1]\n"
      "v_pk_add_f32 v[32:33], v[32:33], v[44:45] neg_lo:[0,1] neg_hi:[0,1]\n"
      "v_pk_add_f32 v[34:35], v[34:35], v[46:47] neg_lo:[0,1] neg_hi:[0,1]\n"
      "v_pk_fma_f32 v[44:45], v[32:33], s[44:45], v[44:45] op_sel:[0,1,0] op_sel_hi:[1,1,1]\n"
      "v_pk_fma_f32 v[46:47], v[34:35], s[44:45], v[46:47] op_sel:[0,1,0] op_sel_hi:[1,1,1]\n"
      "v_cvt_pk_bf16_f32 v44, v44, v45\n"
      "v_cvt_pk_bf16_f32 v46, v46, v47\n"
      "v_pk_fma_f32 v[36:37], v[60:61], s[46:47], v[36:37] op_sel:[0,0,0] op_sel_hi:[1,0,1]\n"
      "v_pk_fma_f32 v[38:39], v[62:63], s[46:47], v[38:39] op_sel:[0,0,0] op_sel_hi:[1,0,1]\n"
      "v_pk_add_f32 v[36:37], v[36:37], v[48:49] neg_lo:[0,1] neg_hi:[0,1]\n"
      "v_pk_add_f32 v[38:39], v[38:39], v[50:51] neg_lo:[0,1] neg_hi:[0,1]\n"
      "v_pk_fma_f32 v[48:49], v[36:37], s[44:45], v[48:49] op_sel:[0,1,0] op_sel_hi:[1,1,1]\n"
      "v_pk_fma_f32 v[50:51], v[38:39], s[44:45], v[50:51] op_sel:[0,1,0] op_sel_hi:[1,1,1]\n"
      "v_cvt_pk_bf16_f32 v48, v48, v49\n"
      "v_cvt_pk_bf16_f32 v50, v50, v51\n"
      "ds_write_addtid_b32 v40 offset:13312\n"
      "ds_write_addtid_b32 v42 offset:13824\n"
      "ds_write_addtid_b32 v44 offset:21508\n"
      "ds_write_addtid_b32 v46 offset:22020\n"
      "ds_write_addtid_b32 v48 offset:17408\n"
      "ds_write_addtid_b32 v50 offset:17920\n"
      "v_pk_fma_f32 v[28:29], v[28:29], s[46:47], v[64:65] op_sel:[0,1,0] op_sel_hi:[1,1,1]\n"
      "v_pk_fma_f32 v[30:31], v[30:31], s[46:47], v[66:67] op_sel:[0,1,0] op_sel_hi:[1,1,1]\n"
      "v_cvt_pk_bf16_f32 v28, v28, v29\n"
      "v_cvt_pk_bf16_f32 v30, v30, v31\n"
      "v_pk_fma_f32 v[32:33], v[32:33], s[46:47], v[68:69] op_sel:[0,1,0] op_sel_hi:[1,1,1]\n"
      "v_pk_fma_f32 v[34:35], v[34:35], s[46:47], v[70:71] op_sel:[0,1,0] op_sel_hi:[1,1,1]\n"
      "v_cvt_pk_bf16_f32 v32, v32, v33\n"
      "v_cvt_pk_bf16_f32 v34, v34, v35\n"
      "v_pk_fma_f32 v[36:37], v[36:37], s[46:47], v[72:73] op_sel:[0,1,0] op_sel_hi:[1,1,1]\n"
      "v_pk_fma_f32 v[38:39], v[38:39], s[46:47], v[74:75] op_sel:[0,1,0] op_sel_hi:[1,1,1]\n"
      "v_cvt_pk_bf16_f32 v36, v36, v37\n"
      "v_cvt_pk_bf16_f32 v38, v38, v39\n"
      "ds_write_addtid_b32 v28 offset:15360\n"
      "ds_write_addtid_b32 v30 offset:15872\n"
      "ds_write_addtid_b32 v32 offset:23556\n"
      "ds_write_addtid_b32 v34 offset:24068\n"
      "ds_write_addtid_b32 v36 offset:19456\n"
      "ds_write_addtid_b32 v38 offset:19968\n"
      "s_waitcnt lgkmcnt(0)\n"
      "s_barrier\n"
      "ds_read2st64_b32 a[232:233], v12 offset0:4 offset1:6\n"
      "ds_read2st64_b32 a[234:235], v14 offset0:4 offset1:6\n"
      "ds_read2st64_b32 a[236:237], v13 offset0:4 offset1:6\n"
      "ds_read2st64_b32 a[238:239], v15 offset0:4 offset1:6\n"
      "ds_read2st64_b32 a[240:241], v8 offset0:4 offset1:6\n"
      "ds_read2st64_b32 a[242:243], v9 offset0:4 offset1:6\n"
      "s_add_u32 s59, s59, 786432\n"
      "s_waitcnt lgkmcnt(4)\n"
      "buffer_store_dwordx4 a[232:235], v11, s[8:11], s59 offen offset:0 sc1\n"
      "s_waitcnt lgkmcnt(2)\n"
      "buffer_store_dwordx4 a[236:239], v11, s[8:11], s59 offen offset:128 sc1\n"
      "s_waitcnt lgkmcnt(0)\n"
      "buffer_store_dwordx4 a[240:243], v11, s[8:11], s59 offen offset:256 sc1\n"
      "LSend_%=:\n"
      "Lfin_%=:\n"
      "s_waitcnt vmcnt(0) lgkmcnt(0)\n"
      "s_endpgm\n"
      ".rept 512\n"
      "s_nop 0\n"
      ".endr\n"
      ".Lwg_end:\n"
      : "+{v0}"(tid)
      : "{s[0:1]}"(__builtin_amdgcn_kernarg_segment_ptr()), "{s2}"(blockIdx.x)
      : "v1", "v2", "v3", "v4", "v5", "v6", "v7", "v8", "v9", "v10", "v11", "v12", "v13", "v14", "v15", "v16", "v17", "v18", "v19", "v20", "v21", "v22", "v23", "v24", "v25", "v26", "v27", "v28", "v29", "v30", "v31", "v32", "v33", "v34", "v35", "v36", "v37", "v38", "v39", "v40", "v41", "v42", "v43", "v44", "v45", "v46", "v47", "v48", "v49", "v50", "v51", "v52", "v53", "v54", "v55", "v56", "v57", "v58", "v59", "v60", "v61", "v62", "v63", "v64", "v65", "v66", "v67", "v68", "v69", "v70", "v71", "v72", "v73", "v74", "v75", "v76", "v77", "v78", "v79", "v80", "v81", "v82", "v83", "v84", "v85", "v86", "v87", "v88", "v89", "v90", "v91", "v92", "v93", "v94", "v95", "v96", "v97", "v98", "v99", "v100", "v101", "v102", "v103", "v104", "v105", "v106", "v107", "v108", "v109", "v110", "v111", "v112", "v113", "v114", "v115", "v116", "v117", "v118", "v119", "v120", "v121", "v122", "v123", "v124", "v125", "v126", "v127", "v128", "v129", "v130", "v131", "v132", "v133", "v134", "v135", "v136", "v137", "v138", "v139", "v140", "v141", "v142", "v143", "v144", "v145", "v146", "v147", "v148", "v149", "v150", "v151", "v152", "v153", "v154", "v155", "v156", "v157", "v158", "v159", "v160", "v161", "v162", "v163", "v164", "v165", "v166", "v167", "v168", "v169", "v170", "v171", "v172", "v173", "v174", "v175", "v176", "v177", "v178", "v179", "v180", "v181", "v182", "v183", "v184", "v185", "v186", "v187", "v188", "v189", "v190", "v191", "v192", "v193", "v194", "v195", "v196", "v197", "v198", "v199", "v200", "v201", "v202", "v203", "v204", "v205", "v206", "v207", "v208", "v209", "v210", "v211", "v212", "v213", "v214", "v215", "v216", "v217", "v218", "v219", "v220", "v221", "v222", "v223", "v224", "v225", "v226", "v227", "v228", "v229", "v230", "v231", "v232", "v233", "v234", "v235", "v236", "v237", "v238", "v239", "v240", "v241", "v242", "v243", "v244", "v245", "v246", "v247", "v248", "v249", "v250", "v251", "v252", "v253", "v254", "v255", "a0", "a1", "a2", "a3", "a4", "a5", "a6", "a7", "a8", "a9", "a10", "a11", "a12", "a13", "a14", "a15", "a16", "a17", "a18", "a19", "a20", "a21", "a22", "a23", "a24", "a25", "a26", "a27", "a28", "a29", "a30", "a31", "a32", "a33", "a34", "a35", "a36", "a37", "a38", "a39", "a40", "a41", "a42", "a43", "a44", "a45", "a46", "a47", "a48", "a49", "a50", "a51", "a52", "a53", "a54", "a55", "a56", "a57", "a58", "a59", "a60", "a61", "a62", "a63", "a64", "a65", "a66", "a67", "a68", "a69", "a70", "a71", "a72", "a73", "a74", "a75", "a76", "a77", "a78", "a79", "a80", "a81", "a82", "a83", "a84", "a85", "a86", "a87", "a88", "a89", "a90", "a91", "a92", "a93", "a94", "a95", "a96", "a97", "a98", "a99", "a100", "a101", "a102", "a103", "a104", "a105", "a106", "a107", "a108", "a109", "a110", "a111", "a112", "a113", "a114", "a115", "a116", "a117", "a118", "a119", "a120", "a121", "a122", "a123", "a124", "a125", "a126", "a127", "a128", "a129", "a130", "a131", "a132", "a133", "a134", "a135", "a136", "a137", "a138", "a139", "a140", "a141", "a142", "a143", "a144", "a145", "a146", "a147", "a148", "a149", "a150", "a151", "a152", "a153", "a154", "a155", "a156", "a157", "a158", "a159", "a160", "a161", "a162", "a163", "a164", "a165", "a166", "a167", "a168", "a169", "a170", "a171", "a172", "a173", "a174", "a175", "a176", "a177", "a178", "a179", "a180", "a181", "a182", "a183", "a184", "a185", "a186", "a187", "a188", "a189", "a190", "a191", "a192", "a193", "a194", "a195", "a196", "a197", "a198", "a199", "a200", "a201", "a202", "a203", "a204", "a205", "a206", "a207", "a208", "a209", "a210", "a211", "a212", "a213", "a214", "a215", "a216", "a217", "a218", "a219", "a220", "a221", "a222", "a223", "a224", "a225", "a226", "a227", "a228", "a229", "a230", "a231", "a232", "a233", "a234", "a235", "a236", "a237", "a238", "a239", "a240", "a241", "a242", "a243", "a244", "a245", "a246", "a247", "a248", "a249", "a250", "a251", "a252", "a253", "a254", "a255", "s3", "s4", "s5", "s6", "s7", "s8", "s9", "s10", "s11", "s12", "s13", "s14", "s15", "s16", "s17", "s18", "s19", "s20", "s21", "s22", "s23", "s24", "s25", "s26", "s27", "s28", "s29", "s30", "s31", "s33", "s34", "s35", "s36", "s37", "s38", "s39", "s40", "s41", "s42", "s43", "s44", "s45", "s46", "s47", "s48", "s49", "s50", "s51", "s52", "s53", "s54", "s55", "s56", "s57", "s58", "s59", "s60", "s61", "s62", "s63", "s64", "s65", "s66", "s67", "s68", "s69", "s70", "s71", "s72", "s73", "s74", "s75", "s76", "s77", "s78", "s79", "s80", "s81", "s82", "s83", "s84", "s85", "s86", "s87", "s88", "s89", "s90", "s91", "s92", "s93", "s94", "s95", "s96", "s97", "s98", "s99", "scc", "vcc", "memory");
  asm volatile("" :: "v"((unsigned)(uintptr_t)smem) : "memory");
}

extern "C" void kernel_launch(void* const* d_in, const int* in_sizes, int n_in, void* d_out,
                              int out_size, void* d_ws, size_t ws_size, hipStream_t stream) {
  conv_kernel<<<256, 256, 0, stream>>>((const char*)d_in[0], (const char*)d_in[1], (char*)d_out, (char*)d_ws);
}
```
